```python
import torch

def _softmax(x, axis=-1):
    return torch.softmax(x, dim=axis)

def _gelu(x):
    return torch.nn.functional.gelu(x, approximate='tanh')

def _silu(x):
    return torch.nn.functional.silu(x)

def _sigmoid(x):
    return torch.sigmoid(x)

def _layernorm(x, axis=-1, eps=1e-05):
    m = x.mean(dim=axis, keepdim=True)
    v = x.var(dim=axis, keepdim=True, unbiased=False)
    return (x - m) / torch.sqrt(v + eps)

def _scatter_add(tgt, idx, src, axis=0):
    return tgt.clone().index_add_(axis, idx, src)

def _segment_sum(src, seg, n):
    out = torch.zeros((n,) + src.shape[1:], dtype=src.dtype)
    return out.index_add_(0, seg, src)

def _pad(x, pw):
    flat = tuple((v for pair in reversed(pw) for v in pair))
    return torch.nn.functional.pad(x, flat)
kernel_name = 'adaLN_cond__ssm_conv_h3__glu_gated_reglu__prompt'

def setup_inputs():

    _w = lambda a: (lambda t: t.to(torch.bfloat16) if t.is_floating_point() else t)(torch.as_tensor(a))

    def _A_setup():
        B, S, D = (1, 2048, 640)
        ks = 4
        return {'q': _w((lambda *s: torch.randn(*s, dtype=torch.bfloat16) * 0.05)(B, S, D)), 'k': _w((lambda *s: torch.randn(*s, dtype=torch.bfloat16) * 0.8)(B, S, D)), 'v': _w((lambda *s: torch.randn(*s, dtype=torch.bfloat16) * 0.8)(B, S, D)), 'shift_k': _w((lambda *s: torch.randn(*s, dtype=torch.bfloat16) * 0.8)(D, ks) * 0.5), 'ssm_k': _w((lambda *s: torch.randn(*s, dtype=torch.bfloat16) * 0.8)(D, S) * (1.0 / S ** 0.5)), 'ssm_D': _w((lambda *s: torch.randn(*s, dtype=torch.bfloat16) * 0.8)(D))}
    _A_in = _A_setup()

    def _B_setup():
        B, S, D, F = (1, 2048, 640, 1536)
        return {'x': _w((lambda *s: torch.randn(*s, dtype=torch.bfloat16) * 0.05)(B, S, D)), 'W_gate': _w((lambda *s: torch.randn(*s, dtype=torch.bfloat16) * 0.8)(D, F) * (2.0 / D) ** 0.5), 'W_up': _w((lambda *s: torch.randn(*s, dtype=torch.bfloat16) * 0.8)(D, F) * (2.0 / D) ** 0.5), 'W_down': _w((lambda *s: torch.randn(*s, dtype=torch.bfloat16) * 0.8)(F, D) * (1.0 / F) ** 0.5)}
    _B_in = _B_setup()
    return {'A_q': _A_in['q'], 'A_k': _A_in['k'], 'A_v': _A_in['v'], 'A_shift_k': _A_in['shift_k'], 'A_ssm_k': _A_in['ssm_k'], 'A_ssm_D': _A_in['ssm_D'], 'B_x': _B_in['x'], 'B_W_gate': _B_in['W_gate'], 'B_W_up': _B_in['W_up'], 'B_W_down': _B_in['W_down'], 'c': _w((lambda *s: torch.randn(*s, dtype=torch.bfloat16) * 0.2)(1, 640) * 8.0), 'w_mod': _w((lambda *s: torch.randn(*s, dtype=torch.bfloat16) * 0.2)(640, 6 * 640) * 640 ** (-0.5) * 8.0)}

def reference(A_q, A_k, A_v, A_shift_k, A_ssm_k, A_ssm_D, B_x, B_W_gate, B_W_up, B_W_down, c, w_mod):

    def _A_ref(q, k, v, shift_k, ssm_k, ssm_D):
        B, S, D = q.shape
        ks = shift_k.shape[-1]
        k_pad = _pad(k, ((0, 0), (ks - 1, 0), (0, 0)))
        k_shift = torch.zeros_like(k)
        for t in range(ks):
            k_shift = k_shift + k_pad[:, t:t + S, :] * shift_k[:, ks - 1 - t][None, None, :]
        kv = k_shift * v
        L = 2 * S
        kv_pad = _pad(kv, ((0, 0), (0, S), (0, 0)))
        kf_pad = _pad(ssm_k, ((0, 0), (0, S)))
        kv_f = (lambda x, axes=(-2, -1): torch.fft.rfft2(x.float(), dim=axes))(kv_pad[:, None, :, :], axes=(1, 2))
        kf_f = (lambda x, axes=(-2, -1): torch.fft.rfft2(x.float(), dim=axes))(kf_pad[:, None, :], axes=(1, 2))
        kf_f = (lambda x, *d: x.permute(*d))(kf_f, 1, 2, 0)
        y_f = kv_f * kf_f[None, :, :, :]
        y = (lambda x, s, axes=(-2, -1): torch.fft.irfft2(x, s=s, dim=axes).to(torch.bfloat16))(y_f, (1, L), axes=(1, 2))[:, 0, :S, :]
        y = y + kv * ssm_D[None, None, :]
        return q * y

    def _B_ref(x, W_gate, W_up, W_down):
        gate = torch.einsum('bsd,df->bsf', x, W_gate)
        up = torch.einsum('bsd,df->bsf', x, W_up)
        gate = gate * (lambda x, dt: x.to(dt))(gate > 0, gate.dtype)
        h = gate * up
        return torch.einsum('bsf,fd->bsd', h, W_down)
    x = A_q
    mod = (_silu(c) @ w_mod).reshape(c.shape[0], 6, 1, -1)
    sa, ca, ga, sb, cb, gb = (mod[:, 0].clone(), mod[:, 1].clone(), mod[:, 2].clone(), mod[:, 3].clone(), mod[:, 4].clone(), mod[:, 5].clone())
    h = _layernorm(x, axis=-1) * (1 + ca) + sa
    x = x + ga * _A_ref(h, A_k, A_v, A_shift_k, A_ssm_k, A_ssm_D)
    h = _layernorm(x, axis=-1) * (1 + cb) + sb
    x = x + gb * _B_ref(h, B_W_gate, B_W_up, B_W_down)
    return x
```

```cpp
#include <hip/hip_runtime.h>
#include <stdint.h>

typedef unsigned short u16;
typedef unsigned int u32;
typedef __attribute__((ext_vector_type(8))) __bf16 bf16x8;
typedef __attribute__((ext_vector_type(2))) __bf16 bf16x2;
typedef __attribute__((ext_vector_type(4))) short s16x4;
typedef __attribute__((ext_vector_type(4))) float f32x4;
typedef __attribute__((ext_vector_type(2))) float f32x2;
typedef __attribute__((ext_vector_type(4))) u32 u32x4;
typedef __attribute__((ext_vector_type(2))) u32 u32x2;
#define LAS __attribute__((address_space(3)))
#define GAS __attribute__((address_space(1)))
#define DI __device__ __forceinline__

#define STAMP(id)
#define STAMP_DECL
#define STAMP_OUT(kid)
constexpr int S_ = 2048, D_ = 640, F_ = 1536;
constexpr unsigned OFF_KVT = 0u;
constexpr unsigned OFF_XT = 2621440u;
constexpr unsigned OFF_X2P = 5242880u;
constexpr unsigned OFF_H2F = 7864320u;
constexpr unsigned OFF_W1F = 14155776u;
constexpr unsigned OFF_W2F = 18087936u;
constexpr unsigned OFF_MODP = 20054016u;
constexpr unsigned OFF_ST1 = 20115456u;
constexpr unsigned OFF_UB = 20131840u;
constexpr unsigned OFF_ST2 = 20230144u;
constexpr unsigned OFF_FL = 20246528u;
constexpr unsigned WS_END = 20262912u;

constexpr unsigned OOB = 0x80000000u;
DI unsigned rfl(unsigned x) { return __builtin_amdgcn_readfirstlane(x); }
DI float bflo(u32 w) { return __uint_as_float(w << 16); }
DI float bfhi(u32 w) { return __uint_as_float(w & 0xffff0000u); }
DI u32 pack_bf16(float lo, float hi) { bf16x2 v; v[0] = (__bf16)lo; v[1] = (__bf16)hi; return __builtin_bit_cast(u32, v); }
DI float rbf(float x) { __bf16 b = (__bf16)x; return (float)b; }
DI float dot2(u32 a, u32 b, float c) { return __builtin_amdgcn_fdot2_f32_bf16(__builtin_bit_cast(bf16x2, a), __builtin_bit_cast(bf16x2, b), c, false); }
DI f32x4 mfma(u32x4 a, u32x4 b, f32x4 c) { return __builtin_amdgcn_mfma_f32_16x16x32_bf16(__builtin_bit_cast(bf16x8, a), __builtin_bit_cast(bf16x8, b), c, 0, 0, 0); }
DI u32x2 tr_read(const char* p) { return __builtin_bit_cast(u32x2, __builtin_amdgcn_ds_read_tr16_b64_v4i16((LAS s16x4*)p)); }
DI float modval(const char* ws, int col) {
  const float* m = (const float*)(ws + OFF_MODP);
  return rbf((m[col] + m[3840 + col]) + (m[2 * 3840 + col] + m[3 * 3840 + col]));
}

__global__ __launch_bounds__(704) void k_prep(const u16* __restrict__ x, const u16* __restrict__ kIn, const u16* __restrict__ vIn,
                                              const u16* __restrict__ shk, const u16* __restrict__ c, const u16* __restrict__ wmod,
                                              char* __restrict__ ws) {
  __shared__ float red[4][64];
  const int b = blockIdx.x, tid = threadIdx.x, w = tid >> 6, lane = tid & 63;
  const int tb = (b & 7) + 8 * (b >> 4), half = (b >> 3) & 1;
  STAMP_DECL; STAMP(0);
  if (w < 5) {
    const int p = w * 32 + (lane >> 1), o = lane & 1;
    const int d0 = 320 * half + 2 * p, s0 = 16 * tb + 8 * o;
    u32 kk[11], vv[8], xx[8];
#pragma unroll
    for (int j = 0; j < 11; ++j) {
      const int row = s0 - 3 + j;
      const u32 t = __builtin_nontemporal_load((const u32*)(kIn + (size_t)(row < 0 ? 0 : row) * D_ + d0));
      kk[j] = row < 0 ? 0u : t;
    }
#pragma unroll
    for (int j = 0; j < 8; ++j) vv[j] = __builtin_nontemporal_load((const u32*)(vIn + (size_t)(s0 + j) * D_ + d0));
#pragma unroll
    for (int j = 0; j < 8; ++j) xx[j] = *(const u32*)(x + (size_t)(s0 + j) * D_ + d0);
    const u32x4 taps = *(const u32x4*)(shk + d0 * 4);
    const f32x2 w0 = {bflo(taps[0]), bflo(taps[2])}, w1 = {bfhi(taps[0]), bfhi(taps[2])}, w2 = {bflo(taps[1]), bflo(taps[3])}, w3 = {bfhi(taps[1]), bfhi(taps[3])};
    f32x2 kf[11], kv[8];
#pragma unroll
    for (int j = 0; j < 11; ++j) kf[j] = f32x2{bflo(kk[j]), bfhi(kk[j])};
#pragma unroll
    for (int j = 0; j < 8; ++j) {
      f32x2 ks = kf[j] * w3;
      ks = __builtin_elementwise_fma(kf[j + 1], w2, ks);
      ks = __builtin_elementwise_fma(kf[j + 2], w1, ks);
      ks = __builtin_elementwise_fma(kf[j + 3], w0, ks);
      kv[j] = ks * f32x2{bflo(vv[j]), bfhi(vv[j])};
    }
    u32x4 o0, o1, x0, x1;
#pragma unroll
    for (int t = 0; t < 4; ++t) {
      o0[t] = pack_bf16(kv[7 - 2 * t][0], kv[6 - 2 * t][0]);
      o1[t] = pack_bf16(kv[7 - 2 * t][1], kv[6 - 2 * t][1]);
      x0[t] = __builtin_amdgcn_perm(xx[2 * t + 1], xx[2 * t], 0x05040100u);
      x1[t] = __builtin_amdgcn_perm(xx[2 * t + 1], xx[2 * t], 0x07060302u);
    }
    const int off = (tb * 160 + 2 * (d0 >> 3)) * 128 + (((d0 >> 1) & 3) * 2 + o) * 16;
    *(u32x4*)(ws + OFF_KVT + off) = o0;
    *(u32x4*)(ws + OFF_KVT + off + 128) = o1;
    *(u32x4*)(ws + OFF_XT + off) = x0;
    *(u32x4*)(ws + OFF_XT + off + 128) = x1;
  } else if (w < 7) {
    const int s = 16 * tb + 8 * half + (w - 5) * 4 + (lane >> 4);
    float s1 = 0.f, s2 = 0.f;
#pragma unroll
    for (int n = 0; n < 5; ++n) {
      const u32x4 v = *(const u32x4*)(x + (size_t)s * D_ + ((lane & 15) + 16 * n) * 8);
#pragma unroll
      for (int j = 0; j < 4; ++j) { s1 = dot2(v[j], 0x3f803f80u, s1); s2 = dot2(v[j], v[j], s2); }
    }
#pragma unroll
    for (int o = 1; o < 16; o <<= 1) { s1 += __shfl_xor(s1, o); s2 += __shfl_xor(s2, o); }
    const float mean = s1 * (1.f / D_);
    const float var = fmaxf(s2 * (1.f / D_) - mean * mean, 0.f);
    const float rstd = __builtin_amdgcn_rsqf(var + 1e-5f);
    if ((lane & 15) == 0) *(f32x2*)(ws + OFF_ST1 + s * 8) = f32x2{rstd, -mean * rstd};
  } else if (b < 240) {
    const int t = tid - 448, cgp = b % 60, kq = b / 60;
    const int rg = t >> 3, cp = t & 7;
    float acc[8];
#pragma unroll
    for (int i = 0; i < 8; ++i) acc[i] = 0.f;
#pragma unroll
    for (int it = 0; it < 5; ++it) {
      const int row = kq * 160 + rg + 32 * it;
      const float cv = bflo((u32)c[row]);
      const float s = rbf(cv / (1.f + __expf(-cv)));
      const u32x4 wv = __builtin_nontemporal_load((const u32x4*)(wmod + (size_t)row * 3840 + cgp * 64 + cp * 8));
#pragma unroll
      for (int j = 0; j < 4; ++j) { acc[2 * j] += s * bflo(wv[j]); acc[2 * j + 1] += s * bfhi(wv[j]); }
    }
#pragma unroll
    for (int i = 0; i < 8; ++i) { acc[i] += __shfl_xor(acc[i], 8); acc[i] += __shfl_xor(acc[i], 16); acc[i] += __shfl_xor(acc[i], 32); }
    if (lane < 8) {
#pragma unroll
      for (int i = 0; i < 8; ++i) red[w - 7][lane * 8 + i] = acc[i];
    }
  }
  if (tid < 8) *(u32x4*)(ws + OFF_ST2 + b * 128 + tid * 16) = u32x4{0, 0, 0, 0};
  STAMP(1);
  __syncthreads();
  STAMP(2);
  STAMP_OUT(0);
  if (w == 7 && b < 240) {
    const int cgp = b % 60, kq = b / 60;
    ((float*)(ws + OFF_MODP))[kq * 3840 + cgp * 64 + lane] = (red[0][lane] + red[1][lane]) + (red[2][lane] + red[3][lane]);
  }
}

constexpr int FC_PITCH = 4160, FC_CH = 4 * FC_PITCH;
constexpr int KV_PITCH = 4640, X_PITCH = 4128;
constexpr int L_FC = 0, L_KV = 4 * FC_CH, L_X = L_KV + 4 * KV_PITCH, L_ST = L_X + 4 * X_PITCH, L_CONV_END = L_ST + 16384;

DI void conv_body(char* smem, const u16* __restrict__ ssmk, const u16* __restrict__ ssmD, char* __restrict__ ws, int cb) {
  const int tid = threadIdx.x, w = tid >> 6, lane = tid & 63;
  const int d = 8 * (cb >> 1) + 2 * w + (cb & 1);
  STAMP_DECL; STAMP(0);
  u32x4 kvr[4], xr[4], str[4], fa[4]; u32x2 fb[4];
  const u16* frow = ssmk + (size_t)d * S_;
#pragma unroll
  for (int n = 0; n < 4; ++n) {
    const int fc = lane + 64 * n;
    fa[n] = *(const u32x4*)(frow + fc * 8);
    const u32x2 t = *(const u32x2*)(frow + (fc < 255 ? fc + 1 : 0) * 8);
    fb[n] = fc < 255 ? t : u32x2{0, 0};
  }
#pragma unroll
  for (int n = 0; n < 4; ++n) kvr[n] = *(const u32x4*)(ws + OFF_KVT + ((n * 32 + (tid >> 3)) * 160 + cb) * 128 + (tid & 7) * 16);
#pragma unroll
  for (int n = 0; n < 4; ++n) xr[n] = *(const u32x4*)(ws + OFF_XT + ((n * 32 + (tid >> 3)) * 160 + cb) * 128 + (tid & 7) * 16);
#pragma unroll
  for (int n = 0; n < 4; ++n) str[n] = *(const u32x4*)(ws + OFF_ST1 + (n * 256 + tid) * 16);
  const float sa = modval(ws, d), ca = modval(ws, 640 + d), ga = modval(ws, 1280 + d);
  const float Dv = bflo((u32)ssmD[d]);
  STAMP(1);
  char* fcw = smem + L_FC + w * FC_CH;
  auto put = [&](int u, u32x4 a, u32x2 h) {
    const u32 W[6] = {a[0], a[1], a[2], a[3], h[0], h[1]};
    u32 O[5];
#pragma unroll
    for (int i = 0; i < 5; ++i) O[i] = __builtin_amdgcn_alignbit(W[i + 1], W[i], 16);
    *(u32x4*)(fcw + 0 * FC_PITCH + u * 16) = a;
    *(u32x4*)(fcw + 1 * FC_PITCH + u * 16) = u32x4{O[0], O[1], O[2], O[3]};
    *(u32x4*)(fcw + 2 * FC_PITCH + u * 16) = u32x4{W[1], W[2], W[3], W[4]};
    *(u32x4*)(fcw + 3 * FC_PITCH + u * 16) = u32x4{O[1], O[2], O[3], O[4]};
  };
#pragma unroll
  for (int n = 0; n < 4; ++n) put(lane + 64 * n + 2, fa[n], fb[n]);
  if (lane == 0) { put(0, u32x4{0, 0, 0, 0}, u32x2{0, 0}); put(1, u32x4{0, 0, 0, 0}, u32x2{fa[0][0], fa[0][1]}); }
  if (lane < 32) *(u32x4*)(smem + L_KV + w * KV_PITCH + lane * 16) = u32x4{0, 0, 0, 0};
#pragma unroll
  for (int n = 0; n < 4; ++n) {
    const int g16 = (n * 32 + (tid >> 3)) * 2 + (tid & 1), ch = (tid & 7) >> 1;
    *(u32x4*)(smem + L_KV + ch * KV_PITCH + 512 + g16 * 16) = kvr[n];
    *(u32x4*)(smem + L_X + ch * X_PITCH + g16 * 16) = xr[n];
    *(u32x4*)(smem + L_ST + (n * 256 + tid) * 16) = str[n];
  }
  __syncthreads();
  STAMP(2);
  const int r = lane & 15, g = lane >> 4;
  const int pp = 25 + r - 8 * g;
  const unsigned lb = (unsigned)(size_t)smem;
  const unsigned va = lb + L_FC + w * FC_CH + (pp & 3) * FC_PITCH + (pp & ~3) * 2;
  const unsigned vb = lb + L_KV + w * KV_PITCH + (240 + 16 * r + 8 * g) * 2 - 448;
  const unsigned t0 = 16 * r + 4 * g;
  const unsigned vx = lb + L_X + w * X_PITCH + t0 * 2, vk = lb + L_KV + w * KV_PITCH + 512 + (t0 ^ 4) * 2, vs = lb + L_ST + t0 * 8;
  const unsigned to = 256 * (g & 1) + 16 * r + 8 * (g >> 1);
  const unsigned vo = (to >> 6) * 81920u + (to & 63) * 2;
  const __amdgpu_buffer_rsrc_t srd = __builtin_amdgcn_make_buffer_rsrc((void*)ws, 0, WS_END, 0x00020000);
  const unsigned sG = rfl(__float_as_uint(ga * rbf(1.f + ca))), sH = rfl(__float_as_uint(ga * sa)), sD = rfl(__float_as_uint(Dv));
  asm volatile(
  "s_nop 4\n\t"
  "ds_read_b64 v[64:65], %[va] offset:0\n\t"
  "ds_read_b64 v[66:67], %[va] offset:8\n\t"
  "ds_read_b128 v[124:127], %[vb] offset:4032\n\t"
  "ds_read_b128 v[120:123], %[vb] offset:3520\n\t"
  "ds_read_b128 v[116:119], %[vb] offset:3008\n\t"
  "ds_read_b128 v[112:115], %[vb] offset:2496\n\t"
  "ds_read_b128 v[108:111], %[vb] offset:1984\n\t"
  "ds_read_b128 v[104:107], %[vb] offset:1472\n\t"
  "ds_read_b128 v[100:103], %[vb] offset:960\n\t"
  "ds_read_b128 v[96:99], %[vb] offset:448\n\t"
  "ds_read_b64 v[68:69], %[va] offset:512\n\t"
  "ds_read_b64 v[70:71], %[va] offset:520\n\t"
  "ds_read_b64 v[72:73], %[va] offset:1024\n\t"
  "ds_read_b64 v[74:75], %[va] offset:1032\n\t"
  "ds_read_b64 v[76:77], %[va] offset:1536\n\t"
  "ds_read_b64 v[78:79], %[va] offset:1544\n\t"
  "ds_read_b64 v[80:81], %[va] offset:2048\n\t"
  "ds_read_b64 v[82:83], %[va] offset:2056\n\t"
  "ds_read_b64 v[84:85], %[va] offset:2560\n\t"
  "ds_read_b64 v[86:87], %[va] offset:2568\n\t"
  "ds_read_b64 v[88:89], %[va] offset:3072\n\t"
  "ds_read_b64 v[90:91], %[va] offset:3080\n\t"
  "ds_read_b64 v[92:93], %[va] offset:3584\n\t"
  "ds_read_b64 v[94:95], %[va] offset:3592\n\t"
  "v_mov_b32 v252, %[sH]\n\t"
  "s_waitcnt lgkmcnt(15)\n\t"
  "v_mfma_f32_16x16x32_bf16 v[60:63], v[64:67], v[124:127], 0\n\t"
  "v_mfma_f32_16x16x32_bf16 v[56:59], v[64:67], v[120:123], 0\n\t"
  "ds_read_b128 v[156:159], %[vb] offset:3968\n\t"
  "v_mfma_f32_16x16x32_bf16 v[52:55], v[64:67], v[116:119], 0\n\t"
  "ds_read_b64 v[224:225], %[vx] offset:0\n\t"
  "v_mfma_f32_16x16x32_bf16 v[48:51], v[64:67], v[112:115], 0\n\t"
  "ds_read_b64 v[226:227], %[vk] offset:0\n\t"
  "v_mfma_f32_16x16x32_bf16 v[44:47], v[64:67], v[108:111], 0\n\t"
  "ds_read_b128 v[152:155], %[vb] offset:3456\n\t"
  "v_mfma_f32_16x16x32_bf16 v[40:43], v[64:67], v[104:107], 0\n\t"
  "ds_read_b128 v[228:231], %[vs] offset:0\n\t"
  "v_mfma_f32_16x16x32_bf16 v[36:39], v[64:67], v[100:103], 0\n\t"
  "ds_read_b128 v[232:235], %[vs] offset:16\n\t"
  "s_waitcnt lgkmcnt(15)\n\t"
  "v_mfma_f32_16x16x32_bf16 v[32:35], v[64:67], v[96:99], 0\n\t"
  "ds_read_b64 v[64:65], %[va] offset:64\n\t"
  "ds_read_b64 v[66:67], %[va] offset:72\n\t"
  "ds_read_b128 v[148:151], %[vb] offset:2944\n\t"
  "v_mfma_f32_16x16x32_bf16 v[60:63], v[68:71], v[120:123], v[60:63]\n\t"
  "v_mfma_f32_16x16x32_bf16 v[56:59], v[68:71], v[116:119], v[56:59]\n\t"
  "v_mfma_f32_16x16x32_bf16 v[52:55], v[68:71], v[112:115], v[52:55]\n\t"
  "ds_read_b128 v[144:147], %[vb] offset:2432\n\t"
  "v_mfma_f32_16x16x32_bf16 v[48:51], v[68:71], v[108:111], v[48:51]\n\t"
  "v_mfma_f32_16x16x32_bf16 v[44:47], v[68:71], v[104:107], v[44:47]\n\t"
  "v_mfma_f32_16x16x32_bf16 v[40:43], v[68:71], v[100:103], v[40:43]\n\t"
  "ds_read_b128 v[140:143], %[vb] offset:1920\n\t"
  "v_mfma_f32_16x16x32_bf16 v[36:39], v[68:71], v[96:99], v[36:39]\n\t"
  "ds_read_b64 v[68:69], %[va] offset:576\n\t"
  "ds_read_b64 v[70:71], %[va] offset:584\n\t"
  "s_waitcnt lgkmcnt(11)\n\t"
  "v_lshlrev_b32 v192, 16, v224\n\t"
  "s_waitcnt lgkmcnt(10)\n\t"
  "v_and_b32 v248, 0xffff0000, v227\n\t"
  "v_mfma_f32_16x16x32_bf16 v[60:63], v[72:75], v[116:119], v[60:63]\n\t"
  "s_waitcnt lgkmcnt(8)\n\t"
  "v_fma_f32 v160, v192, v228, v229\n\t"
  "v_mfma_f32_16x16x32_bf16 v[56:59], v[72:75], v[112:115], v[56:59]\n\t"
  "ds_read_b128 v[136:139], %[vb] offset:1408\n\t"
  "v_mul_f32 v248, %[sD], v248\n\t"
  "v_mfma_f32_16x16x32_bf16 v[52:55], v[72:75], v[108:111], v[52:55]\n\t"
  "v_fma_f32 v160, v160, %[sG], v252\n\t"
  "v_mfma_f32_16x16x32_bf16 v[48:51], v[72:75], v[104:107], v[48:51]\n\t"
  "v_fmac_f32 v192, v160, v248\n\t"
  "v_mfma_f32_16x16x32_bf16 v[44:47], v[72:75], v[100:103], v[44:47]\n\t"
  "ds_read_b128 v[132:135], %[vb] offset:896\n\t"
  "v_and_b32 v193, 0xffff0000, v224\n\t"
  "v_mfma_f32_16x16x32_bf16 v[40:43], v[72:75], v[96:99], v[40:43]\n\t"
  "ds_read_b64 v[72:73], %[va] offset:1088\n\t"
  "ds_read_b64 v[74:75], %[va] offset:1096\n\t"
  "v_lshlrev_b32 v249, 16, v227\n\t"
  "v_mfma_f32_16x16x32_bf16 v[60:63], v[76:79], v[112:115], v[60:63]\n\t"
  "v_fma_f32 v161, v193, v230, v231\n\t"
  "v_mfma_f32_16x16x32_bf16 v[56:59], v[76:79], v[108:111], v[56:59]\n\t"
  "ds_read_b128 v[128:131], %[vb] offset:384\n\t"
  "v_mul_f32 v249, %[sD], v249\n\t"
  "v_mfma_f32_16x16x32_bf16 v[52:55], v[76:79], v[104:107], v[52:55]\n\t"
  "v_fma_f32 v161, v161, %[sG], v252\n\t"
  "v_mfma_f32_16x16x32_bf16 v[48:51], v[76:79], v[100:103], v[48:51]\n\t"
  "v_fmac_f32 v193, v161, v249\n\t"
  "v_mfma_f32_16x16x32_bf16 v[44:47], v[76:79], v[96:99], v[44:47]\n\t"
  "ds_read_b64 v[76:77], %[va] offset:1600\n\t"
  "ds_read_b64 v[78:79], %[va] offset:1608\n\t"
  "v_lshlrev_b32 v194, 16, v225\n\t"
  "v_and_b32 v250, 0xffff0000, v226\n\t"
  "v_mfma_f32_16x16x32_bf16 v[60:63], v[80:83], v[108:111], v[60:63]\n\t"
  "s_waitcnt lgkmcnt(14)\n\t"
  "v_fma_f32 v162, v194, v232, v233\n\t"
  "v_mfma_f32_16x16x32_bf16 v[56:59], v[80:83], v[104:107], v[56:59]\n\t"
  "v_mul_f32 v250, %[sD], v250\n\t"
  "v_mfma_f32_16x16x32_bf16 v[52:55], v[80:83], v[100:103], v[52:55]\n\t"
  "v_fma_f32 v162, v162, %[sG], v252\n\t"
  "v_mfma_f32_16x16x32_bf16 v[48:51], v[80:83], v[96:99], v[48:51]\n\t"
  "ds_read_b64 v[80:81], %[va] offset:2112\n\t"
  "ds_read_b64 v[82:83], %[va] offset:2120\n\t"
  "v_fmac_f32 v194, v162, v250\n\t"
  "v_mfma_f32_16x16x32_bf16 v[60:63], v[84:87], v[104:107], v[60:63]\n\t"
  "v_and_b32 v195, 0xffff0000, v225\n\t"
  "v_mfma_f32_16x16x32_bf16 v[56:59], v[84:87], v[100:103], v[56:59]\n\t"
  "v_lshlrev_b32 v251, 16, v226\n\t"
  "v_mfma_f32_16x16x32_bf16 v[52:55], v[84:87], v[96:99], v[52:55]\n\t"
  "ds_read_b64 v[84:85], %[va] offset:2624\n\t"
  "ds_read_b64 v[86:87], %[va] offset:2632\n\t"
  "v_fma_f32 v163, v195, v234, v235\n\t"
  "v_mfma_f32_16x16x32_bf16 v[60:63], v[88:91], v[100:103], v[60:63]\n\t"
  "v_mul_f32 v251, %[sD], v251\n\t"
  "v_mfma_f32_16x16x32_bf16 v[56:59], v[88:91], v[96:99], v[56:59]\n\t"
  "ds_read_b64 v[88:89], %[va] offset:3136\n\t"
  "ds_read_b64 v[90:91], %[va] offset:3144\n\t"
  "v_fma_f32 v163, v163, %[sG], v252\n\t"
  "v_mfma_f32_16x16x32_bf16 v[60:63], v[92:95], v[96:99], v[60:63]\n\t"
  "ds_read_b64 v[92:93], %[va] offset:3648\n\t"
  "ds_read_b64 v[94:95], %[va] offset:3656\n\t"
  "v_fmac_f32 v195, v163, v251\n\t"
  "s_waitcnt lgkmcnt(15)\n\t"
  "v_mfma_f32_16x16x32_bf16 v[60:63], v[64:67], v[156:159], v[60:63]\n\t"
  "v_mfma_f32_16x16x32_bf16 v[56:59], v[64:67], v[152:155], v[56:59]\n\t"
  "ds_read_b128 v[124:127], %[vb] offset:3904\n\t"
  "v_mfma_f32_16x16x32_bf16 v[52:55], v[64:67], v[148:151], v[52:55]\n\t"
  "ds_read_b64 v[236:237], %[vx] offset:512\n\t"
  "v_mfma_f32_16x16x32_bf16 v[48:51], v[64:67], v[144:147], v[48:51]\n\t"
  "ds_read_b64 v[238:239], %[vk] offset:512\n\t"
  "v_mfma_f32_16x16x32_bf16 v[44:47], v[64:67], v[140:143], v[44:47]\n\t"
  "ds_read_b128 v[120:123], %[vb] offset:3392\n\t"
  "s_waitcnt lgkmcnt(15)\n\t"
  "v_mfma_f32_16x16x32_bf16 v[40:43], v[64:67], v[136:139], v[40:43]\n\t"
  "ds_read_b128 v[240:243], %[vs] offset:2048\n\t"
  "v_mfma_f32_16x16x32_bf16 v[36:39], v[64:67], v[132:135], v[36:39]\n\t"
  "ds_read_b128 v[244:247], %[vs] offset:2064\n\t"
  "s_waitcnt lgkmcnt(15)\n\t"
  "v_mfma_f32_16x16x32_bf16 v[32:35], v[64:67], v[128:131], v[32:35]\n\t"
  "ds_read_b64 v[64:65], %[va] offset:128\n\t"
  "ds_read_b64 v[66:67], %[va] offset:136\n\t"
  "ds_read_b128 v[116:119], %[vb] offset:2880\n\t"
  "v_mfma_f32_16x16x32_bf16 v[60:63], v[68:71], v[152:155], v[60:63]\n\t"
  "v_mfma_f32_16x16x32_bf16 v[56:59], v[68:71], v[148:151], v[56:59]\n\t"
  "v_mfma_f32_16x16x32_bf16 v[52:55], v[68:71], v[144:147], v[52:55]\n\t"
  "ds_read_b128 v[112:115], %[vb] offset:2368\n\t"
  "v_mfma_f32_16x16x32_bf16 v[48:51], v[68:71], v[140:143], v[48:51]\n\t"
  "v_mfma_f32_16x16x32_bf16 v[44:47], v[68:71], v[136:139], v[44:47]\n\t"
  "v_mfma_f32_16x16x32_bf16 v[40:43], v[68:71], v[132:135], v[40:43]\n\t"
  "ds_read_b128 v[108:111], %[vb] offset:1856\n\t"
  "v_mfma_f32_16x16x32_bf16 v[36:39], v[68:71], v[128:131], v[36:39]\n\t"
  "ds_read_b64 v[68:69], %[va] offset:640\n\t"
  "ds_read_b64 v[70:71], %[va] offset:648\n\t"
  "s_waitcnt lgkmcnt(11)\n\t"
  "v_lshlrev_b32 v196, 16, v236\n\t"
  "s_waitcnt lgkmcnt(10)\n\t"
  "v_and_b32 v248, 0xffff0000, v239\n\t"
  "v_mfma_f32_16x16x32_bf16 v[60:63], v[72:75], v[148:151], v[60:63]\n\t"
  "s_waitcnt lgkmcnt(8)\n\t"
  "v_fma_f32 v164, v196, v240, v241\n\t"
  "v_mfma_f32_16x16x32_bf16 v[56:59], v[72:75], v[144:147], v[56:59]\n\t"
  "ds_read_b128 v[104:107], %[vb] offset:1344\n\t"
  "v_mul_f32 v248, %[sD], v248\n\t"
  "v_mfma_f32_16x16x32_bf16 v[52:55], v[72:75], v[140:143], v[52:55]\n\t"
  "v_fma_f32 v164, v164, %[sG], v252\n\t"
  "v_mfma_f32_16x16x32_bf16 v[48:51], v[72:75], v[136:139], v[48:51]\n\t"
  "v_fmac_f32 v196, v164, v248\n\t"
  "v_mfma_f32_16x16x32_bf16 v[44:47], v[72:75], v[132:135], v[44:47]\n\t"
  "ds_read_b128 v[100:103], %[vb] offset:832\n\t"
  "v_and_b32 v197, 0xffff0000, v236\n\t"
  "v_mfma_f32_16x16x32_bf16 v[40:43], v[72:75], v[128:131], v[40:43]\n\t"
  "ds_read_b64 v[72:73], %[va] offset:1152\n\t"
  "ds_read_b64 v[74:75], %[va] offset:1160\n\t"
  "v_lshlrev_b32 v249, 16, v239\n\t"
  "v_mfma_f32_16x16x32_bf16 v[60:63], v[76:79], v[144:147], v[60:63]\n\t"
  "v_fma_f32 v165, v197, v242, v243\n\t"
  "v_mfma_f32_16x16x32_bf16 v[56:59], v[76:79], v[140:143], v[56:59]\n\t"
  "ds_read_b128 v[96:99], %[vb] offset:320\n\t"
  "v_mul_f32 v249, %[sD], v249\n\t"
  "v_mfma_f32_16x16x32_bf16 v[52:55], v[76:79], v[136:139], v[52:55]\n\t"
  "v_fma_f32 v165, v165, %[sG], v252\n\t"
  "v_mfma_f32_16x16x32_bf16 v[48:51], v[76:79], v[132:135], v[48:51]\n\t"
  "v_fmac_f32 v197, v165, v249\n\t"
  "v_mfma_f32_16x16x32_bf16 v[44:47], v[76:79], v[128:131], v[44:47]\n\t"
  "ds_read_b64 v[76:77], %[va] offset:1664\n\t"
  "ds_read_b64 v[78:79], %[va] offset:1672\n\t"
  "v_lshlrev_b32 v198, 16, v237\n\t"
  "v_and_b32 v250, 0xffff0000, v238\n\t"
  "v_mfma_f32_16x16x32_bf16 v[60:63], v[80:83], v[140:143], v[60:63]\n\t"
  "s_waitcnt lgkmcnt(14)\n\t"
  "v_fma_f32 v166, v198, v244, v245\n\t"
  "v_mfma_f32_16x16x32_bf16 v[56:59], v[80:83], v[136:139], v[56:59]\n\t"
  "v_mul_f32 v250, %[sD], v250\n\t"
  "v_mfma_f32_16x16x32_bf16 v[52:55], v[80:83], v[132:135], v[52:55]\n\t"
  "v_fma_f32 v166, v166, %[sG], v252\n\t"
  "v_mfma_f32_16x16x32_bf16 v[48:51], v[80:83], v[128:131], v[48:51]\n\t"
  "ds_read_b64 v[80:81], %[va] offset:2176\n\t"
  "ds_read_b64 v[82:83], %[va] offset:2184\n\t"
  "v_fmac_f32 v198, v166, v250\n\t"
  "v_mfma_f32_16x16x32_bf16 v[60:63], v[84:87], v[136:139], v[60:63]\n\t"
  "v_and_b32 v199, 0xffff0000, v237\n\t"
  "v_mfma_f32_16x16x32_bf16 v[56:59], v[84:87], v[132:135], v[56:59]\n\t"
  "v_lshlrev_b32 v251, 16, v238\n\t"
  "v_mfma_f32_16x16x32_bf16 v[52:55], v[84:87], v[128:131], v[52:55]\n\t"
  "ds_read_b64 v[84:85], %[va] offset:2688\n\t"
  "ds_read_b64 v[86:87], %[va] offset:2696\n\t"
  "v_fma_f32 v167, v199, v246, v247\n\t"
  "v_mfma_f32_16x16x32_bf16 v[60:63], v[88:91], v[132:135], v[60:63]\n\t"
  "v_mul_f32 v251, %[sD], v251\n\t"
  "v_mfma_f32_16x16x32_bf16 v[56:59], v[88:91], v[128:131], v[56:59]\n\t"
  "ds_read_b64 v[88:89], %[va] offset:3200\n\t"
  "ds_read_b64 v[90:91], %[va] offset:3208\n\t"
  "v_fma_f32 v167, v167, %[sG], v252\n\t"
  "v_mfma_f32_16x16x32_bf16 v[60:63], v[92:95], v[128:131], v[60:63]\n\t"
  "ds_read_b64 v[92:93], %[va] offset:3712\n\t"
  "ds_read_b64 v[94:95], %[va] offset:3720\n\t"
  "v_fmac_f32 v199, v167, v251\n\t"
  "s_waitcnt lgkmcnt(15)\n\t"
  "v_mfma_f32_16x16x32_bf16 v[60:63], v[64:67], v[124:127], v[60:63]\n\t"
  "v_mfma_f32_16x16x32_bf16 v[56:59], v[64:67], v[120:123], v[56:59]\n\t"
  "ds_read_b128 v[156:159], %[vb] offset:3840\n\t"
  "v_mfma_f32_16x16x32_bf16 v[52:55], v[64:67], v[116:119], v[52:55]\n\t"
  "ds_read_b64 v[224:225], %[vx] offset:1024\n\t"
  "v_mfma_f32_16x16x32_bf16 v[48:51], v[64:67], v[112:115], v[48:51]\n\t"
  "ds_read_b64 v[226:227], %[vk] offset:1024\n\t"
  "v_mfma_f32_16x16x32_bf16 v[44:47], v[64:67], v[108:111], v[44:47]\n\t"
  "ds_read_b128 v[152:155], %[vb] offset:3328\n\t"
  "s_waitcnt lgkmcnt(15)\n\t"
  "v_mfma_f32_16x16x32_bf16 v[40:43], v[64:67], v[104:107], v[40:43]\n\t"
  "ds_read_b128 v[228:231], %[vs] offset:4096\n\t"
  "v_mfma_f32_16x16x32_bf16 v[36:39], v[64:67], v[100:103], v[36:39]\n\t"
  "ds_read_b128 v[232:235], %[vs] offset:4112\n\t"
  "s_waitcnt lgkmcnt(15)\n\t"
  "v_mfma_f32_16x16x32_bf16 v[32:35], v[64:67], v[96:99], v[32:35]\n\t"
  "ds_read_b64 v[64:65], %[va] offset:192\n\t"
  "ds_read_b64 v[66:67], %[va] offset:200\n\t"
  "ds_read_b128 v[148:151], %[vb] offset:2816\n\t"
  "v_mfma_f32_16x16x32_bf16 v[60:63], v[68:71], v[120:123], v[60:63]\n\t"
  "v_mfma_f32_16x16x32_bf16 v[56:59], v[68:71], v[116:119], v[56:59]\n\t"
  "v_mfma_f32_16x16x32_bf16 v[52:55], v[68:71], v[112:115], v[52:55]\n\t"
  "ds_read_b128 v[144:147], %[vb] offset:2304\n\t"
  "v_mfma_f32_16x16x32_bf16 v[48:51], v[68:71], v[108:111], v[48:51]\n\t"
  "v_mfma_f32_16x16x32_bf16 v[44:47], v[68:71], v[104:107], v[44:47]\n\t"
  "v_mfma_f32_16x16x32_bf16 v[40:43], v[68:71], v[100:103], v[40:43]\n\t"
  "ds_read_b128 v[140:143], %[vb] offset:1792\n\t"
  "v_mfma_f32_16x16x32_bf16 v[36:39], v[68:71], v[96:99], v[36:39]\n\t"
  "ds_read_b64 v[68:69], %[va] offset:704\n\t"
  "ds_read_b64 v[70:71], %[va] offset:712\n\t"
  "s_waitcnt lgkmcnt(11)\n\t"
  "v_lshlrev_b32 v200, 16, v224\n\t"
  "s_waitcnt lgkmcnt(10)\n\t"
  "v_and_b32 v248, 0xffff0000, v227\n\t"
  "v_mfma_f32_16x16x32_bf16 v[60:63], v[72:75], v[116:119], v[60:63]\n\t"
  "s_waitcnt lgkmcnt(8)\n\t"
  "v_fma_f32 v168, v200, v228, v229\n\t"
  "v_mfma_f32_16x16x32_bf16 v[56:59], v[72:75], v[112:115], v[56:59]\n\t"
  "ds_read_b128 v[136:139], %[vb] offset:1280\n\t"
  "v_mul_f32 v248, %[sD], v248\n\t"
  "v_mfma_f32_16x16x32_bf16 v[52:55], v[72:75], v[108:111], v[52:55]\n\t"
  "v_fma_f32 v168, v168, %[sG], v252\n\t"
  "v_mfma_f32_16x16x32_bf16 v[48:51], v[72:75], v[104:107], v[48:51]\n\t"
  "v_fmac_f32 v200, v168, v248\n\t"
  "v_mfma_f32_16x16x32_bf16 v[44:47], v[72:75], v[100:103], v[44:47]\n\t"
  "ds_read_b128 v[132:135], %[vb] offset:768\n\t"
  "v_and_b32 v201, 0xffff0000, v224\n\t"
  "v_mfma_f32_16x16x32_bf16 v[40:43], v[72:75], v[96:99], v[40:43]\n\t"
  "ds_read_b64 v[72:73], %[va] offset:1216\n\t"
  "ds_read_b64 v[74:75], %[va] offset:1224\n\t"
  "v_lshlrev_b32 v249, 16, v227\n\t"
  "v_mfma_f32_16x16x32_bf16 v[60:63], v[76:79], v[112:115], v[60:63]\n\t"
  "v_fma_f32 v169, v201, v230, v231\n\t"
  "v_mfma_f32_16x16x32_bf16 v[56:59], v[76:79], v[108:111], v[56:59]\n\t"
  "ds_read_b128 v[128:131], %[vb] offset:256\n\t"
  "v_mul_f32 v249, %[sD], v249\n\t"
  "v_mfma_f32_16x16x32_bf16 v[52:55], v[76:79], v[104:107], v[52:55]\n\t"
  "v_fma_f32 v169, v169, %[sG], v252\n\t"
  "v_mfma_f32_16x16x32_bf16 v[48:51], v[76:79], v[100:103], v[48:51]\n\t"
  "v_fmac_f32 v201, v169, v249\n\t"
  "v_mfma_f32_16x16x32_bf16 v[44:47], v[76:79], v[96:99], v[44:47]\n\t"
  "ds_read_b64 v[76:77], %[va] offset:1728\n\t"
  "ds_read_b64 v[78:79], %[va] offset:1736\n\t"
  "v_lshlrev_b32 v202, 16, v225\n\t"
  "v_and_b32 v250, 0xffff0000, v226\n\t"
  "v_mfma_f32_16x16x32_bf16 v[60:63], v[80:83], v[108:111], v[60:63]\n\t"
  "s_waitcnt lgkmcnt(14)\n\t"
  "v_fma_f32 v170, v202, v232, v233\n\t"
  "v_mfma_f32_16x16x32_bf16 v[56:59], v[80:83], v[104:107], v[56:59]\n\t"
  "v_mul_f32 v250, %[sD], v250\n\t"
  "v_mfma_f32_16x16x32_bf16 v[52:55], v[80:83], v[100:103], v[52:55]\n\t"
  "v_fma_f32 v170, v170, %[sG], v252\n\t"
  "v_mfma_f32_16x16x32_bf16 v[48:51], v[80:83], v[96:99], v[48:51]\n\t"
  "ds_read_b64 v[80:81], %[va] offset:2240\n\t"
  "ds_read_b64 v[82:83], %[va] offset:2248\n\t"
  "v_fmac_f32 v202, v170, v250\n\t"
  "v_mfma_f32_16x16x32_bf16 v[60:63], v[84:87], v[104:107], v[60:63]\n\t"
  "v_and_b32 v203, 0xffff0000, v225\n\t"
  "v_mfma_f32_16x16x32_bf16 v[56:59], v[84:87], v[100:103], v[56:59]\n\t"
  "v_lshlrev_b32 v251, 16, v226\n\t"
  "v_mfma_f32_16x16x32_bf16 v[52:55], v[84:87], v[96:99], v[52:55]\n\t"
  "ds_read_b64 v[84:85], %[va] offset:2752\n\t"
  "ds_read_b64 v[86:87], %[va] offset:2760\n\t"
  "v_fma_f32 v171, v203, v234, v235\n\t"
  "v_mfma_f32_16x16x32_bf16 v[60:63], v[88:91], v[100:103], v[60:63]\n\t"
  "v_mul_f32 v251, %[sD], v251\n\t"
  "v_mfma_f32_16x16x32_bf16 v[56:59], v[88:91], v[96:99], v[56:59]\n\t"
  "ds_read_b64 v[88:89], %[va] offset:3264\n\t"
  "ds_read_b64 v[90:91], %[va] offset:3272\n\t"
  "v_fma_f32 v171, v171, %[sG], v252\n\t"
  "v_mfma_f32_16x16x32_bf16 v[60:63], v[92:95], v[96:99], v[60:63]\n\t"
  "ds_read_b64 v[92:93], %[va] offset:3776\n\t"
  "ds_read_b64 v[94:95], %[va] offset:3784\n\t"
  "v_fmac_f32 v203, v171, v251\n\t"
  "s_waitcnt lgkmcnt(15)\n\t"
  "v_mfma_f32_16x16x32_bf16 v[60:63], v[64:67], v[156:159], v[60:63]\n\t"
  "v_mfma_f32_16x16x32_bf16 v[56:59], v[64:67], v[152:155], v[56:59]\n\t"
  "ds_read_b128 v[124:127], %[vb] offset:3776\n\t"
  "v_mfma_f32_16x16x32_bf16 v[52:55], v[64:67], v[148:151], v[52:55]\n\t"
  "ds_read_b64 v[236:237], %[vx] offset:1536\n\t"
  "v_mfma_f32_16x16x32_bf16 v[48:51], v[64:67], v[144:147], v[48:51]\n\t"
  "ds_read_b64 v[238:239], %[vk] offset:1536\n\t"
  "v_mfma_f32_16x16x32_bf16 v[44:47], v[64:67], v[140:143], v[44:47]\n\t"
  "ds_read_b128 v[120:123], %[vb] offset:3264\n\t"
  "s_waitcnt lgkmcnt(15)\n\t"
  "v_mfma_f32_16x16x32_bf16 v[40:43], v[64:67], v[136:139], v[40:43]\n\t"
  "ds_read_b128 v[240:243], %[vs] offset:6144\n\t"
  "v_mfma_f32_16x16x32_bf16 v[36:39], v[64:67], v[132:135], v[36:39]\n\t"
  "ds_read_b128 v[244:247], %[vs] offset:6160\n\t"
  "s_waitcnt lgkmcnt(15)\n\t"
  "v_mfma_f32_16x16x32_bf16 v[32:35], v[64:67], v[128:131], v[32:35]\n\t"
  "ds_read_b64 v[64:65], %[va] offset:256\n\t"
  "ds_read_b64 v[66:67], %[va] offset:264\n\t"
  "ds_read_b128 v[116:119], %[vb] offset:2752\n\t"
  "v_mfma_f32_16x16x32_bf16 v[60:63], v[68:71], v[152:155], v[60:63]\n\t"
  "v_mfma_f32_16x16x32_bf16 v[56:59], v[68:71], v[148:151], v[56:59]\n\t"
  "v_mfma_f32_16x16x32_bf16 v[52:55], v[68:71], v[144:147], v[52:55]\n\t"
  "ds_read_b128 v[112:115], %[vb] offset:2240\n\t"
  "v_mfma_f32_16x16x32_bf16 v[48:51], v[68:71], v[140:143], v[48:51]\n\t"
  "v_mfma_f32_16x16x32_bf16 v[44:47], v[68:71], v[136:139], v[44:47]\n\t"
  "v_mfma_f32_16x16x32_bf16 v[40:43], v[68:71], v[132:135], v[40:43]\n\t"
  "ds_read_b128 v[108:111], %[vb] offset:1728\n\t"
  "v_mfma_f32_16x16x32_bf16 v[36:39], v[68:71], v[128:131], v[36:39]\n\t"
  "ds_read_b64 v[68:69], %[va] offset:768\n\t"
  "ds_read_b64 v[70:71], %[va] offset:776\n\t"
  "s_waitcnt lgkmcnt(11)\n\t"
  "v_lshlrev_b32 v204, 16, v236\n\t"
  "s_waitcnt lgkmcnt(10)\n\t"
  "v_and_b32 v248, 0xffff0000, v239\n\t"
  "v_mfma_f32_16x16x32_bf16 v[60:63], v[72:75], v[148:151], v[60:63]\n\t"
  "s_waitcnt lgkmcnt(8)\n\t"
  "v_fma_f32 v172, v204, v240, v241\n\t"
  "v_mfma_f32_16x16x32_bf16 v[56:59], v[72:75], v[144:147], v[56:59]\n\t"
  "ds_read_b128 v[104:107], %[vb] offset:1216\n\t"
  "v_mul_f32 v248, %[sD], v248\n\t"
  "v_mfma_f32_16x16x32_bf16 v[52:55], v[72:75], v[140:143], v[52:55]\n\t"
  "v_fma_f32 v172, v172, %[sG], v252\n\t"
  "v_mfma_f32_16x16x32_bf16 v[48:51], v[72:75], v[136:139], v[48:51]\n\t"
  "v_fmac_f32 v204, v172, v248\n\t"
  "v_mfma_f32_16x16x32_bf16 v[44:47], v[72:75], v[132:135], v[44:47]\n\t"
  "ds_read_b128 v[100:103], %[vb] offset:704\n\t"
  "v_and_b32 v205, 0xffff0000, v236\n\t"
  "v_mfma_f32_16x16x32_bf16 v[40:43], v[72:75], v[128:131], v[40:43]\n\t"
  "ds_read_b64 v[72:73], %[va] offset:1280\n\t"
  "ds_read_b64 v[74:75], %[va] offset:1288\n\t"
  "v_lshlrev_b32 v249, 16, v239\n\t"
  "v_mfma_f32_16x16x32_bf16 v[60:63], v[76:79], v[144:147], v[60:63]\n\t"
  "v_fma_f32 v173, v205, v242, v243\n\t"
  "v_mfma_f32_16x16x32_bf16 v[56:59], v[76:79], v[140:143], v[56:59]\n\t"
  "ds_read_b128 v[96:99], %[vb] offset:192\n\t"
  "v_mul_f32 v249, %[sD], v249\n\t"
  "v_mfma_f32_16x16x32_bf16 v[52:55], v[76:79], v[136:139], v[52:55]\n\t"
  "v_fma_f32 v173, v173, %[sG], v252\n\t"
  "v_mfma_f32_16x16x32_bf16 v[48:51], v[76:79], v[132:135], v[48:51]\n\t"
  "v_fmac_f32 v205, v173, v249\n\t"
  "v_mfma_f32_16x16x32_bf16 v[44:47], v[76:79], v[128:131], v[44:47]\n\t"
  "ds_read_b64 v[76:77], %[va] offset:1792\n\t"
  "ds_read_b64 v[78:79], %[va] offset:1800\n\t"
  "v_lshlrev_b32 v206, 16, v237\n\t"
  "v_and_b32 v250, 0xffff0000, v238\n\t"
  "v_mfma_f32_16x16x32_bf16 v[60:63], v[80:83], v[140:143], v[60:63]\n\t"
  "s_waitcnt lgkmcnt(14)\n\t"
  "v_fma_f32 v174, v206, v244, v245\n\t"
  "v_mfma_f32_16x16x32_bf16 v[56:59], v[80:83], v[136:139], v[56:59]\n\t"
  "v_mul_f32 v250, %[sD], v250\n\t"
  "v_mfma_f32_16x16x32_bf16 v[52:55], v[80:83], v[132:135], v[52:55]\n\t"
  "v_fma_f32 v174, v174, %[sG], v252\n\t"
  "v_mfma_f32_16x16x32_bf16 v[48:51], v[80:83], v[128:131], v[48:51]\n\t"
  "ds_read_b64 v[80:81], %[va] offset:2304\n\t"
  "ds_read_b64 v[82:83], %[va] offset:2312\n\t"
  "v_fmac_f32 v206, v174, v250\n\t"
  "v_mfma_f32_16x16x32_bf16 v[60:63], v[84:87], v[136:139], v[60:63]\n\t"
  "v_and_b32 v207, 0xffff0000, v237\n\t"
  "v_mfma_f32_16x16x32_bf16 v[56:59], v[84:87], v[132:135], v[56:59]\n\t"
  "v_lshlrev_b32 v251, 16, v238\n\t"
  "v_mfma_f32_16x16x32_bf16 v[52:55], v[84:87], v[128:131], v[52:55]\n\t"
  "ds_read_b64 v[84:85], %[va] offset:2816\n\t"
  "ds_read_b64 v[86:87], %[va] offset:2824\n\t"
  "v_fma_f32 v175, v207, v246, v247\n\t"
  "v_mfma_f32_16x16x32_bf16 v[60:63], v[88:91], v[132:135], v[60:63]\n\t"
  "v_mul_f32 v251, %[sD], v251\n\t"
  "v_mfma_f32_16x16x32_bf16 v[56:59], v[88:91], v[128:131], v[56:59]\n\t"
  "ds_read_b64 v[88:89], %[va] offset:3328\n\t"
  "ds_read_b64 v[90:91], %[va] offset:3336\n\t"
  "v_fma_f32 v175, v175, %[sG], v252\n\t"
  "v_mfma_f32_16x16x32_bf16 v[60:63], v[92:95], v[128:131], v[60:63]\n\t"
  "ds_read_b64 v[92:93], %[va] offset:3840\n\t"
  "ds_read_b64 v[94:95], %[va] offset:3848\n\t"
  "v_fmac_f32 v207, v175, v251\n\t"
  "s_waitcnt lgkmcnt(15)\n\t"
  "v_mfma_f32_16x16x32_bf16 v[60:63], v[64:67], v[124:127], v[60:63]\n\t"
  "v_mfma_f32_16x16x32_bf16 v[56:59], v[64:67], v[120:123], v[56:59]\n\t"
  "ds_read_b128 v[156:159], %[vb] offset:3712\n\t"
  "v_mfma_f32_16x16x32_bf16 v[52:55], v[64:67], v[116:119], v[52:55]\n\t"
  "ds_read_b64 v[224:225], %[vx] offset:2048\n\t"
  "v_mfma_f32_16x16x32_bf16 v[48:51], v[64:67], v[112:115], v[48:51]\n\t"
  "ds_read_b64 v[226:227], %[vk] offset:2048\n\t"
  "v_mfma_f32_16x16x32_bf16 v[44:47], v[64:67], v[108:111], v[44:47]\n\t"
  "ds_read_b128 v[152:155], %[vb] offset:3200\n\t"
  "s_waitcnt lgkmcnt(15)\n\t"
  "v_mfma_f32_16x16x32_bf16 v[40:43], v[64:67], v[104:107], v[40:43]\n\t"
  "ds_read_b128 v[228:231], %[vs] offset:8192\n\t"
  "v_mfma_f32_16x16x32_bf16 v[36:39], v[64:67], v[100:103], v[36:39]\n\t"
  "ds_read_b128 v[232:235], %[vs] offset:8208\n\t"
  "s_waitcnt lgkmcnt(15)\n\t"
  "v_mfma_f32_16x16x32_bf16 v[32:35], v[64:67], v[96:99], v[32:35]\n\t"
  "ds_read_b64 v[64:65], %[va] offset:320\n\t"
  "ds_read_b64 v[66:67], %[va] offset:328\n\t"
  "ds_read_b128 v[148:151], %[vb] offset:2688\n\t"
  "v_mfma_f32_16x16x32_bf16 v[60:63], v[68:71], v[120:123], v[60:63]\n\t"
  "v_mfma_f32_16x16x32_bf16 v[56:59], v[68:71], v[116:119], v[56:59]\n\t"
  "v_mfma_f32_16x16x32_bf16 v[52:55], v[68:71], v[112:115], v[52:55]\n\t"
  "ds_read_b128 v[144:147], %[vb] offset:2176\n\t"
  "v_mfma_f32_16x16x32_bf16 v[48:51], v[68:71], v[108:111], v[48:51]\n\t"
  "v_mfma_f32_16x16x32_bf16 v[44:47], v[68:71], v[104:107], v[44:47]\n\t"
  "v_mfma_f32_16x16x32_bf16 v[40:43], v[68:71], v[100:103], v[40:43]\n\t"
  "ds_read_b128 v[140:143], %[vb] offset:1664\n\t"
  "v_mfma_f32_16x16x32_bf16 v[36:39], v[68:71], v[96:99], v[36:39]\n\t"
  "ds_read_b64 v[68:69], %[va] offset:832\n\t"
  "ds_read_b64 v[70:71], %[va] offset:840\n\t"
  "s_waitcnt lgkmcnt(11)\n\t"
  "v_lshlrev_b32 v208, 16, v224\n\t"
  "s_waitcnt lgkmcnt(10)\n\t"
  "v_and_b32 v248, 0xffff0000, v227\n\t"
  "v_mfma_f32_16x16x32_bf16 v[60:63], v[72:75], v[116:119], v[60:63]\n\t"
  "s_waitcnt lgkmcnt(8)\n\t"
  "v_fma_f32 v176, v208, v228, v229\n\t"
  "v_mfma_f32_16x16x32_bf16 v[56:59], v[72:75], v[112:115], v[56:59]\n\t"
  "ds_read_b128 v[136:139], %[vb] offset:1152\n\t"
  "v_mul_f32 v248, %[sD], v248\n\t"
  "v_mfma_f32_16x16x32_bf16 v[52:55], v[72:75], v[108:111], v[52:55]\n\t"
  "v_fma_f32 v176, v176, %[sG], v252\n\t"
  "v_mfma_f32_16x16x32_bf16 v[48:51], v[72:75], v[104:107], v[48:51]\n\t"
  "v_fmac_f32 v208, v176, v248\n\t"
  "v_mfma_f32_16x16x32_bf16 v[44:47], v[72:75], v[100:103], v[44:47]\n\t"
  "ds_read_b128 v[132:135], %[vb] offset:640\n\t"
  "v_and_b32 v209, 0xffff0000, v224\n\t"
  "v_mfma_f32_16x16x32_bf16 v[40:43], v[72:75], v[96:99], v[40:43]\n\t"
  "ds_read_b64 v[72:73], %[va] offset:1344\n\t"
  "ds_read_b64 v[74:75], %[va] offset:1352\n\t"
  "v_lshlrev_b32 v249, 16, v227\n\t"
  "v_mfma_f32_16x16x32_bf16 v[60:63], v[76:79], v[112:115], v[60:63]\n\t"
  "v_fma_f32 v177, v209, v230, v231\n\t"
  "v_mfma_f32_16x16x32_bf16 v[56:59], v[76:79], v[108:111], v[56:59]\n\t"
  "ds_read_b128 v[128:131], %[vb] offset:128\n\t"
  "v_mul_f32 v249, %[sD], v249\n\t"
  "v_mfma_f32_16x16x32_bf16 v[52:55], v[76:79], v[104:107], v[52:55]\n\t"
  "v_fma_f32 v177, v177, %[sG], v252\n\t"
  "v_mfma_f32_16x16x32_bf16 v[48:51], v[76:79], v[100:103], v[48:51]\n\t"
  "v_fmac_f32 v209, v177, v249\n\t"
  "v_mfma_f32_16x16x32_bf16 v[44:47], v[76:79], v[96:99], v[44:47]\n\t"
  "ds_read_b64 v[76:77], %[va] offset:1856\n\t"
  "ds_read_b64 v[78:79], %[va] offset:1864\n\t"
  "v_lshlrev_b32 v210, 16, v225\n\t"
  "v_and_b32 v250, 0xffff0000, v226\n\t"
  "v_mfma_f32_16x16x32_bf16 v[60:63], v[80:83], v[108:111], v[60:63]\n\t"
  "s_waitcnt lgkmcnt(14)\n\t"
  "v_fma_f32 v178, v210, v232, v233\n\t"
  "v_mfma_f32_16x16x32_bf16 v[56:59], v[80:83], v[104:107], v[56:59]\n\t"
  "v_mul_f32 v250, %[sD], v250\n\t"
  "v_mfma_f32_16x16x32_bf16 v[52:55], v[80:83], v[100:103], v[52:55]\n\t"
  "v_fma_f32 v178, v178, %[sG], v252\n\t"
  "v_mfma_f32_16x16x32_bf16 v[48:51], v[80:83], v[96:99], v[48:51]\n\t"
  "ds_read_b64 v[80:81], %[va] offset:2368\n\t"
  "ds_read_b64 v[82:83], %[va] offset:2376\n\t"
  "v_fmac_f32 v210, v178, v250\n\t"
  "v_mfma_f32_16x16x32_bf16 v[60:63], v[84:87], v[104:107], v[60:63]\n\t"
  "v_and_b32 v211, 0xffff0000, v225\n\t"
  "v_mfma_f32_16x16x32_bf16 v[56:59], v[84:87], v[100:103], v[56:59]\n\t"
  "v_lshlrev_b32 v251, 16, v226\n\t"
  "v_mfma_f32_16x16x32_bf16 v[52:55], v[84:87], v[96:99], v[52:55]\n\t"
  "ds_read_b64 v[84:85], %[va] offset:2880\n\t"
  "ds_read_b64 v[86:87], %[va] offset:2888\n\t"
  "v_fma_f32 v179, v211, v234, v235\n\t"
  "v_mfma_f32_16x16x32_bf16 v[60:63], v[88:91], v[100:103], v[60:63]\n\t"
  "v_mul_f32 v251, %[sD], v251\n\t"
  "v_mfma_f32_16x16x32_bf16 v[56:59], v[88:91], v[96:99], v[56:59]\n\t"
  "ds_read_b64 v[88:89], %[va] offset:3392\n\t"
  "ds_read_b64 v[90:91], %[va] offset:3400\n\t"
  "v_fma_f32 v179, v179, %[sG], v252\n\t"
  "v_mfma_f32_16x16x32_bf16 v[60:63], v[92:95], v[96:99], v[60:63]\n\t"
  "ds_read_b64 v[92:93], %[va] offset:3904\n\t"
  "ds_read_b64 v[94:95], %[va] offset:3912\n\t"
  "v_fmac_f32 v211, v179, v251\n\t"
  "s_waitcnt lgkmcnt(15)\n\t"
  "v_mfma_f32_16x16x32_bf16 v[60:63], v[64:67], v[156:159], v[60:63]\n\t"
  "v_mfma_f32_16x16x32_bf16 v[56:59], v[64:67], v[152:155], v[56:59]\n\t"
  "ds_read_b128 v[124:127], %[vb] offset:3648\n\t"
  "v_mfma_f32_16x16x32_bf16 v[52:55], v[64:67], v[148:151], v[52:55]\n\t"
  "ds_read_b64 v[236:237], %[vx] offset:2560\n\t"
  "v_mfma_f32_16x16x32_bf16 v[48:51], v[64:67], v[144:147], v[48:51]\n\t"
  "ds_read_b64 v[238:239], %[vk] offset:2560\n\t"
  "v_mfma_f32_16x16x32_bf16 v[44:47], v[64:67], v[140:143], v[44:47]\n\t"
  "ds_read_b128 v[120:123], %[vb] offset:3136\n\t"
  "s_waitcnt lgkmcnt(15)\n\t"
  "v_mfma_f32_16x16x32_bf16 v[40:43], v[64:67], v[136:139], v[40:43]\n\t"
  "ds_read_b128 v[240:243], %[vs] offset:10240\n\t"
  "v_mfma_f32_16x16x32_bf16 v[36:39], v[64:67], v[132:135], v[36:39]\n\t"
  "ds_read_b128 v[244:247], %[vs] offset:10256\n\t"
  "s_waitcnt lgkmcnt(15)\n\t"
  "v_mfma_f32_16x16x32_bf16 v[32:35], v[64:67], v[128:131], v[32:35]\n\t"
  "ds_read_b64 v[64:65], %[va] offset:384\n\t"
  "ds_read_b64 v[66:67], %[va] offset:392\n\t"
  "ds_read_b128 v[116:119], %[vb] offset:2624\n\t"
  "v_mfma_f32_16x16x32_bf16 v[60:63], v[68:71], v[152:155], v[60:63]\n\t"
  "v_mfma_f32_16x16x32_bf16 v[56:59], v[68:71], v[148:151], v[56:59]\n\t"
  "v_mfma_f32_16x16x32_bf16 v[52:55], v[68:71], v[144:147], v[52:55]\n\t"
  "ds_read_b128 v[112:115], %[vb] offset:2112\n\t"
  "v_mfma_f32_16x16x32_bf16 v[48:51], v[68:71], v[140:143], v[48:51]\n\t"
  "v_mfma_f32_16x16x32_bf16 v[44:47], v[68:71], v[136:139], v[44:47]\n\t"
  "v_mfma_f32_16x16x32_bf16 v[40:43], v[68:71], v[132:135], v[40:43]\n\t"
  "ds_read_b128 v[108:111], %[vb] offset:1600\n\t"
  "v_mfma_f32_16x16x32_bf16 v[36:39], v[68:71], v[128:131], v[36:39]\n\t"
  "ds_read_b64 v[68:69], %[va] offset:896\n\t"
  "ds_read_b64 v[70:71], %[va] offset:904\n\t"
  "s_waitcnt lgkmcnt(11)\n\t"
  "v_lshlrev_b32 v212, 16, v236\n\t"
  "s_waitcnt lgkmcnt(10)\n\t"
  "v_and_b32 v248, 0xffff0000, v239\n\t"
  "v_mfma_f32_16x16x32_bf16 v[60:63], v[72:75], v[148:151], v[60:63]\n\t"
  "s_waitcnt lgkmcnt(8)\n\t"
  "v_fma_f32 v180, v212, v240, v241\n\t"
  "v_mfma_f32_16x16x32_bf16 v[56:59], v[72:75], v[144:147], v[56:59]\n\t"
  "ds_read_b128 v[104:107], %[vb] offset:1088\n\t"
  "v_mul_f32 v248, %[sD], v248\n\t"
  "v_mfma_f32_16x16x32_bf16 v[52:55], v[72:75], v[140:143], v[52:55]\n\t"
  "v_fma_f32 v180, v180, %[sG], v252\n\t"
  "v_mfma_f32_16x16x32_bf16 v[48:51], v[72:75], v[136:139], v[48:51]\n\t"
  "v_fmac_f32 v212, v180, v248\n\t"
  "v_mfma_f32_16x16x32_bf16 v[44:47], v[72:75], v[132:135], v[44:47]\n\t"
  "ds_read_b128 v[100:103], %[vb] offset:576\n\t"
  "v_and_b32 v213, 0xffff0000, v236\n\t"
  "v_mfma_f32_16x16x32_bf16 v[40:43], v[72:75], v[128:131], v[40:43]\n\t"
  "ds_read_b64 v[72:73], %[va] offset:1408\n\t"
  "ds_read_b64 v[74:75], %[va] offset:1416\n\t"
  "v_lshlrev_b32 v249, 16, v239\n\t"
  "v_mfma_f32_16x16x32_bf16 v[60:63], v[76:79], v[144:147], v[60:63]\n\t"
  "v_fma_f32 v181, v213, v242, v243\n\t"
  "v_mfma_f32_16x16x32_bf16 v[56:59], v[76:79], v[140:143], v[56:59]\n\t"
  "ds_read_b128 v[96:99], %[vb] offset:64\n\t"
  "v_mul_f32 v249, %[sD], v249\n\t"
  "v_mfma_f32_16x16x32_bf16 v[52:55], v[76:79], v[136:139], v[52:55]\n\t"
  "v_fma_f32 v181, v181, %[sG], v252\n\t"
  "v_mfma_f32_16x16x32_bf16 v[48:51], v[76:79], v[132:135], v[48:51]\n\t"
  "v_fmac_f32 v213, v181, v249\n\t"
  "v_mfma_f32_16x16x32_bf16 v[44:47], v[76:79], v[128:131], v[44:47]\n\t"
  "ds_read_b64 v[76:77], %[va] offset:1920\n\t"
  "ds_read_b64 v[78:79], %[va] offset:1928\n\t"
  "v_lshlrev_b32 v214, 16, v237\n\t"
  "v_and_b32 v250, 0xffff0000, v238\n\t"
  "v_mfma_f32_16x16x32_bf16 v[60:63], v[80:83], v[140:143], v[60:63]\n\t"
  "s_waitcnt lgkmcnt(14)\n\t"
  "v_fma_f32 v182, v214, v244, v245\n\t"
  "v_mfma_f32_16x16x32_bf16 v[56:59], v[80:83], v[136:139], v[56:59]\n\t"
  "v_mul_f32 v250, %[sD], v250\n\t"
  "v_mfma_f32_16x16x32_bf16 v[52:55], v[80:83], v[132:135], v[52:55]\n\t"
  "v_fma_f32 v182, v182, %[sG], v252\n\t"
  "v_mfma_f32_16x16x32_bf16 v[48:51], v[80:83], v[128:131], v[48:51]\n\t"
  "ds_read_b64 v[80:81], %[va] offset:2432\n\t"
  "ds_read_b64 v[82:83], %[va] offset:2440\n\t"
  "v_fmac_f32 v214, v182, v250\n\t"
  "v_mfma_f32_16x16x32_bf16 v[60:63], v[84:87], v[136:139], v[60:63]\n\t"
  "v_and_b32 v215, 0xffff0000, v237\n\t"
  "v_mfma_f32_16x16x32_bf16 v[56:59], v[84:87], v[132:135], v[56:59]\n\t"
  "v_lshlrev_b32 v251, 16, v238\n\t"
  "v_mfma_f32_16x16x32_bf16 v[52:55], v[84:87], v[128:131], v[52:55]\n\t"
  "ds_read_b64 v[84:85], %[va] offset:2944\n\t"
  "ds_read_b64 v[86:87], %[va] offset:2952\n\t"
  "v_fma_f32 v183, v215, v246, v247\n\t"
  "v_mfma_f32_16x16x32_bf16 v[60:63], v[88:91], v[132:135], v[60:63]\n\t"
  "v_mul_f32 v251, %[sD], v251\n\t"
  "v_mfma_f32_16x16x32_bf16 v[56:59], v[88:91], v[128:131], v[56:59]\n\t"
  "ds_read_b64 v[88:89], %[va] offset:3456\n\t"
  "ds_read_b64 v[90:91], %[va] offset:3464\n\t"
  "v_fma_f32 v183, v183, %[sG], v252\n\t"
  "v_mfma_f32_16x16x32_bf16 v[60:63], v[92:95], v[128:131], v[60:63]\n\t"
  "ds_read_b64 v[92:93], %[va] offset:3968\n\t"
  "ds_read_b64 v[94:95], %[va] offset:3976\n\t"
  "v_fmac_f32 v215, v183, v251\n\t"
  "s_waitcnt lgkmcnt(15)\n\t"
  "v_mfma_f32_16x16x32_bf16 v[60:63], v[64:67], v[124:127], v[60:63]\n\t"
  "v_mfma_f32_16x16x32_bf16 v[56:59], v[64:67], v[120:123], v[56:59]\n\t"
  "ds_read_b128 v[156:159], %[vb] offset:3584\n\t"
  "v_mfma_f32_16x16x32_bf16 v[52:55], v[64:67], v[116:119], v[52:55]\n\t"
  "ds_read_b64 v[224:225], %[vx] offset:3072\n\t"
  "v_mfma_f32_16x16x32_bf16 v[48:51], v[64:67], v[112:115], v[48:51]\n\t"
  "ds_read_b64 v[226:227], %[vk] offset:3072\n\t"
  "v_mfma_f32_16x16x32_bf16 v[44:47], v[64:67], v[108:111], v[44:47]\n\t"
  "ds_read_b128 v[152:155], %[vb] offset:3072\n\t"
  "s_waitcnt lgkmcnt(15)\n\t"
  "v_mfma_f32_16x16x32_bf16 v[40:43], v[64:67], v[104:107], v[40:43]\n\t"
  "ds_read_b128 v[228:231], %[vs] offset:12288\n\t"
  "v_mfma_f32_16x16x32_bf16 v[36:39], v[64:67], v[100:103], v[36:39]\n\t"
  "ds_read_b128 v[232:235], %[vs] offset:12304\n\t"
  "s_waitcnt lgkmcnt(15)\n\t"
  "v_mfma_f32_16x16x32_bf16 v[32:35], v[64:67], v[96:99], v[32:35]\n\t"
  "ds_read_b64 v[64:65], %[va] offset:448\n\t"
  "ds_read_b64 v[66:67], %[va] offset:456\n\t"
  "ds_read_b128 v[148:151], %[vb] offset:2560\n\t"
  "v_mfma_f32_16x16x32_bf16 v[60:63], v[68:71], v[120:123], v[60:63]\n\t"
  "v_mfma_f32_16x16x32_bf16 v[56:59], v[68:71], v[116:119], v[56:59]\n\t"
  "v_mfma_f32_16x16x32_bf16 v[52:55], v[68:71], v[112:115], v[52:55]\n\t"
  "ds_read_b128 v[144:147], %[vb] offset:2048\n\t"
  "v_mfma_f32_16x16x32_bf16 v[48:51], v[68:71], v[108:111], v[48:51]\n\t"
  "v_mfma_f32_16x16x32_bf16 v[44:47], v[68:71], v[104:107], v[44:47]\n\t"
  "v_mfma_f32_16x16x32_bf16 v[40:43], v[68:71], v[100:103], v[40:43]\n\t"
  "ds_read_b128 v[140:143], %[vb] offset:1536\n\t"
  "v_mfma_f32_16x16x32_bf16 v[36:39], v[68:71], v[96:99], v[36:39]\n\t"
  "ds_read_b64 v[68:69], %[va] offset:960\n\t"
  "ds_read_b64 v[70:71], %[va] offset:968\n\t"
  "s_waitcnt lgkmcnt(11)\n\t"
  "v_lshlrev_b32 v216, 16, v224\n\t"
  "s_waitcnt lgkmcnt(10)\n\t"
  "v_and_b32 v248, 0xffff0000, v227\n\t"
  "v_mfma_f32_16x16x32_bf16 v[60:63], v[72:75], v[116:119], v[60:63]\n\t"
  "s_waitcnt lgkmcnt(8)\n\t"
  "v_fma_f32 v184, v216, v228, v229\n\t"
  "v_mfma_f32_16x16x32_bf16 v[56:59], v[72:75], v[112:115], v[56:59]\n\t"
  "ds_read_b128 v[136:139], %[vb] offset:1024\n\t"
  "v_mul_f32 v248, %[sD], v248\n\t"
  "v_mfma_f32_16x16x32_bf16 v[52:55], v[72:75], v[108:111], v[52:55]\n\t"
  "v_fma_f32 v184, v184, %[sG], v252\n\t"
  "v_mfma_f32_16x16x32_bf16 v[48:51], v[72:75], v[104:107], v[48:51]\n\t"
  "v_fmac_f32 v216, v184, v248\n\t"
  "v_mfma_f32_16x16x32_bf16 v[44:47], v[72:75], v[100:103], v[44:47]\n\t"
  "ds_read_b128 v[132:135], %[vb] offset:512\n\t"
  "v_and_b32 v217, 0xffff0000, v224\n\t"
  "v_mfma_f32_16x16x32_bf16 v[40:43], v[72:75], v[96:99], v[40:43]\n\t"
  "ds_read_b64 v[72:73], %[va] offset:1472\n\t"
  "ds_read_b64 v[74:75], %[va] offset:1480\n\t"
  "v_lshlrev_b32 v249, 16, v227\n\t"
  "v_mfma_f32_16x16x32_bf16 v[60:63], v[76:79], v[112:115], v[60:63]\n\t"
  "v_fma_f32 v185, v217, v230, v231\n\t"
  "v_mfma_f32_16x16x32_bf16 v[56:59], v[76:79], v[108:111], v[56:59]\n\t"
  "ds_read_b128 v[128:131], %[vb] offset:0\n\t"
  "v_mul_f32 v249, %[sD], v249\n\t"
  "v_mfma_f32_16x16x32_bf16 v[52:55], v[76:79], v[104:107], v[52:55]\n\t"
  "v_fma_f32 v185, v185, %[sG], v252\n\t"
  "v_mfma_f32_16x16x32_bf16 v[48:51], v[76:79], v[100:103], v[48:51]\n\t"
  "v_fmac_f32 v217, v185, v249\n\t"
  "v_mfma_f32_16x16x32_bf16 v[44:47], v[76:79], v[96:99], v[44:47]\n\t"
  "ds_read_b64 v[76:77], %[va] offset:1984\n\t"
  "ds_read_b64 v[78:79], %[va] offset:1992\n\t"
  "v_lshlrev_b32 v218, 16, v225\n\t"
  "v_and_b32 v250, 0xffff0000, v226\n\t"
  "v_mfma_f32_16x16x32_bf16 v[60:63], v[80:83], v[108:111], v[60:63]\n\t"
  "s_waitcnt lgkmcnt(14)\n\t"
  "v_fma_f32 v186, v218, v232, v233\n\t"
  "v_mfma_f32_16x16x32_bf16 v[56:59], v[80:83], v[104:107], v[56:59]\n\t"
  "v_mul_f32 v250, %[sD], v250\n\t"
  "v_mfma_f32_16x16x32_bf16 v[52:55], v[80:83], v[100:103], v[52:55]\n\t"
  "v_fma_f32 v186, v186, %[sG], v252\n\t"
  "v_mfma_f32_16x16x32_bf16 v[48:51], v[80:83], v[96:99], v[48:51]\n\t"
  "ds_read_b64 v[80:81], %[va] offset:2496\n\t"
  "ds_read_b64 v[82:83], %[va] offset:2504\n\t"
  "v_fmac_f32 v218, v186, v250\n\t"
  "v_mfma_f32_16x16x32_bf16 v[60:63], v[84:87], v[104:107], v[60:63]\n\t"
  "v_and_b32 v219, 0xffff0000, v225\n\t"
  "v_mfma_f32_16x16x32_bf16 v[56:59], v[84:87], v[100:103], v[56:59]\n\t"
  "v_lshlrev_b32 v251, 16, v226\n\t"
  "v_mfma_f32_16x16x32_bf16 v[52:55], v[84:87], v[96:99], v[52:55]\n\t"
  "ds_read_b64 v[84:85], %[va] offset:3008\n\t"
  "ds_read_b64 v[86:87], %[va] offset:3016\n\t"
  "v_fma_f32 v187, v219, v234, v235\n\t"
  "v_mfma_f32_16x16x32_bf16 v[60:63], v[88:91], v[100:103], v[60:63]\n\t"
  "v_mul_f32 v251, %[sD], v251\n\t"
  "v_mfma_f32_16x16x32_bf16 v[56:59], v[88:91], v[96:99], v[56:59]\n\t"
  "ds_read_b64 v[88:89], %[va] offset:3520\n\t"
  "ds_read_b64 v[90:91], %[va] offset:3528\n\t"
  "v_fma_f32 v187, v187, %[sG], v252\n\t"
  "v_mfma_f32_16x16x32_bf16 v[60:63], v[92:95], v[96:99], v[60:63]\n\t"
  "ds_read_b64 v[92:93], %[va] offset:4032\n\t"
  "ds_read_b64 v[94:95], %[va] offset:4040\n\t"
  "v_fmac_f32 v219, v187, v251\n\t"
  "s_waitcnt lgkmcnt(15)\n\t"
  "v_mfma_f32_16x16x32_bf16 v[60:63], v[64:67], v[156:159], v[60:63]\n\t"
  "v_mfma_f32_16x16x32_bf16 v[56:59], v[64:67], v[152:155], v[56:59]\n\t"
  "v_mfma_f32_16x16x32_bf16 v[52:55], v[64:67], v[148:151], v[52:55]\n\t"
  "ds_read_b64 v[236:237], %[vx] offset:3584\n\t"
  "v_mfma_f32_16x16x32_bf16 v[48:51], v[64:67], v[144:147], v[48:51]\n\t"
  "ds_read_b64 v[238:239], %[vk] offset:3584\n\t"
  "v_mfma_f32_16x16x32_bf16 v[44:47], v[64:67], v[140:143], v[44:47]\n\t"
  "s_waitcnt lgkmcnt(15)\n\t"
  "v_mfma_f32_16x16x32_bf16 v[40:43], v[64:67], v[136:139], v[40:43]\n\t"
  "ds_read_b128 v[240:243], %[vs] offset:14336\n\t"
  "v_mfma_f32_16x16x32_bf16 v[36:39], v[64:67], v[132:135], v[36:39]\n\t"
  "ds_read_b128 v[244:247], %[vs] offset:14352\n\t"
  "s_waitcnt lgkmcnt(14)\n\t"
  "v_mfma_f32_16x16x32_bf16 v[32:35], v[64:67], v[128:131], v[32:35]\n\t"
  "v_mfma_f32_16x16x32_bf16 v[60:63], v[68:71], v[152:155], v[60:63]\n\t"
  "v_mfma_f32_16x16x32_bf16 v[56:59], v[68:71], v[148:151], v[56:59]\n\t"
  "v_mfma_f32_16x16x32_bf16 v[52:55], v[68:71], v[144:147], v[52:55]\n\t"
  "v_mfma_f32_16x16x32_bf16 v[48:51], v[68:71], v[140:143], v[48:51]\n\t"
  "v_mfma_f32_16x16x32_bf16 v[44:47], v[68:71], v[136:139], v[44:47]\n\t"
  "v_mfma_f32_16x16x32_bf16 v[40:43], v[68:71], v[132:135], v[40:43]\n\t"
  "v_mfma_f32_16x16x32_bf16 v[36:39], v[68:71], v[128:131], v[36:39]\n\t"
  "s_waitcnt lgkmcnt(3)\n\t"
  "v_lshlrev_b32 v220, 16, v236\n\t"
  "s_waitcnt lgkmcnt(2)\n\t"
  "v_and_b32 v248, 0xffff0000, v239\n\t"
  "v_mfma_f32_16x16x32_bf16 v[60:63], v[72:75], v[148:151], v[60:63]\n\t"
  "s_waitcnt lgkmcnt(1)\n\t"
  "v_fma_f32 v188, v220, v240, v241\n\t"
  "v_mfma_f32_16x16x32_bf16 v[56:59], v[72:75], v[144:147], v[56:59]\n\t"
  "v_mul_f32 v248, %[sD], v248\n\t"
  "v_mfma_f32_16x16x32_bf16 v[52:55], v[72:75], v[140:143], v[52:55]\n\t"
  "v_fma_f32 v188, v188, %[sG], v252\n\t"
  "v_mfma_f32_16x16x32_bf16 v[48:51], v[72:75], v[136:139], v[48:51]\n\t"
  "v_fmac_f32 v220, v188, v248\n\t"
  "v_mfma_f32_16x16x32_bf16 v[44:47], v[72:75], v[132:135], v[44:47]\n\t"
  "v_and_b32 v221, 0xffff0000, v236\n\t"
  "v_mfma_f32_16x16x32_bf16 v[40:43], v[72:75], v[128:131], v[40:43]\n\t"
  "v_lshlrev_b32 v249, 16, v239\n\t"
  "v_mfma_f32_16x16x32_bf16 v[60:63], v[76:79], v[144:147], v[60:63]\n\t"
  "v_fma_f32 v189, v221, v242, v243\n\t"
  "v_mfma_f32_16x16x32_bf16 v[56:59], v[76:79], v[140:143], v[56:59]\n\t"
  "v_mul_f32 v249, %[sD], v249\n\t"
  "v_mfma_f32_16x16x32_bf16 v[52:55], v[76:79], v[136:139], v[52:55]\n\t"
  "v_fma_f32 v189, v189, %[sG], v252\n\t"
  "v_mfma_f32_16x16x32_bf16 v[48:51], v[76:79], v[132:135], v[48:51]\n\t"
  "v_fmac_f32 v221, v189, v249\n\t"
  "v_mfma_f32_16x16x32_bf16 v[44:47], v[76:79], v[128:131], v[44:47]\n\t"
  "v_lshlrev_b32 v222, 16, v237\n\t"
  "v_and_b32 v250, 0xffff0000, v238\n\t"
  "v_mfma_f32_16x16x32_bf16 v[60:63], v[80:83], v[140:143], v[60:63]\n\t"
  "s_waitcnt lgkmcnt(0)\n\t"
  "v_fma_f32 v190, v222, v244, v245\n\t"
  "v_mfma_f32_16x16x32_bf16 v[56:59], v[80:83], v[136:139], v[56:59]\n\t"
  "v_mul_f32 v250, %[sD], v250\n\t"
  "v_mfma_f32_16x16x32_bf16 v[52:55], v[80:83], v[132:135], v[52:55]\n\t"
  "v_fma_f32 v190, v190, %[sG], v252\n\t"
  "v_mfma_f32_16x16x32_bf16 v[48:51], v[80:83], v[128:131], v[48:51]\n\t"
  "v_fmac_f32 v222, v190, v250\n\t"
  "v_mfma_f32_16x16x32_bf16 v[60:63], v[84:87], v[136:139], v[60:63]\n\t"
  "v_and_b32 v223, 0xffff0000, v237\n\t"
  "v_mfma_f32_16x16x32_bf16 v[56:59], v[84:87], v[132:135], v[56:59]\n\t"
  "v_lshlrev_b32 v251, 16, v238\n\t"
  "v_mfma_f32_16x16x32_bf16 v[52:55], v[84:87], v[128:131], v[52:55]\n\t"
  "v_fma_f32 v191, v223, v246, v247\n\t"
  "v_mfma_f32_16x16x32_bf16 v[60:63], v[88:91], v[132:135], v[60:63]\n\t"
  "v_mul_f32 v251, %[sD], v251\n\t"
  "v_mfma_f32_16x16x32_bf16 v[56:59], v[88:91], v[128:131], v[56:59]\n\t"
  "v_fma_f32 v191, v191, %[sG], v252\n\t"
  "v_mfma_f32_16x16x32_bf16 v[60:63], v[92:95], v[128:131], v[60:63]\n\t"
  "v_fmac_f32 v223, v191, v251\n\t"
  "v_pk_fma_f32 v[192:193], v[160:161], v[32:33], v[192:193]\n\t"
  "v_pk_fma_f32 v[194:195], v[162:163], v[34:35], v[194:195]\n\t"
  "v_pk_fma_f32 v[196:197], v[164:165], v[36:37], v[196:197]\n\t"
  "v_pk_fma_f32 v[198:199], v[166:167], v[38:39], v[198:199]\n\t"
  "v_cvt_pk_bf16_f32 v64, v192, v193\n\t"
  "v_cvt_pk_bf16_f32 v66, v196, v197\n\t"
  "v_cvt_pk_bf16_f32 v65, v194, v195\n\t"
  "v_cvt_pk_bf16_f32 v67, v198, v199\n\t"
  "s_add_u32 s44, %[sO], 0\n\t"
  "v_permlane16_swap_b32 v64, v66\n\t"
  "v_permlane16_swap_b32 v65, v67\n\t"
  "buffer_store_dwordx4 v[64:67], %[vo], %[srd], s44 offen\n\t"
  "v_pk_fma_f32 v[200:201], v[168:169], v[40:41], v[200:201]\n\t"
  "v_pk_fma_f32 v[202:203], v[170:171], v[42:43], v[202:203]\n\t"
  "v_pk_fma_f32 v[204:205], v[172:173], v[44:45], v[204:205]\n\t"
  "v_pk_fma_f32 v[206:207], v[174:175], v[46:47], v[206:207]\n\t"
  "v_cvt_pk_bf16_f32 v68, v200, v201\n\t"
  "v_cvt_pk_bf16_f32 v70, v204, v205\n\t"
  "v_cvt_pk_bf16_f32 v69, v202, v203\n\t"
  "v_cvt_pk_bf16_f32 v71, v206, v207\n\t"
  "s_add_u32 s44, %[sO], 655360\n\t"
  "v_permlane16_swap_b32 v68, v70\n\t"
  "v_permlane16_swap_b32 v69, v71\n\t"
  "buffer_store_dwordx4 v[68:71], %[vo], %[srd], s44 offen\n\t"
  "v_pk_fma_f32 v[208:209], v[176:177], v[48:49], v[208:209]\n\t"
  "v_pk_fma_f32 v[210:211], v[178:179], v[50:51], v[210:211]\n\t"
  "v_pk_fma_f32 v[212:213], v[180:181], v[52:53], v[212:213]\n\t"
  "v_pk_fma_f32 v[214:215], v[182:183], v[54:55], v[214:215]\n\t"
  "v_cvt_pk_bf16_f32 v72, v208, v209\n\t"
  "v_cvt_pk_bf16_f32 v74, v212, v213\n\t"
  "v_cvt_pk_bf16_f32 v73, v210, v211\n\t"
  "v_cvt_pk_bf16_f32 v75, v214, v215\n\t"
  "s_add_u32 s44, %[sO], 1310720\n\t"
  "v_permlane16_swap_b32 v72, v74\n\t"
  "v_permlane16_swap_b32 v73, v75\n\t"
  "buffer_store_dwordx4 v[72:75], %[vo], %[srd], s44 offen\n\t"
  "v_pk_fma_f32 v[216:217], v[184:185], v[56:57], v[216:217]\n\t"
  "v_pk_fma_f32 v[218:219], v[186:187], v[58:59], v[218:219]\n\t"
  "v_pk_fma_f32 v[220:221], v[188:189], v[60:61], v[220:221]\n\t"
  "v_pk_fma_f32 v[222:223], v[190:191], v[62:63], v[222:223]\n\t"
  "v_cvt_pk_bf16_f32 v76, v216, v217\n\t"
  "v_cvt_pk_bf16_f32 v78, v220, v221\n\t"
  "v_cvt_pk_bf16_f32 v77, v218, v219\n\t"
  "v_cvt_pk_bf16_f32 v79, v222, v223\n\t"
  "s_add_u32 s44, %[sO], 1966080\n\t"
  "v_permlane16_swap_b32 v76, v78\n\t"
  "v_permlane16_swap_b32 v77, v79\n\t"
  "buffer_store_dwordx4 v[76:79], %[vo], %[srd], s44 offen\n\t"
  "s_nop 1\n\t"
    :
    : [srd] "s"(srd), [sG] "s"(sG), [sH] "s"(sH), [sD] "s"(sD), [sO] "s"(rfl(OFF_X2P + d * 128u)),
      [va] "v"(va), [vb] "v"(vb), [vx] "v"(vx), [vk] "v"(vk), [vs] "v"(vs), [vo] "v"(vo)
    : "memory", "vcc", "scc",
"s44", "v32", "v33", "v34", "v35", "v36", "v37", "v38", "v39", "v40", "v41", "v42", "v43", "v44", "v45", "v46", "v47", "v48", "v49", "v50", "v51", "v52", "v53", "v54", "v55", "v56", "v57", "v58", "v59", "v60", "v61", "v62", "v63", "v64", "v65", "v66", "v67", "v68", "v69", "v70", "v71", "v72", "v73", "v74", "v75", "v76", "v77", "v78", "v79", "v80", "v81", "v82", "v83", "v84", "v85", "v86", "v87", "v88", "v89", "v90", "v91", "v92", "v93", "v94", "v95", "v96", "v97", "v98", "v99", "v100", "v101", "v102", "v103", "v104", "v105", "v106", "v107", "v108", "v109", "v110", "v111", "v112", "v113", "v114", "v115", "v116", "v117", "v118", "v119", "v120", "v121", "v122", "v123", "v124", "v125", "v126", "v127", "v128", "v129", "v130", "v131", "v132", "v133", "v134", "v135", "v136", "v137", "v138", "v139", "v140", "v141", "v142", "v143", "v144", "v145", "v146", "v147", "v148", "v149", "v150", "v151", "v152", "v153", "v154", "v155", "v156", "v157", "v158", "v159", "v160", "v161", "v162", "v163", "v164", "v165", "v166", "v167", "v168", "v169", "v170", "v171", "v172", "v173", "v174", "v175", "v176", "v177", "v178", "v179", "v180", "v181", "v182", "v183", "v184", "v185", "v186", "v187", "v188", "v189", "v190", "v191", "v192", "v193", "v194", "v195", "v196", "v197", "v198", "v199", "v200", "v201", "v202", "v203", "v204", "v205", "v206", "v207", "v208", "v209", "v210", "v211", "v212", "v213", "v214", "v215", "v216", "v217", "v218", "v219", "v220", "v221", "v222", "v223", "v224", "v225", "v226", "v227", "v228", "v229", "v230", "v231", "v232", "v233", "v234", "v235", "v236", "v237", "v238", "v239", "v240", "v241", "v242", "v243", "v244", "v245", "v246", "v247", "v248", "v249", "v250", "v251", "v252"
  );
  STAMP(4);
  STAMP_OUT(1);
}

DI void wprep1_body(char* smem, const u16* __restrict__ wg, const u16* __restrict__ wu, char* __restrict__ ws, int cg, int kq) {
  const int tid = threadIdx.x, w = tid >> 6, lane = tid & 63;
  constexpr int RP = 400;
  STAMP_DECL; STAMP(0);
  float* sS = (float*)(smem + 160 * RP);
  u16* sB = (u16*)(smem + 160 * RP + 640);
  u32x4 gv[8], uv[8];
#pragma unroll
  for (int n = 0; n < 8; ++n) {
    int ci = n * 256 + tid; if (ci >= 1920) ci = 1919;
    const int row = ci / 12, ck = ci - row * 12;
    const size_t o = (size_t)(160 * kq + row) * F_ + 96 * cg + ck * 8;
    gv[n] = __builtin_nontemporal_load((const u32x4*)(wg + o)); uv[n] = __builtin_nontemporal_load((const u32x4*)(wu + o));
  }
  if (tid < 160) {
    sS[tid] = rbf(1.f + modval(ws, 4 * 640 + 160 * kq + tid));
    const float sb = modval(ws, 3 * 640 + 160 * kq + tid);
    sB[tid] = (u16)(__float_as_uint(sb) >> 16);
  }
#pragma unroll
  for (int n = 0; n < 8; ++n) {
    int ci = n * 256 + tid; if (ci >= 1920) ci = 1919;
    const int row = ci / 12, ck = ci - row * 12;
    *(u32x4*)(smem + row * RP + ck * 32) = u32x4{gv[n][0], uv[n][0], gv[n][1], uv[n][1]};
    *(u32x4*)(smem + row * RP + ck * 32 + 16) = u32x4{gv[n][2], uv[n][2], gv[n][3], uv[n][3]};
  }
  __syncthreads();
  const int g = lane >> 4, q = (lane >> 2) & 3, p = lane & 3;
  f32x4 aU[3], aB[3];
#pragma unroll
  for (int i = 0; i < 3; ++i) { aU[i] = f32x4{0, 0, 0, 0}; aB[i] = f32x4{0, 0, 0, 0}; }
  const u32x4 ones = {0x3f803f80u, 0x3f803f80u, 0x3f803f80u, 0x3f803f80u};
#pragma unroll
  for (int ks = 0; ks < 5; ++ks) {
    const f32x4 s0 = *(const f32x4*)(sS + 32 * ks + 4 * g), s1 = *(const f32x4*)(sS + 32 * ks + 16 + 4 * g);
    const u32x2 b0 = *(const u32x2*)(sB + 32 * ks + 4 * g), b1 = *(const u32x2*)(sB + 32 * ks + 16 + 4 * g);
    const u32x4 sbf = {b0[0], b0[1], b1[0], b1[1]};
#pragma unroll
    for (int i = 0; i < 3; ++i) {
      const int fp = 12 * w + (p < 3 ? 4 * p + i : 4 * i + 3);
      const char* a = smem + (32 * ks + 4 * g + q) * RP + fp * 8;
      const u32x2 lo = tr_read(a), hi = tr_read(a + 16 * RP);
      aB[i] = mfma(u32x4{lo[0], lo[1], hi[0], hi[1]}, sbf, aB[i]);
      const u32x4 sc = {pack_bf16(bflo(lo[0]) * s0[0], bfhi(lo[0]) * s0[1]), pack_bf16(bflo(lo[1]) * s0[2], bfhi(lo[1]) * s0[3]),
                        pack_bf16(bflo(hi[0]) * s1[0], bfhi(hi[0]) * s1[1]), pack_bf16(bflo(hi[1]) * s1[2], bfhi(hi[1]) * s1[3])};
      aU[i] = mfma(sc, ones, aU[i]);
      *(u32x4*)(ws + OFF_W1F + ((size_t)(((cg * 4 + w) * 20 + 5 * kq + ks) * 3 + i) * 64 + lane) * 16) = sc;
    }
  }
  if ((lane & 15) == 0) {
#pragma unroll
    for (int i = 0; i < 3; ++i) {
      char* o = ws + OFF_UB + ((size_t)kq * 3072 + ((cg * 4 + w) * 3 + i) * 16 + 4 * g) * 8;
      *(f32x4*)o = aU[i];
      *(f32x4*)(o + 16) = aB[i];
    }
  }
  STAMP(4);
  STAMP_OUT(1);
}

DI void wprep2_body(char* smem, const u16* __restrict__ wd, char* __restrict__ ws, int ng, int kq) {
  const int tid = threadIdx.x, w = tid >> 6, lane = tid & 63;
  constexpr int RP = 176;
  STAMP_DECL; STAMP(0);
  u32x4 v[15];
#pragma unroll
  for (int n = 0; n < 15; ++n) {
    const int ci = n * 256 + tid, row = ci / 10, ck = ci - row * 10;
    v[n] = __builtin_nontemporal_load((const u32x4*)(wd + (size_t)(384 * kq + row) * D_ + 80 * ng + ck * 8));
  }
  float gb[5];
#pragma unroll
  for (int i = 0; i < 5; ++i) { const int r = lane & 15; gb[i] = modval(ws, 5 * 640 + 80 * ng + (i < 4 ? 32 * (i >> 1) + 8 * (r >> 2) + 4 * (i & 1) + (r & 3) : 64 + r)); }
#pragma unroll
  for (int n = 0; n < 15; ++n) {
    const int ci = n * 256 + tid, row = ci / 10, ck = ci - row * 10;
    *(u32x4*)(smem + row * RP + ck * 16) = v[n];
  }
  __syncthreads();
  const int g = lane >> 4, q = (lane >> 2) & 3, p = lane & 3;
#pragma unroll
  for (int kk = 0; kk < 3; ++kk) {
    const int ks = 3 * w + kk;
#pragma unroll
    for (int i = 0; i < 5; ++i) {
      const int col = i < 4 ? 32 * (i >> 1) + 8 * p + 4 * (i & 1) : 64 + 4 * p;
      const char* a = smem + (32 * ks + 8 * g + q) * RP + col * 2;
      const u32x2 lo = tr_read(a), hi = tr_read(a + 4 * RP);
      const float sc = gb[i];
      *(u32x4*)(ws + OFF_W2F + ((size_t)(((ng * 4 + kq) * 12 + ks) * 5 + i) * 64 + lane) * 16) =
          u32x4{pack_bf16(bflo(lo[0]) * sc, bfhi(lo[0]) * sc), pack_bf16(bflo(lo[1]) * sc, bfhi(lo[1]) * sc), pack_bf16(bflo(hi[0]) * sc, bfhi(hi[0]) * sc), pack_bf16(bflo(hi[1]) * sc, bfhi(hi[1]) * sc)};
    }
  }
  STAMP(4);
  STAMP_OUT(1);
}

__global__ __launch_bounds__(256, 1) void k_conv(const u16* __restrict__ ssmk, const u16* __restrict__ ssmD, const u16* __restrict__ wg,
                                                 const u16* __restrict__ wu, const u16* __restrict__ wd, char* __restrict__ ws) {
  __shared__ __attribute__((aligned(16))) char smem[L_CONV_END];
  const int b = blockIdx.x;
  if ((b >> 3) < 20) conv_body(smem, ssmk, ssmD, ws, (b & 7) * 20 + (b >> 3));
  else {
    const int wb = (b & 7) * 12 + (b >> 3) - 20;
    if (wb < 64) wprep1_body(smem, wg, wu, ws, wb >> 2, wb & 3);
    else wprep2_body(smem, wd, ws, (wb - 64) >> 2, wb & 3);
  }
}

__global__ __launch_bounds__(256, 1) void k_ffn(char* __restrict__ ws, u16* __restrict__ out) {
  __shared__ __attribute__((aligned(16))) char smem[163840];
  const unsigned b = blockIdx.x, tid = threadIdx.x, w = rfl(tid >> 6), lane = tid & 63;
  const unsigned xcd = b & 7, loc = b >> 3, pl = loc & 3, p = 4 * (xcd >> 1) + pl, j = 8 * (xcd & 1) + (loc >> 2), ng = j & 7, sub = j >> 3;
  const __amdgpu_buffer_rsrc_t srd = __builtin_amdgcn_make_buffer_rsrc((void*)ws, 0, WS_END, 0x00020000);
  const __amdgpu_buffer_rsrc_t srdo = __builtin_amdgcn_make_buffer_rsrc((void*)out, 0, S_ * D_ * 2, 0x00020000);
  const unsigned lb = (unsigned)(size_t)smem;
  const unsigned g = lane >> 4, q = (lane >> 2) & 3, pq = lane & 3;
  const unsigned row = 4 * g + q;
  const unsigned vtr = lb + row * 128 + ((row >> 1) & 3) * 32 + pq * 8;
  const unsigned vl16 = lane * 16;
  const unsigned vdma = (lane >> 3) * 128 + (((lane & 7) ^ (((lane >> 4) & 3) << 1)) * 16);
  const unsigned vh2st = lane < 48 ? lane * 16 : OOB;
  const unsigned tk = 4 * j + w;
  const unsigned vstc = lb + lane * 128 + (((tk >> 4) ^ ((lane >> 1) & 3)) * 32) + (tk & 15) * 2;
  const unsigned vstw = lane == 63 ? tk * 8 : OOB;
  const unsigned vflw = lane == 0 ? 0u : OOB;
  const unsigned rs_ = 2 * (w & 1) + sub, rb = lb + (w >> 1) * 81920 + 80 * ng * 128 + pq * 8;
  const unsigned r0 = 8 * g + q, r1 = 8 * g + 4 + q, r2 = 64 + 4 * g + q;
  const unsigned vres0 = rb + r0 * 128 + ((rs_ ^ ((r0 >> 1) & 3)) * 32);
  const unsigned vres1 = rb + r1 * 128 + ((rs_ ^ ((r1 >> 1) & 3)) * 32);
  const unsigned vres2 = rb + r2 * 128 + ((rs_ ^ ((r2 >> 1) & 3)) * 32);
  const unsigned vwr = lb + w * 20480 + lane * 16;
  const unsigned l40 = lane < 40 ? lane : 0, tq = l40 / 10, ci = l40 - tq * 10, tl = 4 * w + tq;
  const unsigned vrd0 = lb + (ci < 8 ? (2 * (ci >> 2)) * 1024 + ((ci & 3) * 16 + tl) * 16 : 4096 + ((2 * (ci - 8)) * 16 + tl) * 16);
  const unsigned vrd1 = vrd0 + (ci < 8 ? 1024 : 256);
  const unsigned vout = lane < 40 ? ((64 * p + 16 * sub + tl) * D_ + 80 * ng + 8 * ci) * 2 : OOB;
  const unsigned rdma = w;
  asm volatile(
  "s_add_u32 s64, %[rotg], 0\n\t"
  "s_and_b32 s64, s64, 3\n\t"
  "s_mul_i32 s44, s64, 20480\n\t"
  "s_add_u32 s48, s44, 81920\n\t"
  "s_mul_i32 s64, s64, 15360\n\t"
  "s_add_u32 s52, s64, %[sW1]\n\t"
  "s_add_u32 s64, %[rotg], 1\n\t"
  "s_and_b32 s64, s64, 3\n\t"
  "s_mul_i32 s45, s64, 20480\n\t"
  "s_add_u32 s49, s45, 81920\n\t"
  "s_mul_i32 s64, s64, 15360\n\t"
  "s_add_u32 s53, s64, %[sW1]\n\t"
  "s_add_u32 s64, %[rotg], 2\n\t"
  "s_and_b32 s64, s64, 3\n\t"
  "s_mul_i32 s46, s64, 20480\n\t"
  "s_add_u32 s50, s46, 81920\n\t"
  "s_mul_i32 s64, s64, 15360\n\t"
  "s_add_u32 s54, s64, %[sW1]\n\t"
  "s_add_u32 s64, %[rotg], 3\n\t"
  "s_and_b32 s64, s64, 3\n\t"
  "s_mul_i32 s47, s64, 20480\n\t"
  "s_add_u32 s51, s47, 81920\n\t"
  "s_mul_i32 s64, s64, 15360\n\t"
  "s_add_u32 s55, s64, %[sW1]\n\t"
  "s_mov_b32 s64, %[rdma]\n\t"
  "s_lshl_b32 s65, s64, 12\n\t"
  "s_mov_b32 m0, s65\n\t"
  "s_add_u32 s65, s65, %[sX2]\n\t"
  "buffer_load_dwordx4 %[vdma], %[srd], s65 offen offset:0 lds\n\t"
  "buffer_load_dwordx4 %[vdma], %[srd], s65 offen offset:1024 lds\n\t"
  "buffer_load_dwordx4 %[vdma], %[srd], s65 offen offset:2048 lds\n\t"
  "buffer_load_dwordx4 %[vdma], %[srd], s65 offen offset:3072 lds\n\t"
  "s_add_u32 s64, s64, 4\n\t"
  "s_sub_u32 s66, s64, 20\n\t"
  "s_cmp_ge_u32 s64, 20\n\t"
  "s_cselect_b32 s64, s66, s64\n\t"
  "s_lshl_b32 s65, s64, 12\n\t"
  "s_mov_b32 m0, s65\n\t"
  "s_add_u32 s65, s65, %[sX2]\n\t"
  "buffer_load_dwordx4 %[vdma], %[srd], s65 offen offset:0 lds\n\t"
  "buffer_load_dwordx4 %[vdma], %[srd], s65 offen offset:1024 lds\n\t"
  "buffer_load_dwordx4 %[vdma], %[srd], s65 offen offset:2048 lds\n\t"
  "buffer_load_dwordx4 %[vdma], %[srd], s65 offen offset:3072 lds\n\t"
  "s_add_u32 s64, s64, 4\n\t"
  "s_sub_u32 s66, s64, 20\n\t"
  "s_cmp_ge_u32 s64, 20\n\t"
  "s_cselect_b32 s64, s66, s64\n\t"
  "s_lshl_b32 s65, s64, 12\n\t"
  "s_mov_b32 m0, s65\n\t"
  "s_add_u32 s65, s65, %[sX2]\n\t"
  "buffer_load_dwordx4 %[vdma], %[srd], s65 offen offset:0 lds\n\t"
  "buffer_load_dwordx4 %[vdma], %[srd], s65 offen offset:1024 lds\n\t"
  "buffer_load_dwordx4 %[vdma], %[srd], s65 offen offset:2048 lds\n\t"
  "buffer_load_dwordx4 %[vdma], %[srd], s65 offen offset:3072 lds\n\t"
  "s_add_u32 s64, s64, 4\n\t"
  "s_sub_u32 s66, s64, 20\n\t"
  "s_cmp_ge_u32 s64, 20\n\t"
  "s_cselect_b32 s64, s66, s64\n\t"
  "s_lshl_b32 s65, s64, 12\n\t"
  "s_mov_b32 m0, s65\n\t"
  "s_add_u32 s65, s65, %[sX2]\n\t"
  "buffer_load_dwordx4 %[vdma], %[srd], s65 offen offset:0 lds\n\t"
  "buffer_load_dwordx4 %[vdma], %[srd], s65 offen offset:1024 lds\n\t"
  "buffer_load_dwordx4 %[vdma], %[srd], s65 offen offset:2048 lds\n\t"
  "buffer_load_dwordx4 %[vdma], %[srd], s65 offen offset:3072 lds\n\t"
  "s_add_u32 s64, s64, 4\n\t"
  "s_sub_u32 s66, s64, 20\n\t"
  "s_cmp_ge_u32 s64, 20\n\t"
  "s_cselect_b32 s64, s66, s64\n\t"
  "s_lshl_b32 s65, s64, 12\n\t"
  "s_mov_b32 m0, s65\n\t"
  "s_add_u32 s65, s65, %[sX2]\n\t"
  "buffer_load_dwordx4 %[vdma], %[srd], s65 offen offset:0 lds\n\t"
  "buffer_load_dwordx4 %[vdma], %[srd], s65 offen offset:1024 lds\n\t"
  "buffer_load_dwordx4 %[vdma], %[srd], s65 offen offset:2048 lds\n\t"
  "buffer_load_dwordx4 %[vdma], %[srd], s65 offen offset:3072 lds\n\t"
  "buffer_load_dwordx4 a[0:3], %[vl16], %[srd], s52 offen offset:0\n\t"
  "buffer_load_dwordx4 a[4:7], %[vl16], %[srd], s52 offen offset:1024\n\t"
  "buffer_load_dwordx4 a[8:11], %[vl16], %[srd], s52 offen offset:2048\n\t"
  "buffer_load_dwordx4 a[12:15], %[vl16], %[srd], s52 offen offset:3072\n\t"
  "s_add_u32 s52, s52, 4096\n\t"
  "buffer_load_dwordx4 a[16:19], %[vl16], %[srd], s52 offen offset:0\n\t"
  "buffer_load_dwordx4 a[20:23], %[vl16], %[srd], s52 offen offset:1024\n\t"
  "buffer_load_dwordx4 a[24:27], %[vl16], %[srd], s52 offen offset:2048\n\t"
  "buffer_load_dwordx4 a[28:31], %[vl16], %[srd], s52 offen offset:3072\n\t"
  "s_add_u32 s52, s52, 4096\n\t"
  "buffer_load_dwordx4 a[32:35], %[vl16], %[srd], s52 offen offset:0\n\t"
  "buffer_load_dwordx4 a[36:39], %[vl16], %[srd], s52 offen offset:1024\n\t"
  "buffer_load_dwordx4 a[40:43], %[vl16], %[srd], s52 offen offset:2048\n\t"
  "buffer_load_dwordx4 a[44:47], %[vl16], %[srd], s52 offen offset:3072\n\t"
  "s_add_u32 s52, s52, 4096\n\t"
  "buffer_load_dwordx4 a[48:51], %[vl16], %[srd], s52 offen offset:0\n\t"
  "buffer_load_dwordx4 a[52:55], %[vl16], %[srd], s52 offen offset:1024\n\t"
  "buffer_load_dwordx4 a[56:59], %[vl16], %[srd], s52 offen offset:2048\n\t"
  "v_lshrrev_b32 v148, 3, %[vl16]\n\t"
  "v_and_b32 v148, 0x60, v148\n\t"
  "s_mov_b32 s64, %[sUB]\n\t"
  "buffer_load_dwordx4 v[104:107], v148, %[srd], s64 offen offset:0\n\t"
  "buffer_load_dwordx4 v[116:119], v148, %[srd], s64 offen offset:16\n\t"
  "buffer_load_dwordx4 v[108:111], v148, %[srd], s64 offen offset:128\n\t"
  "buffer_load_dwordx4 v[120:123], v148, %[srd], s64 offen offset:144\n\t"
  "buffer_load_dwordx4 v[112:115], v148, %[srd], s64 offen offset:256\n\t"
  "buffer_load_dwordx4 v[124:127], v148, %[srd], s64 offen offset:272\n\t"
  "s_add_u32 s64, s64, 24576\n\t"
  "buffer_load_dwordx4 v[152:155], v148, %[srd], s64 offen offset:0\n\t"
  "buffer_load_dwordx4 v[164:167], v148, %[srd], s64 offen offset:16\n\t"
  "buffer_load_dwordx4 v[156:159], v148, %[srd], s64 offen offset:128\n\t"
  "buffer_load_dwordx4 v[168:171], v148, %[srd], s64 offen offset:144\n\t"
  "buffer_load_dwordx4 v[160:163], v148, %[srd], s64 offen offset:256\n\t"
  "buffer_load_dwordx4 v[172:175], v148, %[srd], s64 offen offset:272\n\t"
  "s_add_u32 s64, s64, 24576\n\t"
  "buffer_load_dwordx4 v[176:179], v148, %[srd], s64 offen offset:0\n\t"
  "buffer_load_dwordx4 v[188:191], v148, %[srd], s64 offen offset:16\n\t"
  "buffer_load_dwordx4 v[180:183], v148, %[srd], s64 offen offset:128\n\t"
  "buffer_load_dwordx4 v[192:195], v148, %[srd], s64 offen offset:144\n\t"
  "buffer_load_dwordx4 v[184:187], v148, %[srd], s64 offen offset:256\n\t"
  "buffer_load_dwordx4 v[196:199], v148, %[srd], s64 offen offset:272\n\t"
  "s_add_u32 s64, s64, 24576\n\t"
  "buffer_load_dwordx4 v[200:203], v148, %[srd], s64 offen offset:0\n\t"
  "buffer_load_dwordx4 v[212:215], v148, %[srd], s64 offen offset:16\n\t"
  "buffer_load_dwordx4 v[204:207], v148, %[srd], s64 offen offset:128\n\t"
  "buffer_load_dwordx4 v[216:219], v148, %[srd], s64 offen offset:144\n\t"
  "buffer_load_dwordx4 v[208:211], v148, %[srd], s64 offen offset:256\n\t"
  "buffer_load_dwordx4 v[220:223], v148, %[srd], s64 offen offset:272\n\t"
  "buffer_load_dwordx4 a[60:63], %[vl16], %[srd], s53 offen offset:0\n\t"
  "buffer_load_dwordx4 a[64:67], %[vl16], %[srd], s53 offen offset:1024\n\t"
  "buffer_load_dwordx4 a[68:71], %[vl16], %[srd], s53 offen offset:2048\n\t"
  "buffer_load_dwordx4 a[72:75], %[vl16], %[srd], s53 offen offset:3072\n\t"
  "s_add_u32 s53, s53, 4096\n\t"
  "buffer_load_dwordx4 a[76:79], %[vl16], %[srd], s53 offen offset:0\n\t"
  "buffer_load_dwordx4 a[80:83], %[vl16], %[srd], s53 offen offset:1024\n\t"
  "buffer_load_dwordx4 a[84:87], %[vl16], %[srd], s53 offen offset:2048\n\t"
  "buffer_load_dwordx4 a[88:91], %[vl16], %[srd], s53 offen offset:3072\n\t"
  "s_add_u32 s53, s53, 4096\n\t"
  "buffer_load_dwordx4 a[92:95], %[vl16], %[srd], s53 offen offset:0\n\t"
  "buffer_load_dwordx4 a[96:99], %[vl16], %[srd], s53 offen offset:1024\n\t"
  "buffer_load_dwordx4 a[100:103], %[vl16], %[srd], s53 offen offset:2048\n\t"
  "buffer_load_dwordx4 a[104:107], %[vl16], %[srd], s53 offen offset:3072\n\t"
  "s_add_u32 s53, s53, 4096\n\t"
  "buffer_load_dwordx4 a[108:111], %[vl16], %[srd], s53 offen offset:0\n\t"
  "buffer_load_dwordx4 a[112:115], %[vl16], %[srd], s53 offen offset:1024\n\t"
  "buffer_load_dwordx4 a[116:119], %[vl16], %[srd], s53 offen offset:2048\n\t"
  "buffer_load_dwordx4 a[120:123], %[vl16], %[srd], s54 offen offset:0\n\t"
  "buffer_load_dwordx4 a[124:127], %[vl16], %[srd], s54 offen offset:1024\n\t"
  "buffer_load_dwordx4 a[128:131], %[vl16], %[srd], s54 offen offset:2048\n\t"
  "buffer_load_dwordx4 a[132:135], %[vl16], %[srd], s54 offen offset:3072\n\t"
  "s_add_u32 s54, s54, 4096\n\t"
  "buffer_load_dwordx4 a[136:139], %[vl16], %[srd], s54 offen offset:0\n\t"
  "buffer_load_dwordx4 a[140:143], %[vl16], %[srd], s54 offen offset:1024\n\t"
  "buffer_load_dwordx4 a[144:147], %[vl16], %[srd], s54 offen offset:2048\n\t"
  "buffer_load_dwordx4 a[148:151], %[vl16], %[srd], s54 offen offset:3072\n\t"
  "s_add_u32 s54, s54, 4096\n\t"
  "buffer_load_dwordx4 a[152:155], %[vl16], %[srd], s54 offen offset:0\n\t"
  "buffer_load_dwordx4 a[156:159], %[vl16], %[srd], s54 offen offset:1024\n\t"
  "buffer_load_dwordx4 a[160:163], %[vl16], %[srd], s54 offen offset:2048\n\t"
  "buffer_load_dwordx4 a[164:167], %[vl16], %[srd], s54 offen offset:3072\n\t"
  "s_add_u32 s54, s54, 4096\n\t"
  "buffer_load_dwordx4 a[168:171], %[vl16], %[srd], s54 offen offset:0\n\t"
  "buffer_load_dwordx4 a[172:175], %[vl16], %[srd], s54 offen offset:1024\n\t"
  "buffer_load_dwordx4 a[176:179], %[vl16], %[srd], s54 offen offset:2048\n\t"
  "buffer_load_dwordx4 a[180:183], %[vl16], %[srd], s55 offen offset:0\n\t"
  "buffer_load_dwordx4 a[184:187], %[vl16], %[srd], s55 offen offset:1024\n\t"
  "buffer_load_dwordx4 a[188:191], %[vl16], %[srd], s55 offen offset:2048\n\t"
  "buffer_load_dwordx4 a[192:195], %[vl16], %[srd], s55 offen offset:3072\n\t"
  "s_add_u32 s55, s55, 4096\n\t"
  "buffer_load_dwordx4 a[196:199], %[vl16], %[srd], s55 offen offset:0\n\t"
  "buffer_load_dwordx4 a[200:203], %[vl16], %[srd], s55 offen offset:1024\n\t"
  "buffer_load_dwordx4 a[204:207], %[vl16], %[srd], s55 offen offset:2048\n\t"
  "buffer_load_dwordx4 a[208:211], %[vl16], %[srd], s55 offen offset:3072\n\t"
  "s_add_u32 s55, s55, 4096\n\t"
  "buffer_load_dwordx4 a[212:215], %[vl16], %[srd], s55 offen offset:0\n\t"
  "buffer_load_dwordx4 a[216:219], %[vl16], %[srd], s55 offen offset:1024\n\t"
  "buffer_load_dwordx4 a[220:223], %[vl16], %[srd], s55 offen offset:2048\n\t"
  "buffer_load_dwordx4 a[224:227], %[vl16], %[srd], s55 offen offset:3072\n\t"
  "s_add_u32 s55, s55, 4096\n\t"
  "buffer_load_dwordx4 a[228:231], %[vl16], %[srd], s55 offen offset:0\n\t"
  "buffer_load_dwordx4 a[232:235], %[vl16], %[srd], s55 offen offset:1024\n\t"
  "buffer_load_dwordx4 a[236:239], %[vl16], %[srd], s55 offen offset:2048\n\t"
  "s_mov_b32 s64, %[rdma]\n\t"
  "s_lshl_b32 s65, s64, 12\n\t"
  "s_add_u32 m0, s65, 81920\n\t"
  "s_add_u32 s65, s65, %[sX2]\n\t"
  "s_add_u32 s65, s65, 1310720\n\t"
  "buffer_load_dwordx4 %[vdma], %[srd], s65 offen offset:0 lds\n\t"
  "buffer_load_dwordx4 %[vdma], %[srd], s65 offen offset:1024 lds\n\t"
  "buffer_load_dwordx4 %[vdma], %[srd], s65 offen offset:2048 lds\n\t"
  "buffer_load_dwordx4 %[vdma], %[srd], s65 offen offset:3072 lds\n\t"
  "s_add_u32 s64, s64, 4\n\t"
  "s_sub_u32 s66, s64, 20\n\t"
  "s_cmp_ge_u32 s64, 20\n\t"
  "s_cselect_b32 s64, s66, s64\n\t"
  "s_lshl_b32 s65, s64, 12\n\t"
  "s_add_u32 m0, s65, 81920\n\t"
  "s_add_u32 s65, s65, %[sX2]\n\t"
  "s_add_u32 s65, s65, 1310720\n\t"
  "buffer_load_dwordx4 %[vdma], %[srd], s65 offen offset:0 lds\n\t"
  "buffer_load_dwordx4 %[vdma], %[srd], s65 offen offset:1024 lds\n\t"
  "buffer_load_dwordx4 %[vdma], %[srd], s65 offen offset:2048 lds\n\t"
  "buffer_load_dwordx4 %[vdma], %[srd], s65 offen offset:3072 lds\n\t"
  "s_add_u32 s64, s64, 4\n\t"
  "s_sub_u32 s66, s64, 20\n\t"
  "s_cmp_ge_u32 s64, 20\n\t"
  "s_cselect_b32 s64, s66, s64\n\t"
  "s_lshl_b32 s65, s64, 12\n\t"
  "s_add_u32 m0, s65, 81920\n\t"
  "s_add_u32 s65, s65, %[sX2]\n\t"
  "s_add_u32 s65, s65, 1310720\n\t"
  "buffer_load_dwordx4 %[vdma], %[srd], s65 offen offset:0 lds\n\t"
  "buffer_load_dwordx4 %[vdma], %[srd], s65 offen offset:1024 lds\n\t"
  "buffer_load_dwordx4 %[vdma], %[srd], s65 offen offset:2048 lds\n\t"
  "buffer_load_dwordx4 %[vdma], %[srd], s65 offen offset:3072 lds\n\t"
  "s_add_u32 s64, s64, 4\n\t"
  "s_sub_u32 s66, s64, 20\n\t"
  "s_cmp_ge_u32 s64, 20\n\t"
  "s_cselect_b32 s64, s66, s64\n\t"
  "s_lshl_b32 s65, s64, 12\n\t"
  "s_add_u32 m0, s65, 81920\n\t"
  "s_add_u32 s65, s65, %[sX2]\n\t"
  "s_add_u32 s65, s65, 1310720\n\t"
  "buffer_load_dwordx4 %[vdma], %[srd], s65 offen offset:0 lds\n\t"
  "buffer_load_dwordx4 %[vdma], %[srd], s65 offen offset:1024 lds\n\t"
  "buffer_load_dwordx4 %[vdma], %[srd], s65 offen offset:2048 lds\n\t"
  "buffer_load_dwordx4 %[vdma], %[srd], s65 offen offset:3072 lds\n\t"
  "s_add_u32 s64, s64, 4\n\t"
  "s_sub_u32 s66, s64, 20\n\t"
  "s_cmp_ge_u32 s64, 20\n\t"
  "s_cselect_b32 s64, s66, s64\n\t"
  "s_lshl_b32 s65, s64, 12\n\t"
  "s_add_u32 m0, s65, 81920\n\t"
  "s_add_u32 s65, s65, %[sX2]\n\t"
  "s_add_u32 s65, s65, 1310720\n\t"
  "buffer_load_dwordx4 %[vdma], %[srd], s65 offen offset:0 lds\n\t"
  "buffer_load_dwordx4 %[vdma], %[srd], s65 offen offset:1024 lds\n\t"
  "buffer_load_dwordx4 %[vdma], %[srd], s65 offen offset:2048 lds\n\t"
  "buffer_load_dwordx4 %[vdma], %[srd], s65 offen offset:3072 lds\n\t"
  "s_add_u32 s64, %[rotg2], 0\n\t"
  "s_and_b32 s64, s64, 3\n\t"
  "s_mul_i32 s60, s64, 3072\n\t"
  "s_add_u32 s60, s60, %[sH2ld]\n\t"
  "s_mul_i32 s64, s64, 15360\n\t"
  "s_add_u32 s56, s64, %[sW2]\n\t"
  "s_add_u32 s64, %[rotg2], 1\n\t"
  "s_and_b32 s64, s64, 3\n\t"
  "s_mul_i32 s61, s64, 3072\n\t"
  "s_add_u32 s61, s61, %[sH2ld]\n\t"
  "s_mul_i32 s64, s64, 15360\n\t"
  "s_add_u32 s57, s64, %[sW2]\n\t"
  "s_add_u32 s64, %[rotg2], 2\n\t"
  "s_and_b32 s64, s64, 3\n\t"
  "s_mul_i32 s62, s64, 3072\n\t"
  "s_add_u32 s62, s62, %[sH2ld]\n\t"
  "s_mul_i32 s64, s64, 15360\n\t"
  "s_add_u32 s58, s64, %[sW2]\n\t"
  "s_add_u32 s64, %[rotg2], 3\n\t"
  "s_and_b32 s64, s64, 3\n\t"
  "s_mul_i32 s63, s64, 3072\n\t"
  "s_add_u32 s63, s63, %[sH2ld]\n\t"
  "s_mul_i32 s64, s64, 15360\n\t"
  "s_add_u32 s59, s64, %[sW2]\n\t"
  "v_xor_b32 v232, 0, %[vtr]\n\t"
  "v_xor_b32 v233, 32, %[vtr]\n\t"
  "v_xor_b32 v234, 64, %[vtr]\n\t"
  "v_xor_b32 v235, 96, %[vtr]\n\t"
  "s_waitcnt vmcnt(63)\n\t"
  "v_add_f32 v104, v104, v152\n\t"
  "v_add_f32 v105, v105, v153\n\t"
  "v_add_f32 v106, v106, v154\n\t"
  "v_add_f32 v107, v107, v155\n\t"
  "v_add_f32 v108, v108, v156\n\t"
  "v_add_f32 v109, v109, v157\n\t"
  "v_add_f32 v110, v110, v158\n\t"
  "v_add_f32 v111, v111, v159\n\t"
  "v_add_f32 v112, v112, v160\n\t"
  "v_add_f32 v113, v113, v161\n\t"
  "v_add_f32 v114, v114, v162\n\t"
  "v_add_f32 v115, v115, v163\n\t"
  "v_add_f32 v116, v116, v164\n\t"
  "v_add_f32 v117, v117, v165\n\t"
  "v_add_f32 v118, v118, v166\n\t"
  "v_add_f32 v119, v119, v167\n\t"
  "v_add_f32 v120, v120, v168\n\t"
  "v_add_f32 v121, v121, v169\n\t"
  "v_add_f32 v122, v122, v170\n\t"
  "v_add_f32 v123, v123, v171\n\t"
  "v_add_f32 v124, v124, v172\n\t"
  "v_add_f32 v125, v125, v173\n\t"
  "v_add_f32 v126, v126, v174\n\t"
  "v_add_f32 v127, v127, v175\n\t"
  "v_add_f32 v104, v104, v176\n\t"
  "v_add_f32 v105, v105, v177\n\t"
  "v_add_f32 v106, v106, v178\n\t"
  "v_add_f32 v107, v107, v179\n\t"
  "v_add_f32 v108, v108, v180\n\t"
  "v_add_f32 v109, v109, v181\n\t"
  "v_add_f32 v110, v110, v182\n\t"
  "v_add_f32 v111, v111, v183\n\t"
  "v_add_f32 v112, v112, v184\n\t"
  "v_add_f32 v113, v113, v185\n\t"
  "v_add_f32 v114, v114, v186\n\t"
  "v_add_f32 v115, v115, v187\n\t"
  "v_add_f32 v116, v116, v188\n\t"
  "v_add_f32 v117, v117, v189\n\t"
  "v_add_f32 v118, v118, v190\n\t"
  "v_add_f32 v119, v119, v191\n\t"
  "v_add_f32 v120, v120, v192\n\t"
  "v_add_f32 v121, v121, v193\n\t"
  "v_add_f32 v122, v122, v194\n\t"
  "v_add_f32 v123, v123, v195\n\t"
  "v_add_f32 v124, v124, v196\n\t"
  "v_add_f32 v125, v125, v197\n\t"
  "v_add_f32 v126, v126, v198\n\t"
  "v_add_f32 v127, v127, v199\n\t"
  "v_add_f32 v104, v104, v200\n\t"
  "v_add_f32 v105, v105, v201\n\t"
  "v_add_f32 v106, v106, v202\n\t"
  "v_add_f32 v107, v107, v203\n\t"
  "v_add_f32 v108, v108, v204\n\t"
  "v_add_f32 v109, v109, v205\n\t"
  "v_add_f32 v110, v110, v206\n\t"
  "v_add_f32 v111, v111, v207\n\t"
  "v_add_f32 v112, v112, v208\n\t"
  "v_add_f32 v113, v113, v209\n\t"
  "v_add_f32 v114, v114, v210\n\t"
  "v_add_f32 v115, v115, v211\n\t"
  "v_add_f32 v116, v116, v212\n\t"
  "v_add_f32 v117, v117, v213\n\t"
  "v_add_f32 v118, v118, v214\n\t"
  "v_add_f32 v119, v119, v215\n\t"
  "v_add_f32 v120, v120, v216\n\t"
  "v_add_f32 v121, v121, v217\n\t"
  "v_add_f32 v122, v122, v218\n\t"
  "v_add_f32 v123, v123, v219\n\t"
  "v_add_f32 v124, v124, v220\n\t"
  "v_add_f32 v125, v125, v221\n\t"
  "v_add_f32 v126, v126, v222\n\t"
  "v_add_f32 v127, v127, v223\n\t"
  "s_barrier\n\t"
  "v_add_u32 v144, s44, v232\n\t"
  "v_add_u32 v145, s44, v233\n\t"
  "ds_read_b64_tr_b16 v[80:81], v144 offset:0\n\t"
  "ds_read_b64_tr_b16 v[82:83], v144 offset:2048\n\t"
  "ds_read_b64_tr_b16 v[84:85], v145 offset:0\n\t"
  "ds_read_b64_tr_b16 v[86:87], v145 offset:2048\n\t"
  "ds_read_b64_tr_b16 v[88:89], v144 offset:4096\n\t"
  "ds_read_b64_tr_b16 v[90:91], v144 offset:6144\n\t"
  "ds_read_b64_tr_b16 v[92:93], v145 offset:4096\n\t"
  "ds_read_b64_tr_b16 v[94:95], v145 offset:6144\n\t"
  "s_waitcnt lgkmcnt(6)\n\t"
  "v_mfma_f32_16x16x32_bf16 v[32:35], a[0:3], v[80:83], 0\n\t"
  "ds_read_b64_tr_b16 v[96:97], v144 offset:8192\n\t"
  "v_add_u32 v248, 40960, %[vstc]\n\t"
  "v_mov_b32 v236, 0\n\t"
  "v_mfma_f32_16x16x32_bf16 v[36:39], a[4:7], v[80:83], 0\n\t"
  "ds_read_b64_tr_b16 v[98:99], v144 offset:10240\n\t"
  "v_mov_b32 v237, 0\n\t"
  "v_mfma_f32_16x16x32_bf16 v[40:43], a[8:11], v[80:83], 0\n\t"
  "ds_read_b64_tr_b16 v[100:101], v145 offset:8192\n\t"
  "v_mov_b32 v238, 0\n\t"
  "s_waitcnt lgkmcnt(7)\n\t"
  "v_mfma_f32_16x16x32_bf16 v[44:47], a[0:3], v[84:87], 0\n\t"
  "ds_read_b64_tr_b16 v[102:103], v145 offset:10240\n\t"
  "v_mov_b32 v239, 0\n\t"
  "v_mfma_f32_16x16x32_bf16 v[48:51], a[4:7], v[84:87], 0\n\t"
  "v_mov_b32 v240, 0\n\t"
  "v_mov_b32 v241, 0\n\t"
  "v_mfma_f32_16x16x32_bf16 v[52:55], a[8:11], v[84:87], 0\n\t"
  "v_mov_b32 v242, 0\n\t"
  "s_waitcnt lgkmcnt(6)\n\t"
  "v_mfma_f32_16x16x32_bf16 v[32:35], a[12:15], v[88:91], v[32:35]\n\t"
  "ds_read_b64_tr_b16 v[80:81], v144 offset:12288\n\t"
  "v_mov_b32 v243, 0\n\t"
  "v_mfma_f32_16x16x32_bf16 v[36:39], a[16:19], v[88:91], v[36:39]\n\t"
  "ds_read_b64_tr_b16 v[82:83], v144 offset:14336\n\t"
  "v_mov_b32 v244, 0\n\t"
  "v_mfma_f32_16x16x32_bf16 v[40:43], a[20:23], v[88:91], v[40:43]\n\t"
  "ds_read_b64_tr_b16 v[84:85], v145 offset:12288\n\t"
  "v_mov_b32 v245, 0\n\t"
  "ds_read_u16_d16_hi v236, %[vstc] offset:0\n\t"
  "s_waitcnt lgkmcnt(8)\n\t"
  "v_mfma_f32_16x16x32_bf16 v[44:47], a[12:15], v[92:95], v[44:47]\n\t"
  "ds_read_b64_tr_b16 v[86:87], v145 offset:14336\n\t"
  "ds_read_u16_d16_hi v237, %[vstc] offset:8192\n\t"
  "v_mfma_f32_16x16x32_bf16 v[48:51], a[16:19], v[92:95], v[48:51]\n\t"
  "ds_read_u16_d16_hi v238, %[vstc] offset:16384\n\t"
  "v_mfma_f32_16x16x32_bf16 v[52:55], a[20:23], v[92:95], v[52:55]\n\t"
  "ds_read_u16_d16_hi v239, %[vstc] offset:24576\n\t"
  "s_waitcnt lgkmcnt(10)\n\t"
  "v_mfma_f32_16x16x32_bf16 v[32:35], a[24:27], v[96:99], v[32:35]\n\t"
  "ds_read_b64_tr_b16 v[88:89], v144 offset:16384\n\t"
  "ds_read_u16_d16_hi v240, %[vstc] offset:32768\n\t"
  "ds_read_u16_d16_hi v241, v248 offset:0\n\t"
  "v_mfma_f32_16x16x32_bf16 v[36:39], a[28:31], v[96:99], v[36:39]\n\t"
  "ds_read_b64_tr_b16 v[90:91], v144 offset:18432\n\t"
  "ds_read_u16_d16_hi v242, v248 offset:8192\n\t"
  "v_mfma_f32_16x16x32_bf16 v[40:43], a[32:35], v[96:99], v[40:43]\n\t"
  "ds_read_b64_tr_b16 v[92:93], v145 offset:16384\n\t"
  "ds_read_u16_d16_hi v243, v248 offset:16384\n\t"
  "s_waitcnt lgkmcnt(15)\n\t"
  "v_mfma_f32_16x16x32_bf16 v[44:47], a[24:27], v[100:103], v[44:47]\n\t"
  "ds_read_b64_tr_b16 v[94:95], v145 offset:18432\n\t"
  "ds_read_u16_d16_hi v244, v248 offset:24576\n\t"
  "v_mfma_f32_16x16x32_bf16 v[48:51], a[28:31], v[100:103], v[48:51]\n\t"
  "ds_read_u16_d16_hi v245, v248 offset:32768\n\t"
  "v_mfma_f32_16x16x32_bf16 v[52:55], a[32:35], v[100:103], v[52:55]\n\t"
  "s_waitcnt lgkmcnt(15)\n\t"
  "v_mfma_f32_16x16x32_bf16 v[32:35], a[36:39], v[80:83], v[32:35]\n\t"
  "v_add_u32 v146, s45, v232\n\t"
  "v_add_u32 v147, s45, v233\n\t"
  "v_mfma_f32_16x16x32_bf16 v[36:39], a[40:43], v[80:83], v[36:39]\n\t"
  "ds_read_b64_tr_b16 v[96:97], v146 offset:0\n\t"
  "v_mfma_f32_16x16x32_bf16 v[40:43], a[44:47], v[80:83], v[40:43]\n\t"
  "ds_read_b64_tr_b16 v[98:99], v146 offset:2048\n\t"
  "s_waitcnt lgkmcnt(15)\n\t"
  "v_mfma_f32_16x16x32_bf16 v[44:47], a[36:39], v[84:87], v[44:47]\n\t"
  "ds_read_b64_tr_b16 v[100:101], v147 offset:0\n\t"
  "s_waitcnt lgkmcnt(15)\n\t"
  "v_add_f32 v246, v236, v237\n\t"
  "v_mfma_f32_16x16x32_bf16 v[48:51], a[40:43], v[84:87], v[48:51]\n\t"
  "ds_read_b64_tr_b16 v[102:103], v147 offset:2048\n\t"
  "v_mul_f32 v247, v236, v236\n\t"
  "v_mfma_f32_16x16x32_bf16 v[52:55], a[44:47], v[84:87], v[52:55]\n\t"
  "v_fmac_f32 v247, v237, v237\n\t"
  "s_waitcnt lgkmcnt(10)\n\t"
  "v_mfma_f32_16x16x32_bf16 v[32:35], a[48:51], v[88:91], v[32:35]\n\t"
  "ds_read_b64_tr_b16 v[80:81], v146 offset:4096\n\t"
  "v_add_f32 v246, v246, v238\n\t"
  "v_mfma_f32_16x16x32_bf16 v[36:39], a[52:55], v[88:91], v[36:39]\n\t"
  "ds_read_b64_tr_b16 v[82:83], v146 offset:6144\n\t"
  "v_fmac_f32 v247, v238, v238\n\t"
  "v_add_f32 v246, v246, v239\n\t"
  "v_mfma_f32_16x16x32_bf16 v[40:43], a[56:59], v[88:91], v[40:43]\n\t"
  "ds_read_b64_tr_b16 v[84:85], v147 offset:4096\n\t"
  "v_fmac_f32 v247, v239, v239\n\t"
  "s_waitcnt lgkmcnt(9)\n\t"
  "v_mfma_f32_16x16x32_bf16 v[44:47], a[48:51], v[92:95], v[44:47]\n\t"
  "ds_read_b64_tr_b16 v[86:87], v147 offset:6144\n\t"
  "v_add_f32 v246, v246, v240\n\t"
  "v_mfma_f32_16x16x32_bf16 v[48:51], a[52:55], v[92:95], v[48:51]\n\t"
  "v_fmac_f32 v247, v240, v240\n\t"
  "v_mfma_f32_16x16x32_bf16 v[52:55], a[56:59], v[92:95], v[52:55]\n\t"
  "v_add_f32 v246, v246, v241\n\t"
  "s_waitcnt lgkmcnt(6)\n\t"
  "v_mfma_f32_16x16x32_bf16 v[32:35], a[60:63], v[96:99], v[32:35]\n\t"
  "ds_read_b64_tr_b16 v[88:89], v146 offset:8192\n\t"
  "v_fmac_f32 v247, v241, v241\n\t"
  "v_add_f32 v246, v246, v242\n\t"
  "v_mfma_f32_16x16x32_bf16 v[36:39], a[64:67], v[96:99], v[36:39]\n\t"
  "ds_read_b64_tr_b16 v[90:91], v146 offset:10240\n\t"
  "v_fmac_f32 v247, v242, v242\n\t"
  "s_waitcnt vmcnt(62)\n\t"
  "v_mfma_f32_16x16x32_bf16 v[40:43], a[68:71], v[96:99], v[40:43]\n\t"
  "ds_read_b64_tr_b16 v[92:93], v147 offset:8192\n\t"
  "v_add_f32 v246, v246, v243\n\t"
  "s_waitcnt lgkmcnt(7)\n\t"
  "v_mfma_f32_16x16x32_bf16 v[44:47], a[60:63], v[100:103], v[44:47]\n\t"
  "ds_read_b64_tr_b16 v[94:95], v147 offset:10240\n\t"
  "v_fmac_f32 v247, v243, v243\n\t"
  "v_mfma_f32_16x16x32_bf16 v[48:51], a[64:67], v[100:103], v[48:51]\n\t"
  "v_add_f32 v246, v246, v244\n\t"
  "v_fmac_f32 v247, v244, v244\n\t"
  "v_mfma_f32_16x16x32_bf16 v[52:55], a[68:71], v[100:103], v[52:55]\n\t"
  "v_add_f32 v246, v246, v245\n\t"
  "s_waitcnt vmcnt(61) lgkmcnt(6)\n\t"
  "v_mfma_f32_16x16x32_bf16 v[32:35], a[72:75], v[80:83], v[32:35]\n\t"
  "ds_read_b64_tr_b16 v[96:97], v146 offset:12288\n\t"
  "v_fmac_f32 v247, v245, v245\n\t"
  "s_waitcnt vmcnt(60)\n\t"
  "v_mfma_f32_16x16x32_bf16 v[36:39], a[76:79], v[80:83], v[36:39]\n\t"
  "ds_read_b64_tr_b16 v[98:99], v146 offset:14336\n\t"
  "v_add_f32_dpp v246, v246, v246 quad_perm:[1,0,3,2] row_mask:0xf bank_mask:0xf\n\t"
  "s_waitcnt vmcnt(59)\n\t"
  "v_mfma_f32_16x16x32_bf16 v[40:43], a[80:83], v[80:83], v[40:43]\n\t"
  "ds_read_b64_tr_b16 v[100:101], v147 offset:12288\n\t"
  "v_add_f32_dpp v247, v247, v247 quad_perm:[1,0,3,2] row_mask:0xf bank_mask:0xf\n\t"
  "s_waitcnt lgkmcnt(7)\n\t"
  "v_mfma_f32_16x16x32_bf16 v[44:47], a[72:75], v[84:87], v[44:47]\n\t"
  "ds_read_b64_tr_b16 v[102:103], v147 offset:14336\n\t"
  "v_add_f32_dpp v246, v246, v246 quad_perm:[2,3,0,1] row_mask:0xf bank_mask:0xf\n\t"
  "v_mfma_f32_16x16x32_bf16 v[48:51], a[76:79], v[84:87], v[48:51]\n\t"
  "v_add_f32_dpp v247, v247, v247 quad_perm:[2,3,0,1] row_mask:0xf bank_mask:0xf\n\t"
  "v_mfma_f32_16x16x32_bf16 v[52:55], a[80:83], v[84:87], v[52:55]\n\t"
  "s_waitcnt vmcnt(58) lgkmcnt(6)\n\t"
  "v_mfma_f32_16x16x32_bf16 v[32:35], a[84:87], v[88:91], v[32:35]\n\t"
  "ds_read_b64_tr_b16 v[80:81], v146 offset:16384\n\t"
  "v_add_f32_dpp v246, v246, v246 row_half_mirror row_mask:0xf bank_mask:0xf\n\t"
  "v_add_f32_dpp v247, v247, v247 row_half_mirror row_mask:0xf bank_mask:0xf\n\t"
  "s_waitcnt vmcnt(57)\n\t"
  "v_mfma_f32_16x16x32_bf16 v[36:39], a[88:91], v[88:91], v[36:39]\n\t"
  "ds_read_b64_tr_b16 v[82:83], v146 offset:18432\n\t"
  "s_waitcnt vmcnt(56)\n\t"
  "v_mfma_f32_16x16x32_bf16 v[40:43], a[92:95], v[88:91], v[40:43]\n\t"
  "ds_read_b64_tr_b16 v[84:85], v147 offset:16384\n\t"
  "v_add_f32_dpp v246, v246, v246 row_mirror row_mask:0xf bank_mask:0xf\n\t"
  "s_waitcnt lgkmcnt(7)\n\t"
  "v_mfma_f32_16x16x32_bf16 v[44:47], a[84:87], v[92:95], v[44:47]\n\t"
  "ds_read_b64_tr_b16 v[86:87], v147 offset:18432\n\t"
  "v_add_f32_dpp v247, v247, v247 row_mirror row_mask:0xf bank_mask:0xf\n\t"
  "v_mfma_f32_16x16x32_bf16 v[48:51], a[88:91], v[92:95], v[48:51]\n\t"
  "v_mfma_f32_16x16x32_bf16 v[52:55], a[92:95], v[92:95], v[52:55]\n\t"
  "v_add_f32_dpp v246, v246, v246 row_bcast:15 row_mask:0xa bank_mask:0xf\n\t"
  "v_add_f32_dpp v247, v247, v247 row_bcast:15 row_mask:0xa bank_mask:0xf\n\t"
  "s_waitcnt vmcnt(55) lgkmcnt(6)\n\t"
  "v_mfma_f32_16x16x32_bf16 v[32:35], a[96:99], v[96:99], v[32:35]\n\t"
  "v_add_u32 v144, s46, v232\n\t"
  "v_add_u32 v145, s46, v233\n\t"
  "s_waitcnt vmcnt(54)\n\t"
  "v_mfma_f32_16x16x32_bf16 v[36:39], a[100:103], v[96:99], v[36:39]\n\t"
  "ds_read_b64_tr_b16 v[88:89], v144 offset:0\n\t"
  "v_add_f32_dpp v246, v246, v246 row_bcast:31 row_mask:0xc bank_mask:0xf\n\t"
  "s_waitcnt vmcnt(53)\n\t"
  "v_mfma_f32_16x16x32_bf16 v[40:43], a[104:107], v[96:99], v[40:43]\n\t"
  "ds_read_b64_tr_b16 v[90:91], v144 offset:2048\n\t"
  "v_add_f32_dpp v247, v247, v247 row_bcast:31 row_mask:0xc bank_mask:0xf\n\t"
  "s_waitcnt lgkmcnt(6)\n\t"
  "v_mfma_f32_16x16x32_bf16 v[44:47], a[96:99], v[100:103], v[44:47]\n\t"
  "ds_read_b64_tr_b16 v[92:93], v145 offset:0\n\t"
  "v_mul_f32 v246, 0x3acccccd, v246\n\t"
  "v_mfma_f32_16x16x32_bf16 v[48:51], a[100:103], v[100:103], v[48:51]\n\t"
  "ds_read_b64_tr_b16 v[94:95], v145 offset:2048\n\t"
  "v_mul_f32 v247, 0x3acccccd, v247\n\t"
  "v_mfma_f32_16x16x32_bf16 v[52:55], a[104:107], v[100:103], v[52:55]\n\t"
  "v_fma_f32 v247, -v246, v246, v247\n\t"
  "s_waitcnt vmcnt(52) lgkmcnt(6)\n\t"
  "v_mfma_f32_16x16x32_bf16 v[32:35], a[108:111], v[80:83], v[32:35]\n\t"
  "ds_read_b64_tr_b16 v[96:97], v144 offset:4096\n\t"
  "v_max_f32 v247, 0, v247\n\t"
  "s_waitcnt vmcnt(51)\n\t"
  "v_mfma_f32_16x16x32_bf16 v[36:39], a[112:115], v[80:83], v[36:39]\n\t"
  "ds_read_b64_tr_b16 v[98:99], v144 offset:6144\n\t"
  "v_add_f32 v247, 0x3727c5ac, v247\n\t"
  "v_rsq_f32 v236, v247\n\t"
  "s_waitcnt vmcnt(50)\n\t"
  "v_mfma_f32_16x16x32_bf16 v[40:43], a[116:119], v[80:83], v[40:43]\n\t"
  "ds_read_b64_tr_b16 v[100:101], v145 offset:4096\n\t"
  "s_waitcnt lgkmcnt(7)\n\t"
  "v_mfma_f32_16x16x32_bf16 v[44:47], a[108:111], v[84:87], v[44:47]\n\t"
  "ds_read_b64_tr_b16 v[102:103], v145 offset:6144\n\t"
  "v_mul_f32_e64 v237, -v246, v236\n\t"
  "v_mfma_f32_16x16x32_bf16 v[48:51], a[112:115], v[84:87], v[48:51]\n\t"
  "v_or_b32 v236, 1, v236\n\t"
  "v_mfma_f32_16x16x32_bf16 v[52:55], a[116:119], v[84:87], v[52:55]\n\t"
  "buffer_store_dwordx2 v[236:237], %[vstw], %[srd], %[sST] offen sc1\n\t"
  "s_waitcnt vmcnt(50) lgkmcnt(6)\n\t"
  "v_mfma_f32_16x16x32_bf16 v[32:35], a[120:123], v[88:91], v[32:35]\n\t"
  "ds_read_b64_tr_b16 v[80:81], v144 offset:8192\n\t"
  "s_waitcnt vmcnt(49)\n\t"
  "v_mfma_f32_16x16x32_bf16 v[36:39], a[124:127], v[88:91], v[36:39]\n\t"
  "ds_read_b64_tr_b16 v[82:83], v144 offset:10240\n\t"
  "s_waitcnt vmcnt(48)\n\t"
  "v_mfma_f32_16x16x32_bf16 v[40:43], a[128:131], v[88:91], v[40:43]\n\t"
  "ds_read_b64_tr_b16 v[84:85], v145 offset:8192\n\t"
  "s_waitcnt lgkmcnt(7)\n\t"
  "v_mfma_f32_16x16x32_bf16 v[44:47], a[120:123], v[92:95], v[44:47]\n\t"
  "ds_read_b64_tr_b16 v[86:87], v145 offset:10240\n\t"
  "v_mfma_f32_16x16x32_bf16 v[48:51], a[124:127], v[92:95], v[48:51]\n\t"
  "v_mfma_f32_16x16x32_bf16 v[52:55], a[128:131], v[92:95], v[52:55]\n\t"
  "s_waitcnt vmcnt(47) lgkmcnt(6)\n\t"
  "v_mfma_f32_16x16x32_bf16 v[32:35], a[132:135], v[96:99], v[32:35]\n\t"
  "ds_read_b64_tr_b16 v[88:89], v144 offset:12288\n\t"
  "s_waitcnt vmcnt(46)\n\t"
  "v_mfma_f32_16x16x32_bf16 v[36:39], a[136:139], v[96:99], v[36:39]\n\t"
  "ds_read_b64_tr_b16 v[90:91], v144 offset:14336\n\t"
  "s_waitcnt vmcnt(45)\n\t"
  "v_mfma_f32_16x16x32_bf16 v[40:43], a[140:143], v[96:99], v[40:43]\n\t"
  "ds_read_b64_tr_b16 v[92:93], v145 offset:12288\n\t"
  "s_waitcnt lgkmcnt(7)\n\t"
  "v_mfma_f32_16x16x32_bf16 v[44:47], a[132:135], v[100:103], v[44:47]\n\t"
  "ds_read_b64_tr_b16 v[94:95], v145 offset:14336\n\t"
  "v_mfma_f32_16x16x32_bf16 v[48:51], a[136:139], v[100:103], v[48:51]\n\t"
  "v_mfma_f32_16x16x32_bf16 v[52:55], a[140:143], v[100:103], v[52:55]\n\t"
  "s_waitcnt vmcnt(44) lgkmcnt(6)\n\t"
  "v_mfma_f32_16x16x32_bf16 v[32:35], a[144:147], v[80:83], v[32:35]\n\t"
  "ds_read_b64_tr_b16 v[96:97], v144 offset:16384\n\t"
  "s_waitcnt vmcnt(43)\n\t"
  "v_mfma_f32_16x16x32_bf16 v[36:39], a[148:151], v[80:83], v[36:39]\n\t"
  "ds_read_b64_tr_b16 v[98:99], v144 offset:18432\n\t"
  "s_waitcnt vmcnt(42)\n\t"
  "v_mfma_f32_16x16x32_bf16 v[40:43], a[152:155], v[80:83], v[40:43]\n\t"
  "ds_read_b64_tr_b16 v[100:101], v145 offset:16384\n\t"
  "s_waitcnt lgkmcnt(7)\n\t"
  "v_mfma_f32_16x16x32_bf16 v[44:47], a[144:147], v[84:87], v[44:47]\n\t"
  "ds_read_b64_tr_b16 v[102:103], v145 offset:18432\n\t"
  "v_mfma_f32_16x16x32_bf16 v[48:51], a[148:151], v[84:87], v[48:51]\n\t"
  "v_mfma_f32_16x16x32_bf16 v[52:55], a[152:155], v[84:87], v[52:55]\n\t"
  "s_waitcnt vmcnt(41) lgkmcnt(6)\n\t"
  "v_mfma_f32_16x16x32_bf16 v[32:35], a[156:159], v[88:91], v[32:35]\n\t"
  "v_add_u32 v146, s47, v232\n\t"
  "v_add_u32 v147, s47, v233\n\t"
  "s_waitcnt vmcnt(40)\n\t"
  "v_mfma_f32_16x16x32_bf16 v[36:39], a[160:163], v[88:91], v[36:39]\n\t"
  "ds_read_b64_tr_b16 v[80:81], v146 offset:0\n\t"
  "s_waitcnt vmcnt(39)\n\t"
  "v_mfma_f32_16x16x32_bf16 v[40:43], a[164:167], v[88:91], v[40:43]\n\t"
  "ds_read_b64_tr_b16 v[82:83], v146 offset:2048\n\t"
  "s_waitcnt lgkmcnt(6)\n\t"
  "v_mfma_f32_16x16x32_bf16 v[44:47], a[156:159], v[92:95], v[44:47]\n\t"
  "ds_read_b64_tr_b16 v[84:85], v147 offset:0\n\t"
  "v_mfma_f32_16x16x32_bf16 v[48:51], a[160:163], v[92:95], v[48:51]\n\t"
  "ds_read_b64_tr_b16 v[86:87], v147 offset:2048\n\t"
  "v_mfma_f32_16x16x32_bf16 v[52:55], a[164:167], v[92:95], v[52:55]\n\t"
  "s_waitcnt vmcnt(38) lgkmcnt(6)\n\t"
  "v_mfma_f32_16x16x32_bf16 v[32:35], a[168:171], v[96:99], v[32:35]\n\t"
  "ds_read_b64_tr_b16 v[88:89], v146 offset:4096\n\t"
  "s_waitcnt vmcnt(1)\n\t"
  "s_barrier\n\t"
  "v_mfma_f32_16x16x32_bf16 v[36:39], a[172:175], v[96:99], v[36:39]\n\t"
  "ds_read_b64_tr_b16 v[90:91], v146 offset:6144\n\t"
  "v_add_u32 v248, 122880, %[vstc]\n\t"
  "v_add_u32 v249, 81920, %[vstc]\n\t"
  "v_mfma_f32_16x16x32_bf16 v[40:43], a[176:179], v[96:99], v[40:43]\n\t"
  "ds_read_b64_tr_b16 v[92:93], v147 offset:4096\n\t"
  "v_mov_b32 v236, 0\n\t"
  "s_waitcnt lgkmcnt(7)\n\t"
  "v_mfma_f32_16x16x32_bf16 v[44:47], a[168:171], v[100:103], v[44:47]\n\t"
  "ds_read_b64_tr_b16 v[94:95], v147 offset:6144\n\t"
  "v_mov_b32 v237, 0\n\t"
  "v_mfma_f32_16x16x32_bf16 v[48:51], a[172:175], v[100:103], v[48:51]\n\t"
  "v_mov_b32 v238, 0\n\t"
  "v_mfma_f32_16x16x32_bf16 v[52:55], a[176:179], v[100:103], v[52:55]\n\t"
  "v_mov_b32 v239, 0\n\t"
  "s_waitcnt lgkmcnt(6)\n\t"
  "v_mfma_f32_16x16x32_bf16 v[32:35], a[180:183], v[80:83], v[32:35]\n\t"
  "ds_read_b64_tr_b16 v[96:97], v146 offset:8192\n\t"
  "v_mov_b32 v240, 0\n\t"
  "v_mov_b32 v241, 0\n\t"
  "v_mfma_f32_16x16x32_bf16 v[36:39], a[184:187], v[80:83], v[36:39]\n\t"
  "ds_read_b64_tr_b16 v[98:99], v146 offset:10240\n\t"
  "v_mov_b32 v242, 0\n\t"
  "v_mfma_f32_16x16x32_bf16 v[40:43], a[188:191], v[80:83], v[40:43]\n\t"
  "ds_read_b64_tr_b16 v[100:101], v147 offset:8192\n\t"
  "v_mov_b32 v243, 0\n\t"
  "s_waitcnt lgkmcnt(7)\n\t"
  "v_mfma_f32_16x16x32_bf16 v[44:47], a[180:183], v[84:87], v[44:47]\n\t"
  "ds_read_b64_tr_b16 v[102:103], v147 offset:10240\n\t"
  "v_mov_b32 v244, 0\n\t"
  "v_mfma_f32_16x16x32_bf16 v[48:51], a[184:187], v[84:87], v[48:51]\n\t"
  "v_mov_b32 v245, 0\n\t"
  "v_mfma_f32_16x16x32_bf16 v[52:55], a[188:191], v[84:87], v[52:55]\n\t"
  "ds_read_u16_d16_hi v236, v249 offset:0\n\t"
  "s_waitcnt lgkmcnt(7)\n\t"
  "v_mfma_f32_16x16x32_bf16 v[32:35], a[192:195], v[88:91], v[32:35]\n\t"
  "ds_read_b64_tr_b16 v[80:81], v146 offset:12288\n\t"
  "ds_read_u16_d16_hi v237, v249 offset:8192\n\t"
  "ds_read_u16_d16_hi v238, v249 offset:16384\n\t"
  "v_mfma_f32_16x16x32_bf16 v[36:39], a[196:199], v[88:91], v[36:39]\n\t"
  "ds_read_b64_tr_b16 v[82:83], v146 offset:14336\n\t"
  "ds_read_u16_d16_hi v239, v249 offset:24576\n\t"
  "v_mfma_f32_16x16x32_bf16 v[40:43], a[200:203], v[88:91], v[40:43]\n\t"
  "ds_read_b64_tr_b16 v[84:85], v147 offset:12288\n\t"
  "ds_read_u16_d16_hi v240, v249 offset:32768\n\t"
  "s_waitcnt lgkmcnt(12)\n\t"
  "v_mfma_f32_16x16x32_bf16 v[44:47], a[192:195], v[92:95], v[44:47]\n\t"
  "ds_read_b64_tr_b16 v[86:87], v147 offset:14336\n\t"
  "ds_read_u16_d16_hi v241, v248 offset:0\n\t"
  "v_mfma_f32_16x16x32_bf16 v[48:51], a[196:199], v[92:95], v[48:51]\n\t"
  "ds_read_u16_d16_hi v242, v248 offset:8192\n\t"
  "v_mfma_f32_16x16x32_bf16 v[52:55], a[200:203], v[92:95], v[52:55]\n\t"
  "ds_read_u16_d16_hi v243, v248 offset:16384\n\t"
  "s_waitcnt lgkmcnt(14)\n\t"
  "v_mfma_f32_16x16x32_bf16 v[32:35], a[204:207], v[96:99], v[32:35]\n\t"
  "ds_read_b64_tr_b16 v[88:89], v146 offset:16384\n\t"
  "ds_read_u16_d16_hi v244, v248 offset:24576\n\t"
  "ds_read_u16_d16_hi v245, v248 offset:32768\n\t"
  "v_mfma_f32_16x16x32_bf16 v[36:39], a[208:211], v[96:99], v[36:39]\n\t"
  "ds_read_b64_tr_b16 v[90:91], v146 offset:18432\n\t"
  "v_mfma_f32_16x16x32_bf16 v[40:43], a[212:215], v[96:99], v[40:43]\n\t"
  "ds_read_b64_tr_b16 v[92:93], v147 offset:16384\n\t"
  "s_waitcnt lgkmcnt(15)\n\t"
  "v_mfma_f32_16x16x32_bf16 v[44:47], a[204:207], v[100:103], v[44:47]\n\t"
  "ds_read_b64_tr_b16 v[94:95], v147 offset:18432\n\t"
  "v_mfma_f32_16x16x32_bf16 v[48:51], a[208:211], v[100:103], v[48:51]\n\t"
  "v_mfma_f32_16x16x32_bf16 v[52:55], a[212:215], v[100:103], v[52:55]\n\t"
  "s_waitcnt lgkmcnt(13)\n\t"
  "v_mfma_f32_16x16x32_bf16 v[32:35], a[216:219], v[80:83], v[32:35]\n\t"
  "v_add_u32 v144, s44, v234\n\t"
  "v_add_u32 v145, s44, v235\n\t"
  "v_add_f32 v246, v236, v237\n\t"
  "v_mfma_f32_16x16x32_bf16 v[36:39], a[220:223], v[80:83], v[36:39]\n\t"
  "ds_read_b64_tr_b16 v[96:97], v144 offset:0\n\t"
  "v_mul_f32 v247, v236, v236\n\t"
  "v_mfma_f32_16x16x32_bf16 v[40:43], a[224:227], v[80:83], v[40:43]\n\t"
  "ds_read_b64_tr_b16 v[98:99], v144 offset:2048\n\t"
  "v_fmac_f32 v247, v237, v237\n\t"
  "s_waitcnt lgkmcnt(11)\n\t"
  "v_mfma_f32_16x16x32_bf16 v[44:47], a[216:219], v[84:87], v[44:47]\n\t"
  "ds_read_b64_tr_b16 v[100:101], v145 offset:0\n\t"
  "v_add_f32 v246, v246, v238\n\t"
  "v_mfma_f32_16x16x32_bf16 v[48:51], a[220:223], v[84:87], v[48:51]\n\t"
  "ds_read_b64_tr_b16 v[102:103], v145 offset:2048\n\t"
  "v_fmac_f32 v247, v238, v238\n\t"
  "v_mfma_f32_16x16x32_bf16 v[52:55], a[224:227], v[84:87], v[52:55]\n\t"
  "v_add_f32 v246, v246, v239\n\t"
  "s_waitcnt lgkmcnt(6)\n\t"
  "v_mfma_f32_16x16x32_bf16 v[32:35], a[228:231], v[88:91], v[32:35]\n\t"
  "ds_read_b64_tr_b16 v[80:81], v144 offset:4096\n\t"
  "v_fmac_f32 v247, v239, v239\n\t"
  "v_add_f32 v246, v246, v240\n\t"
  "v_mfma_f32_16x16x32_bf16 v[36:39], a[232:235], v[88:91], v[36:39]\n\t"
  "ds_read_b64_tr_b16 v[82:83], v144 offset:6144\n\t"
  "v_fmac_f32 v247, v240, v240\n\t"
  "v_mfma_f32_16x16x32_bf16 v[40:43], a[236:239], v[88:91], v[40:43]\n\t"
  "ds_read_b64_tr_b16 v[84:85], v145 offset:4096\n\t"
  "v_add_f32 v246, v246, v241\n\t"
  "s_waitcnt lgkmcnt(7)\n\t"
  "v_mfma_f32_16x16x32_bf16 v[44:47], a[228:231], v[92:95], v[44:47]\n\t"
  "ds_read_b64_tr_b16 v[86:87], v145 offset:6144\n\t"
  "v_fmac_f32 v247, v241, v241\n\t"
  "v_mfma_f32_16x16x32_bf16 v[48:51], a[232:235], v[92:95], v[48:51]\n\t"
  "v_add_f32 v246, v246, v242\n\t"
  "v_mfma_f32_16x16x32_bf16 v[52:55], a[236:239], v[92:95], v[52:55]\n\t"
  "v_fmac_f32 v247, v242, v242\n\t"
  "s_waitcnt lgkmcnt(6)\n\t"
  "v_mfma_f32_16x16x32_bf16 v[56:59], a[0:3], v[96:99], 0\n\t"
  "ds_read_b64_tr_b16 v[88:89], v144 offset:8192\n\t"
  "v_lshrrev_b32 v170, 1, %[vl16]\n\t"
  "v_add_f32 v246, v246, v243\n\t"
  "v_fmac_f32 v247, v243, v243\n\t"
  "v_mfma_f32_16x16x32_bf16 v[60:63], a[4:7], v[96:99], 0\n\t"
  "ds_read_b64_tr_b16 v[90:91], v144 offset:10240\n\t"
  "v_and_b32 v170, 0x78, v170\n\t"
  "v_add_f32 v246, v246, v244\n\t"
  "v_mfma_f32_16x16x32_bf16 v[64:67], a[8:11], v[96:99], 0\n\t"
  "ds_read_b64_tr_b16 v[92:93], v145 offset:8192\n\t"
  "buffer_load_dwordx2 v[128:129], v170, %[srd], %[sST] offen offset:0 sc1\n\t"
  "v_fmac_f32 v247, v244, v244\n\t"
  "s_waitcnt lgkmcnt(7)\n\t"
  "v_mfma_f32_16x16x32_bf16 v[68:71], a[0:3], v[100:103], 0\n\t"
  "ds_read_b64_tr_b16 v[94:95], v145 offset:10240\n\t"
  "buffer_load_dwordx2 v[130:131], v170, %[srd], %[sST] offen offset:128 sc1\n\t"
  "v_add_f32 v246, v246, v245\n\t"
  "v_mfma_f32_16x16x32_bf16 v[72:75], a[4:7], v[100:103], 0\n\t"
  "buffer_load_dwordx2 v[132:133], v170, %[srd], %[sST] offen offset:256 sc1\n\t"
  "v_fmac_f32 v247, v245, v245\n\t"
  "v_mfma_f32_16x16x32_bf16 v[76:79], a[8:11], v[100:103], 0\n\t"
  "buffer_load_dwordx2 v[134:135], v170, %[srd], %[sST] offen offset:384 sc1\n\t"
  "v_add_f32_dpp v246, v246, v246 quad_perm:[1,0,3,2] row_mask:0xf bank_mask:0xf\n\t"
  "s_waitcnt lgkmcnt(6)\n\t"
  "v_mfma_f32_16x16x32_bf16 v[56:59], a[12:15], v[80:83], v[56:59]\n\t"
  "ds_read_b64_tr_b16 v[96:97], v144 offset:12288\n\t"
  "v_add_f32_dpp v247, v247, v247 quad_perm:[1,0,3,2] row_mask:0xf bank_mask:0xf\n\t"
  "ds_read_b64_tr_b16 v[172:173], %[vres0] offset:0\n\t"
  "v_mfma_f32_16x16x32_bf16 v[60:63], a[16:19], v[80:83], v[60:63]\n\t"
  "ds_read_b64_tr_b16 v[98:99], v144 offset:14336\n\t"
  "v_add_f32_dpp v246, v246, v246 quad_perm:[2,3,0,1] row_mask:0xf bank_mask:0xf\n\t"
  "v_mfma_f32_16x16x32_bf16 v[64:67], a[20:23], v[80:83], v[64:67]\n\t"
  "ds_read_b64_tr_b16 v[100:101], v145 offset:12288\n\t"
  "v_add_f32_dpp v247, v247, v247 quad_perm:[2,3,0,1] row_mask:0xf bank_mask:0xf\n\t"
  "s_waitcnt lgkmcnt(8)\n\t"
  "v_mfma_f32_16x16x32_bf16 v[68:71], a[12:15], v[84:87], v[68:71]\n\t"
  "ds_read_b64_tr_b16 v[102:103], v145 offset:14336\n\t"
  "v_mfma_f32_16x16x32_bf16 v[72:75], a[16:19], v[84:87], v[72:75]\n\t"
  "v_add_f32_dpp v246, v246, v246 row_half_mirror row_mask:0xf bank_mask:0xf\n\t"
  "ds_read_b64_tr_b16 v[174:175], %[vres1] offset:0\n\t"
  "v_mfma_f32_16x16x32_bf16 v[76:79], a[20:23], v[84:87], v[76:79]\n\t"
  "v_add_f32_dpp v247, v247, v247 row_half_mirror row_mask:0xf bank_mask:0xf\n\t"
  "s_waitcnt lgkmcnt(8)\n\t"
  "v_mfma_f32_16x16x32_bf16 v[56:59], a[24:27], v[88:91], v[56:59]\n\t"
  "ds_read_b64_tr_b16 v[80:81], v144 offset:16384\n\t"
  "v_add_f32_dpp v246, v246, v246 row_mirror row_mask:0xf bank_mask:0xf\n\t"
  "v_mfma_f32_16x16x32_bf16 v[60:63], a[28:31], v[88:91], v[60:63]\n\t"
  "ds_read_b64_tr_b16 v[82:83], v144 offset:18432\n\t"
  "v_add_f32_dpp v247, v247, v247 row_mirror row_mask:0xf bank_mask:0xf\n\t"
  "v_mfma_f32_16x16x32_bf16 v[64:67], a[32:35], v[88:91], v[64:67]\n\t"
  "ds_read_b64_tr_b16 v[84:85], v145 offset:16384\n\t"
  "s_waitcnt lgkmcnt(9)\n\t"
  "v_mfma_f32_16x16x32_bf16 v[68:71], a[24:27], v[92:95], v[68:71]\n\t"
  "ds_read_b64_tr_b16 v[86:87], v145 offset:18432\n\t"
  "v_add_f32_dpp v246, v246, v246 row_bcast:15 row_mask:0xa bank_mask:0xf\n\t"
  "ds_read_b64_tr_b16 v[176:177], %[vres0] offset:4096\n\t"
  "v_mfma_f32_16x16x32_bf16 v[72:75], a[28:31], v[92:95], v[72:75]\n\t"
  "v_add_f32_dpp v247, v247, v247 row_bcast:15 row_mask:0xa bank_mask:0xf\n\t"
  "v_mfma_f32_16x16x32_bf16 v[76:79], a[32:35], v[92:95], v[76:79]\n\t"
  "s_waitcnt lgkmcnt(8)\n\t"
  "v_mfma_f32_16x16x32_bf16 v[56:59], a[36:39], v[96:99], v[56:59]\n\t"
  "v_add_u32 v146, s45, v234\n\t"
  "v_add_u32 v147, s45, v235\n\t"
  "v_add_f32_dpp v246, v246, v246 row_bcast:31 row_mask:0xc bank_mask:0xf\n\t"
  "v_add_f32_dpp v247, v247, v247 row_bcast:31 row_mask:0xc bank_mask:0xf\n\t"
  "v_mfma_f32_16x16x32_bf16 v[60:63], a[40:43], v[96:99], v[60:63]\n\t"
  "ds_read_b64_tr_b16 v[88:89], v146 offset:0\n\t"
  "v_mfma_f32_16x16x32_bf16 v[64:67], a[44:47], v[96:99], v[64:67]\n\t"
  "ds_read_b64_tr_b16 v[90:91], v146 offset:2048\n\t"
  "v_mul_f32 v246, 0x3acccccd, v246\n\t"
  "ds_read_b64_tr_b16 v[178:179], %[vres1] offset:4096\n\t"
  "s_waitcnt lgkmcnt(9)\n\t"
  "v_mfma_f32_16x16x32_bf16 v[68:71], a[36:39], v[100:103], v[68:71]\n\t"
  "ds_read_b64_tr_b16 v[92:93], v147 offset:0\n\t"
  "v_mul_f32 v247, 0x3acccccd, v247\n\t"
  "v_mfma_f32_16x16x32_bf16 v[72:75], a[40:43], v[100:103], v[72:75]\n\t"
  "ds_read_b64_tr_b16 v[94:95], v147 offset:2048\n\t"
  "v_fma_f32 v247, -v246, v246, v247\n\t"
  "v_mfma_f32_16x16x32_bf16 v[76:79], a[44:47], v[100:103], v[76:79]\n\t"
  "v_max_f32 v247, 0, v247\n\t"
  "s_waitcnt lgkmcnt(8)\n\t"
  "v_mfma_f32_16x16x32_bf16 v[56:59], a[48:51], v[80:83], v[56:59]\n\t"
  "ds_read_b64_tr_b16 v[96:97], v146 offset:4096\n\t"
  "v_add_f32 v247, 0x3727c5ac, v247\n\t"
  "v_rsq_f32 v236, v247\n\t"
  "v_mfma_f32_16x16x32_bf16 v[60:63], a[52:55], v[80:83], v[60:63]\n\t"
  "ds_read_b64_tr_b16 v[98:99], v146 offset:6144\n\t"
  "ds_read_b64_tr_b16 v[180:181], %[vres2] offset:0\n\t"
  "v_mfma_f32_16x16x32_bf16 v[64:67], a[56:59], v[80:83], v[64:67]\n\t"
  "ds_read_b64_tr_b16 v[100:101], v147 offset:4096\n\t"
  "v_mul_f32_e64 v237, -v246, v236\n\t"
  "s_waitcnt lgkmcnt(10)\n\t"
  "v_mfma_f32_16x16x32_bf16 v[68:71], a[48:51], v[84:87], v[68:71]\n\t"
  "ds_read_b64_tr_b16 v[102:103], v147 offset:6144\n\t"
  "v_or_b32 v236, 1, v236\n\t"
  "v_mfma_f32_16x16x32_bf16 v[72:75], a[52:55], v[84:87], v[72:75]\n\t"
  "s_add_u32 s66, %[sST], 8192\n\t"
  "v_mfma_f32_16x16x32_bf16 v[76:79], a[56:59], v[84:87], v[76:79]\n\t"
  "buffer_store_dwordx2 v[236:237], %[vstw], %[srd], s66 offen sc1\n\t"
  "s_waitcnt lgkmcnt(8)\n\t"
  "v_mfma_f32_16x16x32_bf16 v[56:59], a[60:63], v[88:91], v[56:59]\n\t"
  "ds_read_b64_tr_b16 v[80:81], v146 offset:8192\n\t"
  "v_mfma_f32_16x16x32_bf16 v[60:63], a[64:67], v[88:91], v[60:63]\n\t"
  "ds_read_b64_tr_b16 v[82:83], v146 offset:10240\n\t"
  "v_mfma_f32_16x16x32_bf16 v[64:67], a[68:71], v[88:91], v[64:67]\n\t"
  "ds_read_b64_tr_b16 v[84:85], v147 offset:8192\n\t"
  "s_waitcnt lgkmcnt(8)\n\t"
  "v_mfma_f32_16x16x32_bf16 v[68:71], a[60:63], v[92:95], v[68:71]\n\t"
  "ds_read_b64_tr_b16 v[86:87], v147 offset:10240\n\t"
  "v_mfma_f32_16x16x32_bf16 v[72:75], a[64:67], v[92:95], v[72:75]\n\t"
  "v_mfma_f32_16x16x32_bf16 v[76:79], a[68:71], v[92:95], v[76:79]\n\t"
  "s_waitcnt lgkmcnt(7)\n\t"
  "v_mfma_f32_16x16x32_bf16 v[56:59], a[72:75], v[96:99], v[56:59]\n\t"
  "ds_read_b64_tr_b16 v[88:89], v146 offset:12288\n\t"
  "s_waitcnt vmcnt(3)\n\t"
  "v_min_u32 v171, v128, v130\n\t"
  "s_waitcnt vmcnt(1)\n\t"
  "v_min3_u32 v171, v171, v132, v134\n\t"
  "v_cmp_ne_u32 vcc, 0, v171\n\t"
  "s_nop 3\n\t"
  "s_cmp_eq_u64 vcc, -1\n\t"
  "s_cbranch_scc0 Lst0_slow%=\n\t"
  "Lst0_back%=:\n\t"
  "v_mfma_f32_16x16x32_bf16 v[60:63], a[76:79], v[96:99], v[60:63]\n\t"
  "ds_read_b64_tr_b16 v[90:91], v146 offset:14336\n\t"
  "v_fma_f32 v164, v104, v129, v116\n\t"
  "v_fma_f32 v165, v105, v129, v117\n\t"
  "v_mfma_f32_16x16x32_bf16 v[64:67], a[80:83], v[96:99], v[64:67]\n\t"
  "ds_read_b64_tr_b16 v[92:93], v147 offset:12288\n\t"
  "v_fma_f32 v166, v106, v129, v118\n\t"
  "s_waitcnt lgkmcnt(7)\n\t"
  "v_mfma_f32_16x16x32_bf16 v[68:71], a[72:75], v[100:103], v[68:71]\n\t"
  "ds_read_b64_tr_b16 v[94:95], v147 offset:14336\n\t"
  "v_fma_f32 v167, v107, v129, v119\n\t"
  "v_mfma_f32_16x16x32_bf16 v[72:75], a[76:79], v[100:103], v[72:75]\n\t"
  "v_fma_f32 v32, v32, v128, v164\n\t"
  "v_mfma_f32_16x16x32_bf16 v[76:79], a[80:83], v[100:103], v[76:79]\n\t"
  "v_fma_f32 v33, v33, v128, v165\n\t"
  "v_fma_f32 v34, v34, v128, v166\n\t"
  "s_waitcnt lgkmcnt(6)\n\t"
  "v_mfma_f32_16x16x32_bf16 v[56:59], a[84:87], v[80:83], v[56:59]\n\t"
  "ds_read_b64_tr_b16 v[96:97], v146 offset:16384\n\t"
  "v_fma_f32 v35, v35, v128, v167\n\t"
  "v_mfma_f32_16x16x32_bf16 v[60:63], a[88:91], v[80:83], v[60:63]\n\t"
  "ds_read_b64_tr_b16 v[98:99], v146 offset:18432\n\t"
  "v_max_f32 v32, 0, v32\n\t"
  "v_mfma_f32_16x16x32_bf16 v[64:67], a[92:95], v[80:83], v[64:67]\n\t"
  "ds_read_b64_tr_b16 v[100:101], v147 offset:16384\n\t"
  "v_max_f32 v33, 0, v33\n\t"
  "s_waitcnt lgkmcnt(7)\n\t"
  "v_mfma_f32_16x16x32_bf16 v[68:71], a[84:87], v[84:87], v[68:71]\n\t"
  "ds_read_b64_tr_b16 v[102:103], v147 offset:18432\n\t"
  "v_mul_f32 v32, v32, v34\n\t"
  "v_mul_f32 v33, v33, v35\n\t"
  "v_mfma_f32_16x16x32_bf16 v[72:75], a[88:91], v[84:87], v[72:75]\n\t"
  "v_cvt_pk_bf16_f32 v148, v32, v33\n\t"
  "v_mfma_f32_16x16x32_bf16 v[76:79], a[92:95], v[84:87], v[76:79]\n\t"
  "v_cvt_pk_bf16_f32 v152, v32, v33\n\t"
  "s_waitcnt lgkmcnt(6)\n\t"
  "v_mfma_f32_16x16x32_bf16 v[56:59], a[96:99], v[88:91], v[56:59]\n\t"
  "v_add_u32 v144, s46, v234\n\t"
  "v_add_u32 v145, s46, v235\n\t"
  "v_fma_f32 v164, v108, v129, v120\n\t"
  "v_mfma_f32_16x16x32_bf16 v[60:63], a[100:103], v[88:91], v[60:63]\n\t"
  "ds_read_b64_tr_b16 v[80:81], v144 offset:0\n\t"
  "v_fma_f32 v165, v109, v129, v121\n\t"
  "v_fma_f32 v166, v110, v129, v122\n\t"
  "v_mfma_f32_16x16x32_bf16 v[64:67], a[104:107], v[88:91], v[64:67]\n\t"
  "ds_read_b64_tr_b16 v[82:83], v144 offset:2048\n\t"
  "v_fma_f32 v167, v111, v129, v123\n\t"
  "s_waitcnt lgkmcnt(6)\n\t"
  "v_mfma_f32_16x16x32_bf16 v[68:71], a[96:99], v[92:95], v[68:71]\n\t"
  "ds_read_b64_tr_b16 v[84:85], v145 offset:0\n\t"
  "v_fma_f32 v36, v36, v128, v164\n\t"
  "v_mfma_f32_16x16x32_bf16 v[72:75], a[100:103], v[92:95], v[72:75]\n\t"
  "ds_read_b64_tr_b16 v[86:87], v145 offset:2048\n\t"
  "v_fma_f32 v37, v37, v128, v165\n\t"
  "v_mfma_f32_16x16x32_bf16 v[76:79], a[104:107], v[92:95], v[76:79]\n\t"
  "v_fma_f32 v38, v38, v128, v166\n\t"
  "v_fma_f32 v39, v39, v128, v167\n\t"
  "s_waitcnt lgkmcnt(6)\n\t"
  "v_mfma_f32_16x16x32_bf16 v[56:59], a[108:111], v[96:99], v[56:59]\n\t"
  "ds_read_b64_tr_b16 v[88:89], v144 offset:4096\n\t"
  "v_max_f32 v36, 0, v36\n\t"
  "v_mfma_f32_16x16x32_bf16 v[60:63], a[112:115], v[96:99], v[60:63]\n\t"
  "ds_read_b64_tr_b16 v[90:91], v144 offset:6144\n\t"
  "v_max_f32 v37, 0, v37\n\t"
  "v_mfma_f32_16x16x32_bf16 v[64:67], a[116:119], v[96:99], v[64:67]\n\t"
  "ds_read_b64_tr_b16 v[92:93], v145 offset:4096\n\t"
  "v_mul_f32 v36, v36, v38\n\t"
  "s_waitcnt lgkmcnt(7)\n\t"
  "v_mfma_f32_16x16x32_bf16 v[68:71], a[108:111], v[100:103], v[68:71]\n\t"
  "ds_read_b64_tr_b16 v[94:95], v145 offset:6144\n\t"
  "v_mul_f32 v37, v37, v39\n\t"
  "v_cvt_pk_bf16_f32 v149, v36, v37\n\t"
  "v_mfma_f32_16x16x32_bf16 v[72:75], a[112:115], v[100:103], v[72:75]\n\t"
  "v_cvt_pk_bf16_f32 v153, v36, v37\n\t"
  "v_mfma_f32_16x16x32_bf16 v[76:79], a[116:119], v[100:103], v[76:79]\n\t"
  "v_fma_f32 v164, v112, v129, v124\n\t"
  "s_waitcnt lgkmcnt(6)\n\t"
  "v_mfma_f32_16x16x32_bf16 v[56:59], a[120:123], v[80:83], v[56:59]\n\t"
  "ds_read_b64_tr_b16 v[96:97], v144 offset:8192\n\t"
  "v_fma_f32 v165, v113, v129, v125\n\t"
  "v_mfma_f32_16x16x32_bf16 v[60:63], a[124:127], v[80:83], v[60:63]\n\t"
  "ds_read_b64_tr_b16 v[98:99], v144 offset:10240\n\t"
  "v_fma_f32 v166, v114, v129, v126\n\t"
  "v_fma_f32 v167, v115, v129, v127\n\t"
  "v_mfma_f32_16x16x32_bf16 v[64:67], a[128:131], v[80:83], v[64:67]\n\t"
  "ds_read_b64_tr_b16 v[100:101], v145 offset:8192\n\t"
  "v_fma_f32 v40, v40, v128, v164\n\t"
  "s_waitcnt lgkmcnt(7)\n\t"
  "v_mfma_f32_16x16x32_bf16 v[68:71], a[120:123], v[84:87], v[68:71]\n\t"
  "ds_read_b64_tr_b16 v[102:103], v145 offset:10240\n\t"
  "v_fma_f32 v41, v41, v128, v165\n\t"
  "v_mfma_f32_16x16x32_bf16 v[72:75], a[124:127], v[84:87], v[72:75]\n\t"
  "v_fma_f32 v42, v42, v128, v166\n\t"
  "v_mfma_f32_16x16x32_bf16 v[76:79], a[128:131], v[84:87], v[76:79]\n\t"
  "v_fma_f32 v43, v43, v128, v167\n\t"
  "v_max_f32 v40, 0, v40\n\t"
  "s_waitcnt lgkmcnt(6)\n\t"
  "v_mfma_f32_16x16x32_bf16 v[56:59], a[132:135], v[88:91], v[56:59]\n\t"
  "ds_read_b64_tr_b16 v[80:81], v144 offset:12288\n\t"
  "v_max_f32 v41, 0, v41\n\t"
  "v_mfma_f32_16x16x32_bf16 v[60:63], a[136:139], v[88:91], v[60:63]\n\t"
  "ds_read_b64_tr_b16 v[82:83], v144 offset:14336\n\t"
  "v_mul_f32 v40, v40, v42\n\t"
  "v_mfma_f32_16x16x32_bf16 v[64:67], a[140:143], v[88:91], v[64:67]\n\t"
  "ds_read_b64_tr_b16 v[84:85], v145 offset:12288\n\t"
  "v_mul_f32 v41, v41, v43\n\t"
  "s_waitcnt lgkmcnt(7)\n\t"
  "v_mfma_f32_16x16x32_bf16 v[68:71], a[132:135], v[92:95], v[68:71]\n\t"
  "ds_read_b64_tr_b16 v[86:87], v145 offset:14336\n\t"
  "v_cvt_pk_bf16_f32 v150, v40, v41\n\t"
  "v_cvt_pk_bf16_f32 v154, v40, v41\n\t"
  "v_mfma_f32_16x16x32_bf16 v[72:75], a[136:139], v[92:95], v[72:75]\n\t"
  "v_permlane16_swap_b32 v152, v153\n\t"
  "v_mfma_f32_16x16x32_bf16 v[76:79], a[140:143], v[92:95], v[76:79]\n\t"
  "s_add_u32 s65, %[sH2st], 0\n\t"
  "s_waitcnt lgkmcnt(6)\n\t"
  "v_mfma_f32_16x16x32_bf16 v[56:59], a[144:147], v[96:99], v[56:59]\n\t"
  "ds_read_b64_tr_b16 v[88:89], v144 offset:16384\n\t"
  "v_permlane16_swap_b32 v154, v151\n\t"
  "v_mfma_f32_16x16x32_bf16 v[60:63], a[148:151], v[96:99], v[60:63]\n\t"
  "ds_read_b64_tr_b16 v[90:91], v144 offset:18432\n\t"
  "v_permlane32_swap_b32 v153, v151\n\t"
  "v_mfma_f32_16x16x32_bf16 v[64:67], a[152:155], v[96:99], v[64:67]\n\t"
  "ds_read_b64_tr_b16 v[92:93], v145 offset:16384\n\t"
  "buffer_store_dwordx4 v[148:151], %[vh2st], %[srd], s65 offen sc1\n\t"
  "s_waitcnt lgkmcnt(7)\n\t"
  "v_mfma_f32_16x16x32_bf16 v[68:71], a[144:147], v[100:103], v[68:71]\n\t"
  "ds_read_b64_tr_b16 v[94:95], v145 offset:18432\n\t"
  "v_fma_f32 v164, v104, v131, v116\n\t"
  "v_mfma_f32_16x16x32_bf16 v[72:75], a[148:151], v[100:103], v[72:75]\n\t"
  "v_fma_f32 v165, v105, v131, v117\n\t"
  "v_mfma_f32_16x16x32_bf16 v[76:79], a[152:155], v[100:103], v[76:79]\n\t"
  "v_fma_f32 v166, v106, v131, v118\n\t"
  "v_fma_f32 v167, v107, v131, v119\n\t"
  "s_waitcnt lgkmcnt(6)\n\t"
  "v_mfma_f32_16x16x32_bf16 v[56:59], a[156:159], v[80:83], v[56:59]\n\t"
  "v_add_u32 v146, s47, v234\n\t"
  "v_add_u32 v147, s47, v235\n\t"
  "v_fma_f32 v44, v44, v130, v164\n\t"
  "v_mfma_f32_16x16x32_bf16 v[60:63], a[160:163], v[80:83], v[60:63]\n\t"
  "ds_read_b64_tr_b16 v[96:97], v146 offset:0\n\t"
  "v_fma_f32 v45, v45, v130, v165\n\t"
  "v_mfma_f32_16x16x32_bf16 v[64:67], a[164:167], v[80:83], v[64:67]\n\t"
  "ds_read_b64_tr_b16 v[98:99], v146 offset:2048\n\t"
  "v_fma_f32 v46, v46, v130, v166\n\t"
  "s_waitcnt lgkmcnt(6)\n\t"
  "v_mfma_f32_16x16x32_bf16 v[68:71], a[156:159], v[84:87], v[68:71]\n\t"
  "ds_read_b64_tr_b16 v[100:101], v147 offset:0\n\t"
  "v_fma_f32 v47, v47, v130, v167\n\t"
  "v_max_f32 v44, 0, v44\n\t"
  "v_mfma_f32_16x16x32_bf16 v[72:75], a[160:163], v[84:87], v[72:75]\n\t"
  "ds_read_b64_tr_b16 v[102:103], v147 offset:2048\n\t"
  "v_max_f32 v45, 0, v45\n\t"
  "v_mfma_f32_16x16x32_bf16 v[76:79], a[164:167], v[84:87], v[76:79]\n\t"
  "v_mul_f32 v44, v44, v46\n\t"
  "s_waitcnt lgkmcnt(6)\n\t"
  "v_mfma_f32_16x16x32_bf16 v[56:59], a[168:171], v[88:91], v[56:59]\n\t"
  "ds_read_b64_tr_b16 v[80:81], v146 offset:4096\n\t"
  "v_mul_f32 v45, v45, v47\n\t"
  "v_mfma_f32_16x16x32_bf16 v[60:63], a[172:175], v[88:91], v[60:63]\n\t"
  "ds_read_b64_tr_b16 v[82:83], v146 offset:6144\n\t"
  "v_cvt_pk_bf16_f32 v156, v44, v45\n\t"
  "v_cvt_pk_bf16_f32 v160, v44, v45\n\t"
  "v_mfma_f32_16x16x32_bf16 v[64:67], a[176:179], v[88:91], v[64:67]\n\t"
  "ds_read_b64_tr_b16 v[84:85], v147 offset:4096\n\t"
  "v_fma_f32 v164, v108, v131, v120\n\t"
  "s_waitcnt lgkmcnt(7)\n\t"
  "v_mfma_f32_16x16x32_bf16 v[68:71], a[168:171], v[92:95], v[68:71]\n\t"
  "ds_read_b64_tr_b16 v[86:87], v147 offset:6144\n\t"
  "v_fma_f32 v165, v109, v131, v121\n\t"
  "v_mfma_f32_16x16x32_bf16 v[72:75], a[172:175], v[92:95], v[72:75]\n\t"
  "v_fma_f32 v166, v110, v131, v122\n\t"
  "v_mfma_f32_16x16x32_bf16 v[76:79], a[176:179], v[92:95], v[76:79]\n\t"
  "v_fma_f32 v167, v111, v131, v123\n\t"
  "v_fma_f32 v48, v48, v130, v164\n\t"
  "s_waitcnt lgkmcnt(6)\n\t"
  "v_mfma_f32_16x16x32_bf16 v[56:59], a[180:183], v[96:99], v[56:59]\n\t"
  "ds_read_b64_tr_b16 v[88:89], v146 offset:8192\n\t"
  "v_fma_f32 v49, v49, v130, v165\n\t"
  "v_mfma_f32_16x16x32_bf16 v[60:63], a[184:187], v[96:99], v[60:63]\n\t"
  "ds_read_b64_tr_b16 v[90:91], v146 offset:10240\n\t"
  "v_fma_f32 v50, v50, v130, v166\n\t"
  "v_mfma_f32_16x16x32_bf16 v[64:67], a[188:191], v[96:99], v[64:67]\n\t"
  "ds_read_b64_tr_b16 v[92:93], v147 offset:8192\n\t"
  "v_fma_f32 v51, v51, v130, v167\n\t"
  "s_waitcnt lgkmcnt(7)\n\t"
  "v_mfma_f32_16x16x32_bf16 v[68:71], a[180:183], v[100:103], v[68:71]\n\t"
  "ds_read_b64_tr_b16 v[94:95], v147 offset:10240\n\t"
  "v_max_f32 v48, 0, v48\n\t"
  "v_max_f32 v49, 0, v49\n\t"
  "v_mfma_f32_16x16x32_bf16 v[72:75], a[184:187], v[100:103], v[72:75]\n\t"
  "v_mul_f32 v48, v48, v50\n\t"
  "v_mfma_f32_16x16x32_bf16 v[76:79], a[188:191], v[100:103], v[76:79]\n\t"
  "v_mul_f32 v49, v49, v51\n\t"
  "s_waitcnt lgkmcnt(6)\n\t"
  "v_mfma_f32_16x16x32_bf16 v[56:59], a[192:195], v[80:83], v[56:59]\n\t"
  "ds_read_b64_tr_b16 v[96:97], v146 offset:12288\n\t"
  "v_cvt_pk_bf16_f32 v157, v48, v49\n\t"
  "v_mfma_f32_16x16x32_bf16 v[60:63], a[196:199], v[80:83], v[60:63]\n\t"
  "ds_read_b64_tr_b16 v[98:99], v146 offset:14336\n\t"
  "v_cvt_pk_bf16_f32 v161, v48, v49\n\t"
  "v_fma_f32 v164, v112, v131, v124\n\t"
  "v_mfma_f32_16x16x32_bf16 v[64:67], a[200:203], v[80:83], v[64:67]\n\t"
  "ds_read_b64_tr_b16 v[100:101], v147 offset:12288\n\t"
  "v_fma_f32 v165, v113, v131, v125\n\t"
  "s_waitcnt lgkmcnt(7)\n\t"
  "v_mfma_f32_16x16x32_bf16 v[68:71], a[192:195], v[84:87], v[68:71]\n\t"
  "ds_read_b64_tr_b16 v[102:103], v147 offset:14336\n\t"
  "v_fma_f32 v166, v114, v131, v126\n\t"
  "v_mfma_f32_16x16x32_bf16 v[72:75], a[196:199], v[84:87], v[72:75]\n\t"
  "v_fma_f32 v167, v115, v131, v127\n\t"
  "v_mfma_f32_16x16x32_bf16 v[76:79], a[200:203], v[84:87], v[76:79]\n\t"
  "v_fma_f32 v52, v52, v130, v164\n\t"
  "v_fma_f32 v53, v53, v130, v165\n\t"
  "s_waitcnt lgkmcnt(6)\n\t"
  "v_mfma_f32_16x16x32_bf16 v[56:59], a[204:207], v[88:91], v[56:59]\n\t"
  "ds_read_b64_tr_b16 v[80:81], v146 offset:16384\n\t"
  "v_fma_f32 v54, v54, v130, v166\n\t"
  "v_mfma_f32_16x16x32_bf16 v[60:63], a[208:211], v[88:91], v[60:63]\n\t"
  "ds_read_b64_tr_b16 v[82:83], v146 offset:18432\n\t"
  "v_fma_f32 v55, v55, v130, v167\n\t"
  "v_mfma_f32_16x16x32_bf16 v[64:67], a[212:215], v[88:91], v[64:67]\n\t"
  "ds_read_b64_tr_b16 v[84:85], v147 offset:16384\n\t"
  "v_max_f32 v52, 0, v52\n\t"
  "s_waitcnt lgkmcnt(7)\n\t"
  "v_mfma_f32_16x16x32_bf16 v[68:71], a[204:207], v[92:95], v[68:71]\n\t"
  "ds_read_b64_tr_b16 v[86:87], v147 offset:18432\n\t"
  "v_max_f32 v53, 0, v53\n\t"
  "v_mul_f32 v52, v52, v54\n\t"
  "v_mfma_f32_16x16x32_bf16 v[72:75], a[208:211], v[92:95], v[72:75]\n\t"
  "v_mul_f32 v53, v53, v55\n\t"
  "v_mfma_f32_16x16x32_bf16 v[76:79], a[212:215], v[92:95], v[76:79]\n\t"
  "v_cvt_pk_bf16_f32 v158, v52, v53\n\t"
  "s_waitcnt lgkmcnt(6)\n\t"
  "v_mfma_f32_16x16x32_bf16 v[56:59], a[216:219], v[96:99], v[56:59]\n\t"
  "v_add_u32 v144, s48, v232\n\t"
  "v_add_u32 v145, s48, v233\n\t"
  "v_cvt_pk_bf16_f32 v162, v52, v53\n\t"
  "v_mfma_f32_16x16x32_bf16 v[60:63], a[220:223], v[96:99], v[60:63]\n\t"
  "ds_read_b64_tr_b16 v[88:89], v144 offset:0\n\t"
  "v_permlane16_swap_b32 v160, v161\n\t"
  "s_add_u32 s65, %[sH2st], 49152\n\t"
  "v_mfma_f32_16x16x32_bf16 v[64:67], a[224:227], v[96:99], v[64:67]\n\t"
  "ds_read_b64_tr_b16 v[90:91], v144 offset:2048\n\t"
  "v_permlane16_swap_b32 v162, v159\n\t"
  "s_waitcnt lgkmcnt(6)\n\t"
  "v_mfma_f32_16x16x32_bf16 v[68:71], a[216:219], v[100:103], v[68:71]\n\t"
  "ds_read_b64_tr_b16 v[92:93], v145 offset:0\n\t"
  "v_mfma_f32_16x16x32_bf16 v[72:75], a[220:223], v[100:103], v[72:75]\n\t"
  "ds_read_b64_tr_b16 v[94:95], v145 offset:2048\n\t"
  "v_permlane32_swap_b32 v161, v159\n\t"
  "v_mfma_f32_16x16x32_bf16 v[76:79], a[224:227], v[100:103], v[76:79]\n\t"
  "buffer_store_dwordx4 v[156:159], %[vh2st], %[srd], s65 offen sc1\n\t"
  "s_waitcnt lgkmcnt(6)\n\t"
  "v_mfma_f32_16x16x32_bf16 v[56:59], a[228:231], v[80:83], v[56:59]\n\t"
  "ds_read_b64_tr_b16 v[96:97], v144 offset:4096\n\t"
  "v_mfma_f32_16x16x32_bf16 v[60:63], a[232:235], v[80:83], v[60:63]\n\t"
  "ds_read_b64_tr_b16 v[98:99], v144 offset:6144\n\t"
  "v_mfma_f32_16x16x32_bf16 v[64:67], a[236:239], v[80:83], v[64:67]\n\t"
  "ds_read_b64_tr_b16 v[100:101], v145 offset:4096\n\t"
  "s_waitcnt lgkmcnt(7)\n\t"
  "v_mfma_f32_16x16x32_bf16 v[68:71], a[228:231], v[84:87], v[68:71]\n\t"
  "ds_read_b64_tr_b16 v[102:103], v145 offset:6144\n\t"
  "v_mfma_f32_16x16x32_bf16 v[72:75], a[232:235], v[84:87], v[72:75]\n\t"
  "v_mfma_f32_16x16x32_bf16 v[76:79], a[236:239], v[84:87], v[76:79]\n\t"
  "s_waitcnt lgkmcnt(6)\n\t"
  "v_mfma_f32_16x16x32_bf16 v[32:35], a[0:3], v[88:91], 0\n\t"
  "ds_read_b64_tr_b16 v[80:81], v144 offset:8192\n\t"
  "v_fma_f32 v164, v104, v133, v116\n\t"
  "v_fma_f32 v165, v105, v133, v117\n\t"
  "v_mfma_f32_16x16x32_bf16 v[36:39], a[4:7], v[88:91], 0\n\t"
  "ds_read_b64_tr_b16 v[82:83], v144 offset:10240\n\t"
  "v_fma_f32 v166, v106, v133, v118\n\t"
  "v_mfma_f32_16x16x32_bf16 v[40:43], a[8:11], v[88:91], 0\n\t"
  "ds_read_b64_tr_b16 v[84:85], v145 offset:8192\n\t"
  "v_fma_f32 v167, v107, v133, v119\n\t"
  "s_waitcnt lgkmcnt(7)\n\t"
  "v_mfma_f32_16x16x32_bf16 v[44:47], a[0:3], v[92:95], 0\n\t"
  "ds_read_b64_tr_b16 v[86:87], v145 offset:10240\n\t"
  "v_fma_f32 v56, v56, v132, v164\n\t"
  "v_mfma_f32_16x16x32_bf16 v[48:51], a[4:7], v[92:95], 0\n\t"
  "v_fma_f32 v57, v57, v132, v165\n\t"
  "v_fma_f32 v58, v58, v132, v166\n\t"
  "v_mfma_f32_16x16x32_bf16 v[52:55], a[8:11], v[92:95], 0\n\t"
  "v_fma_f32 v59, v59, v132, v167\n\t"
  "s_waitcnt lgkmcnt(6)\n\t"
  "v_mfma_f32_16x16x32_bf16 v[32:35], a[12:15], v[96:99], v[32:35]\n\t"
  "ds_read_b64_tr_b16 v[88:89], v144 offset:12288\n\t"
  "v_max_f32 v56, 0, v56\n\t"
  "v_mfma_f32_16x16x32_bf16 v[36:39], a[16:19], v[96:99], v[36:39]\n\t"
  "ds_read_b64_tr_b16 v[90:91], v144 offset:14336\n\t"
  "v_max_f32 v57, 0, v57\n\t"
  "v_mfma_f32_16x16x32_bf16 v[40:43], a[20:23], v[96:99], v[40:43]\n\t"
  "ds_read_b64_tr_b16 v[92:93], v145 offset:12288\n\t"
  "v_mul_f32 v56, v56, v58\n\t"
  "v_mul_f32 v57, v57, v59\n\t"
  "s_waitcnt lgkmcnt(7)\n\t"
  "v_mfma_f32_16x16x32_bf16 v[44:47], a[12:15], v[100:103], v[44:47]\n\t"
  "ds_read_b64_tr_b16 v[94:95], v145 offset:14336\n\t"
  "v_cvt_pk_bf16_f32 v148, v56, v57\n\t"
  "v_mfma_f32_16x16x32_bf16 v[48:51], a[16:19], v[100:103], v[48:51]\n\t"
  "v_cvt_pk_bf16_f32 v152, v56, v57\n\t"
  "v_mfma_f32_16x16x32_bf16 v[52:55], a[20:23], v[100:103], v[52:55]\n\t"
  "v_fma_f32 v164, v108, v133, v120\n\t"
  "s_waitcnt lgkmcnt(6)\n\t"
  "v_mfma_f32_16x16x32_bf16 v[32:35], a[24:27], v[80:83], v[32:35]\n\t"
  "ds_read_b64_tr_b16 v[96:97], v144 offset:16384\n\t"
  "v_fma_f32 v165, v109, v133, v121\n\t"
  "v_mfma_f32_16x16x32_bf16 v[36:39], a[28:31], v[80:83], v[36:39]\n\t"
  "ds_read_b64_tr_b16 v[98:99], v144 offset:18432\n\t"
  "v_fma_f32 v166, v110, v133, v122\n\t"
  "v_fma_f32 v167, v111, v133, v123\n\t"
  "v_mfma_f32_16x16x32_bf16 v[40:43], a[32:35], v[80:83], v[40:43]\n\t"
  "ds_read_b64_tr_b16 v[100:101], v145 offset:16384\n\t"
  "v_fma_f32 v60, v60, v132, v164\n\t"
  "s_waitcnt lgkmcnt(7)\n\t"
  "v_mfma_f32_16x16x32_bf16 v[44:47], a[24:27], v[84:87], v[44:47]\n\t"
  "ds_read_b64_tr_b16 v[102:103], v145 offset:18432\n\t"
  "v_fma_f32 v61, v61, v132, v165\n\t"
  "v_mfma_f32_16x16x32_bf16 v[48:51], a[28:31], v[84:87], v[48:51]\n\t"
  "v_fma_f32 v62, v62, v132, v166\n\t"
  "v_mfma_f32_16x16x32_bf16 v[52:55], a[32:35], v[84:87], v[52:55]\n\t"
  "v_fma_f32 v63, v63, v132, v167\n\t"
  "v_max_f32 v60, 0, v60\n\t"
  "s_waitcnt lgkmcnt(6)\n\t"
  "v_mfma_f32_16x16x32_bf16 v[32:35], a[36:39], v[88:91], v[32:35]\n\t"
  "v_add_u32 v146, s49, v232\n\t"
  "v_add_u32 v147, s49, v233\n\t"
  "v_max_f32 v61, 0, v61\n\t"
  "v_mfma_f32_16x16x32_bf16 v[36:39], a[40:43], v[88:91], v[36:39]\n\t"
  "ds_read_b64_tr_b16 v[80:81], v146 offset:0\n\t"
  "v_mul_f32 v60, v60, v62\n\t"
  "v_mfma_f32_16x16x32_bf16 v[40:43], a[44:47], v[88:91], v[40:43]\n\t"
  "ds_read_b64_tr_b16 v[82:83], v146 offset:2048\n\t"
  "v_mul_f32 v61, v61, v63\n\t"
  "s_waitcnt lgkmcnt(6)\n\t"
  "v_mfma_f32_16x16x32_bf16 v[44:47], a[36:39], v[92:95], v[44:47]\n\t"
  "ds_read_b64_tr_b16 v[84:85], v147 offset:0\n\t"
  "v_cvt_pk_bf16_f32 v149, v60, v61\n\t"
  "v_cvt_pk_bf16_f32 v153, v60, v61\n\t"
  "v_mfma_f32_16x16x32_bf16 v[48:51], a[40:43], v[92:95], v[48:51]\n\t"
  "ds_read_b64_tr_b16 v[86:87], v147 offset:2048\n\t"
  "v_fma_f32 v164, v112, v133, v124\n\t"
  "v_mfma_f32_16x16x32_bf16 v[52:55], a[44:47], v[92:95], v[52:55]\n\t"
  "v_fma_f32 v165, v113, v133, v125\n\t"
  "s_waitcnt lgkmcnt(6)\n\t"
  "v_mfma_f32_16x16x32_bf16 v[32:35], a[48:51], v[96:99], v[32:35]\n\t"
  "ds_read_b64_tr_b16 v[88:89], v146 offset:4096\n\t"
  "v_fma_f32 v166, v114, v133, v126\n\t"
  "v_mfma_f32_16x16x32_bf16 v[36:39], a[52:55], v[96:99], v[36:39]\n\t"
  "ds_read_b64_tr_b16 v[90:91], v146 offset:6144\n\t"
  "v_fma_f32 v167, v115, v133, v127\n\t"
  "v_mfma_f32_16x16x32_bf16 v[40:43], a[56:59], v[96:99], v[40:43]\n\t"
  "ds_read_b64_tr_b16 v[92:93], v147 offset:4096\n\t"
  "v_fma_f32 v64, v64, v132, v164\n\t"
  "v_fma_f32 v65, v65, v132, v165\n\t"
  "s_waitcnt lgkmcnt(7)\n\t"
  "v_mfma_f32_16x16x32_bf16 v[44:47], a[48:51], v[100:103], v[44:47]\n\t"
  "ds_read_b64_tr_b16 v[94:95], v147 offset:6144\n\t"
  "v_fma_f32 v66, v66, v132, v166\n\t"
  "v_mfma_f32_16x16x32_bf16 v[48:51], a[52:55], v[100:103], v[48:51]\n\t"
  "v_fma_f32 v67, v67, v132, v167\n\t"
  "v_mfma_f32_16x16x32_bf16 v[52:55], a[56:59], v[100:103], v[52:55]\n\t"
  "v_max_f32 v64, 0, v64\n\t"
  "s_waitcnt lgkmcnt(6)\n\t"
  "v_mfma_f32_16x16x32_bf16 v[32:35], a[60:63], v[80:83], v[32:35]\n\t"
  "ds_read_b64_tr_b16 v[96:97], v146 offset:8192\n\t"
  "v_max_f32 v65, 0, v65\n\t"
  "v_mul_f32 v64, v64, v66\n\t"
  "v_mfma_f32_16x16x32_bf16 v[36:39], a[64:67], v[80:83], v[36:39]\n\t"
  "ds_read_b64_tr_b16 v[98:99], v146 offset:10240\n\t"
  "v_mul_f32 v65, v65, v67\n\t"
  "v_mfma_f32_16x16x32_bf16 v[40:43], a[68:71], v[80:83], v[40:43]\n\t"
  "ds_read_b64_tr_b16 v[100:101], v147 offset:8192\n\t"
  "v_cvt_pk_bf16_f32 v150, v64, v65\n\t"
  "s_waitcnt lgkmcnt(7)\n\t"
  "v_mfma_f32_16x16x32_bf16 v[44:47], a[60:63], v[84:87], v[44:47]\n\t"
  "ds_read_b64_tr_b16 v[102:103], v147 offset:10240\n\t"
  "v_cvt_pk_bf16_f32 v154, v64, v65\n\t"
  "v_mfma_f32_16x16x32_bf16 v[48:51], a[64:67], v[84:87], v[48:51]\n\t"
  "v_permlane16_swap_b32 v152, v153\n\t"
  "s_add_u32 s65, %[sH2st], 98304\n\t"
  "v_mfma_f32_16x16x32_bf16 v[52:55], a[68:71], v[84:87], v[52:55]\n\t"
  "v_permlane16_swap_b32 v154, v151\n\t"
  "s_waitcnt lgkmcnt(6)\n\t"
  "v_mfma_f32_16x16x32_bf16 v[32:35], a[72:75], v[88:91], v[32:35]\n\t"
  "ds_read_b64_tr_b16 v[80:81], v146 offset:12288\n\t"
  "v_mfma_f32_16x16x32_bf16 v[36:39], a[76:79], v[88:91], v[36:39]\n\t"
  "ds_read_b64_tr_b16 v[82:83], v146 offset:14336\n\t"
  "v_permlane32_swap_b32 v153, v151\n\t"
  "v_mfma_f32_16x16x32_bf16 v[40:43], a[80:83], v[88:91], v[40:43]\n\t"
  "ds_read_b64_tr_b16 v[84:85], v147 offset:12288\n\t"
  "buffer_store_dwordx4 v[148:151], %[vh2st], %[srd], s65 offen sc1\n\t"
  "s_waitcnt lgkmcnt(7)\n\t"
  "v_mfma_f32_16x16x32_bf16 v[44:47], a[72:75], v[92:95], v[44:47]\n\t"
  "ds_read_b64_tr_b16 v[86:87], v147 offset:14336\n\t"
  "v_fma_f32 v164, v104, v135, v116\n\t"
  "v_fma_f32 v165, v105, v135, v117\n\t"
  "v_mfma_f32_16x16x32_bf16 v[48:51], a[76:79], v[92:95], v[48:51]\n\t"
  "v_fma_f32 v166, v106, v135, v118\n\t"
  "v_mfma_f32_16x16x32_bf16 v[52:55], a[80:83], v[92:95], v[52:55]\n\t"
  "v_fma_f32 v167, v107, v135, v119\n\t"
  "s_waitcnt lgkmcnt(6)\n\t"
  "v_mfma_f32_16x16x32_bf16 v[32:35], a[84:87], v[96:99], v[32:35]\n\t"
  "ds_read_b64_tr_b16 v[88:89], v146 offset:16384\n\t"
  "v_fma_f32 v68, v68, v134, v164\n\t"
  "v_mfma_f32_16x16x32_bf16 v[36:39], a[88:91], v[96:99], v[36:39]\n\t"
  "ds_read_b64_tr_b16 v[90:91], v146 offset:18432\n\t"
  "v_fma_f32 v69, v69, v134, v165\n\t"
  "v_fma_f32 v70, v70, v134, v166\n\t"
  "v_mfma_f32_16x16x32_bf16 v[40:43], a[92:95], v[96:99], v[40:43]\n\t"
  "ds_read_b64_tr_b16 v[92:93], v147 offset:16384\n\t"
  "v_fma_f32 v71, v71, v134, v167\n\t"
  "s_waitcnt lgkmcnt(7)\n\t"
  "v_mfma_f32_16x16x32_bf16 v[44:47], a[84:87], v[100:103], v[44:47]\n\t"
  "ds_read_b64_tr_b16 v[94:95], v147 offset:18432\n\t"
  "v_max_f32 v68, 0, v68\n\t"
  "v_mfma_f32_16x16x32_bf16 v[48:51], a[88:91], v[100:103], v[48:51]\n\t"
  "v_max_f32 v69, 0, v69\n\t"
  "v_mfma_f32_16x16x32_bf16 v[52:55], a[92:95], v[100:103], v[52:55]\n\t"
  "v_mul_f32 v68, v68, v70\n\t"
  "v_mul_f32 v69, v69, v71\n\t"
  "s_waitcnt lgkmcnt(6)\n\t"
  "v_mfma_f32_16x16x32_bf16 v[32:35], a[96:99], v[80:83], v[32:35]\n\t"
  "v_add_u32 v144, s50, v232\n\t"
  "v_add_u32 v145, s50, v233\n\t"
  "v_cvt_pk_bf16_f32 v156, v68, v69\n\t"
  "s_waitcnt vmcnt(1)\n\t"
  "v_mov_b32 v169, 1\n\t"
  "buffer_store_dword v169, %[vflw], %[srd], %[sFLw] offen offset:0 sc1\n\t"
  "v_mfma_f32_16x16x32_bf16 v[36:39], a[100:103], v[80:83], v[36:39]\n\t"
  "ds_read_b64_tr_b16 v[96:97], v144 offset:0\n\t"
  "v_cvt_pk_bf16_f32 v160, v68, v69\n\t"
  "v_mfma_f32_16x16x32_bf16 v[40:43], a[104:107], v[80:83], v[40:43]\n\t"
  "ds_read_b64_tr_b16 v[98:99], v144 offset:2048\n\t"
  "v_fma_f32 v164, v108, v135, v120\n\t"
  "s_waitcnt lgkmcnt(6)\n\t"
  "v_mfma_f32_16x16x32_bf16 v[44:47], a[96:99], v[84:87], v[44:47]\n\t"
  "ds_read_b64_tr_b16 v[100:101], v145 offset:0\n\t"
  "v_fma_f32 v165, v109, v135, v121\n\t"
  "v_mfma_f32_16x16x32_bf16 v[48:51], a[100:103], v[84:87], v[48:51]\n\t"
  "ds_read_b64_tr_b16 v[102:103], v145 offset:2048\n\t"
  "v_fma_f32 v166, v110, v135, v122\n\t"
  "v_fma_f32 v167, v111, v135, v123\n\t"
  "v_mfma_f32_16x16x32_bf16 v[52:55], a[104:107], v[84:87], v[52:55]\n\t"
  "v_fma_f32 v72, v72, v134, v164\n\t"
  "s_waitcnt lgkmcnt(6)\n\t"
  "v_mfma_f32_16x16x32_bf16 v[32:35], a[108:111], v[88:91], v[32:35]\n\t"
  "ds_read_b64_tr_b16 v[80:81], v144 offset:4096\n\t"
  "v_fma_f32 v73, v73, v134, v165\n\t"
  "v_mfma_f32_16x16x32_bf16 v[36:39], a[112:115], v[88:91], v[36:39]\n\t"
  "ds_read_b64_tr_b16 v[82:83], v144 offset:6144\n\t"
  "v_fma_f32 v74, v74, v134, v166\n\t"
  "v_mfma_f32_16x16x32_bf16 v[40:43], a[116:119], v[88:91], v[40:43]\n\t"
  "ds_read_b64_tr_b16 v[84:85], v145 offset:4096\n\t"
  "v_fma_f32 v75, v75, v134, v167\n\t"
  "v_max_f32 v72, 0, v72\n\t"
  "s_waitcnt lgkmcnt(7)\n\t"
  "v_mfma_f32_16x16x32_bf16 v[44:47], a[108:111], v[92:95], v[44:47]\n\t"
  "ds_read_b64_tr_b16 v[86:87], v145 offset:6144\n\t"
  "v_max_f32 v73, 0, v73\n\t"
  "v_mfma_f32_16x16x32_bf16 v[48:51], a[112:115], v[92:95], v[48:51]\n\t"
  "v_mul_f32 v72, v72, v74\n\t"
  "v_mfma_f32_16x16x32_bf16 v[52:55], a[116:119], v[92:95], v[52:55]\n\t"
  "v_mul_f32 v73, v73, v75\n\t"
  "s_waitcnt lgkmcnt(6)\n\t"
  "v_mfma_f32_16x16x32_bf16 v[32:35], a[120:123], v[96:99], v[32:35]\n\t"
  "ds_read_b64_tr_b16 v[88:89], v144 offset:8192\n\t"
  "v_cvt_pk_bf16_f32 v157, v72, v73\n\t"
  "v_cvt_pk_bf16_f32 v161, v72, v73\n\t"
  "v_mfma_f32_16x16x32_bf16 v[36:39], a[124:127], v[96:99], v[36:39]\n\t"
  "ds_read_b64_tr_b16 v[90:91], v144 offset:10240\n\t"
  "v_fma_f32 v164, v112, v135, v124\n\t"
  "v_mfma_f32_16x16x32_bf16 v[40:43], a[128:131], v[96:99], v[40:43]\n\t"
  "ds_read_b64_tr_b16 v[92:93], v145 offset:8192\n\t"
  "v_fma_f32 v165, v113, v135, v125\n\t"
  "s_waitcnt lgkmcnt(7)\n\t"
  "v_mfma_f32_16x16x32_bf16 v[44:47], a[120:123], v[100:103], v[44:47]\n\t"
  "ds_read_b64_tr_b16 v[94:95], v145 offset:10240\n\t"
  "v_fma_f32 v166, v114, v135, v126\n\t"
  "v_mfma_f32_16x16x32_bf16 v[48:51], a[124:127], v[100:103], v[48:51]\n\t"
  "v_fma_f32 v167, v115, v135, v127\n\t"
  "v_mfma_f32_16x16x32_bf16 v[52:55], a[128:131], v[100:103], v[52:55]\n\t"
  "v_fma_f32 v76, v76, v134, v164\n\t"
  "v_fma_f32 v77, v77, v134, v165\n\t"
  "s_waitcnt lgkmcnt(6)\n\t"
  "v_mfma_f32_16x16x32_bf16 v[32:35], a[132:135], v[80:83], v[32:35]\n\t"
  "ds_read_b64_tr_b16 v[96:97], v144 offset:12288\n\t"
  "v_fma_f32 v78, v78, v134, v166\n\t"
  "v_mfma_f32_16x16x32_bf16 v[36:39], a[136:139], v[80:83], v[36:39]\n\t"
  "ds_read_b64_tr_b16 v[98:99], v144 offset:14336\n\t"
  "v_fma_f32 v79, v79, v134, v167\n\t"
  "v_mfma_f32_16x16x32_bf16 v[40:43], a[140:143], v[80:83], v[40:43]\n\t"
  "ds_read_b64_tr_b16 v[100:101], v145 offset:12288\n\t"
  "v_max_f32 v76, 0, v76\n\t"
  "s_waitcnt lgkmcnt(7)\n\t"
  "v_mfma_f32_16x16x32_bf16 v[44:47], a[132:135], v[84:87], v[44:47]\n\t"
  "ds_read_b64_tr_b16 v[102:103], v145 offset:14336\n\t"
  "v_max_f32 v77, 0, v77\n\t"
  "v_mul_f32 v76, v76, v78\n\t"
  "v_mfma_f32_16x16x32_bf16 v[48:51], a[136:139], v[84:87], v[48:51]\n\t"
  "v_mul_f32 v77, v77, v79\n\t"
  "v_mfma_f32_16x16x32_bf16 v[52:55], a[140:143], v[84:87], v[52:55]\n\t"
  "v_cvt_pk_bf16_f32 v158, v76, v77\n\t"
  "s_waitcnt lgkmcnt(6)\n\t"
  "v_mfma_f32_16x16x32_bf16 v[32:35], a[144:147], v[88:91], v[32:35]\n\t"
  "ds_read_b64_tr_b16 v[80:81], v144 offset:16384\n\t"
  "v_cvt_pk_bf16_f32 v162, v76, v77\n\t"
  "v_mfma_f32_16x16x32_bf16 v[36:39], a[148:151], v[88:91], v[36:39]\n\t"
  "ds_read_b64_tr_b16 v[82:83], v144 offset:18432\n\t"
  "v_permlane16_swap_b32 v160, v161\n\t"
  "s_add_u32 s65, %[sH2st], 147456\n\t"
  "v_mfma_f32_16x16x32_bf16 v[40:43], a[152:155], v[88:91], v[40:43]\n\t"
  "ds_read_b64_tr_b16 v[84:85], v145 offset:16384\n\t"
  "v_permlane16_swap_b32 v162, v159\n\t"
  "s_waitcnt lgkmcnt(7)\n\t"
  "v_mfma_f32_16x16x32_bf16 v[44:47], a[144:147], v[92:95], v[44:47]\n\t"
  "ds_read_b64_tr_b16 v[86:87], v145 offset:18432\n\t"
  "v_mfma_f32_16x16x32_bf16 v[48:51], a[148:151], v[92:95], v[48:51]\n\t"
  "v_permlane32_swap_b32 v161, v159\n\t"
  "v_mfma_f32_16x16x32_bf16 v[52:55], a[152:155], v[92:95], v[52:55]\n\t"
  "buffer_store_dwordx4 v[156:159], %[vh2st], %[srd], s65 offen sc1\n\t"
  "s_waitcnt lgkmcnt(6)\n\t"
  "v_mfma_f32_16x16x32_bf16 v[32:35], a[156:159], v[96:99], v[32:35]\n\t"
  "v_add_u32 v146, s51, v232\n\t"
  "v_add_u32 v147, s51, v233\n\t"
  "v_lshrrev_b32 v170, 1, %[vl16]\n\t"
  "v_and_b32 v170, 0x78, v170\n\t"
  "v_mfma_f32_16x16x32_bf16 v[36:39], a[160:163], v[96:99], v[36:39]\n\t"
  "ds_read_b64_tr_b16 v[88:89], v146 offset:0\n\t"
  "s_add_u32 s66, %[sST], 8192\n\t"
  "v_mfma_f32_16x16x32_bf16 v[40:43], a[164:167], v[96:99], v[40:43]\n\t"
  "ds_read_b64_tr_b16 v[90:91], v146 offset:2048\n\t"
  "buffer_load_dwordx2 v[136:137], v170, %[srd], s66 offen offset:0 sc1\n\t"
  "s_waitcnt lgkmcnt(6)\n\t"
  "v_mfma_f32_16x16x32_bf16 v[44:47], a[156:159], v[100:103], v[44:47]\n\t"
  "ds_read_b64_tr_b16 v[92:93], v147 offset:0\n\t"
  "buffer_load_dwordx2 v[138:139], v170, %[srd], s66 offen offset:128 sc1\n\t"
  "v_mfma_f32_16x16x32_bf16 v[48:51], a[160:163], v[100:103], v[48:51]\n\t"
  "ds_read_b64_tr_b16 v[94:95], v147 offset:2048\n\t"
  "buffer_load_dwordx2 v[140:141], v170, %[srd], s66 offen offset:256 sc1\n\t"
  "v_mfma_f32_16x16x32_bf16 v[52:55], a[164:167], v[100:103], v[52:55]\n\t"
  "buffer_load_dwordx2 v[142:143], v170, %[srd], s66 offen offset:384 sc1\n\t"
  "s_waitcnt lgkmcnt(6)\n\t"
  "v_mfma_f32_16x16x32_bf16 v[32:35], a[168:171], v[80:83], v[32:35]\n\t"
  "ds_read_b64_tr_b16 v[96:97], v146 offset:4096\n\t"
  "v_mfma_f32_16x16x32_bf16 v[36:39], a[172:175], v[80:83], v[36:39]\n\t"
  "ds_read_b64_tr_b16 v[98:99], v146 offset:6144\n\t"
  "v_mfma_f32_16x16x32_bf16 v[40:43], a[176:179], v[80:83], v[40:43]\n\t"
  "ds_read_b64_tr_b16 v[100:101], v147 offset:4096\n\t"
  "s_waitcnt lgkmcnt(7)\n\t"
  "v_mfma_f32_16x16x32_bf16 v[44:47], a[168:171], v[84:87], v[44:47]\n\t"
  "ds_read_b64_tr_b16 v[102:103], v147 offset:6144\n\t"
  "v_mfma_f32_16x16x32_bf16 v[48:51], a[172:175], v[84:87], v[48:51]\n\t"
  "v_mfma_f32_16x16x32_bf16 v[52:55], a[176:179], v[84:87], v[52:55]\n\t"
  "s_waitcnt lgkmcnt(6)\n\t"
  "v_mfma_f32_16x16x32_bf16 v[32:35], a[180:183], v[88:91], v[32:35]\n\t"
  "ds_read_b64_tr_b16 v[80:81], v146 offset:8192\n\t"
  "v_mfma_f32_16x16x32_bf16 v[36:39], a[184:187], v[88:91], v[36:39]\n\t"
  "ds_read_b64_tr_b16 v[82:83], v146 offset:10240\n\t"
  "v_mfma_f32_16x16x32_bf16 v[40:43], a[188:191], v[88:91], v[40:43]\n\t"
  "ds_read_b64_tr_b16 v[84:85], v147 offset:8192\n\t"
  "s_waitcnt lgkmcnt(7)\n\t"
  "v_mfma_f32_16x16x32_bf16 v[44:47], a[180:183], v[92:95], v[44:47]\n\t"
  "ds_read_b64_tr_b16 v[86:87], v147 offset:10240\n\t"
  "v_mfma_f32_16x16x32_bf16 v[48:51], a[184:187], v[92:95], v[48:51]\n\t"
  "v_mfma_f32_16x16x32_bf16 v[52:55], a[188:191], v[92:95], v[52:55]\n\t"
  "s_waitcnt lgkmcnt(6)\n\t"
  "v_mfma_f32_16x16x32_bf16 v[32:35], a[192:195], v[96:99], v[32:35]\n\t"
  "ds_read_b64_tr_b16 v[88:89], v146 offset:12288\n\t"
  "v_mfma_f32_16x16x32_bf16 v[36:39], a[196:199], v[96:99], v[36:39]\n\t"
  "ds_read_b64_tr_b16 v[90:91], v146 offset:14336\n\t"
  "v_mfma_f32_16x16x32_bf16 v[40:43], a[200:203], v[96:99], v[40:43]\n\t"
  "ds_read_b64_tr_b16 v[92:93], v147 offset:12288\n\t"
  "s_waitcnt lgkmcnt(7)\n\t"
  "v_mfma_f32_16x16x32_bf16 v[44:47], a[192:195], v[100:103], v[44:47]\n\t"
  "ds_read_b64_tr_b16 v[94:95], v147 offset:14336\n\t"
  "v_mfma_f32_16x16x32_bf16 v[48:51], a[196:199], v[100:103], v[48:51]\n\t"
  "v_mfma_f32_16x16x32_bf16 v[52:55], a[200:203], v[100:103], v[52:55]\n\t"
  "s_waitcnt lgkmcnt(6)\n\t"
  "v_mfma_f32_16x16x32_bf16 v[32:35], a[204:207], v[80:83], v[32:35]\n\t"
  "ds_read_b64_tr_b16 v[96:97], v146 offset:16384\n\t"
  "v_mfma_f32_16x16x32_bf16 v[36:39], a[208:211], v[80:83], v[36:39]\n\t"
  "ds_read_b64_tr_b16 v[98:99], v146 offset:18432\n\t"
  "v_mfma_f32_16x16x32_bf16 v[40:43], a[212:215], v[80:83], v[40:43]\n\t"
  "ds_read_b64_tr_b16 v[100:101], v147 offset:16384\n\t"
  "s_waitcnt lgkmcnt(7)\n\t"
  "v_mfma_f32_16x16x32_bf16 v[44:47], a[204:207], v[84:87], v[44:47]\n\t"
  "ds_read_b64_tr_b16 v[102:103], v147 offset:18432\n\t"
  "v_mfma_f32_16x16x32_bf16 v[48:51], a[208:211], v[84:87], v[48:51]\n\t"
  "v_mfma_f32_16x16x32_bf16 v[52:55], a[212:215], v[84:87], v[52:55]\n\t"
  "s_waitcnt lgkmcnt(6)\n\t"
  "v_mfma_f32_16x16x32_bf16 v[32:35], a[216:219], v[88:91], v[32:35]\n\t"
  "v_add_u32 v144, s48, v234\n\t"
  "v_add_u32 v145, s48, v235\n\t"
  "v_mfma_f32_16x16x32_bf16 v[36:39], a[220:223], v[88:91], v[36:39]\n\t"
  "ds_read_b64_tr_b16 v[80:81], v144 offset:0\n\t"
  "v_mfma_f32_16x16x32_bf16 v[40:43], a[224:227], v[88:91], v[40:43]\n\t"
  "ds_read_b64_tr_b16 v[82:83], v144 offset:2048\n\t"
  "s_waitcnt lgkmcnt(6)\n\t"
  "v_mfma_f32_16x16x32_bf16 v[44:47], a[216:219], v[92:95], v[44:47]\n\t"
  "ds_read_b64_tr_b16 v[84:85], v145 offset:0\n\t"
  "v_mfma_f32_16x16x32_bf16 v[48:51], a[220:223], v[92:95], v[48:51]\n\t"
  "ds_read_b64_tr_b16 v[86:87], v145 offset:2048\n\t"
  "v_mfma_f32_16x16x32_bf16 v[52:55], a[224:227], v[92:95], v[52:55]\n\t"
  "s_waitcnt lgkmcnt(6)\n\t"
  "v_mfma_f32_16x16x32_bf16 v[32:35], a[228:231], v[96:99], v[32:35]\n\t"
  "ds_read_b64_tr_b16 v[88:89], v144 offset:4096\n\t"
  "s_waitcnt vmcnt(2)\n\t"
  "v_min_u32 v171, v136, v138\n\t"
  "s_waitcnt vmcnt(0)\n\t"
  "v_min3_u32 v171, v171, v140, v142\n\t"
  "v_cmp_ne_u32 vcc, 0, v171\n\t"
  "s_nop 3\n\t"
  "s_cmp_eq_u64 vcc, -1\n\t"
  "s_cbranch_scc0 Lst1_slow%=\n\t"
  "Lst1_back%=:\n\t"
  "v_mfma_f32_16x16x32_bf16 v[36:39], a[232:235], v[96:99], v[36:39]\n\t"
  "ds_read_b64_tr_b16 v[90:91], v144 offset:6144\n\t"
  "v_lshrrev_b32 v170, 2, %[vl16]\n\t"
  "v_and_b32 v170, 0x3c, v170\n\t"
  "v_mfma_f32_16x16x32_bf16 v[40:43], a[236:239], v[96:99], v[40:43]\n\t"
  "ds_read_b64_tr_b16 v[92:93], v145 offset:4096\n\t"
  "s_lshl_b32 s66, %[w], 6\n\t"
  "s_waitcnt lgkmcnt(7)\n\t"
  "v_mfma_f32_16x16x32_bf16 v[44:47], a[228:231], v[100:103], v[44:47]\n\t"
  "ds_read_b64_tr_b16 v[94:95], v145 offset:6144\n\t"
  "s_add_u32 s66, s66, %[sFL]\n\t"
  "v_mfma_f32_16x16x32_bf16 v[48:51], a[232:235], v[100:103], v[48:51]\n\t"
  "buffer_load_dword v171, v170, %[srd], s66 offen offset:0 sc1\n\t"
  "v_mfma_f32_16x16x32_bf16 v[52:55], a[236:239], v[100:103], v[52:55]\n\t"
  "s_waitcnt lgkmcnt(6)\n\t"
  "v_mfma_f32_16x16x32_bf16 v[56:59], a[0:3], v[80:83], 0\n\t"
  "ds_read_b64_tr_b16 v[96:97], v144 offset:8192\n\t"
  "v_fma_f32 v164, v104, v137, v116\n\t"
  "v_fma_f32 v165, v105, v137, v117\n\t"
  "v_mfma_f32_16x16x32_bf16 v[60:63], a[4:7], v[80:83], 0\n\t"
  "ds_read_b64_tr_b16 v[98:99], v144 offset:10240\n\t"
  "v_fma_f32 v166, v106, v137, v118\n\t"
  "v_mfma_f32_16x16x32_bf16 v[64:67], a[8:11], v[80:83], 0\n\t"
  "ds_read_b64_tr_b16 v[100:101], v145 offset:8192\n\t"
  "v_fma_f32 v167, v107, v137, v119\n\t"
  "s_waitcnt lgkmcnt(7)\n\t"
  "v_mfma_f32_16x16x32_bf16 v[68:71], a[0:3], v[84:87], 0\n\t"
  "ds_read_b64_tr_b16 v[102:103], v145 offset:10240\n\t"
  "v_fma_f32 v32, v32, v136, v164\n\t"
  "v_fma_f32 v33, v33, v136, v165\n\t"
  "v_mfma_f32_16x16x32_bf16 v[72:75], a[4:7], v[84:87], 0\n\t"
  "v_fma_f32 v34, v34, v136, v166\n\t"
  "v_mfma_f32_16x16x32_bf16 v[76:79], a[8:11], v[84:87], 0\n\t"
  "v_fma_f32 v35, v35, v136, v167\n\t"
  "s_waitcnt lgkmcnt(6)\n\t"
  "v_mfma_f32_16x16x32_bf16 v[56:59], a[12:15], v[88:91], v[56:59]\n\t"
  "ds_read_b64_tr_b16 v[80:81], v144 offset:12288\n\t"
  "v_max_f32 v32, 0, v32\n\t"
  "v_max_f32 v33, 0, v33\n\t"
  "buffer_load_dwordx4 a[0:3], %[vl16], %[srd], s56 offen offset:0\n\t"
  "v_mfma_f32_16x16x32_bf16 v[60:63], a[16:19], v[88:91], v[60:63]\n\t"
  "ds_read_b64_tr_b16 v[82:83], v144 offset:14336\n\t"
  "v_mul_f32 v32, v32, v34\n\t"
  "v_mfma_f32_16x16x32_bf16 v[64:67], a[20:23], v[88:91], v[64:67]\n\t"
  "ds_read_b64_tr_b16 v[84:85], v145 offset:12288\n\t"
  "v_mul_f32 v33, v33, v35\n\t"
  "buffer_load_dwordx4 a[4:7], %[vl16], %[srd], s56 offen offset:1024\n\t"
  "s_waitcnt lgkmcnt(7)\n\t"
  "v_mfma_f32_16x16x32_bf16 v[68:71], a[12:15], v[92:95], v[68:71]\n\t"
  "ds_read_b64_tr_b16 v[86:87], v145 offset:14336\n\t"
  "v_cvt_pk_bf16_f32 v148, v32, v33\n\t"
  "v_cvt_pk_bf16_f32 v152, v32, v33\n\t"
  "v_mfma_f32_16x16x32_bf16 v[72:75], a[16:19], v[92:95], v[72:75]\n\t"
  "v_fma_f32 v164, v108, v137, v120\n\t"
  "buffer_load_dwordx4 a[8:11], %[vl16], %[srd], s56 offen offset:2048\n\t"
  "v_mfma_f32_16x16x32_bf16 v[76:79], a[20:23], v[92:95], v[76:79]\n\t"
  "v_fma_f32 v165, v109, v137, v121\n\t"
  "s_waitcnt lgkmcnt(6)\n\t"
  "v_mfma_f32_16x16x32_bf16 v[56:59], a[24:27], v[96:99], v[56:59]\n\t"
  "ds_read_b64_tr_b16 v[88:89], v144 offset:16384\n\t"
  "v_fma_f32 v166, v110, v137, v122\n\t"
  "v_fma_f32 v167, v111, v137, v123\n\t"
  "v_mov_b32 v169, 1\n\t"
  "buffer_store_dword v169, %[vflw], %[srd], %[sFLw] offen offset:256 sc1\n\t"
  "buffer_load_dwordx4 a[12:15], %[vl16], %[srd], s56 offen offset:3072\n\t"
  "v_mfma_f32_16x16x32_bf16 v[60:63], a[28:31], v[96:99], v[60:63]\n\t"
  "ds_read_b64_tr_b16 v[90:91], v144 offset:18432\n\t"
  "v_fma_f32 v36, v36, v136, v164\n\t"
  "s_add_u32 s56, s56, 4096\n\t"
  "v_mfma_f32_16x16x32_bf16 v[64:67], a[32:35], v[96:99], v[64:67]\n\t"
  "ds_read_b64_tr_b16 v[92:93], v145 offset:16384\n\t"
  "v_fma_f32 v37, v37, v136, v165\n\t"
  "s_waitcnt lgkmcnt(7)\n\t"
  "v_mfma_f32_16x16x32_bf16 v[68:71], a[24:27], v[100:103], v[68:71]\n\t"
  "ds_read_b64_tr_b16 v[94:95], v145 offset:18432\n\t"
  "v_fma_f32 v38, v38, v136, v166\n\t"
  "v_fma_f32 v39, v39, v136, v167\n\t"
  "buffer_load_dwordx4 a[16:19], %[vl16], %[srd], s56 offen offset:0\n\t"
  "v_mfma_f32_16x16x32_bf16 v[72:75], a[28:31], v[100:103], v[72:75]\n\t"
  "v_max_f32 v36, 0, v36\n\t"
  "buffer_load_dwordx4 a[20:23], %[vl16], %[srd], s56 offen offset:1024\n\t"
  "v_mfma_f32_16x16x32_bf16 v[76:79], a[32:35], v[100:103], v[76:79]\n\t"
  "v_max_f32 v37, 0, v37\n\t"
  "s_waitcnt lgkmcnt(6)\n\t"
  "v_mfma_f32_16x16x32_bf16 v[56:59], a[36:39], v[80:83], v[56:59]\n\t"
  "v_add_u32 v146, s49, v234\n\t"
  "v_add_u32 v147, s49, v235\n\t"
  "v_mul_f32 v36, v36, v38\n\t"
  "v_mul_f32 v37, v37, v39\n\t"
  "buffer_load_dwordx4 a[24:27], %[vl16], %[srd], s56 offen offset:2048\n\t"
  "v_mfma_f32_16x16x32_bf16 v[60:63], a[40:43], v[80:83], v[60:63]\n\t"
  "ds_read_b64_tr_b16 v[96:97], v146 offset:0\n\t"
  "v_cvt_pk_bf16_f32 v149, v36, v37\n\t"
  "buffer_load_dwordx4 a[28:31], %[vl16], %[srd], s56 offen offset:3072\n\t"
  "v_mfma_f32_16x16x32_bf16 v[64:67], a[44:47], v[80:83], v[64:67]\n\t"
  "ds_read_b64_tr_b16 v[98:99], v146 offset:2048\n\t"
  "v_cvt_pk_bf16_f32 v153, v36, v37\n\t"
  "s_waitcnt lgkmcnt(6)\n\t"
  "v_mfma_f32_16x16x32_bf16 v[68:71], a[36:39], v[84:87], v[68:71]\n\t"
  "ds_read_b64_tr_b16 v[100:101], v147 offset:0\n\t"
  "v_fma_f32 v164, v112, v137, v124\n\t"
  "v_fma_f32 v165, v113, v137, v125\n\t"
  "s_add_u32 s56, s56, 4096\n\t"
  "v_mfma_f32_16x16x32_bf16 v[72:75], a[40:43], v[84:87], v[72:75]\n\t"
  "ds_read_b64_tr_b16 v[102:103], v147 offset:2048\n\t"
  "v_fma_f32 v166, v114, v137, v126\n\t"
  "buffer_load_dwordx4 a[32:35], %[vl16], %[srd], s56 offen offset:0\n\t"
  "v_mfma_f32_16x16x32_bf16 v[76:79], a[44:47], v[84:87], v[76:79]\n\t"
  "v_fma_f32 v167, v115, v137, v127\n\t"
  "s_waitcnt lgkmcnt(6)\n\t"
  "v_mfma_f32_16x16x32_bf16 v[56:59], a[48:51], v[88:91], v[56:59]\n\t"
  "ds_read_b64_tr_b16 v[80:81], v146 offset:4096\n\t"
  "v_fma_f32 v40, v40, v136, v164\n\t"
  "v_fma_f32 v41, v41, v136, v165\n\t"
  "s_waitcnt vmcnt(10)\n\t"
  "v_cmp_ne_u32 vcc, 0, v171\n\t"
  "s_nop 3\n\t"
  "s_cmp_eq_u64 vcc, -1\n\t"
  "s_cbranch_scc0 Lfl0_slow%=\n\t"
  "Lfl0_back%=:\n\t"
  "buffer_load_dwordx4 a[36:39], %[vl16], %[srd], s56 offen offset:1024\n\t"
  "v_mfma_f32_16x16x32_bf16 v[60:63], a[52:55], v[88:91], v[60:63]\n\t"
  "ds_read_b64_tr_b16 v[82:83], v146 offset:6144\n\t"
  "v_fma_f32 v42, v42, v136, v166\n\t"
  "s_add_u32 s64, s60, 0\n\t"
  "v_mfma_f32_16x16x32_bf16 v[64:67], a[56:59], v[88:91], v[64:67]\n\t"
  "ds_read_b64_tr_b16 v[84:85], v147 offset:4096\n\t"
  "v_fma_f32 v43, v43, v136, v167\n\t"
  "buffer_load_dwordx4 v[184:187], %[vl16], %[srd], s64 offen offset:0 sc1\n\t"
  "buffer_load_dwordx4 a[40:43], %[vl16], %[srd], s56 offen offset:2048\n\t"
  "s_waitcnt lgkmcnt(7)\n\t"
  "v_mfma_f32_16x16x32_bf16 v[68:71], a[48:51], v[92:95], v[68:71]\n\t"
  "ds_read_b64_tr_b16 v[86:87], v147 offset:6144\n\t"
  "v_max_f32 v40, 0, v40\n\t"
  "v_max_f32 v41, 0, v41\n\t"
  "buffer_load_dwordx4 v[188:191], %[vl16], %[srd], s64 offen offset:1024 sc1\n\t"
  "v_mfma_f32_16x16x32_bf16 v[72:75], a[52:55], v[92:95], v[72:75]\n\t"
  "v_mul_f32 v40, v40, v42\n\t"
  "buffer_load_dwordx4 a[44:47], %[vl16], %[srd], s56 offen offset:3072\n\t"
  "v_mfma_f32_16x16x32_bf16 v[76:79], a[56:59], v[92:95], v[76:79]\n\t"
  "v_mul_f32 v41, v41, v43\n\t"
  "buffer_load_dwordx4 v[192:195], %[vl16], %[srd], s64 offen offset:2048 sc1\n\t"
  "s_waitcnt lgkmcnt(6)\n\t"
  "v_mfma_f32_16x16x32_bf16 v[56:59], a[60:63], v[96:99], v[56:59]\n\t"
  "ds_read_b64_tr_b16 v[88:89], v146 offset:8192\n\t"
  "v_cvt_pk_bf16_f32 v150, v40, v41\n\t"
  "v_cvt_pk_bf16_f32 v154, v40, v41\n\t"
  "s_add_u32 s64, s61, 0\n\t"
  "s_add_u32 s56, s56, 4096\n\t"
  "v_mfma_f32_16x16x32_bf16 v[60:63], a[64:67], v[96:99], v[60:63]\n\t"
  "ds_read_b64_tr_b16 v[90:91], v146 offset:10240\n\t"
  "v_permlane16_swap_b32 v152, v153\n\t"
  "buffer_load_dwordx4 a[48:51], %[vl16], %[srd], s56 offen offset:0\n\t"
  "v_mfma_f32_16x16x32_bf16 v[64:67], a[68:71], v[96:99], v[64:67]\n\t"
  "ds_read_b64_tr_b16 v[92:93], v147 offset:8192\n\t"
  "s_add_u32 s65, %[sH2st], 3145728\n\t"
  "buffer_load_dwordx4 v[196:199], %[vl16], %[srd], s64 offen offset:0 sc1\n\t"
  "s_waitcnt lgkmcnt(7)\n\t"
  "v_mfma_f32_16x16x32_bf16 v[68:71], a[60:63], v[100:103], v[68:71]\n\t"
  "ds_read_b64_tr_b16 v[94:95], v147 offset:10240\n\t"
  "v_permlane16_swap_b32 v154, v151\n\t"
  "buffer_load_dwordx4 v[200:203], %[vl16], %[srd], s64 offen offset:1024 sc1\n\t"
  "buffer_load_dwordx4 a[52:55], %[vl16], %[srd], s56 offen offset:1024\n\t"
  "v_mfma_f32_16x16x32_bf16 v[72:75], a[64:67], v[100:103], v[72:75]\n\t"
  "v_permlane32_swap_b32 v153, v151\n\t"
  "buffer_load_dwordx4 a[56:59], %[vl16], %[srd], s56 offen offset:2048\n\t"
  "v_mfma_f32_16x16x32_bf16 v[76:79], a[68:71], v[100:103], v[76:79]\n\t"
  "buffer_store_dwordx4 v[148:151], %[vh2st], %[srd], s65 offen sc1\n\t"
  "buffer_load_dwordx4 v[204:207], %[vl16], %[srd], s64 offen offset:2048 sc1\n\t"
  "s_waitcnt lgkmcnt(6)\n\t"
  "v_mfma_f32_16x16x32_bf16 v[56:59], a[72:75], v[80:83], v[56:59]\n\t"
  "ds_read_b64_tr_b16 v[96:97], v146 offset:12288\n\t"
  "v_fma_f32 v164, v104, v139, v116\n\t"
  "v_fma_f32 v165, v105, v139, v117\n\t"
  "s_add_u32 s64, s62, 0\n\t"
  "buffer_load_dwordx4 a[60:63], %[vl16], %[srd], s57 offen offset:0\n\t"
  "v_mfma_f32_16x16x32_bf16 v[60:63], a[76:79], v[80:83], v[60:63]\n\t"
  "ds_read_b64_tr_b16 v[98:99], v146 offset:14336\n\t"
  "v_fma_f32 v166, v106, v139, v118\n\t"
  "buffer_load_dwordx4 v[208:211], %[vl16], %[srd], s64 offen offset:0 sc1\n\t"
  "v_mfma_f32_16x16x32_bf16 v[64:67], a[80:83], v[80:83], v[64:67]\n\t"
  "ds_read_b64_tr_b16 v[100:101], v147 offset:12288\n\t"
  "v_fma_f32 v167, v107, v139, v119\n\t"
  "buffer_load_dwordx4 a[64:67], %[vl16], %[srd], s57 offen offset:1024\n\t"
  "s_waitcnt lgkmcnt(7)\n\t"
  "v_mfma_f32_16x16x32_bf16 v[68:71], a[72:75], v[84:87], v[68:71]\n\t"
  "ds_read_b64_tr_b16 v[102:103], v147 offset:14336\n\t"
  "v_fma_f32 v44, v44, v138, v164\n\t"
  "v_fma_f32 v45, v45, v138, v165\n\t"
  "buffer_load_dwordx4 v[212:215], %[vl16], %[srd], s64 offen offset:1024 sc1\n\t"
  "v_mfma_f32_16x16x32_bf16 v[72:75], a[76:79], v[84:87], v[72:75]\n\t"
  "v_fma_f32 v46, v46, v138, v166\n\t"
  "buffer_load_dwordx4 v[216:219], %[vl16], %[srd], s64 offen offset:2048 sc1\n\t"
  "buffer_load_dwordx4 a[68:71], %[vl16], %[srd], s57 offen offset:2048\n\t"
  "v_mfma_f32_16x16x32_bf16 v[76:79], a[80:83], v[84:87], v[76:79]\n\t"
  "v_fma_f32 v47, v47, v138, v167\n\t"
  "s_waitcnt lgkmcnt(6)\n\t"
  "v_mfma_f32_16x16x32_bf16 v[56:59], a[84:87], v[88:91], v[56:59]\n\t"
  "ds_read_b64_tr_b16 v[80:81], v146 offset:16384\n\t"
  "v_max_f32 v44, 0, v44\n\t"
  "v_max_f32 v45, 0, v45\n\t"
  "s_add_u32 s64, s63, 0\n\t"
  "buffer_load_dwordx4 a[72:75], %[vl16], %[srd], s57 offen offset:3072\n\t"
  "v_mfma_f32_16x16x32_bf16 v[60:63], a[88:91], v[88:91], v[60:63]\n\t"
  "ds_read_b64_tr_b16 v[82:83], v146 offset:18432\n\t"
  "v_mul_f32 v44, v44, v46\n\t"
  "buffer_load_dwordx4 v[220:223], %[vl16], %[srd], s64 offen offset:0 sc1\n\t"
  "s_add_u32 s57, s57, 4096\n\t"
  "v_mfma_f32_16x16x32_bf16 v[64:67], a[92:95], v[88:91], v[64:67]\n\t"
  "ds_read_b64_tr_b16 v[84:85], v147 offset:16384\n\t"
  "v_mul_f32 v45, v45, v47\n\t"
  "s_waitcnt lgkmcnt(7)\n\t"
  "v_mfma_f32_16x16x32_bf16 v[68:71], a[84:87], v[92:95], v[68:71]\n\t"
  "ds_read_b64_tr_b16 v[86:87], v147 offset:18432\n\t"
  "v_cvt_pk_bf16_f32 v156, v44, v45\n\t"
  "v_cvt_pk_bf16_f32 v160, v44, v45\n\t"
  "buffer_load_dwordx4 v[224:227], %[vl16], %[srd], s64 offen offset:1024 sc1\n\t"
  "buffer_load_dwordx4 a[76:79], %[vl16], %[srd], s57 offen offset:0\n\t"
  "v_mfma_f32_16x16x32_bf16 v[72:75], a[88:91], v[92:95], v[72:75]\n\t"
  "v_fma_f32 v164, v108, v139, v120\n\t"
  "buffer_load_dwordx4 v[228:231], %[vl16], %[srd], s64 offen offset:2048 sc1\n\t"
  "buffer_load_dwordx4 a[80:83], %[vl16], %[srd], s57 offen offset:1024\n\t"
  "v_mfma_f32_16x16x32_bf16 v[76:79], a[92:95], v[92:95], v[76:79]\n\t"
  "v_fma_f32 v165, v109, v139, v121\n\t"
  "s_waitcnt lgkmcnt(6)\n\t"
  "v_mfma_f32_16x16x32_bf16 v[56:59], a[96:99], v[96:99], v[56:59]\n\t"
  "v_add_u32 v144, s50, v234\n\t"
  "v_add_u32 v145, s50, v235\n\t"
  "v_fma_f32 v166, v110, v139, v122\n\t"
  "v_fma_f32 v167, v111, v139, v123\n\t"
  "buffer_load_dwordx4 a[84:87], %[vl16], %[srd], s57 offen offset:2048\n\t"
  "v_mfma_f32_16x16x32_bf16 v[60:63], a[100:103], v[96:99], v[60:63]\n\t"
  "ds_read_b64_tr_b16 v[88:89], v144 offset:0\n\t"
  "v_fma_f32 v48, v48, v138, v164\n\t"
  "buffer_load_dwordx4 a[88:91], %[vl16], %[srd], s57 offen offset:3072\n\t"
  "v_mfma_f32_16x16x32_bf16 v[64:67], a[104:107], v[96:99], v[64:67]\n\t"
  "ds_read_b64_tr_b16 v[90:91], v144 offset:2048\n\t"
  "v_fma_f32 v49, v49, v138, v165\n\t"
  "s_waitcnt lgkmcnt(6)\n\t"
  "v_mfma_f32_16x16x32_bf16 v[68:71], a[96:99], v[100:103], v[68:71]\n\t"
  "ds_read_b64_tr_b16 v[92:93], v145 offset:0\n\t"
  "v_fma_f32 v50, v50, v138, v166\n\t"
  "v_fma_f32 v51, v51, v138, v167\n\t"
  "s_add_u32 s57, s57, 4096\n\t"
  "v_mfma_f32_16x16x32_bf16 v[72:75], a[100:103], v[100:103], v[72:75]\n\t"
  "ds_read_b64_tr_b16 v[94:95], v145 offset:2048\n\t"
  "v_max_f32 v48, 0, v48\n\t"
  "buffer_load_dwordx4 a[92:95], %[vl16], %[srd], s57 offen offset:0\n\t"
  "v_mfma_f32_16x16x32_bf16 v[76:79], a[104:107], v[100:103], v[76:79]\n\t"
  "v_max_f32 v49, 0, v49\n\t"
  "s_waitcnt lgkmcnt(6)\n\t"
  "v_mfma_f32_16x16x32_bf16 v[56:59], a[108:111], v[80:83], v[56:59]\n\t"
  "ds_read_b64_tr_b16 v[96:97], v144 offset:4096\n\t"
  "v_mul_f32 v48, v48, v50\n\t"
  "v_mul_f32 v49, v49, v51\n\t"
  "buffer_load_dwordx4 a[96:99], %[vl16], %[srd], s57 offen offset:1024\n\t"
  "v_mfma_f32_16x16x32_bf16 v[60:63], a[112:115], v[80:83], v[60:63]\n\t"
  "ds_read_b64_tr_b16 v[98:99], v144 offset:6144\n\t"
  "v_cvt_pk_bf16_f32 v157, v48, v49\n\t"
  "v_mfma_f32_16x16x32_bf16 v[64:67], a[116:119], v[80:83], v[64:67]\n\t"
  "ds_read_b64_tr_b16 v[100:101], v145 offset:4096\n\t"
  "v_cvt_pk_bf16_f32 v161, v48, v49\n\t"
  "buffer_load_dwordx4 a[100:103], %[vl16], %[srd], s57 offen offset:2048\n\t"
  "s_waitcnt lgkmcnt(7)\n\t"
  "v_mfma_f32_16x16x32_bf16 v[68:71], a[108:111], v[84:87], v[68:71]\n\t"
  "ds_read_b64_tr_b16 v[102:103], v145 offset:6144\n\t"
  "v_fma_f32 v164, v112, v139, v124\n\t"
  "v_fma_f32 v165, v113, v139, v125\n\t"
  "v_mfma_f32_16x16x32_bf16 v[72:75], a[112:115], v[84:87], v[72:75]\n\t"
  "v_fma_f32 v166, v114, v139, v126\n\t"
  "buffer_load_dwordx4 a[104:107], %[vl16], %[srd], s57 offen offset:3072\n\t"
  "v_mfma_f32_16x16x32_bf16 v[76:79], a[116:119], v[84:87], v[76:79]\n\t"
  "v_fma_f32 v167, v115, v139, v127\n\t"
  "s_waitcnt lgkmcnt(6)\n\t"
  "v_mfma_f32_16x16x32_bf16 v[56:59], a[120:123], v[88:91], v[56:59]\n\t"
  "ds_read_b64_tr_b16 v[80:81], v144 offset:8192\n\t"
  "v_fma_f32 v52, v52, v138, v164\n\t"
  "v_fma_f32 v53, v53, v138, v165\n\t"
  "s_add_u32 s57, s57, 4096\n\t"
  "v_mfma_f32_16x16x32_bf16 v[60:63], a[124:127], v[88:91], v[60:63]\n\t"
  "ds_read_b64_tr_b16 v[82:83], v144 offset:10240\n\t"
  "v_fma_f32 v54, v54, v138, v166\n\t"
  "buffer_load_dwordx4 a[108:111], %[vl16], %[srd], s57 offen offset:0\n\t"
  "v_mfma_f32_16x16x32_bf16 v[64:67], a[128:131], v[88:91], v[64:67]\n\t"
  "ds_read_b64_tr_b16 v[84:85], v145 offset:8192\n\t"
  "v_fma_f32 v55, v55, v138, v167\n\t"
  "s_waitcnt lgkmcnt(7)\n\t"
  "v_mfma_f32_16x16x32_bf16 v[68:71], a[120:123], v[92:95], v[68:71]\n\t"
  "ds_read_b64_tr_b16 v[86:87], v145 offset:10240\n\t"
  "v_max_f32 v52, 0, v52\n\t"
  "v_max_f32 v53, 0, v53\n\t"
  "buffer_load_dwordx4 a[112:115], %[vl16], %[srd], s57 offen offset:1024\n\t"
  "v_mfma_f32_16x16x32_bf16 v[72:75], a[124:127], v[92:95], v[72:75]\n\t"
  "v_mul_f32 v52, v52, v54\n\t"
  "buffer_load_dwordx4 a[116:119], %[vl16], %[srd], s57 offen offset:2048\n\t"
  "v_mfma_f32_16x16x32_bf16 v[76:79], a[128:131], v[92:95], v[76:79]\n\t"
  "v_mul_f32 v53, v53, v55\n\t"
  "s_waitcnt lgkmcnt(6)\n\t"
  "v_mfma_f32_16x16x32_bf16 v[56:59], a[132:135], v[96:99], v[56:59]\n\t"
  "ds_read_b64_tr_b16 v[88:89], v144 offset:12288\n\t"
  "v_cvt_pk_bf16_f32 v158, v52, v53\n\t"
  "v_cvt_pk_bf16_f32 v162, v52, v53\n\t"
  "buffer_load_dwordx4 a[120:123], %[vl16], %[srd], s58 offen offset:0\n\t"
  "v_mfma_f32_16x16x32_bf16 v[60:63], a[136:139], v[96:99], v[60:63]\n\t"
  "ds_read_b64_tr_b16 v[90:91], v144 offset:14336\n\t"
  "v_permlane16_swap_b32 v160, v161\n\t"
  "v_mfma_f32_16x16x32_bf16 v[64:67], a[140:143], v[96:99], v[64:67]\n\t"
  "ds_read_b64_tr_b16 v[92:93], v145 offset:12288\n\t"
  "s_add_u32 s65, %[sH2st], 3194880\n\t"
  "buffer_load_dwordx4 a[124:127], %[vl16], %[srd], s58 offen offset:1024\n\t"
  "s_waitcnt lgkmcnt(7)\n\t"
  "v_mfma_f32_16x16x32_bf16 v[68:71], a[132:135], v[100:103], v[68:71]\n\t"
  "ds_read_b64_tr_b16 v[94:95], v145 offset:14336\n\t"
  "v_permlane16_swap_b32 v162, v159\n\t"
  "v_mfma_f32_16x16x32_bf16 v[72:75], a[136:139], v[100:103], v[72:75]\n\t"
  "s_nop 0\n\t"
  "v_permlane32_swap_b32 v161, v159\n\t"
  "buffer_load_dwordx4 a[128:131], %[vl16], %[srd], s58 offen offset:2048\n\t"
  "v_mfma_f32_16x16x32_bf16 v[76:79], a[140:143], v[100:103], v[76:79]\n\t"
  "buffer_store_dwordx4 v[156:159], %[vh2st], %[srd], s65 offen sc1\n\t"
  "s_waitcnt lgkmcnt(6)\n\t"
  "v_mfma_f32_16x16x32_bf16 v[56:59], a[144:147], v[80:83], v[56:59]\n\t"
  "ds_read_b64_tr_b16 v[96:97], v144 offset:16384\n\t"
  "buffer_load_dwordx4 a[132:135], %[vl16], %[srd], s58 offen offset:3072\n\t"
  "v_mfma_f32_16x16x32_bf16 v[60:63], a[148:151], v[80:83], v[60:63]\n\t"
  "ds_read_b64_tr_b16 v[98:99], v144 offset:18432\n\t"
  "s_add_u32 s58, s58, 4096\n\t"
  "v_mfma_f32_16x16x32_bf16 v[64:67], a[152:155], v[80:83], v[64:67]\n\t"
  "ds_read_b64_tr_b16 v[100:101], v145 offset:16384\n\t"
  "s_waitcnt lgkmcnt(7)\n\t"
  "v_mfma_f32_16x16x32_bf16 v[68:71], a[144:147], v[84:87], v[68:71]\n\t"
  "ds_read_b64_tr_b16 v[102:103], v145 offset:18432\n\t"
  "buffer_load_dwordx4 a[136:139], %[vl16], %[srd], s58 offen offset:0\n\t"
  "v_mfma_f32_16x16x32_bf16 v[72:75], a[148:151], v[84:87], v[72:75]\n\t"
  "buffer_load_dwordx4 a[140:143], %[vl16], %[srd], s58 offen offset:1024\n\t"
  "v_mfma_f32_16x16x32_bf16 v[76:79], a[152:155], v[84:87], v[76:79]\n\t"
  "s_waitcnt lgkmcnt(6)\n\t"
  "v_mfma_f32_16x16x32_bf16 v[56:59], a[156:159], v[88:91], v[56:59]\n\t"
  "v_add_u32 v146, s51, v234\n\t"
  "v_add_u32 v147, s51, v235\n\t"
  "buffer_load_dwordx4 a[144:147], %[vl16], %[srd], s58 offen offset:2048\n\t"
  "v_mfma_f32_16x16x32_bf16 v[60:63], a[160:163], v[88:91], v[60:63]\n\t"
  "ds_read_b64_tr_b16 v[80:81], v146 offset:0\n\t"
  "buffer_load_dwordx4 a[148:151], %[vl16], %[srd], s58 offen offset:3072\n\t"
  "v_mfma_f32_16x16x32_bf16 v[64:67], a[164:167], v[88:91], v[64:67]\n\t"
  "ds_read_b64_tr_b16 v[82:83], v146 offset:2048\n\t"
  "s_waitcnt lgkmcnt(6)\n\t"
  "v_mfma_f32_16x16x32_bf16 v[68:71], a[156:159], v[92:95], v[68:71]\n\t"
  "ds_read_b64_tr_b16 v[84:85], v147 offset:0\n\t"
  "s_add_u32 s58, s58, 4096\n\t"
  "v_mfma_f32_16x16x32_bf16 v[72:75], a[160:163], v[92:95], v[72:75]\n\t"
  "ds_read_b64_tr_b16 v[86:87], v147 offset:2048\n\t"
  "buffer_load_dwordx4 a[152:155], %[vl16], %[srd], s58 offen offset:0\n\t"
  "v_mfma_f32_16x16x32_bf16 v[76:79], a[164:167], v[92:95], v[76:79]\n\t"
  "s_waitcnt lgkmcnt(6)\n\t"
  "v_mfma_f32_16x16x32_bf16 v[56:59], a[168:171], v[96:99], v[56:59]\n\t"
  "ds_read_b64_tr_b16 v[88:89], v146 offset:4096\n\t"
  "buffer_load_dwordx4 a[156:159], %[vl16], %[srd], s58 offen offset:1024\n\t"
  "v_mfma_f32_16x16x32_bf16 v[60:63], a[172:175], v[96:99], v[60:63]\n\t"
  "ds_read_b64_tr_b16 v[90:91], v146 offset:6144\n\t"
  "v_mfma_f32_16x16x32_bf16 v[64:67], a[176:179], v[96:99], v[64:67]\n\t"
  "ds_read_b64_tr_b16 v[92:93], v147 offset:4096\n\t"
  "buffer_load_dwordx4 a[160:163], %[vl16], %[srd], s58 offen offset:2048\n\t"
  "s_waitcnt lgkmcnt(7)\n\t"
  "v_mfma_f32_16x16x32_bf16 v[68:71], a[168:171], v[100:103], v[68:71]\n\t"
  "ds_read_b64_tr_b16 v[94:95], v147 offset:6144\n\t"
  "v_mfma_f32_16x16x32_bf16 v[72:75], a[172:175], v[100:103], v[72:75]\n\t"
  "buffer_load_dwordx4 a[164:167], %[vl16], %[srd], s58 offen offset:3072\n\t"
  "v_mfma_f32_16x16x32_bf16 v[76:79], a[176:179], v[100:103], v[76:79]\n\t"
  "s_waitcnt lgkmcnt(6)\n\t"
  "v_mfma_f32_16x16x32_bf16 v[56:59], a[180:183], v[80:83], v[56:59]\n\t"
  "ds_read_b64_tr_b16 v[96:97], v146 offset:8192\n\t"
  "s_add_u32 s58, s58, 4096\n\t"
  "v_mfma_f32_16x16x32_bf16 v[60:63], a[184:187], v[80:83], v[60:63]\n\t"
  "ds_read_b64_tr_b16 v[98:99], v146 offset:10240\n\t"
  "buffer_load_dwordx4 a[168:171], %[vl16], %[srd], s58 offen offset:0\n\t"
  "v_mfma_f32_16x16x32_bf16 v[64:67], a[188:191], v[80:83], v[64:67]\n\t"
  "ds_read_b64_tr_b16 v[100:101], v147 offset:8192\n\t"
  "s_waitcnt lgkmcnt(7)\n\t"
  "v_mfma_f32_16x16x32_bf16 v[68:71], a[180:183], v[84:87], v[68:71]\n\t"
  "ds_read_b64_tr_b16 v[102:103], v147 offset:10240\n\t"
  "buffer_load_dwordx4 a[172:175], %[vl16], %[srd], s58 offen offset:1024\n\t"
  "v_mfma_f32_16x16x32_bf16 v[72:75], a[184:187], v[84:87], v[72:75]\n\t"
  "buffer_load_dwordx4 a[176:179], %[vl16], %[srd], s58 offen offset:2048\n\t"
  "v_mfma_f32_16x16x32_bf16 v[76:79], a[188:191], v[84:87], v[76:79]\n\t"
  "s_waitcnt lgkmcnt(6)\n\t"
  "v_mfma_f32_16x16x32_bf16 v[56:59], a[192:195], v[88:91], v[56:59]\n\t"
  "ds_read_b64_tr_b16 v[80:81], v146 offset:12288\n\t"
  "buffer_load_dwordx4 a[180:183], %[vl16], %[srd], s59 offen offset:0\n\t"
  "v_mfma_f32_16x16x32_bf16 v[60:63], a[196:199], v[88:91], v[60:63]\n\t"
  "ds_read_b64_tr_b16 v[82:83], v146 offset:14336\n\t"
  "v_mfma_f32_16x16x32_bf16 v[64:67], a[200:203], v[88:91], v[64:67]\n\t"
  "ds_read_b64_tr_b16 v[84:85], v147 offset:12288\n\t"
  "buffer_load_dwordx4 a[184:187], %[vl16], %[srd], s59 offen offset:1024\n\t"
  "s_waitcnt lgkmcnt(7)\n\t"
  "v_mfma_f32_16x16x32_bf16 v[68:71], a[192:195], v[92:95], v[68:71]\n\t"
  "ds_read_b64_tr_b16 v[86:87], v147 offset:14336\n\t"
  "v_mfma_f32_16x16x32_bf16 v[72:75], a[196:199], v[92:95], v[72:75]\n\t"
  "buffer_load_dwordx4 a[188:191], %[vl16], %[srd], s59 offen offset:2048\n\t"
  "v_mfma_f32_16x16x32_bf16 v[76:79], a[200:203], v[92:95], v[76:79]\n\t"
  "s_waitcnt lgkmcnt(6)\n\t"
  "v_mfma_f32_16x16x32_bf16 v[56:59], a[204:207], v[96:99], v[56:59]\n\t"
  "ds_read_b64_tr_b16 v[88:89], v146 offset:16384\n\t"
  "buffer_load_dwordx4 a[192:195], %[vl16], %[srd], s59 offen offset:3072\n\t"
  "v_mfma_f32_16x16x32_bf16 v[60:63], a[208:211], v[96:99], v[60:63]\n\t"
  "ds_read_b64_tr_b16 v[90:91], v146 offset:18432\n\t"
  "s_add_u32 s59, s59, 4096\n\t"
  "v_mfma_f32_16x16x32_bf16 v[64:67], a[212:215], v[96:99], v[64:67]\n\t"
  "ds_read_b64_tr_b16 v[92:93], v147 offset:16384\n\t"
  "s_waitcnt lgkmcnt(7)\n\t"
  "v_mfma_f32_16x16x32_bf16 v[68:71], a[204:207], v[100:103], v[68:71]\n\t"
  "ds_read_b64_tr_b16 v[94:95], v147 offset:18432\n\t"
  "buffer_load_dwordx4 a[196:199], %[vl16], %[srd], s59 offen offset:0\n\t"
  "v_mfma_f32_16x16x32_bf16 v[72:75], a[208:211], v[100:103], v[72:75]\n\t"
  "buffer_load_dwordx4 a[200:203], %[vl16], %[srd], s59 offen offset:1024\n\t"
  "v_mfma_f32_16x16x32_bf16 v[76:79], a[212:215], v[100:103], v[76:79]\n\t"
  "s_waitcnt lgkmcnt(6)\n\t"
  "v_mfma_f32_16x16x32_bf16 v[56:59], a[216:219], v[80:83], v[56:59]\n\t"
  "buffer_load_dwordx4 a[204:207], %[vl16], %[srd], s59 offen offset:2048\n\t"
  "v_mfma_f32_16x16x32_bf16 v[60:63], a[220:223], v[80:83], v[60:63]\n\t"
  "buffer_load_dwordx4 a[208:211], %[vl16], %[srd], s59 offen offset:3072\n\t"
  "v_mfma_f32_16x16x32_bf16 v[64:67], a[224:227], v[80:83], v[64:67]\n\t"
  "s_waitcnt lgkmcnt(4)\n\t"
  "v_mfma_f32_16x16x32_bf16 v[68:71], a[216:219], v[84:87], v[68:71]\n\t"
  "s_add_u32 s59, s59, 4096\n\t"
  "v_mfma_f32_16x16x32_bf16 v[72:75], a[220:223], v[84:87], v[72:75]\n\t"
  "buffer_load_dwordx4 a[212:215], %[vl16], %[srd], s59 offen offset:0\n\t"
  "v_mfma_f32_16x16x32_bf16 v[76:79], a[224:227], v[84:87], v[76:79]\n\t"
  "s_waitcnt lgkmcnt(2)\n\t"
  "v_mfma_f32_16x16x32_bf16 v[56:59], a[228:231], v[88:91], v[56:59]\n\t"
  "buffer_load_dwordx4 a[216:219], %[vl16], %[srd], s59 offen offset:1024\n\t"
  "s_waitcnt vmcnt(22)\n\t"
  "v_mov_b32 v169, 1\n\t"
  "buffer_store_dword v169, %[vflw], %[srd], %[sFLw] offen offset:512 sc1\n\t"
  "v_mfma_f32_16x16x32_bf16 v[60:63], a[232:235], v[88:91], v[60:63]\n\t"
  "v_mfma_f32_16x16x32_bf16 v[64:67], a[236:239], v[88:91], v[64:67]\n\t"
  "buffer_load_dwordx4 a[220:223], %[vl16], %[srd], s59 offen offset:2048\n\t"
  "s_waitcnt lgkmcnt(0)\n\t"
  "v_mfma_f32_16x16x32_bf16 v[68:71], a[228:231], v[92:95], v[68:71]\n\t"
  "v_mfma_f32_16x16x32_bf16 v[72:75], a[232:235], v[92:95], v[72:75]\n\t"
  "buffer_load_dwordx4 a[224:227], %[vl16], %[srd], s59 offen offset:3072\n\t"
  "v_mfma_f32_16x16x32_bf16 v[76:79], a[236:239], v[92:95], v[76:79]\n\t"
  "s_add_u32 s59, s59, 4096\n\t"
  "buffer_load_dwordx4 a[228:231], %[vl16], %[srd], s59 offen offset:0\n\t"
  "buffer_load_dwordx4 a[232:235], %[vl16], %[srd], s59 offen offset:1024\n\t"
  "buffer_load_dwordx4 a[236:239], %[vl16], %[srd], s59 offen offset:2048\n\t"
  "v_fma_f32 v164, v104, v141, v116\n\t"
  "v_fma_f32 v165, v105, v141, v117\n\t"
  "v_fma_f32 v166, v106, v141, v118\n\t"
  "v_fma_f32 v167, v107, v141, v119\n\t"
  "v_fma_f32 v56, v56, v140, v164\n\t"
  "v_fma_f32 v57, v57, v140, v165\n\t"
  "v_fma_f32 v58, v58, v140, v166\n\t"
  "v_fma_f32 v59, v59, v140, v167\n\t"
  "v_max_f32 v56, 0, v56\n\t"
  "v_max_f32 v57, 0, v57\n\t"
  "v_mul_f32 v56, v56, v58\n\t"
  "v_mul_f32 v57, v57, v59\n\t"
  "v_cvt_pk_bf16_f32 v148, v56, v57\n\t"
  "v_cvt_pk_bf16_f32 v152, v56, v57\n\t"
  "v_fma_f32 v164, v108, v141, v120\n\t"
  "v_fma_f32 v165, v109, v141, v121\n\t"
  "v_fma_f32 v166, v110, v141, v122\n\t"
  "v_fma_f32 v167, v111, v141, v123\n\t"
  "v_fma_f32 v60, v60, v140, v164\n\t"
  "v_fma_f32 v61, v61, v140, v165\n\t"
  "v_fma_f32 v62, v62, v140, v166\n\t"
  "v_fma_f32 v63, v63, v140, v167\n\t"
  "v_max_f32 v60, 0, v60\n\t"
  "v_max_f32 v61, 0, v61\n\t"
  "v_mul_f32 v60, v60, v62\n\t"
  "v_mul_f32 v61, v61, v63\n\t"
  "v_cvt_pk_bf16_f32 v149, v60, v61\n\t"
  "v_cvt_pk_bf16_f32 v153, v60, v61\n\t"
  "v_fma_f32 v164, v112, v141, v124\n\t"
  "v_fma_f32 v165, v113, v141, v125\n\t"
  "v_fma_f32 v166, v114, v141, v126\n\t"
  "v_fma_f32 v167, v115, v141, v127\n\t"
  "v_fma_f32 v64, v64, v140, v164\n\t"
  "v_fma_f32 v65, v65, v140, v165\n\t"
  "v_fma_f32 v66, v66, v140, v166\n\t"
  "v_fma_f32 v67, v67, v140, v167\n\t"
  "v_max_f32 v64, 0, v64\n\t"
  "v_max_f32 v65, 0, v65\n\t"
  "v_mul_f32 v64, v64, v66\n\t"
  "v_mul_f32 v65, v65, v67\n\t"
  "v_cvt_pk_bf16_f32 v150, v64, v65\n\t"
  "v_cvt_pk_bf16_f32 v154, v64, v65\n\t"
  "v_permlane16_swap_b32 v152, v153\n\t"
  "s_add_u32 s65, %[sH2st], 3244032\n\t"
  "v_permlane16_swap_b32 v154, v151\n\t"
  "s_nop 1\n\t"
  "v_permlane32_swap_b32 v153, v151\n\t"
  "buffer_store_dwordx4 v[148:151], %[vh2st], %[srd], s65 offen sc1\n\t"
  "v_fma_f32 v164, v104, v143, v116\n\t"
  "v_fma_f32 v165, v105, v143, v117\n\t"
  "v_fma_f32 v166, v106, v143, v118\n\t"
  "v_fma_f32 v167, v107, v143, v119\n\t"
  "v_fma_f32 v68, v68, v142, v164\n\t"
  "v_fma_f32 v69, v69, v142, v165\n\t"
  "v_fma_f32 v70, v70, v142, v166\n\t"
  "v_fma_f32 v71, v71, v142, v167\n\t"
  "v_max_f32 v68, 0, v68\n\t"
  "v_max_f32 v69, 0, v69\n\t"
  "v_mul_f32 v68, v68, v70\n\t"
  "v_mul_f32 v69, v69, v71\n\t"
  "v_cvt_pk_bf16_f32 v156, v68, v69\n\t"
  "v_cvt_pk_bf16_f32 v160, v68, v69\n\t"
  "v_fma_f32 v164, v108, v143, v120\n\t"
  "v_fma_f32 v165, v109, v143, v121\n\t"
  "v_fma_f32 v166, v110, v143, v122\n\t"
  "v_fma_f32 v167, v111, v143, v123\n\t"
  "v_fma_f32 v72, v72, v142, v164\n\t"
  "v_fma_f32 v73, v73, v142, v165\n\t"
  "v_fma_f32 v74, v74, v142, v166\n\t"
  "v_fma_f32 v75, v75, v142, v167\n\t"
  "v_max_f32 v72, 0, v72\n\t"
  "v_max_f32 v73, 0, v73\n\t"
  "v_mul_f32 v72, v72, v74\n\t"
  "v_mul_f32 v73, v73, v75\n\t"
  "v_cvt_pk_bf16_f32 v157, v72, v73\n\t"
  "v_cvt_pk_bf16_f32 v161, v72, v73\n\t"
  "v_fma_f32 v164, v112, v143, v124\n\t"
  "v_fma_f32 v165, v113, v143, v125\n\t"
  "v_fma_f32 v166, v114, v143, v126\n\t"
  "v_fma_f32 v167, v115, v143, v127\n\t"
  "v_fma_f32 v76, v76, v142, v164\n\t"
  "v_fma_f32 v77, v77, v142, v165\n\t"
  "v_fma_f32 v78, v78, v142, v166\n\t"
  "v_fma_f32 v79, v79, v142, v167\n\t"
  "v_max_f32 v76, 0, v76\n\t"
  "v_max_f32 v77, 0, v77\n\t"
  "v_mul_f32 v76, v76, v78\n\t"
  "v_mul_f32 v77, v77, v79\n\t"
  "v_cvt_pk_bf16_f32 v158, v76, v77\n\t"
  "v_cvt_pk_bf16_f32 v162, v76, v77\n\t"
  "v_permlane16_swap_b32 v160, v161\n\t"
  "s_add_u32 s65, %[sH2st], 3293184\n\t"
  "v_permlane16_swap_b32 v162, v159\n\t"
  "s_nop 1\n\t"
  "v_permlane32_swap_b32 v161, v159\n\t"
  "buffer_store_dwordx4 v[156:159], %[vh2st], %[srd], s65 offen sc1\n\t"
  "s_barrier\n\t"
  "v_lshrrev_b32 v170, 2, %[vl16]\n\t"
  "v_and_b32 v170, 0x3c, v170\n\t"
  "s_lshl_b32 s66, %[w], 6\n\t"
  "s_add_u32 s66, s66, %[sFL]\n\t"
  "buffer_load_dword v171, v170, %[srd], s66 offen offset:256 sc1\n\t"
  "s_cmp_eq_u32 %[w], 0\n\t"
  "s_mov_b32 s68, 0x0c0c0c0c\n\t"
  "s_mov_b32 s69, 0x0c0c0c0c\n\t"
  "s_cmov_b32 s68, 0x01000c0c\n\t"
  "s_cmov_b32 s69, 0x03020c0c\n\t"
  "v_perm_b32 v32, v172, v172, s68\n\t"
  "v_perm_b32 v33, v172, v172, s69\n\t"
  "v_perm_b32 v34, v173, v173, s68\n\t"
  "v_perm_b32 v35, v173, v173, s69\n\t"
  "v_perm_b32 v36, v174, v174, s68\n\t"
  "v_perm_b32 v37, v174, v174, s69\n\t"
  "v_perm_b32 v38, v175, v175, s68\n\t"
  "v_perm_b32 v39, v175, v175, s69\n\t"
  "v_perm_b32 v40, v176, v176, s68\n\t"
  "v_perm_b32 v41, v176, v176, s69\n\t"
  "v_perm_b32 v42, v177, v177, s68\n\t"
  "v_perm_b32 v43, v177, v177, s69\n\t"
  "v_perm_b32 v44, v178, v178, s68\n\t"
  "v_perm_b32 v45, v178, v178, s69\n\t"
  "v_perm_b32 v46, v179, v179, s68\n\t"
  "v_perm_b32 v47, v179, v179, s69\n\t"
  "v_perm_b32 v48, v180, v180, s68\n\t"
  "v_perm_b32 v49, v180, v180, s69\n\t"
  "v_perm_b32 v50, v181, v181, s68\n\t"
  "v_perm_b32 v51, v181, v181, s69\n\t"
  "v_mfma_f32_16x16x32_bf16 v[32:35], a[0:3], v[184:187], v[32:35]\n\t"
  "v_mfma_f32_16x16x32_bf16 v[36:39], a[4:7], v[184:187], v[36:39]\n\t"
  "v_mfma_f32_16x16x32_bf16 v[40:43], a[8:11], v[184:187], v[40:43]\n\t"
  "v_mfma_f32_16x16x32_bf16 v[44:47], a[12:15], v[184:187], v[44:47]\n\t"
  "v_mfma_f32_16x16x32_bf16 v[48:51], a[16:19], v[184:187], v[48:51]\n\t"
  "s_waitcnt vmcnt(0)\n\t"
  "v_cmp_ne_u32 vcc, 0, v171\n\t"
  "s_nop 3\n\t"
  "s_cmp_eq_u64 vcc, -1\n\t"
  "s_cbranch_scc0 Lfl1_slow%=\n\t"
  "Lfl1_back%=:\n\t"
  "v_mfma_f32_16x16x32_bf16 v[32:35], a[20:23], v[188:191], v[32:35]\n\t"
  "s_add_u32 s64, s60, 98304\n\t"
  "buffer_load_dwordx4 v[72:75], %[vl16], %[srd], s64 offen offset:0 sc1\n\t"
  "v_mfma_f32_16x16x32_bf16 v[36:39], a[24:27], v[188:191], v[36:39]\n\t"
  "buffer_load_dwordx4 v[76:79], %[vl16], %[srd], s64 offen offset:1024 sc1\n\t"
  "v_mfma_f32_16x16x32_bf16 v[40:43], a[28:31], v[188:191], v[40:43]\n\t"
  "buffer_load_dwordx4 v[80:83], %[vl16], %[srd], s64 offen offset:2048 sc1\n\t"
  "v_mfma_f32_16x16x32_bf16 v[44:47], a[32:35], v[188:191], v[44:47]\n\t"
  "s_add_u32 s64, s61, 98304\n\t"
  "v_mfma_f32_16x16x32_bf16 v[48:51], a[36:39], v[188:191], v[48:51]\n\t"
  "buffer_load_dwordx4 v[84:87], %[vl16], %[srd], s64 offen offset:0 sc1\n\t"
  "v_mfma_f32_16x16x32_bf16 v[32:35], a[40:43], v[192:195], v[32:35]\n\t"
  "buffer_load_dwordx4 v[88:91], %[vl16], %[srd], s64 offen offset:1024 sc1\n\t"
  "v_mfma_f32_16x16x32_bf16 v[36:39], a[44:47], v[192:195], v[36:39]\n\t"
  "buffer_load_dwordx4 v[92:95], %[vl16], %[srd], s64 offen offset:2048 sc1\n\t"
  "v_mfma_f32_16x16x32_bf16 v[40:43], a[48:51], v[192:195], v[40:43]\n\t"
  "s_add_u32 s64, s62, 98304\n\t"
  "v_mfma_f32_16x16x32_bf16 v[44:47], a[52:55], v[192:195], v[44:47]\n\t"
  "buffer_load_dwordx4 v[96:99], %[vl16], %[srd], s64 offen offset:0 sc1\n\t"
  "v_mfma_f32_16x16x32_bf16 v[48:51], a[56:59], v[192:195], v[48:51]\n\t"
  "buffer_load_dwordx4 v[100:103], %[vl16], %[srd], s64 offen offset:1024 sc1\n\t"
  "v_mfma_f32_16x16x32_bf16 v[32:35], a[60:63], v[196:199], v[32:35]\n\t"
  "buffer_load_dwordx4 v[104:107], %[vl16], %[srd], s64 offen offset:2048 sc1\n\t"
  "v_mfma_f32_16x16x32_bf16 v[36:39], a[64:67], v[196:199], v[36:39]\n\t"
  "s_add_u32 s64, s63, 98304\n\t"
  "v_mfma_f32_16x16x32_bf16 v[40:43], a[68:71], v[196:199], v[40:43]\n\t"
  "buffer_load_dwordx4 v[108:111], %[vl16], %[srd], s64 offen offset:0 sc1\n\t"
  "v_mfma_f32_16x16x32_bf16 v[44:47], a[72:75], v[196:199], v[44:47]\n\t"
  "buffer_load_dwordx4 v[112:115], %[vl16], %[srd], s64 offen offset:1024 sc1\n\t"
  "v_mfma_f32_16x16x32_bf16 v[48:51], a[76:79], v[196:199], v[48:51]\n\t"
  "buffer_load_dwordx4 v[116:119], %[vl16], %[srd], s64 offen offset:2048 sc1\n\t"
  "v_mfma_f32_16x16x32_bf16 v[32:35], a[80:83], v[200:203], v[32:35]\n\t"
  "v_lshrrev_b32 v170, 2, %[vl16]\n\t"
  "v_and_b32 v170, 0x3c, v170\n\t"
  "v_mfma_f32_16x16x32_bf16 v[36:39], a[84:87], v[200:203], v[36:39]\n\t"
  "s_lshl_b32 s66, %[w], 6\n\t"
  "v_mfma_f32_16x16x32_bf16 v[40:43], a[88:91], v[200:203], v[40:43]\n\t"
  "s_add_u32 s66, s66, %[sFL]\n\t"
  "v_mfma_f32_16x16x32_bf16 v[44:47], a[92:95], v[200:203], v[44:47]\n\t"
  "buffer_load_dword v171, v170, %[srd], s66 offen offset:512 sc1\n\t"
  "v_mfma_f32_16x16x32_bf16 v[48:51], a[96:99], v[200:203], v[48:51]\n\t"
  "v_mfma_f32_16x16x32_bf16 v[32:35], a[100:103], v[204:207], v[32:35]\n\t"
  "v_mfma_f32_16x16x32_bf16 v[36:39], a[104:107], v[204:207], v[36:39]\n\t"
  "v_mfma_f32_16x16x32_bf16 v[40:43], a[108:111], v[204:207], v[40:43]\n\t"
  "v_mfma_f32_16x16x32_bf16 v[44:47], a[112:115], v[204:207], v[44:47]\n\t"
  "v_mfma_f32_16x16x32_bf16 v[48:51], a[116:119], v[204:207], v[48:51]\n\t"
  "v_mfma_f32_16x16x32_bf16 v[32:35], a[120:123], v[208:211], v[32:35]\n\t"
  "v_mov_b32 v169, 1\n\t"
  "buffer_store_dword v169, %[vflw], %[srd], %[sFLw] offen offset:768 sc1\n\t"
  "s_cmp_eq_u32 %[w], 1\n\t"
  "s_mov_b32 s68, 0x0c0c0c0c\n\t"
  "s_mov_b32 s69, 0x0c0c0c0c\n\t"
  "s_cmov_b32 s68, 0x01000c0c\n\t"
  "s_cmov_b32 s69, 0x03020c0c\n\t"
  "v_mfma_f32_16x16x32_bf16 v[36:39], a[124:127], v[208:211], v[36:39]\n\t"
  "v_perm_b32 v52, v172, v172, s68\n\t"
  "v_mfma_f32_16x16x32_bf16 v[40:43], a[128:131], v[208:211], v[40:43]\n\t"
  "v_perm_b32 v53, v172, v172, s69\n\t"
  "v_mfma_f32_16x16x32_bf16 v[44:47], a[132:135], v[208:211], v[44:47]\n\t"
  "v_perm_b32 v54, v173, v173, s68\n\t"
  "v_mfma_f32_16x16x32_bf16 v[48:51], a[136:139], v[208:211], v[48:51]\n\t"
  "v_perm_b32 v55, v173, v173, s69\n\t"
  "v_mfma_f32_16x16x32_bf16 v[32:35], a[140:143], v[212:215], v[32:35]\n\t"
  "v_mfma_f32_16x16x32_bf16 v[36:39], a[144:147], v[212:215], v[36:39]\n\t"
  "v_perm_b32 v56, v174, v174, s68\n\t"
  "v_mfma_f32_16x16x32_bf16 v[40:43], a[148:151], v[212:215], v[40:43]\n\t"
  "v_perm_b32 v57, v174, v174, s69\n\t"
  "v_mfma_f32_16x16x32_bf16 v[44:47], a[152:155], v[212:215], v[44:47]\n\t"
  "v_perm_b32 v58, v175, v175, s68\n\t"
  "v_mfma_f32_16x16x32_bf16 v[48:51], a[156:159], v[212:215], v[48:51]\n\t"
  "v_perm_b32 v59, v175, v175, s69\n\t"
  "v_mfma_f32_16x16x32_bf16 v[32:35], a[160:163], v[216:219], v[32:35]\n\t"
  "s_waitcnt vmcnt(1)\n\t"
  "v_cmp_ne_u32 vcc, 0, v171\n\t"
  "s_nop 3\n\t"
  "s_cmp_eq_u64 vcc, -1\n\t"
  "s_cbranch_scc0 Lfl2_slow%=\n\t"
  "Lfl2_back%=:\n\t"
  "v_mfma_f32_16x16x32_bf16 v[36:39], a[164:167], v[216:219], v[36:39]\n\t"
  "s_add_u32 s64, s60, 3145728\n\t"
  "buffer_load_dwordx4 v[120:123], %[vl16], %[srd], s64 offen offset:0 sc1\n\t"
  "v_perm_b32 v60, v176, v176, s68\n\t"
  "v_mfma_f32_16x16x32_bf16 v[40:43], a[168:171], v[216:219], v[40:43]\n\t"
  "buffer_load_dwordx4 v[124:127], %[vl16], %[srd], s64 offen offset:1024 sc1\n\t"
  "v_perm_b32 v61, v176, v176, s69\n\t"
  "v_mfma_f32_16x16x32_bf16 v[44:47], a[172:175], v[216:219], v[44:47]\n\t"
  "buffer_load_dwordx4 v[128:131], %[vl16], %[srd], s64 offen offset:2048 sc1\n\t"
  "v_perm_b32 v62, v177, v177, s68\n\t"
  "v_mfma_f32_16x16x32_bf16 v[48:51], a[176:179], v[216:219], v[48:51]\n\t"
  "s_add_u32 s64, s61, 3145728\n\t"
  "v_perm_b32 v63, v177, v177, s69\n\t"
  "v_mfma_f32_16x16x32_bf16 v[32:35], a[180:183], v[220:223], v[32:35]\n\t"
  "buffer_load_dwordx4 v[132:135], %[vl16], %[srd], s64 offen offset:0 sc1\n\t"
  "v_mfma_f32_16x16x32_bf16 v[36:39], a[184:187], v[220:223], v[36:39]\n\t"
  "buffer_load_dwordx4 v[136:139], %[vl16], %[srd], s64 offen offset:1024 sc1\n\t"
  "v_perm_b32 v64, v178, v178, s68\n\t"
  "v_mfma_f32_16x16x32_bf16 v[40:43], a[188:191], v[220:223], v[40:43]\n\t"
  "buffer_load_dwordx4 v[140:143], %[vl16], %[srd], s64 offen offset:2048 sc1\n\t"
  "v_perm_b32 v65, v178, v178, s69\n\t"
  "v_mfma_f32_16x16x32_bf16 v[44:47], a[192:195], v[220:223], v[44:47]\n\t"
  "s_add_u32 s64, s62, 3145728\n\t"
  "v_perm_b32 v66, v179, v179, s68\n\t"
  "v_mfma_f32_16x16x32_bf16 v[48:51], a[196:199], v[220:223], v[48:51]\n\t"
  "buffer_load_dwordx4 v[144:147], %[vl16], %[srd], s64 offen offset:0 sc1\n\t"
  "v_perm_b32 v67, v179, v179, s69\n\t"
  "v_mfma_f32_16x16x32_bf16 v[32:35], a[200:203], v[224:227], v[32:35]\n\t"
  "buffer_load_dwordx4 v[148:151], %[vl16], %[srd], s64 offen offset:1024 sc1\n\t"
  "v_mfma_f32_16x16x32_bf16 v[36:39], a[204:207], v[224:227], v[36:39]\n\t"
  "buffer_load_dwordx4 v[152:155], %[vl16], %[srd], s64 offen offset:2048 sc1\n\t"
  "v_perm_b32 v68, v180, v180, s68\n\t"
  "v_mfma_f32_16x16x32_bf16 v[40:43], a[208:211], v[224:227], v[40:43]\n\t"
  "s_add_u32 s64, s63, 3145728\n\t"
  "v_perm_b32 v69, v180, v180, s69\n\t"
  "v_mfma_f32_16x16x32_bf16 v[44:47], a[212:215], v[224:227], v[44:47]\n\t"
  "buffer_load_dwordx4 v[156:159], %[vl16], %[srd], s64 offen offset:0 sc1\n\t"
  "v_perm_b32 v70, v181, v181, s68\n\t"
  "v_mfma_f32_16x16x32_bf16 v[48:51], a[216:219], v[224:227], v[48:51]\n\t"
  "buffer_load_dwordx4 v[160:163], %[vl16], %[srd], s64 offen offset:1024 sc1\n\t"
  "v_perm_b32 v71, v181, v181, s69\n\t"
  "v_mfma_f32_16x16x32_bf16 v[32:35], a[220:223], v[228:231], v[32:35]\n\t"
  "buffer_load_dwordx4 v[164:167], %[vl16], %[srd], s64 offen offset:2048 sc1\n\t"
  "v_mfma_f32_16x16x32_bf16 v[36:39], a[224:227], v[228:231], v[36:39]\n\t"
  "v_mfma_f32_16x16x32_bf16 v[40:43], a[228:231], v[228:231], v[40:43]\n\t"
  "v_mfma_f32_16x16x32_bf16 v[44:47], a[232:235], v[228:231], v[44:47]\n\t"
  "v_mfma_f32_16x16x32_bf16 v[48:51], a[236:239], v[228:231], v[48:51]\n\t"
  "v_mfma_f32_16x16x32_bf16 v[52:55], a[0:3], v[72:75], v[52:55]\n\t"
  "v_mfma_f32_16x16x32_bf16 v[56:59], a[4:7], v[72:75], v[56:59]\n\t"
  "v_mfma_f32_16x16x32_bf16 v[60:63], a[8:11], v[72:75], v[60:63]\n\t"
  "v_mfma_f32_16x16x32_bf16 v[64:67], a[12:15], v[72:75], v[64:67]\n\t"
  "v_mfma_f32_16x16x32_bf16 v[68:71], a[16:19], v[72:75], v[68:71]\n\t"
  "ds_write_b128 %[vwr], v[32:35] offset:0\n\t"
  "v_mfma_f32_16x16x32_bf16 v[52:55], a[20:23], v[76:79], v[52:55]\n\t"
  "ds_write_b128 %[vwr], v[36:39] offset:1024\n\t"
  "v_mfma_f32_16x16x32_bf16 v[56:59], a[24:27], v[76:79], v[56:59]\n\t"
  "v_mfma_f32_16x16x32_bf16 v[60:63], a[28:31], v[76:79], v[60:63]\n\t"
  "ds_write_b128 %[vwr], v[40:43] offset:2048\n\t"
  "v_mfma_f32_16x16x32_bf16 v[64:67], a[32:35], v[76:79], v[64:67]\n\t"
  "ds_write_b128 %[vwr], v[44:47] offset:3072\n\t"
  "v_mfma_f32_16x16x32_bf16 v[68:71], a[36:39], v[76:79], v[68:71]\n\t"
  "v_mfma_f32_16x16x32_bf16 v[52:55], a[40:43], v[80:83], v[52:55]\n\t"
  "ds_write_b128 %[vwr], v[48:51] offset:4096\n\t"
  "v_mfma_f32_16x16x32_bf16 v[56:59], a[44:47], v[80:83], v[56:59]\n\t"
  "v_mfma_f32_16x16x32_bf16 v[60:63], a[48:51], v[80:83], v[60:63]\n\t"
  "s_cmp_eq_u32 %[w], 2\n\t"
  "s_mov_b32 s68, 0x0c0c0c0c\n\t"
  "s_mov_b32 s69, 0x0c0c0c0c\n\t"
  "s_cmov_b32 s68, 0x01000c0c\n\t"
  "s_cmov_b32 s69, 0x03020c0c\n\t"
  "v_perm_b32 v32, v172, v172, s68\n\t"
  "v_mfma_f32_16x16x32_bf16 v[64:67], a[52:55], v[80:83], v[64:67]\n\t"
  "v_perm_b32 v33, v172, v172, s69\n\t"
  "v_mfma_f32_16x16x32_bf16 v[68:71], a[56:59], v[80:83], v[68:71]\n\t"
  "v_perm_b32 v34, v173, v173, s68\n\t"
  "v_mfma_f32_16x16x32_bf16 v[52:55], a[60:63], v[84:87], v[52:55]\n\t"
  "v_perm_b32 v35, v173, v173, s69\n\t"
  "v_mfma_f32_16x16x32_bf16 v[56:59], a[64:67], v[84:87], v[56:59]\n\t"
  "v_perm_b32 v36, v174, v174, s68\n\t"
  "v_mfma_f32_16x16x32_bf16 v[60:63], a[68:71], v[84:87], v[60:63]\n\t"
  "v_perm_b32 v37, v174, v174, s69\n\t"
  "v_mfma_f32_16x16x32_bf16 v[64:67], a[72:75], v[84:87], v[64:67]\n\t"
  "v_perm_b32 v38, v175, v175, s68\n\t"
  "v_mfma_f32_16x16x32_bf16 v[68:71], a[76:79], v[84:87], v[68:71]\n\t"
  "v_perm_b32 v39, v175, v175, s69\n\t"
  "v_mfma_f32_16x16x32_bf16 v[52:55], a[80:83], v[88:91], v[52:55]\n\t"
  "s_waitcnt lgkmcnt(0)\n\t"
  "s_barrier\n\t"
  "v_perm_b32 v40, v176, v176, s68\n\t"
  "v_mfma_f32_16x16x32_bf16 v[56:59], a[84:87], v[88:91], v[56:59]\n\t"
  "v_perm_b32 v41, v176, v176, s69\n\t"
  "v_add_u32 v216, 61440, %[vrd0]\n\t"
  "v_add_u32 v217, 61440, %[vrd1]\n\t"
  "v_mfma_f32_16x16x32_bf16 v[60:63], a[88:91], v[88:91], v[60:63]\n\t"
  "v_perm_b32 v42, v177, v177, s68\n\t"
  "ds_read_b128 v[184:187], %[vrd0] offset:0\n\t"
  "v_mfma_f32_16x16x32_bf16 v[64:67], a[92:95], v[88:91], v[64:67]\n\t"
  "v_perm_b32 v43, v177, v177, s69\n\t"
  "ds_read_b128 v[188:191], %[vrd1] offset:0\n\t"
  "v_mfma_f32_16x16x32_bf16 v[68:71], a[96:99], v[88:91], v[68:71]\n\t"
  "v_perm_b32 v44, v178, v178, s68\n\t"
  "ds_read_b128 v[192:195], %[vrd0] offset:20480\n\t"
  "ds_read_b128 v[196:199], %[vrd1] offset:20480\n\t"
  "v_mfma_f32_16x16x32_bf16 v[52:55], a[100:103], v[92:95], v[52:55]\n\t"
  "v_perm_b32 v45, v178, v178, s69\n\t"
  "ds_read_b128 v[200:203], %[vrd0] offset:40960\n\t"
  "v_mfma_f32_16x16x32_bf16 v[56:59], a[104:107], v[92:95], v[56:59]\n\t"
  "v_perm_b32 v46, v179, v179, s68\n\t"
  "ds_read_b128 v[204:207], %[vrd1] offset:40960\n\t"
  "v_mfma_f32_16x16x32_bf16 v[60:63], a[108:111], v[92:95], v[60:63]\n\t"
  "v_perm_b32 v47, v179, v179, s69\n\t"
  "ds_read_b128 v[208:211], v216 offset:0\n\t"
  "v_mfma_f32_16x16x32_bf16 v[64:67], a[112:115], v[92:95], v[64:67]\n\t"
  "v_perm_b32 v48, v180, v180, s68\n\t"
  "ds_read_b128 v[212:215], v217 offset:0\n\t"
  "v_mfma_f32_16x16x32_bf16 v[68:71], a[116:119], v[92:95], v[68:71]\n\t"
  "v_perm_b32 v49, v180, v180, s69\n\t"
  "v_mfma_f32_16x16x32_bf16 v[52:55], a[120:123], v[96:99], v[52:55]\n\t"
  "v_perm_b32 v50, v181, v181, s68\n\t"
  "v_mfma_f32_16x16x32_bf16 v[56:59], a[124:127], v[96:99], v[56:59]\n\t"
  "v_perm_b32 v51, v181, v181, s69\n\t"
  "v_mfma_f32_16x16x32_bf16 v[60:63], a[128:131], v[96:99], v[60:63]\n\t"
  "s_waitcnt lgkmcnt(5)\n\t"
  "v_add_f32 v184, v184, v192\n\t"
  "v_add_f32 v185, v185, v193\n\t"
  "v_mfma_f32_16x16x32_bf16 v[64:67], a[132:135], v[96:99], v[64:67]\n\t"
  "v_add_f32 v186, v186, v194\n\t"
  "v_mfma_f32_16x16x32_bf16 v[68:71], a[136:139], v[96:99], v[68:71]\n\t"
  "v_add_f32 v187, v187, v195\n\t"
  "v_mfma_f32_16x16x32_bf16 v[52:55], a[140:143], v[100:103], v[52:55]\n\t"
  "s_waitcnt lgkmcnt(4)\n\t"
  "v_add_f32 v188, v188, v196\n\t"
  "v_mfma_f32_16x16x32_bf16 v[56:59], a[144:147], v[100:103], v[56:59]\n\t"
  "v_add_f32 v189, v189, v197\n\t"
  "v_add_f32 v190, v190, v198\n\t"
  "v_mfma_f32_16x16x32_bf16 v[60:63], a[148:151], v[100:103], v[60:63]\n\t"
  "v_add_f32 v191, v191, v199\n\t"
  "v_mfma_f32_16x16x32_bf16 v[64:67], a[152:155], v[100:103], v[64:67]\n\t"
  "s_waitcnt lgkmcnt(1)\n\t"
  "v_add_f32 v200, v200, v208\n\t"
  "v_mfma_f32_16x16x32_bf16 v[68:71], a[156:159], v[100:103], v[68:71]\n\t"
  "v_add_f32 v201, v201, v209\n\t"
  "v_mfma_f32_16x16x32_bf16 v[52:55], a[160:163], v[104:107], v[52:55]\n\t"
  "v_add_f32 v202, v202, v210\n\t"
  "v_add_f32 v203, v203, v211\n\t"
  "v_mfma_f32_16x16x32_bf16 v[56:59], a[164:167], v[104:107], v[56:59]\n\t"
  "s_waitcnt lgkmcnt(0)\n\t"
  "v_add_f32 v204, v204, v212\n\t"
  "v_mfma_f32_16x16x32_bf16 v[60:63], a[168:171], v[104:107], v[60:63]\n\t"
  "v_add_f32 v205, v205, v213\n\t"
  "v_mfma_f32_16x16x32_bf16 v[64:67], a[172:175], v[104:107], v[64:67]\n\t"
  "v_add_f32 v206, v206, v214\n\t"
  "v_mfma_f32_16x16x32_bf16 v[68:71], a[176:179], v[104:107], v[68:71]\n\t"
  "v_add_f32 v207, v207, v215\n\t"
  "v_add_f32 v184, v184, v200\n\t"
  "v_mfma_f32_16x16x32_bf16 v[52:55], a[180:183], v[108:111], v[52:55]\n\t"
  "v_add_f32 v185, v185, v201\n\t"
  "v_mfma_f32_16x16x32_bf16 v[56:59], a[184:187], v[108:111], v[56:59]\n\t"
  "v_add_f32 v186, v186, v202\n\t"
  "v_mfma_f32_16x16x32_bf16 v[60:63], a[188:191], v[108:111], v[60:63]\n\t"
  "v_add_f32 v187, v187, v203\n\t"
  "v_mfma_f32_16x16x32_bf16 v[64:67], a[192:195], v[108:111], v[64:67]\n\t"
  "v_add_f32 v188, v188, v204\n\t"
  "v_add_f32 v189, v189, v205\n\t"
  "v_mfma_f32_16x16x32_bf16 v[68:71], a[196:199], v[108:111], v[68:71]\n\t"
  "v_add_f32 v190, v190, v206\n\t"
  "v_mfma_f32_16x16x32_bf16 v[52:55], a[200:203], v[112:115], v[52:55]\n\t"
  "v_add_f32 v191, v191, v207\n\t"
  "v_mfma_f32_16x16x32_bf16 v[56:59], a[204:207], v[112:115], v[56:59]\n\t"
  "v_cvt_pk_bf16_f32 v192, v184, v185\n\t"
  "v_mfma_f32_16x16x32_bf16 v[60:63], a[208:211], v[112:115], v[60:63]\n\t"
  "v_cvt_pk_bf16_f32 v193, v186, v187\n\t"
  "v_cvt_pk_bf16_f32 v194, v188, v189\n\t"
  "v_mfma_f32_16x16x32_bf16 v[64:67], a[212:215], v[112:115], v[64:67]\n\t"
  "v_cvt_pk_bf16_f32 v195, v190, v191\n\t"
  "v_mfma_f32_16x16x32_bf16 v[68:71], a[216:219], v[112:115], v[68:71]\n\t"
  "s_mov_b32 s65, 0\n\t"
  "v_mfma_f32_16x16x32_bf16 v[52:55], a[220:223], v[116:119], v[52:55]\n\t"
  "buffer_store_dwordx4 v[192:195], %[vout], %[srdo], s65 offen\n\t"
  "v_mfma_f32_16x16x32_bf16 v[56:59], a[224:227], v[116:119], v[56:59]\n\t"
  "v_lshrrev_b32 v170, 2, %[vl16]\n\t"
  "v_and_b32 v170, 0x3c, v170\n\t"
  "v_mfma_f32_16x16x32_bf16 v[60:63], a[228:231], v[116:119], v[60:63]\n\t"
  "s_lshl_b32 s66, %[w], 6\n\t"
  "s_add_u32 s66, s66, %[sFL]\n\t"
  "v_mfma_f32_16x16x32_bf16 v[64:67], a[232:235], v[116:119], v[64:67]\n\t"
  "buffer_load_dword v171, v170, %[srd], s66 offen offset:768 sc1\n\t"
  "v_mfma_f32_16x16x32_bf16 v[68:71], a[236:239], v[116:119], v[68:71]\n\t"
  "s_waitcnt vmcnt(13)\n\t"
  "v_mfma_f32_16x16x32_bf16 v[32:35], a[0:3], v[120:123], v[32:35]\n\t"
  "v_mfma_f32_16x16x32_bf16 v[36:39], a[4:7], v[120:123], v[36:39]\n\t"
  "v_mfma_f32_16x16x32_bf16 v[40:43], a[8:11], v[120:123], v[40:43]\n\t"
  "v_mfma_f32_16x16x32_bf16 v[44:47], a[12:15], v[120:123], v[44:47]\n\t"
  "v_mfma_f32_16x16x32_bf16 v[48:51], a[16:19], v[120:123], v[48:51]\n\t"
  "ds_write_b128 %[vwr], v[52:55] offset:5120\n\t"
  "s_waitcnt vmcnt(12)\n\t"
  "v_mfma_f32_16x16x32_bf16 v[32:35], a[20:23], v[124:127], v[32:35]\n\t"
  "ds_write_b128 %[vwr], v[56:59] offset:6144\n\t"
  "v_mfma_f32_16x16x32_bf16 v[36:39], a[24:27], v[124:127], v[36:39]\n\t"
  "v_mfma_f32_16x16x32_bf16 v[40:43], a[28:31], v[124:127], v[40:43]\n\t"
  "ds_write_b128 %[vwr], v[60:63] offset:7168\n\t"
  "v_mfma_f32_16x16x32_bf16 v[44:47], a[32:35], v[124:127], v[44:47]\n\t"
  "ds_write_b128 %[vwr], v[64:67] offset:8192\n\t"
  "v_mfma_f32_16x16x32_bf16 v[48:51], a[36:39], v[124:127], v[48:51]\n\t"
  "s_waitcnt vmcnt(11)\n\t"
  "v_mfma_f32_16x16x32_bf16 v[32:35], a[40:43], v[128:131], v[32:35]\n\t"
  "ds_write_b128 %[vwr], v[68:71] offset:9216\n\t"
  "s_waitcnt vmcnt(0)\n\t"
  "v_cmp_ne_u32 vcc, 0, v171\n\t"
  "s_nop 3\n\t"
  "s_cmp_eq_u64 vcc, -1\n\t"
  "s_cbranch_scc0 Lfl3_slow%=\n\t"
  "Lfl3_back%=:\n\t"
  "v_mfma_f32_16x16x32_bf16 v[36:39], a[44:47], v[128:131], v[36:39]\n\t"
  "s_add_u32 s64, s60, 3244032\n\t"
  "buffer_load_dwordx4 v[184:187], %[vl16], %[srd], s64 offen offset:0 sc1\n\t"
  "v_mfma_f32_16x16x32_bf16 v[40:43], a[48:51], v[128:131], v[40:43]\n\t"
  "s_cmp_eq_u32 %[w], 3\n\t"
  "s_mov_b32 s68, 0x0c0c0c0c\n\t"
  "s_mov_b32 s69, 0x0c0c0c0c\n\t"
  "s_cmov_b32 s68, 0x01000c0c\n\t"
  "s_cmov_b32 s69, 0x03020c0c\n\t"
  "v_perm_b32 v52, v172, v172, s68\n\t"
  "buffer_load_dwordx4 v[188:191], %[vl16], %[srd], s64 offen offset:1024 sc1\n\t"
  "v_mfma_f32_16x16x32_bf16 v[44:47], a[52:55], v[128:131], v[44:47]\n\t"
  "v_perm_b32 v53, v172, v172, s69\n\t"
  "buffer_load_dwordx4 v[192:195], %[vl16], %[srd], s64 offen offset:2048 sc1\n\t"
  "v_mfma_f32_16x16x32_bf16 v[48:51], a[56:59], v[128:131], v[48:51]\n\t"
  "v_perm_b32 v54, v173, v173, s68\n\t"
  "s_add_u32 s64, s61, 3244032\n\t"
  "v_mfma_f32_16x16x32_bf16 v[32:35], a[60:63], v[132:135], v[32:35]\n\t"
  "v_perm_b32 v55, v173, v173, s69\n\t"
  "buffer_load_dwordx4 v[196:199], %[vl16], %[srd], s64 offen offset:0 sc1\n\t"
  "buffer_load_dwordx4 v[200:203], %[vl16], %[srd], s64 offen offset:1024 sc1\n\t"
  "v_mfma_f32_16x16x32_bf16 v[36:39], a[64:67], v[132:135], v[36:39]\n\t"
  "v_perm_b32 v56, v174, v174, s68\n\t"
  "buffer_load_dwordx4 v[204:207], %[vl16], %[srd], s64 offen offset:2048 sc1\n\t"
  "v_mfma_f32_16x16x32_bf16 v[40:43], a[68:71], v[132:135], v[40:43]\n\t"
  "v_perm_b32 v57, v174, v174, s69\n\t"
  "s_add_u32 s64, s62, 3244032\n\t"
  "v_mfma_f32_16x16x32_bf16 v[44:47], a[72:75], v[132:135], v[44:47]\n\t"
  "v_perm_b32 v58, v175, v175, s68\n\t"
  "buffer_load_dwordx4 v[208:211], %[vl16], %[srd], s64 offen offset:0 sc1\n\t"
  "v_mfma_f32_16x16x32_bf16 v[48:51], a[76:79], v[132:135], v[48:51]\n\t"
  "v_perm_b32 v59, v175, v175, s69\n\t"
  "buffer_load_dwordx4 v[212:215], %[vl16], %[srd], s64 offen offset:1024 sc1\n\t"
  "buffer_load_dwordx4 v[216:219], %[vl16], %[srd], s64 offen offset:2048 sc1\n\t"
  "v_mfma_f32_16x16x32_bf16 v[32:35], a[80:83], v[136:139], v[32:35]\n\t"
  "s_waitcnt lgkmcnt(0)\n\t"
  "s_barrier\n\t"
  "v_perm_b32 v60, v176, v176, s68\n\t"
  "s_add_u32 s64, s63, 3244032\n\t"
  "v_mfma_f32_16x16x32_bf16 v[36:39], a[84:87], v[136:139], v[36:39]\n\t"
  "v_perm_b32 v61, v176, v176, s69\n\t"
  "v_add_u32 v104, 61440, %[vrd0]\n\t"
  "v_add_u32 v105, 61440, %[vrd1]\n\t"
  "buffer_load_dwordx4 v[220:223], %[vl16], %[srd], s64 offen offset:0 sc1\n\t"
  "v_mfma_f32_16x16x32_bf16 v[40:43], a[88:91], v[136:139], v[40:43]\n\t"
  "v_perm_b32 v62, v177, v177, s68\n\t"
  "ds_read_b128 v[72:75], %[vrd0] offset:5120\n\t"
  "buffer_load_dwordx4 v[224:227], %[vl16], %[srd], s64 offen offset:1024 sc1\n\t"
  "v_mfma_f32_16x16x32_bf16 v[44:47], a[92:95], v[136:139], v[44:47]\n\t"
  "v_perm_b32 v63, v177, v177, s69\n\t"
  "ds_read_b128 v[76:79], %[vrd1] offset:5120\n\t"
  "buffer_load_dwordx4 v[228:231], %[vl16], %[srd], s64 offen offset:2048 sc1\n\t"
  "v_mfma_f32_16x16x32_bf16 v[48:51], a[96:99], v[136:139], v[48:51]\n\t"
  "v_perm_b32 v64, v178, v178, s68\n\t"
  "ds_read_b128 v[80:83], %[vrd0] offset:25600\n\t"
  "ds_read_b128 v[84:87], %[vrd1] offset:25600\n\t"
  "v_mfma_f32_16x16x32_bf16 v[32:35], a[100:103], v[140:143], v[32:35]\n\t"
  "v_perm_b32 v65, v178, v178, s69\n\t"
  "ds_read_b128 v[88:91], %[vrd0] offset:46080\n\t"
  "v_mfma_f32_16x16x32_bf16 v[36:39], a[104:107], v[140:143], v[36:39]\n\t"
  "v_perm_b32 v66, v179, v179, s68\n\t"
  "ds_read_b128 v[92:95], %[vrd1] offset:46080\n\t"
  "v_mfma_f32_16x16x32_bf16 v[40:43], a[108:111], v[140:143], v[40:43]\n\t"
  "v_perm_b32 v67, v179, v179, s69\n\t"
  "ds_read_b128 v[96:99], v104 offset:5120\n\t"
  "v_mfma_f32_16x16x32_bf16 v[44:47], a[112:115], v[140:143], v[44:47]\n\t"
  "v_perm_b32 v68, v180, v180, s68\n\t"
  "ds_read_b128 v[100:103], v105 offset:5120\n\t"
  "v_mfma_f32_16x16x32_bf16 v[48:51], a[116:119], v[140:143], v[48:51]\n\t"
  "v_perm_b32 v69, v180, v180, s69\n\t"
  "v_mfma_f32_16x16x32_bf16 v[32:35], a[120:123], v[144:147], v[32:35]\n\t"
  "v_perm_b32 v70, v181, v181, s68\n\t"
  "v_mfma_f32_16x16x32_bf16 v[36:39], a[124:127], v[144:147], v[36:39]\n\t"
  "v_perm_b32 v71, v181, v181, s69\n\t"
  "v_mfma_f32_16x16x32_bf16 v[40:43], a[128:131], v[144:147], v[40:43]\n\t"
  "s_waitcnt lgkmcnt(5)\n\t"
  "v_add_f32 v72, v72, v80\n\t"
  "v_add_f32 v73, v73, v81\n\t"
  "v_mfma_f32_16x16x32_bf16 v[44:47], a[132:135], v[144:147], v[44:47]\n\t"
  "v_add_f32 v74, v74, v82\n\t"
  "v_mfma_f32_16x16x32_bf16 v[48:51], a[136:139], v[144:147], v[48:51]\n\t"
  "v_add_f32 v75, v75, v83\n\t"
  "v_mfma_f32_16x16x32_bf16 v[32:35], a[140:143], v[148:151], v[32:35]\n\t"
  "s_waitcnt lgkmcnt(4)\n\t"
  "v_add_f32 v76, v76, v84\n\t"
  "v_mfma_f32_16x16x32_bf16 v[36:39], a[144:147], v[148:151], v[36:39]\n\t"
  "v_add_f32 v77, v77, v85\n\t"
  "v_add_f32 v78, v78, v86\n\t"
  "v_mfma_f32_16x16x32_bf16 v[40:43], a[148:151], v[148:151], v[40:43]\n\t"
  "v_add_f32 v79, v79, v87\n\t"
  "v_mfma_f32_16x16x32_bf16 v[44:47], a[152:155], v[148:151], v[44:47]\n\t"
  "s_waitcnt lgkmcnt(1)\n\t"
  "v_add_f32 v88, v88, v96\n\t"
  "v_mfma_f32_16x16x32_bf16 v[48:51], a[156:159], v[148:151], v[48:51]\n\t"
  "v_add_f32 v89, v89, v97\n\t"
  "v_mfma_f32_16x16x32_bf16 v[32:35], a[160:163], v[152:155], v[32:35]\n\t"
  "v_add_f32 v90, v90, v98\n\t"
  "v_add_f32 v91, v91, v99\n\t"
  "v_mfma_f32_16x16x32_bf16 v[36:39], a[164:167], v[152:155], v[36:39]\n\t"
  "s_waitcnt lgkmcnt(0)\n\t"
  "v_add_f32 v92, v92, v100\n\t"
  "v_mfma_f32_16x16x32_bf16 v[40:43], a[168:171], v[152:155], v[40:43]\n\t"
  "v_add_f32 v93, v93, v101\n\t"
  "v_mfma_f32_16x16x32_bf16 v[44:47], a[172:175], v[152:155], v[44:47]\n\t"
  "v_add_f32 v94, v94, v102\n\t"
  "v_mfma_f32_16x16x32_bf16 v[48:51], a[176:179], v[152:155], v[48:51]\n\t"
  "v_add_f32 v95, v95, v103\n\t"
  "v_add_f32 v72, v72, v88\n\t"
  "v_mfma_f32_16x16x32_bf16 v[32:35], a[180:183], v[156:159], v[32:35]\n\t"
  "v_add_f32 v73, v73, v89\n\t"
  "v_mfma_f32_16x16x32_bf16 v[36:39], a[184:187], v[156:159], v[36:39]\n\t"
  "v_add_f32 v74, v74, v90\n\t"
  "v_mfma_f32_16x16x32_bf16 v[40:43], a[188:191], v[156:159], v[40:43]\n\t"
  "v_add_f32 v75, v75, v91\n\t"
  "v_mfma_f32_16x16x32_bf16 v[44:47], a[192:195], v[156:159], v[44:47]\n\t"
  "v_add_f32 v76, v76, v92\n\t"
  "v_add_f32 v77, v77, v93\n\t"
  "v_mfma_f32_16x16x32_bf16 v[48:51], a[196:199], v[156:159], v[48:51]\n\t"
  "v_add_f32 v78, v78, v94\n\t"
  "v_mfma_f32_16x16x32_bf16 v[32:35], a[200:203], v[160:163], v[32:35]\n\t"
  "v_add_f32 v79, v79, v95\n\t"
  "v_mfma_f32_16x16x32_bf16 v[36:39], a[204:207], v[160:163], v[36:39]\n\t"
  "v_cvt_pk_bf16_f32 v80, v72, v73\n\t"
  "v_mfma_f32_16x16x32_bf16 v[40:43], a[208:211], v[160:163], v[40:43]\n\t"
  "v_cvt_pk_bf16_f32 v81, v74, v75\n\t"
  "v_cvt_pk_bf16_f32 v82, v76, v77\n\t"
  "v_mfma_f32_16x16x32_bf16 v[44:47], a[212:215], v[160:163], v[44:47]\n\t"
  "v_cvt_pk_bf16_f32 v83, v78, v79\n\t"
  "v_mfma_f32_16x16x32_bf16 v[48:51], a[216:219], v[160:163], v[48:51]\n\t"
  "s_mov_b32 s65, 40960\n\t"
  "v_mfma_f32_16x16x32_bf16 v[32:35], a[220:223], v[164:167], v[32:35]\n\t"
  "buffer_store_dwordx4 v[80:83], %[vout], %[srdo], s65 offen\n\t"
  "v_mfma_f32_16x16x32_bf16 v[36:39], a[224:227], v[164:167], v[36:39]\n\t"
  "v_mfma_f32_16x16x32_bf16 v[40:43], a[228:231], v[164:167], v[40:43]\n\t"
  "v_mfma_f32_16x16x32_bf16 v[44:47], a[232:235], v[164:167], v[44:47]\n\t"
  "v_mfma_f32_16x16x32_bf16 v[48:51], a[236:239], v[164:167], v[48:51]\n\t"
  "s_waitcnt vmcnt(12)\n\t"
  "v_mfma_f32_16x16x32_bf16 v[52:55], a[0:3], v[184:187], v[52:55]\n\t"
  "v_mfma_f32_16x16x32_bf16 v[56:59], a[4:7], v[184:187], v[56:59]\n\t"
  "v_mfma_f32_16x16x32_bf16 v[60:63], a[8:11], v[184:187], v[60:63]\n\t"
  "v_mfma_f32_16x16x32_bf16 v[64:67], a[12:15], v[184:187], v[64:67]\n\t"
  "v_mfma_f32_16x16x32_bf16 v[68:71], a[16:19], v[184:187], v[68:71]\n\t"
  "ds_write_b128 %[vwr], v[32:35] offset:10240\n\t"
  "s_waitcnt vmcnt(11)\n\t"
  "v_mfma_f32_16x16x32_bf16 v[52:55], a[20:23], v[188:191], v[52:55]\n\t"
  "ds_write_b128 %[vwr], v[36:39] offset:11264\n\t"
  "v_mfma_f32_16x16x32_bf16 v[56:59], a[24:27], v[188:191], v[56:59]\n\t"
  "v_mfma_f32_16x16x32_bf16 v[60:63], a[28:31], v[188:191], v[60:63]\n\t"
  "ds_write_b128 %[vwr], v[40:43] offset:12288\n\t"
  "v_mfma_f32_16x16x32_bf16 v[64:67], a[32:35], v[188:191], v[64:67]\n\t"
  "ds_write_b128 %[vwr], v[44:47] offset:13312\n\t"
  "v_mfma_f32_16x16x32_bf16 v[68:71], a[36:39], v[188:191], v[68:71]\n\t"
  "s_waitcnt vmcnt(10)\n\t"
  "v_mfma_f32_16x16x32_bf16 v[52:55], a[40:43], v[192:195], v[52:55]\n\t"
  "ds_write_b128 %[vwr], v[48:51] offset:14336\n\t"
  "v_mfma_f32_16x16x32_bf16 v[56:59], a[44:47], v[192:195], v[56:59]\n\t"
  "v_mfma_f32_16x16x32_bf16 v[60:63], a[48:51], v[192:195], v[60:63]\n\t"
  "v_mfma_f32_16x16x32_bf16 v[64:67], a[52:55], v[192:195], v[64:67]\n\t"
  "v_mfma_f32_16x16x32_bf16 v[68:71], a[56:59], v[192:195], v[68:71]\n\t"
  "s_waitcnt vmcnt(9)\n\t"
  "v_mfma_f32_16x16x32_bf16 v[52:55], a[60:63], v[196:199], v[52:55]\n\t"
  "v_mfma_f32_16x16x32_bf16 v[56:59], a[64:67], v[196:199], v[56:59]\n\t"
  "v_mfma_f32_16x16x32_bf16 v[60:63], a[68:71], v[196:199], v[60:63]\n\t"
  "v_mfma_f32_16x16x32_bf16 v[64:67], a[72:75], v[196:199], v[64:67]\n\t"
  "v_mfma_f32_16x16x32_bf16 v[68:71], a[76:79], v[196:199], v[68:71]\n\t"
  "s_waitcnt vmcnt(8)\n\t"
  "v_mfma_f32_16x16x32_bf16 v[52:55], a[80:83], v[200:203], v[52:55]\n\t"
  "s_waitcnt lgkmcnt(0)\n\t"
  "s_barrier\n\t"
  "v_mfma_f32_16x16x32_bf16 v[56:59], a[84:87], v[200:203], v[56:59]\n\t"
  "v_add_u32 v152, 61440, %[vrd0]\n\t"
  "v_add_u32 v153, 61440, %[vrd1]\n\t"
  "v_mfma_f32_16x16x32_bf16 v[60:63], a[88:91], v[200:203], v[60:63]\n\t"
  "ds_read_b128 v[120:123], %[vrd0] offset:10240\n\t"
  "v_mfma_f32_16x16x32_bf16 v[64:67], a[92:95], v[200:203], v[64:67]\n\t"
  "ds_read_b128 v[124:127], %[vrd1] offset:10240\n\t"
  "v_mfma_f32_16x16x32_bf16 v[68:71], a[96:99], v[200:203], v[68:71]\n\t"
  "ds_read_b128 v[128:131], %[vrd0] offset:30720\n\t"
  "ds_read_b128 v[132:135], %[vrd1] offset:30720\n\t"
  "s_waitcnt vmcnt(7)\n\t"
  "v_mfma_f32_16x16x32_bf16 v[52:55], a[100:103], v[204:207], v[52:55]\n\t"
  "ds_read_b128 v[136:139], %[vrd0] offset:51200\n\t"
  "v_mfma_f32_16x16x32_bf16 v[56:59], a[104:107], v[204:207], v[56:59]\n\t"
  "ds_read_b128 v[140:143], %[vrd1] offset:51200\n\t"
  "v_mfma_f32_16x16x32_bf16 v[60:63], a[108:111], v[204:207], v[60:63]\n\t"
  "ds_read_b128 v[144:147], v152 offset:10240\n\t"
  "v_mfma_f32_16x16x32_bf16 v[64:67], a[112:115], v[204:207], v[64:67]\n\t"
  "ds_read_b128 v[148:151], v153 offset:10240\n\t"
  "v_mfma_f32_16x16x32_bf16 v[68:71], a[116:119], v[204:207], v[68:71]\n\t"
  "s_waitcnt vmcnt(6)\n\t"
  "v_mfma_f32_16x16x32_bf16 v[52:55], a[120:123], v[208:211], v[52:55]\n\t"
  "v_mfma_f32_16x16x32_bf16 v[56:59], a[124:127], v[208:211], v[56:59]\n\t"
  "v_mfma_f32_16x16x32_bf16 v[60:63], a[128:131], v[208:211], v[60:63]\n\t"
  "s_waitcnt lgkmcnt(5)\n\t"
  "v_add_f32 v120, v120, v128\n\t"
  "v_add_f32 v121, v121, v129\n\t"
  "v_mfma_f32_16x16x32_bf16 v[64:67], a[132:135], v[208:211], v[64:67]\n\t"
  "v_add_f32 v122, v122, v130\n\t"
  "v_mfma_f32_16x16x32_bf16 v[68:71], a[136:139], v[208:211], v[68:71]\n\t"
  "v_add_f32 v123, v123, v131\n\t"
  "s_waitcnt vmcnt(5)\n\t"
  "v_mfma_f32_16x16x32_bf16 v[52:55], a[140:143], v[212:215], v[52:55]\n\t"
  "s_waitcnt lgkmcnt(4)\n\t"
  "v_add_f32 v124, v124, v132\n\t"
  "v_mfma_f32_16x16x32_bf16 v[56:59], a[144:147], v[212:215], v[56:59]\n\t"
  "v_add_f32 v125, v125, v133\n\t"
  "v_add_f32 v126, v126, v134\n\t"
  "v_mfma_f32_16x16x32_bf16 v[60:63], a[148:151], v[212:215], v[60:63]\n\t"
  "v_add_f32 v127, v127, v135\n\t"
  "v_mfma_f32_16x16x32_bf16 v[64:67], a[152:155], v[212:215], v[64:67]\n\t"
  "s_waitcnt lgkmcnt(1)\n\t"
  "v_add_f32 v136, v136, v144\n\t"
  "v_mfma_f32_16x16x32_bf16 v[68:71], a[156:159], v[212:215], v[68:71]\n\t"
  "v_add_f32 v137, v137, v145\n\t"
  "s_waitcnt vmcnt(4)\n\t"
  "v_mfma_f32_16x16x32_bf16 v[52:55], a[160:163], v[216:219], v[52:55]\n\t"
  "v_add_f32 v138, v138, v146\n\t"
  "v_add_f32 v139, v139, v147\n\t"
  "v_mfma_f32_16x16x32_bf16 v[56:59], a[164:167], v[216:219], v[56:59]\n\t"
  "s_waitcnt lgkmcnt(0)\n\t"
  "v_add_f32 v140, v140, v148\n\t"
  "v_mfma_f32_16x16x32_bf16 v[60:63], a[168:171], v[216:219], v[60:63]\n\t"
  "v_add_f32 v141, v141, v149\n\t"
  "v_mfma_f32_16x16x32_bf16 v[64:67], a[172:175], v[216:219], v[64:67]\n\t"
  "v_add_f32 v142, v142, v150\n\t"
  "v_mfma_f32_16x16x32_bf16 v[68:71], a[176:179], v[216:219], v[68:71]\n\t"
  "v_add_f32 v143, v143, v151\n\t"
  "v_add_f32 v120, v120, v136\n\t"
  "s_waitcnt vmcnt(3)\n\t"
  "v_mfma_f32_16x16x32_bf16 v[52:55], a[180:183], v[220:223], v[52:55]\n\t"
  "v_add_f32 v121, v121, v137\n\t"
  "v_mfma_f32_16x16x32_bf16 v[56:59], a[184:187], v[220:223], v[56:59]\n\t"
  "v_add_f32 v122, v122, v138\n\t"
  "v_mfma_f32_16x16x32_bf16 v[60:63], a[188:191], v[220:223], v[60:63]\n\t"
  "v_add_f32 v123, v123, v139\n\t"
  "v_mfma_f32_16x16x32_bf16 v[64:67], a[192:195], v[220:223], v[64:67]\n\t"
  "v_add_f32 v124, v124, v140\n\t"
  "v_add_f32 v125, v125, v141\n\t"
  "v_mfma_f32_16x16x32_bf16 v[68:71], a[196:199], v[220:223], v[68:71]\n\t"
  "v_add_f32 v126, v126, v142\n\t"
  "s_waitcnt vmcnt(2)\n\t"
  "v_mfma_f32_16x16x32_bf16 v[52:55], a[200:203], v[224:227], v[52:55]\n\t"
  "v_add_f32 v127, v127, v143\n\t"
  "v_mfma_f32_16x16x32_bf16 v[56:59], a[204:207], v[224:227], v[56:59]\n\t"
  "v_cvt_pk_bf16_f32 v128, v120, v121\n\t"
  "v_mfma_f32_16x16x32_bf16 v[60:63], a[208:211], v[224:227], v[60:63]\n\t"
  "v_cvt_pk_bf16_f32 v129, v122, v123\n\t"
  "v_cvt_pk_bf16_f32 v130, v124, v125\n\t"
  "v_mfma_f32_16x16x32_bf16 v[64:67], a[212:215], v[224:227], v[64:67]\n\t"
  "v_cvt_pk_bf16_f32 v131, v126, v127\n\t"
  "v_mfma_f32_16x16x32_bf16 v[68:71], a[216:219], v[224:227], v[68:71]\n\t"
  "s_mov_b32 s65, 1310720\n\t"
  "s_waitcnt vmcnt(1)\n\t"
  "v_mfma_f32_16x16x32_bf16 v[52:55], a[220:223], v[228:231], v[52:55]\n\t"
  "buffer_store_dwordx4 v[128:131], %[vout], %[srdo], s65 offen\n\t"
  "v_mfma_f32_16x16x32_bf16 v[56:59], a[224:227], v[228:231], v[56:59]\n\t"
  "v_mfma_f32_16x16x32_bf16 v[60:63], a[228:231], v[228:231], v[60:63]\n\t"
  "v_mfma_f32_16x16x32_bf16 v[64:67], a[232:235], v[228:231], v[64:67]\n\t"
  "v_mfma_f32_16x16x32_bf16 v[68:71], a[236:239], v[228:231], v[68:71]\n\t"
  "s_nop 2\n\t"
  "ds_write_b128 %[vwr], v[52:55] offset:15360\n\t"
  "s_nop 0\n\t"
  "ds_write_b128 %[vwr], v[56:59] offset:16384\n\t"
  "ds_write_b128 %[vwr], v[60:63] offset:17408\n\t"
  "ds_write_b128 %[vwr], v[64:67] offset:18432\n\t"
  "ds_write_b128 %[vwr], v[68:71] offset:19456\n\t"
  "s_waitcnt lgkmcnt(0)\n\t"
  "s_barrier\n\t"
  "v_add_u32 v104, 61440, %[vrd0]\n\t"
  "v_add_u32 v105, 61440, %[vrd1]\n\t"
  "ds_read_b128 v[72:75], %[vrd0] offset:15360\n\t"
  "ds_read_b128 v[76:79], %[vrd1] offset:15360\n\t"
  "ds_read_b128 v[80:83], %[vrd0] offset:35840\n\t"
  "ds_read_b128 v[84:87], %[vrd1] offset:35840\n\t"
  "ds_read_b128 v[88:91], %[vrd0] offset:56320\n\t"
  "ds_read_b128 v[92:95], %[vrd1] offset:56320\n\t"
  "ds_read_b128 v[96:99], v104 offset:15360\n\t"
  "ds_read_b128 v[100:103], v105 offset:15360\n\t"
  "s_waitcnt lgkmcnt(5)\n\t"
  "v_pk_add_f32 v[72:73], v[72:73], v[80:81]\n\t"
  "v_pk_add_f32 v[74:75], v[74:75], v[82:83]\n\t"
  "s_waitcnt lgkmcnt(4)\n\t"
  "v_pk_add_f32 v[76:77], v[76:77], v[84:85]\n\t"
  "v_pk_add_f32 v[78:79], v[78:79], v[86:87]\n\t"
  "s_waitcnt lgkmcnt(1)\n\t"
  "v_pk_add_f32 v[88:89], v[88:89], v[96:97]\n\t"
  "v_pk_add_f32 v[90:91], v[90:91], v[98:99]\n\t"
  "s_waitcnt lgkmcnt(0)\n\t"
  "v_pk_add_f32 v[92:93], v[92:93], v[100:101]\n\t"
  "v_pk_add_f32 v[94:95], v[94:95], v[102:103]\n\t"
  "v_pk_add_f32 v[72:73], v[72:73], v[88:89]\n\t"
  "v_pk_add_f32 v[74:75], v[74:75], v[90:91]\n\t"
  "v_pk_add_f32 v[76:77], v[76:77], v[92:93]\n\t"
  "v_pk_add_f32 v[78:79], v[78:79], v[94:95]\n\t"
  "v_cvt_pk_bf16_f32 v80, v72, v73\n\t"
  "v_cvt_pk_bf16_f32 v81, v74, v75\n\t"
  "v_cvt_pk_bf16_f32 v82, v76, v77\n\t"
  "v_cvt_pk_bf16_f32 v83, v78, v79\n\t"
  "s_mov_b32 s65, 1351680\n\t"
  "buffer_store_dwordx4 v[80:83], %[vout], %[srdo], s65 offen\n\t"
  "s_branch Lend%=\n\t"
  "Lst0_slow%=:\n\t"
  "s_nop 7\n\t"
  "s_nop 3\n\t"
  "s_mov_b32 s67, 0x40000\n\t"
  "Lst0_loop%=:\n\t"
  "s_sleep 1\n\t"
  "v_lshrrev_b32 v170, 1, %[vl16]\n\t"
  "v_and_b32 v170, 0x78, v170\n\t"
  "buffer_load_dwordx2 v[128:129], v170, %[srd], %[sST] offen offset:0 sc1\n\t"
  "buffer_load_dwordx2 v[130:131], v170, %[srd], %[sST] offen offset:128 sc1\n\t"
  "buffer_load_dwordx2 v[132:133], v170, %[srd], %[sST] offen offset:256 sc1\n\t"
  "buffer_load_dwordx2 v[134:135], v170, %[srd], %[sST] offen offset:384 sc1\n\t"
  "s_waitcnt vmcnt(0)\n\t"
  "v_min_u32 v171, v128, v130\n\t"
  "v_min3_u32 v171, v171, v132, v134\n\t"
  "v_cmp_ne_u32 vcc, 0, v171\n\t"
  "s_nop 3\n\t"
  "s_cmp_eq_u64 vcc, -1\n\t"
  "s_cbranch_scc1 Lst0_back%=\n\t"
  "s_sub_u32 s67, s67, 1\n\t"
  "s_cmp_lg_u32 s67, 0\n\t"
  "s_cbranch_scc1 Lst0_loop%=\n\t"
  "s_branch Lst0_back%=\n\t"
  "Lst1_slow%=:\n\t"
  "s_nop 7\n\t"
  "s_nop 3\n\t"
  "s_mov_b32 s67, 0x40000\n\t"
  "Lst1_loop%=:\n\t"
  "s_sleep 1\n\t"
  "v_lshrrev_b32 v170, 1, %[vl16]\n\t"
  "v_and_b32 v170, 0x78, v170\n\t"
  "s_add_u32 s66, %[sST], 8192\n\t"
  "buffer_load_dwordx2 v[136:137], v170, %[srd], s66 offen offset:0 sc1\n\t"
  "buffer_load_dwordx2 v[138:139], v170, %[srd], s66 offen offset:128 sc1\n\t"
  "buffer_load_dwordx2 v[140:141], v170, %[srd], s66 offen offset:256 sc1\n\t"
  "buffer_load_dwordx2 v[142:143], v170, %[srd], s66 offen offset:384 sc1\n\t"
  "s_waitcnt vmcnt(0)\n\t"
  "v_min_u32 v171, v136, v138\n\t"
  "v_min3_u32 v171, v171, v140, v142\n\t"
  "v_cmp_ne_u32 vcc, 0, v171\n\t"
  "s_nop 3\n\t"
  "s_cmp_eq_u64 vcc, -1\n\t"
  "s_cbranch_scc1 Lst1_back%=\n\t"
  "s_sub_u32 s67, s67, 1\n\t"
  "s_cmp_lg_u32 s67, 0\n\t"
  "s_cbranch_scc1 Lst1_loop%=\n\t"
  "s_branch Lst1_back%=\n\t"
  "Lfl0_slow%=:\n\t"
  "s_nop 7\n\t"
  "s_nop 3\n\t"
  "s_mov_b32 s67, 0x40000\n\t"
  "Lfl0_loop%=:\n\t"
  "s_sleep 1\n\t"
  "v_lshrrev_b32 v170, 2, %[vl16]\n\t"
  "v_and_b32 v170, 0x3c, v170\n\t"
  "s_lshl_b32 s66, %[w], 6\n\t"
  "s_add_u32 s66, s66, %[sFL]\n\t"
  "buffer_load_dword v171, v170, %[srd], s66 offen offset:0 sc1\n\t"
  "s_waitcnt vmcnt(0)\n\t"
  "v_cmp_ne_u32 vcc, 0, v171\n\t"
  "s_nop 3\n\t"
  "s_cmp_eq_u64 vcc, -1\n\t"
  "s_cbranch_scc1 Lfl0_back%=\n\t"
  "s_sub_u32 s67, s67, 1\n\t"
  "s_cmp_lg_u32 s67, 0\n\t"
  "s_cbranch_scc1 Lfl0_loop%=\n\t"
  "s_branch Lfl0_back%=\n\t"
  "Lfl1_slow%=:\n\t"
  "s_nop 7\n\t"
  "s_nop 3\n\t"
  "s_mov_b32 s67, 0x40000\n\t"
  "Lfl1_loop%=:\n\t"
  "s_sleep 1\n\t"
  "v_lshrrev_b32 v170, 2, %[vl16]\n\t"
  "v_and_b32 v170, 0x3c, v170\n\t"
  "s_lshl_b32 s66, %[w], 6\n\t"
  "s_add_u32 s66, s66, %[sFL]\n\t"
  "buffer_load_dword v171, v170, %[srd], s66 offen offset:256 sc1\n\t"
  "s_waitcnt vmcnt(0)\n\t"
  "v_cmp_ne_u32 vcc, 0, v171\n\t"
  "s_nop 3\n\t"
  "s_cmp_eq_u64 vcc, -1\n\t"
  "s_cbranch_scc1 Lfl1_back%=\n\t"
  "s_sub_u32 s67, s67, 1\n\t"
  "s_cmp_lg_u32 s67, 0\n\t"
  "s_cbranch_scc1 Lfl1_loop%=\n\t"
  "s_branch Lfl1_back%=\n\t"
  "Lfl2_slow%=:\n\t"
  "s_nop 7\n\t"
  "s_nop 3\n\t"
  "s_mov_b32 s67, 0x40000\n\t"
  "Lfl2_loop%=:\n\t"
  "s_sleep 1\n\t"
  "v_lshrrev_b32 v170, 2, %[vl16]\n\t"
  "v_and_b32 v170, 0x3c, v170\n\t"
  "s_lshl_b32 s66, %[w], 6\n\t"
  "s_add_u32 s66, s66, %[sFL]\n\t"
  "buffer_load_dword v171, v170, %[srd], s66 offen offset:512 sc1\n\t"
  "s_waitcnt vmcnt(0)\n\t"
  "v_cmp_ne_u32 vcc, 0, v171\n\t"
  "s_nop 3\n\t"
  "s_cmp_eq_u64 vcc, -1\n\t"
  "s_cbranch_scc1 Lfl2_back%=\n\t"
  "s_sub_u32 s67, s67, 1\n\t"
  "s_cmp_lg_u32 s67, 0\n\t"
  "s_cbranch_scc1 Lfl2_loop%=\n\t"
  "s_branch Lfl2_back%=\n\t"
  "Lfl3_slow%=:\n\t"
  "s_nop 7\n\t"
  "s_nop 3\n\t"
  "s_mov_b32 s67, 0x40000\n\t"
  "Lfl3_loop%=:\n\t"
  "s_sleep 1\n\t"
  "v_lshrrev_b32 v170, 2, %[vl16]\n\t"
  "v_and_b32 v170, 0x3c, v170\n\t"
  "s_lshl_b32 s66, %[w], 6\n\t"
  "s_add_u32 s66, s66, %[sFL]\n\t"
  "buffer_load_dword v171, v170, %[srd], s66 offen offset:768 sc1\n\t"
  "s_waitcnt vmcnt(0)\n\t"
  "v_cmp_ne_u32 vcc, 0, v171\n\t"
  "s_nop 3\n\t"
  "s_cmp_eq_u64 vcc, -1\n\t"
  "s_cbranch_scc1 Lfl3_back%=\n\t"
  "s_sub_u32 s67, s67, 1\n\t"
  "s_cmp_lg_u32 s67, 0\n\t"
  "s_cbranch_scc1 Lfl3_loop%=\n\t"
  "s_branch Lfl3_back%=\n\t"
  "Lend%=:\n\t"
    :
    : [srd] "s"(srd), [srdo] "s"(srdo), [w] "s"(w), [rotg] "s"(0), [rotg2] "s"(0), [rdma] "s"(rdma),
      [sX2] "s"(OFF_X2P + p * 81920u), [sW1] "s"(OFF_W1F + (j * 4 + w) * 61440u), [sW2] "s"(OFF_W2F + (ng * 4 + w) * 61440u),
      [sUB] "s"(OFF_UB + (j * 4 + w) * 384u), [sH2st] "s"(OFF_H2F + (4 * p * 192 + 12 * j + 3 * w) * 256u),
      [sH2ld] "s"(OFF_H2F + ((4 * p + sub) * 48 + 12 * w) * 1024u), [sST] "s"(OFF_ST2 + p * 512u), [sFL] "s"(OFF_FL + p * 1024u),
      [sFLw] "s"(OFF_FL + p * 1024u + (j * 4 + w) * 4u),
      [vtr] "v"(vtr), [vl16] "v"(vl16), [vdma] "v"(vdma), [vh2st] "v"(vh2st), [vstc] "v"(vstc), [vstw] "v"(vstw), [vflw] "v"(vflw),
      [vres0] "v"(vres0), [vres1] "v"(vres1), [vres2] "v"(vres2), [vwr] "v"(vwr), [vrd0] "v"(vrd0), [vrd1] "v"(vrd1), [vout] "v"(vout)
    : "memory", "vcc", "scc",
"a0", "a1", "a2", "a3", "a4", "a5", "a6", "a7", "a8", "a9", "a10", "a11", "a12", "a13", "a14", "a15", "a16", "a17", "a18", "a19", "a20", "a21", "a22", "a23", "a24", "a25", "a26", "a27", "a28", "a29", "a30", "a31", "a32", "a33", "a34", "a35", "a36", "a37", "a38", "a39", "a40", "a41", "a42", "a43", "a44", "a45", "a46", "a47", "a48", "a49", "a50", "a51", "a52", "a53", "a54", "a55", "a56", "a57", "a58", "a59", "a60", "a61", "a62", "a63", "a64", "a65", "a66", "a67", "a68", "a69", "a70", "a71", "a72", "a73", "a74", "a75", "a76", "a77", "a78", "a79", "a80", "a81", "a82", "a83", "a84", "a85", "a86", "a87", "a88", "a89", "a90", "a91", "a92", "a93", "a94", "a95", "a96", "a97", "a98", "a99", "a100", "a101", "a102", "a103", "a104", "a105", "a106", "a107", "a108", "a109", "a110", "a111", "a112", "a113", "a114", "a115", "a116", "a117", "a118", "a119", "a120", "a121", "a122", "a123", "a124", "a125", "a126", "a127", "a128", "a129", "a130", "a131", "a132", "a133", "a134", "a135", "a136", "a137", "a138", "a139", "a140", "a141", "a142", "a143", "a144", "a145", "a146", "a147", "a148", "a149", "a150", "a151", "a152", "a153", "a154", "a155", "a156", "a157", "a158", "a159", "a160", "a161", "a162", "a163", "a164", "a165", "a166", "a167", "a168", "a169", "a170", "a171", "a172", "a173", "a174", "a175", "a176", "a177", "a178", "a179", "a180", "a181", "a182", "a183", "a184", "a185", "a186", "a187", "a188", "a189", "a190", "a191", "a192", "a193", "a194", "a195", "a196", "a197", "a198", "a199", "a200", "a201", "a202", "a203", "a204", "a205", "a206", "a207", "a208", "a209", "a210", "a211", "a212", "a213", "a214", "a215", "a216", "a217", "a218", "a219", "a220", "a221", "a222", "a223", "a224", "a225", "a226", "a227", "a228", "a229", "a230", "a231", "a232", "a233", "a234", "a235", "a236", "a237", "a238", "a239", "s44", "s45", "s46", "s47", "s48", "s49", "s50", "s51", "s52", "s53", "s54", "s55", "s56", "s57", "s58", "s59", "s60", "s61", "s62", "s63", "s64", "s65", "s66", "s67", "s68", "s69", "v32", "v33", "v34", "v35", "v36", "v37", "v38", "v39", "v40", "v41", "v42", "v43", "v44", "v45", "v46", "v47", "v48", "v49", "v50", "v51", "v52", "v53", "v54", "v55", "v56", "v57", "v58", "v59", "v60", "v61", "v62", "v63", "v64", "v65", "v66", "v67", "v68", "v69", "v70", "v71", "v72", "v73", "v74", "v75", "v76", "v77", "v78", "v79", "v80", "v81", "v82", "v83", "v84", "v85", "v86", "v87", "v88", "v89", "v90", "v91", "v92", "v93", "v94", "v95", "v96", "v97", "v98", "v99", "v100", "v101", "v102", "v103", "v104", "v105", "v106", "v107", "v108", "v109", "v110", "v111", "v112", "v113", "v114", "v115", "v116", "v117", "v118", "v119", "v120", "v121", "v122", "v123", "v124", "v125", "v126", "v127", "v128", "v129", "v130", "v131", "v132", "v133", "v134", "v135", "v136", "v137", "v138", "v139", "v140", "v141", "v142", "v143", "v144", "v145", "v146", "v147", "v148", "v149", "v150", "v151", "v152", "v153", "v154", "v155", "v156", "v157", "v158", "v159", "v160", "v161", "v162", "v163", "v164", "v165", "v166", "v167", "v168", "v169", "v170", "v171", "v172", "v173", "v174", "v175", "v176", "v177", "v178", "v179", "v180", "v181", "v182", "v183", "v184", "v185", "v186", "v187", "v188", "v189", "v190", "v191", "v192", "v193", "v194", "v195", "v196", "v197", "v198", "v199", "v200", "v201", "v202", "v203", "v204", "v205", "v206", "v207", "v208", "v209", "v210", "v211", "v212", "v213", "v214", "v215", "v216", "v217", "v218", "v219", "v220", "v221", "v222", "v223", "v224", "v225", "v226", "v227", "v228", "v229", "v230", "v231", "v232", "v233", "v234", "v235", "v236", "v237", "v238", "v239", "v240", "v241", "v242", "v243", "v244", "v245", "v246", "v247", "v248", "v249"
  );
}

extern "C" void kernel_launch(void* const* d_in, const int* in_sizes, int n_in, void* d_out, int out_size,
                              void* d_ws, size_t ws_size, hipStream_t stream) {
  const u16* q = (const u16*)d_in[0];
  const u16* k = (const u16*)d_in[1];
  const u16* v = (const u16*)d_in[2];
  const u16* shk = (const u16*)d_in[3];
  const u16* ssmk = (const u16*)d_in[4];
  const u16* ssmD = (const u16*)d_in[5];
  const u16* wg = (const u16*)d_in[7];
  const u16* wu = (const u16*)d_in[8];
  const u16* wd = (const u16*)d_in[9];
  const u16* c = (const u16*)d_in[10];
  const u16* wmod = (const u16*)d_in[11];
  char* ws = (char*)d_ws;
  k_prep<<<256, 704, 0, stream>>>(q, k, v, shk, c, wmod, ws);
  k_conv<<<256, 256, 0, stream>>>(ssmk, ssmD, wg, wu, wd, ws);
  k_ffn<<<256, 256, 0, stream>>>(ws, (u16*)d_out);
}
```

```cpp
#include <hip/hip_runtime.h>
#include <stdint.h>

typedef unsigned short u16;
typedef unsigned int u32;
typedef unsigned long long u64;
typedef __attribute__((ext_vector_type(8))) __bf16 bf16x8;
typedef __attribute__((ext_vector_type(2))) __bf16 bf16x2;
typedef __attribute__((ext_vector_type(4))) short s16x4;
typedef __attribute__((ext_vector_type(4))) float f32x4;
typedef __attribute__((ext_vector_type(2))) float f32x2;
typedef __attribute__((ext_vector_type(4))) u32 u32x4;
typedef __attribute__((ext_vector_type(2))) u32 u32x2;
#define LAS __attribute__((address_space(3)))
#define GAS __attribute__((address_space(1)))
#define DI __device__ __forceinline__

#define STAMP(id)
#define STAMP_DECL
#define STAMP_OUT(kid)
#define A1_R 4
#define A1_P 8
#define A1_S 2
#define A1_S0 0
#define A1_X 0
#define DBG_W 1
#define DBG_X 1
#define DBG_M 1
constexpr int S_ = 2048, D_ = 640, F_ = 1536;
constexpr unsigned OFF_EP = 0u;
constexpr unsigned OFF_FA1 = 4096u;
constexpr unsigned OFF_FA2 = 24576u;
constexpr unsigned OFF_FA3 = 55296u;
constexpr unsigned OFF_FX = 71680u;
constexpr unsigned OFF_FW1 = 92160u;
constexpr unsigned OFF_FU = 100352u;
constexpr unsigned OFF_FW2 = 108544u;
constexpr unsigned OFF_RT = 112640u;
constexpr unsigned OFF_ST2 = 118784u;
constexpr unsigned OFF_FL = 249856u;
constexpr unsigned OFF_MODP = 774144u;
constexpr unsigned OFF_ST1 = 835584u;
constexpr unsigned OFF_UB = 851968u;
constexpr unsigned OFF_KVT = 950272u;
constexpr unsigned OFF_XT = 3571712u;
constexpr unsigned OFF_X2P = 6193152u;
constexpr unsigned OFF_H2F = 8814592u;
constexpr unsigned OFF_W1F = 15106048u;
constexpr unsigned OFF_W2F = 19038208u;
constexpr unsigned WS_END = 21004288u;
constexpr unsigned TAG_A = 2654435769u;
constexpr unsigned TAG_B = 2246822507u;
constexpr unsigned TAG_C = 2135587861u;
constexpr unsigned TAG_D = 3266489909u;
constexpr unsigned FC_PITCH = 4160u;
constexpr unsigned FC_CH = 16640u;
constexpr unsigned L_FC = 0u;
constexpr unsigned L_KV = 66560u;
constexpr unsigned L_X = 84992u;
constexpr unsigned L_ST = 101376u;
constexpr unsigned L_XR = 117760u;
constexpr unsigned L_UN = 71680u;
constexpr unsigned UN_SZ = 22528u;

constexpr unsigned OOB = 0x80000000u;
DI unsigned rfl(unsigned x) { return __builtin_amdgcn_readfirstlane(x); }
DI float bflo(u32 w) { return __uint_as_float(w << 16); }
DI float bfhi(u32 w) { return __uint_as_float(w & 0xffff0000u); }
DI u32 pack_bf16(float lo, float hi) { bf16x2 v; v[0] = (__bf16)lo; v[1] = (__bf16)hi; return __builtin_bit_cast(u32, v); }
DI float rbf(float x) { __bf16 b = (__bf16)x; return (float)b; }
DI float dot2(u32 a, u32 b, float c) { return __builtin_amdgcn_fdot2_f32_bf16(__builtin_bit_cast(bf16x2, a), __builtin_bit_cast(bf16x2, b), c, false); }
DI f32x4 mfma(u32x4 a, u32x4 b, f32x4 c) { return __builtin_amdgcn_mfma_f32_16x16x32_bf16(__builtin_bit_cast(bf16x8, a), __builtin_bit_cast(bf16x8, b), c, 0, 0, 0); }
DI u32x2 tr_read(const char* p) { return __builtin_bit_cast(u32x2, __builtin_amdgcn_ds_read_tr16_b64_v4i16((LAS s16x4*)p)); }
typedef __amdgpu_buffer_rsrc_t rsrc_t;
DI rsrc_t mkrsrc(const void* p, unsigned bytes) { return __builtin_amdgcn_make_buffer_rsrc((void*)p, 0, bytes, 0x00020000); }
#define BAR() asm volatile("s_waitcnt lgkmcnt(0)\n\ts_barrier" ::: "memory")
#define FENCE() do { asm volatile("" ::: "memory"); __builtin_amdgcn_sched_barrier(0); } while (0)
constexpr int SC1 = 16, NT = 2, W_IDLE_SLEEP = 8;

DI u64 load_tag(const char* ws) {
  u64 epoch;
  asm volatile("s_load_dwordx2 %0, %1, 0\n\ts_waitcnt lgkmcnt(0)" : "=&s"(epoch) : "s"(ws) : "memory");
  const u32 tl = ((u32)epoch ^ TAG_A) * TAG_B, th = ((u32)(epoch >> 32) ^ TAG_C) * TAG_D + tl;
  return ((u64)th << 32) | tl;
}
template <int N, int SL = 2>
DI void poll_flags(rsrc_t srd, unsigned voff, u64 tag) {
  const u32 lo = (u32)tag, hi = (u32)(tag >> 32);
#pragma unroll 1
  for (int spin = 0; spin < (1 << 18); ++spin) {
    u32 bad = 0;
#pragma unroll
    for (int k = 0; k < N; ++k) { const u32x2 f = __builtin_amdgcn_raw_buffer_load_b64(srd, voff + k * 512, 0, SC1); bad |= (f[0] ^ lo) | (f[1] ^ hi); }
    asm volatile("" ::: "memory");
    if (__all(bad == 0)) break;
    __builtin_amdgcn_s_sleep(SL);
  }
  FENCE();
}
DI void put_flag(rsrc_t srd, unsigned off, unsigned lane, u64 tag) {
  __builtin_amdgcn_raw_buffer_store_b128(u32x4{(u32)tag, (u32)(tag >> 32), (u32)tag, (u32)(tag >> 32)}, srd, lane < 8 ? off + lane * 16 : OOB, 0, SC1);
  FENCE();
}
DI float modval(rsrc_t srd, unsigned col) {
  float m[4];
#pragma unroll
  for (int k = 0; k < 4; ++k) m[k] = __uint_as_float(__builtin_amdgcn_raw_buffer_load_b32(srd, col * 4, OFF_MODP + k * 15360, SC1));
  return rbf((m[0] + m[1]) + (m[2] + m[3]));
}

constexpr int RP1 = 400, RP2 = 176, L_WS = 69632;

DI void wprep2_finish(char* smem, rsrc_t srd, unsigned ng, unsigned kq, unsigned w, unsigned lane) {
  const float* sG = (const float*)(smem + L_WS);
  const unsigned g = lane >> 4, q = (lane >> 2) & 3, p = lane & 3, r = lane & 15;
  float gb[5];
#pragma unroll
  for (int i = 0; i < 5; ++i) gb[i] = sG[i < 4 ? 32 * (i >> 1) + 8 * (r >> 2) + 4 * (i & 1) + (r & 3) : 64 + r];
#pragma unroll
  for (int kk = 0; kk < 3; ++kk) {
    const unsigned ks = 3 * w + kk;
#pragma unroll
    for (int i = 0; i < 5; ++i) {
      const unsigned col = i < 4 ? 32 * (i >> 1) + 8 * p + 4 * (i & 1) : 64 + 4 * p;
      const char* a = smem + (32 * ks + 8 * g + q) * RP2 + col * 2;
      const u32x2 lo = tr_read(a), hi = tr_read(a + 4 * RP2);
      const float sc = gb[i];
      __builtin_amdgcn_raw_buffer_store_b128(
          u32x4{pack_bf16(bflo(lo[0]) * sc, bfhi(lo[0]) * sc), pack_bf16(bflo(lo[1]) * sc, bfhi(lo[1]) * sc), pack_bf16(bflo(hi[0]) * sc, bfhi(hi[0]) * sc), pack_bf16(bflo(hi[1]) * sc, bfhi(hi[1]) * sc)},
          srd, OFF_W2F + ((((ng * 4 + kq) * 12 + ks) * 5 + i) * 64 + lane) * 16, 0, SC1);
    }
  }
}

__global__ __launch_bounds__(256, 1) void k_main(const u16* __restrict__ x, const u16* __restrict__ kIn, const u16* __restrict__ vIn,
                                                 const u16* __restrict__ shk, const u16* __restrict__ ssmk, const u16* __restrict__ ssmD,
                                                 const u16* __restrict__ wg, const u16* __restrict__ wu, const u16* __restrict__ wd,
                                                 const u16* __restrict__ cIn, const u16* __restrict__ wmod, char* ws, u16* __restrict__ out) {
  __shared__ __attribute__((aligned(16))) char smem[163840];
  const unsigned b = blockIdx.x, tid = threadIdx.x, w = rfl(tid >> 6), lane = tid & 63;
  const unsigned xcd = b & 7, loc = b >> 3, pl = loc & 3, p = 4 * (xcd >> 1) + pl, j = 8 * (xcd & 1) + (loc >> 2), ng = j & 7, sub = j >> 3;
  asm volatile("" :: "s"(x), "s"(kIn), "s"(vIn), "s"(shk), "s"(ssmk), "s"(ssmD), "s"(wg), "s"(wu), "s"(wd), "s"(cIn), "s"(wmod), "s"(ws), "s"(out));
  STAMP_DECL; STAMP(0);
  const rsrc_t srd = mkrsrc(ws, WS_END);
  const rsrc_t srdo = mkrsrc(out, S_ * D_ * 2);
  const unsigned lb = (unsigned)(size_t)smem;
  const unsigned cb = xcd * 20 + loc;
  const unsigned w1 = xcd * 8 + loc - 20, w2 = xcd * 4 + loc - 28;
  const u32x4 zero4 = {0, 0, 0, 0};
  unsigned wdone = 0;
  u64 tag;
  if (loc < 20) {
    const rsrc_t rk = mkrsrc(kIn, S_ * D_ * 2), rv = mkrsrc(vIn, S_ * D_ * 2), rf = mkrsrc(ssmk, S_ * D_ * 2);
    const unsigned tb = 4 * (cb & 31) + w, d0 = 128 * (cb >> 5) + 2 * lane;
    u32x4 fa[4];
    const unsigned lkv = L_KV + 2048 + w * 9216;
    asm volatile(
  "s_mov_b32 m0, %[lk]\n\t"
  "s_nop 3\n\t"
  "buffer_load_dwordx4 %[vo], %[rk], %[sh] offen nt lds\n\t"
  "s_add_u32 m0, %[lk], 1024\n\t"
  "s_add_u32 s44, %[so], 0\n\t"
  "buffer_load_dwordx4 %[vo], %[rk], s44 offen nt lds\n\t"
  "s_add_u32 m0, %[lk], 5120\n\t"
  "s_nop 0\n\t"
  "buffer_load_dwordx4 %[vo], %[rv], s44 offen nt lds\n\t"
  "s_add_u32 m0, %[lk], 2048\n\t"
  "s_add_u32 s44, %[so], 5120\n\t"
  "buffer_load_dwordx4 %[vo], %[rk], s44 offen nt lds\n\t"
  "s_add_u32 m0, %[lk], 6144\n\t"
  "s_nop 0\n\t"
  "buffer_load_dwordx4 %[vo], %[rv], s44 offen nt lds\n\t"
  "s_add_u32 m0, %[lk], 3072\n\t"
  "s_add_u32 s44, %[so], 10240\n\t"
  "buffer_load_dwordx4 %[vo], %[rk], s44 offen nt lds\n\t"
  "s_add_u32 m0, %[lk], 7168\n\t"
  "s_nop 0\n\t"
  "buffer_load_dwordx4 %[vo], %[rv], s44 offen nt lds\n\t"
  "s_add_u32 m0, %[lk], 4096\n\t"
  "s_add_u32 s44, %[so], 15360\n\t"
  "buffer_load_dwordx4 %[vo], %[rk], s44 offen nt lds\n\t"
  "s_add_u32 m0, %[lk], 8192\n\t"
  "s_nop 0\n\t"
  "buffer_load_dwordx4 %[vo], %[rv], s44 offen nt lds\n\t"
      :: [rk] "s"(rk), [rv] "s"(rv), [lk] "s"(lb + lkv), [so] "s"(tb * 20480u + (cb >> 5) * 256u), [sh] "s"(tb ? tb * 20480u + (cb >> 5) * 256u - 5120u : OOB),
         [vo] "v"((lane >> 4) * 1280 + (lane & 15) * 16) : "memory", "scc", "s44");
    const u32x4 taps = *(const u32x4*)(shk + d0 * 4);
    FENCE();
    __builtin_amdgcn_raw_buffer_store_b128(u32x4{0, 0, 0, 0}, srd, tid < 64 && cb < 128 ? OFF_ST2 + cb * 1024 + tid * 16 : OOB, 0, SC1);
    FENCE();
#pragma unroll
    for (int n = 0; n < 4; ++n) fa[n] = __builtin_amdgcn_raw_buffer_load_b128(rf, lane * 16 + n * 1024, (8 * (cb >> 1) + 2 * w + (cb & 1)) * 4096u, NT);
    u32x4 xr[5];
#pragma unroll
    for (int n = 0; n < 5; ++n) xr[n] = __builtin_amdgcn_raw_buffer_load_b128(mkrsrc(x, S_ * D_ * 2 * DBG_X), (lane >> 3) * 1280 + (lane & 7) * 16 + n * 256, cb * 20480u + (w & 1) * 10240u + (w >> 1) * 128u, NT);
    FENCE();
    STAMP(6);
    tag = load_tag(ws);
    if (w < 2) *(u32x4*)(smem + L_KV + tid * 16) = u32x4{0, 0, 0, 0};
    FENCE();
    const f32x2 w0 = {bflo(taps[0]), bflo(taps[2])}, w1 = {bfhi(taps[0]), bfhi(taps[2])}, w2 = {bflo(taps[1]), bflo(taps[3])}, w3 = {bfhi(taps[1]), bfhi(taps[3])};
    asm volatile("s_waitcnt vmcnt(10)" ::: "memory");
    STAMP(7);
    u32 kk[20], vv[16];
    {
      unsigned la = lb + lkv + lane * 4;
      asm volatile("" : "+v"(la));
      const LAS u32* lp = (const LAS u32*)la;
#pragma unroll
      for (int i = 0; i < 12; ++i) kk[i] = lp[(i + 1) * 64];
#pragma unroll
      for (int i = 0; i < 8; ++i) vv[i] = lp[(20 + i) * 64];
      __builtin_amdgcn_sched_barrier(0);
#pragma unroll
      for (int i = 12; i < 19; ++i) kk[i] = lp[(i + 1) * 64];
#pragma unroll
      for (int i = 8; i < 16; ++i) vv[i] = lp[(20 + i) * 64];
      __builtin_amdgcn_sched_barrier(0);
    }
    f32x2 kf[19];
#pragma unroll
    for (int n = 0; n < 2; ++n) {
      f32x2 kv[8];
#pragma unroll
      for (int i = n ? 11 : 0; i < (n ? 19 : 11); ++i) kf[i] = f32x2{bflo(kk[i]), bfhi(kk[i])};
#pragma unroll
      for (int i = 0; i < 8; ++i) {
        f32x2 ks = kf[8 * n + i] * w3;
        ks = __builtin_elementwise_fma(kf[8 * n + i + 1], w2, ks);
        ks = __builtin_elementwise_fma(kf[8 * n + i + 2], w1, ks);
        ks = __builtin_elementwise_fma(kf[8 * n + i + 3], w0, ks);
        kv[i] = ks * f32x2{bflo(vv[8 * n + i]), bfhi(vv[8 * n + i])};
      }
      u32x4 o0, o1;
#pragma unroll
      for (int t = 0; t < 4; ++t) {
        o0[t] = pack_bf16(kv[7 - 2 * t][0], kv[6 - 2 * t][0]);
        o1[t] = pack_bf16(kv[7 - 2 * t][1], kv[6 - 2 * t][1]);
      }
      const unsigned off = OFF_KVT + (tb * 160 + 2 * (d0 >> 3)) * 128 + n * 64 + (lane & 3) * 16;
      __builtin_amdgcn_raw_buffer_store_b128(o0, srd, off, 0, SC1);
      __builtin_amdgcn_raw_buffer_store_b128(o1, srd, off + 128, 0, SC1);
      FENCE();
    }
    STAMP(1);
#pragma unroll
    for (int n = 0; n < 5; ++n) *(u32x4*)(smem + L_XR + n * 4096 + w * 1024 + lane * 16) = xr[n];
    FENCE();
    asm volatile("s_waitcnt vmcnt(0) lgkmcnt(0)\n\ts_barrier" ::: "memory");
    __builtin_amdgcn_raw_buffer_store_b128(u32x4{(u32)tag, (u32)(tag >> 32), (u32)tag, (u32)(tag >> 32)}, srd, tid < 8 ? OFF_FA1 + cb * 128 + tid * 16 : OOB, 0, SC1);
    FENCE();
    STAMP(2);
    if (w > 0 && cb < 128 * DBG_X) {
      const unsigned g = lane >> 4, q = (lane >> 2) & 3, ch = 16 * (g >> 1) + (lane & 15);
      asm volatile(
  "s_nop 4\n\t"
  "s_cmp_eq_u32 %[w], 3\n\t"
  "s_cbranch_scc1 Lxs%=\n\t"
  "ds_read_b64_tr_b16 v[96:97], %[vtx] offset:0\n\t"
  "ds_read_b64_tr_b16 v[98:99], %[vtx] offset:512\n\t"
  "ds_read_b64_tr_b16 v[100:101], %[vtx] offset:64\n\t"
  "ds_read_b64_tr_b16 v[102:103], %[vtx] offset:576\n\t"
  "ds_read_b64_tr_b16 v[104:105], %[vtx] offset:2048\n\t"
  "ds_read_b64_tr_b16 v[106:107], %[vtx] offset:2560\n\t"
  "ds_read_b64_tr_b16 v[108:109], %[vtx] offset:2112\n\t"
  "ds_read_b64_tr_b16 v[110:111], %[vtx] offset:2624\n\t"
  "ds_read_b64_tr_b16 v[112:113], %[vtx] offset:4096\n\t"
  "ds_read_b64_tr_b16 v[114:115], %[vtx] offset:4608\n\t"
  "ds_read_b64_tr_b16 v[116:117], %[vtx] offset:4160\n\t"
  "ds_read_b64_tr_b16 v[118:119], %[vtx] offset:4672\n\t"
  "ds_read_b64_tr_b16 v[120:121], %[vtx] offset:6144\n\t"
  "ds_read_b64_tr_b16 v[122:123], %[vtx] offset:6656\n\t"
  "ds_read_b64_tr_b16 v[124:125], %[vtx] offset:6208\n\t"
  "ds_read_b64_tr_b16 v[126:127], %[vtx] offset:6720\n\t"
  "s_add_u32 s44, %[sXo], 0\n\t"
  "s_waitcnt lgkmcnt(14)\n\t"
  "buffer_store_dwordx4 v[96:99], %[vxo], %[srd], s44 offen offset:0 sc1\n\t"
  "ds_read_b64_tr_b16 v[128:129], %[vtx] offset:8192\n\t"
  "ds_read_b64_tr_b16 v[130:131], %[vtx] offset:8704\n\t"
  "s_waitcnt lgkmcnt(14)\n\t"
  "buffer_store_dwordx4 v[100:103], %[vxo], %[srd], s44 offen offset:1024 sc1\n\t"
  "ds_read_b64_tr_b16 v[132:133], %[vtx] offset:8256\n\t"
  "ds_read_b64_tr_b16 v[134:135], %[vtx] offset:8768\n\t"
  "s_waitcnt lgkmcnt(14)\n\t"
  "buffer_store_dwordx4 v[104:107], %[vxo], %[srd], s44 offen offset:2048 sc1\n\t"
  "s_waitcnt lgkmcnt(12)\n\t"
  "buffer_store_dwordx4 v[108:111], %[vxo], %[srd], s44 offen offset:3072 sc1\n\t"
  "s_add_u32 s44, %[sXo], 4096\n\t"
  "s_waitcnt lgkmcnt(10)\n\t"
  "buffer_store_dwordx4 v[112:115], %[vxo], %[srd], s44 offen offset:0 sc1\n\t"
  "s_waitcnt lgkmcnt(8)\n\t"
  "buffer_store_dwordx4 v[116:119], %[vxo], %[srd], s44 offen offset:1024 sc1\n\t"
  "s_waitcnt lgkmcnt(6)\n\t"
  "buffer_store_dwordx4 v[120:123], %[vxo], %[srd], s44 offen offset:2048 sc1\n\t"
  "s_waitcnt lgkmcnt(4)\n\t"
  "buffer_store_dwordx4 v[124:127], %[vxo], %[srd], s44 offen offset:3072 sc1\n\t"
  "s_add_u32 s44, %[sXo], 8192\n\t"
  "s_waitcnt lgkmcnt(2)\n\t"
  "buffer_store_dwordx4 v[128:131], %[vxo], %[srd], s44 offen offset:0 sc1\n\t"
  "s_waitcnt lgkmcnt(0)\n\t"
  "buffer_store_dwordx4 v[132:135], %[vxo], %[srd], s44 offen offset:1024 sc1\n\t"
  "s_branch Lxe%=\n\t"
  "Lxs%=:\n\t"
  "ds_read_b128 v[96:99], %[vsx] offset:0\n\t"
  "ds_read_b128 v[100:103], %[vsx] offset:64\n\t"
  "ds_read_b128 v[104:107], %[vsx] offset:2048\n\t"
  "ds_read_b128 v[108:111], %[vsx] offset:2112\n\t"
  "ds_read_b128 v[112:115], %[vsx] offset:4096\n\t"
  "ds_read_b128 v[116:119], %[vsx] offset:4160\n\t"
  "ds_read_b128 v[120:123], %[vsx] offset:6144\n\t"
  "ds_read_b128 v[124:127], %[vsx] offset:6208\n\t"
  "ds_read_b128 v[128:131], %[vsx] offset:8192\n\t"
  "ds_read_b128 v[132:135], %[vsx] offset:8256\n\t"
  "ds_read_b128 v[136:139], %[vsx] offset:10240\n\t"
  "ds_read_b128 v[140:143], %[vsx] offset:10304\n\t"
  "ds_read_b128 v[144:147], %[vsx] offset:12288\n\t"
  "ds_read_b128 v[148:151], %[vsx] offset:12352\n\t"
  "ds_read_b128 v[152:155], %[vsx] offset:14336\n\t"
  "ds_read_b128 v[156:159], %[vsx] offset:14400\n\t"
  "ds_read_b128 v[160:163], %[vsx] offset:16384\n\t"
  "ds_read_b128 v[164:167], %[vsx] offset:16448\n\t"
  "ds_read_b128 v[168:171], %[vsx] offset:18432\n\t"
  "ds_read_b128 v[172:175], %[vsx] offset:18496\n\t"
  "v_mov_b32 v184, 0\n\t"
  "v_mov_b32 v185, 0\n\t"
  "v_mov_b32 v186, 0x3f803f80\n\t"
  "s_waitcnt lgkmcnt(15)\n\t"
  "v_dot2c_f32_bf16 v184, v96, v186\n\t"
  "v_dot2c_f32_bf16 v185, v96, v96\n\t"
  "v_dot2c_f32_bf16 v184, v97, v186\n\t"
  "v_dot2c_f32_bf16 v185, v97, v97\n\t"
  "v_dot2c_f32_bf16 v184, v98, v186\n\t"
  "v_dot2c_f32_bf16 v185, v98, v98\n\t"
  "v_dot2c_f32_bf16 v184, v99, v186\n\t"
  "v_dot2c_f32_bf16 v185, v99, v99\n\t"
  "v_dot2c_f32_bf16 v184, v100, v186\n\t"
  "v_dot2c_f32_bf16 v185, v100, v100\n\t"
  "v_dot2c_f32_bf16 v184, v101, v186\n\t"
  "v_dot2c_f32_bf16 v185, v101, v101\n\t"
  "v_dot2c_f32_bf16 v184, v102, v186\n\t"
  "v_dot2c_f32_bf16 v185, v102, v102\n\t"
  "v_dot2c_f32_bf16 v184, v103, v186\n\t"
  "v_dot2c_f32_bf16 v185, v103, v103\n\t"
  "v_dot2c_f32_bf16 v184, v104, v186\n\t"
  "v_dot2c_f32_bf16 v185, v104, v104\n\t"
  "v_dot2c_f32_bf16 v184, v105, v186\n\t"
  "v_dot2c_f32_bf16 v185, v105, v105\n\t"
  "v_dot2c_f32_bf16 v184, v106, v186\n\t"
  "v_dot2c_f32_bf16 v185, v106, v106\n\t"
  "v_dot2c_f32_bf16 v184, v107, v186\n\t"
  "v_dot2c_f32_bf16 v185, v107, v107\n\t"
  "v_dot2c_f32_bf16 v184, v108, v186\n\t"
  "v_dot2c_f32_bf16 v185, v108, v108\n\t"
  "v_dot2c_f32_bf16 v184, v109, v186\n\t"
  "v_dot2c_f32_bf16 v185, v109, v109\n\t"
  "v_dot2c_f32_bf16 v184, v110, v186\n\t"
  "v_dot2c_f32_bf16 v185, v110, v110\n\t"
  "v_dot2c_f32_bf16 v184, v111, v186\n\t"
  "v_dot2c_f32_bf16 v185, v111, v111\n\t"
  "v_dot2c_f32_bf16 v184, v112, v186\n\t"
  "v_dot2c_f32_bf16 v185, v112, v112\n\t"
  "v_dot2c_f32_bf16 v184, v113, v186\n\t"
  "v_dot2c_f32_bf16 v185, v113, v113\n\t"
  "v_dot2c_f32_bf16 v184, v114, v186\n\t"
  "v_dot2c_f32_bf16 v185, v114, v114\n\t"
  "v_dot2c_f32_bf16 v184, v115, v186\n\t"
  "v_dot2c_f32_bf16 v185, v115, v115\n\t"
  "s_waitcnt lgkmcnt(14)\n\t"
  "v_dot2c_f32_bf16 v184, v116, v186\n\t"
  "v_dot2c_f32_bf16 v185, v116, v116\n\t"
  "v_dot2c_f32_bf16 v184, v117, v186\n\t"
  "v_dot2c_f32_bf16 v185, v117, v117\n\t"
  "v_dot2c_f32_bf16 v184, v118, v186\n\t"
  "v_dot2c_f32_bf16 v185, v118, v118\n\t"
  "v_dot2c_f32_bf16 v184, v119, v186\n\t"
  "v_dot2c_f32_bf16 v185, v119, v119\n\t"
  "s_waitcnt lgkmcnt(13)\n\t"
  "v_dot2c_f32_bf16 v184, v120, v186\n\t"
  "v_dot2c_f32_bf16 v185, v120, v120\n\t"
  "v_dot2c_f32_bf16 v184, v121, v186\n\t"
  "v_dot2c_f32_bf16 v185, v121, v121\n\t"
  "v_dot2c_f32_bf16 v184, v122, v186\n\t"
  "v_dot2c_f32_bf16 v185, v122, v122\n\t"
  "v_dot2c_f32_bf16 v184, v123, v186\n\t"
  "v_dot2c_f32_bf16 v185, v123, v123\n\t"
  "s_waitcnt lgkmcnt(12)\n\t"
  "v_dot2c_f32_bf16 v184, v124, v186\n\t"
  "v_dot2c_f32_bf16 v185, v124, v124\n\t"
  "v_dot2c_f32_bf16 v184, v125, v186\n\t"
  "v_dot2c_f32_bf16 v185, v125, v125\n\t"
  "v_dot2c_f32_bf16 v184, v126, v186\n\t"
  "v_dot2c_f32_bf16 v185, v126, v126\n\t"
  "v_dot2c_f32_bf16 v184, v127, v186\n\t"
  "v_dot2c_f32_bf16 v185, v127, v127\n\t"
  "s_waitcnt lgkmcnt(11)\n\t"
  "v_dot2c_f32_bf16 v184, v128, v186\n\t"
  "v_dot2c_f32_bf16 v185, v128, v128\n\t"
  "v_dot2c_f32_bf16 v184, v129, v186\n\t"
  "v_dot2c_f32_bf16 v185, v129, v129\n\t"
  "v_dot2c_f32_bf16 v184, v130, v186\n\t"
  "v_dot2c_f32_bf16 v185, v130, v130\n\t"
  "v_dot2c_f32_bf16 v184, v131, v186\n\t"
  "v_dot2c_f32_bf16 v185, v131, v131\n\t"
  "s_waitcnt lgkmcnt(10)\n\t"
  "v_dot2c_f32_bf16 v184, v132, v186\n\t"
  "v_dot2c_f32_bf16 v185, v132, v132\n\t"
  "v_dot2c_f32_bf16 v184, v133, v186\n\t"
  "v_dot2c_f32_bf16 v185, v133, v133\n\t"
  "v_dot2c_f32_bf16 v184, v134, v186\n\t"
  "v_dot2c_f32_bf16 v185, v134, v134\n\t"
  "v_dot2c_f32_bf16 v184, v135, v186\n\t"
  "v_dot2c_f32_bf16 v185, v135, v135\n\t"
  "s_waitcnt lgkmcnt(9)\n\t"
  "v_dot2c_f32_bf16 v184, v136, v186\n\t"
  "v_dot2c_f32_bf16 v185, v136, v136\n\t"
  "v_dot2c_f32_bf16 v184, v137, v186\n\t"
  "v_dot2c_f32_bf16 v185, v137, v137\n\t"
  "v_dot2c_f32_bf16 v184, v138, v186\n\t"
  "v_dot2c_f32_bf16 v185, v138, v138\n\t"
  "v_dot2c_f32_bf16 v184, v139, v186\n\t"
  "v_dot2c_f32_bf16 v185, v139, v139\n\t"
  "s_waitcnt lgkmcnt(8)\n\t"
  "v_dot2c_f32_bf16 v184, v140, v186\n\t"
  "v_dot2c_f32_bf16 v185, v140, v140\n\t"
  "v_dot2c_f32_bf16 v184, v141, v186\n\t"
  "v_dot2c_f32_bf16 v185, v141, v141\n\t"
  "v_dot2c_f32_bf16 v184, v142, v186\n\t"
  "v_dot2c_f32_bf16 v185, v142, v142\n\t"
  "v_dot2c_f32_bf16 v184, v143, v186\n\t"
  "v_dot2c_f32_bf16 v185, v143, v143\n\t"
  "s_waitcnt lgkmcnt(7)\n\t"
  "v_dot2c_f32_bf16 v184, v144, v186\n\t"
  "v_dot2c_f32_bf16 v185, v144, v144\n\t"
  "v_dot2c_f32_bf16 v184, v145, v186\n\t"
  "v_dot2c_f32_bf16 v185, v145, v145\n\t"
  "v_dot2c_f32_bf16 v184, v146, v186\n\t"
  "v_dot2c_f32_bf16 v185, v146, v146\n\t"
  "v_dot2c_f32_bf16 v184, v147, v186\n\t"
  "v_dot2c_f32_bf16 v185, v147, v147\n\t"
  "s_waitcnt lgkmcnt(6)\n\t"
  "v_dot2c_f32_bf16 v184, v148, v186\n\t"
  "v_dot2c_f32_bf16 v185, v148, v148\n\t"
  "v_dot2c_f32_bf16 v184, v149, v186\n\t"
  "v_dot2c_f32_bf16 v185, v149, v149\n\t"
  "v_dot2c_f32_bf16 v184, v150, v186\n\t"
  "v_dot2c_f32_bf16 v185, v150, v150\n\t"
  "v_dot2c_f32_bf16 v184, v151, v186\n\t"
  "v_dot2c_f32_bf16 v185, v151, v151\n\t"
  "s_waitcnt lgkmcnt(5)\n\t"
  "v_dot2c_f32_bf16 v184, v152, v186\n\t"
  "v_dot2c_f32_bf16 v185, v152, v152\n\t"
  "v_dot2c_f32_bf16 v184, v153, v186\n\t"
  "v_dot2c_f32_bf16 v185, v153, v153\n\t"
  "v_dot2c_f32_bf16 v184, v154, v186\n\t"
  "v_dot2c_f32_bf16 v185, v154, v154\n\t"
  "v_dot2c_f32_bf16 v184, v155, v186\n\t"
  "v_dot2c_f32_bf16 v185, v155, v155\n\t"
  "s_waitcnt lgkmcnt(4)\n\t"
  "v_dot2c_f32_bf16 v184, v156, v186\n\t"
  "v_dot2c_f32_bf16 v185, v156, v156\n\t"
  "v_dot2c_f32_bf16 v184, v157, v186\n\t"
  "v_dot2c_f32_bf16 v185, v157, v157\n\t"
  "v_dot2c_f32_bf16 v184, v158, v186\n\t"
  "v_dot2c_f32_bf16 v185, v158, v158\n\t"
  "v_dot2c_f32_bf16 v184, v159, v186\n\t"
  "v_dot2c_f32_bf16 v185, v159, v159\n\t"
  "s_waitcnt lgkmcnt(3)\n\t"
  "v_dot2c_f32_bf16 v184, v160, v186\n\t"
  "v_dot2c_f32_bf16 v185, v160, v160\n\t"
  "v_dot2c_f32_bf16 v184, v161, v186\n\t"
  "v_dot2c_f32_bf16 v185, v161, v161\n\t"
  "v_dot2c_f32_bf16 v184, v162, v186\n\t"
  "v_dot2c_f32_bf16 v185, v162, v162\n\t"
  "v_dot2c_f32_bf16 v184, v163, v186\n\t"
  "v_dot2c_f32_bf16 v185, v163, v163\n\t"
  "s_waitcnt lgkmcnt(2)\n\t"
  "v_dot2c_f32_bf16 v184, v164, v186\n\t"
  "v_dot2c_f32_bf16 v185, v164, v164\n\t"
  "v_dot2c_f32_bf16 v184, v165, v186\n\t"
  "v_dot2c_f32_bf16 v185, v165, v165\n\t"
  "v_dot2c_f32_bf16 v184, v166, v186\n\t"
  "v_dot2c_f32_bf16 v185, v166, v166\n\t"
  "v_dot2c_f32_bf16 v184, v167, v186\n\t"
  "v_dot2c_f32_bf16 v185, v167, v167\n\t"
  "s_waitcnt lgkmcnt(1)\n\t"
  "v_dot2c_f32_bf16 v184, v168, v186\n\t"
  "v_dot2c_f32_bf16 v185, v168, v168\n\t"
  "v_dot2c_f32_bf16 v184, v169, v186\n\t"
  "v_dot2c_f32_bf16 v185, v169, v169\n\t"
  "v_dot2c_f32_bf16 v184, v170, v186\n\t"
  "v_dot2c_f32_bf16 v185, v170, v170\n\t"
  "v_dot2c_f32_bf16 v184, v171, v186\n\t"
  "v_dot2c_f32_bf16 v185, v171, v171\n\t"
  "s_waitcnt lgkmcnt(0)\n\t"
  "v_dot2c_f32_bf16 v184, v172, v186\n\t"
  "v_dot2c_f32_bf16 v185, v172, v172\n\t"
  "v_dot2c_f32_bf16 v184, v173, v186\n\t"
  "v_dot2c_f32_bf16 v185, v173, v173\n\t"
  "v_dot2c_f32_bf16 v184, v174, v186\n\t"
  "v_dot2c_f32_bf16 v185, v174, v174\n\t"
  "v_dot2c_f32_bf16 v184, v175, v186\n\t"
  "v_dot2c_f32_bf16 v185, v175, v175\n\t"
  "s_nop 1\n\t"
  "v_add_f32_dpp v184, v184, v184 quad_perm:[1,0,3,2] row_mask:0xf bank_mask:0xf\n\t"
  "v_add_f32_dpp v185, v185, v185 quad_perm:[1,0,3,2] row_mask:0xf bank_mask:0xf\n\t"
  "s_nop 0\n\t"
  "v_add_f32_dpp v184, v184, v184 quad_perm:[2,3,0,1] row_mask:0xf bank_mask:0xf\n\t"
  "v_add_f32_dpp v185, v185, v185 quad_perm:[2,3,0,1] row_mask:0xf bank_mask:0xf\n\t"
  "v_mul_f32 v184, 0xbacccccd, v184\n\t"
  "v_mul_f32 v185, 0x3acccccd, v185\n\t"
  "v_fma_f32 v185, -v184, v184, v185\n\t"
  "v_max_f32 v185, 0, v185\n\t"
  "v_cvt_pk_bf16_f32 v185, 0, v185\n\t"
  "v_cvt_pk_bf16_f32 v184, 0, v184\n\t"
  "v_add_f32 v185, 0x3727c5ac, v185\n\t"
  "v_cvt_pk_bf16_f32 v185, 0, v185\n\t"
  "v_sqrt_f32 v185, v185\n\t"
  "s_nop 0\n\t"
  "v_cvt_pk_bf16_f32 v185, 0, v185\n\t"
  "v_rcp_f32 v188, v185\n\t"
  "s_nop 0\n\t"
  "v_mul_f32 v189, v184, v188\n\t"
  "buffer_store_dwordx2 v[188:189], %[vso], %[srd], %[sSo] offen sc1\n\t"
  "Lxe%=:\n\t"
  "s_nop 1\n\t"
        :: [srd] "s"(srd), [w] "s"(w), [sSo] "s"(OFF_ST1 + cb * 128u), [sXo] "s"(OFF_XT + cb * 20480u + (w - 1) * 10240u),
           [vtx] "v"(lb + L_XR + (w - 1) * 10240 + (8 * (g & 1) + q) * 128 + (g >> 1) * 32 + (lane & 3) * 8), [vsx] "v"(lb + L_XR + (lane >> 2) * 128 + (lane & 3) * 16),
           [vso] "v"((lane & 3) == 0 ? lane * 2 : OOB), [vxo] "v"((2 * (ch >> 3) + (ch & 1)) * 128 + (((ch >> 1) & 3) + 4 * (g & 1)) * 16)
        : "memory", "vcc", "scc",
"s44", "v96", "v97", "v98", "v99", "v100", "v101", "v102", "v103", "v104", "v105", "v106", "v107", "v108", "v109", "v110", "v111", "v112", "v113", "v114", "v115", "v116", "v117", "v118", "v119", "v120", "v121", "v122", "v123", "v124", "v125", "v126", "v127", "v128", "v129", "v130", "v131", "v132", "v133", "v134", "v135", "v136", "v137", "v138", "v139", "v140", "v141", "v142", "v143", "v144", "v145", "v146", "v147", "v148", "v149", "v150", "v151", "v152", "v153", "v154", "v155", "v156", "v157", "v158", "v159", "v160", "v161", "v162", "v163", "v164", "v165", "v166", "v167", "v168", "v169", "v170", "v171", "v172", "v173", "v174", "v175", "v184", "v185", "v186", "v188", "v189"
      );
    }
    char* fcw = smem + L_FC + w * FC_CH;
    auto put = [&](unsigned u, u32x4 a, u32x2 h) {
      const u32 W[6] = {a[0], a[1], a[2], a[3], h[0], h[1]};
      u32 O[5];
#pragma unroll
      for (int i = 0; i < 5; ++i) O[i] = __builtin_amdgcn_alignbit(W[i + 1], W[i], 16);
      *(u32x4*)(fcw + 0 * FC_PITCH + u * 16) = a;
      *(u32x4*)(fcw + 1 * FC_PITCH + u * 16) = u32x4{O[0], O[1], O[2], O[3]};
      *(u32x4*)(fcw + 2 * FC_PITCH + u * 16) = u32x4{W[1], W[2], W[3], W[4]};
      *(u32x4*)(fcw + 3 * FC_PITCH + u * 16) = u32x4{O[1], O[2], O[3], O[4]};
    };
#pragma unroll
    for (int n = 0; n < 4; ++n) {
      u32x2 h;
#pragma unroll
      for (int e = 0; e < 2; ++e) h[e] = __builtin_amdgcn_update_dpp(n < 3 ? rfl(fa[n < 3 ? n + 1 : n][e]) : 0u, fa[n][e], 0x130, 0xf, 0xf, false);
      put(lane + 64 * n + 2, fa[n], h);
    }
    if (lane < 2) put(1 - lane, u32x4{0, 0, 0, 0}, lane == 0 ? u32x2{fa[0][0], fa[0][1]} : u32x2{0, 0});
    FENCE();
    STAMP(5);
    poll_flags<1, A1_S>(srd, OFF_FA1 + ((cb & ~31u) + 8 * w + (lane & 7)) * 128, tag);
    STAMP(3);
  } else {
    const bool is1 = loc < 28;
    const unsigned ui = is1 ? 128 + 64 * w + w1 : 4 * w2 + w, gi = ui >> 2;
    const unsigned kq = ui & 3, cgp = gi < 30 || gi >= 50 ? gi : gi < 40 ? gi + 10 : gi - 10, lu = lb + L_UN + w * UN_SZ;
    const unsigned g = lane >> 4, q = (lane >> 2) & 3, p4 = lane & 3;
    if (ui < 240)
      asm volatile(
  "s_nop 4\n\t"
  "s_add_u32 m0, %[lu], 20480\n\t"
  "s_nop 0\n\t"
  "buffer_load_dwordx4 %[vl16], %[rc], %[sC] offen lds\n\t"
  "s_add_u32 m0, %[lu], 0\n\t"
  "s_add_u32 s44, %[sMod], 0\n\t"
  "buffer_load_dwordx4 %[vm], %[rm], s44 offen nt lds\n\t"
  "s_add_u32 m0, %[lu], 1024\n\t"
  "s_add_u32 s44, %[sMod], 61440\n\t"
  "buffer_load_dwordx4 %[vm], %[rm], s44 offen nt lds\n\t"
  "s_add_u32 m0, %[lu], 2048\n\t"
  "s_add_u32 s44, %[sMod], 122880\n\t"
  "buffer_load_dwordx4 %[vm], %[rm], s44 offen nt lds\n\t"
  "s_add_u32 m0, %[lu], 3072\n\t"
  "s_add_u32 s44, %[sMod], 184320\n\t"
  "buffer_load_dwordx4 %[vm], %[rm], s44 offen nt lds\n\t"
  "s_add_u32 m0, %[lu], 4096\n\t"
  "s_add_u32 s44, %[sMod], 245760\n\t"
  "buffer_load_dwordx4 %[vm], %[rm], s44 offen nt lds\n\t"
  "s_add_u32 m0, %[lu], 5120\n\t"
  "s_add_u32 s44, %[sMod], 307200\n\t"
  "buffer_load_dwordx4 %[vm], %[rm], s44 offen nt lds\n\t"
  "s_add_u32 m0, %[lu], 6144\n\t"
  "s_add_u32 s44, %[sMod], 368640\n\t"
  "buffer_load_dwordx4 %[vm], %[rm], s44 offen nt lds\n\t"
  "s_add_u32 m0, %[lu], 7168\n\t"
  "s_add_u32 s44, %[sMod], 430080\n\t"
  "buffer_load_dwordx4 %[vm], %[rm], s44 offen nt lds\n\t"
  "s_add_u32 m0, %[lu], 8192\n\t"
  "s_add_u32 s44, %[sMod], 491520\n\t"
  "buffer_load_dwordx4 %[vm], %[rm], s44 offen nt lds\n\t"
  "s_add_u32 m0, %[lu], 9216\n\t"
  "s_add_u32 s44, %[sMod], 552960\n\t"
  "buffer_load_dwordx4 %[vm], %[rm], s44 offen nt lds\n\t"
  "s_add_u32 m0, %[lu], 10240\n\t"
  "s_add_u32 s44, %[sMod], 614400\n\t"
  "buffer_load_dwordx4 %[vm], %[rm], s44 offen nt lds\n\t"
  "s_add_u32 m0, %[lu], 11264\n\t"
  "s_add_u32 s44, %[sMod], 675840\n\t"
  "buffer_load_dwordx4 %[vm], %[rm], s44 offen nt lds\n\t"
  "s_add_u32 m0, %[lu], 12288\n\t"
  "s_add_u32 s44, %[sMod], 737280\n\t"
  "buffer_load_dwordx4 %[vm], %[rm], s44 offen nt lds\n\t"
  "s_add_u32 m0, %[lu], 13312\n\t"
  "s_add_u32 s44, %[sMod], 798720\n\t"
  "buffer_load_dwordx4 %[vm], %[rm], s44 offen nt lds\n\t"
  "s_add_u32 m0, %[lu], 14336\n\t"
  "s_add_u32 s44, %[sMod], 860160\n\t"
  "buffer_load_dwordx4 %[vm], %[rm], s44 offen nt lds\n\t"
  "s_add_u32 m0, %[lu], 15360\n\t"
  "s_add_u32 s44, %[sMod], 921600\n\t"
  "buffer_load_dwordx4 %[vm], %[rm], s44 offen nt lds\n\t"
  "s_add_u32 m0, %[lu], 16384\n\t"
  "s_add_u32 s44, %[sMod], 983040\n\t"
  "buffer_load_dwordx4 %[vm], %[rm], s44 offen nt lds\n\t"
  "s_add_u32 m0, %[lu], 17408\n\t"
  "s_add_u32 s44, %[sMod], 1044480\n\t"
  "buffer_load_dwordx4 %[vm], %[rm], s44 offen nt lds\n\t"
  "s_add_u32 m0, %[lu], 18432\n\t"
  "s_add_u32 s44, %[sMod], 1105920\n\t"
  "buffer_load_dwordx4 %[vm], %[rm], s44 offen nt lds\n\t"
  "s_add_u32 m0, %[lu], 19456\n\t"
  "s_add_u32 s44, %[sMod], 1167360\n\t"
  "buffer_load_dwordx4 %[vm], %[rm], s44 offen nt lds\n\t"
        :: [rm] "s"(mkrsrc(wmod, D_ * 3840 * 2 * DBG_M)), [rc] "s"(mkrsrc(cIn, D_ * 2)), [lu] "s"(lu), [sC] "s"(kq * 320u), [sMod] "s"(kq * 160u * 7680u + cgp * 128u),
           [vl16] "v"(lane * 16), [vm] "v"((lane >> 3) * 7680 + (((lane & 7) ^ (((lane >> 4) & 3) << 1)) * 16))
        : "memory", "scc", "s44");
    else __builtin_amdgcn_s_sleep(W_IDLE_SLEEP);
    u32x4 raw[16];
    const unsigned rw = is1 ? 5 * w + lane / 12 : 6 * w + lane / 10, ck = is1 ? lane % 12 : lane % 10;
    if (is1) {
      const rsrc_t rg = mkrsrc(wg, D_ * F_ * 2 * DBG_W), ru = mkrsrc(wu, D_ * F_ * 2 * DBG_W);
      const unsigned vo = lane < 60 ? rw * (F_ * 2) + ck * 16 : OOB, so = (160 * (w1 & 3) * F_ + 96 * (w1 >> 2)) * 2;
#pragma unroll
      for (int n = 0; n < 8; ++n) {
        raw[2 * n] = __builtin_amdgcn_raw_buffer_load_b128(rg, vo, so + n * 20 * F_ * 2, NT);
        raw[2 * n + 1] = __builtin_amdgcn_raw_buffer_load_b128(ru, vo, so + n * 20 * F_ * 2, NT);
      }
    }
    FENCE();
    tag = load_tag(ws);
    STAMP(6);
    if (ui < 240) {
      const unsigned row = lane >> 2;
      asm volatile(
  "s_nop 4\n\t"
  "v_lshlrev_b32 v176, 1, %[vl]\n\t"
  "v_add_u32 v176, %[lu], v176\n\t"
  "v_mov_b32 v96, 0\n\t"
  "v_mov_b32 v97, 0\n\t"
  "v_mov_b32 v98, 0\n\t"
  "s_cmp_lt_u32 %[loc], 28\n\t"
  "s_waitcnt vmcnt(36)\n\t"
  "s_cbranch_scc1 Lu2a%=\n\t"
  "s_waitcnt vmcnt(20)\n\t"
  "Lu2a%=:\n\t"
  "ds_read_u16_d16_hi v96, v176 offset:20480\n\t"
  "ds_read_u16_d16_hi v97, v176 offset:20608\n\t"
  "ds_read_u16_d16_hi v98, v176 offset:20736\n\t"
  "s_waitcnt lgkmcnt(2)\n\t"
  "v_mul_f32 v177, 0xbfb8aa3b, v96\n\t"
  "s_waitcnt lgkmcnt(1)\n\t"
  "v_mul_f32 v178, 0xbfb8aa3b, v97\n\t"
  "s_waitcnt lgkmcnt(0)\n\t"
  "v_mul_f32 v179, 0xbfb8aa3b, v98\n\t"
  "v_exp_f32 v177, v177\n\t"
  "v_exp_f32 v178, v178\n\t"
  "v_exp_f32 v179, v179\n\t"
  "v_add_f32 v177, 1.0, v177\n\t"
  "v_add_f32 v178, 1.0, v178\n\t"
  "v_add_f32 v179, 1.0, v179\n\t"
  "v_rcp_f32 v177, v177\n\t"
  "v_rcp_f32 v178, v178\n\t"
  "v_rcp_f32 v179, v179\n\t"
  "v_mul_f32 v177, v177, v96\n\t"
  "v_mul_f32 v178, v178, v97\n\t"
  "v_mul_f32 v179, v179, v98\n\t"
  "v_cvt_pk_bf16_f32 v177, v177, v177\n\t"
  "v_cvt_pk_bf16_f32 v178, v178, v178\n\t"
  "v_cvt_pk_bf16_f32 v179, v179, v179\n\t"
  "ds_write_b16 v176, v177 offset:21504\n\t"
  "ds_write_b16 v176, v178 offset:21632\n\t"
  "ds_write_b16 v176, v179 offset:21760\n\t"
  "v_lshrrev_b32 v180, 4, %[vl]\n\t"
  "v_lshlrev_b32 v180, 3, v180\n\t"
  "v_add_u32 v180, %[lu], v180\n\t"
  "ds_read_b64 v[100:101], v180 offset:21504\n\t"
  "ds_read_b64 v[102:103], v180 offset:21536\n\t"
  "ds_read_b64 v[104:105], v180 offset:21568\n\t"
  "ds_read_b64 v[106:107], v180 offset:21600\n\t"
  "ds_read_b64 v[108:109], v180 offset:21632\n\t"
  "ds_read_b64 v[110:111], v180 offset:21664\n\t"
  "ds_read_b64 v[112:113], v180 offset:21696\n\t"
  "ds_read_b64 v[114:115], v180 offset:21728\n\t"
  "ds_read_b64 v[116:117], v180 offset:21760\n\t"
  "ds_read_b64 v[118:119], v180 offset:21792\n\t"
  "v_xor_b32 v181, 32, %[vtm]\n\t"
  "v_xor_b32 v182, 64, %[vtm]\n\t"
  "v_xor_b32 v183, 96, %[vtm]\n\t"
  "s_waitcnt vmcnt(16)\n\t"
  "s_cbranch_scc1 Lu2b%=\n\t"
  "s_waitcnt vmcnt(0)\n\t"
  "Lu2b%=:\n\t"
  "ds_read_b64_tr_b16 v[120:121], %[vtm] offset:0\n\t"
  "ds_read_b64_tr_b16 v[122:123], %[vtm] offset:2048\n\t"
  "ds_read_b64_tr_b16 v[124:125], v181 offset:0\n\t"
  "ds_read_b64_tr_b16 v[126:127], v181 offset:2048\n\t"
  "ds_read_b64_tr_b16 v[128:129], v182 offset:0\n\t"
  "ds_read_b64_tr_b16 v[130:131], v182 offset:2048\n\t"
  "ds_read_b64_tr_b16 v[132:133], v183 offset:0\n\t"
  "ds_read_b64_tr_b16 v[134:135], v183 offset:2048\n\t"
  "ds_read_b64_tr_b16 v[136:137], %[vtm] offset:4096\n\t"
  "ds_read_b64_tr_b16 v[138:139], %[vtm] offset:6144\n\t"
  "ds_read_b64_tr_b16 v[140:141], v181 offset:4096\n\t"
  "ds_read_b64_tr_b16 v[142:143], v181 offset:6144\n\t"
  "ds_read_b64_tr_b16 v[144:145], v182 offset:4096\n\t"
  "ds_read_b64_tr_b16 v[146:147], v182 offset:6144\n\t"
  "ds_read_b64_tr_b16 v[148:149], v183 offset:4096\n\t"
  "ds_read_b64_tr_b16 v[150:151], v183 offset:6144\n\t"
  "s_waitcnt lgkmcnt(14)\n\t"
  "v_mfma_f32_16x16x32_bf16 v[152:155], v[100:103], v[120:123], 0\n\t"
  "s_waitcnt lgkmcnt(12)\n\t"
  "v_mfma_f32_16x16x32_bf16 v[156:159], v[100:103], v[124:127], 0\n\t"
  "s_waitcnt lgkmcnt(10)\n\t"
  "v_mfma_f32_16x16x32_bf16 v[160:163], v[100:103], v[128:131], 0\n\t"
  "s_waitcnt lgkmcnt(8)\n\t"
  "v_mfma_f32_16x16x32_bf16 v[164:167], v[100:103], v[132:135], 0\n\t"
  "ds_read_b64_tr_b16 v[120:121], %[vtm] offset:8192\n\t"
  "ds_read_b64_tr_b16 v[122:123], %[vtm] offset:10240\n\t"
  "ds_read_b64_tr_b16 v[124:125], v181 offset:8192\n\t"
  "ds_read_b64_tr_b16 v[126:127], v181 offset:10240\n\t"
  "ds_read_b64_tr_b16 v[128:129], v182 offset:8192\n\t"
  "ds_read_b64_tr_b16 v[130:131], v182 offset:10240\n\t"
  "ds_read_b64_tr_b16 v[132:133], v183 offset:8192\n\t"
  "ds_read_b64_tr_b16 v[134:135], v183 offset:10240\n\t"
  "s_waitcnt lgkmcnt(14)\n\t"
  "v_mfma_f32_16x16x32_bf16 v[152:155], v[104:107], v[136:139], v[152:155]\n\t"
  "s_waitcnt lgkmcnt(12)\n\t"
  "v_mfma_f32_16x16x32_bf16 v[156:159], v[104:107], v[140:143], v[156:159]\n\t"
  "s_waitcnt lgkmcnt(10)\n\t"
  "v_mfma_f32_16x16x32_bf16 v[160:163], v[104:107], v[144:147], v[160:163]\n\t"
  "s_waitcnt lgkmcnt(8)\n\t"
  "v_mfma_f32_16x16x32_bf16 v[164:167], v[104:107], v[148:151], v[164:167]\n\t"
  "ds_read_b64_tr_b16 v[136:137], %[vtm] offset:12288\n\t"
  "ds_read_b64_tr_b16 v[138:139], %[vtm] offset:14336\n\t"
  "ds_read_b64_tr_b16 v[140:141], v181 offset:12288\n\t"
  "ds_read_b64_tr_b16 v[142:143], v181 offset:14336\n\t"
  "ds_read_b64_tr_b16 v[144:145], v182 offset:12288\n\t"
  "ds_read_b64_tr_b16 v[146:147], v182 offset:14336\n\t"
  "ds_read_b64_tr_b16 v[148:149], v183 offset:12288\n\t"
  "ds_read_b64_tr_b16 v[150:151], v183 offset:14336\n\t"
  "s_waitcnt lgkmcnt(14)\n\t"
  "v_mfma_f32_16x16x32_bf16 v[152:155], v[108:111], v[120:123], v[152:155]\n\t"
  "s_waitcnt lgkmcnt(12)\n\t"
  "v_mfma_f32_16x16x32_bf16 v[156:159], v[108:111], v[124:127], v[156:159]\n\t"
  "s_waitcnt lgkmcnt(10)\n\t"
  "v_mfma_f32_16x16x32_bf16 v[160:163], v[108:111], v[128:131], v[160:163]\n\t"
  "s_waitcnt lgkmcnt(8)\n\t"
  "v_mfma_f32_16x16x32_bf16 v[164:167], v[108:111], v[132:135], v[164:167]\n\t"
  "ds_read_b64_tr_b16 v[120:121], %[vtm] offset:16384\n\t"
  "ds_read_b64_tr_b16 v[122:123], %[vtm] offset:18432\n\t"
  "ds_read_b64_tr_b16 v[124:125], v181 offset:16384\n\t"
  "ds_read_b64_tr_b16 v[126:127], v181 offset:18432\n\t"
  "ds_read_b64_tr_b16 v[128:129], v182 offset:16384\n\t"
  "ds_read_b64_tr_b16 v[130:131], v182 offset:18432\n\t"
  "ds_read_b64_tr_b16 v[132:133], v183 offset:16384\n\t"
  "ds_read_b64_tr_b16 v[134:135], v183 offset:18432\n\t"
  "s_waitcnt lgkmcnt(14)\n\t"
  "v_mfma_f32_16x16x32_bf16 v[152:155], v[112:115], v[136:139], v[152:155]\n\t"
  "s_waitcnt lgkmcnt(12)\n\t"
  "v_mfma_f32_16x16x32_bf16 v[156:159], v[112:115], v[140:143], v[156:159]\n\t"
  "s_waitcnt lgkmcnt(10)\n\t"
  "v_mfma_f32_16x16x32_bf16 v[160:163], v[112:115], v[144:147], v[160:163]\n\t"
  "s_waitcnt lgkmcnt(8)\n\t"
  "v_mfma_f32_16x16x32_bf16 v[164:167], v[112:115], v[148:151], v[164:167]\n\t"
  "s_waitcnt lgkmcnt(6)\n\t"
  "v_mfma_f32_16x16x32_bf16 v[152:155], v[116:119], v[120:123], v[152:155]\n\t"
  "s_waitcnt lgkmcnt(4)\n\t"
  "v_mfma_f32_16x16x32_bf16 v[156:159], v[116:119], v[124:127], v[156:159]\n\t"
  "s_waitcnt lgkmcnt(2)\n\t"
  "v_mfma_f32_16x16x32_bf16 v[160:163], v[116:119], v[128:131], v[160:163]\n\t"
  "s_waitcnt lgkmcnt(0)\n\t"
  "v_mfma_f32_16x16x32_bf16 v[164:167], v[116:119], v[132:135], v[164:167]\n\t"
  "v_lshlrev_b32 v176, 2, %[vl]\n\t"
  "s_nop 2\n\t"
  "v_mov_b32_dpp v152, v156 quad_perm:[0,1,2,3] row_mask:0x2 bank_mask:0xf\n\t"
  "s_nop 1\n\t"
  "v_mov_b32_dpp v152, v160 quad_perm:[0,1,2,3] row_mask:0x4 bank_mask:0xf\n\t"
  "s_nop 1\n\t"
  "v_mov_b32_dpp v152, v164 quad_perm:[0,1,2,3] row_mask:0x8 bank_mask:0xf\n\t"
  "buffer_store_dword v152, v176, %[srd], %[sMo] offen sc1\n\t"
  "s_nop 1\n\t"
        :: [srd] "s"(srd), [lu] "s"(lu), [loc] "s"(loc), [sMo] "s"(OFF_MODP + kq * 15360u + cgp * 256u), [vl] "v"(lane), [vtm] "v"(lu + row * 128 + ((row >> 1) & 3) * 32 + (lane & 3) * 8)
        : "memory", "vcc", "scc",
"v96", "v97", "v98", "v100", "v101", "v102", "v103", "v104", "v105", "v106", "v107", "v108", "v109", "v110", "v111", "v112", "v113", "v114", "v115", "v116", "v117", "v118", "v119", "v120", "v121", "v122", "v123", "v124", "v125", "v126", "v127", "v128", "v129", "v130", "v131", "v132", "v133", "v134", "v135", "v136", "v137", "v138", "v139", "v140", "v141", "v142", "v143", "v144", "v145", "v146", "v147", "v148", "v149", "v150", "v151", "v152", "v153", "v154", "v155", "v156", "v157", "v158", "v159", "v160", "v161", "v162", "v163", "v164", "v165", "v166", "v167", "v176", "v177", "v178", "v179", "v180", "v181", "v182", "v183"
      );
    }
    STAMP(1);
    if (is1) {
      if (lane < 60) {
        char* o = smem + rw * RP1 + ck * 32;
#pragma unroll
        for (int n = 0; n < 8; ++n) {
          *(u32x4*)(o + n * 20 * RP1) = u32x4{raw[2 * n][0], raw[2 * n + 1][0], raw[2 * n][1], raw[2 * n + 1][1]};
          *(u32x4*)(o + n * 20 * RP1 + 16) = u32x4{raw[2 * n][2], raw[2 * n + 1][2], raw[2 * n][3], raw[2 * n + 1][3]};
        }
      }
      FENCE();
    }
    asm volatile("s_waitcnt vmcnt(0)" ::: "memory");
    put_flag(srd, ui < 240 ? OFF_FA2 + (kq * 60 + cgp) * 128 : OOB, lane, tag);
    STAMP(2);
    if (is1) {
      BAR();
#pragma unroll
      for (int n = 0; n < 15; ++n) {
        const int ks = n / 3, i = n % 3;
        const char* a = smem + (32 * ks + 4 * g + q) * RP1 + (12 * w + (p4 < 3 ? 4 * p4 + i : 4 * i + 3)) * 8;
        const u32x2 lo = tr_read(a), hi = tr_read(a + 16 * RP1);
        raw[n] = u32x4{lo[0], lo[1], hi[0], hi[1]};
      }
      FENCE();
#pragma unroll
      for (int n = 0; n < 15; ++n)
        __builtin_amdgcn_raw_buffer_store_b128(raw[n], srd, lane * 16 + (n & 3) * 1024, OFF_W1F + (((w1 & ~3u) + w) * 60 + 15 * (w1 & 3) + (n & ~3)) * 1024, SC1);
      FENCE();
      asm volatile("s_waitcnt vmcnt(0)\n\ts_barrier" ::: "memory");
      if (w == 0) put_flag(srd, OFF_FW1 + w1 * 128, lane, tag);
      STAMP(7);
    } else {
      const rsrc_t rd = mkrsrc(wd, D_ * F_ * 2 * DBG_W);
      const unsigned vo = lane < 60 ? rw * (D_ * 2) + ck * 16 : OOB, so = (384 * (w2 & 3) * D_ + 80 * (w2 >> 2)) * 2;
#pragma unroll
      for (int n = 0; n < 16; ++n) raw[n] = __builtin_amdgcn_raw_buffer_load_b128(rd, vo, so + n * 24 * D_ * 2, NT);
      FENCE();
      if (lane < 60) {
#pragma unroll
        for (int n = 0; n < 16; ++n) *(u32x4*)(smem + rw * RP2 + ck * 16 + n * 24 * RP2) = raw[n];
      }
      STAMP(7);
    }
    FENCE();
    if (w == 3) {
      {
        const unsigned c0 = is1 ? ((lane & 16) ? 3 * 640 : 4 * 640) + 160 * (w1 & 3) : 5 * 640 + 80 * (w2 >> 2), span = is1 ? 159 : 79;
        const unsigned col = c0 + min(64 * ((lane >> 2) & 3), span);
        poll_flags<1, 4>(srd, OFF_FA2 + ((lane & 3) * 60 + (col >> 6)) * 128, tag);
      }
      if (is1) {
        float cbv[3], sbv[3];
#pragma unroll
        for (int n = 0; n < 3; ++n) { const unsigned r = 160 * (w1 & 3) + min(lane + 64 * n, 159u); cbv[n] = modval(srd, 4 * 640 + r); sbv[n] = modval(srd, 3 * 640 + r); }
#pragma unroll
        for (int n = 0; n < 3; ++n) {
          const float sc = rbf(1.f + cbv[n]);
          *(u16*)(smem + L_WS + (lane + 64 * n) * 2) = (u16)(__float_as_uint(sc) >> 16);
          *(u16*)(smem + L_WS + 1024 + (lane + 64 * n) * 2) = (u16)(__float_as_uint(sbv[n]) >> 16);
          const unsigned dch = 160 * (w1 & 3) + lane + 64 * n, nn = dch >> 6;
          const bool on = w1 < 4 && lane + 64 * n < 160;
          const u32 rv = __float_as_uint(sc == 0.f ? 0x1p40f : 1.f / sc);
          __builtin_amdgcn_raw_buffer_store_b32(rv, srd, on ? OFF_RT + dch * 4 : OOB, 0, SC1);
          __builtin_amdgcn_raw_buffer_store_b32(rv, srd, on ? OFF_RT + 2560 + (dch & 63) * 48 + (nn < 2 ? nn : nn + 2) * 4 : OOB, 0, SC1);
        }
      } else {
        const unsigned c0 = 5 * 640 + 80 * (w2 >> 2);
        const float g0 = modval(srd, c0 + lane), g1 = modval(srd, c0 + 64 + (lane & 15));
        *(float*)(smem + L_WS + lane * 4) = g0;
        *(float*)(smem + L_WS + 256 + (lane & 15) * 4) = g1;
      }
    }
    STAMP(5);
    BAR();
    STAMP(3);
    if (is1) {
      f32x4 aU[3], aB[3];
#pragma unroll
      for (int i = 0; i < 3; ++i) { aU[i] = f32x4{0, 0, 0, 0}; aB[i] = f32x4{0, 0, 0, 0}; }
#pragma unroll
      for (int ks = 0; ks < 5; ++ks) {
        const char* t = smem + L_WS + (32 * ks + 4 * g) * 2;
        const u32x2 s0 = *(const u32x2*)t, s1 = *(const u32x2*)(t + 32), b0 = *(const u32x2*)(t + 1024), b1 = *(const u32x2*)(t + 1056);
#pragma unroll
        for (int i = 0; i < 3; ++i) {
          aU[i] = mfma(u32x4{s0[0], s0[1], s1[0], s1[1]}, raw[3 * ks + i], aU[i]);
          aB[i] = mfma(u32x4{b0[0], b0[1], b1[0], b1[1]}, raw[3 * ks + i], aB[i]);
        }
      }
      const float u = g == 0 ? aU[0][0] : g == 1 ? aU[1][0] : aU[2][0], bb = g == 0 ? aB[0][0] : g == 1 ? aB[1][0] : aB[2][0];
      const float uh = rbf(u), bh = rbf(bb);
      __builtin_amdgcn_raw_buffer_store_b64(u32x2{pack_bf16(uh, u - uh), pack_bf16(bh, bb - bh)}, srd,
                                            g < 3 ? OFF_UB + ((((w1 & ~3u) + w) * 3 + g) * 64 + (w1 & 3) * 16 + (lane & 15)) * 8 : OOB, 0, SC1);
    } else wprep2_finish(smem, srd, w2 >> 2, w2 & 3, w, lane);
  }
  if (loc < 20) {
    const unsigned d = 8 * (cb >> 1) + 2 * w + (cb & 1);
    const unsigned r = lane & 15, g = lane >> 4;
    const unsigned pp = 25 + r - 8 * g;
    const unsigned va = lb + L_FC + w * FC_CH + (pp & 3) * FC_PITCH + (pp & ~3u) * 2;
    unsigned vb[4];
#pragma unroll
    for (int k = 0; k < 4; ++k) { const unsigned G = 14 - 4 * k + 2 * r + g; vb[k] = lb + L_KV + w * 256 + (G >> 4) * 1024 + (G & 15) * 16; }
    const unsigned gx = w * 256 + (r >> 3) * 1024 + ((2 * r + (g >> 1)) & 15) * 16;
    const unsigned vx = lb + L_X + gx + (g & 1) * 8, vs = lb + L_ST + (16 * r + 4 * g) * 8;
    const unsigned to = 256 * (g & 1) + 16 * r + 8 * (g >> 1);
    const unsigned vo = (to >> 6) * 81920u + (to & 63) * 2;
    const unsigned wh = (lane >> 2) & 3, mcol = (wh < 3 ? wh : 4) * 640 + d;
    const unsigned jd = 8 * g - 9 - r, dv = (u32)ssmD[d] << (16 * (jd & 1));
    const u32x4 vd = {jd >> 1 == 0 ? dv : 0u, jd >> 1 == 1 ? dv : 0u, jd >> 1 == 2 ? dv : 0u, jd >> 1 == 3 ? dv : 0u};
    asm volatile(
  "s_nop 4\n\t"
  "s_lshl_b32 s45, %[w], 12\n\t"
  "s_add_u32 m0, s45, 68608\n\t"
  "s_add_u32 s44, %[sKV], 0\n\t"
  "buffer_load_dwordx4 %[vkv], %[srd], s44 offen sc1 lds\n\t"
  "s_add_u32 m0, s45, 69632\n\t"
  "s_add_u32 s44, %[sKV], 163840\n\t"
  "buffer_load_dwordx4 %[vkv], %[srd], s44 offen sc1 lds\n\t"
  "s_add_u32 m0, s45, 70656\n\t"
  "s_add_u32 s44, %[sKV], 327680\n\t"
  "buffer_load_dwordx4 %[vkv], %[srd], s44 offen sc1 lds\n\t"
  "s_add_u32 m0, s45, 71680\n\t"
  "s_add_u32 s44, %[sKV], 491520\n\t"
  "buffer_load_dwordx4 %[vkv], %[srd], s44 offen sc1 lds\n\t"
  "v_mov_b32 v252, %[tlo]\n\t"
  "v_mov_b32 v253, %[thi]\n\t"
  "v_mov_b32 v254, %[tlo]\n\t"
  "v_mov_b32 v255, %[thi]\n\t"
  "s_waitcnt vmcnt(0) lgkmcnt(0)\n\t"
  "s_barrier\n\t"
  "buffer_store_dwordx4 v[252:255], %[vf3], %[srd], 0 offen sc1\n\t"
  "ds_read_b64 v[64:65], %[va] offset:0\n\t"
  "ds_read_b64 v[66:67], %[va] offset:8\n\t"
  "ds_read_b128 v[124:127], %[vb0] offset:15360\n\t"
  "ds_read_b128 v[120:123], %[vb0] offset:13312\n\t"
  "ds_read_b128 v[116:119], %[vb0] offset:11264\n\t"
  "ds_read_b128 v[112:115], %[vb0] offset:9216\n\t"
  "ds_read_b128 v[108:111], %[vb0] offset:7168\n\t"
  "ds_read_b128 v[104:107], %[vb0] offset:5120\n\t"
  "ds_read_b128 v[100:103], %[vb0] offset:3072\n\t"
  "ds_read_b128 v[96:99], %[vb0] offset:1024\n\t"
  "ds_read_b64 v[68:69], %[va] offset:512\n\t"
  "ds_read_b64 v[70:71], %[va] offset:520\n\t"
  "ds_read_b64 v[72:73], %[va] offset:1024\n\t"
  "ds_read_b64 v[74:75], %[va] offset:1032\n\t"
  "ds_read_b64 v[76:77], %[va] offset:1536\n\t"
  "ds_read_b64 v[78:79], %[va] offset:1544\n\t"
  "ds_read_b64 v[80:81], %[va] offset:2048\n\t"
  "ds_read_b64 v[82:83], %[va] offset:2056\n\t"
  "ds_read_b64 v[84:85], %[va] offset:2560\n\t"
  "ds_read_b64 v[86:87], %[va] offset:2568\n\t"
  "ds_read_b64 v[88:89], %[va] offset:3072\n\t"
  "ds_read_b64 v[90:91], %[va] offset:3080\n\t"
  "ds_read_b64 v[92:93], %[va] offset:3584\n\t"
  "ds_read_b64 v[94:95], %[va] offset:3592\n\t"
  "s_waitcnt lgkmcnt(15)\n\t"
  "v_mfma_f32_16x16x32_bf16 v[60:63], v[64:67], v[124:127], 0\n\t"
  "ds_read_b128 v[156:159], %[vb1] offset:15360\n\t"
  "v_mfma_f32_16x16x32_bf16 v[56:59], v[64:67], v[120:123], 0\n\t"
  "v_mfma_f32_16x16x32_bf16 v[52:55], v[64:67], v[116:119], 0\n\t"
  "v_mfma_f32_16x16x32_bf16 v[48:51], v[64:67], v[112:115], 0\n\t"
  "v_mfma_f32_16x16x32_bf16 v[44:47], v[64:67], v[108:111], 0\n\t"
  "ds_read_b128 v[152:155], %[vb1] offset:13312\n\t"
  "v_mfma_f32_16x16x32_bf16 v[40:43], v[64:67], v[104:107], 0\n\t"
  "v_mfma_f32_16x16x32_bf16 v[36:39], v[64:67], v[100:103], 0\n\t"
  "s_waitcnt lgkmcnt(15)\n\t"
  "v_mfma_f32_16x16x32_bf16 v[32:35], v[64:67], v[96:99], 0\n\t"
  "v_mfma_f32_16x16x32_bf16 v[60:63], %[vd], v[124:127], v[60:63]\n\t"
  "ds_read_b128 v[148:151], %[vb1] offset:11264\n\t"
  "v_mfma_f32_16x16x32_bf16 v[56:59], %[vd], v[120:123], v[56:59]\n\t"
  "v_mfma_f32_16x16x32_bf16 v[52:55], %[vd], v[116:119], v[52:55]\n\t"
  "v_mfma_f32_16x16x32_bf16 v[48:51], %[vd], v[112:115], v[48:51]\n\t"
  "v_mfma_f32_16x16x32_bf16 v[44:47], %[vd], v[108:111], v[44:47]\n\t"
  "ds_read_b128 v[144:147], %[vb1] offset:9216\n\t"
  "v_mfma_f32_16x16x32_bf16 v[40:43], %[vd], v[104:107], v[40:43]\n\t"
  "v_mfma_f32_16x16x32_bf16 v[36:39], %[vd], v[100:103], v[36:39]\n\t"
  "v_mfma_f32_16x16x32_bf16 v[32:35], %[vd], v[96:99], v[32:35]\n\t"
  "ds_read_b64 v[64:65], %[va] offset:64\n\t"
  "ds_read_b64 v[66:67], %[va] offset:72\n\t"
  "s_waitcnt lgkmcnt(15)\n\t"
  "v_mfma_f32_16x16x32_bf16 v[60:63], v[68:71], v[120:123], v[60:63]\n\t"
  "ds_read_b128 v[140:143], %[vb1] offset:7168\n\t"
  "v_mfma_f32_16x16x32_bf16 v[56:59], v[68:71], v[116:119], v[56:59]\n\t"
  "v_mfma_f32_16x16x32_bf16 v[52:55], v[68:71], v[112:115], v[52:55]\n\t"
  "v_mfma_f32_16x16x32_bf16 v[48:51], v[68:71], v[108:111], v[48:51]\n\t"
  "v_mfma_f32_16x16x32_bf16 v[44:47], v[68:71], v[104:107], v[44:47]\n\t"
  "ds_read_b128 v[136:139], %[vb1] offset:5120\n\t"
  "v_mfma_f32_16x16x32_bf16 v[40:43], v[68:71], v[100:103], v[40:43]\n\t"
  "v_mfma_f32_16x16x32_bf16 v[36:39], v[68:71], v[96:99], v[36:39]\n\t"
  "ds_read_b64 v[68:69], %[va] offset:576\n\t"
  "ds_read_b64 v[70:71], %[va] offset:584\n\t"
  "v_mfma_f32_16x16x32_bf16 v[60:63], v[72:75], v[116:119], v[60:63]\n\t"
  "v_mfma_f32_16x16x32_bf16 v[56:59], v[72:75], v[112:115], v[56:59]\n\t"
  "ds_read_b128 v[132:135], %[vb1] offset:3072\n\t"
  "v_mfma_f32_16x16x32_bf16 v[52:55], v[72:75], v[108:111], v[52:55]\n\t"
  "v_mfma_f32_16x16x32_bf16 v[48:51], v[72:75], v[104:107], v[48:51]\n\t"
  "v_mfma_f32_16x16x32_bf16 v[44:47], v[72:75], v[100:103], v[44:47]\n\t"
  "v_mfma_f32_16x16x32_bf16 v[40:43], v[72:75], v[96:99], v[40:43]\n\t"
  "ds_read_b64 v[72:73], %[va] offset:1088\n\t"
  "ds_read_b128 v[128:131], %[vb1] offset:1024\n\t"
  "s_waitcnt lgkmcnt(15)\n\t"
  "v_mfma_f32_16x16x32_bf16 v[60:63], v[76:79], v[112:115], v[60:63]\n\t"
  "ds_read_b64 v[74:75], %[va] offset:1096\n\t"
  "v_mfma_f32_16x16x32_bf16 v[56:59], v[76:79], v[108:111], v[56:59]\n\t"
  "v_mfma_f32_16x16x32_bf16 v[52:55], v[76:79], v[104:107], v[52:55]\n\t"
  "v_mfma_f32_16x16x32_bf16 v[48:51], v[76:79], v[100:103], v[48:51]\n\t"
  "v_mfma_f32_16x16x32_bf16 v[44:47], v[76:79], v[96:99], v[44:47]\n\t"
  "ds_read_b64 v[76:77], %[va] offset:1600\n\t"
  "ds_read_b64 v[78:79], %[va] offset:1608\n\t"
  "v_mfma_f32_16x16x32_bf16 v[60:63], v[80:83], v[108:111], v[60:63]\n\t"
  "v_mfma_f32_16x16x32_bf16 v[56:59], v[80:83], v[104:107], v[56:59]\n\t"
  "v_mfma_f32_16x16x32_bf16 v[52:55], v[80:83], v[100:103], v[52:55]\n\t"
  "v_mfma_f32_16x16x32_bf16 v[48:51], v[80:83], v[96:99], v[48:51]\n\t"
  "ds_read_b64 v[80:81], %[va] offset:2112\n\t"
  "ds_read_b64 v[82:83], %[va] offset:2120\n\t"
  "v_mfma_f32_16x16x32_bf16 v[60:63], v[84:87], v[104:107], v[60:63]\n\t"
  "v_mfma_f32_16x16x32_bf16 v[56:59], v[84:87], v[100:103], v[56:59]\n\t"
  "v_mfma_f32_16x16x32_bf16 v[52:55], v[84:87], v[96:99], v[52:55]\n\t"
  "ds_read_b64 v[84:85], %[va] offset:2624\n\t"
  "ds_read_b64 v[86:87], %[va] offset:2632\n\t"
  "v_mfma_f32_16x16x32_bf16 v[60:63], v[88:91], v[100:103], v[60:63]\n\t"
  "v_mfma_f32_16x16x32_bf16 v[56:59], v[88:91], v[96:99], v[56:59]\n\t"
  "ds_read_b64 v[88:89], %[va] offset:3136\n\t"
  "ds_read_b64 v[90:91], %[va] offset:3144\n\t"
  "s_waitcnt lgkmcnt(15)\n\t"
  "v_mfma_f32_16x16x32_bf16 v[60:63], v[92:95], v[96:99], v[60:63]\n\t"
  "s_waitcnt lgkmcnt(9)\n\t"
  "ds_read_b64 v[92:93], %[va] offset:3648\n\t"
  "v_mfma_f32_16x16x32_bf16 v[60:63], v[64:67], v[156:159], v[60:63]\n\t"
  "ds_read_b128 v[124:127], %[vb2] offset:15360\n\t"
  "ds_read_b64 v[94:95], %[va] offset:3656\n\t"
  "v_mfma_f32_16x16x32_bf16 v[56:59], v[64:67], v[152:155], v[56:59]\n\t"
  "v_mfma_f32_16x16x32_bf16 v[52:55], v[64:67], v[148:151], v[52:55]\n\t"
  "v_mfma_f32_16x16x32_bf16 v[48:51], v[64:67], v[144:147], v[48:51]\n\t"
  "ds_read_b128 v[120:123], %[vb2] offset:13312\n\t"
  "v_mfma_f32_16x16x32_bf16 v[44:47], v[64:67], v[140:143], v[44:47]\n\t"
  "v_mfma_f32_16x16x32_bf16 v[40:43], v[64:67], v[136:139], v[40:43]\n\t"
  "v_mfma_f32_16x16x32_bf16 v[36:39], v[64:67], v[132:135], v[36:39]\n\t"
  "ds_read_b128 v[116:119], %[vb2] offset:11264\n\t"
  "buffer_load_dwordx2 v[162:163], %[vf2], %[srd], 0 offen sc1\n\t"
  "v_mfma_f32_16x16x32_bf16 v[32:35], v[64:67], v[128:131], v[32:35]\n\t"
  "ds_read_b64 v[64:65], %[va] offset:128\n\t"
  "ds_read_b64 v[66:67], %[va] offset:136\n\t"
  "v_mfma_f32_16x16x32_bf16 v[60:63], v[68:71], v[152:155], v[60:63]\n\t"
  "v_mfma_f32_16x16x32_bf16 v[56:59], v[68:71], v[148:151], v[56:59]\n\t"
  "ds_read_b128 v[112:115], %[vb2] offset:9216\n\t"
  "v_mfma_f32_16x16x32_bf16 v[52:55], v[68:71], v[144:147], v[52:55]\n\t"
  "v_mfma_f32_16x16x32_bf16 v[48:51], v[68:71], v[140:143], v[48:51]\n\t"
  "v_mfma_f32_16x16x32_bf16 v[44:47], v[68:71], v[136:139], v[44:47]\n\t"
  "ds_read_b128 v[108:111], %[vb2] offset:7168\n\t"
  "v_mfma_f32_16x16x32_bf16 v[40:43], v[68:71], v[132:135], v[40:43]\n\t"
  "v_mfma_f32_16x16x32_bf16 v[36:39], v[68:71], v[128:131], v[36:39]\n\t"
  "s_waitcnt lgkmcnt(11)\n\t"
  "ds_read_b64 v[68:69], %[va] offset:640\n\t"
  "v_mfma_f32_16x16x32_bf16 v[60:63], v[72:75], v[148:151], v[60:63]\n\t"
  "ds_read_b128 v[104:107], %[vb2] offset:5120\n\t"
  "ds_read_b64 v[70:71], %[va] offset:648\n\t"
  "v_mfma_f32_16x16x32_bf16 v[56:59], v[72:75], v[144:147], v[56:59]\n\t"
  "v_mfma_f32_16x16x32_bf16 v[52:55], v[72:75], v[140:143], v[52:55]\n\t"
  "v_mfma_f32_16x16x32_bf16 v[48:51], v[72:75], v[136:139], v[48:51]\n\t"
  "ds_read_b128 v[100:103], %[vb2] offset:3072\n\t"
  "v_mfma_f32_16x16x32_bf16 v[44:47], v[72:75], v[132:135], v[44:47]\n\t"
  "v_mfma_f32_16x16x32_bf16 v[40:43], v[72:75], v[128:131], v[40:43]\n\t"
  "ds_read_b64 v[72:73], %[va] offset:1152\n\t"
  "ds_read_b64 v[74:75], %[va] offset:1160\n\t"
  "v_mfma_f32_16x16x32_bf16 v[60:63], v[76:79], v[144:147], v[60:63]\n\t"
  "ds_read_b128 v[96:99], %[vb2] offset:1024\n\t"
  "v_mfma_f32_16x16x32_bf16 v[56:59], v[76:79], v[140:143], v[56:59]\n\t"
  "v_mfma_f32_16x16x32_bf16 v[52:55], v[76:79], v[136:139], v[52:55]\n\t"
  "v_mfma_f32_16x16x32_bf16 v[48:51], v[76:79], v[132:135], v[48:51]\n\t"
  "v_mfma_f32_16x16x32_bf16 v[44:47], v[76:79], v[128:131], v[44:47]\n\t"
  "ds_read_b64 v[76:77], %[va] offset:1664\n\t"
  "ds_read_b64 v[78:79], %[va] offset:1672\n\t"
  "v_mfma_f32_16x16x32_bf16 v[60:63], v[80:83], v[140:143], v[60:63]\n\t"
  "v_mfma_f32_16x16x32_bf16 v[56:59], v[80:83], v[136:139], v[56:59]\n\t"
  "v_mfma_f32_16x16x32_bf16 v[52:55], v[80:83], v[132:135], v[52:55]\n\t"
  "v_mfma_f32_16x16x32_bf16 v[48:51], v[80:83], v[128:131], v[48:51]\n\t"
  "ds_read_b64 v[80:81], %[va] offset:2176\n\t"
  "ds_read_b64 v[82:83], %[va] offset:2184\n\t"
  "v_mfma_f32_16x16x32_bf16 v[60:63], v[84:87], v[136:139], v[60:63]\n\t"
  "v_mfma_f32_16x16x32_bf16 v[56:59], v[84:87], v[132:135], v[56:59]\n\t"
  "v_mfma_f32_16x16x32_bf16 v[52:55], v[84:87], v[128:131], v[52:55]\n\t"
  "s_waitcnt lgkmcnt(15)\n\t"
  "ds_read_b64 v[84:85], %[va] offset:2688\n\t"
  "v_mfma_f32_16x16x32_bf16 v[60:63], v[88:91], v[132:135], v[60:63]\n\t"
  "ds_read_b64 v[86:87], %[va] offset:2696\n\t"
  "v_mfma_f32_16x16x32_bf16 v[56:59], v[88:91], v[128:131], v[56:59]\n\t"
  "ds_read_b64 v[88:89], %[va] offset:3200\n\t"
  "ds_read_b64 v[90:91], %[va] offset:3208\n\t"
  "v_mfma_f32_16x16x32_bf16 v[60:63], v[92:95], v[128:131], v[60:63]\n\t"
  "s_waitcnt lgkmcnt(8)\n\t"
  "ds_read_b64 v[92:93], %[va] offset:3712\n\t"
  "v_mfma_f32_16x16x32_bf16 v[60:63], v[64:67], v[124:127], v[60:63]\n\t"
  "ds_read_b128 v[156:159], %[vb3] offset:15360\n\t"
  "ds_read_b64 v[94:95], %[va] offset:3720\n\t"
  "v_mfma_f32_16x16x32_bf16 v[56:59], v[64:67], v[120:123], v[56:59]\n\t"
  "v_mfma_f32_16x16x32_bf16 v[52:55], v[64:67], v[116:119], v[52:55]\n\t"
  "v_mfma_f32_16x16x32_bf16 v[48:51], v[64:67], v[112:115], v[48:51]\n\t"
  "ds_read_b128 v[152:155], %[vb3] offset:13312\n\t"
  "v_mfma_f32_16x16x32_bf16 v[44:47], v[64:67], v[108:111], v[44:47]\n\t"
  "v_mfma_f32_16x16x32_bf16 v[40:43], v[64:67], v[104:107], v[40:43]\n\t"
  "v_mfma_f32_16x16x32_bf16 v[36:39], v[64:67], v[100:103], v[36:39]\n\t"
  "ds_read_b128 v[148:151], %[vb3] offset:11264\n\t"
  "v_mfma_f32_16x16x32_bf16 v[32:35], v[64:67], v[96:99], v[32:35]\n\t"
  "ds_read_b64 v[64:65], %[va] offset:192\n\t"
  "ds_read_b64 v[66:67], %[va] offset:200\n\t"
  "v_mfma_f32_16x16x32_bf16 v[60:63], v[68:71], v[120:123], v[60:63]\n\t"
  "v_mfma_f32_16x16x32_bf16 v[56:59], v[68:71], v[116:119], v[56:59]\n\t"
  "ds_read_b128 v[144:147], %[vb3] offset:9216\n\t"
  "v_mfma_f32_16x16x32_bf16 v[52:55], v[68:71], v[112:115], v[52:55]\n\t"
  "v_mfma_f32_16x16x32_bf16 v[48:51], v[68:71], v[108:111], v[48:51]\n\t"
  "v_mfma_f32_16x16x32_bf16 v[44:47], v[68:71], v[104:107], v[44:47]\n\t"
  "ds_read_b128 v[140:143], %[vb3] offset:7168\n\t"
  "v_mfma_f32_16x16x32_bf16 v[40:43], v[68:71], v[100:103], v[40:43]\n\t"
  "v_mfma_f32_16x16x32_bf16 v[36:39], v[68:71], v[96:99], v[36:39]\n\t"
  "s_waitcnt lgkmcnt(11)\n\t"
  "ds_read_b64 v[68:69], %[va] offset:704\n\t"
  "v_mfma_f32_16x16x32_bf16 v[60:63], v[72:75], v[116:119], v[60:63]\n\t"
  "ds_read_b128 v[136:139], %[vb3] offset:5120\n\t"
  "ds_read_b64 v[70:71], %[va] offset:712\n\t"
  "v_mfma_f32_16x16x32_bf16 v[56:59], v[72:75], v[112:115], v[56:59]\n\t"
  "s_waitcnt vmcnt(0)\n\t"
  "v_cmp_ne_u64 vcc, v[252:253], v[162:163]\n\t"
  "v_mfma_f32_16x16x32_bf16 v[52:55], v[72:75], v[108:111], v[52:55]\n\t"
  "v_mfma_f32_16x16x32_bf16 v[48:51], v[72:75], v[104:107], v[48:51]\n\t"
  "ds_read_b128 v[132:135], %[vb3] offset:3072\n\t"
  "s_cbranch_vccnz La2_slow%=\n\t"
  "La2_back%=:\n\t"
  "v_mfma_f32_16x16x32_bf16 v[44:47], v[72:75], v[100:103], v[44:47]\n\t"
  "buffer_load_dword v248, %[vmod], %[srd], 0 offen sc1\n\t"
  "s_add_u32 m0, s45, 84992\n\t"
  "v_mfma_f32_16x16x32_bf16 v[40:43], v[72:75], v[96:99], v[40:43]\n\t"
  "ds_read_b64 v[72:73], %[va] offset:1216\n\t"
  "ds_read_b64 v[74:75], %[va] offset:1224\n\t"
  "v_mfma_f32_16x16x32_bf16 v[60:63], v[76:79], v[112:115], v[60:63]\n\t"
  "ds_read_b128 v[128:131], %[vb3] offset:1024\n\t"
  "s_add_u32 s44, %[sKV], 2621440\n\t"
  "v_mfma_f32_16x16x32_bf16 v[56:59], v[76:79], v[108:111], v[56:59]\n\t"
  "buffer_load_dwordx4 %[vkv], %[srd], s44 offen sc1 lds\n\t"
  "s_add_u32 m0, s45, 86016\n\t"
  "v_mfma_f32_16x16x32_bf16 v[52:55], v[76:79], v[104:107], v[52:55]\n\t"
  "s_add_u32 s44, %[sKV], 2785280\n\t"
  "buffer_load_dwordx4 %[vkv], %[srd], s44 offen sc1 lds\n\t"
  "v_mfma_f32_16x16x32_bf16 v[48:51], v[76:79], v[100:103], v[48:51]\n\t"
  "s_add_u32 m0, s45, 87040\n\t"
  "s_add_u32 s44, %[sKV], 2949120\n\t"
  "v_mfma_f32_16x16x32_bf16 v[44:47], v[76:79], v[96:99], v[44:47]\n\t"
  "ds_read_b64 v[76:77], %[va] offset:1728\n\t"
  "ds_read_b64 v[78:79], %[va] offset:1736\n\t"
  "v_mfma_f32_16x16x32_bf16 v[60:63], v[80:83], v[108:111], v[60:63]\n\t"
  "buffer_load_dwordx4 %[vkv], %[srd], s44 offen sc1 lds\n\t"
  "s_add_u32 m0, s45, 88064\n\t"
  "v_mfma_f32_16x16x32_bf16 v[56:59], v[80:83], v[104:107], v[56:59]\n\t"
  "s_add_u32 s44, %[sKV], 3112960\n\t"
  "buffer_load_dwordx4 %[vkv], %[srd], s44 offen sc1 lds\n\t"
  "v_mfma_f32_16x16x32_bf16 v[52:55], v[80:83], v[100:103], v[52:55]\n\t"
  "s_add_u32 m0, s45, 101376\n\t"
  "s_add_u32 s44, s45, 835584\n\t"
  "v_mfma_f32_16x16x32_bf16 v[48:51], v[80:83], v[96:99], v[48:51]\n\t"
  "ds_read_b64 v[80:81], %[va] offset:2240\n\t"
  "ds_read_b64 v[82:83], %[va] offset:2248\n\t"
  "v_mfma_f32_16x16x32_bf16 v[60:63], v[84:87], v[104:107], v[60:63]\n\t"
  "buffer_load_dwordx4 %[vl16], %[srd], s44 offen offset:0 sc1 lds\n\t"
  "buffer_load_dwordx4 %[vl16], %[srd], s44 offen offset:1024 sc1 lds\n\t"
  "v_mfma_f32_16x16x32_bf16 v[56:59], v[84:87], v[100:103], v[56:59]\n\t"
  "buffer_load_dwordx4 %[vl16], %[srd], s44 offen offset:2048 sc1 lds\n\t"
  "buffer_load_dwordx4 %[vl16], %[srd], s44 offen offset:3072 sc1 lds\n\t"
  "v_mfma_f32_16x16x32_bf16 v[52:55], v[84:87], v[96:99], v[52:55]\n\t"
  "s_waitcnt lgkmcnt(15)\n\t"
  "ds_read_b64 v[84:85], %[va] offset:2752\n\t"
  "v_mfma_f32_16x16x32_bf16 v[60:63], v[88:91], v[100:103], v[60:63]\n\t"
  "ds_read_b64 v[86:87], %[va] offset:2760\n\t"
  "v_mfma_f32_16x16x32_bf16 v[56:59], v[88:91], v[96:99], v[56:59]\n\t"
  "ds_read_b64 v[88:89], %[va] offset:3264\n\t"
  "ds_read_b64 v[90:91], %[va] offset:3272\n\t"
  "v_mfma_f32_16x16x32_bf16 v[60:63], v[92:95], v[96:99], v[60:63]\n\t"
  "s_waitcnt lgkmcnt(8)\n\t"
  "ds_read_b64 v[92:93], %[va] offset:3776\n\t"
  "v_mfma_f32_16x16x32_bf16 v[60:63], v[64:67], v[156:159], v[60:63]\n\t"
  "ds_read_b128 v[124:127], %[vb0] offset:14336\n\t"
  "ds_read_b64 v[94:95], %[va] offset:3784\n\t"
  "v_mfma_f32_16x16x32_bf16 v[56:59], v[64:67], v[152:155], v[56:59]\n\t"
  "v_mfma_f32_16x16x32_bf16 v[52:55], v[64:67], v[148:151], v[52:55]\n\t"
  "v_mfma_f32_16x16x32_bf16 v[48:51], v[64:67], v[144:147], v[48:51]\n\t"
  "ds_read_b128 v[120:123], %[vb0] offset:12288\n\t"
  "v_mfma_f32_16x16x32_bf16 v[44:47], v[64:67], v[140:143], v[44:47]\n\t"
  "v_mfma_f32_16x16x32_bf16 v[40:43], v[64:67], v[136:139], v[40:43]\n\t"
  "v_mfma_f32_16x16x32_bf16 v[36:39], v[64:67], v[132:135], v[36:39]\n\t"
  "ds_read_b128 v[116:119], %[vb0] offset:10240\n\t"
  "v_mfma_f32_16x16x32_bf16 v[32:35], v[64:67], v[128:131], v[32:35]\n\t"
  "ds_read_b64 v[64:65], %[va] offset:256\n\t"
  "ds_read_b64 v[66:67], %[va] offset:264\n\t"
  "v_mfma_f32_16x16x32_bf16 v[60:63], v[68:71], v[152:155], v[60:63]\n\t"
  "v_mfma_f32_16x16x32_bf16 v[56:59], v[68:71], v[148:151], v[56:59]\n\t"
  "ds_read_b128 v[112:115], %[vb0] offset:8192\n\t"
  "v_mfma_f32_16x16x32_bf16 v[52:55], v[68:71], v[144:147], v[52:55]\n\t"
  "v_mfma_f32_16x16x32_bf16 v[48:51], v[68:71], v[140:143], v[48:51]\n\t"
  "v_mfma_f32_16x16x32_bf16 v[44:47], v[68:71], v[136:139], v[44:47]\n\t"
  "ds_read_b128 v[108:111], %[vb0] offset:6144\n\t"
  "v_mfma_f32_16x16x32_bf16 v[40:43], v[68:71], v[132:135], v[40:43]\n\t"
  "v_mfma_f32_16x16x32_bf16 v[36:39], v[68:71], v[128:131], v[36:39]\n\t"
  "s_waitcnt lgkmcnt(11)\n\t"
  "ds_read_b64 v[68:69], %[va] offset:768\n\t"
  "v_mfma_f32_16x16x32_bf16 v[60:63], v[72:75], v[148:151], v[60:63]\n\t"
  "ds_read_b128 v[104:107], %[vb0] offset:4096\n\t"
  "ds_read_b64 v[70:71], %[va] offset:776\n\t"
  "v_mfma_f32_16x16x32_bf16 v[56:59], v[72:75], v[144:147], v[56:59]\n\t"
  "v_mfma_f32_16x16x32_bf16 v[52:55], v[72:75], v[140:143], v[52:55]\n\t"
  "v_mfma_f32_16x16x32_bf16 v[48:51], v[72:75], v[136:139], v[48:51]\n\t"
  "ds_read_b128 v[100:103], %[vb0] offset:2048\n\t"
  "v_mfma_f32_16x16x32_bf16 v[44:47], v[72:75], v[132:135], v[44:47]\n\t"
  "v_mfma_f32_16x16x32_bf16 v[40:43], v[72:75], v[128:131], v[40:43]\n\t"
  "ds_read_b64 v[72:73], %[va] offset:1280\n\t"
  "ds_read_b64 v[74:75], %[va] offset:1288\n\t"
  "v_mfma_f32_16x16x32_bf16 v[60:63], v[76:79], v[144:147], v[60:63]\n\t"
  "ds_read_b128 v[96:99], %[vb0] offset:0\n\t"
  "v_mfma_f32_16x16x32_bf16 v[56:59], v[76:79], v[140:143], v[56:59]\n\t"
  "v_mfma_f32_16x16x32_bf16 v[52:55], v[76:79], v[136:139], v[52:55]\n\t"
  "v_mfma_f32_16x16x32_bf16 v[48:51], v[76:79], v[132:135], v[48:51]\n\t"
  "v_mfma_f32_16x16x32_bf16 v[44:47], v[76:79], v[128:131], v[44:47]\n\t"
  "ds_read_b64 v[76:77], %[va] offset:1792\n\t"
  "ds_read_b64 v[78:79], %[va] offset:1800\n\t"
  "v_mfma_f32_16x16x32_bf16 v[60:63], v[80:83], v[140:143], v[60:63]\n\t"
  "buffer_load_dwordx2 v[250:251], %[vfw], %[srd], 0 offen sc1\n\t"
  "s_waitcnt vmcnt(9)\n\t"
  "v_add_f32_dpp v248, v248, v248 quad_perm:[1,0,3,2] row_mask:0xf bank_mask:0xf\n\t"
  "v_mfma_f32_16x16x32_bf16 v[56:59], v[80:83], v[136:139], v[56:59]\n\t"
  "v_mfma_f32_16x16x32_bf16 v[52:55], v[80:83], v[132:135], v[52:55]\n\t"
  "v_add_f32_dpp v248, v248, v248 quad_perm:[2,3,0,1] row_mask:0xf bank_mask:0xf\n\t"
  "v_cvt_pk_bf16_f32 v248, 0, v248\n\t"
  "v_mfma_f32_16x16x32_bf16 v[48:51], v[80:83], v[128:131], v[48:51]\n\t"
  "ds_read_b64 v[80:81], %[va] offset:2304\n\t"
  "ds_read_b64 v[82:83], %[va] offset:2312\n\t"
  "v_mfma_f32_16x16x32_bf16 v[60:63], v[84:87], v[136:139], v[60:63]\n\t"
  "v_add_f32 v249, 1.0, v248\n\t"
  "v_cvt_pk_bf16_f32 v249, 0, v249\n\t"
  "v_mfma_f32_16x16x32_bf16 v[56:59], v[84:87], v[132:135], v[56:59]\n\t"
  "v_readlane_b32 s46, v248, 0\n\t"
  "v_readlane_b32 s48, v248, 8\n\t"
  "v_mfma_f32_16x16x32_bf16 v[52:55], v[84:87], v[128:131], v[52:55]\n\t"
  "s_waitcnt lgkmcnt(15)\n\t"
  "ds_read_b64 v[84:85], %[va] offset:2816\n\t"
  "v_mfma_f32_16x16x32_bf16 v[60:63], v[88:91], v[132:135], v[60:63]\n\t"
  "ds_read_b64 v[86:87], %[va] offset:2824\n\t"
  "v_readlane_b32 s47, v249, 4\n\t"
  "v_mfma_f32_16x16x32_bf16 v[56:59], v[88:91], v[128:131], v[56:59]\n\t"
  "ds_read_b64 v[88:89], %[va] offset:3328\n\t"
  "ds_read_b64 v[90:91], %[va] offset:3336\n\t"
  "v_mfma_f32_16x16x32_bf16 v[60:63], v[92:95], v[128:131], v[60:63]\n\t"
  "s_waitcnt lgkmcnt(8)\n\t"
  "ds_read_b64 v[92:93], %[va] offset:3840\n\t"
  "v_mfma_f32_16x16x32_bf16 v[60:63], v[64:67], v[124:127], v[60:63]\n\t"
  "ds_read_b128 v[156:159], %[vb1] offset:14336\n\t"
  "ds_read_b64 v[94:95], %[va] offset:3848\n\t"
  "v_mfma_f32_16x16x32_bf16 v[56:59], v[64:67], v[120:123], v[56:59]\n\t"
  "v_readlane_b32 s51, v249, 12\n\t"
  "v_mfma_f32_16x16x32_bf16 v[52:55], v[64:67], v[116:119], v[52:55]\n\t"
  "v_mfma_f32_16x16x32_bf16 v[48:51], v[64:67], v[112:115], v[48:51]\n\t"
  "ds_read_b128 v[152:155], %[vb1] offset:12288\n\t"
  "s_nop 0\n\t"
  "s_and_b32 s52, s51, 0x7fffffff\n\t"
  "s_cselect_b32 s51, s51, 0x2b800000\n\t"
  "v_mfma_f32_16x16x32_bf16 v[44:47], v[64:67], v[108:111], v[44:47]\n\t"
  "v_mov_b32 v249, s48\n\t"
  "v_mul_f32 v249, s51, v249\n\t"
  "v_mfma_f32_16x16x32_bf16 v[40:43], v[64:67], v[104:107], v[40:43]\n\t"
  "v_mul_f32 v248, s47, v249\n\t"
  "v_mul_f32 v249, s46, v249\n\t"
  "v_mfma_f32_16x16x32_bf16 v[36:39], v[64:67], v[100:103], v[36:39]\n\t"
  "ds_read_b128 v[148:151], %[vb1] offset:10240\n\t"
  "s_waitcnt vmcnt(1)\n\t"
  "s_barrier\n\t"
  "v_mfma_f32_16x16x32_bf16 v[32:35], v[64:67], v[96:99], v[32:35]\n\t"
  "ds_read_b64 v[64:65], %[va] offset:320\n\t"
  "ds_read_b64 v[66:67], %[va] offset:328\n\t"
  "v_mfma_f32_16x16x32_bf16 v[60:63], v[68:71], v[120:123], v[60:63]\n\t"
  "ds_read_b64 v[224:225], %[vx] offset:0\n\t"
  "ds_read_b128 v[228:231], %[vs] offset:0\n\t"
  "v_mfma_f32_16x16x32_bf16 v[56:59], v[68:71], v[116:119], v[56:59]\n\t"
  "ds_read_b128 v[144:147], %[vb1] offset:8192\n\t"
  "ds_read_b128 v[232:235], %[vs] offset:16\n\t"
  "v_mfma_f32_16x16x32_bf16 v[52:55], v[68:71], v[112:115], v[52:55]\n\t"
  "ds_read_b64 v[236:237], %[vx] offset:2048\n\t"
  "ds_read_b128 v[240:243], %[vs] offset:2048\n\t"
  "v_mfma_f32_16x16x32_bf16 v[48:51], v[68:71], v[108:111], v[48:51]\n\t"
  "ds_read_b128 v[244:247], %[vs] offset:2064\n\t"
  "v_mfma_f32_16x16x32_bf16 v[44:47], v[68:71], v[104:107], v[44:47]\n\t"
  "ds_read_b128 v[140:143], %[vb1] offset:6144\n\t"
  "v_mfma_f32_16x16x32_bf16 v[40:43], v[68:71], v[100:103], v[40:43]\n\t"
  "v_mfma_f32_16x16x32_bf16 v[36:39], v[68:71], v[96:99], v[36:39]\n\t"
  "s_waitcnt lgkmcnt(15)\n\t"
  "ds_read_b64 v[68:69], %[va] offset:832\n\t"
  "v_mfma_f32_16x16x32_bf16 v[60:63], v[72:75], v[116:119], v[60:63]\n\t"
  "ds_read_b128 v[136:139], %[vb1] offset:4096\n\t"
  "ds_read_b64 v[70:71], %[va] offset:840\n\t"
  "v_mfma_f32_16x16x32_bf16 v[56:59], v[72:75], v[112:115], v[56:59]\n\t"
  "v_mfma_f32_16x16x32_bf16 v[52:55], v[72:75], v[108:111], v[52:55]\n\t"
  "v_mfma_f32_16x16x32_bf16 v[48:51], v[72:75], v[104:107], v[48:51]\n\t"
  "ds_read_b128 v[132:135], %[vb1] offset:2048\n\t"
  "v_mfma_f32_16x16x32_bf16 v[44:47], v[72:75], v[100:103], v[44:47]\n\t"
  "s_waitcnt lgkmcnt(11)\n\t"
  "v_lshlrev_b32 v192, 16, v224\n\t"
  "v_mfma_f32_16x16x32_bf16 v[40:43], v[72:75], v[96:99], v[40:43]\n\t"
  "ds_read_b64 v[72:73], %[va] offset:1344\n\t"
  "ds_read_b64 v[74:75], %[va] offset:1352\n\t"
  "v_mfma_f32_16x16x32_bf16 v[60:63], v[76:79], v[112:115], v[60:63]\n\t"
  "ds_read_b128 v[128:131], %[vb1] offset:0\n\t"
  "v_and_b32 v193, 0xffff0000, v224\n\t"
  "v_mfma_f32_16x16x32_bf16 v[56:59], v[76:79], v[108:111], v[56:59]\n\t"
  "v_lshlrev_b32 v194, 16, v225\n\t"
  "v_and_b32 v195, 0xffff0000, v225\n\t"
  "v_mfma_f32_16x16x32_bf16 v[52:55], v[76:79], v[104:107], v[52:55]\n\t"
  "s_waitcnt lgkmcnt(11)\n\t"
  "v_fma_f32 v163, v195, v234, v235\n\t"
  "v_mfma_f32_16x16x32_bf16 v[48:51], v[76:79], v[100:103], v[48:51]\n\t"
  "v_fma_f32 v162, v194, v232, v233\n\t"
  "v_fma_f32 v161, v193, v230, v231\n\t"
  "v_mfma_f32_16x16x32_bf16 v[44:47], v[76:79], v[96:99], v[44:47]\n\t"
  "ds_read_b64 v[76:77], %[va] offset:1856\n\t"
  "ds_read_b64 v[78:79], %[va] offset:1864\n\t"
  "v_mfma_f32_16x16x32_bf16 v[60:63], v[80:83], v[108:111], v[60:63]\n\t"
  "v_fma_f32 v160, v192, v228, v229\n\t"
  "ds_read_b64 v[224:225], %[vx] offset:4096\n\t"
  "v_mfma_f32_16x16x32_bf16 v[56:59], v[80:83], v[104:107], v[56:59]\n\t"
  "ds_read_b128 v[228:231], %[vs] offset:4096\n\t"
  "ds_read_b128 v[232:235], %[vs] offset:4112\n\t"
  "v_mfma_f32_16x16x32_bf16 v[52:55], v[80:83], v[100:103], v[52:55]\n\t"
  "v_fma_f32 v160, v160, v248, v249\n\t"
  "v_mul_f32 v192, s51, v192\n\t"
  "v_mfma_f32_16x16x32_bf16 v[48:51], v[80:83], v[96:99], v[48:51]\n\t"
  "ds_read_b64 v[80:81], %[va] offset:2368\n\t"
  "ds_read_b64 v[82:83], %[va] offset:2376\n\t"
  "v_mfma_f32_16x16x32_bf16 v[60:63], v[84:87], v[104:107], v[60:63]\n\t"
  "v_fma_f32 v161, v161, v248, v249\n\t"
  "v_mul_f32 v193, s51, v193\n\t"
  "v_mfma_f32_16x16x32_bf16 v[56:59], v[84:87], v[100:103], v[56:59]\n\t"
  "v_fma_f32 v162, v162, v248, v249\n\t"
  "v_mul_f32 v194, s51, v194\n\t"
  "v_mfma_f32_16x16x32_bf16 v[52:55], v[84:87], v[96:99], v[52:55]\n\t"
  "ds_read_b64 v[84:85], %[va] offset:2880\n\t"
  "ds_read_b64 v[86:87], %[va] offset:2888\n\t"
  "v_mfma_f32_16x16x32_bf16 v[60:63], v[88:91], v[100:103], v[60:63]\n\t"
  "v_fma_f32 v163, v163, v248, v249\n\t"
  "v_mul_f32 v195, s51, v195\n\t"
  "v_mfma_f32_16x16x32_bf16 v[56:59], v[88:91], v[96:99], v[56:59]\n\t"
  "ds_read_b64 v[88:89], %[va] offset:3392\n\t"
  "ds_read_b64 v[90:91], %[va] offset:3400\n\t"
  "v_mfma_f32_16x16x32_bf16 v[60:63], v[92:95], v[96:99], v[60:63]\n\t"
  "s_waitcnt lgkmcnt(11)\n\t"
  "ds_read_b64 v[92:93], %[va] offset:3904\n\t"
  "v_mfma_f32_16x16x32_bf16 v[60:63], v[64:67], v[156:159], v[60:63]\n\t"
  "ds_read_b128 v[124:127], %[vb2] offset:14336\n\t"
  "ds_read_b64 v[94:95], %[va] offset:3912\n\t"
  "v_mfma_f32_16x16x32_bf16 v[56:59], v[64:67], v[152:155], v[56:59]\n\t"
  "s_waitcnt vmcnt(0)\n\t"
  "v_cmp_eq_u64 s[54:55], v[252:253], v[250:251]\n\t"
  "v_mfma_f32_16x16x32_bf16 v[52:55], v[64:67], v[148:151], v[52:55]\n\t"
  "v_lshlrev_b32 v196, 16, v236\n\t"
  "v_and_b32 v197, 0xffff0000, v236\n\t"
  "v_mfma_f32_16x16x32_bf16 v[48:51], v[64:67], v[144:147], v[48:51]\n\t"
  "ds_read_b128 v[120:123], %[vb2] offset:12288\n\t"
  "v_lshlrev_b32 v198, 16, v237\n\t"
  "v_mfma_f32_16x16x32_bf16 v[44:47], v[64:67], v[140:143], v[44:47]\n\t"
  "s_cmp_eq_u64 s[54:55], -1\n\t"
  "s_cselect_b32 %[wdone], 1, 0\n\t"
  "s_cselect_b32 s50, %[sW1], 0x70000000\n\t"
  "v_mfma_f32_16x16x32_bf16 v[40:43], v[64:67], v[136:139], v[40:43]\n\t"
  "buffer_load_dwordx4 a[0:3], %[vl16], %[srd], s50 offen offset:0 sc1\n\t"
  "buffer_load_dwordx4 a[4:7], %[vl16], %[srd], s50 offen offset:1024 sc1\n\t"
  "v_mfma_f32_16x16x32_bf16 v[36:39], v[64:67], v[132:135], v[36:39]\n\t"
  "ds_read_b128 v[116:119], %[vb2] offset:10240\n\t"
  "v_and_b32 v199, 0xffff0000, v237\n\t"
  "v_mfma_f32_16x16x32_bf16 v[32:35], v[64:67], v[128:131], v[32:35]\n\t"
  "ds_read_b64 v[64:65], %[va] offset:384\n\t"
  "ds_read_b64 v[66:67], %[va] offset:392\n\t"
  "v_mfma_f32_16x16x32_bf16 v[60:63], v[68:71], v[152:155], v[60:63]\n\t"
  "v_fma_f32 v167, v199, v246, v247\n\t"
  "v_fma_f32 v166, v198, v244, v245\n\t"
  "v_mfma_f32_16x16x32_bf16 v[56:59], v[68:71], v[148:151], v[56:59]\n\t"
  "ds_read_b128 v[112:115], %[vb2] offset:8192\n\t"
  "buffer_load_dwordx4 a[8:11], %[vl16], %[srd], s50 offen offset:2048 sc1\n\t"
  "v_mfma_f32_16x16x32_bf16 v[52:55], v[68:71], v[144:147], v[52:55]\n\t"
  "v_fma_f32 v165, v197, v242, v243\n\t"
  "v_fma_f32 v164, v196, v240, v241\n\t"
  "v_mfma_f32_16x16x32_bf16 v[48:51], v[68:71], v[140:143], v[48:51]\n\t"
  "ds_read_b64 v[236:237], %[vx] offset:6144\n\t"
  "ds_read_b128 v[240:243], %[vs] offset:6144\n\t"
  "v_mfma_f32_16x16x32_bf16 v[44:47], v[68:71], v[136:139], v[44:47]\n\t"
  "ds_read_b128 v[108:111], %[vb2] offset:6144\n\t"
  "buffer_load_dwordx4 a[12:15], %[vl16], %[srd], s50 offen offset:3072 sc1\n\t"
  "v_mfma_f32_16x16x32_bf16 v[40:43], v[68:71], v[132:135], v[40:43]\n\t"
  "ds_read_b128 v[244:247], %[vs] offset:6160\n\t"
  "v_fma_f32 v164, v164, v248, v249\n\t"
  "v_mfma_f32_16x16x32_bf16 v[36:39], v[68:71], v[128:131], v[36:39]\n\t"
  "s_waitcnt lgkmcnt(14)\n\t"
  "ds_read_b64 v[68:69], %[va] offset:896\n\t"
  "v_mfma_f32_16x16x32_bf16 v[60:63], v[72:75], v[148:151], v[60:63]\n\t"
  "ds_read_b128 v[104:107], %[vb2] offset:4096\n\t"
  "ds_read_b64 v[70:71], %[va] offset:904\n\t"
  "v_mfma_f32_16x16x32_bf16 v[56:59], v[72:75], v[144:147], v[56:59]\n\t"
  "s_add_u32 s50, s50, 4096\n\t"
  "v_mul_f32 v196, s51, v196\n\t"
  "v_mfma_f32_16x16x32_bf16 v[52:55], v[72:75], v[140:143], v[52:55]\n\t"
  "v_fma_f32 v165, v165, v248, v249\n\t"
  "v_mul_f32 v197, s51, v197\n\t"
  "v_mfma_f32_16x16x32_bf16 v[48:51], v[72:75], v[136:139], v[48:51]\n\t"
  "ds_read_b128 v[100:103], %[vb2] offset:2048\n\t"
  "v_fma_f32 v166, v166, v248, v249\n\t"
  "v_mfma_f32_16x16x32_bf16 v[44:47], v[72:75], v[132:135], v[44:47]\n\t"
  "buffer_load_dwordx4 a[16:19], %[vl16], %[srd], s50 offen offset:0 sc1\n\t"
  "v_mul_f32 v198, s51, v198\n\t"
  "v_mfma_f32_16x16x32_bf16 v[40:43], v[72:75], v[128:131], v[40:43]\n\t"
  "ds_read_b64 v[72:73], %[va] offset:1408\n\t"
  "ds_read_b64 v[74:75], %[va] offset:1416\n\t"
  "v_mfma_f32_16x16x32_bf16 v[60:63], v[76:79], v[144:147], v[60:63]\n\t"
  "ds_read_b128 v[96:99], %[vb2] offset:0\n\t"
  "v_fma_f32 v167, v167, v248, v249\n\t"
  "v_mfma_f32_16x16x32_bf16 v[56:59], v[76:79], v[140:143], v[56:59]\n\t"
  "v_mul_f32 v199, s51, v199\n\t"
  "v_lshlrev_b32 v200, 16, v224\n\t"
  "v_mfma_f32_16x16x32_bf16 v[52:55], v[76:79], v[136:139], v[52:55]\n\t"
  "buffer_load_dwordx4 a[20:23], %[vl16], %[srd], s50 offen offset:1024 sc1\n\t"
  "v_and_b32 v201, 0xffff0000, v224\n\t"
  "v_mfma_f32_16x16x32_bf16 v[48:51], v[76:79], v[132:135], v[48:51]\n\t"
  "v_lshlrev_b32 v202, 16, v225\n\t"
  "v_and_b32 v203, 0xffff0000, v225\n\t"
  "v_mfma_f32_16x16x32_bf16 v[44:47], v[76:79], v[128:131], v[44:47]\n\t"
  "ds_read_b64 v[76:77], %[va] offset:1920\n\t"
  "ds_read_b64 v[78:79], %[va] offset:1928\n\t"
  "v_mfma_f32_16x16x32_bf16 v[60:63], v[80:83], v[140:143], v[60:63]\n\t"
  "buffer_load_dwordx4 a[24:27], %[vl16], %[srd], s50 offen offset:2048 sc1\n\t"
  "v_fma_f32 v171, v203, v234, v235\n\t"
  "v_mfma_f32_16x16x32_bf16 v[56:59], v[80:83], v[136:139], v[56:59]\n\t"
  "v_fma_f32 v170, v202, v232, v233\n\t"
  "v_fma_f32 v169, v201, v230, v231\n\t"
  "v_mfma_f32_16x16x32_bf16 v[52:55], v[80:83], v[132:135], v[52:55]\n\t"
  "v_fma_f32 v168, v200, v228, v229\n\t"
  "ds_read_b64 v[224:225], %[vx] offset:8192\n\t"
  "v_mfma_f32_16x16x32_bf16 v[48:51], v[80:83], v[128:131], v[48:51]\n\t"
  "ds_read_b64 v[80:81], %[va] offset:2432\n\t"
  "ds_read_b64 v[82:83], %[va] offset:2440\n\t"
  "v_mfma_f32_16x16x32_bf16 v[60:63], v[84:87], v[136:139], v[60:63]\n\t"
  "buffer_load_dwordx4 a[28:31], %[vl16], %[srd], s50 offen offset:3072 sc1\n\t"
  "ds_read_b128 v[228:231], %[vs] offset:8192\n\t"
  "v_mfma_f32_16x16x32_bf16 v[56:59], v[84:87], v[132:135], v[56:59]\n\t"
  "ds_read_b128 v[232:235], %[vs] offset:8208\n\t"
  "v_fma_f32 v168, v168, v248, v249\n\t"
  "v_mfma_f32_16x16x32_bf16 v[52:55], v[84:87], v[128:131], v[52:55]\n\t"
  "s_waitcnt lgkmcnt(15)\n\t"
  "ds_read_b64 v[84:85], %[va] offset:2944\n\t"
  "v_mfma_f32_16x16x32_bf16 v[60:63], v[88:91], v[132:135], v[60:63]\n\t"
  "ds_read_b64 v[86:87], %[va] offset:2952\n\t"
  "s_add_u32 s50, s50, 4096\n\t"
  "v_mfma_f32_16x16x32_bf16 v[56:59], v[88:91], v[128:131], v[56:59]\n\t"
  "ds_read_b64 v[88:89], %[va] offset:3456\n\t"
  "ds_read_b64 v[90:91], %[va] offset:3464\n\t"
  "v_mfma_f32_16x16x32_bf16 v[60:63], v[92:95], v[128:131], v[60:63]\n\t"
  "s_waitcnt lgkmcnt(11)\n\t"
  "ds_read_b64 v[92:93], %[va] offset:3968\n\t"
  "v_mfma_f32_16x16x32_bf16 v[60:63], v[64:67], v[124:127], v[60:63]\n\t"
  "ds_read_b128 v[156:159], %[vb3] offset:14336\n\t"
  "ds_read_b64 v[94:95], %[va] offset:3976\n\t"
  "v_mfma_f32_16x16x32_bf16 v[56:59], v[64:67], v[120:123], v[56:59]\n\t"
  "buffer_load_dwordx4 a[32:35], %[vl16], %[srd], s50 offen offset:0 sc1\n\t"
  "v_mul_f32 v200, s51, v200\n\t"
  "v_mfma_f32_16x16x32_bf16 v[52:55], v[64:67], v[116:119], v[52:55]\n\t"
  "v_fma_f32 v169, v169, v248, v249\n\t"
  "v_mul_f32 v201, s51, v201\n\t"
  "v_mfma_f32_16x16x32_bf16 v[48:51], v[64:67], v[112:115], v[48:51]\n\t"
  "ds_read_b128 v[152:155], %[vb3] offset:12288\n\t"
  "v_fma_f32 v170, v170, v248, v249\n\t"
  "v_mfma_f32_16x16x32_bf16 v[44:47], v[64:67], v[108:111], v[44:47]\n\t"
  "buffer_load_dwordx4 a[36:39], %[vl16], %[srd], s50 offen offset:1024 sc1\n\t"
  "v_mul_f32 v202, s51, v202\n\t"
  "v_mfma_f32_16x16x32_bf16 v[40:43], v[64:67], v[104:107], v[40:43]\n\t"
  "v_fma_f32 v171, v171, v248, v249\n\t"
  "v_mul_f32 v203, s51, v203\n\t"
  "v_mfma_f32_16x16x32_bf16 v[36:39], v[64:67], v[100:103], v[36:39]\n\t"
  "ds_read_b128 v[148:151], %[vb3] offset:10240\n\t"
  "v_lshlrev_b32 v204, 16, v236\n\t"
  "v_mfma_f32_16x16x32_bf16 v[32:35], v[64:67], v[96:99], v[32:35]\n\t"
  "ds_read_b64 v[64:65], %[va] offset:448\n\t"
  "ds_read_b64 v[66:67], %[va] offset:456\n\t"
  "v_mfma_f32_16x16x32_bf16 v[60:63], v[68:71], v[120:123], v[60:63]\n\t"
  "buffer_load_dwordx4 a[40:43], %[vl16], %[srd], s50 offen offset:2048 sc1\n\t"
  "v_and_b32 v205, 0xffff0000, v236\n\t"
  "v_mfma_f32_16x16x32_bf16 v[56:59], v[68:71], v[116:119], v[56:59]\n\t"
  "ds_read_b128 v[144:147], %[vb3] offset:8192\n\t"
  "v_lshlrev_b32 v206, 16, v237\n\t"
  "v_mfma_f32_16x16x32_bf16 v[52:55], v[68:71], v[112:115], v[52:55]\n\t"
  "v_and_b32 v207, 0xffff0000, v237\n\t"
  "v_fma_f32 v175, v207, v246, v247\n\t"
  "v_mfma_f32_16x16x32_bf16 v[48:51], v[68:71], v[108:111], v[48:51]\n\t"
  "buffer_load_dwordx4 a[44:47], %[vl16], %[srd], s50 offen offset:3072 sc1\n\t"
  "v_fma_f32 v174, v206, v244, v245\n\t"
  "v_mfma_f32_16x16x32_bf16 v[44:47], v[68:71], v[104:107], v[44:47]\n\t"
  "ds_read_b128 v[140:143], %[vb3] offset:6144\n\t"
  "v_fma_f32 v173, v205, v242, v243\n\t"
  "v_mfma_f32_16x16x32_bf16 v[40:43], v[68:71], v[100:103], v[40:43]\n\t"
  "v_fma_f32 v172, v204, v240, v241\n\t"
  "ds_read_b64 v[236:237], %[vx] offset:10240\n\t"
  "v_mfma_f32_16x16x32_bf16 v[36:39], v[68:71], v[96:99], v[36:39]\n\t"
  "s_waitcnt lgkmcnt(12)\n\t"
  "ds_read_b64 v[68:69], %[va] offset:960\n\t"
  "v_mfma_f32_16x16x32_bf16 v[60:63], v[72:75], v[116:119], v[60:63]\n\t"
  "ds_read_b128 v[136:139], %[vb3] offset:4096\n\t"
  "ds_read_b64 v[70:71], %[va] offset:968\n\t"
  "v_mfma_f32_16x16x32_bf16 v[56:59], v[72:75], v[112:115], v[56:59]\n\t"
  "s_add_u32 s50, s50, 4096\n\t"
  "ds_read_b128 v[240:243], %[vs] offset:10240\n\t"
  "v_mfma_f32_16x16x32_bf16 v[52:55], v[72:75], v[108:111], v[52:55]\n\t"
  "ds_read_b128 v[244:247], %[vs] offset:10256\n\t"
  "v_fma_f32 v172, v172, v248, v249\n\t"
  "v_mfma_f32_16x16x32_bf16 v[48:51], v[72:75], v[104:107], v[48:51]\n\t"
  "ds_read_b128 v[132:135], %[vb3] offset:2048\n\t"
  "buffer_load_dwordx4 a[48:51], %[vl16], %[srd], s50 offen offset:0 sc1\n\t"
  "v_mfma_f32_16x16x32_bf16 v[44:47], v[72:75], v[100:103], v[44:47]\n\t"
  "v_mul_f32 v204, s51, v204\n\t"
  "v_fma_f32 v173, v173, v248, v249\n\t"
  "v_mfma_f32_16x16x32_bf16 v[40:43], v[72:75], v[96:99], v[40:43]\n\t"
  "ds_read_b64 v[72:73], %[va] offset:1472\n\t"
  "ds_read_b64 v[74:75], %[va] offset:1480\n\t"
  "v_mfma_f32_16x16x32_bf16 v[60:63], v[76:79], v[112:115], v[60:63]\n\t"
  "ds_read_b128 v[128:131], %[vb3] offset:0\n\t"
  "v_mul_f32 v205, s51, v205\n\t"
  "v_mfma_f32_16x16x32_bf16 v[56:59], v[76:79], v[108:111], v[56:59]\n\t"
  "buffer_load_dwordx4 a[52:55], %[vl16], %[srd], s50 offen offset:1024 sc1\n\t"
  "v_fma_f32 v174, v174, v248, v249\n\t"
  "v_mfma_f32_16x16x32_bf16 v[52:55], v[76:79], v[104:107], v[52:55]\n\t"
  "v_mul_f32 v206, s51, v206\n\t"
  "v_fma_f32 v175, v175, v248, v249\n\t"
  "v_mfma_f32_16x16x32_bf16 v[48:51], v[76:79], v[100:103], v[48:51]\n\t"
  "v_mul_f32 v207, s51, v207\n\t"
  "v_lshlrev_b32 v208, 16, v224\n\t"
  "v_mfma_f32_16x16x32_bf16 v[44:47], v[76:79], v[96:99], v[44:47]\n\t"
  "ds_read_b64 v[76:77], %[va] offset:1984\n\t"
  "ds_read_b64 v[78:79], %[va] offset:1992\n\t"
  "v_mfma_f32_16x16x32_bf16 v[60:63], v[80:83], v[108:111], v[60:63]\n\t"
  "buffer_load_dwordx4 a[56:59], %[vl16], %[srd], s50 offen offset:2048 sc1\n\t"
  "v_and_b32 v209, 0xffff0000, v224\n\t"
  "v_mfma_f32_16x16x32_bf16 v[56:59], v[80:83], v[104:107], v[56:59]\n\t"
  "v_lshlrev_b32 v210, 16, v225\n\t"
  "v_and_b32 v211, 0xffff0000, v225\n\t"
  "v_mfma_f32_16x16x32_bf16 v[52:55], v[80:83], v[100:103], v[52:55]\n\t"
  "v_fma_f32 v179, v211, v234, v235\n\t"
  "v_fma_f32 v178, v210, v232, v233\n\t"
  "v_mfma_f32_16x16x32_bf16 v[48:51], v[80:83], v[96:99], v[48:51]\n\t"
  "ds_read_b64 v[80:81], %[va] offset:2496\n\t"
  "ds_read_b64 v[82:83], %[va] offset:2504\n\t"
  "v_mfma_f32_16x16x32_bf16 v[60:63], v[84:87], v[104:107], v[60:63]\n\t"
  "buffer_load_dwordx4 a[60:63], %[vl16], %[srd], s50 offen offset:3072 sc1\n\t"
  "v_fma_f32 v177, v209, v230, v231\n\t"
  "v_mfma_f32_16x16x32_bf16 v[56:59], v[84:87], v[100:103], v[56:59]\n\t"
  "v_fma_f32 v176, v208, v228, v229\n\t"
  "ds_read_b64 v[224:225], %[vx] offset:12288\n\t"
  "v_mfma_f32_16x16x32_bf16 v[52:55], v[84:87], v[96:99], v[52:55]\n\t"
  "s_waitcnt lgkmcnt(15)\n\t"
  "ds_read_b64 v[84:85], %[va] offset:3008\n\t"
  "v_mfma_f32_16x16x32_bf16 v[60:63], v[88:91], v[100:103], v[60:63]\n\t"
  "ds_read_b64 v[86:87], %[va] offset:3016\n\t"
  "s_add_u32 s50, s50, 4096\n\t"
  "v_mfma_f32_16x16x32_bf16 v[56:59], v[88:91], v[96:99], v[56:59]\n\t"
  "ds_read_b64 v[88:89], %[va] offset:3520\n\t"
  "ds_read_b64 v[90:91], %[va] offset:3528\n\t"
  "v_mfma_f32_16x16x32_bf16 v[60:63], v[92:95], v[96:99], v[60:63]\n\t"
  "s_waitcnt lgkmcnt(9)\n\t"
  "ds_read_b64 v[92:93], %[va] offset:4032\n\t"
  "v_mfma_f32_16x16x32_bf16 v[60:63], v[64:67], v[156:159], v[60:63]\n\t"
  "ds_read_b64 v[94:95], %[va] offset:4040\n\t"
  "buffer_load_dwordx4 a[64:67], %[vl16], %[srd], s50 offen offset:0 sc1\n\t"
  "v_mfma_f32_16x16x32_bf16 v[56:59], v[64:67], v[152:155], v[56:59]\n\t"
  "ds_read_b128 v[228:231], %[vs] offset:12288\n\t"
  "ds_read_b128 v[232:235], %[vs] offset:12304\n\t"
  "v_mfma_f32_16x16x32_bf16 v[52:55], v[64:67], v[148:151], v[52:55]\n\t"
  "v_fma_f32 v176, v176, v248, v249\n\t"
  "v_mul_f32 v208, s51, v208\n\t"
  "v_mfma_f32_16x16x32_bf16 v[48:51], v[64:67], v[144:147], v[48:51]\n\t"
  "buffer_load_dwordx4 a[68:71], %[vl16], %[srd], s50 offen offset:1024 sc1\n\t"
  "v_fma_f32 v177, v177, v248, v249\n\t"
  "v_mfma_f32_16x16x32_bf16 v[44:47], v[64:67], v[140:143], v[44:47]\n\t"
  "v_mul_f32 v209, s51, v209\n\t"
  "v_fma_f32 v178, v178, v248, v249\n\t"
  "v_mfma_f32_16x16x32_bf16 v[40:43], v[64:67], v[136:139], v[40:43]\n\t"
  "v_mul_f32 v210, s51, v210\n\t"
  "v_fma_f32 v179, v179, v248, v249\n\t"
  "v_mfma_f32_16x16x32_bf16 v[36:39], v[64:67], v[132:135], v[36:39]\n\t"
  "v_mul_f32 v211, s51, v211\n\t"
  "v_lshlrev_b32 v212, 16, v236\n\t"
  "v_mfma_f32_16x16x32_bf16 v[32:35], v[64:67], v[128:131], v[32:35]\n\t"
  "buffer_load_dwordx4 a[72:75], %[vl16], %[srd], s50 offen offset:2048 sc1\n\t"
  "v_and_b32 v213, 0xffff0000, v236\n\t"
  "v_mfma_f32_16x16x32_bf16 v[60:63], v[68:71], v[152:155], v[60:63]\n\t"
  "v_lshlrev_b32 v214, 16, v237\n\t"
  "v_and_b32 v215, 0xffff0000, v237\n\t"
  "v_mfma_f32_16x16x32_bf16 v[56:59], v[68:71], v[148:151], v[56:59]\n\t"
  "v_fma_f32 v183, v215, v246, v247\n\t"
  "v_fma_f32 v182, v214, v244, v245\n\t"
  "v_mfma_f32_16x16x32_bf16 v[52:55], v[68:71], v[144:147], v[52:55]\n\t"
  "buffer_load_dwordx4 a[76:79], %[vl16], %[srd], s50 offen offset:3072 sc1\n\t"
  "v_fma_f32 v181, v213, v242, v243\n\t"
  "v_mfma_f32_16x16x32_bf16 v[48:51], v[68:71], v[140:143], v[48:51]\n\t"
  "v_fma_f32 v180, v212, v240, v241\n\t"
  "ds_read_b64 v[236:237], %[vx] offset:14336\n\t"
  "v_mfma_f32_16x16x32_bf16 v[44:47], v[68:71], v[136:139], v[44:47]\n\t"
  "ds_read_b128 v[240:243], %[vs] offset:14336\n\t"
  "ds_read_b128 v[244:247], %[vs] offset:14352\n\t"
  "v_mfma_f32_16x16x32_bf16 v[40:43], v[68:71], v[132:135], v[40:43]\n\t"
  "v_fma_f32 v180, v180, v248, v249\n\t"
  "v_mul_f32 v212, s51, v212\n\t"
  "v_mfma_f32_16x16x32_bf16 v[36:39], v[68:71], v[128:131], v[36:39]\n\t"
  "s_waitcnt lgkmcnt(9)\n\t"
  "s_add_u32 s50, s50, 4096\n\t"
  "v_mfma_f32_16x16x32_bf16 v[60:63], v[72:75], v[148:151], v[60:63]\n\t"
  "v_fma_f32 v181, v181, v248, v249\n\t"
  "v_mul_f32 v213, s51, v213\n\t"
  "v_mfma_f32_16x16x32_bf16 v[56:59], v[72:75], v[144:147], v[56:59]\n\t"
  "v_fma_f32 v182, v182, v248, v249\n\t"
  "v_mul_f32 v214, s51, v214\n\t"
  "v_mfma_f32_16x16x32_bf16 v[52:55], v[72:75], v[140:143], v[52:55]\n\t"
  "buffer_load_dwordx4 a[80:83], %[vl16], %[srd], s50 offen offset:0 sc1\n\t"
  "v_fma_f32 v183, v183, v248, v249\n\t"
  "v_mfma_f32_16x16x32_bf16 v[48:51], v[72:75], v[136:139], v[48:51]\n\t"
  "v_mul_f32 v215, s51, v215\n\t"
  "v_lshlrev_b32 v216, 16, v224\n\t"
  "v_mfma_f32_16x16x32_bf16 v[44:47], v[72:75], v[132:135], v[44:47]\n\t"
  "v_and_b32 v217, 0xffff0000, v224\n\t"
  "v_lshlrev_b32 v218, 16, v225\n\t"
  "v_mfma_f32_16x16x32_bf16 v[40:43], v[72:75], v[128:131], v[40:43]\n\t"
  "v_and_b32 v219, 0xffff0000, v225\n\t"
  "s_waitcnt lgkmcnt(3)\n\t"
  "v_fma_f32 v187, v219, v234, v235\n\t"
  "v_mfma_f32_16x16x32_bf16 v[60:63], v[76:79], v[144:147], v[60:63]\n\t"
  "buffer_load_dwordx4 a[84:87], %[vl16], %[srd], s50 offen offset:1024 sc1\n\t"
  "v_fma_f32 v186, v218, v232, v233\n\t"
  "v_mfma_f32_16x16x32_bf16 v[56:59], v[76:79], v[140:143], v[56:59]\n\t"
  "v_fma_f32 v185, v217, v230, v231\n\t"
  "v_fma_f32 v184, v216, v228, v229\n\t"
  "v_mfma_f32_16x16x32_bf16 v[52:55], v[76:79], v[136:139], v[52:55]\n\t"
  "v_fma_f32 v184, v184, v248, v249\n\t"
  "v_mul_f32 v216, s51, v216\n\t"
  "v_mfma_f32_16x16x32_bf16 v[48:51], v[76:79], v[132:135], v[48:51]\n\t"
  "buffer_load_dwordx4 a[88:91], %[vl16], %[srd], s50 offen offset:2048 sc1\n\t"
  "v_fma_f32 v185, v185, v248, v249\n\t"
  "v_mfma_f32_16x16x32_bf16 v[44:47], v[76:79], v[128:131], v[44:47]\n\t"
  "v_mul_f32 v217, s51, v217\n\t"
  "v_fma_f32 v186, v186, v248, v249\n\t"
  "v_mfma_f32_16x16x32_bf16 v[60:63], v[80:83], v[140:143], v[60:63]\n\t"
  "v_mul_f32 v218, s51, v218\n\t"
  "v_fma_f32 v187, v187, v248, v249\n\t"
  "v_mfma_f32_16x16x32_bf16 v[56:59], v[80:83], v[136:139], v[56:59]\n\t"
  "v_mul_f32 v219, s51, v219\n\t"
  "s_waitcnt lgkmcnt(2)\n\t"
  "v_lshlrev_b32 v220, 16, v236\n\t"
  "v_mfma_f32_16x16x32_bf16 v[52:55], v[80:83], v[132:135], v[52:55]\n\t"
  "buffer_load_dwordx4 a[92:95], %[vl16], %[srd], s50 offen offset:3072 sc1\n\t"
  "v_and_b32 v221, 0xffff0000, v236\n\t"
  "v_mfma_f32_16x16x32_bf16 v[48:51], v[80:83], v[128:131], v[48:51]\n\t"
  "v_lshlrev_b32 v222, 16, v237\n\t"
  "v_and_b32 v223, 0xffff0000, v237\n\t"
  "v_mfma_f32_16x16x32_bf16 v[60:63], v[84:87], v[136:139], v[60:63]\n\t"
  "s_waitcnt lgkmcnt(0)\n\t"
  "v_fma_f32 v191, v223, v246, v247\n\t"
  "v_mfma_f32_16x16x32_bf16 v[56:59], v[84:87], v[132:135], v[56:59]\n\t"
  "v_fma_f32 v190, v222, v244, v245\n\t"
  "v_fma_f32 v189, v221, v242, v243\n\t"
  "v_mfma_f32_16x16x32_bf16 v[52:55], v[84:87], v[128:131], v[52:55]\n\t"
  "v_fma_f32 v188, v220, v240, v241\n\t"
  "v_fma_f32 v188, v188, v248, v249\n\t"
  "v_mfma_f32_16x16x32_bf16 v[60:63], v[88:91], v[132:135], v[60:63]\n\t"
  "v_mul_f32 v220, s51, v220\n\t"
  "v_fma_f32 v189, v189, v248, v249\n\t"
  "v_mfma_f32_16x16x32_bf16 v[56:59], v[88:91], v[128:131], v[56:59]\n\t"
  "v_mul_f32 v221, s51, v221\n\t"
  "v_fma_f32 v190, v190, v248, v249\n\t"
  "v_mfma_f32_16x16x32_bf16 v[60:63], v[92:95], v[128:131], v[60:63]\n\t"
  "v_mul_f32 v222, s51, v222\n\t"
  "v_fma_f32 v191, v191, v248, v249\n\t"
  "v_mul_f32 v223, s51, v223\n\t"
  "v_pk_fma_f32 v[192:193], v[160:161], v[32:33], v[192:193]\n\t"
  "v_pk_fma_f32 v[194:195], v[162:163], v[34:35], v[194:195]\n\t"
  "v_pk_fma_f32 v[196:197], v[164:165], v[36:37], v[196:197]\n\t"
  "v_pk_fma_f32 v[198:199], v[166:167], v[38:39], v[198:199]\n\t"
  "v_cvt_pk_bf16_f32 v64, v192, v193\n\t"
  "v_cvt_pk_bf16_f32 v66, v196, v197\n\t"
  "v_cvt_pk_bf16_f32 v65, v194, v195\n\t"
  "v_cvt_pk_bf16_f32 v67, v198, v199\n\t"
  "s_add_u32 s44, %[sO], 0\n\t"
  "v_permlane16_swap_b32 v64, v66\n\t"
  "v_permlane16_swap_b32 v65, v67\n\t"
  "buffer_store_dwordx4 v[64:67], %[vo], %[srd], s44 offen sc1\n\t"
  "v_pk_fma_f32 v[200:201], v[168:169], v[40:41], v[200:201]\n\t"
  "v_pk_fma_f32 v[202:203], v[170:171], v[42:43], v[202:203]\n\t"
  "v_pk_fma_f32 v[204:205], v[172:173], v[44:45], v[204:205]\n\t"
  "v_pk_fma_f32 v[206:207], v[174:175], v[46:47], v[206:207]\n\t"
  "v_cvt_pk_bf16_f32 v68, v200, v201\n\t"
  "v_cvt_pk_bf16_f32 v70, v204, v205\n\t"
  "v_cvt_pk_bf16_f32 v69, v202, v203\n\t"
  "v_cvt_pk_bf16_f32 v71, v206, v207\n\t"
  "s_add_u32 s44, %[sO], 655360\n\t"
  "v_permlane16_swap_b32 v68, v70\n\t"
  "v_permlane16_swap_b32 v69, v71\n\t"
  "buffer_store_dwordx4 v[68:71], %[vo], %[srd], s44 offen sc1\n\t"
  "v_pk_fma_f32 v[208:209], v[176:177], v[48:49], v[208:209]\n\t"
  "v_pk_fma_f32 v[210:211], v[178:179], v[50:51], v[210:211]\n\t"
  "v_pk_fma_f32 v[212:213], v[180:181], v[52:53], v[212:213]\n\t"
  "v_pk_fma_f32 v[214:215], v[182:183], v[54:55], v[214:215]\n\t"
  "v_cvt_pk_bf16_f32 v72, v208, v209\n\t"
  "v_cvt_pk_bf16_f32 v74, v212, v213\n\t"
  "v_cvt_pk_bf16_f32 v73, v210, v211\n\t"
  "v_cvt_pk_bf16_f32 v75, v214, v215\n\t"
  "s_add_u32 s44, %[sO], 1310720\n\t"
  "v_permlane16_swap_b32 v72, v74\n\t"
  "v_permlane16_swap_b32 v73, v75\n\t"
  "buffer_store_dwordx4 v[72:75], %[vo], %[srd], s44 offen sc1\n\t"
  "v_pk_fma_f32 v[216:217], v[184:185], v[56:57], v[216:217]\n\t"
  "v_pk_fma_f32 v[218:219], v[186:187], v[58:59], v[218:219]\n\t"
  "v_pk_fma_f32 v[220:221], v[188:189], v[60:61], v[220:221]\n\t"
  "v_pk_fma_f32 v[222:223], v[190:191], v[62:63], v[222:223]\n\t"
  "v_cvt_pk_bf16_f32 v76, v216, v217\n\t"
  "v_cvt_pk_bf16_f32 v78, v220, v221\n\t"
  "v_cvt_pk_bf16_f32 v77, v218, v219\n\t"
  "v_cvt_pk_bf16_f32 v79, v222, v223\n\t"
  "s_add_u32 s44, %[sO], 1966080\n\t"
  "v_permlane16_swap_b32 v76, v78\n\t"
  "v_permlane16_swap_b32 v77, v79\n\t"
  "buffer_store_dwordx4 v[76:79], %[vo], %[srd], s44 offen sc1\n\t"
  "s_nop 1\n\t"
  "s_branch Lcend%=\n\t"
  "La2_slow%=:\n\t"
  "s_nop 7\n\t"
  "s_nop 3\n\t"
  "s_mov_b32 s49, 0x40000\n\t"
  "La2_loop%=:\n\t"
  "s_sleep 8\n\t"
  "buffer_load_dwordx2 v[162:163], %[vf2], %[srd], 0 offen sc1\n\t"
  "s_waitcnt vmcnt(0)\n\t"
  "v_cmp_ne_u64 vcc, v[252:253], v[162:163]\n\t"
  "s_nop 3\n\t"
  "s_cmp_eq_u64 vcc, 0\n\t"
  "s_cbranch_scc1 La2_back%=\n\t"
  "s_sub_u32 s49, s49, 1\n\t"
  "s_cmp_lg_u32 s49, 0\n\t"
  "s_cbranch_scc1 La2_loop%=\n\t"
  "s_branch La2_back%=\n\t"
  "Lcend%=:\n\t"
      : [wdone] "=&s"(wdone)
      : [srd] "s"(srd), [sO] "s"(OFF_X2P + d * 128u), [w] "s"(w), [sKV] "s"(OFF_KVT + cb * 128u + w * 655360u),
        [tlo] "s"((u32)tag), [thi] "s"((u32)(tag >> 32)), [sW1] "s"(OFF_W1F + (j * 4 + w) * 61440u),
        [vf2] "v"(lane < 32 ? OFF_FA3 + (32 * w + lane) * 128 : OFF_FA2 + ((lane & 3) * 60 + (mcol >> 6)) * 128),
        [vf3] "v"(tid < 8 && cb < 128 ? OFF_FA3 + cb * 128 + lane * 16 : OOB), [vfw] "v"(OFF_FW1 + (j * 4 + (lane & 3)) * 128),
        [va] "v"(va), [vb0] "v"(vb[0]), [vb1] "v"(vb[1]), [vb2] "v"(vb[2]), [vb3] "v"(vb[3]), [vx] "v"(vx), [vs] "v"(vs), [vo] "v"(vo), [vd] "v"(vd),
        [vl16] "v"(lane * 16), [vkv] "v"(((lane >> 1) & 7) * 20480 + ((lane >> 4) + 4 * (lane & 1)) * 16),
        [vmod] "v"(OFF_MODP + ((lane & 3) * 3840 + mcol) * 4)
      : "memory", "vcc", "scc",
"a0", "a1", "a2", "a3", "a4", "a5", "a6", "a7", "a8", "a9", "a10", "a11", "a12", "a13", "a14", "a15", "a16", "a17", "a18", "a19", "a20", "a21", "a22", "a23", "a24", "a25", "a26", "a27", "a28", "a29", "a30", "a31", "a32", "a33", "a34", "a35", "a36", "a37", "a38", "a39", "a40", "a41", "a42", "a43", "a44", "a45", "a46", "a47", "a48", "a49", "a50", "a51", "a52", "a53", "a54", "a55", "a56", "a57", "a58", "a59", "a60", "a61", "a62", "a63", "a64", "a65", "a66", "a67", "a68", "a69", "a70", "a71", "a72", "a73", "a74", "a75", "a76", "a77", "a78", "a79", "a80", "a81", "a82", "a83", "a84", "a85", "a86", "a87", "a88", "a89", "a90", "a91", "a92", "a93", "a94", "a95", "s44", "s45", "s46", "s47", "s48", "s49", "s50", "s51", "s52", "s54", "s55", "v32", "v33", "v34", "v35", "v36", "v37", "v38", "v39", "v40", "v41", "v42", "v43", "v44", "v45", "v46", "v47", "v48", "v49", "v50", "v51", "v52", "v53", "v54", "v55", "v56", "v57", "v58", "v59", "v60", "v61", "v62", "v63", "v64", "v65", "v66", "v67", "v68", "v69", "v70", "v71", "v72", "v73", "v74", "v75", "v76", "v77", "v78", "v79", "v80", "v81", "v82", "v83", "v84", "v85", "v86", "v87", "v88", "v89", "v90", "v91", "v92", "v93", "v94", "v95", "v96", "v97", "v98", "v99", "v100", "v101", "v102", "v103", "v104", "v105", "v106", "v107", "v108", "v109", "v110", "v111", "v112", "v113", "v114", "v115", "v116", "v117", "v118", "v119", "v120", "v121", "v122", "v123", "v124", "v125", "v126", "v127", "v128", "v129", "v130", "v131", "v132", "v133", "v134", "v135", "v136", "v137", "v138", "v139", "v140", "v141", "v142", "v143", "v144", "v145", "v146", "v147", "v148", "v149", "v150", "v151", "v152", "v153", "v154", "v155", "v156", "v157", "v158", "v159", "v160", "v161", "v162", "v163", "v164", "v165", "v166", "v167", "v168", "v169", "v170", "v171", "v172", "v173", "v174", "v175", "v176", "v177", "v178", "v179", "v180", "v181", "v182", "v183", "v184", "v185", "v186", "v187", "v188", "v189", "v190", "v191", "v192", "v193", "v194", "v195", "v196", "v197", "v198", "v199", "v200", "v201", "v202", "v203", "v204", "v205", "v206", "v207", "v208", "v209", "v210", "v211", "v212", "v213", "v214", "v215", "v216", "v217", "v218", "v219", "v220", "v221", "v222", "v223", "v224", "v225", "v228", "v229", "v230", "v231", "v232", "v233", "v234", "v235", "v236", "v237", "v240", "v241", "v242", "v243", "v244", "v245", "v246", "v247", "v248", "v249", "v250", "v251", "v252", "v253", "v254", "v255"
    );
  }
  STAMP(4);
  STAMP_OUT(1);
  const unsigned vpub = tid < 8 ? (loc < 20 ? OFF_FX + cb * 128 : loc < 28 ? OFF_FU + w1 * 128 : OFF_FW2 + w2 * 128) + tid * 16 : OOB;
  const unsigned vpw = lane < 4 ? OFF_FW1 + (j * 4 + lane) * 128 : OFF_FW2 + (ng * 4 + w) * 128;
  const unsigned vpx = lane < 40 ? OFF_FX + (32 * (lane >> 3) + 8 * w + (lane & 7)) * 128 : OFF_FU + ((lane & 4 ? j * 4 : 0) + (lane & 3)) * 128;
  const unsigned g = lane >> 4, q = (lane >> 2) & 3, pq = lane & 3;
  const unsigned row = 4 * g + q;
  const unsigned vtr = lb + row * 128 + ((row >> 1) & 3) * 32 + ((pq * 8) ^ ((row & 8) * 2));
  const unsigned vl16 = lane * 16;
  const unsigned vdma = OFF_X2P + p * 81920u + w * 4096u + (lane >> 3) * 128 + (((lane & 7) ^ (((lane >> 4) & 3) << 1)) * 16);
  const unsigned vh2st = lane < 48 ? OFF_H2F + (4 * p * 192 + 12 * j + 3 * w) * 256u + lane * 16 : OOB;
  const unsigned tk = 4 * j + w;
  const unsigned vstc = lb + lane * 128 + (((tk >> 4) ^ ((lane >> 1) & 3)) * 32) + (((tk & 15) * 2) ^ ((lane & 8) * 2));
  const unsigned vstw = lane == 63 ? tk * 64 : OOB;
  const unsigned vflw = lane < 8 ? OFF_FL + p * 32768u + (j * 4 + w) * 128u + lane * 16 : OOB;
  const unsigned vep = b == 0 && tid == 0 ? OFF_EP : OOB;
  const unsigned rs_ = 2 * (w & 1) + sub, rb = lb + (w >> 1) * 81920 + 80 * ng * 128;
  const unsigned r0 = 8 * g + q, r1 = 8 * g + 4 + q, r2 = 64 + 4 * g + q;
  const unsigned vres0 = rb + r0 * 128 + ((rs_ ^ ((r0 >> 1) & 3)) * 32) + ((pq * 8) ^ ((r0 & 8) * 2));
  const unsigned vres1 = rb + r1 * 128 + ((rs_ ^ ((r1 >> 1) & 3)) * 32) + ((pq * 8) ^ ((r1 & 8) * 2));
  const unsigned vres2 = rb + r2 * 128 + ((rs_ ^ ((r2 >> 1) & 3)) * 32) + ((pq * 8) ^ ((r2 & 8) * 2));
  const unsigned vwr = lb + w * 20480 + g * 256 + ((lane + 4 * g) & 15) * 16;
  const unsigned tq = lane < 32 ? q : (lane >> 1) & 3, tl = 4 * w + tq, g0 = lane < 32 ? pq : 2 * (lane & 1), g1 = lane < 32 ? pq : g0 + 1;
  const unsigned vrd0 = lb + (lane < 32 ? g * 2048 : 4096) + g0 * 256 + ((tl + 4 * g0) & 15) * 16;
  const unsigned vrd1 = lb + (lane < 32 ? g * 2048 + 1024 : 4096) + g1 * 256 + ((tl + 4 * g1) & 15) * 16;
  const unsigned vout = lane < 40 ? ((64 * p + 16 * sub + tl) * D_ + 80 * ng + (lane < 32 ? 32 * g + 8 * pq : 64 + 8 * (lane & 1))) * 2 : OOB;
  asm volatile(
  "v_mov_b32 v132, %[tlo]\n\t"
  "v_mov_b32 v133, %[thi]\n\t"
  "v_mov_b32 v134, %[tlo]\n\t"
  "v_mov_b32 v135, %[thi]\n\t"
  "v_xor_b32 v140, 16, %[vdma]\n\t"
  "s_lshl_b32 s52, %[w], 12\n\t"
  "s_waitcnt vmcnt(0) lgkmcnt(0)\n\t"
  "s_barrier\n\t"
  "buffer_store_dwordx4 v[132:135], %[vpub], %[srd], 0 offen sc1\n\t"
  "s_cmp_lg_u32 %[wdone], 0\n\t"
  "s_cbranch_scc1 Lwskip%=\n\t"
  "s_mov_b32 s49, 0x40000\n\t"
  "Lpw_loop%=:\n\t"
  "buffer_load_dwordx2 v[112:113], %[vpw], %[srd], 0 offen sc1\n\t"
  "s_waitcnt vmcnt(0)\n\t"
  "v_cmp_eq_u64 vcc, v[132:133], v[112:113]\n\t"
  "s_nop 3\n\t"
  "s_orn2_b64 vcc, vcc, 15\n\t"
  "s_cmp_eq_u64 vcc, -1\n\t"
  "s_cbranch_scc1 Lpw_done%=\n\t"
  "s_sleep 2\n\t"
  "s_sub_u32 s49, s49, 1\n\t"
  "s_cmp_lg_u32 s49, 0\n\t"
  "s_cbranch_scc1 Lpw_loop%=\n\t"
  "Lpw_done%=:\n\t"
  "s_add_u32 s44, %[sW1], 0\n\t"
  "buffer_load_dwordx4 a[0:3], %[vl16], %[srd], s44 offen offset:0 sc1\n\t"
  "s_add_u32 s44, %[sW1], 0\n\t"
  "buffer_load_dwordx4 a[4:7], %[vl16], %[srd], s44 offen offset:1024 sc1\n\t"
  "s_add_u32 s44, %[sW1], 0\n\t"
  "buffer_load_dwordx4 a[8:11], %[vl16], %[srd], s44 offen offset:2048 sc1\n\t"
  "s_add_u32 s44, %[sW1], 0\n\t"
  "buffer_load_dwordx4 a[12:15], %[vl16], %[srd], s44 offen offset:3072 sc1\n\t"
  "s_add_u32 s44, %[sW1], 4096\n\t"
  "buffer_load_dwordx4 a[16:19], %[vl16], %[srd], s44 offen offset:0 sc1\n\t"
  "s_add_u32 s44, %[sW1], 4096\n\t"
  "buffer_load_dwordx4 a[20:23], %[vl16], %[srd], s44 offen offset:1024 sc1\n\t"
  "s_add_u32 s44, %[sW1], 4096\n\t"
  "buffer_load_dwordx4 a[24:27], %[vl16], %[srd], s44 offen offset:2048 sc1\n\t"
  "s_add_u32 s44, %[sW1], 4096\n\t"
  "buffer_load_dwordx4 a[28:31], %[vl16], %[srd], s44 offen offset:3072 sc1\n\t"
  "s_add_u32 s44, %[sW1], 8192\n\t"
  "buffer_load_dwordx4 a[32:35], %[vl16], %[srd], s44 offen offset:0 sc1\n\t"
  "s_add_u32 s44, %[sW1], 8192\n\t"
  "buffer_load_dwordx4 a[36:39], %[vl16], %[srd], s44 offen offset:1024 sc1\n\t"
  "s_add_u32 s44, %[sW1], 8192\n\t"
  "buffer_load_dwordx4 a[40:43], %[vl16], %[srd], s44 offen offset:2048 sc1\n\t"
  "s_add_u32 s44, %[sW1], 8192\n\t"
  "buffer_load_dwordx4 a[44:47], %[vl16], %[srd], s44 offen offset:3072 sc1\n\t"
  "s_add_u32 s44, %[sW1], 12288\n\t"
  "buffer_load_dwordx4 a[48:51], %[vl16], %[srd], s44 offen offset:0 sc1\n\t"
  "s_add_u32 s44, %[sW1], 12288\n\t"
  "buffer_load_dwordx4 a[52:55], %[vl16], %[srd], s44 offen offset:1024 sc1\n\t"
  "s_add_u32 s44, %[sW1], 12288\n\t"
  "buffer_load_dwordx4 a[56:59], %[vl16], %[srd], s44 offen offset:2048 sc1\n\t"
  "s_add_u32 s44, %[sW1], 12288\n\t"
  "buffer_load_dwordx4 a[60:63], %[vl16], %[srd], s44 offen offset:3072 sc1\n\t"
  "s_add_u32 s44, %[sW1], 16384\n\t"
  "buffer_load_dwordx4 a[64:67], %[vl16], %[srd], s44 offen offset:0 sc1\n\t"
  "s_add_u32 s44, %[sW1], 16384\n\t"
  "buffer_load_dwordx4 a[68:71], %[vl16], %[srd], s44 offen offset:1024 sc1\n\t"
  "s_add_u32 s44, %[sW1], 16384\n\t"
  "buffer_load_dwordx4 a[72:75], %[vl16], %[srd], s44 offen offset:2048 sc1\n\t"
  "s_add_u32 s44, %[sW1], 16384\n\t"
  "buffer_load_dwordx4 a[76:79], %[vl16], %[srd], s44 offen offset:3072 sc1\n\t"
  "s_add_u32 s44, %[sW1], 20480\n\t"
  "buffer_load_dwordx4 a[80:83], %[vl16], %[srd], s44 offen offset:0 sc1\n\t"
  "s_add_u32 s44, %[sW1], 20480\n\t"
  "buffer_load_dwordx4 a[84:87], %[vl16], %[srd], s44 offen offset:1024 sc1\n\t"
  "s_add_u32 s44, %[sW1], 20480\n\t"
  "buffer_load_dwordx4 a[88:91], %[vl16], %[srd], s44 offen offset:2048 sc1\n\t"
  "s_add_u32 s44, %[sW1], 20480\n\t"
  "buffer_load_dwordx4 a[92:95], %[vl16], %[srd], s44 offen offset:3072 sc1\n\t"
  "Lwskip%=:\n\t"
  "s_add_u32 s44, %[sW1], 24576\n\t"
  "buffer_load_dwordx4 a[96:99], %[vl16], %[srd], s44 offen offset:0 sc1\n\t"
  "s_add_u32 s44, %[sW1], 24576\n\t"
  "buffer_load_dwordx4 a[100:103], %[vl16], %[srd], s44 offen offset:1024 sc1\n\t"
  "s_add_u32 s44, %[sW1], 24576\n\t"
  "buffer_load_dwordx4 a[104:107], %[vl16], %[srd], s44 offen offset:2048 sc1\n\t"
  "s_add_u32 s44, %[sW1], 24576\n\t"
  "buffer_load_dwordx4 a[108:111], %[vl16], %[srd], s44 offen offset:3072 sc1\n\t"
  "s_add_u32 s44, %[sW1], 28672\n\t"
  "buffer_load_dwordx4 a[112:115], %[vl16], %[srd], s44 offen offset:0 sc1\n\t"
  "s_add_u32 s44, %[sW1], 28672\n\t"
  "buffer_load_dwordx4 a[116:119], %[vl16], %[srd], s44 offen offset:1024 sc1\n\t"
  "s_add_u32 s44, %[sW1], 28672\n\t"
  "buffer_load_dwordx4 a[120:123], %[vl16], %[srd], s44 offen offset:2048 sc1\n\t"
  "s_add_u32 s44, %[sW1], 28672\n\t"
  "buffer_load_dwordx4 a[124:127], %[vl16], %[srd], s44 offen offset:3072 sc1\n\t"
  "s_add_u32 s44, %[sW1], 32768\n\t"
  "buffer_load_dwordx4 a[128:131], %[vl16], %[srd], s44 offen offset:0 sc1\n\t"
  "s_add_u32 s44, %[sW1], 32768\n\t"
  "buffer_load_dwordx4 a[132:135], %[vl16], %[srd], s44 offen offset:1024 sc1\n\t"
  "s_add_u32 s44, %[sW1], 32768\n\t"
  "buffer_load_dwordx4 a[136:139], %[vl16], %[srd], s44 offen offset:2048 sc1\n\t"
  "s_add_u32 s44, %[sW1], 32768\n\t"
  "buffer_load_dwordx4 a[140:143], %[vl16], %[srd], s44 offen offset:3072 sc1\n\t"
  "s_add_u32 s44, %[sW1], 36864\n\t"
  "buffer_load_dwordx4 a[144:147], %[vl16], %[srd], s44 offen offset:0 sc1\n\t"
  "s_add_u32 s44, %[sW1], 36864\n\t"
  "buffer_load_dwordx4 a[148:151], %[vl16], %[srd], s44 offen offset:1024 sc1\n\t"
  "s_add_u32 s44, %[sW1], 36864\n\t"
  "buffer_load_dwordx4 a[152:155], %[vl16], %[srd], s44 offen offset:2048 sc1\n\t"
  "s_add_u32 s44, %[sW1], 36864\n\t"
  "buffer_load_dwordx4 a[156:159], %[vl16], %[srd], s44 offen offset:3072 sc1\n\t"
  "s_add_u32 s44, %[sW1], 40960\n\t"
  "buffer_load_dwordx4 a[160:163], %[vl16], %[srd], s44 offen offset:0 sc1\n\t"
  "s_add_u32 s44, %[sW1], 40960\n\t"
  "buffer_load_dwordx4 a[164:167], %[vl16], %[srd], s44 offen offset:1024 sc1\n\t"
  "s_add_u32 s44, %[sW1], 40960\n\t"
  "buffer_load_dwordx4 a[168:171], %[vl16], %[srd], s44 offen offset:2048 sc1\n\t"
  "s_add_u32 s44, %[sW1], 40960\n\t"
  "buffer_load_dwordx4 a[172:175], %[vl16], %[srd], s44 offen offset:3072 sc1\n\t"
  "s_add_u32 s44, %[sW1], 45056\n\t"
  "buffer_load_dwordx4 a[176:179], %[vl16], %[srd], s44 offen offset:0 sc1\n\t"
  "s_add_u32 s44, %[sW1], 45056\n\t"
  "buffer_load_dwordx4 a[180:183], %[vl16], %[srd], s44 offen offset:1024 sc1\n\t"
  "s_add_u32 s44, %[sW1], 45056\n\t"
  "buffer_load_dwordx4 a[184:187], %[vl16], %[srd], s44 offen offset:2048 sc1\n\t"
  "s_add_u32 s44, %[sW1], 45056\n\t"
  "buffer_load_dwordx4 a[188:191], %[vl16], %[srd], s44 offen offset:3072 sc1\n\t"
  "s_add_u32 s44, %[sW1], 49152\n\t"
  "buffer_load_dwordx4 a[192:195], %[vl16], %[srd], s44 offen offset:0 sc1\n\t"
  "s_add_u32 s44, %[sW1], 49152\n\t"
  "buffer_load_dwordx4 a[196:199], %[vl16], %[srd], s44 offen offset:1024 sc1\n\t"
  "s_add_u32 s44, %[sW1], 49152\n\t"
  "buffer_load_dwordx4 a[200:203], %[vl16], %[srd], s44 offen offset:2048 sc1\n\t"
  "s_add_u32 s44, %[sW1], 49152\n\t"
  "buffer_load_dwordx4 a[204:207], %[vl16], %[srd], s44 offen offset:3072 sc1\n\t"
  "s_add_u32 s44, %[sW1], 53248\n\t"
  "buffer_load_dwordx4 a[208:211], %[vl16], %[srd], s44 offen offset:0 sc1\n\t"
  "s_add_u32 s44, %[sW1], 53248\n\t"
  "buffer_load_dwordx4 a[212:215], %[vl16], %[srd], s44 offen offset:1024 sc1\n\t"
  "s_add_u32 s44, %[sW1], 53248\n\t"
  "buffer_load_dwordx4 a[216:219], %[vl16], %[srd], s44 offen offset:2048 sc1\n\t"
  "s_add_u32 s44, %[sW1], 53248\n\t"
  "buffer_load_dwordx4 a[220:223], %[vl16], %[srd], s44 offen offset:3072 sc1\n\t"
  "s_add_u32 s44, %[sW1], 57344\n\t"
  "buffer_load_dwordx4 a[224:227], %[vl16], %[srd], s44 offen offset:0 sc1\n\t"
  "s_add_u32 s44, %[sW1], 57344\n\t"
  "buffer_load_dwordx4 a[228:231], %[vl16], %[srd], s44 offen offset:1024 sc1\n\t"
  "s_add_u32 s44, %[sW1], 57344\n\t"
  "buffer_load_dwordx4 a[232:235], %[vl16], %[srd], s44 offen offset:2048 sc1\n\t"
  "s_add_u32 s44, %[sW1], 57344\n\t"
  "buffer_load_dwordx4 a[236:239], %[vl16], %[srd], s44 offen offset:3072 sc1\n\t"
  "v_lshrrev_b32 v138, 1, %[vl16]\n\t"
  "v_mul_u32_u24 v139, 3, %[vl16]\n\t"
  "s_mov_b32 s47, 115200\n\t"
  "s_mov_b32 s49, 0x40000\n\t"
  "Lpx_loop%=:\n\t"
  "buffer_load_dwordx2 v[112:113], %[vpx], %[srd], 0 offen sc1\n\t"
  "s_waitcnt vmcnt(0)\n\t"
  "v_cmp_eq_u64 vcc, v[132:133], v[112:113]\n\t"
  "s_nop 3\n\t"
  "s_cmp_eq_u64 vcc, -1\n\t"
  "s_cbranch_scc1 Lpx_done%=\n\t"
  "s_sleep 2\n\t"
  "s_sub_u32 s49, s49, 1\n\t"
  "s_cmp_lg_u32 s49, 0\n\t"
  "s_cbranch_scc1 Lpx_loop%=\n\t"
  "Lpx_done%=:\n\t"
  "s_add_u32 m0, s52, 0\n\t"
  "s_mov_b32 s46, 0\n\t"
  "buffer_load_dwordx4 %[vdma], %[srd], s46 offen offset:0 sc1 lds\n\t"
  "buffer_load_dwordx4 v140, %[srd], s46 offen offset:1024 sc1 lds\n\t"
  "buffer_load_dwordx4 %[vdma], %[srd], s46 offen offset:2048 sc1 lds\n\t"
  "buffer_load_dwordx4 v140, %[srd], s46 offen offset:3072 sc1 lds\n\t"
  "s_add_u32 m0, s52, 16384\n\t"
  "s_mov_b32 s46, 16384\n\t"
  "buffer_load_dwordx4 %[vdma], %[srd], s46 offen offset:0 sc1 lds\n\t"
  "buffer_load_dwordx4 v140, %[srd], s46 offen offset:1024 sc1 lds\n\t"
  "buffer_load_dwordx4 %[vdma], %[srd], s46 offen offset:2048 sc1 lds\n\t"
  "buffer_load_dwordx4 v140, %[srd], s46 offen offset:3072 sc1 lds\n\t"
  "s_add_u32 m0, s52, 32768\n\t"
  "s_mov_b32 s46, 32768\n\t"
  "buffer_load_dwordx4 %[vdma], %[srd], s46 offen offset:0 sc1 lds\n\t"
  "buffer_load_dwordx4 v140, %[srd], s46 offen offset:1024 sc1 lds\n\t"
  "buffer_load_dwordx4 %[vdma], %[srd], s46 offen offset:2048 sc1 lds\n\t"
  "buffer_load_dwordx4 v140, %[srd], s46 offen offset:3072 sc1 lds\n\t"
  "s_add_u32 m0, s52, 49152\n\t"
  "s_mov_b32 s46, 49152\n\t"
  "buffer_load_dwordx4 %[vdma], %[srd], s46 offen offset:0 sc1 lds\n\t"
  "buffer_load_dwordx4 v140, %[srd], s46 offen offset:1024 sc1 lds\n\t"
  "buffer_load_dwordx4 %[vdma], %[srd], s46 offen offset:2048 sc1 lds\n\t"
  "buffer_load_dwordx4 v140, %[srd], s46 offen offset:3072 sc1 lds\n\t"
  "s_add_u32 m0, s52, 65536\n\t"
  "s_mov_b32 s46, 65536\n\t"
  "buffer_load_dwordx4 %[vdma], %[srd], s46 offen offset:0 sc1 lds\n\t"
  "buffer_load_dwordx4 v140, %[srd], s46 offen offset:1024 sc1 lds\n\t"
  "buffer_load_dwordx4 %[vdma], %[srd], s46 offen offset:2048 sc1 lds\n\t"
  "buffer_load_dwordx4 v140, %[srd], s46 offen offset:3072 sc1 lds\n\t"
  "buffer_load_dwordx2 v[142:143], v139, %[srd], s47 offen sc1\n\t"
  "buffer_load_dwordx4 v[248:251], v139, %[srd], s47 offen offset:16 sc1\n\t"
  "buffer_load_dwordx4 v[252:255], v139, %[srd], s47 offen offset:32 sc1\n\t"
  "buffer_load_dwordx2 a[240:241], v138, %[srd], %[sUB] offen offset:0 sc1\n\t"
  "buffer_load_dwordx2 a[242:243], v138, %[srd], %[sUB] offen offset:512 sc1\n\t"
  "buffer_load_dwordx2 a[244:245], v138, %[srd], %[sUB] offen offset:1024 sc1\n\t"
  "v_add_u32 v124, 40960, %[vstc]\n\t"
  "v_mov_b32 v112, 0\n\t"
  "v_mov_b32 v113, 0\n\t"
  "v_mov_b32 v114, 0\n\t"
  "v_mov_b32 v115, 0\n\t"
  "v_mov_b32 v116, 0\n\t"
  "v_mov_b32 v117, 0\n\t"
  "v_mov_b32 v118, 0\n\t"
  "v_mov_b32 v119, 0\n\t"
  "v_mov_b32 v120, 0\n\t"
  "v_mov_b32 v121, 0\n\t"
  "v_and_b32 v136, 0xf0, %[vl16]\n\t"
  "v_lshlrev_b32 v137, 3, v136\n\t"
  "v_lshlrev_b32 v136, 2, v136\n\t"
  "s_add_u32 s54, %[sST], 65536\n\t"
  "s_mul_i32 s48, %[w], 20480\n\t"
  "v_add_u32 v141, s48, %[vl16]\n\t"
  "v_xor_b32 v109, 32, %[vtr]\n\t"
  "v_mov_b32 v108, %[vtr]\n\t"
  "s_waitcnt vmcnt(6)\n\t"
  "s_barrier\n\t"
  "ds_read_b64_tr_b16 v[68:69], v108 offset:0\n\t"
  "ds_read_b64_tr_b16 v[70:71], v108 offset:2048\n\t"
  "ds_read_b64_tr_b16 v[72:73], v109 offset:0\n\t"
  "ds_read_b64_tr_b16 v[74:75], v109 offset:2048\n\t"
  "ds_read_b64_tr_b16 v[76:77], v108 offset:4096\n\t"
  "ds_read_b64_tr_b16 v[78:79], v108 offset:6144\n\t"
  "ds_read_b64_tr_b16 v[80:81], v109 offset:4096\n\t"
  "ds_read_b64_tr_b16 v[82:83], v109 offset:6144\n\t"
  "ds_read_u16_d16_hi v112, %[vstc] offset:0\n\t"
  "ds_read_u16_d16_hi v113, %[vstc] offset:8192\n\t"
  "ds_read_u16_d16_hi v114, %[vstc] offset:16384\n\t"
  "ds_read_u16_d16_hi v115, %[vstc] offset:24576\n\t"
  "ds_read_u16_d16_hi v116, %[vstc] offset:32768\n\t"
  "ds_read_u16_d16_hi v117, v124 offset:0\n\t"
  "ds_read_u16_d16_hi v118, v124 offset:8192\n\t"
  "ds_read_u16_d16_hi v119, v124 offset:16384\n\t"
  "ds_read_u16_d16_hi v120, v124 offset:24576\n\t"
  "ds_read_u16_d16_hi v121, v124 offset:32768\n\t"
  "s_waitcnt lgkmcnt(15)\n\t"
  "v_mfma_f32_16x16x32_bf16 v[20:23], a[0:3], v[68:71], 0\n\t"
  "ds_read_b64_tr_b16 v[84:85], v108 offset:8192\n\t"
  "v_add_u32 v110, 40960, %[vtr]\n\t"
  "v_mfma_f32_16x16x32_bf16 v[24:27], a[4:7], v[68:71], 0\n\t"
  "ds_read_b64_tr_b16 v[86:87], v108 offset:10240\n\t"
  "v_xor_b32 v111, 32, %[vtr]\n\t"
  "v_mfma_f32_16x16x32_bf16 v[28:31], a[8:11], v[68:71], 0\n\t"
  "ds_read_b64_tr_b16 v[88:89], v109 offset:8192\n\t"
  "v_add_u32 v111, 40960, v111\n\t"
  "s_waitcnt lgkmcnt(15)\n\t"
  "v_mfma_f32_16x16x32_bf16 v[32:35], a[0:3], v[72:75], 0\n\t"
  "ds_read_b64_tr_b16 v[90:91], v109 offset:10240\n\t"
  "s_add_u32 m0, s52, 81920\n\t"
  "v_mfma_f32_16x16x32_bf16 v[36:39], a[4:7], v[72:75], 0\n\t"
  "s_mov_b32 s46, 1310720\n\t"
  "v_mfma_f32_16x16x32_bf16 v[40:43], a[8:11], v[72:75], 0\n\t"
  "s_waitcnt lgkmcnt(14)\n\t"
  "buffer_load_dwordx4 %[vdma], %[srd], s46 offen offset:0 sc1 lds\n\t"
  "v_mfma_f32_16x16x32_bf16 v[20:23], a[12:15], v[76:79], v[20:23]\n\t"
  "ds_read_b64_tr_b16 v[68:69], v108 offset:12288\n\t"
  "v_mfma_f32_16x16x32_bf16 v[24:27], a[16:19], v[76:79], v[24:27]\n\t"
  "ds_read_b64_tr_b16 v[70:71], v108 offset:14336\n\t"
  "v_mfma_f32_16x16x32_bf16 v[28:31], a[20:23], v[76:79], v[28:31]\n\t"
  "ds_read_b64_tr_b16 v[72:73], v109 offset:12288\n\t"
  "buffer_load_dwordx4 v140, %[srd], s46 offen offset:1024 sc1 lds\n\t"
  "v_mfma_f32_16x16x32_bf16 v[32:35], a[12:15], v[80:83], v[32:35]\n\t"
  "ds_read_b64_tr_b16 v[74:75], v109 offset:14336\n\t"
  "v_mfma_f32_16x16x32_bf16 v[36:39], a[16:19], v[80:83], v[36:39]\n\t"
  "v_mfma_f32_16x16x32_bf16 v[40:43], a[20:23], v[80:83], v[40:43]\n\t"
  "s_waitcnt lgkmcnt(4)\n\t"
  "buffer_load_dwordx4 %[vdma], %[srd], s46 offen offset:2048 sc1 lds\n\t"
  "v_mfma_f32_16x16x32_bf16 v[20:23], a[24:27], v[84:87], v[20:23]\n\t"
  "ds_read_b64_tr_b16 v[76:77], v108 offset:16384\n\t"
  "s_waitcnt vmcnt(8)\n\t"
  "v_mul_f32 v112, v112, v142\n\t"
  "v_mfma_f32_16x16x32_bf16 v[24:27], a[28:31], v[84:87], v[24:27]\n\t"
  "ds_read_b64_tr_b16 v[78:79], v108 offset:18432\n\t"
  "v_mul_f32 v113, v113, v143\n\t"
  "v_mfma_f32_16x16x32_bf16 v[28:31], a[32:35], v[84:87], v[28:31]\n\t"
  "ds_read_b64_tr_b16 v[80:81], v109 offset:16384\n\t"
  "buffer_load_dwordx4 v140, %[srd], s46 offen offset:3072 sc1 lds\n\t"
  "v_mfma_f32_16x16x32_bf16 v[32:35], a[24:27], v[88:91], v[32:35]\n\t"
  "ds_read_b64_tr_b16 v[82:83], v109 offset:18432\n\t"
  "s_waitcnt vmcnt(8)\n\t"
  "v_mul_f32 v114, v114, v248\n\t"
  "v_mfma_f32_16x16x32_bf16 v[36:39], a[28:31], v[88:91], v[36:39]\n\t"
  "s_add_u32 m0, s52, 98304\n\t"
  "v_mul_f32 v115, v115, v249\n\t"
  "v_mfma_f32_16x16x32_bf16 v[40:43], a[32:35], v[88:91], v[40:43]\n\t"
  "s_waitcnt lgkmcnt(4)\n\t"
  "v_mul_f32 v116, v116, v250\n\t"
  "v_mfma_f32_16x16x32_bf16 v[20:23], a[36:39], v[68:71], v[20:23]\n\t"
  "ds_read_b64_tr_b16 v[84:85], v108 offset:20480\n\t"
  "v_mul_f32 v117, v117, v251\n\t"
  "v_mfma_f32_16x16x32_bf16 v[24:27], a[40:43], v[68:71], v[24:27]\n\t"
  "ds_read_b64_tr_b16 v[86:87], v108 offset:22528\n\t"
  "s_mov_b32 s46, 1327104\n\t"
  "v_mfma_f32_16x16x32_bf16 v[28:31], a[44:47], v[68:71], v[28:31]\n\t"
  "ds_read_b64_tr_b16 v[88:89], v109 offset:20480\n\t"
  "s_waitcnt vmcnt(7)\n\t"
  "v_mul_f32 v118, v118, v252\n\t"
  "v_mfma_f32_16x16x32_bf16 v[32:35], a[36:39], v[72:75], v[32:35]\n\t"
  "ds_read_b64_tr_b16 v[90:91], v109 offset:22528\n\t"
  "v_mul_f32 v119, v119, v253\n\t"
  "v_mfma_f32_16x16x32_bf16 v[36:39], a[40:43], v[72:75], v[36:39]\n\t"
  "buffer_load_dwordx4 %[vdma], %[srd], s46 offen offset:0 sc1 lds\n\t"
  "v_mul_f32 v120, v120, v254\n\t"
  "v_mfma_f32_16x16x32_bf16 v[40:43], a[44:47], v[72:75], v[40:43]\n\t"
  "s_waitcnt lgkmcnt(4)\n\t"
  "v_mul_f32 v121, v121, v255\n\t"
  "v_mfma_f32_16x16x32_bf16 v[20:23], a[48:51], v[76:79], v[20:23]\n\t"
  "ds_read_b64_tr_b16 v[68:69], v108 offset:24576\n\t"
  "v_add_f32 v122, v112, v113\n\t"
  "v_mfma_f32_16x16x32_bf16 v[24:27], a[52:55], v[76:79], v[24:27]\n\t"
  "ds_read_b64_tr_b16 v[70:71], v108 offset:26624\n\t"
  "buffer_load_dwordx4 v140, %[srd], s46 offen offset:1024 sc1 lds\n\t"
  "v_mfma_f32_16x16x32_bf16 v[28:31], a[56:59], v[76:79], v[28:31]\n\t"
  "ds_read_b64_tr_b16 v[72:73], v109 offset:24576\n\t"
  "v_mul_f32 v123, v112, v112\n\t"
  "v_mfma_f32_16x16x32_bf16 v[32:35], a[48:51], v[80:83], v[32:35]\n\t"
  "ds_read_b64_tr_b16 v[74:75], v109 offset:26624\n\t"
  "v_fmac_f32 v123, v113, v113\n\t"
  "v_mfma_f32_16x16x32_bf16 v[36:39], a[52:55], v[80:83], v[36:39]\n\t"
  "buffer_load_dwordx4 %[vdma], %[srd], s46 offen offset:2048 sc1 lds\n\t"
  "v_add_f32 v122, v122, v114\n\t"
  "v_mfma_f32_16x16x32_bf16 v[40:43], a[56:59], v[80:83], v[40:43]\n\t"
  "s_waitcnt lgkmcnt(4)\n\t"
  "v_fmac_f32 v123, v114, v114\n\t"
  "v_mfma_f32_16x16x32_bf16 v[20:23], a[60:63], v[84:87], v[20:23]\n\t"
  "ds_read_b64_tr_b16 v[76:77], v108 offset:28672\n\t"
  "buffer_load_dwordx4 v140, %[srd], s46 offen offset:3072 sc1 lds\n\t"
  "v_mfma_f32_16x16x32_bf16 v[24:27], a[64:67], v[84:87], v[24:27]\n\t"
  "ds_read_b64_tr_b16 v[78:79], v108 offset:30720\n\t"
  "v_add_f32 v122, v122, v115\n\t"
  "v_mfma_f32_16x16x32_bf16 v[28:31], a[68:71], v[84:87], v[28:31]\n\t"
  "ds_read_b64_tr_b16 v[80:81], v109 offset:28672\n\t"
  "v_fmac_f32 v123, v115, v115\n\t"
  "v_mfma_f32_16x16x32_bf16 v[32:35], a[60:63], v[88:91], v[32:35]\n\t"
  "ds_read_b64_tr_b16 v[82:83], v109 offset:30720\n\t"
  "s_add_u32 m0, s52, 114688\n\t"
  "v_mfma_f32_16x16x32_bf16 v[36:39], a[64:67], v[88:91], v[36:39]\n\t"
  "v_add_f32 v122, v122, v116\n\t"
  "v_fmac_f32 v123, v116, v116\n\t"
  "v_mfma_f32_16x16x32_bf16 v[40:43], a[68:71], v[88:91], v[40:43]\n\t"
  "s_waitcnt lgkmcnt(4)\n\t"
  "v_add_f32 v122, v122, v117\n\t"
  "v_mfma_f32_16x16x32_bf16 v[20:23], a[72:75], v[68:71], v[20:23]\n\t"
  "ds_read_b64_tr_b16 v[84:85], v108 offset:32768\n\t"
  "s_mov_b32 s46, 1343488\n\t"
  "v_mfma_f32_16x16x32_bf16 v[24:27], a[76:79], v[68:71], v[24:27]\n\t"
  "ds_read_b64_tr_b16 v[86:87], v108 offset:34816\n\t"
  "v_fmac_f32 v123, v117, v117\n\t"
  "v_mfma_f32_16x16x32_bf16 v[28:31], a[80:83], v[68:71], v[28:31]\n\t"
  "ds_read_b64_tr_b16 v[88:89], v109 offset:32768\n\t"
  "v_add_f32 v122, v122, v118\n\t"
  "v_mfma_f32_16x16x32_bf16 v[32:35], a[72:75], v[72:75], v[32:35]\n\t"
  "ds_read_b64_tr_b16 v[90:91], v109 offset:34816\n\t"
  "buffer_load_dwordx4 %[vdma], %[srd], s46 offen offset:0 sc1 lds\n\t"
  "v_mfma_f32_16x16x32_bf16 v[36:39], a[76:79], v[72:75], v[36:39]\n\t"
  "v_fmac_f32 v123, v118, v118\n\t"
  "v_add_f32 v122, v122, v119\n\t"
  "v_mfma_f32_16x16x32_bf16 v[40:43], a[80:83], v[72:75], v[40:43]\n\t"
  "s_waitcnt lgkmcnt(4)\n\t"
  "v_fmac_f32 v123, v119, v119\n\t"
  "v_mfma_f32_16x16x32_bf16 v[20:23], a[84:87], v[76:79], v[20:23]\n\t"
  "ds_read_b64_tr_b16 v[68:69], v108 offset:36864\n\t"
  "buffer_load_dwordx4 v140, %[srd], s46 offen offset:1024 sc1 lds\n\t"
  "v_mfma_f32_16x16x32_bf16 v[24:27], a[88:91], v[76:79], v[24:27]\n\t"
  "ds_read_b64_tr_b16 v[70:71], v108 offset:38912\n\t"
  "v_add_f32 v122, v122, v120\n\t"
  "v_mfma_f32_16x16x32_bf16 v[28:31], a[92:95], v[76:79], v[28:31]\n\t"
  "ds_read_b64_tr_b16 v[72:73], v109 offset:36864\n\t"
  "buffer_load_dwordx2 v[138:139], %[vpw], %[srd], 0 offen sc1\n\t"
  "v_mfma_f32_16x16x32_bf16 v[32:35], a[84:87], v[80:83], v[32:35]\n\t"
  "ds_read_b64_tr_b16 v[74:75], v109 offset:38912\n\t"
  "buffer_load_dwordx4 %[vdma], %[srd], s46 offen offset:2048 sc1 lds\n\t"
  "v_mfma_f32_16x16x32_bf16 v[36:39], a[88:91], v[80:83], v[36:39]\n\t"
  "v_fmac_f32 v123, v120, v120\n\t"
  "v_add_f32 v122, v122, v121\n\t"
  "v_mfma_f32_16x16x32_bf16 v[40:43], a[92:95], v[80:83], v[40:43]\n\t"
  "s_waitcnt lgkmcnt(4)\n\t"
  "buffer_load_dwordx4 v140, %[srd], s46 offen offset:3072 sc1 lds\n\t"
  "v_mfma_f32_16x16x32_bf16 v[20:23], a[96:99], v[84:87], v[20:23]\n\t"
  "ds_read_b64_tr_b16 v[76:77], v110 offset:0\n\t"
  "v_fmac_f32 v123, v121, v121\n\t"
  "v_mfma_f32_16x16x32_bf16 v[24:27], a[100:103], v[84:87], v[24:27]\n\t"
  "ds_read_b64_tr_b16 v[78:79], v110 offset:2048\n\t"
  "v_add_f32_dpp v122, v122, v122 quad_perm:[1,0,3,2] row_mask:0xf bank_mask:0xf\n\t"
  "v_mfma_f32_16x16x32_bf16 v[28:31], a[104:107], v[84:87], v[28:31]\n\t"
  "ds_read_b64_tr_b16 v[80:81], v111 offset:0\n\t"
  "s_add_u32 m0, s52, 131072\n\t"
  "v_mfma_f32_16x16x32_bf16 v[32:35], a[96:99], v[88:91], v[32:35]\n\t"
  "ds_read_b64_tr_b16 v[82:83], v111 offset:2048\n\t"
  "v_add_f32_dpp v123, v123, v123 quad_perm:[1,0,3,2] row_mask:0xf bank_mask:0xf\n\t"
  "v_mfma_f32_16x16x32_bf16 v[36:39], a[100:103], v[88:91], v[36:39]\n\t"
  "v_add_f32_dpp v122, v122, v122 quad_perm:[2,3,0,1] row_mask:0xf bank_mask:0xf\n\t"
  "v_add_f32_dpp v123, v123, v123 quad_perm:[2,3,0,1] row_mask:0xf bank_mask:0xf\n\t"
  "v_mfma_f32_16x16x32_bf16 v[40:43], a[104:107], v[88:91], v[40:43]\n\t"
  "s_waitcnt lgkmcnt(4)\n\t"
  "s_mov_b32 s46, 1359872\n\t"
  "v_mfma_f32_16x16x32_bf16 v[20:23], a[108:111], v[68:71], v[20:23]\n\t"
  "ds_read_b64_tr_b16 v[84:85], v110 offset:4096\n\t"
  "v_add_f32_dpp v122, v122, v122 row_half_mirror row_mask:0xf bank_mask:0xf\n\t"
  "v_mfma_f32_16x16x32_bf16 v[24:27], a[112:115], v[68:71], v[24:27]\n\t"
  "ds_read_b64_tr_b16 v[86:87], v110 offset:6144\n\t"
  "v_add_f32_dpp v123, v123, v123 row_half_mirror row_mask:0xf bank_mask:0xf\n\t"
  "v_mfma_f32_16x16x32_bf16 v[28:31], a[116:119], v[68:71], v[28:31]\n\t"
  "ds_read_b64_tr_b16 v[88:89], v111 offset:4096\n\t"
  "buffer_load_dwordx4 %[vdma], %[srd], s46 offen offset:0 sc1 lds\n\t"
  "v_mfma_f32_16x16x32_bf16 v[32:35], a[108:111], v[72:75], v[32:35]\n\t"
  "ds_read_b64_tr_b16 v[90:91], v111 offset:6144\n\t"
  "v_add_f32_dpp v122, v122, v122 row_mirror row_mask:0xf bank_mask:0xf\n\t"
  "v_mfma_f32_16x16x32_bf16 v[36:39], a[112:115], v[72:75], v[36:39]\n\t"
  "buffer_load_dwordx4 v140, %[srd], s46 offen offset:1024 sc1 lds\n\t"
  "v_add_f32_dpp v123, v123, v123 row_mirror row_mask:0xf bank_mask:0xf\n\t"
  "v_mfma_f32_16x16x32_bf16 v[40:43], a[116:119], v[72:75], v[40:43]\n\t"
  "s_waitcnt lgkmcnt(4)\n\t"
  "v_add_f32_dpp v122, v122, v122 row_bcast:15 row_mask:0xa bank_mask:0xf\n\t"
  "v_mfma_f32_16x16x32_bf16 v[20:23], a[120:123], v[76:79], v[20:23]\n\t"
  "ds_read_b64_tr_b16 v[68:69], v110 offset:8192\n\t"
  "v_xor_b32 v108, 64, %[vtr]\n\t"
  "v_mfma_f32_16x16x32_bf16 v[24:27], a[124:127], v[76:79], v[24:27]\n\t"
  "ds_read_b64_tr_b16 v[70:71], v110 offset:10240\n\t"
  "v_add_u32 v108, 0, v108\n\t"
  "v_mfma_f32_16x16x32_bf16 v[28:31], a[128:131], v[76:79], v[28:31]\n\t"
  "ds_read_b64_tr_b16 v[72:73], v111 offset:8192\n\t"
  "v_xor_b32 v109, 96, %[vtr]\n\t"
  "v_mfma_f32_16x16x32_bf16 v[32:35], a[120:123], v[80:83], v[32:35]\n\t"
  "ds_read_b64_tr_b16 v[74:75], v111 offset:10240\n\t"
  "v_add_u32 v109, 0, v109\n\t"
  "v_mfma_f32_16x16x32_bf16 v[36:39], a[124:127], v[80:83], v[36:39]\n\t"
  "buffer_load_dwordx4 %[vdma], %[srd], s46 offen offset:2048 sc1 lds\n\t"
  "buffer_load_dwordx4 v140, %[srd], s46 offen offset:3072 sc1 lds\n\t"
  "v_mfma_f32_16x16x32_bf16 v[40:43], a[128:131], v[80:83], v[40:43]\n\t"
  "s_waitcnt lgkmcnt(4)\n\t"
  "v_add_f32_dpp v123, v123, v123 row_bcast:15 row_mask:0xa bank_mask:0xf\n\t"
  "v_mfma_f32_16x16x32_bf16 v[20:23], a[132:135], v[84:87], v[20:23]\n\t"
  "ds_read_b64_tr_b16 v[76:77], v110 offset:12288\n\t"
  "v_add_f32_dpp v122, v122, v122 row_bcast:31 row_mask:0xc bank_mask:0xf\n\t"
  "v_mfma_f32_16x16x32_bf16 v[24:27], a[136:139], v[84:87], v[24:27]\n\t"
  "ds_read_b64_tr_b16 v[78:79], v110 offset:14336\n\t"
  "s_add_u32 m0, s52, 147456\n\t"
  "v_mfma_f32_16x16x32_bf16 v[28:31], a[140:143], v[84:87], v[28:31]\n\t"
  "ds_read_b64_tr_b16 v[80:81], v111 offset:12288\n\t"
  "v_add_f32_dpp v123, v123, v123 row_bcast:31 row_mask:0xc bank_mask:0xf\n\t"
  "v_mfma_f32_16x16x32_bf16 v[32:35], a[132:135], v[88:91], v[32:35]\n\t"
  "ds_read_b64_tr_b16 v[82:83], v111 offset:14336\n\t"
  "v_mul_f32 v122, 0xbacccccd, v122\n\t"
  "v_mfma_f32_16x16x32_bf16 v[36:39], a[136:139], v[88:91], v[36:39]\n\t"
  "s_mov_b32 s46, 1376256\n\t"
  "v_mul_f32 v123, 0x3acccccd, v123\n\t"
  "v_mfma_f32_16x16x32_bf16 v[40:43], a[140:143], v[88:91], v[40:43]\n\t"
  "s_waitcnt lgkmcnt(4)\n\t"
  "v_fma_f32 v123, -v122, v122, v123\n\t"
  "v_mfma_f32_16x16x32_bf16 v[20:23], a[144:147], v[68:71], v[20:23]\n\t"
  "ds_read_b64_tr_b16 v[84:85], v110 offset:16384\n\t"
  "buffer_load_dwordx4 %[vdma], %[srd], s46 offen offset:0 sc1 lds\n\t"
  "v_mfma_f32_16x16x32_bf16 v[24:27], a[148:151], v[68:71], v[24:27]\n\t"
  "ds_read_b64_tr_b16 v[86:87], v110 offset:18432\n\t"
  "v_max_f32 v123, 0, v123\n\t"
  "v_mfma_f32_16x16x32_bf16 v[28:31], a[152:155], v[68:71], v[28:31]\n\t"
  "ds_read_b64_tr_b16 v[88:89], v111 offset:16384\n\t"
  "v_cvt_pk_bf16_f32 v123, 0, v123\n\t"
  "v_mfma_f32_16x16x32_bf16 v[32:35], a[144:147], v[72:75], v[32:35]\n\t"
  "ds_read_b64_tr_b16 v[90:91], v111 offset:18432\n\t"
  "buffer_load_dwordx4 v140, %[srd], s46 offen offset:1024 sc1 lds\n\t"
  "v_mfma_f32_16x16x32_bf16 v[36:39], a[148:151], v[72:75], v[36:39]\n\t"
  "v_cvt_pk_bf16_f32 v126, v122, v122\n\t"
  "v_add_f32 v123, 0x3727c5ac, v123\n\t"
  "v_mfma_f32_16x16x32_bf16 v[40:43], a[152:155], v[72:75], v[40:43]\n\t"
  "s_waitcnt lgkmcnt(4)\n\t"
  "v_cvt_pk_bf16_f32 v123, 0, v123\n\t"
  "v_mfma_f32_16x16x32_bf16 v[20:23], a[156:159], v[76:79], v[20:23]\n\t"
  "ds_read_b64_tr_b16 v[68:69], v110 offset:20480\n\t"
  "buffer_load_dwordx4 %[vdma], %[srd], s46 offen offset:2048 sc1 lds\n\t"
  "v_mfma_f32_16x16x32_bf16 v[24:27], a[160:163], v[76:79], v[24:27]\n\t"
  "ds_read_b64_tr_b16 v[70:71], v110 offset:22528\n\t"
  "v_sqrt_f32 v123, v123\n\t"
  "v_mfma_f32_16x16x32_bf16 v[28:31], a[164:167], v[76:79], v[28:31]\n\t"
  "ds_read_b64_tr_b16 v[72:73], v111 offset:20480\n\t"
  "v_cvt_pk_bf16_f32 v127, v123, v123\n\t"
  "v_mfma_f32_16x16x32_bf16 v[32:35], a[156:159], v[80:83], v[32:35]\n\t"
  "ds_read_b64_tr_b16 v[74:75], v111 offset:22528\n\t"
  "buffer_load_dwordx4 v140, %[srd], s46 offen offset:3072 sc1 lds\n\t"
  "v_mfma_f32_16x16x32_bf16 v[36:39], a[160:163], v[80:83], v[36:39]\n\t"
  "buffer_store_dwordx2 v[126:127], %[vstw], %[srd], %[sST] offen sc1\n\t"
  "v_mfma_f32_16x16x32_bf16 v[40:43], a[164:167], v[80:83], v[40:43]\n\t"
  "s_waitcnt lgkmcnt(4)\n\t"
  "v_mfma_f32_16x16x32_bf16 v[20:23], a[168:171], v[84:87], v[20:23]\n\t"
  "ds_read_b64_tr_b16 v[76:77], v110 offset:24576\n\t"
  "s_waitcnt vmcnt(11)\n\t"
  "v_cmp_ne_u64 vcc, v[132:133], v[138:139]\n\t"
  "v_mfma_f32_16x16x32_bf16 v[24:27], a[172:175], v[84:87], v[24:27]\n\t"
  "ds_read_b64_tr_b16 v[78:79], v110 offset:26624\n\t"
  "v_mfma_f32_16x16x32_bf16 v[28:31], a[176:179], v[84:87], v[28:31]\n\t"
  "ds_read_b64_tr_b16 v[80:81], v111 offset:24576\n\t"
  "s_cbranch_vccnz Lw2_slow%=\n\t"
  "Lw2_back%=:\n\t"
  "v_mfma_f32_16x16x32_bf16 v[32:35], a[168:171], v[88:91], v[32:35]\n\t"
  "ds_read_b64_tr_b16 v[82:83], v111 offset:26624\n\t"
  "v_mfma_f32_16x16x32_bf16 v[36:39], a[172:175], v[88:91], v[36:39]\n\t"
  "v_mfma_f32_16x16x32_bf16 v[40:43], a[176:179], v[88:91], v[40:43]\n\t"
  "s_waitcnt lgkmcnt(4)\n\t"
  "v_mfma_f32_16x16x32_bf16 v[20:23], a[180:183], v[68:71], v[20:23]\n\t"
  "ds_read_b64_tr_b16 v[84:85], v110 offset:28672\n\t"
  "s_add_u32 s45, %[sW2], 0\n\t"
  "v_mfma_f32_16x16x32_bf16 v[24:27], a[184:187], v[68:71], v[24:27]\n\t"
  "ds_read_b64_tr_b16 v[86:87], v110 offset:30720\n\t"
  "v_mfma_f32_16x16x32_bf16 v[28:31], a[188:191], v[68:71], v[28:31]\n\t"
  "ds_read_b64_tr_b16 v[88:89], v111 offset:28672\n\t"
  "v_mfma_f32_16x16x32_bf16 v[32:35], a[180:183], v[72:75], v[32:35]\n\t"
  "ds_read_b64_tr_b16 v[90:91], v111 offset:30720\n\t"
  "v_mfma_f32_16x16x32_bf16 v[36:39], a[184:187], v[72:75], v[36:39]\n\t"
  "buffer_load_dwordx4 v[144:147], %[vl16], %[srd], s45 offen offset:0 sc1\n\t"
  "v_mfma_f32_16x16x32_bf16 v[40:43], a[188:191], v[72:75], v[40:43]\n\t"
  "s_waitcnt lgkmcnt(4)\n\t"
  "v_mfma_f32_16x16x32_bf16 v[20:23], a[192:195], v[76:79], v[20:23]\n\t"
  "ds_read_b64_tr_b16 v[68:69], v110 offset:32768\n\t"
  "v_mfma_f32_16x16x32_bf16 v[24:27], a[196:199], v[76:79], v[24:27]\n\t"
  "ds_read_b64_tr_b16 v[70:71], v110 offset:34816\n\t"
  "v_mfma_f32_16x16x32_bf16 v[28:31], a[200:203], v[76:79], v[28:31]\n\t"
  "ds_read_b64_tr_b16 v[72:73], v111 offset:32768\n\t"
  "v_mfma_f32_16x16x32_bf16 v[32:35], a[192:195], v[80:83], v[32:35]\n\t"
  "ds_read_b64_tr_b16 v[74:75], v111 offset:34816\n\t"
  "buffer_load_dwordx4 v[148:151], %[vl16], %[srd], s45 offen offset:1024 sc1\n\t"
  "v_mfma_f32_16x16x32_bf16 v[36:39], a[196:199], v[80:83], v[36:39]\n\t"
  "v_mfma_f32_16x16x32_bf16 v[40:43], a[200:203], v[80:83], v[40:43]\n\t"
  "s_waitcnt lgkmcnt(4)\n\t"
  "v_mfma_f32_16x16x32_bf16 v[20:23], a[204:207], v[84:87], v[20:23]\n\t"
  "ds_read_b64_tr_b16 v[76:77], v110 offset:36864\n\t"
  "v_mfma_f32_16x16x32_bf16 v[24:27], a[208:211], v[84:87], v[24:27]\n\t"
  "ds_read_b64_tr_b16 v[78:79], v110 offset:38912\n\t"
  "v_mfma_f32_16x16x32_bf16 v[28:31], a[212:215], v[84:87], v[28:31]\n\t"
  "ds_read_b64_tr_b16 v[80:81], v111 offset:36864\n\t"
  "buffer_load_dwordx4 v[152:155], %[vl16], %[srd], s45 offen offset:2048 sc1\n\t"
  "v_mfma_f32_16x16x32_bf16 v[32:35], a[204:207], v[88:91], v[32:35]\n\t"
  "ds_read_b64_tr_b16 v[82:83], v111 offset:38912\n\t"
  "v_mfma_f32_16x16x32_bf16 v[36:39], a[208:211], v[88:91], v[36:39]\n\t"
  "v_mfma_f32_16x16x32_bf16 v[40:43], a[212:215], v[88:91], v[40:43]\n\t"
  "s_waitcnt lgkmcnt(4)\n\t"
  "v_mfma_f32_16x16x32_bf16 v[20:23], a[216:219], v[68:71], v[20:23]\n\t"
  "ds_read_b64_tr_b16 v[84:85], v108 offset:0\n\t"
  "v_mfma_f32_16x16x32_bf16 v[24:27], a[220:223], v[68:71], v[24:27]\n\t"
  "ds_read_b64_tr_b16 v[86:87], v108 offset:2048\n\t"
  "buffer_load_dwordx4 v[156:159], %[vl16], %[srd], s45 offen offset:3072 sc1\n\t"
  "v_mfma_f32_16x16x32_bf16 v[28:31], a[224:227], v[68:71], v[28:31]\n\t"
  "ds_read_b64_tr_b16 v[88:89], v109 offset:0\n\t"
  "v_mfma_f32_16x16x32_bf16 v[32:35], a[216:219], v[72:75], v[32:35]\n\t"
  "ds_read_b64_tr_b16 v[90:91], v109 offset:2048\n\t"
  "v_mfma_f32_16x16x32_bf16 v[36:39], a[220:223], v[72:75], v[36:39]\n\t"
  "v_mfma_f32_16x16x32_bf16 v[40:43], a[224:227], v[72:75], v[40:43]\n\t"
  "s_waitcnt lgkmcnt(4)\n\t"
  "v_mfma_f32_16x16x32_bf16 v[20:23], a[228:231], v[76:79], v[20:23]\n\t"
  "ds_read_b64_tr_b16 v[68:69], v108 offset:4096\n\t"
  "s_add_u32 s45, %[sW2], 4096\n\t"
  "v_mfma_f32_16x16x32_bf16 v[24:27], a[232:235], v[76:79], v[24:27]\n\t"
  "ds_read_b64_tr_b16 v[70:71], v108 offset:6144\n\t"
  "v_mfma_f32_16x16x32_bf16 v[28:31], a[236:239], v[76:79], v[28:31]\n\t"
  "ds_read_b64_tr_b16 v[72:73], v109 offset:4096\n\t"
  "s_waitcnt vmcnt(5)\n\t"
  "s_barrier\n\t"
  "v_mfma_f32_16x16x32_bf16 v[32:35], a[228:231], v[80:83], v[32:35]\n\t"
  "ds_read_b64_tr_b16 v[74:75], v109 offset:6144\n\t"
  "v_add_u32 v124, 122880, %[vstc]\n\t"
  "v_mfma_f32_16x16x32_bf16 v[36:39], a[232:235], v[80:83], v[36:39]\n\t"
  "v_add_u32 v125, 81920, %[vstc]\n\t"
  "v_mov_b32 v112, 0\n\t"
  "v_mfma_f32_16x16x32_bf16 v[40:43], a[236:239], v[80:83], v[40:43]\n\t"
  "s_waitcnt lgkmcnt(4)\n\t"
  "buffer_load_dwordx4 v[160:163], %[vl16], %[srd], s45 offen offset:0 sc1\n\t"
  "v_mfma_f32_16x16x32_bf16 v[44:47], a[0:3], v[84:87], 0\n\t"
  "ds_read_b64_tr_b16 v[76:77], v108 offset:8192\n\t"
  "v_xor_b32 v110, 64, %[vtr]\n\t"
  "v_mfma_f32_16x16x32_bf16 v[48:51], a[4:7], v[84:87], 0\n\t"
  "ds_read_b64_tr_b16 v[78:79], v108 offset:10240\n\t"
  "v_add_u32 v110, 40960, v110\n\t"
  "v_mfma_f32_16x16x32_bf16 v[52:55], a[8:11], v[84:87], 0\n\t"
  "ds_read_b64_tr_b16 v[80:81], v109 offset:8192\n\t"
  "v_xor_b32 v111, 96, %[vtr]\n\t"
  "v_mfma_f32_16x16x32_bf16 v[56:59], a[0:3], v[88:91], 0\n\t"
  "ds_read_b64_tr_b16 v[82:83], v109 offset:10240\n\t"
  "v_add_u32 v111, 40960, v111\n\t"
  "v_mfma_f32_16x16x32_bf16 v[60:63], a[4:7], v[88:91], 0\n\t"
  "buffer_load_dwordx2 v[92:93], v136, %[srd], %[sST] offen offset:0 sc1\n\t"
  "buffer_load_dwordx2 v[94:95], v136, %[srd], %[sST] offen offset:1024 sc1\n\t"
  "v_mfma_f32_16x16x32_bf16 v[64:67], a[8:11], v[88:91], 0\n\t"
  "s_waitcnt lgkmcnt(4)\n\t"
  "buffer_load_dwordx2 v[96:97], v136, %[srd], %[sST] offen offset:2048 sc1\n\t"
  "v_mfma_f32_16x16x32_bf16 v[44:47], a[12:15], v[68:71], v[44:47]\n\t"
  "ds_read_b64_tr_b16 v[84:85], v108 offset:12288\n\t"
  "buffer_load_dwordx2 v[98:99], v136, %[srd], %[sST] offen offset:3072 sc1\n\t"
  "v_mfma_f32_16x16x32_bf16 v[48:51], a[16:19], v[68:71], v[48:51]\n\t"
  "ds_read_b64_tr_b16 v[86:87], v108 offset:14336\n\t"
  "buffer_load_dwordx4 v[164:167], %[vl16], %[srd], s45 offen offset:1024 sc1\n\t"
  "v_mfma_f32_16x16x32_bf16 v[52:55], a[20:23], v[68:71], v[52:55]\n\t"
  "ds_read_b64_tr_b16 v[88:89], v109 offset:12288\n\t"
  "v_mov_b32 v113, 0\n\t"
  "v_mfma_f32_16x16x32_bf16 v[56:59], a[12:15], v[72:75], v[56:59]\n\t"
  "ds_read_b64_tr_b16 v[90:91], v109 offset:14336\n\t"
  "buffer_load_dwordx4 v[168:171], %[vl16], %[srd], s45 offen offset:2048 sc1\n\t"
  "v_mfma_f32_16x16x32_bf16 v[60:63], a[16:19], v[72:75], v[60:63]\n\t"
  "v_mov_b32 v114, 0\n\t"
  "v_mov_b32 v115, 0\n\t"
  "v_mfma_f32_16x16x32_bf16 v[64:67], a[20:23], v[72:75], v[64:67]\n\t"
  "s_waitcnt lgkmcnt(4)\n\t"
  "v_mov_b32 v116, 0\n\t"
  "v_mfma_f32_16x16x32_bf16 v[44:47], a[24:27], v[76:79], v[44:47]\n\t"
  "ds_read_b64_tr_b16 v[68:69], v108 offset:16384\n\t"
  "v_mov_b32 v117, 0\n\t"
  "v_mfma_f32_16x16x32_bf16 v[48:51], a[28:31], v[76:79], v[48:51]\n\t"
  "ds_read_b64_tr_b16 v[70:71], v108 offset:18432\n\t"
  "v_mov_b32 v118, 0\n\t"
  "v_mfma_f32_16x16x32_bf16 v[52:55], a[32:35], v[76:79], v[52:55]\n\t"
  "ds_read_b64_tr_b16 v[72:73], v109 offset:16384\n\t"
  "buffer_load_dwordx4 v[172:175], %[vl16], %[srd], s45 offen offset:3072 sc1\n\t"
  "v_mfma_f32_16x16x32_bf16 v[56:59], a[24:27], v[80:83], v[56:59]\n\t"
  "ds_read_b64_tr_b16 v[74:75], v109 offset:18432\n\t"
  "v_mov_b32 v119, 0\n\t"
  "v_mfma_f32_16x16x32_bf16 v[60:63], a[28:31], v[80:83], v[60:63]\n\t"
  "v_mov_b32 v120, 0\n\t"
  "v_mov_b32 v121, 0\n\t"
  "v_mfma_f32_16x16x32_bf16 v[64:67], a[32:35], v[80:83], v[64:67]\n\t"
  "s_waitcnt lgkmcnt(4)\n\t"
  "ds_read_u16_d16_hi v112, v125 offset:0\n\t"
  "v_mfma_f32_16x16x32_bf16 v[44:47], a[36:39], v[84:87], v[44:47]\n\t"
  "ds_read_b64_tr_b16 v[76:77], v108 offset:20480\n\t"
  "v_mfma_f32_16x16x32_bf16 v[48:51], a[40:43], v[84:87], v[48:51]\n\t"
  "ds_read_b64_tr_b16 v[78:79], v108 offset:22528\n\t"
  "s_add_u32 s45, %[sW2], 8192\n\t"
  "v_mfma_f32_16x16x32_bf16 v[52:55], a[44:47], v[84:87], v[52:55]\n\t"
  "ds_read_b64_tr_b16 v[80:81], v109 offset:20480\n\t"
  "ds_read_u16_d16_hi v113, v125 offset:8192\n\t"
  "v_mfma_f32_16x16x32_bf16 v[56:59], a[36:39], v[88:91], v[56:59]\n\t"
  "ds_read_b64_tr_b16 v[82:83], v109 offset:22528\n\t"
  "v_mfma_f32_16x16x32_bf16 v[60:63], a[40:43], v[88:91], v[60:63]\n\t"
  "ds_read_u16_d16_hi v114, v125 offset:16384\n\t"
  "v_mfma_f32_16x16x32_bf16 v[64:67], a[44:47], v[88:91], v[64:67]\n\t"
  "s_waitcnt lgkmcnt(7)\n\t"
  "v_mfma_f32_16x16x32_bf16 v[44:47], a[48:51], v[68:71], v[44:47]\n\t"
  "ds_read_b64_tr_b16 v[84:85], v108 offset:24576\n\t"
  "buffer_load_dwordx4 v[176:179], %[vl16], %[srd], s45 offen offset:0 sc1\n\t"
  "v_mfma_f32_16x16x32_bf16 v[48:51], a[52:55], v[68:71], v[48:51]\n\t"
  "ds_read_b64_tr_b16 v[86:87], v108 offset:26624\n\t"
  "ds_read_u16_d16_hi v115, v125 offset:24576\n\t"
  "v_mfma_f32_16x16x32_bf16 v[52:55], a[56:59], v[68:71], v[52:55]\n\t"
  "ds_read_b64_tr_b16 v[88:89], v109 offset:24576\n\t"
  "v_mfma_f32_16x16x32_bf16 v[56:59], a[48:51], v[72:75], v[56:59]\n\t"
  "ds_read_b64_tr_b16 v[90:91], v109 offset:26624\n\t"
  "ds_read_u16_d16_hi v116, v125 offset:32768\n\t"
  "v_mfma_f32_16x16x32_bf16 v[60:63], a[52:55], v[72:75], v[60:63]\n\t"
  "buffer_load_dwordx4 v[180:183], %[vl16], %[srd], s45 offen offset:1024 sc1\n\t"
  "v_mfma_f32_16x16x32_bf16 v[64:67], a[56:59], v[72:75], v[64:67]\n\t"
  "s_waitcnt lgkmcnt(7)\n\t"
  "ds_read_u16_d16_hi v117, v124 offset:0\n\t"
  "v_mfma_f32_16x16x32_bf16 v[44:47], a[60:63], v[76:79], v[44:47]\n\t"
  "ds_read_b64_tr_b16 v[68:69], v108 offset:28672\n\t"
  "v_mfma_f32_16x16x32_bf16 v[48:51], a[64:67], v[76:79], v[48:51]\n\t"
  "ds_read_b64_tr_b16 v[70:71], v108 offset:30720\n\t"
  "ds_read_u16_d16_hi v118, v124 offset:8192\n\t"
  "v_mfma_f32_16x16x32_bf16 v[52:55], a[68:71], v[76:79], v[52:55]\n\t"
  "ds_read_b64_tr_b16 v[72:73], v109 offset:28672\n\t"
  "v_mfma_f32_16x16x32_bf16 v[56:59], a[60:63], v[80:83], v[56:59]\n\t"
  "ds_read_b64_tr_b16 v[74:75], v109 offset:30720\n\t"
  "buffer_load_dwordx4 v[184:187], %[vl16], %[srd], s45 offen offset:2048 sc1\n\t"
  "v_mfma_f32_16x16x32_bf16 v[60:63], a[64:67], v[80:83], v[60:63]\n\t"
  "ds_read_u16_d16_hi v119, v124 offset:16384\n\t"
  "v_mfma_f32_16x16x32_bf16 v[64:67], a[68:71], v[80:83], v[64:67]\n\t"
  "s_waitcnt lgkmcnt(8)\n\t"
  "v_mfma_f32_16x16x32_bf16 v[44:47], a[72:75], v[84:87], v[44:47]\n\t"
  "ds_read_b64_tr_b16 v[76:77], v108 offset:32768\n\t"
  "ds_read_u16_d16_hi v120, v124 offset:24576\n\t"
  "v_mfma_f32_16x16x32_bf16 v[48:51], a[76:79], v[84:87], v[48:51]\n\t"
  "ds_read_b64_tr_b16 v[78:79], v108 offset:34816\n\t"
  "v_mfma_f32_16x16x32_bf16 v[52:55], a[80:83], v[84:87], v[52:55]\n\t"
  "ds_read_b64_tr_b16 v[80:81], v109 offset:32768\n\t"
  "buffer_load_dwordx4 v[188:191], %[vl16], %[srd], s45 offen offset:3072 sc1\n\t"
  "v_mfma_f32_16x16x32_bf16 v[56:59], a[72:75], v[88:91], v[56:59]\n\t"
  "ds_read_b64_tr_b16 v[82:83], v109 offset:34816\n\t"
  "ds_read_u16_d16_hi v121, v124 offset:32768\n\t"
  "v_mfma_f32_16x16x32_bf16 v[60:63], a[76:79], v[88:91], v[60:63]\n\t"
  "v_mfma_f32_16x16x32_bf16 v[64:67], a[80:83], v[88:91], v[64:67]\n\t"
  "s_waitcnt lgkmcnt(7)\n\t"
  "v_mfma_f32_16x16x32_bf16 v[44:47], a[84:87], v[68:71], v[44:47]\n\t"
  "ds_read_b64_tr_b16 v[84:85], v108 offset:36864\n\t"
  "v_mfma_f32_16x16x32_bf16 v[48:51], a[88:91], v[68:71], v[48:51]\n\t"
  "ds_read_b64_tr_b16 v[86:87], v108 offset:38912\n\t"
  "s_add_u32 s45, %[sW2], 12288\n\t"
  "v_mfma_f32_16x16x32_bf16 v[52:55], a[92:95], v[68:71], v[52:55]\n\t"
  "ds_read_b64_tr_b16 v[88:89], v109 offset:36864\n\t"
  "v_mfma_f32_16x16x32_bf16 v[56:59], a[84:87], v[72:75], v[56:59]\n\t"
  "ds_read_b64_tr_b16 v[90:91], v109 offset:38912\n\t"
  "v_mfma_f32_16x16x32_bf16 v[60:63], a[88:91], v[72:75], v[60:63]\n\t"
  "s_waitcnt vmcnt(9)\n\t"
  "v_min_u32 v131, v93, v95\n\t"
  "v_mfma_f32_16x16x32_bf16 v[64:67], a[92:95], v[72:75], v[64:67]\n\t"
  "s_waitcnt lgkmcnt(5)\n\t"
  "s_waitcnt vmcnt(7)\n\t"
  "v_min3_u32 v131, v131, v97, v99\n\t"
  "v_mfma_f32_16x16x32_bf16 v[44:47], a[96:99], v[76:79], v[44:47]\n\t"
  "ds_read_b64_tr_b16 v[68:69], v110 offset:0\n\t"
  "v_cmp_eq_u32 vcc, 0, v131\n\t"
  "v_mfma_f32_16x16x32_bf16 v[48:51], a[100:103], v[76:79], v[48:51]\n\t"
  "ds_read_b64_tr_b16 v[70:71], v110 offset:2048\n\t"
  "buffer_load_dwordx4 v[192:195], %[vl16], %[srd], s45 offen offset:0 sc1\n\t"
  "v_mfma_f32_16x16x32_bf16 v[52:55], a[104:107], v[76:79], v[52:55]\n\t"
  "ds_read_b64_tr_b16 v[72:73], v111 offset:0\n\t"
  "s_cbranch_vccnz Lst0_slow%=\n\t"
  "Lst0_back%=:\n\t"
  "v_mfma_f32_16x16x32_bf16 v[56:59], a[96:99], v[80:83], v[56:59]\n\t"
  "ds_read_b64_tr_b16 v[74:75], v111 offset:2048\n\t"
  "v_mfma_f32_16x16x32_bf16 v[60:63], a[100:103], v[80:83], v[60:63]\n\t"
  "v_mul_f32 v112, v112, v142\n\t"
  "v_mul_f32 v113, v113, v143\n\t"
  "v_mfma_f32_16x16x32_bf16 v[64:67], a[104:107], v[80:83], v[64:67]\n\t"
  "s_waitcnt lgkmcnt(4)\n\t"
  "buffer_load_dwordx4 v[196:199], %[vl16], %[srd], s45 offen offset:1024 sc1\n\t"
  "v_mfma_f32_16x16x32_bf16 v[44:47], a[108:111], v[84:87], v[44:47]\n\t"
  "ds_read_b64_tr_b16 v[76:77], v110 offset:4096\n\t"
  "v_mul_f32 v114, v114, v248\n\t"
  "v_mfma_f32_16x16x32_bf16 v[48:51], a[112:115], v[84:87], v[48:51]\n\t"
  "ds_read_b64_tr_b16 v[78:79], v110 offset:6144\n\t"
  "v_mul_f32 v115, v115, v249\n\t"
  "v_mfma_f32_16x16x32_bf16 v[52:55], a[116:119], v[84:87], v[52:55]\n\t"
  "ds_read_b64_tr_b16 v[80:81], v111 offset:4096\n\t"
  "v_mul_f32 v116, v116, v250\n\t"
  "v_mfma_f32_16x16x32_bf16 v[56:59], a[108:111], v[88:91], v[56:59]\n\t"
  "ds_read_b64_tr_b16 v[82:83], v111 offset:6144\n\t"
  "v_mul_f32 v117, v117, v251\n\t"
  "v_mfma_f32_16x16x32_bf16 v[60:63], a[112:115], v[88:91], v[60:63]\n\t"
  "buffer_load_dwordx4 v[200:203], %[vl16], %[srd], s45 offen offset:2048 sc1\n\t"
  "v_mul_f32 v118, v118, v252\n\t"
  "v_mfma_f32_16x16x32_bf16 v[64:67], a[116:119], v[88:91], v[64:67]\n\t"
  "v_mul_f32 v119, v119, v253\n\t"
  "v_mul_f32 v120, v120, v254\n\t"
  "v_mfma_f32_16x16x16_bf16 v[20:23], a[240:241], v[92:93], v[20:23]\n\t"
  "v_mul_f32 v121, v121, v255\n\t"
  "v_add_f32 v122, v112, v113\n\t"
  "v_mfma_f32_16x16x16_bf16 v[24:27], a[242:243], v[92:93], v[24:27]\n\t"
  "v_mul_f32 v123, v112, v112\n\t"
  "v_fmac_f32 v123, v113, v113\n\t"
  "v_mfma_f32_16x16x16_bf16 v[28:31], a[244:245], v[92:93], v[28:31]\n\t"
  "v_add_f32 v122, v122, v114\n\t"
  "v_fmac_f32 v123, v114, v114\n\t"
  "v_mfma_f32_16x16x16_bf16 v[32:35], a[240:241], v[94:95], v[32:35]\n\t"
  "buffer_load_dwordx4 v[204:207], %[vl16], %[srd], s45 offen offset:3072 sc1\n\t"
  "v_add_f32 v122, v122, v115\n\t"
  "v_mfma_f32_16x16x16_bf16 v[36:39], a[242:243], v[94:95], v[36:39]\n\t"
  "v_fmac_f32 v123, v115, v115\n\t"
  "v_add_f32 v122, v122, v116\n\t"
  "v_mfma_f32_16x16x16_bf16 v[40:43], a[244:245], v[94:95], v[40:43]\n\t"
  "s_waitcnt lgkmcnt(4)\n\t"
  "v_fmac_f32 v123, v116, v116\n\t"
  "v_mfma_f32_16x16x32_bf16 v[44:47], a[120:123], v[68:71], v[44:47]\n\t"
  "ds_read_b64_tr_b16 v[84:85], v110 offset:8192\n\t"
  "v_add_u32 v108, 81920, %[vtr]\n\t"
  "v_mfma_f32_16x16x32_bf16 v[48:51], a[124:127], v[68:71], v[48:51]\n\t"
  "ds_read_b64_tr_b16 v[86:87], v110 offset:10240\n\t"
  "v_xor_b32 v109, 32, %[vtr]\n\t"
  "v_mfma_f32_16x16x32_bf16 v[52:55], a[128:131], v[68:71], v[52:55]\n\t"
  "ds_read_b64_tr_b16 v[88:89], v111 offset:8192\n\t"
  "v_add_u32 v109, 81920, v109\n\t"
  "v_mfma_f32_16x16x32_bf16 v[56:59], a[120:123], v[72:75], v[56:59]\n\t"
  "ds_read_b64_tr_b16 v[90:91], v111 offset:10240\n\t"
  "s_add_u32 s45, %[sW2], 16384\n\t"
  "v_mfma_f32_16x16x32_bf16 v[60:63], a[124:127], v[72:75], v[60:63]\n\t"
  "v_add_f32 v122, v122, v117\n\t"
  "v_fmac_f32 v123, v117, v117\n\t"
  "v_mfma_f32_16x16x32_bf16 v[64:67], a[128:131], v[72:75], v[64:67]\n\t"
  "s_waitcnt lgkmcnt(4)\n\t"
  "v_add_f32 v122, v122, v118\n\t"
  "v_mfma_f32_16x16x32_bf16 v[44:47], a[132:135], v[76:79], v[44:47]\n\t"
  "ds_read_b64_tr_b16 v[68:69], v110 offset:12288\n\t"
  "v_fmac_f32 v123, v118, v118\n\t"
  "v_mfma_f32_16x16x32_bf16 v[48:51], a[136:139], v[76:79], v[48:51]\n\t"
  "ds_read_b64_tr_b16 v[70:71], v110 offset:14336\n\t"
  "buffer_load_dwordx4 v[208:211], %[vl16], %[srd], s45 offen offset:0 sc1\n\t"
  "v_mfma_f32_16x16x32_bf16 v[52:55], a[140:143], v[76:79], v[52:55]\n\t"
  "ds_read_b64_tr_b16 v[72:73], v111 offset:12288\n\t"
  "v_add_f32 v122, v122, v119\n\t"
  "v_mfma_f32_16x16x32_bf16 v[56:59], a[132:135], v[80:83], v[56:59]\n\t"
  "ds_read_b64_tr_b16 v[74:75], v111 offset:14336\n\t"
  "v_fmac_f32 v123, v119, v119\n\t"
  "v_mfma_f32_16x16x32_bf16 v[60:63], a[136:139], v[80:83], v[60:63]\n\t"
  "v_add_f32 v122, v122, v120\n\t"
  "v_fmac_f32 v123, v120, v120\n\t"
  "v_mfma_f32_16x16x32_bf16 v[64:67], a[140:143], v[80:83], v[64:67]\n\t"
  "s_waitcnt lgkmcnt(4)\n\t"
  "v_add_f32 v122, v122, v121\n\t"
  "v_fmac_f32 v123, v121, v121\n\t"
  "s_nop 0\n\t"
  "v_add_f32_dpp v122, v122, v122 quad_perm:[1,0,3,2] row_mask:0xf bank_mask:0xf\n\t"
  "v_add_f32_dpp v123, v123, v123 quad_perm:[1,0,3,2] row_mask:0xf bank_mask:0xf\n\t"
  "s_nop 0\n\t"
  "v_add_f32_dpp v122, v122, v122 quad_perm:[2,3,0,1] row_mask:0xf bank_mask:0xf\n\t"
  "v_add_f32_dpp v123, v123, v123 quad_perm:[2,3,0,1] row_mask:0xf bank_mask:0xf\n\t"
  "s_nop 0\n\t"
  "v_add_f32_dpp v122, v122, v122 row_half_mirror row_mask:0xf bank_mask:0xf\n\t"
  "v_add_f32_dpp v123, v123, v123 row_half_mirror row_mask:0xf bank_mask:0xf\n\t"
  "s_nop 0\n\t"
  "v_add_f32_dpp v122, v122, v122 row_mirror row_mask:0xf bank_mask:0xf\n\t"
  "v_add_f32_dpp v123, v123, v123 row_mirror row_mask:0xf bank_mask:0xf\n\t"
  "s_nop 0\n\t"
  "v_add_f32_dpp v122, v122, v122 row_bcast:15 row_mask:0xa bank_mask:0xf\n\t"
  "v_add_f32_dpp v123, v123, v123 row_bcast:15 row_mask:0xa bank_mask:0xf\n\t"
  "s_nop 0\n\t"
  "v_add_f32_dpp v122, v122, v122 row_bcast:31 row_mask:0xc bank_mask:0xf\n\t"
  "v_add_f32_dpp v123, v123, v123 row_bcast:31 row_mask:0xc bank_mask:0xf\n\t"
  "v_mul_f32 v122, 0xbacccccd, v122\n\t"
  "v_mul_f32 v123, 0x3acccccd, v123\n\t"
  "v_fma_f32 v123, -v122, v122, v123\n\t"
  "v_max_f32 v123, 0, v123\n\t"
  "v_cvt_pk_bf16_f32 v123, 0, v123\n\t"
  "v_cvt_pk_bf16_f32 v126, v122, v122\n\t"
  "v_add_f32 v123, 0x3727c5ac, v123\n\t"
  "v_cvt_pk_bf16_f32 v123, 0, v123\n\t"
  "v_sqrt_f32 v123, v123\n\t"
  "s_nop 0\n\t"
  "v_cvt_pk_bf16_f32 v127, v123, v123\n\t"
  "buffer_store_dwordx2 v[126:127], %[vstw], %[srd], s54 offen sc1\n\t"
  "ds_read_b64_tr_b16 a[246:247], %[vres0] offset:0\n\t"
  "ds_read_b64_tr_b16 a[248:249], %[vres1] offset:0\n\t"
  "ds_read_b64_tr_b16 a[250:251], %[vres0] offset:4096\n\t"
  "ds_read_b64_tr_b16 a[252:253], %[vres1] offset:4096\n\t"
  "ds_read_b64_tr_b16 a[254:255], %[vres2] offset:0\n\t"
  "v_mfma_f32_16x16x32_bf16 v[44:47], a[144:147], v[84:87], v[44:47]\n\t"
  "ds_read_b64_tr_b16 v[76:77], v110 offset:16384\n\t"
  "buffer_load_dwordx4 v[212:215], %[vl16], %[srd], s45 offen offset:1024 sc1\n\t"
  "v_mfma_f32_16x16x32_bf16 v[48:51], a[148:151], v[84:87], v[48:51]\n\t"
  "ds_read_b64_tr_b16 v[78:79], v110 offset:18432\n\t"
  "v_and_b32 v128, 0xffff0000, v93\n\t"
  "v_mfma_f32_16x16x32_bf16 v[52:55], a[152:155], v[84:87], v[52:55]\n\t"
  "ds_read_b64_tr_b16 v[80:81], v111 offset:16384\n\t"
  "v_and_b32 v129, 0xffff0000, v95\n\t"
  "v_mfma_f32_16x16x32_bf16 v[56:59], a[144:147], v[88:91], v[56:59]\n\t"
  "ds_read_b64_tr_b16 v[82:83], v111 offset:18432\n\t"
  "v_rcp_f32 v128, v128\n\t"
  "v_mfma_f32_16x16x32_bf16 v[60:63], a[148:151], v[88:91], v[60:63]\n\t"
  "buffer_load_dwordx4 v[216:219], %[vl16], %[srd], s45 offen offset:2048 sc1\n\t"
  "v_rcp_f32 v129, v129\n\t"
  "v_mfma_f32_16x16x32_bf16 v[64:67], a[152:155], v[88:91], v[64:67]\n\t"
  "s_waitcnt lgkmcnt(9)\n\t"
  "v_mul_f32 v128, v128, v128\n\t"
  "v_mfma_f32_16x16x32_bf16 v[44:47], a[156:159], v[68:71], v[44:47]\n\t"
  "ds_read_b64_tr_b16 v[84:85], v110 offset:20480\n\t"
  "v_mul_f32 v129, v129, v129\n\t"
  "v_mfma_f32_16x16x32_bf16 v[48:51], a[160:163], v[68:71], v[48:51]\n\t"
  "ds_read_b64_tr_b16 v[86:87], v110 offset:22528\n\t"
  "v_max_f32 v20, 0, v20\n\t"
  "v_mfma_f32_16x16x32_bf16 v[52:55], a[164:167], v[68:71], v[52:55]\n\t"
  "ds_read_b64_tr_b16 v[88:89], v111 offset:20480\n\t"
  "v_max_f32 v21, 0, v21\n\t"
  "v_mfma_f32_16x16x32_bf16 v[56:59], a[156:159], v[72:75], v[56:59]\n\t"
  "ds_read_b64_tr_b16 v[90:91], v111 offset:22528\n\t"
  "buffer_load_dwordx4 v[220:223], %[vl16], %[srd], s45 offen offset:3072 sc1\n\t"
  "v_mfma_f32_16x16x32_bf16 v[60:63], a[160:163], v[72:75], v[60:63]\n\t"
  "v_mul_f32 v22, v22, v128\n\t"
  "v_mul_f32 v23, v23, v128\n\t"
  "v_mfma_f32_16x16x32_bf16 v[64:67], a[164:167], v[72:75], v[64:67]\n\t"
  "s_waitcnt lgkmcnt(4)\n\t"
  "v_mul_f32 v20, v20, v22\n\t"
  "v_mfma_f32_16x16x32_bf16 v[44:47], a[168:171], v[76:79], v[44:47]\n\t"
  "ds_read_b64_tr_b16 v[68:69], v110 offset:24576\n\t"
  "v_mul_f32 v21, v21, v23\n\t"
  "v_mfma_f32_16x16x32_bf16 v[48:51], a[172:175], v[76:79], v[48:51]\n\t"
  "ds_read_b64_tr_b16 v[70:71], v110 offset:26624\n\t"
  "v_cvt_pk_bf16_f32 v116, v20, v21\n\t"
  "v_mfma_f32_16x16x32_bf16 v[52:55], a[176:179], v[76:79], v[52:55]\n\t"
  "ds_read_b64_tr_b16 v[72:73], v111 offset:24576\n\t"
  "s_add_u32 s45, %[sW2], 20480\n\t"
  "v_mfma_f32_16x16x32_bf16 v[56:59], a[168:171], v[80:83], v[56:59]\n\t"
  "ds_read_b64_tr_b16 v[74:75], v111 offset:26624\n\t"
  "v_cvt_pk_bf16_f32 v112, v20, v21\n\t"
  "v_mfma_f32_16x16x32_bf16 v[60:63], a[172:175], v[80:83], v[60:63]\n\t"
  "v_max_f32 v24, 0, v24\n\t"
  "v_max_f32 v25, 0, v25\n\t"
  "v_mfma_f32_16x16x32_bf16 v[64:67], a[176:179], v[80:83], v[64:67]\n\t"
  "s_waitcnt lgkmcnt(4)\n\t"
  "v_mul_f32 v26, v26, v128\n\t"
  "v_mfma_f32_16x16x32_bf16 v[44:47], a[180:183], v[84:87], v[44:47]\n\t"
  "ds_read_b64_tr_b16 v[76:77], v110 offset:28672\n\t"
  "v_mul_f32 v27, v27, v128\n\t"
  "v_mfma_f32_16x16x32_bf16 v[48:51], a[184:187], v[84:87], v[48:51]\n\t"
  "ds_read_b64_tr_b16 v[78:79], v110 offset:30720\n\t"
  "buffer_load_dwordx4 v[224:227], %[vl16], %[srd], s45 offen offset:0 sc1\n\t"
  "v_mfma_f32_16x16x32_bf16 v[52:55], a[188:191], v[84:87], v[52:55]\n\t"
  "ds_read_b64_tr_b16 v[80:81], v111 offset:28672\n\t"
  "v_mul_f32 v24, v24, v26\n\t"
  "v_mfma_f32_16x16x32_bf16 v[56:59], a[180:183], v[88:91], v[56:59]\n\t"
  "ds_read_b64_tr_b16 v[82:83], v111 offset:30720\n\t"
  "v_mul_f32 v25, v25, v27\n\t"
  "v_mfma_f32_16x16x32_bf16 v[60:63], a[184:187], v[88:91], v[60:63]\n\t"
  "v_cvt_pk_bf16_f32 v117, v24, v25\n\t"
  "v_cvt_pk_bf16_f32 v113, v24, v25\n\t"
  "v_mfma_f32_16x16x32_bf16 v[64:67], a[188:191], v[88:91], v[64:67]\n\t"
  "s_waitcnt lgkmcnt(4)\n\t"
  "v_max_f32 v28, 0, v28\n\t"
  "v_mfma_f32_16x16x32_bf16 v[44:47], a[192:195], v[68:71], v[44:47]\n\t"
  "ds_read_b64_tr_b16 v[84:85], v110 offset:32768\n\t"
  "buffer_load_dwordx4 v[228:231], %[vl16], %[srd], s45 offen offset:1024 sc1\n\t"
  "v_mfma_f32_16x16x32_bf16 v[48:51], a[196:199], v[68:71], v[48:51]\n\t"
  "ds_read_b64_tr_b16 v[86:87], v110 offset:34816\n\t"
  "v_max_f32 v29, 0, v29\n\t"
  "v_mfma_f32_16x16x32_bf16 v[52:55], a[200:203], v[68:71], v[52:55]\n\t"
  "ds_read_b64_tr_b16 v[88:89], v111 offset:32768\n\t"
  "v_mul_f32 v30, v30, v128\n\t"
  "v_mfma_f32_16x16x32_bf16 v[56:59], a[192:195], v[72:75], v[56:59]\n\t"
  "ds_read_b64_tr_b16 v[90:91], v111 offset:34816\n\t"
  "v_mul_f32 v31, v31, v128\n\t"
  "v_mfma_f32_16x16x32_bf16 v[60:63], a[196:199], v[72:75], v[60:63]\n\t"
  "v_mul_f32 v28, v28, v30\n\t"
  "v_permlane16_swap_b32 v116, v117\n\t"
  "v_mfma_f32_16x16x32_bf16 v[64:67], a[200:203], v[72:75], v[64:67]\n\t"
  "s_waitcnt lgkmcnt(4)\n\t"
  "buffer_load_dwordx4 v[232:235], %[vl16], %[srd], s45 offen offset:2048 sc1\n\t"
  "v_mfma_f32_16x16x32_bf16 v[44:47], a[204:207], v[76:79], v[44:47]\n\t"
  "ds_read_b64_tr_b16 v[68:69], v110 offset:36864\n\t"
  "v_mul_f32 v29, v29, v31\n\t"
  "v_mfma_f32_16x16x32_bf16 v[48:51], a[208:211], v[76:79], v[48:51]\n\t"
  "ds_read_b64_tr_b16 v[70:71], v110 offset:38912\n\t"
  "v_cvt_pk_bf16_f32 v118, v28, v29\n\t"
  "v_mfma_f32_16x16x32_bf16 v[52:55], a[212:215], v[76:79], v[52:55]\n\t"
  "ds_read_b64_tr_b16 v[72:73], v111 offset:36864\n\t"
  "v_cvt_pk_bf16_f32 v114, v28, v29\n\t"
  "v_mfma_f32_16x16x32_bf16 v[56:59], a[204:207], v[80:83], v[56:59]\n\t"
  "ds_read_b64_tr_b16 v[74:75], v111 offset:38912\n\t"
  "s_mov_b32 s55, 0\n\t"
  "v_mfma_f32_16x16x32_bf16 v[60:63], a[208:211], v[80:83], v[60:63]\n\t"
  "buffer_load_dwordx4 v[236:239], %[vl16], %[srd], s45 offen offset:3072 sc1\n\t"
  "v_permlane16_swap_b32 v118, v115\n\t"
  "v_mfma_f32_16x16x32_bf16 v[64:67], a[212:215], v[80:83], v[64:67]\n\t"
  "s_waitcnt lgkmcnt(4)\n\t"
  "v_permlane32_swap_b32 v117, v115\n\t"
  "v_mfma_f32_16x16x32_bf16 v[44:47], a[216:219], v[84:87], v[44:47]\n\t"
  "ds_read_b64_tr_b16 v[76:77], v108 offset:0\n\t"
  "buffer_store_dwordx4 v[112:115], %[vh2st], %[srd], s55 offen sc1\n\t"
  "v_mfma_f32_16x16x32_bf16 v[48:51], a[220:223], v[84:87], v[48:51]\n\t"
  "ds_read_b64_tr_b16 v[78:79], v108 offset:2048\n\t"
  "v_max_f32 v32, 0, v32\n\t"
  "v_mfma_f32_16x16x32_bf16 v[52:55], a[224:227], v[84:87], v[52:55]\n\t"
  "ds_read_b64_tr_b16 v[80:81], v109 offset:0\n\t"
  "v_max_f32 v33, 0, v33\n\t"
  "v_mfma_f32_16x16x32_bf16 v[56:59], a[216:219], v[88:91], v[56:59]\n\t"
  "ds_read_b64_tr_b16 v[82:83], v109 offset:2048\n\t"
  "s_add_u32 s45, %[sW2], 24576\n\t"
  "v_mfma_f32_16x16x32_bf16 v[60:63], a[220:223], v[88:91], v[60:63]\n\t"
  "v_mul_f32 v34, v34, v129\n\t"
  "v_mul_f32 v35, v35, v129\n\t"
  "v_mfma_f32_16x16x32_bf16 v[64:67], a[224:227], v[88:91], v[64:67]\n\t"
  "s_waitcnt lgkmcnt(4)\n\t"
  "v_mul_f32 v32, v32, v34\n\t"
  "v_mfma_f32_16x16x32_bf16 v[44:47], a[228:231], v[68:71], v[44:47]\n\t"
  "ds_read_b64_tr_b16 v[84:85], v108 offset:4096\n\t"
  "v_mul_f32 v33, v33, v35\n\t"
  "v_mfma_f32_16x16x32_bf16 v[48:51], a[232:235], v[68:71], v[48:51]\n\t"
  "ds_read_b64_tr_b16 v[86:87], v108 offset:6144\n\t"
  "v_cvt_pk_bf16_f32 v124, v32, v33\n\t"
  "v_mfma_f32_16x16x32_bf16 v[52:55], a[236:239], v[68:71], v[52:55]\n\t"
  "ds_read_b64_tr_b16 v[88:89], v109 offset:4096\n\t"
  "buffer_load_dwordx4 v[240:243], %[vl16], %[srd], s45 offen offset:0 sc1\n\t"
  "v_mfma_f32_16x16x32_bf16 v[56:59], a[228:231], v[72:75], v[56:59]\n\t"
  "ds_read_b64_tr_b16 v[90:91], v109 offset:6144\n\t"
  "v_cvt_pk_bf16_f32 v120, v32, v33\n\t"
  "v_mfma_f32_16x16x32_bf16 v[60:63], a[232:235], v[72:75], v[60:63]\n\t"
  "v_max_f32 v36, 0, v36\n\t"
  "v_max_f32 v37, 0, v37\n\t"
  "v_mfma_f32_16x16x32_bf16 v[64:67], a[236:239], v[72:75], v[64:67]\n\t"
  "v_mul_f32 v38, v38, v129\n\t"
  "v_mul_f32 v39, v39, v129\n\t"
  "v_mfma_f32_16x16x16_bf16 v[44:47], a[240:241], v[96:97], v[44:47]\n\t"
  "v_mul_f32 v36, v36, v38\n\t"
  "v_mul_f32 v37, v37, v39\n\t"
  "v_mfma_f32_16x16x16_bf16 v[48:51], a[242:243], v[96:97], v[48:51]\n\t"
  "buffer_load_dwordx4 v[244:247], %[vl16], %[srd], s45 offen offset:1024 sc1\n\t"
  "v_cvt_pk_bf16_f32 v125, v36, v37\n\t"
  "v_mfma_f32_16x16x16_bf16 v[52:55], a[244:245], v[96:97], v[52:55]\n\t"
  "v_cvt_pk_bf16_f32 v121, v36, v37\n\t"
  "v_max_f32 v40, 0, v40\n\t"
  "v_mfma_f32_16x16x16_bf16 v[56:59], a[240:241], v[98:99], v[56:59]\n\t"
  "v_max_f32 v41, 0, v41\n\t"
  "v_mul_f32 v42, v42, v129\n\t"
  "v_mfma_f32_16x16x16_bf16 v[60:63], a[242:243], v[98:99], v[60:63]\n\t"
  "v_mul_f32 v43, v43, v129\n\t"
  "v_mul_f32 v40, v40, v42\n\t"
  "v_permlane16_swap_b32 v124, v125\n\t"
  "v_mul_f32 v41, v41, v43\n\t"
  "v_cvt_pk_bf16_f32 v126, v40, v41\n\t"
  "v_cvt_pk_bf16_f32 v122, v40, v41\n\t"
  "s_mov_b32 s55, 49152\n\t"
  "v_permlane16_swap_b32 v126, v123\n\t"
  "s_nop 1\n\t"
  "v_permlane32_swap_b32 v125, v123\n\t"
  "buffer_store_dwordx4 v[120:123], %[vh2st], %[srd], s55 offen sc1\n\t"
  "v_mfma_f32_16x16x16_bf16 v[64:67], a[244:245], v[98:99], v[64:67]\n\t"
  "s_waitcnt lgkmcnt(4)\n\t"
  "v_mfma_f32_16x16x32_bf16 v[20:23], a[0:3], v[76:79], 0\n\t"
  "ds_read_b64_tr_b16 v[68:69], v108 offset:8192\n\t"
  "v_add_u32 v110, 122880, %[vtr]\n\t"
  "v_mfma_f32_16x16x32_bf16 v[24:27], a[4:7], v[76:79], 0\n\t"
  "ds_read_b64_tr_b16 v[70:71], v108 offset:10240\n\t"
  "v_xor_b32 v111, 32, %[vtr]\n\t"
  "v_mfma_f32_16x16x32_bf16 v[28:31], a[8:11], v[76:79], 0\n\t"
  "ds_read_b64_tr_b16 v[72:73], v109 offset:8192\n\t"
  "v_add_u32 v111, 122880, v111\n\t"
  "v_mfma_f32_16x16x32_bf16 v[32:35], a[0:3], v[80:83], 0\n\t"
  "ds_read_b64_tr_b16 v[74:75], v109 offset:10240\n\t"
  "v_and_b32 v128, 0xffff0000, v97\n\t"
  "v_mfma_f32_16x16x32_bf16 v[36:39], a[4:7], v[80:83], 0\n\t"
  "v_and_b32 v129, 0xffff0000, v99\n\t"
  "v_rcp_f32 v128, v128\n\t"
  "v_mfma_f32_16x16x32_bf16 v[40:43], a[8:11], v[80:83], 0\n\t"
  "s_waitcnt lgkmcnt(4)\n\t"
  "v_rcp_f32 v129, v129\n\t"
  "v_mfma_f32_16x16x32_bf16 v[20:23], a[12:15], v[84:87], v[20:23]\n\t"
  "ds_read_b64_tr_b16 v[76:77], v108 offset:12288\n\t"
  "s_barrier\n\t"
  "v_mfma_f32_16x16x32_bf16 v[24:27], a[16:19], v[84:87], v[24:27]\n\t"
  "ds_read_b64_tr_b16 v[78:79], v108 offset:14336\n\t"
  "v_mul_f32 v128, v128, v128\n\t"
  "v_mfma_f32_16x16x32_bf16 v[28:31], a[20:23], v[84:87], v[28:31]\n\t"
  "ds_read_b64_tr_b16 v[80:81], v109 offset:12288\n\t"
  "v_mul_f32 v129, v129, v129\n\t"
  "v_mfma_f32_16x16x32_bf16 v[32:35], a[12:15], v[88:91], v[32:35]\n\t"
  "ds_read_b64_tr_b16 v[82:83], v109 offset:14336\n\t"
  "s_mul_i32 s52, %[w], 20480\n\t"
  "v_mfma_f32_16x16x32_bf16 v[36:39], a[16:19], v[88:91], v[36:39]\n\t"
  "v_max_f32 v44, 0, v44\n\t"
  "v_max_f32 v45, 0, v45\n\t"
  "v_mfma_f32_16x16x32_bf16 v[40:43], a[20:23], v[88:91], v[40:43]\n\t"
  "s_waitcnt lgkmcnt(4)\n\t"
  "v_mul_f32 v46, v46, v128\n\t"
  "v_mfma_f32_16x16x32_bf16 v[20:23], a[24:27], v[68:71], v[20:23]\n\t"
  "ds_read_b64_tr_b16 v[84:85], v108 offset:16384\n\t"
  "s_add_u32 m0, s52, 0\n\t"
  "v_mfma_f32_16x16x32_bf16 v[24:27], a[28:31], v[68:71], v[24:27]\n\t"
  "ds_read_b64_tr_b16 v[86:87], v108 offset:18432\n\t"
  "v_mul_f32 v47, v47, v128\n\t"
  "v_mfma_f32_16x16x32_bf16 v[28:31], a[32:35], v[68:71], v[28:31]\n\t"
  "ds_read_b64_tr_b16 v[88:89], v109 offset:16384\n\t"
  "v_mul_f32 v44, v44, v46\n\t"
  "v_mfma_f32_16x16x32_bf16 v[32:35], a[24:27], v[72:75], v[32:35]\n\t"
  "ds_read_b64_tr_b16 v[90:91], v109 offset:18432\n\t"
  "v_mul_f32 v45, v45, v47\n\t"
  "v_mfma_f32_16x16x32_bf16 v[36:39], a[28:31], v[72:75], v[36:39]\n\t"
  "s_add_u32 s46, %[sW2], 26624\n\t"
  "v_cvt_pk_bf16_f32 v116, v44, v45\n\t"
  "v_mfma_f32_16x16x32_bf16 v[40:43], a[32:35], v[72:75], v[40:43]\n\t"
  "s_waitcnt lgkmcnt(4)\n\t"
  "v_cvt_pk_bf16_f32 v112, v44, v45\n\t"
  "v_mfma_f32_16x16x32_bf16 v[20:23], a[36:39], v[76:79], v[20:23]\n\t"
  "ds_read_b64_tr_b16 v[68:69], v108 offset:20480\n\t"
  "v_max_f32 v48, 0, v48\n\t"
  "v_mfma_f32_16x16x32_bf16 v[24:27], a[40:43], v[76:79], v[24:27]\n\t"
  "ds_read_b64_tr_b16 v[70:71], v108 offset:22528\n\t"
  "buffer_load_dwordx4 %[vl16], %[srd], s46 offen offset:0 sc1 lds\n\t"
  "v_mfma_f32_16x16x32_bf16 v[28:31], a[44:47], v[76:79], v[28:31]\n\t"
  "ds_read_b64_tr_b16 v[72:73], v109 offset:20480\n\t"
  "v_max_f32 v49, 0, v49\n\t"
  "v_mfma_f32_16x16x32_bf16 v[32:35], a[36:39], v[80:83], v[32:35]\n\t"
  "ds_read_b64_tr_b16 v[74:75], v109 offset:22528\n\t"
  "v_mul_f32 v50, v50, v128\n\t"
  "v_mfma_f32_16x16x32_bf16 v[36:39], a[40:43], v[80:83], v[36:39]\n\t"
  "buffer_load_dwordx4 %[vl16], %[srd], s46 offen offset:1024 sc1 lds\n\t"
  "v_mul_f32 v51, v51, v128\n\t"
  "v_mfma_f32_16x16x32_bf16 v[40:43], a[44:47], v[80:83], v[40:43]\n\t"
  "s_waitcnt lgkmcnt(4)\n\t"
  "v_mul_f32 v48, v48, v50\n\t"
  "v_mfma_f32_16x16x32_bf16 v[20:23], a[48:51], v[84:87], v[20:23]\n\t"
  "ds_read_b64_tr_b16 v[76:77], v108 offset:24576\n\t"
  "v_mul_f32 v49, v49, v51\n\t"
  "v_mfma_f32_16x16x32_bf16 v[24:27], a[52:55], v[84:87], v[24:27]\n\t"
  "ds_read_b64_tr_b16 v[78:79], v108 offset:26624\n\t"
  "v_cvt_pk_bf16_f32 v117, v48, v49\n\t"
  "v_mfma_f32_16x16x32_bf16 v[28:31], a[56:59], v[84:87], v[28:31]\n\t"
  "ds_read_b64_tr_b16 v[80:81], v109 offset:24576\n\t"
  "buffer_load_dwordx4 %[vl16], %[srd], s46 offen offset:2048 sc1 lds\n\t"
  "v_mfma_f32_16x16x32_bf16 v[32:35], a[48:51], v[88:91], v[32:35]\n\t"
  "ds_read_b64_tr_b16 v[82:83], v109 offset:26624\n\t"
  "v_cvt_pk_bf16_f32 v113, v48, v49\n\t"
  "v_mfma_f32_16x16x32_bf16 v[36:39], a[52:55], v[88:91], v[36:39]\n\t"
  "v_max_f32 v52, 0, v52\n\t"
  "v_max_f32 v53, 0, v53\n\t"
  "v_mfma_f32_16x16x32_bf16 v[40:43], a[56:59], v[88:91], v[40:43]\n\t"
  "s_waitcnt lgkmcnt(4)\n\t"
  "buffer_load_dwordx4 %[vl16], %[srd], s46 offen offset:3072 sc1 lds\n\t"
  "v_mfma_f32_16x16x32_bf16 v[20:23], a[60:63], v[68:71], v[20:23]\n\t"
  "ds_read_b64_tr_b16 v[84:85], v108 offset:28672\n\t"
  "v_mul_f32 v54, v54, v128\n\t"
  "v_mfma_f32_16x16x32_bf16 v[24:27], a[64:67], v[68:71], v[24:27]\n\t"
  "ds_read_b64_tr_b16 v[86:87], v108 offset:30720\n\t"
  "v_mul_f32 v55, v55, v128\n\t"
  "v_mfma_f32_16x16x32_bf16 v[28:31], a[68:71], v[68:71], v[28:31]\n\t"
  "ds_read_b64_tr_b16 v[88:89], v109 offset:28672\n\t"
  "v_mul_f32 v52, v52, v54\n\t"
  "v_mfma_f32_16x16x32_bf16 v[32:35], a[60:63], v[72:75], v[32:35]\n\t"
  "ds_read_b64_tr_b16 v[90:91], v109 offset:30720\n\t"
  "s_add_u32 m0, s52, 4096\n\t"
  "v_mfma_f32_16x16x32_bf16 v[36:39], a[64:67], v[72:75], v[36:39]\n\t"
  "v_permlane16_swap_b32 v116, v117\n\t"
  "v_mul_f32 v53, v53, v55\n\t"
  "v_mfma_f32_16x16x32_bf16 v[40:43], a[68:71], v[72:75], v[40:43]\n\t"
  "s_waitcnt lgkmcnt(4)\n\t"
  "v_cvt_pk_bf16_f32 v118, v52, v53\n\t"
  "v_mfma_f32_16x16x32_bf16 v[20:23], a[72:75], v[76:79], v[20:23]\n\t"
  "ds_read_b64_tr_b16 v[68:69], v108 offset:32768\n\t"
  "s_add_u32 s46, %[sW2], 30720\n\t"
  "v_mfma_f32_16x16x32_bf16 v[24:27], a[76:79], v[76:79], v[24:27]\n\t"
  "ds_read_b64_tr_b16 v[70:71], v108 offset:34816\n\t"
  "v_cvt_pk_bf16_f32 v114, v52, v53\n\t"
  "v_mfma_f32_16x16x32_bf16 v[28:31], a[80:83], v[76:79], v[28:31]\n\t"
  "ds_read_b64_tr_b16 v[72:73], v109 offset:32768\n\t"
  "s_movk_i32 s56, 0\n\t"
  "v_mfma_f32_16x16x32_bf16 v[32:35], a[72:75], v[80:83], v[32:35]\n\t"
  "ds_read_b64_tr_b16 v[74:75], v109 offset:34816\n\t"
  "s_waitcnt vmcnt(4)\n\t"
  "v_mfma_f32_16x16x32_bf16 v[36:39], a[76:79], v[80:83], v[36:39]\n\t"
  "buffer_store_dwordx4 v[132:135], %[vflw], %[srd], s56 offen sc1\n\t"
  "buffer_load_dwordx4 %[vl16], %[srd], s46 offen offset:0 sc1 lds\n\t"
  "v_mfma_f32_16x16x32_bf16 v[40:43], a[80:83], v[80:83], v[40:43]\n\t"
  "s_waitcnt lgkmcnt(4)\n\t"
  "s_mov_b32 s55, 98304\n\t"
  "v_mfma_f32_16x16x32_bf16 v[20:23], a[84:87], v[84:87], v[20:23]\n\t"
  "ds_read_b64_tr_b16 v[76:77], v108 offset:36864\n\t"
  "v_permlane16_swap_b32 v118, v115\n\t"
  "v_mfma_f32_16x16x32_bf16 v[24:27], a[88:91], v[84:87], v[24:27]\n\t"
  "ds_read_b64_tr_b16 v[78:79], v108 offset:38912\n\t"
  "buffer_load_dwordx4 %[vl16], %[srd], s46 offen offset:1024 sc1 lds\n\t"
  "v_mfma_f32_16x16x32_bf16 v[28:31], a[92:95], v[84:87], v[28:31]\n\t"
  "ds_read_b64_tr_b16 v[80:81], v109 offset:36864\n\t"
  "v_permlane32_swap_b32 v117, v115\n\t"
  "v_mfma_f32_16x16x32_bf16 v[32:35], a[84:87], v[88:91], v[32:35]\n\t"
  "ds_read_b64_tr_b16 v[82:83], v109 offset:38912\n\t"
  "buffer_store_dwordx4 v[112:115], %[vh2st], %[srd], s55 offen sc1\n\t"
  "v_mfma_f32_16x16x32_bf16 v[36:39], a[88:91], v[88:91], v[36:39]\n\t"
  "buffer_load_dwordx4 %[vl16], %[srd], s46 offen offset:2048 sc1 lds\n\t"
  "v_max_f32 v56, 0, v56\n\t"
  "v_mfma_f32_16x16x32_bf16 v[40:43], a[92:95], v[88:91], v[40:43]\n\t"
  "s_waitcnt lgkmcnt(4)\n\t"
  "v_max_f32 v57, 0, v57\n\t"
  "v_mfma_f32_16x16x32_bf16 v[20:23], a[96:99], v[68:71], v[20:23]\n\t"
  "ds_read_b64_tr_b16 v[84:85], v110 offset:0\n\t"
  "buffer_load_dwordx2 v[100:101], v136, %[srd], s54 offen offset:0 sc1\n\t"
  "v_mfma_f32_16x16x32_bf16 v[24:27], a[100:103], v[68:71], v[24:27]\n\t"
  "ds_read_b64_tr_b16 v[86:87], v110 offset:2048\n\t"
  "buffer_load_dwordx2 v[102:103], v136, %[srd], s54 offen offset:1024 sc1\n\t"
  "v_mfma_f32_16x16x32_bf16 v[28:31], a[104:107], v[68:71], v[28:31]\n\t"
  "ds_read_b64_tr_b16 v[88:89], v111 offset:0\n\t"
  "buffer_load_dwordx2 v[104:105], v136, %[srd], s54 offen offset:2048 sc1\n\t"
  "v_mfma_f32_16x16x32_bf16 v[32:35], a[96:99], v[72:75], v[32:35]\n\t"
  "ds_read_b64_tr_b16 v[90:91], v111 offset:2048\n\t"
  "buffer_load_dwordx2 v[106:107], v136, %[srd], s54 offen offset:3072 sc1\n\t"
  "v_mfma_f32_16x16x32_bf16 v[36:39], a[100:103], v[72:75], v[36:39]\n\t"
  "buffer_load_dwordx4 %[vl16], %[srd], s46 offen offset:3072 sc1 lds\n\t"
  "v_mul_f32 v58, v58, v129\n\t"
  "v_mfma_f32_16x16x32_bf16 v[40:43], a[104:107], v[72:75], v[40:43]\n\t"
  "s_waitcnt lgkmcnt(4)\n\t"
  "s_add_u32 m0, s52, 8192\n\t"
  "v_mfma_f32_16x16x32_bf16 v[20:23], a[108:111], v[76:79], v[20:23]\n\t"
  "ds_read_b64_tr_b16 v[68:69], v110 offset:4096\n\t"
  "v_mul_f32 v59, v59, v129\n\t"
  "v_mfma_f32_16x16x32_bf16 v[24:27], a[112:115], v[76:79], v[24:27]\n\t"
  "ds_read_b64_tr_b16 v[70:71], v110 offset:6144\n\t"
  "v_mul_f32 v56, v56, v58\n\t"
  "v_mfma_f32_16x16x32_bf16 v[28:31], a[116:119], v[76:79], v[28:31]\n\t"
  "ds_read_b64_tr_b16 v[72:73], v111 offset:4096\n\t"
  "s_add_u32 s46, %[sW2], 34816\n\t"
  "v_mfma_f32_16x16x32_bf16 v[32:35], a[108:111], v[80:83], v[32:35]\n\t"
  "ds_read_b64_tr_b16 v[74:75], v111 offset:6144\n\t"
  "v_mul_f32 v57, v57, v59\n\t"
  "v_mfma_f32_16x16x32_bf16 v[36:39], a[112:115], v[80:83], v[36:39]\n\t"
  "v_cvt_pk_bf16_f32 v124, v56, v57\n\t"
  "v_cvt_pk_bf16_f32 v120, v56, v57\n\t"
  "v_mfma_f32_16x16x32_bf16 v[40:43], a[116:119], v[80:83], v[40:43]\n\t"
  "s_waitcnt lgkmcnt(4)\n\t"
  "v_max_f32 v60, 0, v60\n\t"
  "v_mfma_f32_16x16x32_bf16 v[20:23], a[120:123], v[84:87], v[20:23]\n\t"
  "ds_read_b64_tr_b16 v[76:77], v110 offset:8192\n\t"
  "v_xor_b32 v108, 64, %[vtr]\n\t"
  "v_mfma_f32_16x16x32_bf16 v[24:27], a[124:127], v[84:87], v[24:27]\n\t"
  "ds_read_b64_tr_b16 v[78:79], v110 offset:10240\n\t"
  "v_add_u32 v108, 81920, v108\n\t"
  "v_mfma_f32_16x16x32_bf16 v[28:31], a[128:131], v[84:87], v[28:31]\n\t"
  "ds_read_b64_tr_b16 v[80:81], v111 offset:8192\n\t"
  "v_xor_b32 v109, 96, %[vtr]\n\t"
  "v_mfma_f32_16x16x32_bf16 v[32:35], a[120:123], v[88:91], v[32:35]\n\t"
  "ds_read_b64_tr_b16 v[82:83], v111 offset:10240\n\t"
  "v_add_u32 v109, 81920, v109\n\t"
  "v_mfma_f32_16x16x32_bf16 v[36:39], a[124:127], v[88:91], v[36:39]\n\t"
  "buffer_load_dwordx4 %[vl16], %[srd], s46 offen offset:0 sc1 lds\n\t"
  "buffer_load_dwordx4 %[vl16], %[srd], s46 offen offset:1024 sc1 lds\n\t"
  "v_mfma_f32_16x16x32_bf16 v[40:43], a[128:131], v[88:91], v[40:43]\n\t"
  "s_waitcnt lgkmcnt(4)\n\t"
  "v_max_f32 v61, 0, v61\n\t"
  "v_mfma_f32_16x16x32_bf16 v[20:23], a[132:135], v[68:71], v[20:23]\n\t"
  "ds_read_b64_tr_b16 v[84:85], v110 offset:12288\n\t"
  "buffer_load_dwordx4 %[vl16], %[srd], s46 offen offset:2048 sc1 lds\n\t"
  "v_mfma_f32_16x16x32_bf16 v[24:27], a[136:139], v[68:71], v[24:27]\n\t"
  "ds_read_b64_tr_b16 v[86:87], v110 offset:14336\n\t"
  "v_mul_f32 v62, v62, v129\n\t"
  "v_mfma_f32_16x16x32_bf16 v[28:31], a[140:143], v[68:71], v[28:31]\n\t"
  "ds_read_b64_tr_b16 v[88:89], v111 offset:12288\n\t"
  "v_mul_f32 v63, v63, v129\n\t"
  "v_mfma_f32_16x16x32_bf16 v[32:35], a[132:135], v[72:75], v[32:35]\n\t"
  "ds_read_b64_tr_b16 v[90:91], v111 offset:14336\n\t"
  "v_mul_f32 v60, v60, v62\n\t"
  "v_mfma_f32_16x16x32_bf16 v[36:39], a[136:139], v[72:75], v[36:39]\n\t"
  "buffer_load_dwordx4 %[vl16], %[srd], s46 offen offset:3072 sc1 lds\n\t"
  "v_mul_f32 v61, v61, v63\n\t"
  "v_mfma_f32_16x16x32_bf16 v[40:43], a[140:143], v[72:75], v[40:43]\n\t"
  "s_waitcnt lgkmcnt(4)\n\t"
  "v_cvt_pk_bf16_f32 v125, v60, v61\n\t"
  "v_mfma_f32_16x16x32_bf16 v[20:23], a[144:147], v[76:79], v[20:23]\n\t"
  "ds_read_b64_tr_b16 v[68:69], v110 offset:16384\n\t"
  "v_cvt_pk_bf16_f32 v121, v60, v61\n\t"
  "v_mfma_f32_16x16x32_bf16 v[24:27], a[148:151], v[76:79], v[24:27]\n\t"
  "ds_read_b64_tr_b16 v[70:71], v110 offset:18432\n\t"
  "s_add_u32 m0, s52, 12288\n\t"
  "v_mfma_f32_16x16x32_bf16 v[28:31], a[152:155], v[76:79], v[28:31]\n\t"
  "ds_read_b64_tr_b16 v[72:73], v111 offset:16384\n\t"
  "v_max_f32 v64, 0, v64\n\t"
  "v_mfma_f32_16x16x32_bf16 v[32:35], a[144:147], v[80:83], v[32:35]\n\t"
  "ds_read_b64_tr_b16 v[74:75], v111 offset:18432\n\t"
  "v_max_f32 v65, 0, v65\n\t"
  "v_mfma_f32_16x16x32_bf16 v[36:39], a[148:151], v[80:83], v[36:39]\n\t"
  "s_add_u32 s46, %[sW2], 38912\n\t"
  "v_mul_f32 v66, v66, v129\n\t"
  "v_mfma_f32_16x16x32_bf16 v[40:43], a[152:155], v[80:83], v[40:43]\n\t"
  "s_waitcnt lgkmcnt(4)\n\t"
  "v_mul_f32 v67, v67, v129\n\t"
  "v_mfma_f32_16x16x32_bf16 v[20:23], a[156:159], v[84:87], v[20:23]\n\t"
  "ds_read_b64_tr_b16 v[76:77], v110 offset:20480\n\t"
  "v_mul_f32 v64, v64, v66\n\t"
  "v_mfma_f32_16x16x32_bf16 v[24:27], a[160:163], v[84:87], v[24:27]\n\t"
  "ds_read_b64_tr_b16 v[78:79], v110 offset:22528\n\t"
  "v_permlane16_swap_b32 v124, v125\n\t"
  "v_mfma_f32_16x16x32_bf16 v[28:31], a[164:167], v[84:87], v[28:31]\n\t"
  "ds_read_b64_tr_b16 v[80:81], v111 offset:20480\n\t"
  "buffer_load_dwordx4 %[vl16], %[srd], s46 offen offset:0 sc1 lds\n\t"
  "v_mfma_f32_16x16x32_bf16 v[32:35], a[156:159], v[88:91], v[32:35]\n\t"
  "ds_read_b64_tr_b16 v[82:83], v111 offset:22528\n\t"
  "v_mul_f32 v65, v65, v67\n\t"
  "v_mfma_f32_16x16x32_bf16 v[36:39], a[160:163], v[88:91], v[36:39]\n\t"
  "v_cvt_pk_bf16_f32 v126, v64, v65\n\t"
  "v_cvt_pk_bf16_f32 v122, v64, v65\n\t"
  "v_mfma_f32_16x16x32_bf16 v[40:43], a[164:167], v[88:91], v[40:43]\n\t"
  "s_waitcnt lgkmcnt(4)\n\t"
  "buffer_load_dwordx4 %[vl16], %[srd], s46 offen offset:1024 sc1 lds\n\t"
  "v_mfma_f32_16x16x32_bf16 v[20:23], a[168:171], v[68:71], v[20:23]\n\t"
  "ds_read_b64_tr_b16 v[84:85], v110 offset:24576\n\t"
  "s_mov_b32 s55, 147456\n\t"
  "v_mfma_f32_16x16x32_bf16 v[24:27], a[172:175], v[68:71], v[24:27]\n\t"
  "ds_read_b64_tr_b16 v[86:87], v110 offset:26624\n\t"
  "v_permlane16_swap_b32 v126, v123\n\t"
  "v_mfma_f32_16x16x32_bf16 v[28:31], a[176:179], v[68:71], v[28:31]\n\t"
  "ds_read_b64_tr_b16 v[88:89], v111 offset:24576\n\t"
  "v_permlane32_swap_b32 v125, v123\n\t"
  "v_mfma_f32_16x16x32_bf16 v[32:35], a[168:171], v[72:75], v[32:35]\n\t"
  "ds_read_b64_tr_b16 v[90:91], v111 offset:26624\n\t"
  "buffer_load_dwordx4 %[vl16], %[srd], s46 offen offset:2048 sc1 lds\n\t"
  "v_mfma_f32_16x16x32_bf16 v[36:39], a[172:175], v[72:75], v[36:39]\n\t"
  "buffer_store_dwordx4 v[120:123], %[vh2st], %[srd], s55 offen sc1\n\t"
  "v_mfma_f32_16x16x32_bf16 v[40:43], a[176:179], v[72:75], v[40:43]\n\t"
  "s_waitcnt lgkmcnt(4)\n\t"
  "v_mfma_f32_16x16x32_bf16 v[20:23], a[180:183], v[76:79], v[20:23]\n\t"
  "ds_read_b64_tr_b16 v[68:69], v110 offset:28672\n\t"
  "buffer_load_dwordx4 %[vl16], %[srd], s46 offen offset:3072 sc1 lds\n\t"
  "v_mfma_f32_16x16x32_bf16 v[24:27], a[184:187], v[76:79], v[24:27]\n\t"
  "ds_read_b64_tr_b16 v[70:71], v110 offset:30720\n\t"
  "s_waitcnt vmcnt(12)\n\t"
  "v_min_u32 v131, v101, v103\n\t"
  "v_mfma_f32_16x16x32_bf16 v[28:31], a[188:191], v[76:79], v[28:31]\n\t"
  "ds_read_b64_tr_b16 v[72:73], v111 offset:28672\n\t"
  "s_waitcnt vmcnt(10)\n\t"
  "v_min3_u32 v131, v131, v105, v107\n\t"
  "v_mfma_f32_16x16x32_bf16 v[32:35], a[180:183], v[80:83], v[32:35]\n\t"
  "ds_read_b64_tr_b16 v[74:75], v111 offset:30720\n\t"
  "v_cmp_eq_u32 vcc, 0, v131\n\t"
  "v_mfma_f32_16x16x32_bf16 v[36:39], a[184:187], v[80:83], v[36:39]\n\t"
  "s_add_u32 m0, s52, 16384\n\t"
  "v_mfma_f32_16x16x32_bf16 v[40:43], a[188:191], v[80:83], v[40:43]\n\t"
  "s_waitcnt lgkmcnt(4)\n\t"
  "s_cbranch_vccnz Lst1_slow%=\n\t"
  "Lst1_back%=:\n\t"
  "v_mfma_f32_16x16x32_bf16 v[20:23], a[192:195], v[84:87], v[20:23]\n\t"
  "ds_read_b64_tr_b16 v[76:77], v110 offset:32768\n\t"
  "v_mfma_f32_16x16x32_bf16 v[24:27], a[196:199], v[84:87], v[24:27]\n\t"
  "ds_read_b64_tr_b16 v[78:79], v110 offset:34816\n\t"
  "s_add_u32 s46, %[sW2], 43008\n\t"
  "v_mfma_f32_16x16x32_bf16 v[28:31], a[200:203], v[84:87], v[28:31]\n\t"
  "ds_read_b64_tr_b16 v[80:81], v111 offset:32768\n\t"
  "v_mfma_f32_16x16x32_bf16 v[32:35], a[192:195], v[88:91], v[32:35]\n\t"
  "ds_read_b64_tr_b16 v[82:83], v111 offset:34816\n\t"
  "v_mfma_f32_16x16x32_bf16 v[36:39], a[196:199], v[88:91], v[36:39]\n\t"
  "buffer_load_dwordx4 %[vl16], %[srd], s46 offen offset:0 sc1 lds\n\t"
  "v_mfma_f32_16x16x32_bf16 v[40:43], a[200:203], v[88:91], v[40:43]\n\t"
  "s_waitcnt lgkmcnt(4)\n\t"
  "v_mfma_f32_16x16x32_bf16 v[20:23], a[204:207], v[68:71], v[20:23]\n\t"
  "ds_read_b64_tr_b16 v[84:85], v110 offset:36864\n\t"
  "v_mfma_f32_16x16x32_bf16 v[24:27], a[208:211], v[68:71], v[24:27]\n\t"
  "ds_read_b64_tr_b16 v[86:87], v110 offset:38912\n\t"
  "buffer_load_dwordx4 %[vl16], %[srd], s46 offen offset:1024 sc1 lds\n\t"
  "v_mfma_f32_16x16x32_bf16 v[28:31], a[212:215], v[68:71], v[28:31]\n\t"
  "ds_read_b64_tr_b16 v[88:89], v111 offset:36864\n\t"
  "v_mfma_f32_16x16x32_bf16 v[32:35], a[204:207], v[72:75], v[32:35]\n\t"
  "ds_read_b64_tr_b16 v[90:91], v111 offset:38912\n\t"
  "v_mfma_f32_16x16x32_bf16 v[36:39], a[208:211], v[72:75], v[36:39]\n\t"
  "v_mfma_f32_16x16x32_bf16 v[40:43], a[212:215], v[72:75], v[40:43]\n\t"
  "s_waitcnt lgkmcnt(4)\n\t"
  "buffer_load_dwordx4 %[vl16], %[srd], s46 offen offset:2048 sc1 lds\n\t"
  "v_mfma_f32_16x16x32_bf16 v[20:23], a[216:219], v[76:79], v[20:23]\n\t"
  "ds_read_b64_tr_b16 v[68:69], v108 offset:0\n\t"
  "v_mfma_f32_16x16x32_bf16 v[24:27], a[220:223], v[76:79], v[24:27]\n\t"
  "ds_read_b64_tr_b16 v[70:71], v108 offset:2048\n\t"
  "v_mfma_f32_16x16x32_bf16 v[28:31], a[224:227], v[76:79], v[28:31]\n\t"
  "ds_read_b64_tr_b16 v[72:73], v109 offset:0\n\t"
  "buffer_load_dwordx4 %[vl16], %[srd], s46 offen offset:3072 sc1 lds\n\t"
  "v_mfma_f32_16x16x32_bf16 v[32:35], a[216:219], v[80:83], v[32:35]\n\t"
  "ds_read_b64_tr_b16 v[74:75], v109 offset:2048\n\t"
  "v_mfma_f32_16x16x32_bf16 v[36:39], a[220:223], v[80:83], v[36:39]\n\t"
  "v_mfma_f32_16x16x32_bf16 v[40:43], a[224:227], v[80:83], v[40:43]\n\t"
  "s_waitcnt lgkmcnt(4)\n\t"
  "v_mfma_f32_16x16x32_bf16 v[20:23], a[228:231], v[84:87], v[20:23]\n\t"
  "ds_read_b64_tr_b16 v[76:77], v108 offset:4096\n\t"
  "v_mfma_f32_16x16x32_bf16 v[24:27], a[232:235], v[84:87], v[24:27]\n\t"
  "ds_read_b64_tr_b16 v[78:79], v108 offset:6144\n\t"
  "v_mfma_f32_16x16x32_bf16 v[28:31], a[236:239], v[84:87], v[28:31]\n\t"
  "ds_read_b64_tr_b16 v[80:81], v109 offset:4096\n\t"
  "v_mfma_f32_16x16x32_bf16 v[32:35], a[228:231], v[88:91], v[32:35]\n\t"
  "ds_read_b64_tr_b16 v[82:83], v109 offset:6144\n\t"
  "v_mfma_f32_16x16x32_bf16 v[36:39], a[232:235], v[88:91], v[36:39]\n\t"
  "v_mfma_f32_16x16x32_bf16 v[40:43], a[236:239], v[88:91], v[40:43]\n\t"
  "v_mfma_f32_16x16x16_bf16 v[20:23], a[240:241], v[100:101], v[20:23]\n\t"
  "v_mfma_f32_16x16x16_bf16 v[24:27], a[242:243], v[100:101], v[24:27]\n\t"
  "v_mfma_f32_16x16x16_bf16 v[28:31], a[244:245], v[100:101], v[28:31]\n\t"
  "v_mfma_f32_16x16x16_bf16 v[32:35], a[240:241], v[102:103], v[32:35]\n\t"
  "v_mfma_f32_16x16x16_bf16 v[36:39], a[242:243], v[102:103], v[36:39]\n\t"
  "v_mfma_f32_16x16x16_bf16 v[40:43], a[244:245], v[102:103], v[40:43]\n\t"
  "s_waitcnt lgkmcnt(4)\n\t"
  "v_mfma_f32_16x16x32_bf16 v[44:47], a[0:3], v[68:71], 0\n\t"
  "ds_read_b64_tr_b16 v[84:85], v108 offset:8192\n\t"
  "v_xor_b32 v110, 64, %[vtr]\n\t"
  "v_mfma_f32_16x16x32_bf16 v[48:51], a[4:7], v[68:71], 0\n\t"
  "ds_read_b64_tr_b16 v[86:87], v108 offset:10240\n\t"
  "v_add_u32 v110, 122880, v110\n\t"
  "v_mfma_f32_16x16x32_bf16 v[52:55], a[8:11], v[68:71], 0\n\t"
  "ds_read_b64_tr_b16 v[88:89], v109 offset:8192\n\t"
  "s_movk_i32 s56, 8192\n\t"
  "v_mfma_f32_16x16x32_bf16 v[56:59], a[0:3], v[72:75], 0\n\t"
  "ds_read_b64_tr_b16 v[90:91], v109 offset:10240\n\t"
  "s_waitcnt vmcnt(5)\n\t"
  "v_mfma_f32_16x16x32_bf16 v[60:63], a[4:7], v[72:75], 0\n\t"
  "v_xor_b32 v111, 96, %[vtr]\n\t"
  "v_add_u32 v111, 122880, v111\n\t"
  "v_mfma_f32_16x16x32_bf16 v[64:67], a[8:11], v[72:75], 0\n\t"
  "s_waitcnt lgkmcnt(4)\n\t"
  "buffer_store_dwordx4 v[132:135], %[vflw], %[srd], s56 offen sc1\n\t"
  "v_mfma_f32_16x16x32_bf16 v[44:47], a[12:15], v[76:79], v[44:47]\n\t"
  "ds_read_b64_tr_b16 v[68:69], v108 offset:12288\n\t"
  "s_add_u32 s45, %[sW2], 47104\n\t"
  "v_mfma_f32_16x16x32_bf16 v[48:51], a[16:19], v[76:79], v[48:51]\n\t"
  "ds_read_b64_tr_b16 v[70:71], v108 offset:14336\n\t"
  "v_and_b32 v128, 0xffff0000, v101\n\t"
  "v_mfma_f32_16x16x32_bf16 v[52:55], a[20:23], v[76:79], v[52:55]\n\t"
  "ds_read_b64_tr_b16 v[72:73], v109 offset:12288\n\t"
  "v_and_b32 v129, 0xffff0000, v103\n\t"
  "v_mfma_f32_16x16x32_bf16 v[56:59], a[12:15], v[80:83], v[56:59]\n\t"
  "ds_read_b64_tr_b16 v[74:75], v109 offset:14336\n\t"
  "buffer_load_dwordx4 a[0:3], %[vl16], %[srd], s45 offen offset:0 sc1\n\t"
  "v_mfma_f32_16x16x32_bf16 v[60:63], a[16:19], v[80:83], v[60:63]\n\t"
  "v_rcp_f32 v128, v128\n\t"
  "v_rcp_f32 v129, v129\n\t"
  "v_mfma_f32_16x16x32_bf16 v[64:67], a[20:23], v[80:83], v[64:67]\n\t"
  "s_waitcnt lgkmcnt(4)\n\t"
  "v_mul_f32 v128, v128, v128\n\t"
  "v_mfma_f32_16x16x32_bf16 v[44:47], a[24:27], v[84:87], v[44:47]\n\t"
  "ds_read_b64_tr_b16 v[76:77], v108 offset:16384\n\t"
  "buffer_load_dwordx4 a[4:7], %[vl16], %[srd], s45 offen offset:1024 sc1\n\t"
  "v_mfma_f32_16x16x32_bf16 v[48:51], a[28:31], v[84:87], v[48:51]\n\t"
  "ds_read_b64_tr_b16 v[78:79], v108 offset:18432\n\t"
  "v_mul_f32 v129, v129, v129\n\t"
  "v_mfma_f32_16x16x32_bf16 v[52:55], a[32:35], v[84:87], v[52:55]\n\t"
  "ds_read_b64_tr_b16 v[80:81], v109 offset:16384\n\t"
  "v_max_f32 v20, 0, v20\n\t"
  "v_mfma_f32_16x16x32_bf16 v[56:59], a[24:27], v[88:91], v[56:59]\n\t"
  "ds_read_b64_tr_b16 v[82:83], v109 offset:18432\n\t"
  "v_max_f32 v21, 0, v21\n\t"
  "v_mfma_f32_16x16x32_bf16 v[60:63], a[28:31], v[88:91], v[60:63]\n\t"
  "buffer_load_dwordx4 a[8:11], %[vl16], %[srd], s45 offen offset:2048 sc1\n\t"
  "v_mul_f32 v22, v22, v128\n\t"
  "v_mfma_f32_16x16x32_bf16 v[64:67], a[32:35], v[88:91], v[64:67]\n\t"
  "s_waitcnt lgkmcnt(4)\n\t"
  "v_mul_f32 v23, v23, v128\n\t"
  "v_mfma_f32_16x16x32_bf16 v[44:47], a[36:39], v[68:71], v[44:47]\n\t"
  "ds_read_b64_tr_b16 v[84:85], v108 offset:20480\n\t"
  "v_mul_f32 v20, v20, v22\n\t"
  "v_mfma_f32_16x16x32_bf16 v[48:51], a[40:43], v[68:71], v[48:51]\n\t"
  "ds_read_b64_tr_b16 v[86:87], v108 offset:22528\n\t"
  "buffer_load_dwordx4 a[12:15], %[vl16], %[srd], s45 offen offset:3072 sc1\n\t"
  "v_mfma_f32_16x16x32_bf16 v[52:55], a[44:47], v[68:71], v[52:55]\n\t"
  "ds_read_b64_tr_b16 v[88:89], v109 offset:20480\n\t"
  "v_mul_f32 v21, v21, v23\n\t"
  "v_mfma_f32_16x16x32_bf16 v[56:59], a[36:39], v[72:75], v[56:59]\n\t"
  "ds_read_b64_tr_b16 v[90:91], v109 offset:22528\n\t"
  "v_cvt_pk_bf16_f32 v116, v20, v21\n\t"
  "v_mfma_f32_16x16x32_bf16 v[60:63], a[40:43], v[72:75], v[60:63]\n\t"
  "s_add_u32 s45, %[sW2], 51200\n\t"
  "v_cvt_pk_bf16_f32 v112, v20, v21\n\t"
  "v_mfma_f32_16x16x32_bf16 v[64:67], a[44:47], v[72:75], v[64:67]\n\t"
  "s_waitcnt lgkmcnt(4)\n\t"
  "v_max_f32 v24, 0, v24\n\t"
  "v_mfma_f32_16x16x32_bf16 v[44:47], a[48:51], v[76:79], v[44:47]\n\t"
  "ds_read_b64_tr_b16 v[68:69], v108 offset:24576\n\t"
  "v_max_f32 v25, 0, v25\n\t"
  "v_mfma_f32_16x16x32_bf16 v[48:51], a[52:55], v[76:79], v[48:51]\n\t"
  "ds_read_b64_tr_b16 v[70:71], v108 offset:26624\n\t"
  "v_mul_f32 v26, v26, v128\n\t"
  "v_mfma_f32_16x16x32_bf16 v[52:55], a[56:59], v[76:79], v[52:55]\n\t"
  "ds_read_b64_tr_b16 v[72:73], v109 offset:24576\n\t"
  "buffer_load_dwordx4 a[16:19], %[vl16], %[srd], s45 offen offset:0 sc1\n\t"
  "v_mfma_f32_16x16x32_bf16 v[56:59], a[48:51], v[80:83], v[56:59]\n\t"
  "ds_read_b64_tr_b16 v[74:75], v109 offset:26624\n\t"
  "v_mul_f32 v27, v27, v128\n\t"
  "v_mfma_f32_16x16x32_bf16 v[60:63], a[52:55], v[80:83], v[60:63]\n\t"
  "v_mul_f32 v24, v24, v26\n\t"
  "v_mul_f32 v25, v25, v27\n\t"
  "v_mfma_f32_16x16x32_bf16 v[64:67], a[56:59], v[80:83], v[64:67]\n\t"
  "s_waitcnt lgkmcnt(4)\n\t"
  "buffer_load_dwordx4 a[20:23], %[vl16], %[srd], s45 offen offset:1024 sc1\n\t"
  "v_mfma_f32_16x16x32_bf16 v[44:47], a[60:63], v[84:87], v[44:47]\n\t"
  "ds_read_b64_tr_b16 v[76:77], v108 offset:28672\n\t"
  "s_add_u32 s57, %[sFL], 0\n\t"
  "v_mfma_f32_16x16x32_bf16 v[48:51], a[64:67], v[84:87], v[48:51]\n\t"
  "ds_read_b64_tr_b16 v[78:79], v108 offset:30720\n\t"
  "buffer_load_dwordx2 v[138:139], v137, %[srd], s57 offen sc1\n\t"
  "v_mfma_f32_16x16x32_bf16 v[52:55], a[68:71], v[84:87], v[52:55]\n\t"
  "ds_read_b64_tr_b16 v[80:81], v109 offset:28672\n\t"
  "buffer_load_dwordx4 a[24:27], %[vl16], %[srd], s45 offen offset:2048 sc1\n\t"
  "v_mfma_f32_16x16x32_bf16 v[56:59], a[60:63], v[88:91], v[56:59]\n\t"
  "ds_read_b64_tr_b16 v[82:83], v109 offset:30720\n\t"
  "v_cvt_pk_bf16_f32 v117, v24, v25\n\t"
  "v_mfma_f32_16x16x32_bf16 v[60:63], a[64:67], v[88:91], v[60:63]\n\t"
  "v_cvt_pk_bf16_f32 v113, v24, v25\n\t"
  "v_max_f32 v28, 0, v28\n\t"
  "v_mfma_f32_16x16x32_bf16 v[64:67], a[68:71], v[88:91], v[64:67]\n\t"
  "s_waitcnt lgkmcnt(4)\n\t"
  "v_max_f32 v29, 0, v29\n\t"
  "v_mfma_f32_16x16x32_bf16 v[44:47], a[72:75], v[68:71], v[44:47]\n\t"
  "ds_read_b64_tr_b16 v[84:85], v108 offset:32768\n\t"
  "buffer_load_dwordx4 a[28:31], %[vl16], %[srd], s45 offen offset:3072 sc1\n\t"
  "v_mfma_f32_16x16x32_bf16 v[48:51], a[76:79], v[68:71], v[48:51]\n\t"
  "ds_read_b64_tr_b16 v[86:87], v108 offset:34816\n\t"
  "v_mul_f32 v30, v30, v128\n\t"
  "v_mfma_f32_16x16x32_bf16 v[52:55], a[80:83], v[68:71], v[52:55]\n\t"
  "ds_read_b64_tr_b16 v[88:89], v109 offset:32768\n\t"
  "v_mul_f32 v31, v31, v128\n\t"
  "v_mfma_f32_16x16x32_bf16 v[56:59], a[72:75], v[72:75], v[56:59]\n\t"
  "ds_read_b64_tr_b16 v[90:91], v109 offset:34816\n\t"
  "s_add_u32 s45, %[sW2], 55296\n\t"
  "v_mfma_f32_16x16x32_bf16 v[60:63], a[76:79], v[72:75], v[60:63]\n\t"
  "v_mul_f32 v28, v28, v30\n\t"
  "v_permlane16_swap_b32 v116, v117\n\t"
  "v_mfma_f32_16x16x32_bf16 v[64:67], a[80:83], v[72:75], v[64:67]\n\t"
  "s_waitcnt lgkmcnt(4)\n\t"
  "v_mul_f32 v29, v29, v31\n\t"
  "v_mfma_f32_16x16x32_bf16 v[44:47], a[84:87], v[76:79], v[44:47]\n\t"
  "ds_read_b64_tr_b16 v[68:69], v108 offset:36864\n\t"
  "buffer_load_dwordx4 a[32:35], %[vl16], %[srd], s45 offen offset:0 sc1\n\t"
  "v_mfma_f32_16x16x32_bf16 v[48:51], a[88:91], v[76:79], v[48:51]\n\t"
  "ds_read_b64_tr_b16 v[70:71], v108 offset:38912\n\t"
  "v_cvt_pk_bf16_f32 v118, v28, v29\n\t"
  "v_mfma_f32_16x16x32_bf16 v[52:55], a[92:95], v[76:79], v[52:55]\n\t"
  "ds_read_b64_tr_b16 v[72:73], v109 offset:36864\n\t"
  "v_cvt_pk_bf16_f32 v114, v28, v29\n\t"
  "v_mfma_f32_16x16x32_bf16 v[56:59], a[84:87], v[80:83], v[56:59]\n\t"
  "ds_read_b64_tr_b16 v[74:75], v109 offset:38912\n\t"
  "s_mov_b32 s55, 3145728\n\t"
  "v_mfma_f32_16x16x32_bf16 v[60:63], a[88:91], v[80:83], v[60:63]\n\t"
  "buffer_load_dwordx4 a[36:39], %[vl16], %[srd], s45 offen offset:1024 sc1\n\t"
  "v_permlane16_swap_b32 v118, v115\n\t"
  "v_mfma_f32_16x16x32_bf16 v[64:67], a[92:95], v[80:83], v[64:67]\n\t"
  "s_waitcnt lgkmcnt(4)\n\t"
  "v_permlane32_swap_b32 v117, v115\n\t"
  "v_mfma_f32_16x16x32_bf16 v[44:47], a[96:99], v[84:87], v[44:47]\n\t"
  "ds_read_b64_tr_b16 v[76:77], v110 offset:0\n\t"
  "buffer_store_dwordx4 v[112:115], %[vh2st], %[srd], s55 offen sc1\n\t"
  "v_mfma_f32_16x16x32_bf16 v[48:51], a[100:103], v[84:87], v[48:51]\n\t"
  "ds_read_b64_tr_b16 v[78:79], v110 offset:2048\n\t"
  "buffer_load_dwordx4 a[40:43], %[vl16], %[srd], s45 offen offset:2048 sc1\n\t"
  "v_mfma_f32_16x16x32_bf16 v[52:55], a[104:107], v[84:87], v[52:55]\n\t"
  "ds_read_b64_tr_b16 v[80:81], v111 offset:0\n\t"
  "v_max_f32 v32, 0, v32\n\t"
  "v_mfma_f32_16x16x32_bf16 v[56:59], a[96:99], v[88:91], v[56:59]\n\t"
  "ds_read_b64_tr_b16 v[82:83], v111 offset:2048\n\t"
  "v_max_f32 v33, 0, v33\n\t"
  "v_mfma_f32_16x16x32_bf16 v[60:63], a[100:103], v[88:91], v[60:63]\n\t"
  "buffer_load_dwordx4 a[44:47], %[vl16], %[srd], s45 offen offset:3072 sc1\n\t"
  "v_mul_f32 v34, v34, v129\n\t"
  "v_mfma_f32_16x16x32_bf16 v[64:67], a[104:107], v[88:91], v[64:67]\n\t"
  "s_waitcnt lgkmcnt(4)\n\t"
  "v_mul_f32 v35, v35, v129\n\t"
  "v_mfma_f32_16x16x32_bf16 v[44:47], a[108:111], v[68:71], v[44:47]\n\t"
  "ds_read_b64_tr_b16 v[84:85], v110 offset:4096\n\t"
  "v_mul_f32 v32, v32, v34\n\t"
  "v_mfma_f32_16x16x32_bf16 v[48:51], a[112:115], v[68:71], v[48:51]\n\t"
  "ds_read_b64_tr_b16 v[86:87], v110 offset:6144\n\t"
  "v_mul_f32 v33, v33, v35\n\t"
  "v_mfma_f32_16x16x32_bf16 v[52:55], a[116:119], v[68:71], v[52:55]\n\t"
  "ds_read_b64_tr_b16 v[88:89], v111 offset:4096\n\t"
  "s_add_u32 s45, %[sW2], 59392\n\t"
  "v_mfma_f32_16x16x32_bf16 v[56:59], a[108:111], v[72:75], v[56:59]\n\t"
  "ds_read_b64_tr_b16 v[90:91], v111 offset:6144\n\t"
  "v_cvt_pk_bf16_f32 v124, v32, v33\n\t"
  "v_mfma_f32_16x16x32_bf16 v[60:63], a[112:115], v[72:75], v[60:63]\n\t"
  "v_cvt_pk_bf16_f32 v120, v32, v33\n\t"
  "v_max_f32 v36, 0, v36\n\t"
  "v_mfma_f32_16x16x32_bf16 v[64:67], a[116:119], v[72:75], v[64:67]\n\t"
  "s_waitcnt lgkmcnt(4)\n\t"
  "buffer_load_dwordx4 a[48:51], %[vl16], %[srd], s45 offen offset:0 sc1\n\t"
  "v_mfma_f32_16x16x32_bf16 v[44:47], a[120:123], v[76:79], v[44:47]\n\t"
  "ds_read_b64_tr_b16 v[68:69], v110 offset:8192\n\t"
  "v_max_f32 v37, 0, v37\n\t"
  "v_mfma_f32_16x16x32_bf16 v[48:51], a[124:127], v[76:79], v[48:51]\n\t"
  "ds_read_b64_tr_b16 v[70:71], v110 offset:10240\n\t"
  "v_mul_f32 v38, v38, v129\n\t"
  "v_mfma_f32_16x16x32_bf16 v[52:55], a[128:131], v[76:79], v[52:55]\n\t"
  "ds_read_b64_tr_b16 v[72:73], v111 offset:8192\n\t"
  "buffer_load_dwordx4 a[52:55], %[vl16], %[srd], s45 offen offset:1024 sc1\n\t"
  "v_mfma_f32_16x16x32_bf16 v[56:59], a[120:123], v[80:83], v[56:59]\n\t"
  "ds_read_b64_tr_b16 v[74:75], v111 offset:10240\n\t"
  "v_mul_f32 v39, v39, v129\n\t"
  "v_mfma_f32_16x16x32_bf16 v[60:63], a[124:127], v[80:83], v[60:63]\n\t"
  "v_mul_f32 v36, v36, v38\n\t"
  "v_mul_f32 v37, v37, v39\n\t"
  "v_mfma_f32_16x16x32_bf16 v[64:67], a[128:131], v[80:83], v[64:67]\n\t"
  "s_waitcnt lgkmcnt(4)\n\t"
  "v_cvt_pk_bf16_f32 v125, v36, v37\n\t"
  "v_mfma_f32_16x16x32_bf16 v[44:47], a[132:135], v[84:87], v[44:47]\n\t"
  "ds_read_b64_tr_b16 v[76:77], v110 offset:12288\n\t"
  "v_cvt_pk_bf16_f32 v121, v36, v37\n\t"
  "v_mfma_f32_16x16x32_bf16 v[48:51], a[136:139], v[84:87], v[48:51]\n\t"
  "ds_read_b64_tr_b16 v[78:79], v110 offset:14336\n\t"
  "v_max_f32 v40, 0, v40\n\t"
  "v_mfma_f32_16x16x32_bf16 v[52:55], a[140:143], v[84:87], v[52:55]\n\t"
  "ds_read_b64_tr_b16 v[80:81], v111 offset:12288\n\t"
  "v_max_f32 v41, 0, v41\n\t"
  "v_mfma_f32_16x16x32_bf16 v[56:59], a[132:135], v[88:91], v[56:59]\n\t"
  "ds_read_b64_tr_b16 v[82:83], v111 offset:14336\n\t"
  "v_mul_f32 v42, v42, v129\n\t"
  "v_mfma_f32_16x16x32_bf16 v[60:63], a[136:139], v[88:91], v[60:63]\n\t"
  "v_mul_f32 v43, v43, v129\n\t"
  "v_mul_f32 v40, v40, v42\n\t"
  "v_mfma_f32_16x16x32_bf16 v[64:67], a[140:143], v[88:91], v[64:67]\n\t"
  "s_waitcnt lgkmcnt(4)\n\t"
  "s_waitcnt vmcnt(9)\n\t"
  "v_cmp_ne_u64 vcc, v[132:133], v[138:139]\n\t"
  "v_mfma_f32_16x16x32_bf16 v[44:47], a[144:147], v[68:71], v[44:47]\n\t"
  "ds_read_b64_tr_b16 v[84:85], v110 offset:16384\n\t"
  "v_permlane16_swap_b32 v124, v125\n\t"
  "v_mfma_f32_16x16x32_bf16 v[48:51], a[148:151], v[68:71], v[48:51]\n\t"
  "ds_read_b64_tr_b16 v[86:87], v110 offset:18432\n\t"
  "s_cbranch_vccnz Lfl0_slow%=\n\t"
  "Lfl0_back%=:\n\t"
  "v_mfma_f32_16x16x32_bf16 v[52:55], a[152:155], v[68:71], v[52:55]\n\t"
  "ds_read_b64_tr_b16 v[88:89], v111 offset:16384\n\t"
  "s_add_u32 s44, %[sH2ld], 0\n\t"
  "v_mfma_f32_16x16x32_bf16 v[56:59], a[144:147], v[72:75], v[56:59]\n\t"
  "ds_read_b64_tr_b16 v[90:91], v111 offset:18432\n\t"
  "v_mul_f32 v41, v41, v43\n\t"
  "v_mfma_f32_16x16x32_bf16 v[60:63], a[148:151], v[72:75], v[60:63]\n\t"
  "v_cvt_pk_bf16_f32 v126, v40, v41\n\t"
  "v_cvt_pk_bf16_f32 v122, v40, v41\n\t"
  "v_mfma_f32_16x16x32_bf16 v[64:67], a[152:155], v[72:75], v[64:67]\n\t"
  "s_waitcnt lgkmcnt(4)\n\t"
  "buffer_load_dwordx4 a[56:59], %[vl16], %[srd], s44 offen offset:0 sc1\n\t"
  "v_mfma_f32_16x16x32_bf16 v[44:47], a[156:159], v[76:79], v[44:47]\n\t"
  "ds_read_b64_tr_b16 v[68:69], v110 offset:20480\n\t"
  "s_mov_b32 s55, 3194880\n\t"
  "v_mfma_f32_16x16x32_bf16 v[48:51], a[160:163], v[76:79], v[48:51]\n\t"
  "ds_read_b64_tr_b16 v[70:71], v110 offset:22528\n\t"
  "v_permlane16_swap_b32 v126, v123\n\t"
  "v_mfma_f32_16x16x32_bf16 v[52:55], a[164:167], v[76:79], v[52:55]\n\t"
  "ds_read_b64_tr_b16 v[72:73], v111 offset:20480\n\t"
  "buffer_load_dwordx4 a[60:63], %[vl16], %[srd], s44 offen offset:1024 sc1\n\t"
  "v_mfma_f32_16x16x32_bf16 v[56:59], a[156:159], v[80:83], v[56:59]\n\t"
  "ds_read_b64_tr_b16 v[74:75], v111 offset:22528\n\t"
  "v_permlane32_swap_b32 v125, v123\n\t"
  "v_mfma_f32_16x16x32_bf16 v[60:63], a[160:163], v[80:83], v[60:63]\n\t"
  "buffer_store_dwordx4 v[120:123], %[vh2st], %[srd], s55 offen sc1\n\t"
  "v_and_b32 v128, 0xffff0000, v105\n\t"
  "v_mfma_f32_16x16x32_bf16 v[64:67], a[164:167], v[80:83], v[64:67]\n\t"
  "s_waitcnt lgkmcnt(4)\n\t"
  "v_and_b32 v129, 0xffff0000, v107\n\t"
  "v_mfma_f32_16x16x32_bf16 v[44:47], a[168:171], v[84:87], v[44:47]\n\t"
  "ds_read_b64_tr_b16 v[76:77], v110 offset:24576\n\t"
  "buffer_load_dwordx4 a[64:67], %[vl16], %[srd], s44 offen offset:2048 sc1\n\t"
  "v_mfma_f32_16x16x32_bf16 v[48:51], a[172:175], v[84:87], v[48:51]\n\t"
  "ds_read_b64_tr_b16 v[78:79], v110 offset:26624\n\t"
  "v_rcp_f32 v128, v128\n\t"
  "v_mfma_f32_16x16x32_bf16 v[52:55], a[176:179], v[84:87], v[52:55]\n\t"
  "ds_read_b64_tr_b16 v[80:81], v111 offset:24576\n\t"
  "v_rcp_f32 v129, v129\n\t"
  "v_mfma_f32_16x16x32_bf16 v[56:59], a[168:171], v[88:91], v[56:59]\n\t"
  "ds_read_b64_tr_b16 v[82:83], v111 offset:26624\n\t"
  "buffer_load_dwordx4 a[68:71], %[vl16], %[srd], s44 offen offset:3072 sc1\n\t"
  "v_mfma_f32_16x16x32_bf16 v[60:63], a[172:175], v[88:91], v[60:63]\n\t"
  "v_mul_f32 v128, v128, v128\n\t"
  "ds_read_b128 a[160:163], v141 offset:0\n\t"
  "v_mfma_f32_16x16x32_bf16 v[64:67], a[176:179], v[88:91], v[64:67]\n\t"
  "s_waitcnt lgkmcnt(5)\n\t"
  "ds_read_b128 a[164:167], v141 offset:1024\n\t"
  "v_mfma_f32_16x16x32_bf16 v[44:47], a[180:183], v[68:71], v[44:47]\n\t"
  "ds_read_b64_tr_b16 v[84:85], v110 offset:28672\n\t"
  "ds_read_b128 a[168:171], v141 offset:2048\n\t"
  "v_mfma_f32_16x16x32_bf16 v[48:51], a[184:187], v[68:71], v[48:51]\n\t"
  "ds_read_b64_tr_b16 v[86:87], v110 offset:30720\n\t"
  "s_add_u32 s44, %[sH2ld], 4096\n\t"
  "v_mfma_f32_16x16x32_bf16 v[52:55], a[188:191], v[68:71], v[52:55]\n\t"
  "ds_read_b64_tr_b16 v[88:89], v111 offset:28672\n\t"
  "ds_read_b128 a[172:175], v141 offset:3072\n\t"
  "v_mfma_f32_16x16x32_bf16 v[56:59], a[180:183], v[72:75], v[56:59]\n\t"
  "ds_read_b64_tr_b16 v[90:91], v111 offset:30720\n\t"
  "ds_read_b128 a[176:179], v141 offset:4096\n\t"
  "v_mfma_f32_16x16x32_bf16 v[60:63], a[184:187], v[72:75], v[60:63]\n\t"
  "buffer_load_dwordx4 a[72:75], %[vl16], %[srd], s44 offen offset:0 sc1\n\t"
  "v_mul_f32 v129, v129, v129\n\t"
  "v_mfma_f32_16x16x32_bf16 v[64:67], a[188:191], v[72:75], v[64:67]\n\t"
  "s_waitcnt lgkmcnt(9)\n\t"
  "ds_read_b128 a[180:183], v141 offset:5120\n\t"
  "v_mfma_f32_16x16x32_bf16 v[44:47], a[192:195], v[76:79], v[44:47]\n\t"
  "ds_read_b64_tr_b16 v[68:69], v110 offset:32768\n\t"
  "ds_read_b128 a[184:187], v141 offset:6144\n\t"
  "v_mfma_f32_16x16x32_bf16 v[48:51], a[196:199], v[76:79], v[48:51]\n\t"
  "ds_read_b64_tr_b16 v[70:71], v110 offset:34816\n\t"
  "ds_read_b128 a[188:191], v141 offset:7168\n\t"
  "v_mfma_f32_16x16x32_bf16 v[52:55], a[200:203], v[76:79], v[52:55]\n\t"
  "ds_read_b64_tr_b16 v[72:73], v111 offset:32768\n\t"
  "buffer_load_dwordx4 a[76:79], %[vl16], %[srd], s44 offen offset:1024 sc1\n\t"
  "v_mfma_f32_16x16x32_bf16 v[56:59], a[192:195], v[80:83], v[56:59]\n\t"
  "ds_read_b64_tr_b16 v[74:75], v111 offset:34816\n\t"
  "v_mfma_f32_16x16x32_bf16 v[60:63], a[196:199], v[80:83], v[60:63]\n\t"
  "v_mfma_f32_16x16x32_bf16 v[64:67], a[200:203], v[80:83], v[64:67]\n\t"
  "s_waitcnt lgkmcnt(8)\n\t"
  "buffer_load_dwordx4 a[80:83], %[vl16], %[srd], s44 offen offset:2048 sc1\n\t"
  "v_mfma_f32_16x16x32_bf16 v[44:47], a[204:207], v[84:87], v[44:47]\n\t"
  "ds_read_b64_tr_b16 v[76:77], v110 offset:36864\n\t"
  "ds_read_b128 a[192:195], v141 offset:8192\n\t"
  "v_mfma_f32_16x16x32_bf16 v[48:51], a[208:211], v[84:87], v[48:51]\n\t"
  "ds_read_b64_tr_b16 v[78:79], v110 offset:38912\n\t"
  "ds_read_b128 a[196:199], v141 offset:9216\n\t"
  "v_mfma_f32_16x16x32_bf16 v[52:55], a[212:215], v[84:87], v[52:55]\n\t"
  "ds_read_b64_tr_b16 v[80:81], v111 offset:36864\n\t"
  "ds_read_b128 a[200:203], v141 offset:10240\n\t"
  "v_mfma_f32_16x16x32_bf16 v[56:59], a[204:207], v[88:91], v[56:59]\n\t"
  "ds_read_b64_tr_b16 v[82:83], v111 offset:38912\n\t"
  "buffer_load_dwordx4 a[84:87], %[vl16], %[srd], s44 offen offset:3072 sc1\n\t"
  "v_mfma_f32_16x16x32_bf16 v[60:63], a[208:211], v[88:91], v[60:63]\n\t"
  "v_mfma_f32_16x16x32_bf16 v[64:67], a[212:215], v[88:91], v[64:67]\n\t"
  "s_waitcnt lgkmcnt(7)\n\t"
  "ds_read_b128 a[204:207], v141 offset:11264\n\t"
  "v_mfma_f32_16x16x32_bf16 v[44:47], a[216:219], v[68:71], v[44:47]\n\t"
  "s_add_u32 s44, %[sH2ld], 8192\n\t"
  "ds_read_b128 a[208:211], v141 offset:12288\n\t"
  "v_mfma_f32_16x16x32_bf16 v[48:51], a[220:223], v[68:71], v[48:51]\n\t"
  "ds_read_b128 a[212:215], v141 offset:13312\n\t"
  "v_mfma_f32_16x16x32_bf16 v[52:55], a[224:227], v[68:71], v[52:55]\n\t"
  "v_mfma_f32_16x16x32_bf16 v[56:59], a[216:219], v[72:75], v[56:59]\n\t"
  "v_mfma_f32_16x16x32_bf16 v[60:63], a[220:223], v[72:75], v[60:63]\n\t"
  "buffer_load_dwordx4 a[88:91], %[vl16], %[srd], s44 offen offset:0 sc1\n\t"
  "v_mfma_f32_16x16x32_bf16 v[64:67], a[224:227], v[72:75], v[64:67]\n\t"
  "s_waitcnt lgkmcnt(3)\n\t"
  "ds_read_b128 a[216:219], v141 offset:14336\n\t"
  "v_mfma_f32_16x16x32_bf16 v[44:47], a[228:231], v[76:79], v[44:47]\n\t"
  "s_add_u32 s57, %[sFL], 8192\n\t"
  "buffer_load_dwordx2 v[138:139], v137, %[srd], s57 offen sc1\n\t"
  "v_mfma_f32_16x16x32_bf16 v[48:51], a[232:235], v[76:79], v[48:51]\n\t"
  "buffer_load_dwordx4 a[92:95], %[vl16], %[srd], s44 offen offset:1024 sc1\n\t"
  "ds_read_b128 a[220:223], v141 offset:15360\n\t"
  "v_mfma_f32_16x16x32_bf16 v[52:55], a[236:239], v[76:79], v[52:55]\n\t"
  "ds_read_b128 a[224:227], v141 offset:16384\n\t"
  "v_mfma_f32_16x16x32_bf16 v[56:59], a[228:231], v[80:83], v[56:59]\n\t"
  "v_mfma_f32_16x16x32_bf16 v[60:63], a[232:235], v[80:83], v[60:63]\n\t"
  "v_mfma_f32_16x16x32_bf16 v[64:67], a[236:239], v[80:83], v[64:67]\n\t"
  "buffer_load_dwordx4 a[96:99], %[vl16], %[srd], s44 offen offset:2048 sc1\n\t"
  "ds_read_b128 a[228:231], v141 offset:17408\n\t"
  "v_mfma_f32_16x16x16_bf16 v[44:47], a[240:241], v[104:105], v[44:47]\n\t"
  "ds_read_b128 a[232:235], v141 offset:18432\n\t"
  "ds_read_b128 a[236:239], v141 offset:19456\n\t"
  "v_mfma_f32_16x16x16_bf16 v[48:51], a[242:243], v[104:105], v[48:51]\n\t"
  "v_mfma_f32_16x16x16_bf16 v[52:55], a[244:245], v[104:105], v[52:55]\n\t"
  "s_movk_i32 s56, 16384\n\t"
  "s_waitcnt vmcnt(10)\n\t"
  "v_mfma_f32_16x16x16_bf16 v[56:59], a[240:241], v[106:107], v[56:59]\n\t"
  "buffer_store_dwordx4 v[132:135], %[vflw], %[srd], s56 offen sc1\n\t"
  "buffer_load_dwordx4 a[100:103], %[vl16], %[srd], s44 offen offset:3072 sc1\n\t"
  "v_mfma_f32_16x16x16_bf16 v[60:63], a[242:243], v[106:107], v[60:63]\n\t"
  "v_mfma_f32_16x16x16_bf16 v[64:67], a[244:245], v[106:107], v[64:67]\n\t"
  "v_max_f32 v44, 0, v44\n\t"
  "v_max_f32 v45, 0, v45\n\t"
  "v_pk_mul_f32 v[46:47], v[46:47], v[128:129] op_sel:[0,0] op_sel_hi:[1,0]\n\t"
  "v_pk_mul_f32 v[44:45], v[44:45], v[46:47]\n\t"
  "v_cvt_pk_bf16_f32 v116, v44, v45\n\t"
  "v_cvt_pk_bf16_f32 v112, v44, v45\n\t"
  "v_max_f32 v48, 0, v48\n\t"
  "v_max_f32 v49, 0, v49\n\t"
  "v_pk_mul_f32 v[50:51], v[50:51], v[128:129] op_sel:[0,0] op_sel_hi:[1,0]\n\t"
  "v_pk_mul_f32 v[48:49], v[48:49], v[50:51]\n\t"
  "v_cvt_pk_bf16_f32 v117, v48, v49\n\t"
  "v_cvt_pk_bf16_f32 v113, v48, v49\n\t"
  "v_max_f32 v52, 0, v52\n\t"
  "v_max_f32 v53, 0, v53\n\t"
  "v_pk_mul_f32 v[54:55], v[54:55], v[128:129] op_sel:[0,0] op_sel_hi:[1,0]\n\t"
  "v_permlane16_swap_b32 v116, v117\n\t"
  "v_pk_mul_f32 v[52:53], v[52:53], v[54:55]\n\t"
  "v_cvt_pk_bf16_f32 v118, v52, v53\n\t"
  "v_cvt_pk_bf16_f32 v114, v52, v53\n\t"
  "s_mov_b32 s55, 3244032\n\t"
  "v_permlane16_swap_b32 v118, v115\n\t"
  "s_nop 1\n\t"
  "v_permlane32_swap_b32 v117, v115\n\t"
  "buffer_store_dwordx4 v[112:115], %[vh2st], %[srd], s55 offen sc1\n\t"
  "v_max_f32 v56, 0, v56\n\t"
  "v_max_f32 v57, 0, v57\n\t"
  "v_pk_mul_f32 v[58:59], v[58:59], v[128:129] op_sel:[0,1] op_sel_hi:[1,1]\n\t"
  "v_pk_mul_f32 v[56:57], v[56:57], v[58:59]\n\t"
  "v_cvt_pk_bf16_f32 v124, v56, v57\n\t"
  "v_cvt_pk_bf16_f32 v120, v56, v57\n\t"
  "v_max_f32 v60, 0, v60\n\t"
  "v_max_f32 v61, 0, v61\n\t"
  "v_pk_mul_f32 v[62:63], v[62:63], v[128:129] op_sel:[0,1] op_sel_hi:[1,1]\n\t"
  "v_pk_mul_f32 v[60:61], v[60:61], v[62:63]\n\t"
  "v_cvt_pk_bf16_f32 v125, v60, v61\n\t"
  "v_cvt_pk_bf16_f32 v121, v60, v61\n\t"
  "v_max_f32 v64, 0, v64\n\t"
  "v_max_f32 v65, 0, v65\n\t"
  "v_pk_mul_f32 v[66:67], v[66:67], v[128:129] op_sel:[0,1] op_sel_hi:[1,1]\n\t"
  "v_permlane16_swap_b32 v124, v125\n\t"
  "v_pk_mul_f32 v[64:65], v[64:65], v[66:67]\n\t"
  "v_cvt_pk_bf16_f32 v126, v64, v65\n\t"
  "v_cvt_pk_bf16_f32 v122, v64, v65\n\t"
  "s_mov_b32 s55, 3293184\n\t"
  "v_permlane16_swap_b32 v126, v123\n\t"
  "s_nop 1\n\t"
  "v_permlane32_swap_b32 v125, v123\n\t"
  "buffer_store_dwordx4 v[120:123], %[vh2st], %[srd], s55 offen sc1\n\t"
  "s_waitcnt lgkmcnt(0)\n\t"
  "s_barrier\n\t"
  "v_mfma_f32_16x16x32_bf16 v[20:23], v[144:147], a[56:59], 0\n\t"
  "v_lshrrev_b32 v63, 3, %[vl16]\n\t"
  "v_and_b32 v63, 0x60, v63\n\t"
  "v_mfma_f32_16x16x32_bf16 v[24:27], v[148:151], a[56:59], 0\n\t"
  "v_lshrrev_b32 v64, 1, v63\n\t"
  "buffer_load_dwordx4 v[68:71], v63, %[srd], %[sRT] offen offset:0 sc1\n\t"
  "v_mfma_f32_16x16x32_bf16 v[28:31], v[152:155], a[56:59], 0\n\t"
  "buffer_load_dwordx4 v[72:75], v63, %[srd], %[sRT] offen offset:16 sc1\n\t"
  "buffer_load_dwordx4 v[76:79], v63, %[srd], %[sRT] offen offset:128 sc1\n\t"
  "v_mfma_f32_16x16x32_bf16 v[32:35], v[156:159], a[56:59], 0\n\t"
  "buffer_load_dwordx4 v[80:83], v63, %[srd], %[sRT] offen offset:144 sc1\n\t"
  "buffer_load_dwordx4 v[84:87], v64, %[srd], %[sRT] offen offset:256 sc1\n\t"
  "v_mfma_f32_16x16x32_bf16 v[36:39], v[160:163], a[56:59], 0\n\t"
  "s_mul_i32 s48, %[w], -15360\n\t"
  "s_add_u32 s48, s48, 81920\n\t"
  "v_mfma_f32_16x16x32_bf16 v[20:23], v[164:167], a[60:63], v[20:23]\n\t"
  "v_add_u32 v62, s48, %[vwr]\n\t"
  "v_add_u32 v60, 61440, %[vrd0]\n\t"
  "v_mfma_f32_16x16x32_bf16 v[24:27], v[168:171], a[60:63], v[24:27]\n\t"
  "v_add_u32 v61, 61440, %[vrd1]\n\t"
  "v_accvgpr_read_b32 v89, a246\n\t"
  "v_mfma_f32_16x16x32_bf16 v[28:31], v[172:175], a[60:63], v[28:31]\n\t"
  "v_lshlrev_b32 v88, 16, v89\n\t"
  "v_and_b32 v89, 0xffff0000, v89\n\t"
  "v_mfma_f32_16x16x32_bf16 v[32:35], v[176:179], a[60:63], v[32:35]\n\t"
  "s_waitcnt vmcnt(11)\n\t"
  "v_cmp_ne_u64 vcc, v[132:133], v[138:139]\n\t"
  "v_mfma_f32_16x16x32_bf16 v[36:39], v[180:183], a[60:63], v[36:39]\n\t"
  "v_accvgpr_read_b32 v91, a247\n\t"
  "v_lshlrev_b32 v90, 16, v91\n\t"
  "v_mfma_f32_16x16x32_bf16 v[20:23], v[184:187], a[64:67], v[20:23]\n\t"
  "s_cbranch_vccnz Lfl1_slow%=\n\t"
  "Lfl1_back%=:\n\t"
  "v_mfma_f32_16x16x32_bf16 v[24:27], v[188:191], a[64:67], v[24:27]\n\t"
  "v_and_b32 v91, 0xffff0000, v91\n\t"
  "v_accvgpr_read_b32 v93, a248\n\t"
  "v_mfma_f32_16x16x32_bf16 v[28:31], v[192:195], a[64:67], v[28:31]\n\t"
  "s_add_u32 s45, %[sH2ld], 98304\n\t"
  "v_lshlrev_b32 v92, 16, v93\n\t"
  "v_mfma_f32_16x16x32_bf16 v[32:35], v[196:199], a[64:67], v[32:35]\n\t"
  "v_and_b32 v93, 0xffff0000, v93\n\t"
  "v_accvgpr_read_b32 v95, a249\n\t"
  "v_mfma_f32_16x16x32_bf16 v[36:39], v[200:203], a[64:67], v[36:39]\n\t"
  "v_lshlrev_b32 v94, 16, v95\n\t"
  "v_and_b32 v95, 0xffff0000, v95\n\t"
  "v_mfma_f32_16x16x32_bf16 v[20:23], v[204:207], a[68:71], v[20:23]\n\t"
  "buffer_load_dwordx4 a[104:107], %[vl16], %[srd], s45 offen offset:0 sc1\n\t"
  "v_accvgpr_read_b32 v97, a250\n\t"
  "v_mfma_f32_16x16x32_bf16 v[24:27], v[208:211], a[68:71], v[24:27]\n\t"
  "v_lshlrev_b32 v96, 16, v97\n\t"
  "v_and_b32 v97, 0xffff0000, v97\n\t"
  "v_mfma_f32_16x16x32_bf16 v[28:31], v[212:215], a[68:71], v[28:31]\n\t"
  "v_accvgpr_read_b32 v99, a251\n\t"
  "v_lshlrev_b32 v98, 16, v99\n\t"
  "v_mfma_f32_16x16x32_bf16 v[32:35], v[216:219], a[68:71], v[32:35]\n\t"
  "buffer_load_dwordx4 a[108:111], %[vl16], %[srd], s45 offen offset:1024 sc1\n\t"
  "v_and_b32 v99, 0xffff0000, v99\n\t"
  "v_mfma_f32_16x16x32_bf16 v[36:39], v[220:223], a[68:71], v[36:39]\n\t"
  "v_accvgpr_read_b32 v101, a252\n\t"
  "v_lshlrev_b32 v100, 16, v101\n\t"
  "v_mfma_f32_16x16x32_bf16 v[20:23], v[224:227], a[72:75], v[20:23]\n\t"
  "v_and_b32 v101, 0xffff0000, v101\n\t"
  "v_accvgpr_read_b32 v103, a253\n\t"
  "v_mfma_f32_16x16x32_bf16 v[24:27], v[228:231], a[72:75], v[24:27]\n\t"
  "buffer_load_dwordx4 a[112:115], %[vl16], %[srd], s45 offen offset:2048 sc1\n\t"
  "v_lshlrev_b32 v102, 16, v103\n\t"
  "v_mfma_f32_16x16x32_bf16 v[28:31], v[232:235], a[72:75], v[28:31]\n\t"
  "v_and_b32 v103, 0xffff0000, v103\n\t"
  "v_accvgpr_read_b32 v105, a254\n\t"
  "v_mfma_f32_16x16x32_bf16 v[32:35], v[236:239], a[72:75], v[32:35]\n\t"
  "v_lshlrev_b32 v104, 16, v105\n\t"
  "v_and_b32 v105, 0xffff0000, v105\n\t"
  "v_mfma_f32_16x16x32_bf16 v[36:39], v[240:243], a[72:75], v[36:39]\n\t"
  "buffer_load_dwordx4 a[116:119], %[vl16], %[srd], s45 offen offset:3072 sc1\n\t"
  "s_add_u32 s57, %[sFL], 16384\n\t"
  "v_mfma_f32_16x16x32_bf16 v[20:23], v[244:247], a[76:79], v[20:23]\n\t"
  "buffer_load_dwordx2 v[138:139], v137, %[srd], s57 offen sc1\n\t"
  "v_accvgpr_read_b32 v107, a255\n\t"
  "v_mfma_f32_16x16x32_bf16 v[24:27], a[160:163], a[76:79], v[24:27]\n\t"
  "s_movk_i32 s56, 24576\n\t"
  "s_waitcnt vmcnt(10)\n\t"
  "v_mfma_f32_16x16x32_bf16 v[28:31], a[164:167], a[76:79], v[28:31]\n\t"
  "buffer_store_dwordx4 v[132:135], %[vflw], %[srd], s56 offen sc1\n\t"
  "v_lshlrev_b32 v106, 16, v107\n\t"
  "v_mfma_f32_16x16x32_bf16 v[32:35], a[168:171], a[76:79], v[32:35]\n\t"
  "s_add_u32 s45, %[sH2ld], 102400\n\t"
  "v_and_b32 v107, 0xffff0000, v107\n\t"
  "v_mfma_f32_16x16x32_bf16 v[36:39], a[172:175], a[76:79], v[36:39]\n\t"
  "v_mfma_f32_16x16x32_bf16 v[20:23], a[176:179], a[80:83], v[20:23]\n\t"
  "v_mfma_f32_16x16x32_bf16 v[24:27], a[180:183], a[80:83], v[24:27]\n\t"
  "buffer_load_dwordx4 a[120:123], %[vl16], %[srd], s45 offen offset:0 sc1\n\t"
  "v_mfma_f32_16x16x32_bf16 v[28:31], a[184:187], a[80:83], v[28:31]\n\t"
  "v_mfma_f32_16x16x32_bf16 v[32:35], a[188:191], a[80:83], v[32:35]\n\t"
  "v_mfma_f32_16x16x32_bf16 v[36:39], a[192:195], a[80:83], v[36:39]\n\t"
  "buffer_load_dwordx4 a[124:127], %[vl16], %[srd], s45 offen offset:1024 sc1\n\t"
  "v_mfma_f32_16x16x32_bf16 v[20:23], a[196:199], a[84:87], v[20:23]\n\t"
  "v_mfma_f32_16x16x32_bf16 v[24:27], a[200:203], a[84:87], v[24:27]\n\t"
  "v_mfma_f32_16x16x32_bf16 v[28:31], a[204:207], a[84:87], v[28:31]\n\t"
  "buffer_load_dwordx4 a[128:131], %[vl16], %[srd], s45 offen offset:2048 sc1\n\t"
  "v_mfma_f32_16x16x32_bf16 v[32:35], a[208:211], a[84:87], v[32:35]\n\t"
  "v_mfma_f32_16x16x32_bf16 v[36:39], a[212:215], a[84:87], v[36:39]\n\t"
  "v_mfma_f32_16x16x32_bf16 v[20:23], a[216:219], a[88:91], v[20:23]\n\t"
  "buffer_load_dwordx4 a[132:135], %[vl16], %[srd], s45 offen offset:3072 sc1\n\t"
  "v_mfma_f32_16x16x32_bf16 v[24:27], a[220:223], a[88:91], v[24:27]\n\t"
  "v_mfma_f32_16x16x32_bf16 v[28:31], a[224:227], a[88:91], v[28:31]\n\t"
  "v_mfma_f32_16x16x32_bf16 v[32:35], a[228:231], a[88:91], v[32:35]\n\t"
  "v_mfma_f32_16x16x32_bf16 v[36:39], a[232:235], a[88:91], v[36:39]\n\t"
  "s_add_u32 s45, %[sH2ld], 106496\n\t"
  "s_waitcnt vmcnt(14)\n\t"
  "v_mul_f32 v88, v88, v68\n\t"
  "v_mfma_f32_16x16x32_bf16 v[20:23], a[236:239], a[92:95], v[20:23]\n\t"
  "v_mul_f32 v89, v89, v69\n\t"
  "v_mul_f32 v90, v90, v70\n\t"
  "v_mfma_f32_16x16x32_bf16 v[24:27], a[0:3], a[92:95], v[24:27]\n\t"
  "v_mul_f32 v91, v91, v71\n\t"
  "ds_write_b128 v62, v[88:91] offset:0\n\t"
  "v_mfma_f32_16x16x32_bf16 v[28:31], a[4:7], a[92:95], v[28:31]\n\t"
  "buffer_load_dwordx4 a[136:139], %[vl16], %[srd], s45 offen offset:0 sc1\n\t"
  "s_waitcnt vmcnt(14)\n\t"
  "v_mul_f32 v92, v92, v72\n\t"
  "v_mfma_f32_16x16x32_bf16 v[32:35], a[8:11], a[92:95], v[32:35]\n\t"
  "v_mul_f32 v93, v93, v73\n\t"
  "v_mul_f32 v94, v94, v74\n\t"
  "v_mfma_f32_16x16x32_bf16 v[36:39], a[12:15], a[92:95], v[36:39]\n\t"
  "v_mul_f32 v95, v95, v75\n\t"
  "ds_write_b128 v62, v[92:95] offset:1024\n\t"
  "v_mfma_f32_16x16x32_bf16 v[20:23], a[16:19], a[96:99], v[20:23]\n\t"
  "buffer_load_dwordx4 a[140:143], %[vl16], %[srd], s45 offen offset:1024 sc1\n\t"
  "s_waitcnt vmcnt(14)\n\t"
  "v_mul_f32 v96, v96, v76\n\t"
  "v_mfma_f32_16x16x32_bf16 v[24:27], a[20:23], a[96:99], v[24:27]\n\t"
  "v_mul_f32 v97, v97, v77\n\t"
  "v_mul_f32 v98, v98, v78\n\t"
  "v_mfma_f32_16x16x32_bf16 v[28:31], a[24:27], a[96:99], v[28:31]\n\t"
  "v_mul_f32 v99, v99, v79\n\t"
  "ds_write_b128 v62, v[96:99] offset:2048\n\t"
  "v_mfma_f32_16x16x32_bf16 v[32:35], a[28:31], a[96:99], v[32:35]\n\t"
  "buffer_load_dwordx4 a[144:147], %[vl16], %[srd], s45 offen offset:2048 sc1\n\t"
  "s_waitcnt vmcnt(14)\n\t"
  "v_mul_f32 v100, v100, v80\n\t"
  "v_mfma_f32_16x16x32_bf16 v[36:39], a[32:35], a[96:99], v[36:39]\n\t"
  "v_mul_f32 v101, v101, v81\n\t"
  "v_mul_f32 v102, v102, v82\n\t"
  "v_mfma_f32_16x16x32_bf16 v[20:23], a[36:39], a[100:103], v[20:23]\n\t"
  "v_mul_f32 v103, v103, v83\n\t"
  "ds_write_b128 v62, v[100:103] offset:3072\n\t"
  "v_mfma_f32_16x16x32_bf16 v[24:27], a[40:43], a[100:103], v[24:27]\n\t"
  "buffer_load_dwordx4 a[148:151], %[vl16], %[srd], s45 offen offset:3072 sc1\n\t"
  "s_waitcnt vmcnt(14)\n\t"
  "v_mul_f32 v104, v104, v84\n\t"
  "v_mfma_f32_16x16x32_bf16 v[28:31], a[44:47], a[100:103], v[28:31]\n\t"
  "v_mul_f32 v105, v105, v85\n\t"
  "v_mul_f32 v106, v106, v86\n\t"
  "v_mfma_f32_16x16x32_bf16 v[32:35], a[48:51], a[100:103], v[32:35]\n\t"
  "v_mul_f32 v107, v107, v87\n\t"
  "ds_write_b128 v62, v[104:107] offset:4096\n\t"
  "v_mfma_f32_16x16x32_bf16 v[36:39], a[52:55], a[100:103], v[36:39]\n\t"
  "s_waitcnt vmcnt(12)\n\t"
  "v_mfma_f32_16x16x32_bf16 v[40:43], v[144:147], a[104:107], 0\n\t"
  "v_mfma_f32_16x16x32_bf16 v[44:47], v[148:151], a[104:107], 0\n\t"
  "v_mfma_f32_16x16x32_bf16 v[48:51], v[152:155], a[104:107], 0\n\t"
  "v_mfma_f32_16x16x32_bf16 v[52:55], v[156:159], a[104:107], 0\n\t"
  "ds_write_b128 %[vwr], v[20:23] offset:0\n\t"
  "ds_write_b128 %[vwr], v[24:27] offset:1024\n\t"
  "v_mfma_f32_16x16x32_bf16 v[56:59], v[160:163], a[104:107], 0\n\t"
  "ds_write_b128 %[vwr], v[28:31] offset:2048\n\t"
  "ds_write_b128 %[vwr], v[32:35] offset:3072\n\t"
  "v_mfma_f32_16x16x32_bf16 v[40:43], v[164:167], a[108:111], v[40:43]\n\t"
  "s_waitcnt vmcnt(9)\n\t"
  "v_cmp_ne_u64 vcc, v[132:133], v[138:139]\n\t"
  "v_mfma_f32_16x16x32_bf16 v[44:47], v[168:171], a[108:111], v[44:47]\n\t"
  "ds_write_b128 %[vwr], v[36:39] offset:4096\n\t"
  "v_mfma_f32_16x16x32_bf16 v[48:51], v[172:175], a[108:111], v[48:51]\n\t"
  "s_cbranch_vccnz Lfl2_slow%=\n\t"
  "Lfl2_back%=:\n\t"
  "v_mfma_f32_16x16x32_bf16 v[52:55], v[176:179], a[108:111], v[52:55]\n\t"
  "s_add_u32 s44, %[sH2ld], 3145728\n\t"
  "v_mfma_f32_16x16x32_bf16 v[56:59], v[180:183], a[108:111], v[56:59]\n\t"
  "v_mfma_f32_16x16x32_bf16 v[40:43], v[184:187], a[112:115], v[40:43]\n\t"
  "v_mfma_f32_16x16x32_bf16 v[44:47], v[188:191], a[112:115], v[44:47]\n\t"
  "buffer_load_dwordx4 a[152:155], %[vl16], %[srd], s44 offen offset:0 sc1\n\t"
  "v_mfma_f32_16x16x32_bf16 v[48:51], v[192:195], a[112:115], v[48:51]\n\t"
  "v_mfma_f32_16x16x32_bf16 v[52:55], v[196:199], a[112:115], v[52:55]\n\t"
  "v_mfma_f32_16x16x32_bf16 v[56:59], v[200:203], a[112:115], v[56:59]\n\t"
  "buffer_load_dwordx4 a[156:159], %[vl16], %[srd], s44 offen offset:1024 sc1\n\t"
  "v_mfma_f32_16x16x32_bf16 v[40:43], v[204:207], a[116:119], v[40:43]\n\t"
  "v_mfma_f32_16x16x32_bf16 v[44:47], v[208:211], a[116:119], v[44:47]\n\t"
  "v_mfma_f32_16x16x32_bf16 v[48:51], v[212:215], a[116:119], v[48:51]\n\t"
  "buffer_load_dwordx4 v[248:251], %[vl16], %[srd], s44 offen offset:2048 sc1\n\t"
  "v_mfma_f32_16x16x32_bf16 v[52:55], v[216:219], a[116:119], v[52:55]\n\t"
  "v_mfma_f32_16x16x32_bf16 v[56:59], v[220:223], a[116:119], v[56:59]\n\t"
  "s_waitcnt vmcnt(9)\n\t"
  "v_mfma_f32_16x16x32_bf16 v[40:43], v[224:227], a[120:123], v[40:43]\n\t"
  "buffer_load_dwordx4 v[252:255], %[vl16], %[srd], s44 offen offset:3072 sc1\n\t"
  "v_mfma_f32_16x16x32_bf16 v[44:47], v[228:231], a[120:123], v[44:47]\n\t"
  "v_mfma_f32_16x16x32_bf16 v[48:51], v[232:235], a[120:123], v[48:51]\n\t"
  "s_waitcnt lgkmcnt(0)\n\t"
  "s_barrier\n\t"
  "v_mfma_f32_16x16x32_bf16 v[52:55], v[236:239], a[120:123], v[52:55]\n\t"
  "ds_read_b128 v[100:103], v60 offset:20480\n\t"
  "ds_read_b128 v[104:107], v61 offset:20480\n\t"
  "v_mfma_f32_16x16x32_bf16 v[56:59], v[240:243], a[120:123], v[56:59]\n\t"
  "s_add_u32 s44, %[sH2ld], 3149824\n\t"
  "s_add_u32 s57, %[sFL], 24576\n\t"
  "v_mfma_f32_16x16x32_bf16 v[40:43], v[244:247], a[124:127], v[40:43]\n\t"
  "buffer_load_dwordx2 v[138:139], v137, %[srd], s57 offen sc1\n\t"
  "ds_read_b128 v[68:71], %[vrd0] offset:0\n\t"
  "v_mfma_f32_16x16x32_bf16 v[44:47], a[160:163], a[124:127], v[44:47]\n\t"
  "ds_read_b128 v[72:75], %[vrd1] offset:0\n\t"
  "ds_read_b128 v[76:79], %[vrd0] offset:20480\n\t"
  "v_mfma_f32_16x16x32_bf16 v[48:51], a[164:167], a[124:127], v[48:51]\n\t"
  "buffer_load_dwordx4 v[112:115], %[vl16], %[srd], s44 offen offset:0 sc1\n\t"
  "ds_read_b128 v[80:83], %[vrd1] offset:20480\n\t"
  "v_mfma_f32_16x16x32_bf16 v[52:55], a[168:171], a[124:127], v[52:55]\n\t"
  "ds_read_b128 v[84:87], %[vrd0] offset:40960\n\t"
  "ds_read_b128 v[88:91], %[vrd1] offset:40960\n\t"
  "v_mfma_f32_16x16x32_bf16 v[56:59], a[172:175], a[124:127], v[56:59]\n\t"
  "s_waitcnt vmcnt(10)\n\t"
  "ds_read_b128 v[92:95], v60 offset:0\n\t"
  "v_mfma_f32_16x16x32_bf16 v[40:43], a[176:179], a[128:131], v[40:43]\n\t"
  "buffer_load_dwordx4 v[116:119], %[vl16], %[srd], s44 offen offset:1024 sc1\n\t"
  "ds_read_b128 v[96:99], v61 offset:0\n\t"
  "v_mfma_f32_16x16x32_bf16 v[44:47], a[180:183], a[128:131], v[44:47]\n\t"
  "v_mfma_f32_16x16x32_bf16 v[48:51], a[184:187], a[128:131], v[48:51]\n\t"
  "v_mfma_f32_16x16x32_bf16 v[52:55], a[188:191], a[128:131], v[52:55]\n\t"
  "buffer_load_dwordx4 v[120:123], %[vl16], %[srd], s44 offen offset:2048 sc1\n\t"
  "v_mfma_f32_16x16x32_bf16 v[56:59], a[192:195], a[128:131], v[56:59]\n\t"
  "v_mfma_f32_16x16x32_bf16 v[40:43], a[196:199], a[132:135], v[40:43]\n\t"
  "v_mfma_f32_16x16x32_bf16 v[44:47], a[200:203], a[132:135], v[44:47]\n\t"
  "buffer_load_dwordx4 v[124:127], %[vl16], %[srd], s44 offen offset:3072 sc1\n\t"
  "v_mfma_f32_16x16x32_bf16 v[48:51], a[204:207], a[132:135], v[48:51]\n\t"
  "v_mfma_f32_16x16x32_bf16 v[52:55], a[208:211], a[132:135], v[52:55]\n\t"
  "s_waitcnt lgkmcnt(0)\n\t"
  "v_add_f32 v68, v68, v100\n\t"
  "v_mfma_f32_16x16x32_bf16 v[56:59], a[212:215], a[132:135], v[56:59]\n\t"
  "s_waitcnt vmcnt(11)\n\t"
  "v_add_f32 v69, v69, v101\n\t"
  "v_mfma_f32_16x16x32_bf16 v[40:43], a[216:219], a[136:139], v[40:43]\n\t"
  "s_add_u32 s44, %[sH2ld], 3153920\n\t"
  "v_add_f32 v70, v70, v102\n\t"
  "v_mfma_f32_16x16x32_bf16 v[44:47], a[220:223], a[136:139], v[44:47]\n\t"
  "v_add_f32 v71, v71, v103\n\t"
  "v_add_f32 v72, v72, v104\n\t"
  "v_mfma_f32_16x16x32_bf16 v[48:51], a[224:227], a[136:139], v[48:51]\n\t"
  "v_add_f32 v73, v73, v105\n\t"
  "v_add_f32 v74, v74, v106\n\t"
  "v_mfma_f32_16x16x32_bf16 v[52:55], a[228:231], a[136:139], v[52:55]\n\t"
  "buffer_load_dwordx4 v[128:131], %[vl16], %[srd], s44 offen offset:0 sc1\n\t"
  "v_add_f32 v75, v75, v107\n\t"
  "v_mfma_f32_16x16x32_bf16 v[56:59], a[232:235], a[136:139], v[56:59]\n\t"
  "v_add_f32 v76, v76, v84\n\t"
  "v_add_f32 v77, v77, v85\n\t"
  "v_mfma_f32_16x16x32_bf16 v[40:43], a[236:239], a[140:143], v[40:43]\n\t"
  "v_add_f32 v78, v78, v86\n\t"
  "v_add_f32 v79, v79, v87\n\t"
  "v_mfma_f32_16x16x32_bf16 v[44:47], a[0:3], a[140:143], v[44:47]\n\t"
  "buffer_load_dwordx4 a[240:243], %[vl16], %[srd], s44 offen offset:1024 sc1\n\t"
  "v_add_f32 v80, v80, v88\n\t"
  "v_mfma_f32_16x16x32_bf16 v[48:51], a[4:7], a[140:143], v[48:51]\n\t"
  "v_add_f32 v81, v81, v89\n\t"
  "v_add_f32 v82, v82, v90\n\t"
  "v_mfma_f32_16x16x32_bf16 v[52:55], a[8:11], a[140:143], v[52:55]\n\t"
  "v_add_f32 v83, v83, v91\n\t"
  "v_add_f32 v68, v68, v92\n\t"
  "v_mfma_f32_16x16x32_bf16 v[56:59], a[12:15], a[140:143], v[56:59]\n\t"
  "s_waitcnt vmcnt(11)\n\t"
  "buffer_load_dwordx4 a[56:59], %[vl16], %[srd], s44 offen offset:2048 sc1\n\t"
  "v_mfma_f32_16x16x32_bf16 v[40:43], a[16:19], a[144:147], v[40:43]\n\t"
  "v_add_f32 v69, v69, v93\n\t"
  "v_add_f32 v70, v70, v94\n\t"
  "v_mfma_f32_16x16x32_bf16 v[44:47], a[20:23], a[144:147], v[44:47]\n\t"
  "v_add_f32 v71, v71, v95\n\t"
  "v_add_f32 v72, v72, v96\n\t"
  "v_mfma_f32_16x16x32_bf16 v[48:51], a[24:27], a[144:147], v[48:51]\n\t"
  "buffer_load_dwordx4 a[60:63], %[vl16], %[srd], s44 offen offset:3072 sc1\n\t"
  "v_add_f32 v73, v73, v97\n\t"
  "v_mfma_f32_16x16x32_bf16 v[52:55], a[28:31], a[144:147], v[52:55]\n\t"
  "v_add_f32 v74, v74, v98\n\t"
  "v_add_f32 v75, v75, v99\n\t"
  "v_mfma_f32_16x16x32_bf16 v[56:59], a[32:35], a[144:147], v[56:59]\n\t"
  "v_add_f32 v68, v68, v76\n\t"
  "v_add_f32 v69, v69, v77\n\t"
  "v_mfma_f32_16x16x32_bf16 v[40:43], a[36:39], a[148:151], v[40:43]\n\t"
  "v_add_f32 v70, v70, v78\n\t"
  "v_add_f32 v71, v71, v79\n\t"
  "v_mfma_f32_16x16x32_bf16 v[44:47], a[40:43], a[148:151], v[44:47]\n\t"
  "v_add_f32 v72, v72, v80\n\t"
  "v_add_f32 v73, v73, v81\n\t"
  "v_mfma_f32_16x16x32_bf16 v[48:51], a[44:47], a[148:151], v[48:51]\n\t"
  "v_add_f32 v74, v74, v82\n\t"
  "v_add_f32 v75, v75, v83\n\t"
  "v_mfma_f32_16x16x32_bf16 v[52:55], a[48:51], a[148:151], v[52:55]\n\t"
  "v_cvt_pk_bf16_f32 v76, v68, v69\n\t"
  "v_cvt_pk_bf16_f32 v77, v70, v71\n\t"
  "v_mfma_f32_16x16x32_bf16 v[56:59], a[52:55], a[148:151], v[56:59]\n\t"
  "s_waitcnt vmcnt(11)\n\t"
  "v_cvt_pk_bf16_f32 v78, v72, v73\n\t"
  "v_mfma_f32_16x16x32_bf16 v[20:23], v[144:147], a[152:155], 0\n\t"
  "v_cvt_pk_bf16_f32 v79, v74, v75\n\t"
  "s_mov_b32 s55, 0\n\t"
  "v_mfma_f32_16x16x32_bf16 v[24:27], v[148:151], a[152:155], 0\n\t"
  "buffer_store_dwordx4 v[76:79], %[vout], %[srdo], s55 offen sc1\n\t"
  "v_mfma_f32_16x16x32_bf16 v[28:31], v[152:155], a[152:155], 0\n\t"
  "v_mfma_f32_16x16x32_bf16 v[32:35], v[156:159], a[152:155], 0\n\t"
  "ds_write_b128 %[vwr], v[40:43] offset:5120\n\t"
  "ds_write_b128 %[vwr], v[44:47] offset:6144\n\t"
  "v_mfma_f32_16x16x32_bf16 v[36:39], v[160:163], a[152:155], 0\n\t"
  "ds_write_b128 %[vwr], v[48:51] offset:7168\n\t"
  "ds_write_b128 %[vwr], v[52:55] offset:8192\n\t"
  "v_mfma_f32_16x16x32_bf16 v[20:23], v[164:167], a[156:159], v[20:23]\n\t"
  "s_waitcnt vmcnt(9)\n\t"
  "v_cmp_ne_u64 vcc, v[132:133], v[138:139]\n\t"
  "v_mfma_f32_16x16x32_bf16 v[24:27], v[168:171], a[156:159], v[24:27]\n\t"
  "ds_write_b128 %[vwr], v[56:59] offset:9216\n\t"
  "v_mfma_f32_16x16x32_bf16 v[28:31], v[172:175], a[156:159], v[28:31]\n\t"
  "s_cbranch_vccnz Lfl3_slow%=\n\t"
  "Lfl3_back%=:\n\t"
  "v_mfma_f32_16x16x32_bf16 v[32:35], v[176:179], a[156:159], v[32:35]\n\t"
  "s_add_u32 s45, %[sH2ld], 3244032\n\t"
  "v_mfma_f32_16x16x32_bf16 v[36:39], v[180:183], a[156:159], v[36:39]\n\t"
  "v_mfma_f32_16x16x32_bf16 v[20:23], v[184:187], v[248:251], v[20:23]\n\t"
  "buffer_load_dwordx4 a[104:107], %[vl16], %[srd], s45 offen offset:0 sc1\n\t"
  "v_mfma_f32_16x16x32_bf16 v[24:27], v[188:191], v[248:251], v[24:27]\n\t"
  "v_mfma_f32_16x16x32_bf16 v[28:31], v[192:195], v[248:251], v[28:31]\n\t"
  "buffer_load_dwordx4 a[108:111], %[vl16], %[srd], s45 offen offset:1024 sc1\n\t"
  "v_mfma_f32_16x16x32_bf16 v[32:35], v[196:199], v[248:251], v[32:35]\n\t"
  "v_mfma_f32_16x16x32_bf16 v[36:39], v[200:203], v[248:251], v[36:39]\n\t"
  "v_mfma_f32_16x16x32_bf16 v[20:23], v[204:207], v[252:255], v[20:23]\n\t"
  "buffer_load_dwordx4 a[64:67], %[vl16], %[srd], s45 offen offset:2048 sc1\n\t"
  "v_mfma_f32_16x16x32_bf16 v[24:27], v[208:211], v[252:255], v[24:27]\n\t"
  "v_mfma_f32_16x16x32_bf16 v[28:31], v[212:215], v[252:255], v[28:31]\n\t"
  "buffer_load_dwordx4 a[68:71], %[vl16], %[srd], s45 offen offset:3072 sc1\n\t"
  "v_mfma_f32_16x16x32_bf16 v[32:35], v[216:219], v[252:255], v[32:35]\n\t"
  "v_mfma_f32_16x16x32_bf16 v[36:39], v[220:223], v[252:255], v[36:39]\n\t"
  "s_waitcnt vmcnt(11)\n\t"
  "v_mfma_f32_16x16x32_bf16 v[20:23], v[224:227], v[112:115], v[20:23]\n\t"
  "s_add_u32 s45, %[sH2ld], 3248128\n\t"
  "v_mfma_f32_16x16x32_bf16 v[24:27], v[228:231], v[112:115], v[24:27]\n\t"
  "v_mfma_f32_16x16x32_bf16 v[28:31], v[232:235], v[112:115], v[28:31]\n\t"
  "buffer_load_dwordx4 a[72:75], %[vl16], %[srd], s45 offen offset:0 sc1\n\t"
  "s_waitcnt lgkmcnt(0)\n\t"
  "s_barrier\n\t"
  "v_mfma_f32_16x16x32_bf16 v[32:35], v[236:239], v[112:115], v[32:35]\n\t"
  "ds_read_b128 v[100:103], v60 offset:25600\n\t"
  "ds_read_b128 v[104:107], v61 offset:25600\n\t"
  "v_mfma_f32_16x16x32_bf16 v[36:39], v[240:243], v[112:115], v[36:39]\n\t"
  "buffer_load_dwordx4 a[76:79], %[vl16], %[srd], s45 offen offset:1024 sc1\n\t"
  "ds_read_b128 v[68:71], %[vrd0] offset:5120\n\t"
  "v_mfma_f32_16x16x32_bf16 v[20:23], v[244:247], v[116:119], v[20:23]\n\t"
  "ds_read_b128 v[72:75], %[vrd1] offset:5120\n\t"
  "ds_read_b128 v[76:79], %[vrd0] offset:25600\n\t"
  "v_mfma_f32_16x16x32_bf16 v[24:27], a[160:163], v[116:119], v[24:27]\n\t"
  "ds_read_b128 v[80:83], %[vrd1] offset:25600\n\t"
  "ds_read_b128 v[84:87], %[vrd0] offset:46080\n\t"
  "v_mfma_f32_16x16x32_bf16 v[28:31], a[164:167], v[116:119], v[28:31]\n\t"
  "buffer_load_dwordx4 a[80:83], %[vl16], %[srd], s45 offen offset:2048 sc1\n\t"
  "ds_read_b128 v[88:91], %[vrd1] offset:46080\n\t"
  "v_mfma_f32_16x16x32_bf16 v[32:35], a[168:171], v[116:119], v[32:35]\n\t"
  "ds_read_b128 v[92:95], v60 offset:5120\n\t"
  "ds_read_b128 v[96:99], v61 offset:5120\n\t"
  "v_mfma_f32_16x16x32_bf16 v[36:39], a[172:175], v[116:119], v[36:39]\n\t"
  "s_waitcnt vmcnt(12)\n\t"
  "buffer_load_dwordx4 a[84:87], %[vl16], %[srd], s45 offen offset:3072 sc1\n\t"
  "v_mfma_f32_16x16x32_bf16 v[20:23], a[176:179], v[120:123], v[20:23]\n\t"
  "v_mfma_f32_16x16x32_bf16 v[24:27], a[180:183], v[120:123], v[24:27]\n\t"
  "v_mfma_f32_16x16x32_bf16 v[28:31], a[184:187], v[120:123], v[28:31]\n\t"
  "s_add_u32 s45, %[sH2ld], 3252224\n\t"
  "v_mfma_f32_16x16x32_bf16 v[32:35], a[188:191], v[120:123], v[32:35]\n\t"
  "v_mfma_f32_16x16x32_bf16 v[36:39], a[192:195], v[120:123], v[36:39]\n\t"
  "buffer_load_dwordx4 a[88:91], %[vl16], %[srd], s45 offen offset:0 sc1\n\t"
  "v_mfma_f32_16x16x32_bf16 v[20:23], a[196:199], v[124:127], v[20:23]\n\t"
  "v_mfma_f32_16x16x32_bf16 v[24:27], a[200:203], v[124:127], v[24:27]\n\t"
  "buffer_load_dwordx4 a[92:95], %[vl16], %[srd], s45 offen offset:1024 sc1\n\t"
  "s_waitcnt lgkmcnt(0)\n\t"
  "v_mfma_f32_16x16x32_bf16 v[28:31], a[204:207], v[124:127], v[28:31]\n\t"
  "v_add_f32 v68, v68, v100\n\t"
  "v_add_f32 v69, v69, v101\n\t"
  "v_mfma_f32_16x16x32_bf16 v[32:35], a[208:211], v[124:127], v[32:35]\n\t"
  "v_add_f32 v70, v70, v102\n\t"
  "v_add_f32 v71, v71, v103\n\t"
  "v_mfma_f32_16x16x32_bf16 v[36:39], a[212:215], v[124:127], v[36:39]\n\t"
  "s_waitcnt vmcnt(13)\n\t"
  "buffer_load_dwordx4 a[96:99], %[vl16], %[srd], s45 offen offset:2048 sc1\n\t"
  "v_mfma_f32_16x16x32_bf16 v[20:23], a[216:219], v[128:131], v[20:23]\n\t"
  "v_add_f32 v72, v72, v104\n\t"
  "v_add_f32 v73, v73, v105\n\t"
  "v_mfma_f32_16x16x32_bf16 v[24:27], a[220:223], v[128:131], v[24:27]\n\t"
  "buffer_load_dwordx4 a[100:103], %[vl16], %[srd], s45 offen offset:3072 sc1\n\t"
  "v_add_f32 v74, v74, v106\n\t"
  "v_mfma_f32_16x16x32_bf16 v[28:31], a[224:227], v[128:131], v[28:31]\n\t"
  "v_add_f32 v75, v75, v107\n\t"
  "v_add_f32 v76, v76, v84\n\t"
  "v_mfma_f32_16x16x32_bf16 v[32:35], a[228:231], v[128:131], v[32:35]\n\t"
  "v_add_f32 v77, v77, v85\n\t"
  "v_add_f32 v78, v78, v86\n\t"
  "v_mfma_f32_16x16x32_bf16 v[36:39], a[232:235], v[128:131], v[36:39]\n\t"
  "v_add_f32 v79, v79, v87\n\t"
  "v_add_f32 v80, v80, v88\n\t"
  "v_mfma_f32_16x16x32_bf16 v[20:23], a[236:239], a[240:243], v[20:23]\n\t"
  "v_add_f32 v81, v81, v89\n\t"
  "v_add_f32 v82, v82, v90\n\t"
  "v_mfma_f32_16x16x32_bf16 v[24:27], a[0:3], a[240:243], v[24:27]\n\t"
  "v_add_f32 v83, v83, v91\n\t"
  "v_add_f32 v68, v68, v92\n\t"
  "v_mfma_f32_16x16x32_bf16 v[28:31], a[4:7], a[240:243], v[28:31]\n\t"
  "v_add_f32 v69, v69, v93\n\t"
  "v_add_f32 v70, v70, v94\n\t"
  "v_mfma_f32_16x16x32_bf16 v[32:35], a[8:11], a[240:243], v[32:35]\n\t"
  "v_add_f32 v71, v71, v95\n\t"
  "v_add_f32 v72, v72, v96\n\t"
  "v_mfma_f32_16x16x32_bf16 v[36:39], a[12:15], a[240:243], v[36:39]\n\t"
  "s_waitcnt vmcnt(13)\n\t"
  "v_add_f32 v73, v73, v97\n\t"
  "v_mfma_f32_16x16x32_bf16 v[20:23], a[16:19], a[56:59], v[20:23]\n\t"
  "v_add_f32 v74, v74, v98\n\t"
  "v_add_f32 v75, v75, v99\n\t"
  "v_mfma_f32_16x16x32_bf16 v[24:27], a[20:23], a[56:59], v[24:27]\n\t"
  "v_add_f32 v68, v68, v76\n\t"
  "v_add_f32 v69, v69, v77\n\t"
  "v_mfma_f32_16x16x32_bf16 v[28:31], a[24:27], a[56:59], v[28:31]\n\t"
  "v_add_f32 v70, v70, v78\n\t"
  "v_add_f32 v71, v71, v79\n\t"
  "v_mfma_f32_16x16x32_bf16 v[32:35], a[28:31], a[56:59], v[32:35]\n\t"
  "v_add_f32 v72, v72, v80\n\t"
  "v_add_f32 v73, v73, v81\n\t"
  "v_mfma_f32_16x16x32_bf16 v[36:39], a[32:35], a[56:59], v[36:39]\n\t"
  "v_add_f32 v74, v74, v82\n\t"
  "v_add_f32 v75, v75, v83\n\t"
  "v_mfma_f32_16x16x32_bf16 v[20:23], a[36:39], a[60:63], v[20:23]\n\t"
  "v_cvt_pk_bf16_f32 v76, v68, v69\n\t"
  "v_cvt_pk_bf16_f32 v77, v70, v71\n\t"
  "v_mfma_f32_16x16x32_bf16 v[24:27], a[40:43], a[60:63], v[24:27]\n\t"
  "v_cvt_pk_bf16_f32 v78, v72, v73\n\t"
  "v_cvt_pk_bf16_f32 v79, v74, v75\n\t"
  "v_mfma_f32_16x16x32_bf16 v[28:31], a[44:47], a[60:63], v[28:31]\n\t"
  "s_mov_b32 s55, 40960\n\t"
  "buffer_store_dwordx4 v[76:79], %[vout], %[srdo], s55 offen sc1\n\t"
  "v_mfma_f32_16x16x32_bf16 v[32:35], a[48:51], a[60:63], v[32:35]\n\t"
  "v_mfma_f32_16x16x32_bf16 v[36:39], a[52:55], a[60:63], v[36:39]\n\t"
  "s_waitcnt vmcnt(11)\n\t"
  "v_mfma_f32_16x16x32_bf16 v[40:43], v[144:147], a[104:107], 0\n\t"
  "v_mfma_f32_16x16x32_bf16 v[44:47], v[148:151], a[104:107], 0\n\t"
  "v_mfma_f32_16x16x32_bf16 v[48:51], v[152:155], a[104:107], 0\n\t"
  "v_mfma_f32_16x16x32_bf16 v[52:55], v[156:159], a[104:107], 0\n\t"
  "ds_write_b128 %[vwr], v[20:23] offset:10240\n\t"
  "ds_write_b128 %[vwr], v[24:27] offset:11264\n\t"
  "v_mfma_f32_16x16x32_bf16 v[56:59], v[160:163], a[104:107], 0\n\t"
  "ds_write_b128 %[vwr], v[28:31] offset:12288\n\t"
  "ds_write_b128 %[vwr], v[32:35] offset:13312\n\t"
  "v_mfma_f32_16x16x32_bf16 v[40:43], v[164:167], a[108:111], v[40:43]\n\t"
  "ds_write_b128 %[vwr], v[36:39] offset:14336\n\t"
  "v_mfma_f32_16x16x32_bf16 v[44:47], v[168:171], a[108:111], v[44:47]\n\t"
  "v_mfma_f32_16x16x32_bf16 v[48:51], v[172:175], a[108:111], v[48:51]\n\t"
  "v_mfma_f32_16x16x32_bf16 v[52:55], v[176:179], a[108:111], v[52:55]\n\t"
  "v_mfma_f32_16x16x32_bf16 v[56:59], v[180:183], a[108:111], v[56:59]\n\t"
  "s_waitcnt vmcnt(9)\n\t"
  "v_mfma_f32_16x16x32_bf16 v[40:43], v[184:187], a[64:67], v[40:43]\n\t"
  "v_mfma_f32_16x16x32_bf16 v[44:47], v[188:191], a[64:67], v[44:47]\n\t"
  "v_mfma_f32_16x16x32_bf16 v[48:51], v[192:195], a[64:67], v[48:51]\n\t"
  "v_mfma_f32_16x16x32_bf16 v[52:55], v[196:199], a[64:67], v[52:55]\n\t"
  "v_mfma_f32_16x16x32_bf16 v[56:59], v[200:203], a[64:67], v[56:59]\n\t"
  "v_mfma_f32_16x16x32_bf16 v[40:43], v[204:207], a[68:71], v[40:43]\n\t"
  "v_mfma_f32_16x16x32_bf16 v[44:47], v[208:211], a[68:71], v[44:47]\n\t"
  "v_mfma_f32_16x16x32_bf16 v[48:51], v[212:215], a[68:71], v[48:51]\n\t"
  "v_mfma_f32_16x16x32_bf16 v[52:55], v[216:219], a[68:71], v[52:55]\n\t"
  "v_mfma_f32_16x16x32_bf16 v[56:59], v[220:223], a[68:71], v[56:59]\n\t"
  "s_waitcnt vmcnt(7)\n\t"
  "v_mfma_f32_16x16x32_bf16 v[40:43], v[224:227], a[72:75], v[40:43]\n\t"
  "v_mfma_f32_16x16x32_bf16 v[44:47], v[228:231], a[72:75], v[44:47]\n\t"
  "s_waitcnt lgkmcnt(0)\n\t"
  "s_barrier\n\t"
  "v_mfma_f32_16x16x32_bf16 v[48:51], v[232:235], a[72:75], v[48:51]\n\t"
  "ds_read_b128 v[100:103], v60 offset:30720\n\t"
  "ds_read_b128 v[104:107], v61 offset:30720\n\t"
  "v_mfma_f32_16x16x32_bf16 v[52:55], v[236:239], a[72:75], v[52:55]\n\t"
  "ds_read_b128 v[68:71], %[vrd0] offset:10240\n\t"
  "ds_read_b128 v[72:75], %[vrd1] offset:10240\n\t"
  "v_mfma_f32_16x16x32_bf16 v[56:59], v[240:243], a[72:75], v[56:59]\n\t"
  "ds_read_b128 v[76:79], %[vrd0] offset:30720\n\t"
  "ds_read_b128 v[80:83], %[vrd1] offset:30720\n\t"
  "v_mfma_f32_16x16x32_bf16 v[40:43], v[244:247], a[76:79], v[40:43]\n\t"
  "ds_read_b128 v[84:87], %[vrd0] offset:51200\n\t"
  "ds_read_b128 v[88:91], %[vrd1] offset:51200\n\t"
  "v_mfma_f32_16x16x32_bf16 v[44:47], a[160:163], a[76:79], v[44:47]\n\t"
  "ds_read_b128 v[92:95], v60 offset:10240\n\t"
  "ds_read_b128 v[96:99], v61 offset:10240\n\t"
  "v_mfma_f32_16x16x32_bf16 v[48:51], a[164:167], a[76:79], v[48:51]\n\t"
  "v_mfma_f32_16x16x32_bf16 v[52:55], a[168:171], a[76:79], v[52:55]\n\t"
  "v_mfma_f32_16x16x32_bf16 v[56:59], a[172:175], a[76:79], v[56:59]\n\t"
  "s_waitcnt vmcnt(5)\n\t"
  "v_mfma_f32_16x16x32_bf16 v[40:43], a[176:179], a[80:83], v[40:43]\n\t"
  "v_mfma_f32_16x16x32_bf16 v[44:47], a[180:183], a[80:83], v[44:47]\n\t"
  "v_mfma_f32_16x16x32_bf16 v[48:51], a[184:187], a[80:83], v[48:51]\n\t"
  "v_mfma_f32_16x16x32_bf16 v[52:55], a[188:191], a[80:83], v[52:55]\n\t"
  "v_mfma_f32_16x16x32_bf16 v[56:59], a[192:195], a[80:83], v[56:59]\n\t"
  "s_waitcnt lgkmcnt(0)\n\t"
  "v_add_f32 v68, v68, v100\n\t"
  "v_mfma_f32_16x16x32_bf16 v[40:43], a[196:199], a[84:87], v[40:43]\n\t"
  "v_add_f32 v69, v69, v101\n\t"
  "v_add_f32 v70, v70, v102\n\t"
  "v_mfma_f32_16x16x32_bf16 v[44:47], a[200:203], a[84:87], v[44:47]\n\t"
  "v_add_f32 v71, v71, v103\n\t"
  "v_add_f32 v72, v72, v104\n\t"
  "v_mfma_f32_16x16x32_bf16 v[48:51], a[204:207], a[84:87], v[48:51]\n\t"
  "v_add_f32 v73, v73, v105\n\t"
  "v_add_f32 v74, v74, v106\n\t"
  "v_mfma_f32_16x16x32_bf16 v[52:55], a[208:211], a[84:87], v[52:55]\n\t"
  "v_add_f32 v75, v75, v107\n\t"
  "v_add_f32 v76, v76, v84\n\t"
  "v_mfma_f32_16x16x32_bf16 v[56:59], a[212:215], a[84:87], v[56:59]\n\t"
  "s_waitcnt vmcnt(3)\n\t"
  "v_add_f32 v77, v77, v85\n\t"
  "v_mfma_f32_16x16x32_bf16 v[40:43], a[216:219], a[88:91], v[40:43]\n\t"
  "v_add_f32 v78, v78, v86\n\t"
  "v_add_f32 v79, v79, v87\n\t"
  "v_mfma_f32_16x16x32_bf16 v[44:47], a[220:223], a[88:91], v[44:47]\n\t"
  "v_add_f32 v80, v80, v88\n\t"
  "v_add_f32 v81, v81, v89\n\t"
  "v_mfma_f32_16x16x32_bf16 v[48:51], a[224:227], a[88:91], v[48:51]\n\t"
  "v_add_f32 v82, v82, v90\n\t"
  "v_add_f32 v83, v83, v91\n\t"
  "v_mfma_f32_16x16x32_bf16 v[52:55], a[228:231], a[88:91], v[52:55]\n\t"
  "v_add_f32 v68, v68, v92\n\t"
  "v_add_f32 v69, v69, v93\n\t"
  "v_mfma_f32_16x16x32_bf16 v[56:59], a[232:235], a[88:91], v[56:59]\n\t"
  "v_add_f32 v70, v70, v94\n\t"
  "v_add_f32 v71, v71, v95\n\t"
  "v_mfma_f32_16x16x32_bf16 v[40:43], a[236:239], a[92:95], v[40:43]\n\t"
  "v_add_f32 v72, v72, v96\n\t"
  "v_add_f32 v73, v73, v97\n\t"
  "v_mfma_f32_16x16x32_bf16 v[44:47], a[0:3], a[92:95], v[44:47]\n\t"
  "v_add_f32 v74, v74, v98\n\t"
  "v_add_f32 v75, v75, v99\n\t"
  "v_mfma_f32_16x16x32_bf16 v[48:51], a[4:7], a[92:95], v[48:51]\n\t"
  "v_add_f32 v68, v68, v76\n\t"
  "v_add_f32 v69, v69, v77\n\t"
  "v_mfma_f32_16x16x32_bf16 v[52:55], a[8:11], a[92:95], v[52:55]\n\t"
  "v_add_f32 v70, v70, v78\n\t"
  "v_add_f32 v71, v71, v79\n\t"
  "v_mfma_f32_16x16x32_bf16 v[56:59], a[12:15], a[92:95], v[56:59]\n\t"
  "s_waitcnt vmcnt(1)\n\t"
  "v_add_f32 v72, v72, v80\n\t"
  "v_mfma_f32_16x16x32_bf16 v[40:43], a[16:19], a[96:99], v[40:43]\n\t"
  "v_add_f32 v73, v73, v81\n\t"
  "v_add_f32 v74, v74, v82\n\t"
  "v_mfma_f32_16x16x32_bf16 v[44:47], a[20:23], a[96:99], v[44:47]\n\t"
  "v_add_f32 v75, v75, v83\n\t"
  "v_cvt_pk_bf16_f32 v76, v68, v69\n\t"
  "v_mfma_f32_16x16x32_bf16 v[48:51], a[24:27], a[96:99], v[48:51]\n\t"
  "v_cvt_pk_bf16_f32 v77, v70, v71\n\t"
  "v_cvt_pk_bf16_f32 v78, v72, v73\n\t"
  "v_mfma_f32_16x16x32_bf16 v[52:55], a[28:31], a[96:99], v[52:55]\n\t"
  "v_cvt_pk_bf16_f32 v79, v74, v75\n\t"
  "s_mov_b32 s55, 1310720\n\t"
  "v_mfma_f32_16x16x32_bf16 v[56:59], a[32:35], a[96:99], v[56:59]\n\t"
  "buffer_store_dwordx4 v[76:79], %[vout], %[srdo], s55 offen sc1\n\t"
  "v_mfma_f32_16x16x32_bf16 v[40:43], a[36:39], a[100:103], v[40:43]\n\t"
  "v_mfma_f32_16x16x32_bf16 v[44:47], a[40:43], a[100:103], v[44:47]\n\t"
  "v_mfma_f32_16x16x32_bf16 v[48:51], a[44:47], a[100:103], v[48:51]\n\t"
  "v_mfma_f32_16x16x32_bf16 v[52:55], a[48:51], a[100:103], v[52:55]\n\t"
  "v_mfma_f32_16x16x32_bf16 v[56:59], a[52:55], a[100:103], v[56:59]\n\t"
  "s_nop 3\n\t"
  "ds_write_b128 %[vwr], v[40:43] offset:15360\n\t"
  "ds_write_b128 %[vwr], v[44:47] offset:16384\n\t"
  "ds_write_b128 %[vwr], v[48:51] offset:17408\n\t"
  "ds_write_b128 %[vwr], v[52:55] offset:18432\n\t"
  "ds_write_b128 %[vwr], v[56:59] offset:19456\n\t"
  "buffer_store_dwordx2 v[132:133], %[vep], %[srd], 0 offen sc1\n\t"
  "s_waitcnt lgkmcnt(0)\n\t"
  "s_barrier\n\t"
  "ds_read_b128 v[100:103], v60 offset:35840\n\t"
  "ds_read_b128 v[104:107], v61 offset:35840\n\t"
  "ds_read_b128 v[68:71], %[vrd0] offset:15360\n\t"
  "ds_read_b128 v[72:75], %[vrd1] offset:15360\n\t"
  "ds_read_b128 v[76:79], %[vrd0] offset:35840\n\t"
  "ds_read_b128 v[80:83], %[vrd1] offset:35840\n\t"
  "ds_read_b128 v[84:87], %[vrd0] offset:56320\n\t"
  "ds_read_b128 v[88:91], %[vrd1] offset:56320\n\t"
  "ds_read_b128 v[92:95], v60 offset:15360\n\t"
  "ds_read_b128 v[96:99], v61 offset:15360\n\t"
  "s_waitcnt lgkmcnt(7)\n\t"
  "v_pk_add_f32 v[68:69], v[68:69], v[100:101]\n\t"
  "v_pk_add_f32 v[70:71], v[70:71], v[102:103]\n\t"
  "s_waitcnt lgkmcnt(6)\n\t"
  "v_pk_add_f32 v[72:73], v[72:73], v[104:105]\n\t"
  "v_pk_add_f32 v[74:75], v[74:75], v[106:107]\n\t"
  "s_waitcnt lgkmcnt(3)\n\t"
  "v_pk_add_f32 v[76:77], v[76:77], v[84:85]\n\t"
  "v_pk_add_f32 v[78:79], v[78:79], v[86:87]\n\t"
  "s_waitcnt lgkmcnt(2)\n\t"
  "v_pk_add_f32 v[80:81], v[80:81], v[88:89]\n\t"
  "v_pk_add_f32 v[82:83], v[82:83], v[90:91]\n\t"
  "s_waitcnt lgkmcnt(1)\n\t"
  "v_pk_add_f32 v[68:69], v[68:69], v[92:93]\n\t"
  "v_pk_add_f32 v[70:71], v[70:71], v[94:95]\n\t"
  "s_waitcnt lgkmcnt(0)\n\t"
  "v_pk_add_f32 v[72:73], v[72:73], v[96:97]\n\t"
  "v_pk_add_f32 v[74:75], v[74:75], v[98:99]\n\t"
  "v_pk_add_f32 v[68:69], v[68:69], v[76:77]\n\t"
  "v_pk_add_f32 v[70:71], v[70:71], v[78:79]\n\t"
  "v_pk_add_f32 v[72:73], v[72:73], v[80:81]\n\t"
  "v_pk_add_f32 v[74:75], v[74:75], v[82:83]\n\t"
  "v_cvt_pk_bf16_f32 v76, v68, v69\n\t"
  "v_cvt_pk_bf16_f32 v77, v70, v71\n\t"
  "v_cvt_pk_bf16_f32 v78, v72, v73\n\t"
  "v_cvt_pk_bf16_f32 v79, v74, v75\n\t"
  "s_mov_b32 s55, 1351680\n\t"
  "buffer_store_dwordx4 v[76:79], %[vout], %[srdo], s55 offen sc1\n\t"
  "s_branch Lfend%=\n\t"
  "Lst0_slow%=:\n\t"
  "s_nop 7\n\t"
  "s_nop 3\n\t"
  "s_mov_b32 s49, 0x40000\n\t"
  "Lst0_loop%=:\n\t"
  "s_sleep 2\n\t"
  "buffer_load_dwordx2 v[92:93], v136, %[srd], %[sST] offen offset:0 sc1\n\t"
  "buffer_load_dwordx2 v[94:95], v136, %[srd], %[sST] offen offset:1024 sc1\n\t"
  "buffer_load_dwordx2 v[96:97], v136, %[srd], %[sST] offen offset:2048 sc1\n\t"
  "buffer_load_dwordx2 v[98:99], v136, %[srd], %[sST] offen offset:3072 sc1\n\t"
  "s_waitcnt vmcnt(0)\n\t"
  "v_min_u32 v131, v93, v95\n\t"
  "v_min3_u32 v131, v131, v97, v99\n\t"
  "v_cmp_eq_u32 vcc, 0, v131\n\t"
  "s_nop 3\n\t"
  "s_cmp_eq_u64 vcc, 0\n\t"
  "s_cbranch_scc1 Lst0_back%=\n\t"
  "s_sub_u32 s49, s49, 1\n\t"
  "s_cmp_lg_u32 s49, 0\n\t"
  "s_cbranch_scc1 Lst0_loop%=\n\t"
  "s_branch Lst0_back%=\n\t"
  "Lw2_slow%=:\n\t"
  "s_nop 7\n\t"
  "s_nop 3\n\t"
  "s_mov_b32 s49, 0x40000\n\t"
  "Lw2_loop%=:\n\t"
  "s_sleep 2\n\t"
  "buffer_load_dwordx2 v[138:139], %[vpw], %[srd], 0 offen sc1\n\t"
  "s_waitcnt vmcnt(0)\n\t"
  "v_cmp_ne_u64 vcc, v[132:133], v[138:139]\n\t"
  "s_nop 3\n\t"
  "s_cmp_eq_u64 vcc, 0\n\t"
  "s_cbranch_scc1 Lw2_back%=\n\t"
  "s_sub_u32 s49, s49, 1\n\t"
  "s_cmp_lg_u32 s49, 0\n\t"
  "s_cbranch_scc1 Lw2_loop%=\n\t"
  "s_branch Lw2_back%=\n\t"
  "Lst1_slow%=:\n\t"
  "s_nop 7\n\t"
  "s_nop 3\n\t"
  "s_mov_b32 s49, 0x40000\n\t"
  "Lst1_loop%=:\n\t"
  "s_sleep 2\n\t"
  "buffer_load_dwordx2 v[100:101], v136, %[srd], s54 offen offset:0 sc1\n\t"
  "buffer_load_dwordx2 v[102:103], v136, %[srd], s54 offen offset:1024 sc1\n\t"
  "buffer_load_dwordx2 v[104:105], v136, %[srd], s54 offen offset:2048 sc1\n\t"
  "buffer_load_dwordx2 v[106:107], v136, %[srd], s54 offen offset:3072 sc1\n\t"
  "s_waitcnt vmcnt(0)\n\t"
  "v_min_u32 v131, v101, v103\n\t"
  "v_min3_u32 v131, v131, v105, v107\n\t"
  "v_cmp_eq_u32 vcc, 0, v131\n\t"
  "s_nop 3\n\t"
  "s_cmp_eq_u64 vcc, 0\n\t"
  "s_cbranch_scc1 Lst1_back%=\n\t"
  "s_sub_u32 s49, s49, 1\n\t"
  "s_cmp_lg_u32 s49, 0\n\t"
  "s_cbranch_scc1 Lst1_loop%=\n\t"
  "s_branch Lst1_back%=\n\t"
  "Lfl0_slow%=:\n\t"
  "s_nop 7\n\t"
  "s_nop 3\n\t"
  "s_mov_b32 s49, 0x40000\n\t"
  "Lfl0_loop%=:\n\t"
  "s_sleep 2\n\t"
  "s_add_u32 s57, %[sFL], 0\n\t"
  "buffer_load_dwordx2 v[138:139], v137, %[srd], s57 offen sc1\n\t"
  "s_waitcnt vmcnt(0)\n\t"
  "v_cmp_ne_u64 vcc, v[132:133], v[138:139]\n\t"
  "s_nop 3\n\t"
  "s_cmp_eq_u64 vcc, 0\n\t"
  "s_cbranch_scc1 Lfl0_back%=\n\t"
  "s_sub_u32 s49, s49, 1\n\t"
  "s_cmp_lg_u32 s49, 0\n\t"
  "s_cbranch_scc1 Lfl0_loop%=\n\t"
  "s_branch Lfl0_back%=\n\t"
  "Lfl1_slow%=:\n\t"
  "s_nop 7\n\t"
  "s_nop 3\n\t"
  "s_mov_b32 s49, 0x40000\n\t"
  "Lfl1_loop%=:\n\t"
  "s_sleep 2\n\t"
  "s_add_u32 s57, %[sFL], 8192\n\t"
  "buffer_load_dwordx2 v[138:139], v137, %[srd], s57 offen sc1\n\t"
  "s_waitcnt vmcnt(0)\n\t"
  "v_cmp_ne_u64 vcc, v[132:133], v[138:139]\n\t"
  "s_nop 3\n\t"
  "s_cmp_eq_u64 vcc, 0\n\t"
  "s_cbranch_scc1 Lfl1_back%=\n\t"
  "s_sub_u32 s49, s49, 1\n\t"
  "s_cmp_lg_u32 s49, 0\n\t"
  "s_cbranch_scc1 Lfl1_loop%=\n\t"
  "s_branch Lfl1_back%=\n\t"
  "Lfl2_slow%=:\n\t"
  "s_nop 7\n\t"
  "s_nop 3\n\t"
  "s_mov_b32 s49, 0x40000\n\t"
  "Lfl2_loop%=:\n\t"
  "s_sleep 2\n\t"
  "s_add_u32 s57, %[sFL], 16384\n\t"
  "buffer_load_dwordx2 v[138:139], v137, %[srd], s57 offen sc1\n\t"
  "s_waitcnt vmcnt(0)\n\t"
  "v_cmp_ne_u64 vcc, v[132:133], v[138:139]\n\t"
  "s_nop 3\n\t"
  "s_cmp_eq_u64 vcc, 0\n\t"
  "s_cbranch_scc1 Lfl2_back%=\n\t"
  "s_sub_u32 s49, s49, 1\n\t"
  "s_cmp_lg_u32 s49, 0\n\t"
  "s_cbranch_scc1 Lfl2_loop%=\n\t"
  "s_branch Lfl2_back%=\n\t"
  "Lfl3_slow%=:\n\t"
  "s_nop 7\n\t"
  "s_nop 3\n\t"
  "s_mov_b32 s49, 0x40000\n\t"
  "Lfl3_loop%=:\n\t"
  "s_sleep 2\n\t"
  "s_add_u32 s57, %[sFL], 24576\n\t"
  "buffer_load_dwordx2 v[138:139], v137, %[srd], s57 offen sc1\n\t"
  "s_waitcnt vmcnt(0)\n\t"
  "v_cmp_ne_u64 vcc, v[132:133], v[138:139]\n\t"
  "s_nop 3\n\t"
  "s_cmp_eq_u64 vcc, 0\n\t"
  "s_cbranch_scc1 Lfl3_back%=\n\t"
  "s_sub_u32 s49, s49, 1\n\t"
  "s_cmp_lg_u32 s49, 0\n\t"
  "s_cbranch_scc1 Lfl3_loop%=\n\t"
  "s_branch Lfl3_back%=\n\t"
  "Lfend%=:\n\t"
    :
    : [srd] "s"(srd), [srdo] "s"(srdo), [w] "s"(w),
      [tlo] "s"((u32)tag), [thi] "s"((u32)(tag >> 32)), [vep] "v"(vep), [sW1] "s"(OFF_W1F + (j * 4 + w) * 61440u), [sW2] "s"(OFF_W2F + (ng * 4 + w) * 61440u),
      [sUB] "s"(OFF_UB + (j * 4 + w) * 1536u),
      [sH2ld] "s"(OFF_H2F + ((4 * p + sub) * 48 + 12 * w) * 1024u), [sST] "s"(OFF_ST2 + p * 4096u), [sFL] "s"(OFF_FL + p * 32768u + w * 2048u),
      [sRT] "s"(OFF_RT + ng * 320u), [wdone] "s"(wdone), [vpub] "v"(vpub), [vpw] "v"(vpw), [vpx] "v"(vpx),
      [vtr] "v"(vtr), [vl16] "v"(vl16), [vdma] "v"(vdma), [vh2st] "v"(vh2st), [vstc] "v"(vstc), [vstw] "v"(vstw), [vflw] "v"(vflw),
      [vres0] "v"(vres0), [vres1] "v"(vres1), [vres2] "v"(vres2), [vwr] "v"(vwr), [vrd0] "v"(vrd0), [vrd1] "v"(vrd1), [vout] "v"(vout)
    : "memory", "vcc", "scc",
"a0", "a1", "a2", "a3", "a4", "a5", "a6", "a7", "a8", "a9", "a10", "a11", "a12", "a13", "a14", "a15", "a16", "a17", "a18", "a19", "a20", "a21", "a22", "a23", "a24", "a25", "a26", "a27", "a28", "a29", "a30", "a31", "a32", "a33", "a34", "a35", "a36", "a37", "a38", "a39", "a40", "a41", "a42", "a43", "a44", "a45", "a46", "a47", "a48", "a49", "a50", "a51", "a52", "a53", "a54", "a55", "a56", "a57", "a58", "a59", "a60", "a61", "a62", "a63", "a64", "a65", "a66", "a67", "a68", "a69", "a70", "a71", "a72", "a73", "a74", "a75", "a76", "a77", "a78", "a79", "a80", "a81", "a82", "a83", "a84", "a85", "a86", "a87", "a88", "a89", "a90", "a91", "a92", "a93", "a94", "a95", "a96", "a97", "a98", "a99", "a100", "a101", "a102", "a103", "a104", "a105", "a106", "a107", "a108", "a109", "a110", "a111", "a112", "a113", "a114", "a115", "a116", "a117", "a118", "a119", "a120", "a121", "a122", "a123", "a124", "a125", "a126", "a127", "a128", "a129", "a130", "a131", "a132", "a133", "a134", "a135", "a136", "a137", "a138", "a139", "a140", "a141", "a142", "a143", "a144", "a145", "a146", "a147", "a148", "a149", "a150", "a151", "a152", "a153", "a154", "a155", "a156", "a157", "a158", "a159", "a160", "a161", "a162", "a163", "a164", "a165", "a166", "a167", "a168", "a169", "a170", "a171", "a172", "a173", "a174", "a175", "a176", "a177", "a178", "a179", "a180", "a181", "a182", "a183", "a184", "a185", "a186", "a187", "a188", "a189", "a190", "a191", "a192", "a193", "a194", "a195", "a196", "a197", "a198", "a199", "a200", "a201", "a202", "a203", "a204", "a205", "a206", "a207", "a208", "a209", "a210", "a211", "a212", "a213", "a214", "a215", "a216", "a217", "a218", "a219", "a220", "a221", "a222", "a223", "a224", "a225", "a226", "a227", "a228", "a229", "a230", "a231", "a232", "a233", "a234", "a235", "a236", "a237", "a238", "a239", "a240", "a241", "a242", "a243", "a244", "a245", "a246", "a247", "a248", "a249", "a250", "a251", "a252", "a253", "a254", "a255", "s44", "s45", "s46", "s47", "s48", "s49", "s52", "s54", "s55", "s56", "s57", "v20", "v21", "v22", "v23", "v24", "v25", "v26", "v27", "v28", "v29", "v30", "v31", "v32", "v33", "v34", "v35", "v36", "v37", "v38", "v39", "v40", "v41", "v42", "v43", "v44", "v45", "v46", "v47", "v48", "v49", "v50", "v51", "v52", "v53", "v54", "v55", "v56", "v57", "v58", "v59", "v60", "v61", "v62", "v63", "v64", "v65", "v66", "v67", "v68", "v69", "v70", "v71", "v72", "v73", "v74", "v75", "v76", "v77", "v78", "v79", "v80", "v81", "v82", "v83", "v84", "v85", "v86", "v87", "v88", "v89", "v90", "v91", "v92", "v93", "v94", "v95", "v96", "v97", "v98", "v99", "v100", "v101", "v102", "v103", "v104", "v105", "v106", "v107", "v108", "v109", "v110", "v111", "v112", "v113", "v114", "v115", "v116", "v117", "v118", "v119", "v120", "v121", "v122", "v123", "v124", "v125", "v126", "v127", "v128", "v129", "v130", "v131", "v132", "v133", "v134", "v135", "v136", "v137", "v138", "v139", "v140", "v141", "v142", "v143", "v144", "v145", "v146", "v147", "v148", "v149", "v150", "v151", "v152", "v153", "v154", "v155", "v156", "v157", "v158", "v159", "v160", "v161", "v162", "v163", "v164", "v165", "v166", "v167", "v168", "v169", "v170", "v171", "v172", "v173", "v174", "v175", "v176", "v177", "v178", "v179", "v180", "v181", "v182", "v183", "v184", "v185", "v186", "v187", "v188", "v189", "v190", "v191", "v192", "v193", "v194", "v195", "v196", "v197", "v198", "v199", "v200", "v201", "v202", "v203", "v204", "v205", "v206", "v207", "v208", "v209", "v210", "v211", "v212", "v213", "v214", "v215", "v216", "v217", "v218", "v219", "v220", "v221", "v222", "v223", "v224", "v225", "v226", "v227", "v228", "v229", "v230", "v231", "v232", "v233", "v234", "v235", "v236", "v237", "v238", "v239", "v240", "v241", "v242", "v243", "v244", "v245", "v246", "v247", "v248", "v249", "v250", "v251", "v252", "v253", "v254", "v255"
  );
}

extern "C" void kernel_launch(void* const* d_in, const int* in_sizes, int n_in, void* d_out, int out_size,
                              void* d_ws, size_t ws_size, hipStream_t stream) {
  k_main<<<256, 256, 0, stream>>>((const u16*)d_in[0], (const u16*)d_in[1], (const u16*)d_in[2], (const u16*)d_in[3], (const u16*)d_in[4],
                                  (const u16*)d_in[5], (const u16*)d_in[7], (const u16*)d_in[8], (const u16*)d_in[9], (const u16*)d_in[10],
                                  (const u16*)d_in[11], (char*)d_ws, (u16*)d_out);
}
```

```cpp
#include <hip/hip_runtime.h>
#include <stdint.h>

typedef unsigned short u16;
typedef unsigned int u32;
typedef unsigned long long u64;
typedef __attribute__((ext_vector_type(8))) __bf16 bf16x8;
typedef __attribute__((ext_vector_type(2))) __bf16 bf16x2;
typedef __attribute__((ext_vector_type(4))) short s16x4;
typedef __attribute__((ext_vector_type(4))) float f32x4;
typedef __attribute__((ext_vector_type(2))) float f32x2;
typedef __attribute__((ext_vector_type(4))) u32 u32x4;
typedef __attribute__((ext_vector_type(2))) u32 u32x2;
#define LAS __attribute__((address_space(3)))
#define GAS __attribute__((address_space(1)))
#define DI __device__ __forceinline__

#define STAMP(id)
#define STAMP_DECL
#define STAMP_OUT(kid)
#define A1_R 4
#define A1_P 8
#define A1_S 2
#define A1_S0 0
#define A1_X 0
#define DBG_W 1
#define DBG_X 1
#define DBG_M 1
#define W_DELAY 12
#define ROLEMAP 0
#define SELF_BAR 0
#define CODEW 1
constexpr int S_ = 2048, D_ = 640, F_ = 1536;
constexpr unsigned OFF_EP = 0u;
constexpr unsigned OFF_FR = 2048u;
constexpr unsigned OFF_FA1 = 4096u;
constexpr unsigned OFF_FA2 = 24576u;
constexpr unsigned OFF_FA3 = 55296u;
constexpr unsigned OFF_FX = 71680u;
constexpr unsigned OFF_FW1 = 92160u;
constexpr unsigned OFF_FU = 100352u;
constexpr unsigned OFF_FW2 = 108544u;
constexpr unsigned OFF_RT = 112640u;
constexpr unsigned OFF_GB = 115200u;
constexpr unsigned OFF_RL = 117760u;
constexpr unsigned OFF_ST2 = 131072u;
constexpr unsigned OFF_FL = 262144u;
constexpr unsigned OFF_MODP = 786432u;
constexpr unsigned OFF_ST1 = 847872u;
constexpr unsigned OFF_UB = 864256u;
constexpr unsigned OFF_KVT = 962560u;
constexpr unsigned OFF_XT = 3584000u;
constexpr unsigned OFF_X2P = 6205440u;
constexpr unsigned OFF_H2F = 8826880u;
constexpr unsigned OFF_W1F = 15118336u;
constexpr unsigned OFF_W2F = 19050496u;
constexpr unsigned WS_END = 21016576u;
constexpr unsigned TAG_A = 2654435769u;
constexpr unsigned TAG_B = 2246822507u;
constexpr unsigned TAG_C = 2135587861u;
constexpr unsigned TAG_D = 3266489909u;
constexpr unsigned FC_PITCH = 4160u;
constexpr unsigned FC_CH = 16640u;
constexpr unsigned L_FC = 0u;
constexpr unsigned L_KV = 66560u;
constexpr unsigned L_X = 84992u;
constexpr unsigned L_ST = 101376u;
constexpr unsigned L_XR = 117760u;
constexpr unsigned L_CNT = 140288u;
constexpr unsigned L_UN = 71680u;
constexpr unsigned UN_SZ = 22528u;

constexpr unsigned OOB = 0x80000000u;
DI unsigned rfl(unsigned x) { return __builtin_amdgcn_readfirstlane(x); }
DI unsigned stkey(unsigned r) { const unsigned k = (r >> 1) & 7; return (((k ^ (k >> 1)) >> 1) & 1) | ((k & 1) << 1) | (k & 4); }
DI float bflo(u32 w) { return __uint_as_float(w << 16); }
DI float bfhi(u32 w) { return __uint_as_float(w & 0xffff0000u); }
DI u32 pack_bf16(float lo, float hi) { bf16x2 v; v[0] = (__bf16)lo; v[1] = (__bf16)hi; return __builtin_bit_cast(u32, v); }
DI float rbf(float x) { __bf16 b = (__bf16)x; return (float)b; }
DI float dot2(u32 a, u32 b, float c) { return __builtin_amdgcn_fdot2_f32_bf16(__builtin_bit_cast(bf16x2, a), __builtin_bit_cast(bf16x2, b), c, false); }
DI f32x4 mfma(u32x4 a, u32x4 b, f32x4 c) { return __builtin_amdgcn_mfma_f32_16x16x32_bf16(__builtin_bit_cast(bf16x8, a), __builtin_bit_cast(bf16x8, b), c, 0, 0, 0); }
DI void mfma_v(f32x4& c, u32x4 a, u32x4 b) { asm volatile("s_nop 1\n\tv_mfma_f32_16x16x32_bf16 %0, %1, %2, %0" : "+v"(c) : "v"(a), "v"(b)); }
DI u32x2 tr_read(const char* p) { return __builtin_bit_cast(u32x2, __builtin_amdgcn_ds_read_tr16_b64_v4i16((LAS s16x4*)p)); }
typedef __amdgpu_buffer_rsrc_t rsrc_t;
DI rsrc_t mkrsrc(const void* p, unsigned bytes) { return __builtin_amdgcn_make_buffer_rsrc((void*)p, 0, bytes, 0x00020000); }
#define BAR() asm volatile("s_waitcnt lgkmcnt(0)\n\ts_barrier" ::: "memory")
#define FENCE() do { asm volatile("" ::: "memory"); __builtin_amdgcn_sched_barrier(0); } while (0)
constexpr int SC1 = 16, NT = 2, W_IDLE_SLEEP = 8;

DI u64 load_tag(const char* ws) {
  u64 epoch;
  asm volatile("s_load_dwordx2 %0, %1, 0\n\ts_waitcnt lgkmcnt(0)" : "=&s"(epoch) : "s"(ws) : "memory");
  const u32 tl = ((u32)epoch ^ TAG_A) * TAG_B, th = ((u32)(epoch >> 32) ^ TAG_C) * TAG_D + tl;
  return ((u64)th << 32) | tl;
}
template <int N, int SL = 2>
DI void poll_flags(rsrc_t srd, unsigned voff, u64 tag) {
  const u32 lo = (u32)tag, hi = (u32)(tag >> 32);
#pragma unroll 1
  for (int spin = 0; spin < (1 << 18); ++spin) {
    u32 bad = 0;
#pragma unroll
    for (int k = 0; k < N; ++k) { const u32x2 f = __builtin_amdgcn_raw_buffer_load_b64(srd, voff + k * 512, 0, SC1); bad |= (f[0] ^ lo) | (f[1] ^ hi); }
    asm volatile("" ::: "memory");
    if (__all(bad == 0)) break;
    __builtin_amdgcn_s_sleep(SL);
  }
  FENCE();
}
DI void put_flag(rsrc_t srd, unsigned off, unsigned lane, u64 tag) {
  __builtin_amdgcn_raw_buffer_store_b128(u32x4{(u32)tag, (u32)(tag >> 32), (u32)tag, (u32)(tag >> 32)}, srd, lane < 8 ? off + lane * 16 : OOB, 0, SC1);
  FENCE();
}
DI float modval(rsrc_t srd, unsigned col) {
  float m[4];
#pragma unroll
  for (int k = 0; k < 4; ++k) m[k] = __uint_as_float(__builtin_amdgcn_raw_buffer_load_b32(srd, col * 4, OFF_MODP + k * 15360, SC1));
  return rbf((m[0] + m[1]) + (m[2] + m[3]));
}

constexpr int RP1 = 400, RP2 = 656, L_WS = 69632;

__global__ __launch_bounds__(256, 1) void k_main(const u16* __restrict__ x, const u16* __restrict__ kIn, const u16* __restrict__ vIn,
                                                 const u16* __restrict__ shk, const u16* __restrict__ ssmk, const u16* __restrict__ ssmD,
                                                 const u16* __restrict__ wg, const u16* __restrict__ wu, const u16* __restrict__ wd,
                                                 const u16* __restrict__ cIn, const u16* __restrict__ wmod, char* ws, u16* __restrict__ out) {
  __shared__ __attribute__((aligned(16))) char smem[163840];
  const unsigned b = blockIdx.x, tid = threadIdx.x, w = rfl(tid >> 6), lane = tid & 63;
  const unsigned xcd = b & 7, loc = b >> 3, pl = loc & 3, p = 4 * (xcd >> 1) + pl, j = 8 * (xcd & 1) + (loc >> 2), ng = j & 7, sub = j >> 3;
  asm volatile("" :: "s"(x), "s"(kIn), "s"(vIn), "s"(shk), "s"(ssmk), "s"(ssmD), "s"(wg), "s"(wu), "s"(wd), "s"(cIn), "s"(wmod), "s"(ws), "s"(out));
  STAMP_DECL; STAMP(0);
  const rsrc_t srd = mkrsrc(ws, WS_END);
  const rsrc_t srdo = mkrsrc(out, S_ * D_ * 2);
  const unsigned lb = (unsigned)(size_t)smem;
  const unsigned role = loc < 20 ? 0 : loc < 28 ? 1 : 2;
  const unsigned cb = xcd * 20 + loc, w1 = xcd * 8 + loc - 20, w2 = xcd * 4 + loc - 28;
  const u32x4 zero4 = {0, 0, 0, 0};
  unsigned wdone = 0, dval = 0;
  if (w == 3 && loc >= 24) {
    u64 pc; unsigned len;
    asm volatile("s_getpc_b64 %0\n.Lk_here:\n\ts_mov_b32 %1, .Lk_end-.Lk_here" : "=s"(pc), "=s"(len));
    asm volatile("buffer_load_dword a255, %0, %1, 0 offen" :: "v"((lane * 8 + loc - 24) * 128), "s"(mkrsrc((const void*)pc, len)) : "a255", "memory");
  }
  const unsigned ui = role == 0 ? cb : role == 1 ? 160 + 64 * w + w1 : 4 * w2 + w, gi = ui >> 2;
  const unsigned kq = ui & 3, cgp = gi < 30 || gi >= 50 ? gi : gi < 40 ? gi + 10 : gi - 10;
  const unsigned vm = (lane >> 3) * 7680 + (((lane & 7) ^ (((lane >> 4) & 3) << 1)) * 16), sMod = kq * 160u * 7680u + cgp * 128u;
#define GEMV_UNIT(lu_, strict_) asm volatile( \
  "s_nop 4\n\t" \
  "v_lshlrev_b32 v176, 1, %[vl]\n\t" \
  "v_add_u32 v176, %[lu], v176\n\t" \
  "v_mov_b32 v96, 0\n\t" \
  "v_mov_b32 v97, 0\n\t" \
  "v_mov_b32 v98, 0\n\t" \
  "s_cmp_lt_u32 %[loc], 28\n\t" \
  "s_waitcnt vmcnt(36)\n\t" \
  "s_cbranch_scc1 Lu2a%=\n\t" \
  "s_waitcnt vmcnt(20)\n\t" \
  "Lu2a%=:\n\t" \
  "ds_read_u16_d16_hi v96, v176 offset:20480\n\t" \
  "ds_read_u16_d16_hi v97, v176 offset:20608\n\t" \
  "ds_read_u16_d16_hi v98, v176 offset:20736\n\t" \
  "s_waitcnt lgkmcnt(2)\n\t" \
  "v_mul_f32 v177, 0xbfb8aa3b, v96\n\t" \
  "s_waitcnt lgkmcnt(1)\n\t" \
  "v_mul_f32 v178, 0xbfb8aa3b, v97\n\t" \
  "s_waitcnt lgkmcnt(0)\n\t" \
  "v_mul_f32 v179, 0xbfb8aa3b, v98\n\t" \
  "v_exp_f32 v177, v177\n\t" \
  "v_exp_f32 v178, v178\n\t" \
  "v_exp_f32 v179, v179\n\t" \
  "v_add_f32 v177, 1.0, v177\n\t" \
  "v_add_f32 v178, 1.0, v178\n\t" \
  "v_add_f32 v179, 1.0, v179\n\t" \
  "v_rcp_f32 v177, v177\n\t" \
  "v_rcp_f32 v178, v178\n\t" \
  "v_rcp_f32 v179, v179\n\t" \
  "v_mul_f32 v177, v177, v96\n\t" \
  "v_mul_f32 v178, v178, v97\n\t" \
  "v_mul_f32 v179, v179, v98\n\t" \
  "v_cvt_pk_bf16_f32 v177, v177, v177\n\t" \
  "v_cvt_pk_bf16_f32 v178, v178, v178\n\t" \
  "v_cvt_pk_bf16_f32 v179, v179, v179\n\t" \
  "ds_write_b16 v176, v177 offset:21504\n\t" \
  "ds_write_b16 v176, v178 offset:21632\n\t" \
  "ds_write_b16 v176, v179 offset:21760\n\t" \
  "v_lshrrev_b32 v180, 4, %[vl]\n\t" \
  "v_lshlrev_b32 v180, 3, v180\n\t" \
  "v_add_u32 v180, %[lu], v180\n\t" \
  "ds_read_b64 v[100:101], v180 offset:21504\n\t" \
  "ds_read_b64 v[102:103], v180 offset:21536\n\t" \
  "ds_read_b64 v[104:105], v180 offset:21568\n\t" \
  "ds_read_b64 v[106:107], v180 offset:21600\n\t" \
  "ds_read_b64 v[108:109], v180 offset:21632\n\t" \
  "ds_read_b64 v[110:111], v180 offset:21664\n\t" \
  "ds_read_b64 v[112:113], v180 offset:21696\n\t" \
  "ds_read_b64 v[114:115], v180 offset:21728\n\t" \
  "ds_read_b64 v[116:117], v180 offset:21760\n\t" \
  "ds_read_b64 v[118:119], v180 offset:21792\n\t" \
  "v_xor_b32 v181, 32, %[vtm]\n\t" \
  "v_xor_b32 v182, 64, %[vtm]\n\t" \
  "v_xor_b32 v183, 96, %[vtm]\n\t" \
  "s_waitcnt vmcnt(16)\n\t" \
  "s_cbranch_scc1 Lu2b%=\n\t" \
  "s_waitcnt vmcnt(0)\n\t" \
  "Lu2b%=:\n\t" \
  "ds_read_b64_tr_b16 v[120:121], %[vtm] offset:0\n\t" \
  "ds_read_b64_tr_b16 v[122:123], %[vtm] offset:2048\n\t" \
  "ds_read_b64_tr_b16 v[124:125], v181 offset:0\n\t" \
  "ds_read_b64_tr_b16 v[126:127], v181 offset:2048\n\t" \
  "ds_read_b64_tr_b16 v[128:129], v182 offset:0\n\t" \
  "ds_read_b64_tr_b16 v[130:131], v182 offset:2048\n\t" \
  "ds_read_b64_tr_b16 v[132:133], v183 offset:0\n\t" \
  "ds_read_b64_tr_b16 v[134:135], v183 offset:2048\n\t" \
  "ds_read_b64_tr_b16 v[136:137], %[vtm] offset:4096\n\t" \
  "ds_read_b64_tr_b16 v[138:139], %[vtm] offset:6144\n\t" \
  "ds_read_b64_tr_b16 v[140:141], v181 offset:4096\n\t" \
  "ds_read_b64_tr_b16 v[142:143], v181 offset:6144\n\t" \
  "ds_read_b64_tr_b16 v[144:145], v182 offset:4096\n\t" \
  "ds_read_b64_tr_b16 v[146:147], v182 offset:6144\n\t" \
  "ds_read_b64_tr_b16 v[148:149], v183 offset:4096\n\t" \
  "ds_read_b64_tr_b16 v[150:151], v183 offset:6144\n\t" \
  "s_waitcnt lgkmcnt(14)\n\t" \
  "v_mfma_f32_16x16x32_bf16 v[152:155], v[100:103], v[120:123], 0\n\t" \
  "s_waitcnt lgkmcnt(12)\n\t" \
  "v_mfma_f32_16x16x32_bf16 v[156:159], v[100:103], v[124:127], 0\n\t" \
  "s_waitcnt lgkmcnt(10)\n\t" \
  "v_mfma_f32_16x16x32_bf16 v[160:163], v[100:103], v[128:131], 0\n\t" \
  "s_waitcnt lgkmcnt(8)\n\t" \
  "v_mfma_f32_16x16x32_bf16 v[164:167], v[100:103], v[132:135], 0\n\t" \
  "ds_read_b64_tr_b16 v[120:121], %[vtm] offset:8192\n\t" \
  "ds_read_b64_tr_b16 v[122:123], %[vtm] offset:10240\n\t" \
  "ds_read_b64_tr_b16 v[124:125], v181 offset:8192\n\t" \
  "ds_read_b64_tr_b16 v[126:127], v181 offset:10240\n\t" \
  "ds_read_b64_tr_b16 v[128:129], v182 offset:8192\n\t" \
  "ds_read_b64_tr_b16 v[130:131], v182 offset:10240\n\t" \
  "ds_read_b64_tr_b16 v[132:133], v183 offset:8192\n\t" \
  "ds_read_b64_tr_b16 v[134:135], v183 offset:10240\n\t" \
  "s_waitcnt lgkmcnt(14)\n\t" \
  "v_mfma_f32_16x16x32_bf16 v[152:155], v[104:107], v[136:139], v[152:155]\n\t" \
  "s_waitcnt lgkmcnt(12)\n\t" \
  "v_mfma_f32_16x16x32_bf16 v[156:159], v[104:107], v[140:143], v[156:159]\n\t" \
  "s_waitcnt lgkmcnt(10)\n\t" \
  "v_mfma_f32_16x16x32_bf16 v[160:163], v[104:107], v[144:147], v[160:163]\n\t" \
  "s_waitcnt lgkmcnt(8)\n\t" \
  "v_mfma_f32_16x16x32_bf16 v[164:167], v[104:107], v[148:151], v[164:167]\n\t" \
  "ds_read_b64_tr_b16 v[136:137], %[vtm] offset:12288\n\t" \
  "ds_read_b64_tr_b16 v[138:139], %[vtm] offset:14336\n\t" \
  "ds_read_b64_tr_b16 v[140:141], v181 offset:12288\n\t" \
  "ds_read_b64_tr_b16 v[142:143], v181 offset:14336\n\t" \
  "ds_read_b64_tr_b16 v[144:145], v182 offset:12288\n\t" \
  "ds_read_b64_tr_b16 v[146:147], v182 offset:14336\n\t" \
  "ds_read_b64_tr_b16 v[148:149], v183 offset:12288\n\t" \
  "ds_read_b64_tr_b16 v[150:151], v183 offset:14336\n\t" \
  "s_waitcnt lgkmcnt(14)\n\t" \
  "v_mfma_f32_16x16x32_bf16 v[152:155], v[108:111], v[120:123], v[152:155]\n\t" \
  "s_waitcnt lgkmcnt(12)\n\t" \
  "v_mfma_f32_16x16x32_bf16 v[156:159], v[108:111], v[124:127], v[156:159]\n\t" \
  "s_waitcnt lgkmcnt(10)\n\t" \
  "v_mfma_f32_16x16x32_bf16 v[160:163], v[108:111], v[128:131], v[160:163]\n\t" \
  "s_waitcnt lgkmcnt(8)\n\t" \
  "v_mfma_f32_16x16x32_bf16 v[164:167], v[108:111], v[132:135], v[164:167]\n\t" \
  "ds_read_b64_tr_b16 v[120:121], %[vtm] offset:16384\n\t" \
  "ds_read_b64_tr_b16 v[122:123], %[vtm] offset:18432\n\t" \
  "ds_read_b64_tr_b16 v[124:125], v181 offset:16384\n\t" \
  "ds_read_b64_tr_b16 v[126:127], v181 offset:18432\n\t" \
  "ds_read_b64_tr_b16 v[128:129], v182 offset:16384\n\t" \
  "ds_read_b64_tr_b16 v[130:131], v182 offset:18432\n\t" \
  "ds_read_b64_tr_b16 v[132:133], v183 offset:16384\n\t" \
  "ds_read_b64_tr_b16 v[134:135], v183 offset:18432\n\t" \
  "s_waitcnt lgkmcnt(14)\n\t" \
  "v_mfma_f32_16x16x32_bf16 v[152:155], v[112:115], v[136:139], v[152:155]\n\t" \
  "s_waitcnt lgkmcnt(12)\n\t" \
  "v_mfma_f32_16x16x32_bf16 v[156:159], v[112:115], v[140:143], v[156:159]\n\t" \
  "s_waitcnt lgkmcnt(10)\n\t" \
  "v_mfma_f32_16x16x32_bf16 v[160:163], v[112:115], v[144:147], v[160:163]\n\t" \
  "s_waitcnt lgkmcnt(8)\n\t" \
  "v_mfma_f32_16x16x32_bf16 v[164:167], v[112:115], v[148:151], v[164:167]\n\t" \
  "s_waitcnt lgkmcnt(6)\n\t" \
  "v_mfma_f32_16x16x32_bf16 v[152:155], v[116:119], v[120:123], v[152:155]\n\t" \
  "s_waitcnt lgkmcnt(4)\n\t" \
  "v_mfma_f32_16x16x32_bf16 v[156:159], v[116:119], v[124:127], v[156:159]\n\t" \
  "s_waitcnt lgkmcnt(2)\n\t" \
  "v_mfma_f32_16x16x32_bf16 v[160:163], v[116:119], v[128:131], v[160:163]\n\t" \
  "s_waitcnt lgkmcnt(0)\n\t" \
  "v_mfma_f32_16x16x32_bf16 v[164:167], v[116:119], v[132:135], v[164:167]\n\t" \
  "v_lshlrev_b32 v176, 2, %[vl]\n\t" \
  "s_nop 2\n\t" \
  "v_mov_b32_dpp v152, v156 quad_perm:[0,1,2,3] row_mask:0x2 bank_mask:0xf\n\t" \
  "s_nop 1\n\t" \
  "v_mov_b32_dpp v152, v160 quad_perm:[0,1,2,3] row_mask:0x4 bank_mask:0xf\n\t" \
  "s_nop 1\n\t" \
  "v_mov_b32_dpp v152, v164 quad_perm:[0,1,2,3] row_mask:0x8 bank_mask:0xf\n\t" \
  "buffer_store_dword v152, v176, %[srd], %[sMo] offen sc1\n\t" \
  "s_nop 1\n\t" \
        :: [srd] "s"(srd), [lu] "s"(lu_), [loc] "s"(strict_), [sMo] "s"(OFF_MODP + kq * 15360u + cgp * 256u), [vl] "v"(lane), \
           [vtm] "v"((lu_) + (lane >> 2) * 128 + ((lane >> 3) & 3) * 32 + (lane & 3) * 8) : "memory", "vcc", "scc", \
"v96", "v97", "v98", "v100", "v101", "v102", "v103", "v104", "v105", "v106", "v107", "v108", "v109", "v110", "v111", "v112", "v113", "v114", "v115", "v116", "v117", "v118", "v119", "v120", "v121", "v122", "v123", "v124", "v125", "v126", "v127", "v128", "v129", "v130", "v131", "v132", "v133", "v134", "v135", "v136", "v137", "v138", "v139", "v140", "v141", "v142", "v143", "v144", "v145", "v146", "v147", "v148", "v149", "v150", "v151", "v152", "v153", "v154", "v155", "v156", "v157", "v158", "v159", "v160", "v161", "v162", "v163", "v164", "v165", "v166", "v167", "v176", "v177", "v178", "v179", "v180", "v181", "v182", "v183" \
      )
  u64 tag;
  if (role == 0) {
    const rsrc_t rk = mkrsrc(kIn, S_ * D_ * 2), rv = mkrsrc(vIn, S_ * D_ * 2), rf = mkrsrc(ssmk, S_ * D_ * 2);
    const unsigned tb = 4 * (cb & 31) + w, d0 = 128 * (cb >> 5) + 2 * lane;
    u32x4 fa[4];
    const unsigned lkv = L_KV + 2048 + w * 9216;
    asm volatile(
  "s_mov_b32 m0, %[lk]\n\t"
  "s_nop 3\n\t"
  "buffer_load_dwordx4 %[vo], %[rk], %[sh] offen lds\n\t"
  "s_add_u32 m0, %[lk], 1024\n\t"
  "s_add_u32 s44, %[so], 0\n\t"
  "buffer_load_dwordx4 %[vo], %[rk], s44 offen lds\n\t"
  "s_add_u32 m0, %[lk], 5120\n\t"
  "s_nop 0\n\t"
  "buffer_load_dwordx4 %[vo], %[rv], s44 offen lds\n\t"
  "s_add_u32 m0, %[lk], 2048\n\t"
  "s_add_u32 s44, %[so], 5120\n\t"
  "buffer_load_dwordx4 %[vo], %[rk], s44 offen lds\n\t"
  "s_add_u32 m0, %[lk], 6144\n\t"
  "s_nop 0\n\t"
  "buffer_load_dwordx4 %[vo], %[rv], s44 offen lds\n\t"
  "s_add_u32 m0, %[lk], 3072\n\t"
  "s_add_u32 s44, %[so], 10240\n\t"
  "buffer_load_dwordx4 %[vo], %[rk], s44 offen lds\n\t"
  "s_add_u32 m0, %[lk], 7168\n\t"
  "s_nop 0\n\t"
  "buffer_load_dwordx4 %[vo], %[rv], s44 offen lds\n\t"
  "s_add_u32 m0, %[lk], 4096\n\t"
  "s_add_u32 s44, %[so], 15360\n\t"
  "buffer_load_dwordx4 %[vo], %[rk], s44 offen lds\n\t"
  "s_add_u32 m0, %[lk], 8192\n\t"
  "s_nop 0\n\t"
  "buffer_load_dwordx4 %[vo], %[rv], s44 offen lds\n\t"
      :: [rk] "s"(rk), [rv] "s"(rv), [lk] "s"(lb + lkv), [so] "s"(tb * 20480u + (cb >> 5) * 256u), [sh] "s"(tb ? tb * 20480u + (cb >> 5) * 256u - 5120u : OOB),
         [vo] "v"((lane >> 4) * 1280 + (lane & 15) * 16) : "memory", "scc", "s44");
    const u32x4 taps = *(const u32x4*)(shk + d0 * 4);
    dval = ssmD[8 * (cb >> 1) + 2 * w + (cb & 1)];
    FENCE();
    __builtin_amdgcn_raw_buffer_store_b128(u32x4{0, 0, 0, 0}, srd, tid < 64 && cb < 128 ? OFF_ST2 + cb * 1024 + tid * 16 : OOB, 0, SC1);
    FENCE();
#pragma unroll
    for (int n = 0; n < 4; ++n) fa[n] = __builtin_amdgcn_raw_buffer_load_b128(rf, lane * 16 + n * 1024, (8 * (cb >> 1) + 2 * w + (cb & 1)) * 4096u, NT);
    u32x4 xr[5];
    const bool isx = cb < 128;
    const rsrc_t rx = mkrsrc(isx ? x : wmod, isx ? S_ * D_ * 2 : D_ * 3840 * 2);
#pragma unroll
    for (int n = 0; n < 5; ++n)
      xr[n] = __builtin_amdgcn_raw_buffer_load_b128(rx, isx ? (lane >> 3) * 1280 + (lane & 7) * 16 : vm,
                                                    isx ? cb * 20480u + (w & 1) * 10240u + (w >> 1) * 128u + n * 256u : sMod + (4 * n + w) * 61440u, NT);
    const u32x4 cr = __builtin_amdgcn_raw_buffer_load_b128(mkrsrc(cIn, isx ? 0 : D_ * 2), lane * 16, kq * 320u, 0);
    FENCE();
    STAMP(6);
    tag = load_tag(ws);
    if (w < 2) *(u32x4*)(smem + L_KV + tid * 16) = zero4;
    if (tid == 128) *(u32x4*)(smem + L_CNT) = zero4;
    FENCE();
    const f32x2 w0 = {bflo(taps[0]), bflo(taps[2])}, w1 = {bfhi(taps[0]), bfhi(taps[2])}, w2 = {bflo(taps[1]), bflo(taps[3])}, w3 = {bfhi(taps[1]), bfhi(taps[3])};
    asm volatile("s_waitcnt vmcnt(11)" ::: "memory");
    STAMP(7);
    u32 kk[20], vv[16];
    {
      unsigned la = lb + lkv + lane * 4;
      asm volatile("" : "+v"(la));
      const LAS u32* lp = (const LAS u32*)la;
#pragma unroll
      for (int i = 0; i < 12; ++i) kk[i] = lp[(i + 1) * 64];
#pragma unroll
      for (int i = 0; i < 8; ++i) vv[i] = lp[(20 + i) * 64];
      __builtin_amdgcn_sched_barrier(0);
#pragma unroll
      for (int i = 12; i < 19; ++i) kk[i] = lp[(i + 1) * 64];
#pragma unroll
      for (int i = 8; i < 16; ++i) vv[i] = lp[(20 + i) * 64];
      __builtin_amdgcn_sched_barrier(0);
    }
    f32x2 kf[19];
#pragma unroll
    for (int n = 0; n < 2; ++n) {
      f32x2 kv[8];
#pragma unroll
      for (int i = n ? 11 : 0; i < (n ? 19 : 11); ++i) kf[i] = f32x2{bflo(kk[i]), bfhi(kk[i])};
#pragma unroll
      for (int i = 0; i < 8; ++i) {
        f32x2 ks = kf[8 * n + i] * w3;
        ks = __builtin_elementwise_fma(kf[8 * n + i + 1], w2, ks);
        ks = __builtin_elementwise_fma(kf[8 * n + i + 2], w1, ks);
        ks = __builtin_elementwise_fma(kf[8 * n + i + 3], w0, ks);
        kv[i] = ks * f32x2{bflo(vv[8 * n + i]), bfhi(vv[8 * n + i])};
      }
      u32x4 o0, o1;
#pragma unroll
      for (int t = 0; t < 4; ++t) {
        o0[t] = pack_bf16(kv[7 - 2 * t][0], kv[6 - 2 * t][0]);
        o1[t] = pack_bf16(kv[7 - 2 * t][1], kv[6 - 2 * t][1]);
      }
      const unsigned off = OFF_KVT + (tb * 160 + 2 * (d0 >> 3)) * 128 + n * 64 + ((lane ^ (tb >> 1)) & 3) * 16;
      __builtin_amdgcn_raw_buffer_store_b128(o0, srd, off, 0, SC1);
      __builtin_amdgcn_raw_buffer_store_b128(o1, srd, off + 128, 0, SC1);
      FENCE();
    }
    STAMP(1);
    char* fcw = smem + L_FC + w * FC_CH;
    auto put = [&](unsigned u, u32x4 a, u32x2 h) {
      const u32 W[6] = {a[0], a[1], a[2], a[3], h[0], h[1]};
      u32 O[5];
#pragma unroll
      for (int i = 0; i < 5; ++i) O[i] = __builtin_amdgcn_alignbit(W[i + 1], W[i], 16);
      *(u32x4*)(fcw + 0 * FC_PITCH + u * 16) = a;
      *(u32x4*)(fcw + 1 * FC_PITCH + u * 16) = u32x4{O[0], O[1], O[2], O[3]};
      *(u32x4*)(fcw + 2 * FC_PITCH + u * 16) = u32x4{W[1], W[2], W[3], W[4]};
      *(u32x4*)(fcw + 3 * FC_PITCH + u * 16) = u32x4{O[1], O[2], O[3], O[4]};
    };
#pragma unroll
    for (int n = 0; n < 4; ++n) {
      u32x2 h;
#pragma unroll
      for (int e = 0; e < 2; ++e) h[e] = __builtin_amdgcn_update_dpp(n < 3 ? rfl(fa[n < 3 ? n + 1 : n][e]) : 0u, fa[n][e], 0x130, 0xf, 0xf, false);
      put(lane + 64 * n + 2, fa[n], h);
    }
    if (lane < 2) put(1 - lane, u32x4{0, 0, 0, 0}, lane == 0 ? u32x2{fa[0][0], fa[0][1]} : u32x2{0, 0});
    FENCE();
#pragma unroll
    for (int n = 0; n < 5; ++n) *(u32x4*)(smem + L_XR + n * 4096 + w * 1024 + lane * 16) = xr[n];
    if (w == 1) *(u32x4*)(smem + L_XR + 20480 + lane * 16) = cr;
    FENCE();
    asm volatile("s_waitcnt vmcnt(0) lgkmcnt(0)\n\ts_barrier" ::: "memory");
    __builtin_amdgcn_raw_buffer_store_b128(u32x4{(u32)tag, (u32)(tag >> 32), (u32)tag, (u32)(tag >> 32)}, srd, tid < 8 ? OFF_FA1 + cb * 128 + tid * 16 : OOB, 0, SC1);
    FENCE();
    STAMP(2);
    if (isx) {
      const unsigned g = lane >> 4, q = (lane >> 2) & 3, ch = 16 * (g >> 1) + (lane & 15);
      asm volatile(
  "s_nop 4\n\t"
  "s_add_u32 s44, %[w], 1\n\t"
  "s_and_b32 s44, s44, 2\n\t"
  "s_cbranch_scc0 Lxs%=\n\t"
  "ds_read_b64_tr_b16 v[96:97], %[vtx] offset:0\n\t"
  "ds_read_b64_tr_b16 v[98:99], %[vtx] offset:512\n\t"
  "ds_read_b64_tr_b16 v[100:101], %[vtx] offset:64\n\t"
  "ds_read_b64_tr_b16 v[102:103], %[vtx] offset:576\n\t"
  "ds_read_b64_tr_b16 v[104:105], %[vtx] offset:2048\n\t"
  "ds_read_b64_tr_b16 v[106:107], %[vtx] offset:2560\n\t"
  "ds_read_b64_tr_b16 v[108:109], %[vtx] offset:2112\n\t"
  "ds_read_b64_tr_b16 v[110:111], %[vtx] offset:2624\n\t"
  "ds_read_b64_tr_b16 v[112:113], %[vtx] offset:4096\n\t"
  "ds_read_b64_tr_b16 v[114:115], %[vtx] offset:4608\n\t"
  "ds_read_b64_tr_b16 v[116:117], %[vtx] offset:4160\n\t"
  "ds_read_b64_tr_b16 v[118:119], %[vtx] offset:4672\n\t"
  "ds_read_b64_tr_b16 v[120:121], %[vtx] offset:6144\n\t"
  "ds_read_b64_tr_b16 v[122:123], %[vtx] offset:6656\n\t"
  "ds_read_b64_tr_b16 v[124:125], %[vtx] offset:6208\n\t"
  "ds_read_b64_tr_b16 v[126:127], %[vtx] offset:6720\n\t"
  "s_add_u32 s44, %[sXo], 0\n\t"
  "s_waitcnt lgkmcnt(14)\n\t"
  "buffer_store_dwordx4 v[96:99], %[vxo], %[srd], s44 offen offset:0 sc1\n\t"
  "ds_read_b64_tr_b16 v[128:129], %[vtx] offset:8192\n\t"
  "ds_read_b64_tr_b16 v[130:131], %[vtx] offset:8704\n\t"
  "s_waitcnt lgkmcnt(14)\n\t"
  "buffer_store_dwordx4 v[100:103], %[vxo], %[srd], s44 offen offset:1024 sc1\n\t"
  "ds_read_b64_tr_b16 v[132:133], %[vtx] offset:8256\n\t"
  "ds_read_b64_tr_b16 v[134:135], %[vtx] offset:8768\n\t"
  "s_waitcnt lgkmcnt(14)\n\t"
  "buffer_store_dwordx4 v[104:107], %[vxo], %[srd], s44 offen offset:2048 sc1\n\t"
  "s_waitcnt lgkmcnt(12)\n\t"
  "buffer_store_dwordx4 v[108:111], %[vxo], %[srd], s44 offen offset:3072 sc1\n\t"
  "s_add_u32 s44, %[sXo], 4096\n\t"
  "s_waitcnt lgkmcnt(10)\n\t"
  "buffer_store_dwordx4 v[112:115], %[vxo], %[srd], s44 offen offset:0 sc1\n\t"
  "s_waitcnt lgkmcnt(8)\n\t"
  "buffer_store_dwordx4 v[116:119], %[vxo], %[srd], s44 offen offset:1024 sc1\n\t"
  "s_waitcnt lgkmcnt(6)\n\t"
  "buffer_store_dwordx4 v[120:123], %[vxo], %[srd], s44 offen offset:2048 sc1\n\t"
  "s_waitcnt lgkmcnt(4)\n\t"
  "buffer_store_dwordx4 v[124:127], %[vxo], %[srd], s44 offen offset:3072 sc1\n\t"
  "s_add_u32 s44, %[sXo], 8192\n\t"
  "s_waitcnt lgkmcnt(2)\n\t"
  "buffer_store_dwordx4 v[128:131], %[vxo], %[srd], s44 offen offset:0 sc1\n\t"
  "s_waitcnt lgkmcnt(0)\n\t"
  "buffer_store_dwordx4 v[132:135], %[vxo], %[srd], s44 offen offset:1024 sc1\n\t"
  "s_branch Lxe%=\n\t"
  "Lxs%=:\n\t"
  "ds_read_b128 v[96:99], %[vsx] offset:0\n\t"
  "ds_read_b128 v[100:103], %[vsx] offset:2048\n\t"
  "ds_read_b128 v[104:107], %[vsx] offset:4096\n\t"
  "ds_read_b128 v[108:111], %[vsx] offset:6144\n\t"
  "ds_read_b128 v[112:115], %[vsx] offset:8192\n\t"
  "ds_read_b128 v[116:119], %[vsx] offset:10240\n\t"
  "ds_read_b128 v[120:123], %[vsx] offset:12288\n\t"
  "ds_read_b128 v[124:127], %[vsx] offset:14336\n\t"
  "ds_read_b128 v[128:131], %[vsx] offset:16384\n\t"
  "ds_read_b128 v[132:135], %[vsx] offset:18432\n\t"
  "v_mov_b32 v184, 0\n\t"
  "v_mov_b32 v185, 0\n\t"
  "v_mov_b32 v186, 0x3f803f80\n\t"
  "s_waitcnt lgkmcnt(9)\n\t"
  "v_dot2c_f32_bf16 v184, v96, v186\n\t"
  "v_dot2c_f32_bf16 v185, v96, v96\n\t"
  "v_dot2c_f32_bf16 v184, v97, v186\n\t"
  "v_dot2c_f32_bf16 v185, v97, v97\n\t"
  "v_dot2c_f32_bf16 v184, v98, v186\n\t"
  "v_dot2c_f32_bf16 v185, v98, v98\n\t"
  "v_dot2c_f32_bf16 v184, v99, v186\n\t"
  "v_dot2c_f32_bf16 v185, v99, v99\n\t"
  "s_waitcnt lgkmcnt(8)\n\t"
  "v_dot2c_f32_bf16 v184, v100, v186\n\t"
  "v_dot2c_f32_bf16 v185, v100, v100\n\t"
  "v_dot2c_f32_bf16 v184, v101, v186\n\t"
  "v_dot2c_f32_bf16 v185, v101, v101\n\t"
  "v_dot2c_f32_bf16 v184, v102, v186\n\t"
  "v_dot2c_f32_bf16 v185, v102, v102\n\t"
  "v_dot2c_f32_bf16 v184, v103, v186\n\t"
  "v_dot2c_f32_bf16 v185, v103, v103\n\t"
  "s_waitcnt lgkmcnt(7)\n\t"
  "v_dot2c_f32_bf16 v184, v104, v186\n\t"
  "v_dot2c_f32_bf16 v185, v104, v104\n\t"
  "v_dot2c_f32_bf16 v184, v105, v186\n\t"
  "v_dot2c_f32_bf16 v185, v105, v105\n\t"
  "v_dot2c_f32_bf16 v184, v106, v186\n\t"
  "v_dot2c_f32_bf16 v185, v106, v106\n\t"
  "v_dot2c_f32_bf16 v184, v107, v186\n\t"
  "v_dot2c_f32_bf16 v185, v107, v107\n\t"
  "s_waitcnt lgkmcnt(6)\n\t"
  "v_dot2c_f32_bf16 v184, v108, v186\n\t"
  "v_dot2c_f32_bf16 v185, v108, v108\n\t"
  "v_dot2c_f32_bf16 v184, v109, v186\n\t"
  "v_dot2c_f32_bf16 v185, v109, v109\n\t"
  "v_dot2c_f32_bf16 v184, v110, v186\n\t"
  "v_dot2c_f32_bf16 v185, v110, v110\n\t"
  "v_dot2c_f32_bf16 v184, v111, v186\n\t"
  "v_dot2c_f32_bf16 v185, v111, v111\n\t"
  "s_waitcnt lgkmcnt(5)\n\t"
  "v_dot2c_f32_bf16 v184, v112, v186\n\t"
  "v_dot2c_f32_bf16 v185, v112, v112\n\t"
  "v_dot2c_f32_bf16 v184, v113, v186\n\t"
  "v_dot2c_f32_bf16 v185, v113, v113\n\t"
  "v_dot2c_f32_bf16 v184, v114, v186\n\t"
  "v_dot2c_f32_bf16 v185, v114, v114\n\t"
  "v_dot2c_f32_bf16 v184, v115, v186\n\t"
  "v_dot2c_f32_bf16 v185, v115, v115\n\t"
  "s_waitcnt lgkmcnt(4)\n\t"
  "v_dot2c_f32_bf16 v184, v116, v186\n\t"
  "v_dot2c_f32_bf16 v185, v116, v116\n\t"
  "v_dot2c_f32_bf16 v184, v117, v186\n\t"
  "v_dot2c_f32_bf16 v185, v117, v117\n\t"
  "v_dot2c_f32_bf16 v184, v118, v186\n\t"
  "v_dot2c_f32_bf16 v185, v118, v118\n\t"
  "v_dot2c_f32_bf16 v184, v119, v186\n\t"
  "v_dot2c_f32_bf16 v185, v119, v119\n\t"
  "s_waitcnt lgkmcnt(3)\n\t"
  "v_dot2c_f32_bf16 v184, v120, v186\n\t"
  "v_dot2c_f32_bf16 v185, v120, v120\n\t"
  "v_dot2c_f32_bf16 v184, v121, v186\n\t"
  "v_dot2c_f32_bf16 v185, v121, v121\n\t"
  "v_dot2c_f32_bf16 v184, v122, v186\n\t"
  "v_dot2c_f32_bf16 v185, v122, v122\n\t"
  "v_dot2c_f32_bf16 v184, v123, v186\n\t"
  "v_dot2c_f32_bf16 v185, v123, v123\n\t"
  "s_waitcnt lgkmcnt(2)\n\t"
  "v_dot2c_f32_bf16 v184, v124, v186\n\t"
  "v_dot2c_f32_bf16 v185, v124, v124\n\t"
  "v_dot2c_f32_bf16 v184, v125, v186\n\t"
  "v_dot2c_f32_bf16 v185, v125, v125\n\t"
  "v_dot2c_f32_bf16 v184, v126, v186\n\t"
  "v_dot2c_f32_bf16 v185, v126, v126\n\t"
  "v_dot2c_f32_bf16 v184, v127, v186\n\t"
  "v_dot2c_f32_bf16 v185, v127, v127\n\t"
  "s_waitcnt lgkmcnt(1)\n\t"
  "v_dot2c_f32_bf16 v184, v128, v186\n\t"
  "v_dot2c_f32_bf16 v185, v128, v128\n\t"
  "v_dot2c_f32_bf16 v184, v129, v186\n\t"
  "v_dot2c_f32_bf16 v185, v129, v129\n\t"
  "v_dot2c_f32_bf16 v184, v130, v186\n\t"
  "v_dot2c_f32_bf16 v185, v130, v130\n\t"
  "v_dot2c_f32_bf16 v184, v131, v186\n\t"
  "v_dot2c_f32_bf16 v185, v131, v131\n\t"
  "s_waitcnt lgkmcnt(0)\n\t"
  "v_dot2c_f32_bf16 v184, v132, v186\n\t"
  "v_dot2c_f32_bf16 v185, v132, v132\n\t"
  "v_dot2c_f32_bf16 v184, v133, v186\n\t"
  "v_dot2c_f32_bf16 v185, v133, v133\n\t"
  "v_dot2c_f32_bf16 v184, v134, v186\n\t"
  "v_dot2c_f32_bf16 v185, v134, v134\n\t"
  "v_dot2c_f32_bf16 v184, v135, v186\n\t"
  "v_dot2c_f32_bf16 v185, v135, v135\n\t"
  "s_nop 1\n\t"
  "v_add_f32_dpp v184, v184, v184 quad_perm:[1,0,3,2] row_mask:0xf bank_mask:0xf\n\t"
  "v_add_f32_dpp v185, v185, v185 quad_perm:[1,0,3,2] row_mask:0xf bank_mask:0xf\n\t"
  "s_nop 0\n\t"
  "v_add_f32_dpp v184, v184, v184 quad_perm:[2,3,0,1] row_mask:0xf bank_mask:0xf\n\t"
  "v_add_f32_dpp v185, v185, v185 quad_perm:[2,3,0,1] row_mask:0xf bank_mask:0xf\n\t"
  "s_nop 0\n\t"
  "v_add_f32_dpp v184, v184, v184 row_half_mirror row_mask:0xf bank_mask:0xf\n\t"
  "v_add_f32_dpp v185, v185, v185 row_half_mirror row_mask:0xf bank_mask:0xf\n\t"
  "v_mul_f32 v184, 0xbacccccd, v184\n\t"
  "v_mul_f32 v185, 0x3acccccd, v185\n\t"
  "v_fma_f32 v185, -v184, v184, v185\n\t"
  "v_max_f32 v185, 0, v185\n\t"
  "v_cvt_pk_bf16_f32 v185, 0, v185\n\t"
  "v_cvt_pk_bf16_f32 v184, 0, v184\n\t"
  "v_add_f32 v185, 0x3727c5ac, v185\n\t"
  "v_cvt_pk_bf16_f32 v185, 0, v185\n\t"
  "v_sqrt_f32 v185, v185\n\t"
  "s_nop 0\n\t"
  "v_cvt_pk_bf16_f32 v185, 0, v185\n\t"
  "v_rcp_f32 v188, v185\n\t"
  "s_nop 0\n\t"
  "v_mul_f32 v189, v184, v188\n\t"
  "s_nop 1\n\t"
  "v_mov_b32_dpp v188, v189 quad_perm:[0,1,2,3] row_mask:0xf bank_mask:0xa\n\t"
  "buffer_store_dword v188, %[vso], %[srd], %[sSo] offen sc1\n\t"
  "Lxe%=:\n\t"
  "s_nop 1\n\t"
        :: [srd] "s"(srd), [w] "s"(w), [sSo] "s"(OFF_ST1 + cb * 128u), [sXo] "s"(OFF_XT + cb * 20480u + (w - 1) * 10240u),
           [vtx] "v"(lb + L_XR + (w - 1) * 10240 + (8 * (g & 1) + q) * 128 + (g >> 1) * 32 + (lane & 3) * 8), [vsx] "v"(lb + L_XR + (w & 1) * 1024 + lane * 16),
           [vso] "v"((lane & 3) == 0 ? (((w & 1) * 4 + g) ^ stkey(cb)) * 16 + ((lane >> 3) & 1) * 4 + (lane & 4) * 2 : OOB), [vxo] "v"((2 * (ch >> 3) + (ch & 1)) * 128 + ((((ch ^ cb) >> 1) & 3) + 4 * (g & 1)) * 16)
        : "memory", "vcc", "scc",
"s44", "v96", "v97", "v98", "v99", "v100", "v101", "v102", "v103", "v104", "v105", "v106", "v107", "v108", "v109", "v110", "v111", "v112", "v113", "v114", "v115", "v116", "v117", "v118", "v119", "v120", "v121", "v122", "v123", "v124", "v125", "v126", "v127", "v128", "v129", "v130", "v131", "v132", "v133", "v134", "v135", "v184", "v185", "v186", "v188", "v189"
      );
    } else if (w == 1) GEMV_UNIT(lb + L_XR, 0u);
    STAMP(5);
  } else {
    const bool is1 = role == 1;
    const unsigned lu = lb + L_UN + w * UN_SZ;
    const unsigned g = lane >> 4, q = (lane >> 2) & 3, p4 = lane & 3;
    u32x4 raw[16];
    if (W_DELAY) __builtin_amdgcn_s_sleep(W_DELAY);
    if (is1) {
      const rsrc_t rg = mkrsrc(wg, D_ * F_ * 2 * DBG_W), ru = mkrsrc(wu, D_ * F_ * 2 * DBG_W);
      const unsigned rw = 5 * w + lane / 12, ck = lane % 12;
      const unsigned vo = lane < 60 ? rw * (F_ * 2) + ck * 16 : OOB, so = (160 * (w1 & 3) * F_ + 96 * (w1 >> 2)) * 2;
#pragma unroll
      for (int n = 0; n < 8; ++n) {
        raw[2 * n] = __builtin_amdgcn_raw_buffer_load_b128(rg, vo, so + n * 20 * F_ * 2, NT);
        raw[2 * n + 1] = __builtin_amdgcn_raw_buffer_load_b128(ru, vo, so + n * 20 * F_ * 2, NT);
      }
      FENCE();
      tag = load_tag(ws);
      char* o = smem + (lane < 60 ? rw * RP1 + ck * 32 : 384);
      u32x4 fr[15];
#pragma unroll
      for (int h = 0; h < 2; ++h) {
#pragma unroll
        for (int n = h ? 5 : 0; n < (h ? 8 : 5); ++n) {
          *(u32x4*)(o + n * 20 * RP1) = u32x4{raw[2 * n][0], raw[2 * n + 1][0], raw[2 * n][1], raw[2 * n + 1][1]};
          *(u32x4*)(o + n * 20 * RP1 + (lane < 60 ? 16 : 0)) = u32x4{raw[2 * n][2], raw[2 * n + 1][2], raw[2 * n][3], raw[2 * n + 1][3]};
        }
        BAR();
#pragma unroll
        for (int n = h ? 9 : 0; n < (h ? 15 : 9); ++n) {
          const int ks = n / 3, i = n % 3;
          const char* a = smem + (32 * ks + 4 * g + q) * RP1 + (12 * w + (p4 < 3 ? 4 * p4 + i : 4 * i + 3)) * 8;
          const u32x2 lo = tr_read(a), hi = tr_read(a + 16 * RP1);
          fr[n] = u32x4{lo[0], lo[1], hi[0], hi[1]};
        }
        FENCE();
#pragma unroll
        for (int n = h ? 9 : 0; n < (h ? 15 : 9); ++n)
          __builtin_amdgcn_raw_buffer_store_b128(fr[n], srd, lane * 16 + (n & 3) * 1024, OFF_W1F + (((w1 & ~3u) + w) * 60 + 15 * (w1 & 3) + (n & ~3)) * 1024, SC1);
        FENCE();
      }
#pragma unroll
      for (int n = 0; n < 15; ++n) raw[n] = fr[n];
      STAMP(6);
    }
    if (ui < 240)
      asm volatile(
  "s_nop 4\n\t"
  "s_add_u32 m0, %[lu], 20480\n\t"
  "s_nop 0\n\t"
  "buffer_load_dwordx4 %[vl16], %[rc], %[sC] offen lds\n\t"
  "s_add_u32 m0, %[lu], 0\n\t"
  "s_add_u32 s44, %[sMod], 0\n\t"
  "buffer_load_dwordx4 %[vm], %[rm], s44 offen lds\n\t"
  "s_add_u32 m0, %[lu], 1024\n\t"
  "s_add_u32 s44, %[sMod], 61440\n\t"
  "buffer_load_dwordx4 %[vm], %[rm], s44 offen lds\n\t"
  "s_add_u32 m0, %[lu], 2048\n\t"
  "s_add_u32 s44, %[sMod], 122880\n\t"
  "buffer_load_dwordx4 %[vm], %[rm], s44 offen lds\n\t"
  "s_add_u32 m0, %[lu], 3072\n\t"
  "s_add_u32 s44, %[sMod], 184320\n\t"
  "buffer_load_dwordx4 %[vm], %[rm], s44 offen lds\n\t"
  "s_add_u32 m0, %[lu], 4096\n\t"
  "s_add_u32 s44, %[sMod], 245760\n\t"
  "buffer_load_dwordx4 %[vm], %[rm], s44 offen lds\n\t"
  "s_add_u32 m0, %[lu], 5120\n\t"
  "s_add_u32 s44, %[sMod], 307200\n\t"
  "buffer_load_dwordx4 %[vm], %[rm], s44 offen lds\n\t"
  "s_add_u32 m0, %[lu], 6144\n\t"
  "s_add_u32 s44, %[sMod], 368640\n\t"
  "buffer_load_dwordx4 %[vm], %[rm], s44 offen lds\n\t"
  "s_add_u32 m0, %[lu], 7168\n\t"
  "s_add_u32 s44, %[sMod], 430080\n\t"
  "buffer_load_dwordx4 %[vm], %[rm], s44 offen lds\n\t"
  "s_add_u32 m0, %[lu], 8192\n\t"
  "s_add_u32 s44, %[sMod], 491520\n\t"
  "buffer_load_dwordx4 %[vm], %[rm], s44 offen lds\n\t"
  "s_add_u32 m0, %[lu], 9216\n\t"
  "s_add_u32 s44, %[sMod], 552960\n\t"
  "buffer_load_dwordx4 %[vm], %[rm], s44 offen lds\n\t"
  "s_add_u32 m0, %[lu], 10240\n\t"
  "s_add_u32 s44, %[sMod], 614400\n\t"
  "buffer_load_dwordx4 %[vm], %[rm], s44 offen lds\n\t"
  "s_add_u32 m0, %[lu], 11264\n\t"
  "s_add_u32 s44, %[sMod], 675840\n\t"
  "buffer_load_dwordx4 %[vm], %[rm], s44 offen lds\n\t"
  "s_add_u32 m0, %[lu], 12288\n\t"
  "s_add_u32 s44, %[sMod], 737280\n\t"
  "buffer_load_dwordx4 %[vm], %[rm], s44 offen lds\n\t"
  "s_add_u32 m0, %[lu], 13312\n\t"
  "s_add_u32 s44, %[sMod], 798720\n\t"
  "buffer_load_dwordx4 %[vm], %[rm], s44 offen lds\n\t"
  "s_add_u32 m0, %[lu], 14336\n\t"
  "s_add_u32 s44, %[sMod], 860160\n\t"
  "buffer_load_dwordx4 %[vm], %[rm], s44 offen lds\n\t"
  "s_add_u32 m0, %[lu], 15360\n\t"
  "s_add_u32 s44, %[sMod], 921600\n\t"
  "buffer_load_dwordx4 %[vm], %[rm], s44 offen lds\n\t"
  "s_add_u32 m0, %[lu], 16384\n\t"
  "s_add_u32 s44, %[sMod], 983040\n\t"
  "buffer_load_dwordx4 %[vm], %[rm], s44 offen lds\n\t"
  "s_add_u32 m0, %[lu], 17408\n\t"
  "s_add_u32 s44, %[sMod], 1044480\n\t"
  "buffer_load_dwordx4 %[vm], %[rm], s44 offen lds\n\t"
  "s_add_u32 m0, %[lu], 18432\n\t"
  "s_add_u32 s44, %[sMod], 1105920\n\t"
  "buffer_load_dwordx4 %[vm], %[rm], s44 offen lds\n\t"
  "s_add_u32 m0, %[lu], 19456\n\t"
  "s_add_u32 s44, %[sMod], 1167360\n\t"
  "buffer_load_dwordx4 %[vm], %[rm], s44 offen lds\n\t"
        :: [rm] "s"(mkrsrc(wmod, D_ * 3840 * 2 * DBG_M)), [rc] "s"(mkrsrc(cIn, D_ * 2)), [lu] "s"(lu), [sC] "s"(kq * 320u), [sMod] "s"(sMod),
           [vl16] "v"(lane * 16), [vm] "v"(vm)
        : "memory", "scc", "s44");
    if (is1) {
      if (ui < 240) asm volatile("s_waitcnt vmcnt(21)" ::: "memory"); else asm volatile("s_waitcnt vmcnt(0)" ::: "memory");
      asm volatile("s_barrier" ::: "memory");
      if (w == 3) put_flag(srd, OFF_FW1 + w1 * 128, lane, tag);
      STAMP(7);
    } else tag = load_tag(ws);
    if (ui < 240) {
      GEMV_UNIT(lu, 28u);
      STAMP(1);
      asm volatile("s_waitcnt vmcnt(0)" ::: "memory");
      put_flag(srd, OFF_FA2 + (kq * 60 + cgp) * 128, lane, tag);
      STAMP(2);
    }
    if (is1) {
    } else {
      const rsrc_t rd = mkrsrc(wd, D_ * F_ * 2 * DBG_W);
      const unsigned r8 = 24 * w + (lane >> 3), so = (96 * (w2 >> 1) * D_ + 320 * (w2 & 1)) * 2;
#pragma unroll
      for (int n = 0; n < 15; ++n) raw[n] = __builtin_amdgcn_raw_buffer_load_b128(rd, r8 * (D_ * 2) + (lane & 7) * 16 + (n % 5) * 128, so + (n / 5) * 8 * D_ * 2, NT);
      FENCE();
#pragma unroll
      for (int n = 0; n < 15; ++n) *(u32x4*)(smem + r8 * RP2 + (lane & 7) * 16 + (n % 5) * 128 + (n / 5) * 8 * RP2) = raw[n];
      if (w2 < 10 && w == 0) {
        poll_flags<1, 4>(srd, OFF_FA2 + ((lane & 3) * 60 + 40 + w2) * 128, tag);
        const float sc = rbf(1.f + modval(srd, 4 * 640 + lane + 64 * w2));
        const u32 rv = __float_as_uint(sc == 0.f ? 0x1p40f : 1.f / sc);
        __builtin_amdgcn_raw_buffer_store_b32(rv, srd, lane * 4, OFF_RT + w2 * 256, SC1);
        __builtin_amdgcn_raw_buffer_store_b32(rv, srd, lane * 48, OFF_RL + (w2 < 2 ? w2 : w2 + 2) * 4, SC1);
      }
      STAMP(7);
      BAR();
#pragma unroll
      for (int n = 0; n < 15; ++n) {
        const int i = n % 5;
        const char* a = smem + (32 * (n / 5) + 8 * g + q) * RP2 + (80 * w + (i < 4 ? 32 * (i >> 1) + 8 * p4 + 4 * (i & 1) : 64 + 4 * p4)) * 2;
        const u32x2 lo = tr_read(a), hi = tr_read(a + 4 * RP2);
        raw[n] = u32x4{lo[0], lo[1], hi[0], hi[1]};
      }
      FENCE();
#pragma unroll
      for (int n = 0; n < 15; ++n)
        __builtin_amdgcn_raw_buffer_store_b128(raw[n], srd, lane * 16 + (n & 3) * 1024, OFF_W2F + ((4 * (w2 & 1) + w) * 240 + 15 * (w2 >> 1) + (n & ~3)) * 1024, SC1);
    }
    FENCE();
    if (is1) {
      poll_flags<1, 4>(srd, OFF_FW1 + (j * 4 + (lane & 3)) * 128, tag);
      asm volatile(
  "s_nop 4\n\t"
  "s_add_u32 s44, %[sW1], 0\n\t"
  "buffer_load_dwordx4 a[0:3], %[vl16], %[srd], s44 offen offset:0 sc1\n\t"
  "buffer_load_dwordx4 a[4:7], %[vl16], %[srd], s44 offen offset:1024 sc1\n\t"
  "buffer_load_dwordx4 a[8:11], %[vl16], %[srd], s44 offen offset:2048 sc1\n\t"
  "buffer_load_dwordx4 a[12:15], %[vl16], %[srd], s44 offen offset:3072 sc1\n\t"
  "s_add_u32 s44, %[sW1], 4096\n\t"
  "buffer_load_dwordx4 a[16:19], %[vl16], %[srd], s44 offen offset:0 sc1\n\t"
  "buffer_load_dwordx4 a[20:23], %[vl16], %[srd], s44 offen offset:1024 sc1\n\t"
  "buffer_load_dwordx4 a[24:27], %[vl16], %[srd], s44 offen offset:2048 sc1\n\t"
  "buffer_load_dwordx4 a[28:31], %[vl16], %[srd], s44 offen offset:3072 sc1\n\t"
  "s_add_u32 s44, %[sW1], 8192\n\t"
  "buffer_load_dwordx4 a[32:35], %[vl16], %[srd], s44 offen offset:0 sc1\n\t"
  "buffer_load_dwordx4 a[36:39], %[vl16], %[srd], s44 offen offset:1024 sc1\n\t"
  "buffer_load_dwordx4 a[40:43], %[vl16], %[srd], s44 offen offset:2048 sc1\n\t"
  "buffer_load_dwordx4 a[44:47], %[vl16], %[srd], s44 offen offset:3072 sc1\n\t"
  "s_add_u32 s44, %[sW1], 12288\n\t"
  "buffer_load_dwordx4 a[48:51], %[vl16], %[srd], s44 offen offset:0 sc1\n\t"
  "buffer_load_dwordx4 a[52:55], %[vl16], %[srd], s44 offen offset:1024 sc1\n\t"
  "buffer_load_dwordx4 a[56:59], %[vl16], %[srd], s44 offen offset:2048 sc1\n\t"
  "buffer_load_dwordx4 a[60:63], %[vl16], %[srd], s44 offen offset:3072 sc1\n\t"
  "s_add_u32 s44, %[sW1], 16384\n\t"
  "buffer_load_dwordx4 a[64:67], %[vl16], %[srd], s44 offen offset:0 sc1\n\t"
  "buffer_load_dwordx4 a[68:71], %[vl16], %[srd], s44 offen offset:1024 sc1\n\t"
  "buffer_load_dwordx4 a[72:75], %[vl16], %[srd], s44 offen offset:2048 sc1\n\t"
  "buffer_load_dwordx4 a[76:79], %[vl16], %[srd], s44 offen offset:3072 sc1\n\t"
  "s_add_u32 s44, %[sW1], 20480\n\t"
  "buffer_load_dwordx4 a[80:83], %[vl16], %[srd], s44 offen offset:0 sc1\n\t"
  "buffer_load_dwordx4 a[84:87], %[vl16], %[srd], s44 offen offset:1024 sc1\n\t"
  "buffer_load_dwordx4 a[88:91], %[vl16], %[srd], s44 offen offset:2048 sc1\n\t"
  "buffer_load_dwordx4 a[92:95], %[vl16], %[srd], s44 offen offset:3072 sc1\n\t"
  "s_add_u32 s44, %[sW1], 24576\n\t"
  "buffer_load_dwordx4 a[96:99], %[vl16], %[srd], s44 offen offset:0 sc1\n\t"
  "buffer_load_dwordx4 a[100:103], %[vl16], %[srd], s44 offen offset:1024 sc1\n\t"
  "buffer_load_dwordx4 a[104:107], %[vl16], %[srd], s44 offen offset:2048 sc1\n\t"
  "buffer_load_dwordx4 a[108:111], %[vl16], %[srd], s44 offen offset:3072 sc1\n\t"
  "s_add_u32 s44, %[sW1], 28672\n\t"
  "buffer_load_dwordx4 a[112:115], %[vl16], %[srd], s44 offen offset:0 sc1\n\t"
  "buffer_load_dwordx4 a[116:119], %[vl16], %[srd], s44 offen offset:1024 sc1\n\t"
  "buffer_load_dwordx4 a[120:123], %[vl16], %[srd], s44 offen offset:2048 sc1\n\t"
  "buffer_load_dwordx4 a[124:127], %[vl16], %[srd], s44 offen offset:3072 sc1\n\t"
  "s_add_u32 s44, %[sW1], 32768\n\t"
  "buffer_load_dwordx4 a[128:131], %[vl16], %[srd], s44 offen offset:0 sc1\n\t"
  "buffer_load_dwordx4 a[132:135], %[vl16], %[srd], s44 offen offset:1024 sc1\n\t"
  "buffer_load_dwordx4 a[136:139], %[vl16], %[srd], s44 offen offset:2048 sc1\n\t"
  "buffer_load_dwordx4 a[140:143], %[vl16], %[srd], s44 offen offset:3072 sc1\n\t"
  "s_add_u32 s44, %[sW1], 36864\n\t"
  "buffer_load_dwordx4 a[144:147], %[vl16], %[srd], s44 offen offset:0 sc1\n\t"
  "buffer_load_dwordx4 a[148:151], %[vl16], %[srd], s44 offen offset:1024 sc1\n\t"
  "buffer_load_dwordx4 a[152:155], %[vl16], %[srd], s44 offen offset:2048 sc1\n\t"
  "buffer_load_dwordx4 a[156:159], %[vl16], %[srd], s44 offen offset:3072 sc1\n\t"
  "s_add_u32 s44, %[sW1], 40960\n\t"
  "buffer_load_dwordx4 a[160:163], %[vl16], %[srd], s44 offen offset:0 sc1\n\t"
  "buffer_load_dwordx4 a[164:167], %[vl16], %[srd], s44 offen offset:1024 sc1\n\t"
  "buffer_load_dwordx4 a[168:171], %[vl16], %[srd], s44 offen offset:2048 sc1\n\t"
  "buffer_load_dwordx4 a[172:175], %[vl16], %[srd], s44 offen offset:3072 sc1\n\t"
  "s_add_u32 s44, %[sW1], 45056\n\t"
  "buffer_load_dwordx4 a[176:179], %[vl16], %[srd], s44 offen offset:0 sc1\n\t"
  "buffer_load_dwordx4 a[180:183], %[vl16], %[srd], s44 offen offset:1024 sc1\n\t"
  "buffer_load_dwordx4 a[184:187], %[vl16], %[srd], s44 offen offset:2048 sc1\n\t"
  "buffer_load_dwordx4 a[188:191], %[vl16], %[srd], s44 offen offset:3072 sc1\n\t"
  "s_add_u32 s44, %[sW1], 49152\n\t"
  "buffer_load_dwordx4 a[192:195], %[vl16], %[srd], s44 offen offset:0 sc1\n\t"
  "buffer_load_dwordx4 a[196:199], %[vl16], %[srd], s44 offen offset:1024 sc1\n\t"
  "buffer_load_dwordx4 a[200:203], %[vl16], %[srd], s44 offen offset:2048 sc1\n\t"
  "buffer_load_dwordx4 a[204:207], %[vl16], %[srd], s44 offen offset:3072 sc1\n\t"
        :: [srd] "s"(srd), [sW1] "s"(OFF_W1F + (j * 4 + w) * 61440u), [vl16] "v"(lane * 16) : "memory", "scc",
"a0", "a1", "a2", "a3", "a4", "a5", "a6", "a7", "a8", "a9", "a10", "a11", "a12", "a13", "a14", "a15", "a16", "a17", "a18", "a19", "a20", "a21", "a22", "a23", "a24", "a25", "a26", "a27", "a28", "a29", "a30", "a31", "a32", "a33", "a34", "a35", "a36", "a37", "a38", "a39", "a40", "a41", "a42", "a43", "a44", "a45", "a46", "a47", "a48", "a49", "a50", "a51", "a52", "a53", "a54", "a55", "a56", "a57", "a58", "a59", "a60", "a61", "a62", "a63", "a64", "a65", "a66", "a67", "a68", "a69", "a70", "a71", "a72", "a73", "a74", "a75", "a76", "a77", "a78", "a79", "a80", "a81", "a82", "a83", "a84", "a85", "a86", "a87", "a88", "a89", "a90", "a91", "a92", "a93", "a94", "a95", "a96", "a97", "a98", "a99", "a100", "a101", "a102", "a103", "a104", "a105", "a106", "a107", "a108", "a109", "a110", "a111", "a112", "a113", "a114", "a115", "a116", "a117", "a118", "a119", "a120", "a121", "a122", "a123", "a124", "a125", "a126", "a127", "a128", "a129", "a130", "a131", "a132", "a133", "a134", "a135", "a136", "a137", "a138", "a139", "a140", "a141", "a142", "a143", "a144", "a145", "a146", "a147", "a148", "a149", "a150", "a151", "a152", "a153", "a154", "a155", "a156", "a157", "a158", "a159", "a160", "a161", "a162", "a163", "a164", "a165", "a166", "a167", "a168", "a169", "a170", "a171", "a172", "a173", "a174", "a175", "a176", "a177", "a178", "a179", "a180", "a181", "a182", "a183", "a184", "a185", "a186", "a187", "a188", "a189", "a190", "a191", "a192", "a193", "a194", "a195", "a196", "a197", "a198", "a199", "a200", "a201", "a202", "a203", "a204", "a205", "a206", "a207", "s44"
      );
      wdone = 7;
      if (w > 1 || (w == 1 && w1 < 4)) {
        const unsigned c0 = (6 - w) * 640 + 160 * (w1 & 3);
        poll_flags<1, 4>(srd, OFF_FA2 + ((lane & 3) * 60 + ((c0 + min(64 * ((lane >> 2) & 3), 159u)) >> 6)) * 128, tag);
        f32x4 m[4];
#pragma unroll
        for (int k = 0; k < 4; ++k) m[k] = __builtin_bit_cast(f32x4, __builtin_amdgcn_raw_buffer_load_b128(srd, lane < 40 ? lane * 16 : OOB, OFF_MODP + k * 15360 + c0 * 4, SC1));
        f32x4 v = (m[0] + m[1]) + (m[2] + m[3]);
#pragma unroll
        for (int e = 0; e < 4; ++e) v[e] = rbf(v[e]);
        if (w == 1) __builtin_amdgcn_raw_buffer_store_b128(__builtin_bit_cast(u32x4, v), srd, lane < 40 ? lane * 16 : OOB, OFF_GB + 640 * w1, SC1);
        else {
          const float o = w == 2 ? 1.f : 0.f;
          *(u32x2*)(smem + L_WS + (w - 2) * 1024 + lane * 8) = u32x2{pack_bf16(o + v[0], o + v[1]), pack_bf16(o + v[2], o + v[3])};
        }
      }
      STAMP(5);
      BAR();
      STAMP(3);
      f32x4 aU[3], aB[3];
#pragma unroll
      for (int i = 0; i < 3; ++i) { aU[i] = f32x4{0, 0, 0, 0}; aB[i] = f32x4{0, 0, 0, 0}; }
#pragma unroll
      for (int ks = 0; ks < 5; ++ks) {
        const char* t = smem + L_WS + (32 * ks + 4 * g) * 2;
        const u32x2 s0 = *(const u32x2*)t, s1 = *(const u32x2*)(t + 32), b0 = *(const u32x2*)(t + 1024), b1 = *(const u32x2*)(t + 1056);
#pragma unroll
        for (int i = 0; i < 3; ++i) {
          mfma_v(aU[i], u32x4{s0[0], s0[1], s1[0], s1[1]}, raw[3 * ks + i]);
          mfma_v(aB[i], u32x4{b0[0], b0[1], b1[0], b1[1]}, raw[3 * ks + i]);
        }
      }
      asm volatile("s_nop 7\n\ts_nop 1" : "+v"(aU[0]), "+v"(aU[1]), "+v"(aU[2]), "+v"(aB[0]), "+v"(aB[1]), "+v"(aB[2]));
      const float u = g == 0 ? aU[0][0] : g == 1 ? aU[1][0] : aU[2][0], bb = g == 0 ? aB[0][0] : g == 1 ? aB[1][0] : aB[2][0];
      const float uh = rbf(u), bh = rbf(bb);
      __builtin_amdgcn_raw_buffer_store_b64(u32x2{pack_bf16(uh, u - uh), pack_bf16(bh, bb - bh)}, srd,
                                            g < 3 ? OFF_UB + ((((w1 & ~3u) + w) * 3 + g) * 64 + (w1 & 3) * 16 + (lane & 15)) * 8 : OOB, 0, SC1);
    }
  }
  if (role == 0) {
    const unsigned d = 8 * (cb >> 1) + 2 * w + (cb & 1);
    const unsigned r = lane & 15, g = lane >> 4;
    const unsigned pp = 25 + r - 8 * g;
    const unsigned va = lb + L_FC + w * FC_CH + (pp & 3) * FC_PITCH + (pp & ~3u) * 2;
    unsigned vb[4];
#pragma unroll
    for (int k = 0; k < 4; ++k) { const unsigned G = 14 - 4 * k + 2 * r + g; vb[k] = lb + L_KV + G * 64 + ((w ^ (G >> 2)) & 3) * 16; }
    const unsigned gx = (2 * r + (g >> 1)) * 64 + ((w ^ (r >> 1)) & 3) * 16;
    const unsigned vx = lb + L_X + gx + (g & 1) * 8, vs = lb + L_ST + r * 128 + ((2 * g) ^ stkey(r)) * 16;
    const unsigned to = 256 * (g & 1) + 16 * r + 8 * (g >> 1);
    const unsigned vo = (to >> 6) * 81920u + (((to & 63) * 2) ^ (((d >> 1) & 3) * 32 + (d & 8) * 2));
    const unsigned wh = (lane >> 2) & 3, mcol = (wh < 3 ? wh : 4) * 640 + d;
    const unsigned jd = 8 * g - 9 - r, dv = dval << (16 * (jd & 1));
    const u32x4 vd = {jd >> 1 == 0 ? dv : 0u, jd >> 1 == 1 ? dv : 0u, jd >> 1 == 2 ? dv : 0u, jd >> 1 == 3 ? dv : 0u};
    asm volatile(
  "s_nop 4\n\t"
  "buffer_load_dwordx2 v[160:161], %[vf1], %[srd], 0 offen sc1\n\t"
  "s_lshl_b32 s45, %[w], 12\n\t"
  "v_mov_b32 v252, %[tlo]\n\t"
  "v_mov_b32 v253, %[thi]\n\t"
  "v_mov_b32 v254, %[tlo]\n\t"
  "v_mov_b32 v255, %[thi]\n\t"
  "v_mov_b32 v162, 140288\n\t"
  "v_mov_b32 v163, 1\n\t"
  "s_mov_b64 exec, 1\n\t"
  "s_waitcnt vmcnt(1)\n\t"
  "ds_add_rtn_u32 v164, v162, v163\n\t"
  "s_waitcnt lgkmcnt(0)\n\t"
  "s_mov_b64 exec, -1\n\t"
  "v_readfirstlane_b32 s44, v164\n\t"
  "s_nop 3\n\t"
  "s_cmp_lg_u32 s44, 3\n\t"
  "s_cbranch_scc1 Lnox%=\n\t"
  "buffer_store_dwordx4 v[252:255], %[vf3], %[srd], 0 offen sc1\n\t"
  "s_waitcnt vmcnt(1)\n\t"
  "s_branch Lx%=\n\t"
  "Lnox%=:\n\t"
  "s_waitcnt vmcnt(0)\n\t"
  "Lx%=:\n\t"
  "v_cmp_ne_u64 vcc, v[252:253], v[160:161]\n\t"
  "s_nop 1\n\t"
  "s_cbranch_vccnz La1_slow%=\n\t"
  "La1_back%=:\n\t"
  "s_add_u32 m0, s45, 68608\n\t"
  "s_add_u32 s44, %[sKV], 0\n\t"
  "buffer_load_dwordx4 %[vkv], %[srd], s44 offen sc1 lds\n\t"
  "s_add_u32 m0, s45, 69632\n\t"
  "s_add_u32 s44, %[sKV], 163840\n\t"
  "buffer_load_dwordx4 %[vkv], %[srd], s44 offen sc1 lds\n\t"
  "s_add_u32 m0, s45, 70656\n\t"
  "s_add_u32 s44, %[sKV], 327680\n\t"
  "buffer_load_dwordx4 %[vkv], %[srd], s44 offen sc1 lds\n\t"
  "s_add_u32 m0, s45, 71680\n\t"
  "s_add_u32 s44, %[sKV], 491520\n\t"
  "buffer_load_dwordx4 %[vkv], %[srd], s44 offen sc1 lds\n\t"
  "s_waitcnt vmcnt(0) lgkmcnt(0)\n\t"
  "s_barrier\n\t"
  "ds_read_b64 v[64:65], %[va] offset:0\n\t"
  "ds_read_b64 v[66:67], %[va] offset:8\n\t"
  "ds_read_b128 v[124:127], %[vb0] offset:15360\n\t"
  "ds_read_b128 v[120:123], %[vb0] offset:13312\n\t"
  "ds_read_b128 v[116:119], %[vb0] offset:11264\n\t"
  "ds_read_b128 v[112:115], %[vb0] offset:9216\n\t"
  "ds_read_b128 v[108:111], %[vb0] offset:7168\n\t"
  "ds_read_b128 v[104:107], %[vb0] offset:5120\n\t"
  "ds_read_b128 v[100:103], %[vb0] offset:3072\n\t"
  "ds_read_b128 v[96:99], %[vb0] offset:1024\n\t"
  "ds_read_b64 v[68:69], %[va] offset:512\n\t"
  "ds_read_b64 v[70:71], %[va] offset:520\n\t"
  "ds_read_b64 v[72:73], %[va] offset:1024\n\t"
  "ds_read_b64 v[74:75], %[va] offset:1032\n\t"
  "ds_read_b64 v[76:77], %[va] offset:1536\n\t"
  "ds_read_b64 v[78:79], %[va] offset:1544\n\t"
  "ds_read_b64 v[80:81], %[va] offset:2048\n\t"
  "ds_read_b64 v[82:83], %[va] offset:2056\n\t"
  "ds_read_b64 v[84:85], %[va] offset:2560\n\t"
  "ds_read_b64 v[86:87], %[va] offset:2568\n\t"
  "ds_read_b64 v[88:89], %[va] offset:3072\n\t"
  "ds_read_b64 v[90:91], %[va] offset:3080\n\t"
  "ds_read_b64 v[92:93], %[va] offset:3584\n\t"
  "ds_read_b64 v[94:95], %[va] offset:3592\n\t"
  "s_waitcnt lgkmcnt(15)\n\t"
  "v_mfma_f32_16x16x32_bf16 v[60:63], v[64:67], v[124:127], 0\n\t"
  "ds_read_b128 v[156:159], %[vb1] offset:15360\n\t"
  "buffer_load_dwordx2 v[250:251], %[vfw], %[srd], 0 offen sc1\n\t"
  "v_mfma_f32_16x16x32_bf16 v[56:59], v[64:67], v[120:123], 0\n\t"
  "v_mfma_f32_16x16x32_bf16 v[52:55], v[64:67], v[116:119], 0\n\t"
  "v_mfma_f32_16x16x32_bf16 v[48:51], v[64:67], v[112:115], 0\n\t"
  "v_mfma_f32_16x16x32_bf16 v[44:47], v[64:67], v[108:111], 0\n\t"
  "ds_read_b128 v[152:155], %[vb1] offset:13312\n\t"
  "v_mfma_f32_16x16x32_bf16 v[40:43], v[64:67], v[104:107], 0\n\t"
  "v_mfma_f32_16x16x32_bf16 v[36:39], v[64:67], v[100:103], 0\n\t"
  "s_waitcnt lgkmcnt(15)\n\t"
  "v_mfma_f32_16x16x32_bf16 v[32:35], v[64:67], v[96:99], 0\n\t"
  "v_mfma_f32_16x16x32_bf16 v[60:63], %[vd], v[124:127], v[60:63]\n\t"
  "ds_read_b128 v[148:151], %[vb1] offset:11264\n\t"
  "v_mfma_f32_16x16x32_bf16 v[56:59], %[vd], v[120:123], v[56:59]\n\t"
  "v_mfma_f32_16x16x32_bf16 v[52:55], %[vd], v[116:119], v[52:55]\n\t"
  "v_mfma_f32_16x16x32_bf16 v[48:51], %[vd], v[112:115], v[48:51]\n\t"
  "v_mfma_f32_16x16x32_bf16 v[44:47], %[vd], v[108:111], v[44:47]\n\t"
  "ds_read_b128 v[144:147], %[vb1] offset:9216\n\t"
  "v_mfma_f32_16x16x32_bf16 v[40:43], %[vd], v[104:107], v[40:43]\n\t"
  "v_mfma_f32_16x16x32_bf16 v[36:39], %[vd], v[100:103], v[36:39]\n\t"
  "v_mfma_f32_16x16x32_bf16 v[32:35], %[vd], v[96:99], v[32:35]\n\t"
  "ds_read_b64 v[64:65], %[va] offset:64\n\t"
  "ds_read_b64 v[66:67], %[va] offset:72\n\t"
  "s_waitcnt lgkmcnt(15)\n\t"
  "v_mfma_f32_16x16x32_bf16 v[60:63], v[68:71], v[120:123], v[60:63]\n\t"
  "ds_read_b128 v[140:143], %[vb1] offset:7168\n\t"
  "v_mfma_f32_16x16x32_bf16 v[56:59], v[68:71], v[116:119], v[56:59]\n\t"
  "v_mfma_f32_16x16x32_bf16 v[52:55], v[68:71], v[112:115], v[52:55]\n\t"
  "v_mfma_f32_16x16x32_bf16 v[48:51], v[68:71], v[108:111], v[48:51]\n\t"
  "v_mfma_f32_16x16x32_bf16 v[44:47], v[68:71], v[104:107], v[44:47]\n\t"
  "ds_read_b128 v[136:139], %[vb1] offset:5120\n\t"
  "v_mfma_f32_16x16x32_bf16 v[40:43], v[68:71], v[100:103], v[40:43]\n\t"
  "v_mfma_f32_16x16x32_bf16 v[36:39], v[68:71], v[96:99], v[36:39]\n\t"
  "ds_read_b64 v[68:69], %[va] offset:576\n\t"
  "ds_read_b64 v[70:71], %[va] offset:584\n\t"
  "v_mfma_f32_16x16x32_bf16 v[60:63], v[72:75], v[116:119], v[60:63]\n\t"
  "v_mfma_f32_16x16x32_bf16 v[56:59], v[72:75], v[112:115], v[56:59]\n\t"
  "ds_read_b128 v[132:135], %[vb1] offset:3072\n\t"
  "v_mfma_f32_16x16x32_bf16 v[52:55], v[72:75], v[108:111], v[52:55]\n\t"
  "v_mfma_f32_16x16x32_bf16 v[48:51], v[72:75], v[104:107], v[48:51]\n\t"
  "v_mfma_f32_16x16x32_bf16 v[44:47], v[72:75], v[100:103], v[44:47]\n\t"
  "v_mfma_f32_16x16x32_bf16 v[40:43], v[72:75], v[96:99], v[40:43]\n\t"
  "ds_read_b64 v[72:73], %[va] offset:1088\n\t"
  "ds_read_b128 v[128:131], %[vb1] offset:1024\n\t"
  "s_waitcnt lgkmcnt(15)\n\t"
  "v_mfma_f32_16x16x32_bf16 v[60:63], v[76:79], v[112:115], v[60:63]\n\t"
  "ds_read_b64 v[74:75], %[va] offset:1096\n\t"
  "v_mfma_f32_16x16x32_bf16 v[56:59], v[76:79], v[108:111], v[56:59]\n\t"
  "v_mfma_f32_16x16x32_bf16 v[52:55], v[76:79], v[104:107], v[52:55]\n\t"
  "v_mfma_f32_16x16x32_bf16 v[48:51], v[76:79], v[100:103], v[48:51]\n\t"
  "v_mfma_f32_16x16x32_bf16 v[44:47], v[76:79], v[96:99], v[44:47]\n\t"
  "ds_read_b64 v[76:77], %[va] offset:1600\n\t"
  "ds_read_b64 v[78:79], %[va] offset:1608\n\t"
  "v_mfma_f32_16x16x32_bf16 v[60:63], v[80:83], v[108:111], v[60:63]\n\t"
  "v_mfma_f32_16x16x32_bf16 v[56:59], v[80:83], v[104:107], v[56:59]\n\t"
  "v_mfma_f32_16x16x32_bf16 v[52:55], v[80:83], v[100:103], v[52:55]\n\t"
  "v_mfma_f32_16x16x32_bf16 v[48:51], v[80:83], v[96:99], v[48:51]\n\t"
  "ds_read_b64 v[80:81], %[va] offset:2112\n\t"
  "ds_read_b64 v[82:83], %[va] offset:2120\n\t"
  "v_mfma_f32_16x16x32_bf16 v[60:63], v[84:87], v[104:107], v[60:63]\n\t"
  "v_mfma_f32_16x16x32_bf16 v[56:59], v[84:87], v[100:103], v[56:59]\n\t"
  "v_mfma_f32_16x16x32_bf16 v[52:55], v[84:87], v[96:99], v[52:55]\n\t"
  "ds_read_b64 v[84:85], %[va] offset:2624\n\t"
  "ds_read_b64 v[86:87], %[va] offset:2632\n\t"
  "v_mfma_f32_16x16x32_bf16 v[60:63], v[88:91], v[100:103], v[60:63]\n\t"
  "s_waitcnt vmcnt(0)\n\t"
  "v_cmp_eq_u64 s[54:55], v[252:253], v[250:251]\n\t"
  "v_mfma_f32_16x16x32_bf16 v[56:59], v[88:91], v[96:99], v[56:59]\n\t"
  "ds_read_b64 v[88:89], %[va] offset:3136\n\t"
  "ds_read_b64 v[90:91], %[va] offset:3144\n\t"
  "s_waitcnt lgkmcnt(15)\n\t"
  "v_mfma_f32_16x16x32_bf16 v[60:63], v[92:95], v[96:99], v[60:63]\n\t"
  "s_waitcnt lgkmcnt(9)\n\t"
  "ds_read_b64 v[92:93], %[va] offset:3648\n\t"
  "v_mfma_f32_16x16x32_bf16 v[60:63], v[64:67], v[156:159], v[60:63]\n\t"
  "ds_read_b128 v[124:127], %[vb2] offset:15360\n\t"
  "ds_read_b64 v[94:95], %[va] offset:3656\n\t"
  "v_mfma_f32_16x16x32_bf16 v[56:59], v[64:67], v[152:155], v[56:59]\n\t"
  "s_cmp_eq_u64 s[54:55], -1\n\t"
  "s_cselect_b32 s53, 1, 0\n\t"
  "s_cselect_b32 s56, %[sW1], 0x70000000\n\t"
  "v_mfma_f32_16x16x32_bf16 v[52:55], v[64:67], v[148:151], v[52:55]\n\t"
  "s_mov_b32 %[wdone], s53\n\t"
  "buffer_load_dwordx4 a[0:3], %[vl16], %[srd], s56 offen offset:0 sc1\n\t"
  "v_mfma_f32_16x16x32_bf16 v[48:51], v[64:67], v[144:147], v[48:51]\n\t"
  "ds_read_b128 v[120:123], %[vb2] offset:13312\n\t"
  "v_mfma_f32_16x16x32_bf16 v[44:47], v[64:67], v[140:143], v[44:47]\n\t"
  "v_mfma_f32_16x16x32_bf16 v[40:43], v[64:67], v[136:139], v[40:43]\n\t"
  "buffer_load_dwordx4 a[4:7], %[vl16], %[srd], s56 offen offset:1024 sc1\n\t"
  "v_mfma_f32_16x16x32_bf16 v[36:39], v[64:67], v[132:135], v[36:39]\n\t"
  "ds_read_b128 v[116:119], %[vb2] offset:11264\n\t"
  "v_mfma_f32_16x16x32_bf16 v[32:35], v[64:67], v[128:131], v[32:35]\n\t"
  "ds_read_b64 v[64:65], %[va] offset:128\n\t"
  "ds_read_b64 v[66:67], %[va] offset:136\n\t"
  "v_mfma_f32_16x16x32_bf16 v[60:63], v[68:71], v[152:155], v[60:63]\n\t"
  "v_mfma_f32_16x16x32_bf16 v[56:59], v[68:71], v[148:151], v[56:59]\n\t"
  "ds_read_b128 v[112:115], %[vb2] offset:9216\n\t"
  "buffer_load_dwordx4 a[8:11], %[vl16], %[srd], s56 offen offset:2048 sc1\n\t"
  "v_mfma_f32_16x16x32_bf16 v[52:55], v[68:71], v[144:147], v[52:55]\n\t"
  "v_mfma_f32_16x16x32_bf16 v[48:51], v[68:71], v[140:143], v[48:51]\n\t"
  "v_mfma_f32_16x16x32_bf16 v[44:47], v[68:71], v[136:139], v[44:47]\n\t"
  "ds_read_b128 v[108:111], %[vb2] offset:7168\n\t"
  "buffer_load_dwordx4 a[12:15], %[vl16], %[srd], s56 offen offset:3072 sc1\n\t"
  "v_mfma_f32_16x16x32_bf16 v[40:43], v[68:71], v[132:135], v[40:43]\n\t"
  "v_mfma_f32_16x16x32_bf16 v[36:39], v[68:71], v[128:131], v[36:39]\n\t"
  "s_waitcnt lgkmcnt(11)\n\t"
  "ds_read_b64 v[68:69], %[va] offset:640\n\t"
  "v_mfma_f32_16x16x32_bf16 v[60:63], v[72:75], v[148:151], v[60:63]\n\t"
  "ds_read_b128 v[104:107], %[vb2] offset:5120\n\t"
  "ds_read_b64 v[70:71], %[va] offset:648\n\t"
  "v_mfma_f32_16x16x32_bf16 v[56:59], v[72:75], v[144:147], v[56:59]\n\t"
  "s_add_u32 s56, s56, 4096\n\t"
  "v_mfma_f32_16x16x32_bf16 v[52:55], v[72:75], v[140:143], v[52:55]\n\t"
  "v_mfma_f32_16x16x32_bf16 v[48:51], v[72:75], v[136:139], v[48:51]\n\t"
  "ds_read_b128 v[100:103], %[vb2] offset:3072\n\t"
  "v_mfma_f32_16x16x32_bf16 v[44:47], v[72:75], v[132:135], v[44:47]\n\t"
  "buffer_load_dwordx4 a[16:19], %[vl16], %[srd], s56 offen offset:0 sc1\n\t"
  "v_mfma_f32_16x16x32_bf16 v[40:43], v[72:75], v[128:131], v[40:43]\n\t"
  "ds_read_b64 v[72:73], %[va] offset:1152\n\t"
  "ds_read_b64 v[74:75], %[va] offset:1160\n\t"
  "v_mfma_f32_16x16x32_bf16 v[60:63], v[76:79], v[144:147], v[60:63]\n\t"
  "ds_read_b128 v[96:99], %[vb2] offset:1024\n\t"
  "v_mfma_f32_16x16x32_bf16 v[56:59], v[76:79], v[140:143], v[56:59]\n\t"
  "v_mfma_f32_16x16x32_bf16 v[52:55], v[76:79], v[136:139], v[52:55]\n\t"
  "buffer_load_dwordx4 a[20:23], %[vl16], %[srd], s56 offen offset:1024 sc1\n\t"
  "v_mfma_f32_16x16x32_bf16 v[48:51], v[76:79], v[132:135], v[48:51]\n\t"
  "v_mfma_f32_16x16x32_bf16 v[44:47], v[76:79], v[128:131], v[44:47]\n\t"
  "ds_read_b64 v[76:77], %[va] offset:1664\n\t"
  "ds_read_b64 v[78:79], %[va] offset:1672\n\t"
  "v_mfma_f32_16x16x32_bf16 v[60:63], v[80:83], v[140:143], v[60:63]\n\t"
  "buffer_load_dwordx4 a[24:27], %[vl16], %[srd], s56 offen offset:2048 sc1\n\t"
  "v_mfma_f32_16x16x32_bf16 v[56:59], v[80:83], v[136:139], v[56:59]\n\t"
  "v_mfma_f32_16x16x32_bf16 v[52:55], v[80:83], v[132:135], v[52:55]\n\t"
  "v_mfma_f32_16x16x32_bf16 v[48:51], v[80:83], v[128:131], v[48:51]\n\t"
  "ds_read_b64 v[80:81], %[va] offset:2176\n\t"
  "ds_read_b64 v[82:83], %[va] offset:2184\n\t"
  "v_mfma_f32_16x16x32_bf16 v[60:63], v[84:87], v[136:139], v[60:63]\n\t"
  "buffer_load_dwordx4 a[28:31], %[vl16], %[srd], s56 offen offset:3072 sc1\n\t"
  "v_mfma_f32_16x16x32_bf16 v[56:59], v[84:87], v[132:135], v[56:59]\n\t"
  "v_mfma_f32_16x16x32_bf16 v[52:55], v[84:87], v[128:131], v[52:55]\n\t"
  "s_waitcnt lgkmcnt(15)\n\t"
  "ds_read_b64 v[84:85], %[va] offset:2688\n\t"
  "v_mfma_f32_16x16x32_bf16 v[60:63], v[88:91], v[132:135], v[60:63]\n\t"
  "ds_read_b64 v[86:87], %[va] offset:2696\n\t"
  "s_add_u32 s56, s56, 4096\n\t"
  "v_mfma_f32_16x16x32_bf16 v[56:59], v[88:91], v[128:131], v[56:59]\n\t"
  "ds_read_b64 v[88:89], %[va] offset:3200\n\t"
  "ds_read_b64 v[90:91], %[va] offset:3208\n\t"
  "v_mfma_f32_16x16x32_bf16 v[60:63], v[92:95], v[128:131], v[60:63]\n\t"
  "s_waitcnt lgkmcnt(8)\n\t"
  "ds_read_b64 v[92:93], %[va] offset:3712\n\t"
  "v_mfma_f32_16x16x32_bf16 v[60:63], v[64:67], v[124:127], v[60:63]\n\t"
  "ds_read_b128 v[156:159], %[vb3] offset:15360\n\t"
  "ds_read_b64 v[94:95], %[va] offset:3720\n\t"
  "v_mfma_f32_16x16x32_bf16 v[56:59], v[64:67], v[120:123], v[56:59]\n\t"
  "buffer_load_dwordx4 a[32:35], %[vl16], %[srd], s56 offen offset:0 sc1\n\t"
  "v_mfma_f32_16x16x32_bf16 v[52:55], v[64:67], v[116:119], v[52:55]\n\t"
  "v_mfma_f32_16x16x32_bf16 v[48:51], v[64:67], v[112:115], v[48:51]\n\t"
  "ds_read_b128 v[152:155], %[vb3] offset:13312\n\t"
  "v_mfma_f32_16x16x32_bf16 v[44:47], v[64:67], v[108:111], v[44:47]\n\t"
  "buffer_load_dwordx4 a[36:39], %[vl16], %[srd], s56 offen offset:1024 sc1\n\t"
  "v_mfma_f32_16x16x32_bf16 v[40:43], v[64:67], v[104:107], v[40:43]\n\t"
  "v_mfma_f32_16x16x32_bf16 v[36:39], v[64:67], v[100:103], v[36:39]\n\t"
  "ds_read_b128 v[148:151], %[vb3] offset:11264\n\t"
  "v_mfma_f32_16x16x32_bf16 v[32:35], v[64:67], v[96:99], v[32:35]\n\t"
  "ds_read_b64 v[64:65], %[va] offset:192\n\t"
  "ds_read_b64 v[66:67], %[va] offset:200\n\t"
  "v_mfma_f32_16x16x32_bf16 v[60:63], v[68:71], v[120:123], v[60:63]\n\t"
  "buffer_load_dwordx4 a[40:43], %[vl16], %[srd], s56 offen offset:2048 sc1\n\t"
  "v_mfma_f32_16x16x32_bf16 v[56:59], v[68:71], v[116:119], v[56:59]\n\t"
  "ds_read_b128 v[144:147], %[vb3] offset:9216\n\t"
  "v_mfma_f32_16x16x32_bf16 v[52:55], v[68:71], v[112:115], v[52:55]\n\t"
  "v_mfma_f32_16x16x32_bf16 v[48:51], v[68:71], v[108:111], v[48:51]\n\t"
  "buffer_load_dwordx4 a[44:47], %[vl16], %[srd], s56 offen offset:3072 sc1\n\t"
  "v_mfma_f32_16x16x32_bf16 v[44:47], v[68:71], v[104:107], v[44:47]\n\t"
  "ds_read_b128 v[140:143], %[vb3] offset:7168\n\t"
  "v_mfma_f32_16x16x32_bf16 v[40:43], v[68:71], v[100:103], v[40:43]\n\t"
  "v_mfma_f32_16x16x32_bf16 v[36:39], v[68:71], v[96:99], v[36:39]\n\t"
  "s_waitcnt lgkmcnt(11)\n\t"
  "ds_read_b64 v[68:69], %[va] offset:704\n\t"
  "v_mfma_f32_16x16x32_bf16 v[60:63], v[72:75], v[116:119], v[60:63]\n\t"
  "ds_read_b128 v[136:139], %[vb3] offset:5120\n\t"
  "ds_read_b64 v[70:71], %[va] offset:712\n\t"
  "v_mfma_f32_16x16x32_bf16 v[56:59], v[72:75], v[112:115], v[56:59]\n\t"
  "s_add_u32 s56, s56, 4096\n\t"
  "v_mfma_f32_16x16x32_bf16 v[52:55], v[72:75], v[108:111], v[52:55]\n\t"
  "v_mfma_f32_16x16x32_bf16 v[48:51], v[72:75], v[104:107], v[48:51]\n\t"
  "ds_read_b128 v[132:135], %[vb3] offset:3072\n\t"
  "buffer_load_dwordx4 a[48:51], %[vl16], %[srd], s56 offen offset:0 sc1\n\t"
  "v_mfma_f32_16x16x32_bf16 v[44:47], v[72:75], v[100:103], v[44:47]\n\t"
  "v_mfma_f32_16x16x32_bf16 v[40:43], v[72:75], v[96:99], v[40:43]\n\t"
  "ds_read_b64 v[72:73], %[va] offset:1216\n\t"
  "ds_read_b64 v[74:75], %[va] offset:1224\n\t"
  "v_mfma_f32_16x16x32_bf16 v[60:63], v[76:79], v[112:115], v[60:63]\n\t"
  "ds_read_b128 v[128:131], %[vb3] offset:1024\n\t"
  "v_mfma_f32_16x16x32_bf16 v[56:59], v[76:79], v[108:111], v[56:59]\n\t"
  "buffer_load_dwordx4 a[52:55], %[vl16], %[srd], s56 offen offset:1024 sc1\n\t"
  "v_mfma_f32_16x16x32_bf16 v[52:55], v[76:79], v[104:107], v[52:55]\n\t"
  "v_mfma_f32_16x16x32_bf16 v[48:51], v[76:79], v[100:103], v[48:51]\n\t"
  "v_mfma_f32_16x16x32_bf16 v[44:47], v[76:79], v[96:99], v[44:47]\n\t"
  "ds_read_b64 v[76:77], %[va] offset:1728\n\t"
  "ds_read_b64 v[78:79], %[va] offset:1736\n\t"
  "v_mfma_f32_16x16x32_bf16 v[60:63], v[80:83], v[108:111], v[60:63]\n\t"
  "buffer_load_dwordx4 a[56:59], %[vl16], %[srd], s56 offen offset:2048 sc1\n\t"
  "v_mfma_f32_16x16x32_bf16 v[56:59], v[80:83], v[104:107], v[56:59]\n\t"
  "v_mfma_f32_16x16x32_bf16 v[52:55], v[80:83], v[100:103], v[52:55]\n\t"
  "v_mfma_f32_16x16x32_bf16 v[48:51], v[80:83], v[96:99], v[48:51]\n\t"
  "ds_read_b64 v[80:81], %[va] offset:2240\n\t"
  "ds_read_b64 v[82:83], %[va] offset:2248\n\t"
  "v_mfma_f32_16x16x32_bf16 v[60:63], v[84:87], v[104:107], v[60:63]\n\t"
  "buffer_load_dwordx4 a[60:63], %[vl16], %[srd], s56 offen offset:3072 sc1\n\t"
  "v_mfma_f32_16x16x32_bf16 v[56:59], v[84:87], v[100:103], v[56:59]\n\t"
  "v_mfma_f32_16x16x32_bf16 v[52:55], v[84:87], v[96:99], v[52:55]\n\t"
  "s_waitcnt lgkmcnt(15)\n\t"
  "ds_read_b64 v[84:85], %[va] offset:2752\n\t"
  "v_mfma_f32_16x16x32_bf16 v[60:63], v[88:91], v[100:103], v[60:63]\n\t"
  "ds_read_b64 v[86:87], %[va] offset:2760\n\t"
  "s_add_u32 s56, s56, 4096\n\t"
  "v_mfma_f32_16x16x32_bf16 v[56:59], v[88:91], v[96:99], v[56:59]\n\t"
  "ds_read_b64 v[88:89], %[va] offset:3264\n\t"
  "ds_read_b64 v[90:91], %[va] offset:3272\n\t"
  "v_mfma_f32_16x16x32_bf16 v[60:63], v[92:95], v[96:99], v[60:63]\n\t"
  "s_waitcnt lgkmcnt(8)\n\t"
  "ds_read_b64 v[92:93], %[va] offset:3776\n\t"
  "v_mfma_f32_16x16x32_bf16 v[60:63], v[64:67], v[156:159], v[60:63]\n\t"
  "ds_read_b128 v[124:127], %[vb0] offset:14336\n\t"
  "ds_read_b64 v[94:95], %[va] offset:3784\n\t"
  "v_mfma_f32_16x16x32_bf16 v[56:59], v[64:67], v[152:155], v[56:59]\n\t"
  "buffer_load_dwordx4 a[64:67], %[vl16], %[srd], s56 offen offset:0 sc1\n\t"
  "v_mfma_f32_16x16x32_bf16 v[52:55], v[64:67], v[148:151], v[52:55]\n\t"
  "v_mfma_f32_16x16x32_bf16 v[48:51], v[64:67], v[144:147], v[48:51]\n\t"
  "ds_read_b128 v[120:123], %[vb0] offset:12288\n\t"
  "buffer_load_dwordx4 a[68:71], %[vl16], %[srd], s56 offen offset:1024 sc1\n\t"
  "v_mfma_f32_16x16x32_bf16 v[44:47], v[64:67], v[140:143], v[44:47]\n\t"
  "v_mfma_f32_16x16x32_bf16 v[40:43], v[64:67], v[136:139], v[40:43]\n\t"
  "v_mfma_f32_16x16x32_bf16 v[36:39], v[64:67], v[132:135], v[36:39]\n\t"
  "ds_read_b128 v[116:119], %[vb0] offset:10240\n\t"
  "v_mfma_f32_16x16x32_bf16 v[32:35], v[64:67], v[128:131], v[32:35]\n\t"
  "ds_read_b64 v[64:65], %[va] offset:256\n\t"
  "ds_read_b64 v[66:67], %[va] offset:264\n\t"
  "v_mfma_f32_16x16x32_bf16 v[60:63], v[68:71], v[152:155], v[60:63]\n\t"
  "buffer_load_dwordx4 a[72:75], %[vl16], %[srd], s56 offen offset:2048 sc1\n\t"
  "v_mfma_f32_16x16x32_bf16 v[56:59], v[68:71], v[148:151], v[56:59]\n\t"
  "ds_read_b128 v[112:115], %[vb0] offset:8192\n\t"
  "v_mfma_f32_16x16x32_bf16 v[52:55], v[68:71], v[144:147], v[52:55]\n\t"
  "buffer_load_dwordx4 a[76:79], %[vl16], %[srd], s56 offen offset:3072 sc1\n\t"
  "v_mfma_f32_16x16x32_bf16 v[48:51], v[68:71], v[140:143], v[48:51]\n\t"
  "v_mfma_f32_16x16x32_bf16 v[44:47], v[68:71], v[136:139], v[44:47]\n\t"
  "ds_read_b128 v[108:111], %[vb0] offset:6144\n\t"
  "v_mfma_f32_16x16x32_bf16 v[40:43], v[68:71], v[132:135], v[40:43]\n\t"
  "v_mfma_f32_16x16x32_bf16 v[36:39], v[68:71], v[128:131], v[36:39]\n\t"
  "s_waitcnt lgkmcnt(11)\n\t"
  "ds_read_b64 v[68:69], %[va] offset:768\n\t"
  "v_mfma_f32_16x16x32_bf16 v[60:63], v[72:75], v[148:151], v[60:63]\n\t"
  "ds_read_b128 v[104:107], %[vb0] offset:4096\n\t"
  "ds_read_b64 v[70:71], %[va] offset:776\n\t"
  "v_mfma_f32_16x16x32_bf16 v[56:59], v[72:75], v[144:147], v[56:59]\n\t"
  "v_mfma_f32_16x16x32_bf16 v[52:55], v[72:75], v[140:143], v[52:55]\n\t"
  "v_mfma_f32_16x16x32_bf16 v[48:51], v[72:75], v[136:139], v[48:51]\n\t"
  "ds_read_b128 v[100:103], %[vb0] offset:2048\n\t"
  "v_mfma_f32_16x16x32_bf16 v[44:47], v[72:75], v[132:135], v[44:47]\n\t"
  "v_mfma_f32_16x16x32_bf16 v[40:43], v[72:75], v[128:131], v[40:43]\n\t"
  "ds_read_b64 v[72:73], %[va] offset:1280\n\t"
  "ds_read_b64 v[74:75], %[va] offset:1288\n\t"
  "v_mfma_f32_16x16x32_bf16 v[60:63], v[76:79], v[144:147], v[60:63]\n\t"
  "ds_read_b128 v[96:99], %[vb0] offset:0\n\t"
  "buffer_load_dwordx2 v[162:163], %[vf2], %[srd], 0 offen sc1\n\t"
  "v_mfma_f32_16x16x32_bf16 v[56:59], v[76:79], v[140:143], v[56:59]\n\t"
  "v_mfma_f32_16x16x32_bf16 v[52:55], v[76:79], v[136:139], v[52:55]\n\t"
  "v_mfma_f32_16x16x32_bf16 v[48:51], v[76:79], v[132:135], v[48:51]\n\t"
  "v_mfma_f32_16x16x32_bf16 v[44:47], v[76:79], v[128:131], v[44:47]\n\t"
  "ds_read_b64 v[76:77], %[va] offset:1792\n\t"
  "ds_read_b64 v[78:79], %[va] offset:1800\n\t"
  "v_mfma_f32_16x16x32_bf16 v[60:63], v[80:83], v[140:143], v[60:63]\n\t"
  "v_mfma_f32_16x16x32_bf16 v[56:59], v[80:83], v[136:139], v[56:59]\n\t"
  "v_mfma_f32_16x16x32_bf16 v[52:55], v[80:83], v[132:135], v[52:55]\n\t"
  "v_mfma_f32_16x16x32_bf16 v[48:51], v[80:83], v[128:131], v[48:51]\n\t"
  "ds_read_b64 v[80:81], %[va] offset:2304\n\t"
  "ds_read_b64 v[82:83], %[va] offset:2312\n\t"
  "v_mfma_f32_16x16x32_bf16 v[60:63], v[84:87], v[136:139], v[60:63]\n\t"
  "v_mfma_f32_16x16x32_bf16 v[56:59], v[84:87], v[132:135], v[56:59]\n\t"
  "v_mfma_f32_16x16x32_bf16 v[52:55], v[84:87], v[128:131], v[52:55]\n\t"
  "s_waitcnt lgkmcnt(15)\n\t"
  "ds_read_b64 v[84:85], %[va] offset:2816\n\t"
  "v_mfma_f32_16x16x32_bf16 v[60:63], v[88:91], v[132:135], v[60:63]\n\t"
  "ds_read_b64 v[86:87], %[va] offset:2824\n\t"
  "v_mfma_f32_16x16x32_bf16 v[56:59], v[88:91], v[128:131], v[56:59]\n\t"
  "ds_read_b64 v[88:89], %[va] offset:3328\n\t"
  "ds_read_b64 v[90:91], %[va] offset:3336\n\t"
  "v_mfma_f32_16x16x32_bf16 v[60:63], v[92:95], v[128:131], v[60:63]\n\t"
  "s_waitcnt lgkmcnt(8)\n\t"
  "ds_read_b64 v[92:93], %[va] offset:3840\n\t"
  "v_mfma_f32_16x16x32_bf16 v[60:63], v[64:67], v[124:127], v[60:63]\n\t"
  "ds_read_b128 v[156:159], %[vb1] offset:14336\n\t"
  "ds_read_b64 v[94:95], %[va] offset:3848\n\t"
  "v_mfma_f32_16x16x32_bf16 v[56:59], v[64:67], v[120:123], v[56:59]\n\t"
  "v_mfma_f32_16x16x32_bf16 v[52:55], v[64:67], v[116:119], v[52:55]\n\t"
  "v_mfma_f32_16x16x32_bf16 v[48:51], v[64:67], v[112:115], v[48:51]\n\t"
  "ds_read_b128 v[152:155], %[vb1] offset:12288\n\t"
  "v_mfma_f32_16x16x32_bf16 v[44:47], v[64:67], v[108:111], v[44:47]\n\t"
  "v_mfma_f32_16x16x32_bf16 v[40:43], v[64:67], v[104:107], v[40:43]\n\t"
  "v_mfma_f32_16x16x32_bf16 v[36:39], v[64:67], v[100:103], v[36:39]\n\t"
  "ds_read_b128 v[148:151], %[vb1] offset:10240\n\t"
  "v_mfma_f32_16x16x32_bf16 v[32:35], v[64:67], v[96:99], v[32:35]\n\t"
  "ds_read_b64 v[64:65], %[va] offset:320\n\t"
  "ds_read_b64 v[66:67], %[va] offset:328\n\t"
  "v_mfma_f32_16x16x32_bf16 v[60:63], v[68:71], v[120:123], v[60:63]\n\t"
  "buffer_load_dwordx2 v[250:251], %[vfw], %[srd], 0 offen sc1\n\t"
  "v_mfma_f32_16x16x32_bf16 v[56:59], v[68:71], v[116:119], v[56:59]\n\t"
  "ds_read_b128 v[144:147], %[vb1] offset:8192\n\t"
  "v_mfma_f32_16x16x32_bf16 v[52:55], v[68:71], v[112:115], v[52:55]\n\t"
  "v_mfma_f32_16x16x32_bf16 v[48:51], v[68:71], v[108:111], v[48:51]\n\t"
  "v_mfma_f32_16x16x32_bf16 v[44:47], v[68:71], v[104:107], v[44:47]\n\t"
  "ds_read_b128 v[140:143], %[vb1] offset:6144\n\t"
  "v_mfma_f32_16x16x32_bf16 v[40:43], v[68:71], v[100:103], v[40:43]\n\t"
  "v_mfma_f32_16x16x32_bf16 v[36:39], v[68:71], v[96:99], v[36:39]\n\t"
  "s_waitcnt lgkmcnt(11)\n\t"
  "ds_read_b64 v[68:69], %[va] offset:832\n\t"
  "v_mfma_f32_16x16x32_bf16 v[60:63], v[72:75], v[116:119], v[60:63]\n\t"
  "ds_read_b128 v[136:139], %[vb1] offset:4096\n\t"
  "ds_read_b64 v[70:71], %[va] offset:840\n\t"
  "v_mfma_f32_16x16x32_bf16 v[56:59], v[72:75], v[112:115], v[56:59]\n\t"
  "v_mfma_f32_16x16x32_bf16 v[52:55], v[72:75], v[108:111], v[52:55]\n\t"
  "v_mfma_f32_16x16x32_bf16 v[48:51], v[72:75], v[104:107], v[48:51]\n\t"
  "ds_read_b128 v[132:135], %[vb1] offset:2048\n\t"
  "v_mfma_f32_16x16x32_bf16 v[44:47], v[72:75], v[100:103], v[44:47]\n\t"
  "v_mfma_f32_16x16x32_bf16 v[40:43], v[72:75], v[96:99], v[40:43]\n\t"
  "ds_read_b64 v[72:73], %[va] offset:1344\n\t"
  "ds_read_b64 v[74:75], %[va] offset:1352\n\t"
  "v_mfma_f32_16x16x32_bf16 v[60:63], v[76:79], v[112:115], v[60:63]\n\t"
  "ds_read_b128 v[128:131], %[vb1] offset:0\n\t"
  "v_mfma_f32_16x16x32_bf16 v[56:59], v[76:79], v[108:111], v[56:59]\n\t"
  "v_mfma_f32_16x16x32_bf16 v[52:55], v[76:79], v[104:107], v[52:55]\n\t"
  "v_mfma_f32_16x16x32_bf16 v[48:51], v[76:79], v[100:103], v[48:51]\n\t"
  "v_mfma_f32_16x16x32_bf16 v[44:47], v[76:79], v[96:99], v[44:47]\n\t"
  "ds_read_b64 v[76:77], %[va] offset:1856\n\t"
  "ds_read_b64 v[78:79], %[va] offset:1864\n\t"
  "v_mfma_f32_16x16x32_bf16 v[60:63], v[80:83], v[108:111], v[60:63]\n\t"
  "s_waitcnt vmcnt(1)\n\t"
  "v_cmp_ne_u64 vcc, v[252:253], v[162:163]\n\t"
  "v_mfma_f32_16x16x32_bf16 v[56:59], v[80:83], v[104:107], v[56:59]\n\t"
  "v_mfma_f32_16x16x32_bf16 v[52:55], v[80:83], v[100:103], v[52:55]\n\t"
  "s_cbranch_vccnz La2_slow%=\n\t"
  "La2_back%=:\n\t"
  "v_mfma_f32_16x16x32_bf16 v[48:51], v[80:83], v[96:99], v[48:51]\n\t"
  "ds_read_b64 v[80:81], %[va] offset:2368\n\t"
  "ds_read_b64 v[82:83], %[va] offset:2376\n\t"
  "v_mfma_f32_16x16x32_bf16 v[60:63], v[84:87], v[104:107], v[60:63]\n\t"
  "buffer_load_dword v244, %[vmod], %[srd], 0 offen sc1\n\t"
  "s_add_u32 m0, s45, 84992\n\t"
  "v_mfma_f32_16x16x32_bf16 v[56:59], v[84:87], v[100:103], v[56:59]\n\t"
  "s_add_u32 s44, %[sKV], 2621440\n\t"
  "buffer_load_dwordx4 %[vkv], %[srd], s44 offen sc1 lds\n\t"
  "v_mfma_f32_16x16x32_bf16 v[52:55], v[84:87], v[96:99], v[52:55]\n\t"
  "s_waitcnt lgkmcnt(15)\n\t"
  "ds_read_b64 v[84:85], %[va] offset:2880\n\t"
  "v_mfma_f32_16x16x32_bf16 v[60:63], v[88:91], v[100:103], v[60:63]\n\t"
  "ds_read_b64 v[86:87], %[va] offset:2888\n\t"
  "s_add_u32 m0, s45, 86016\n\t"
  "v_mfma_f32_16x16x32_bf16 v[56:59], v[88:91], v[96:99], v[56:59]\n\t"
  "ds_read_b64 v[88:89], %[va] offset:3392\n\t"
  "ds_read_b64 v[90:91], %[va] offset:3400\n\t"
  "v_mfma_f32_16x16x32_bf16 v[60:63], v[92:95], v[96:99], v[60:63]\n\t"
  "s_waitcnt lgkmcnt(8)\n\t"
  "ds_read_b64 v[92:93], %[va] offset:3904\n\t"
  "v_mfma_f32_16x16x32_bf16 v[60:63], v[64:67], v[156:159], v[60:63]\n\t"
  "ds_read_b128 v[124:127], %[vb2] offset:14336\n\t"
  "ds_read_b64 v[94:95], %[va] offset:3912\n\t"
  "v_mfma_f32_16x16x32_bf16 v[56:59], v[64:67], v[152:155], v[56:59]\n\t"
  "s_add_u32 s44, %[sKV], 2785280\n\t"
  "buffer_load_dwordx4 %[vkv], %[srd], s44 offen sc1 lds\n\t"
  "v_mfma_f32_16x16x32_bf16 v[52:55], v[64:67], v[148:151], v[52:55]\n\t"
  "s_add_u32 m0, s45, 87040\n\t"
  "s_add_u32 s44, %[sKV], 2949120\n\t"
  "v_mfma_f32_16x16x32_bf16 v[48:51], v[64:67], v[144:147], v[48:51]\n\t"
  "ds_read_b128 v[120:123], %[vb2] offset:12288\n\t"
  "buffer_load_dwordx4 %[vkv], %[srd], s44 offen sc1 lds\n\t"
  "v_mfma_f32_16x16x32_bf16 v[44:47], v[64:67], v[140:143], v[44:47]\n\t"
  "s_add_u32 m0, s45, 88064\n\t"
  "s_add_u32 s44, %[sKV], 3112960\n\t"
  "v_mfma_f32_16x16x32_bf16 v[40:43], v[64:67], v[136:139], v[40:43]\n\t"
  "buffer_load_dwordx4 %[vkv], %[srd], s44 offen sc1 lds\n\t"
  "s_add_u32 m0, s45, 101376\n\t"
  "v_mfma_f32_16x16x32_bf16 v[36:39], v[64:67], v[132:135], v[36:39]\n\t"
  "ds_read_b128 v[116:119], %[vb2] offset:10240\n\t"
  "s_add_u32 s44, s45, 847872\n\t"
  "v_mfma_f32_16x16x32_bf16 v[32:35], v[64:67], v[128:131], v[32:35]\n\t"
  "ds_read_b64 v[64:65], %[va] offset:384\n\t"
  "ds_read_b64 v[66:67], %[va] offset:392\n\t"
  "v_mfma_f32_16x16x32_bf16 v[60:63], v[68:71], v[152:155], v[60:63]\n\t"
  "buffer_load_dwordx4 %[vl16], %[srd], s44 offen offset:0 sc1 lds\n\t"
  "buffer_load_dwordx4 %[vl16], %[srd], s44 offen offset:1024 sc1 lds\n\t"
  "v_mfma_f32_16x16x32_bf16 v[56:59], v[68:71], v[148:151], v[56:59]\n\t"
  "ds_read_b128 v[112:115], %[vb2] offset:8192\n\t"
  "buffer_load_dwordx4 %[vl16], %[srd], s44 offen offset:2048 sc1 lds\n\t"
  "v_mfma_f32_16x16x32_bf16 v[52:55], v[68:71], v[144:147], v[52:55]\n\t"
  "buffer_load_dwordx4 %[vl16], %[srd], s44 offen offset:3072 sc1 lds\n\t"
  "v_mfma_f32_16x16x32_bf16 v[48:51], v[68:71], v[140:143], v[48:51]\n\t"
  "v_mfma_f32_16x16x32_bf16 v[44:47], v[68:71], v[136:139], v[44:47]\n\t"
  "ds_read_b128 v[108:111], %[vb2] offset:6144\n\t"
  "s_waitcnt vmcnt(9)\n\t"
  "v_cmp_eq_u64 s[54:55], v[252:253], v[250:251]\n\t"
  "v_mfma_f32_16x16x32_bf16 v[40:43], v[68:71], v[132:135], v[40:43]\n\t"
  "v_mfma_f32_16x16x32_bf16 v[36:39], v[68:71], v[128:131], v[36:39]\n\t"
  "s_waitcnt lgkmcnt(11)\n\t"
  "ds_read_b64 v[68:69], %[va] offset:896\n\t"
  "v_mfma_f32_16x16x32_bf16 v[60:63], v[72:75], v[148:151], v[60:63]\n\t"
  "ds_read_b128 v[104:107], %[vb2] offset:4096\n\t"
  "ds_read_b64 v[70:71], %[va] offset:904\n\t"
  "v_mfma_f32_16x16x32_bf16 v[56:59], v[72:75], v[144:147], v[56:59]\n\t"
  "s_cmp_eq_u64 s[54:55], -1\n\t"
  "s_cselect_b32 s53, 2, 0\n\t"
  "s_cselect_b32 s57, %[sW1], 0x70000000\n\t"
  "v_mfma_f32_16x16x32_bf16 v[52:55], v[72:75], v[140:143], v[52:55]\n\t"
  "s_or_b32 %[wdone], %[wdone], s53\n\t"
  "v_mfma_f32_16x16x32_bf16 v[48:51], v[72:75], v[136:139], v[48:51]\n\t"
  "ds_read_b128 v[100:103], %[vb2] offset:2048\n\t"
  "s_add_u32 s57, s57, 20480\n\t"
  "v_mfma_f32_16x16x32_bf16 v[44:47], v[72:75], v[132:135], v[44:47]\n\t"
  "v_mfma_f32_16x16x32_bf16 v[40:43], v[72:75], v[128:131], v[40:43]\n\t"
  "ds_read_b64 v[72:73], %[va] offset:1408\n\t"
  "ds_read_b64 v[74:75], %[va] offset:1416\n\t"
  "v_mfma_f32_16x16x32_bf16 v[60:63], v[76:79], v[144:147], v[60:63]\n\t"
  "ds_read_b128 v[96:99], %[vb2] offset:0\n\t"
  "buffer_load_dwordx4 a[80:83], %[vl16], %[srd], s57 offen offset:0 sc1\n\t"
  "v_mfma_f32_16x16x32_bf16 v[56:59], v[76:79], v[140:143], v[56:59]\n\t"
  "v_mfma_f32_16x16x32_bf16 v[52:55], v[76:79], v[136:139], v[52:55]\n\t"
  "v_mfma_f32_16x16x32_bf16 v[48:51], v[76:79], v[132:135], v[48:51]\n\t"
  "buffer_load_dwordx4 a[84:87], %[vl16], %[srd], s57 offen offset:1024 sc1\n\t"
  "v_mfma_f32_16x16x32_bf16 v[44:47], v[76:79], v[128:131], v[44:47]\n\t"
  "ds_read_b64 v[76:77], %[va] offset:1920\n\t"
  "ds_read_b64 v[78:79], %[va] offset:1928\n\t"
  "v_mfma_f32_16x16x32_bf16 v[60:63], v[80:83], v[140:143], v[60:63]\n\t"
  "v_mfma_f32_16x16x32_bf16 v[56:59], v[80:83], v[136:139], v[56:59]\n\t"
  "v_mfma_f32_16x16x32_bf16 v[52:55], v[80:83], v[132:135], v[52:55]\n\t"
  "buffer_load_dwordx4 a[88:91], %[vl16], %[srd], s57 offen offset:2048 sc1\n\t"
  "v_mfma_f32_16x16x32_bf16 v[48:51], v[80:83], v[128:131], v[48:51]\n\t"
  "ds_read_b64 v[80:81], %[va] offset:2432\n\t"
  "ds_read_b64 v[82:83], %[va] offset:2440\n\t"
  "v_mfma_f32_16x16x32_bf16 v[60:63], v[84:87], v[136:139], v[60:63]\n\t"
  "v_mfma_f32_16x16x32_bf16 v[56:59], v[84:87], v[132:135], v[56:59]\n\t"
  "buffer_load_dwordx4 a[92:95], %[vl16], %[srd], s57 offen offset:3072 sc1\n\t"
  "v_mfma_f32_16x16x32_bf16 v[52:55], v[84:87], v[128:131], v[52:55]\n\t"
  "s_waitcnt lgkmcnt(15)\n\t"
  "ds_read_b64 v[84:85], %[va] offset:2944\n\t"
  "v_mfma_f32_16x16x32_bf16 v[60:63], v[88:91], v[132:135], v[60:63]\n\t"
  "ds_read_b64 v[86:87], %[va] offset:2952\n\t"
  "v_mfma_f32_16x16x32_bf16 v[56:59], v[88:91], v[128:131], v[56:59]\n\t"
  "ds_read_b64 v[88:89], %[va] offset:3456\n\t"
  "ds_read_b64 v[90:91], %[va] offset:3464\n\t"
  "v_mfma_f32_16x16x32_bf16 v[60:63], v[92:95], v[128:131], v[60:63]\n\t"
  "s_waitcnt lgkmcnt(8)\n\t"
  "ds_read_b64 v[92:93], %[va] offset:3968\n\t"
  "v_mfma_f32_16x16x32_bf16 v[60:63], v[64:67], v[124:127], v[60:63]\n\t"
  "ds_read_b128 v[156:159], %[vb3] offset:14336\n\t"
  "ds_read_b64 v[94:95], %[va] offset:3976\n\t"
  "v_mfma_f32_16x16x32_bf16 v[56:59], v[64:67], v[120:123], v[56:59]\n\t"
  "s_add_u32 s57, s57, 4096\n\t"
  "v_mfma_f32_16x16x32_bf16 v[52:55], v[64:67], v[116:119], v[52:55]\n\t"
  "buffer_load_dwordx4 a[96:99], %[vl16], %[srd], s57 offen offset:0 sc1\n\t"
  "v_mfma_f32_16x16x32_bf16 v[48:51], v[64:67], v[112:115], v[48:51]\n\t"
  "ds_read_b128 v[152:155], %[vb3] offset:12288\n\t"
  "v_mfma_f32_16x16x32_bf16 v[44:47], v[64:67], v[108:111], v[44:47]\n\t"
  "v_mfma_f32_16x16x32_bf16 v[40:43], v[64:67], v[104:107], v[40:43]\n\t"
  "buffer_load_dwordx4 a[100:103], %[vl16], %[srd], s57 offen offset:1024 sc1\n\t"
  "v_mfma_f32_16x16x32_bf16 v[36:39], v[64:67], v[100:103], v[36:39]\n\t"
  "ds_read_b128 v[148:151], %[vb3] offset:10240\n\t"
  "v_mfma_f32_16x16x32_bf16 v[32:35], v[64:67], v[96:99], v[32:35]\n\t"
  "ds_read_b64 v[64:65], %[va] offset:448\n\t"
  "ds_read_b64 v[66:67], %[va] offset:456\n\t"
  "v_mfma_f32_16x16x32_bf16 v[60:63], v[68:71], v[120:123], v[60:63]\n\t"
  "buffer_load_dwordx4 a[104:107], %[vl16], %[srd], s57 offen offset:2048 sc1\n\t"
  "v_mfma_f32_16x16x32_bf16 v[56:59], v[68:71], v[116:119], v[56:59]\n\t"
  "ds_read_b128 v[144:147], %[vb3] offset:8192\n\t"
  "v_mfma_f32_16x16x32_bf16 v[52:55], v[68:71], v[112:115], v[52:55]\n\t"
  "v_mfma_f32_16x16x32_bf16 v[48:51], v[68:71], v[108:111], v[48:51]\n\t"
  "v_mfma_f32_16x16x32_bf16 v[44:47], v[68:71], v[104:107], v[44:47]\n\t"
  "ds_read_b128 v[140:143], %[vb3] offset:6144\n\t"
  "buffer_load_dwordx4 a[108:111], %[vl16], %[srd], s57 offen offset:3072 sc1\n\t"
  "v_mfma_f32_16x16x32_bf16 v[40:43], v[68:71], v[100:103], v[40:43]\n\t"
  "v_mfma_f32_16x16x32_bf16 v[36:39], v[68:71], v[96:99], v[36:39]\n\t"
  "s_waitcnt lgkmcnt(11)\n\t"
  "ds_read_b64 v[68:69], %[va] offset:960\n\t"
  "v_mfma_f32_16x16x32_bf16 v[60:63], v[72:75], v[116:119], v[60:63]\n\t"
  "ds_read_b128 v[136:139], %[vb3] offset:4096\n\t"
  "ds_read_b64 v[70:71], %[va] offset:968\n\t"
  "v_mfma_f32_16x16x32_bf16 v[56:59], v[72:75], v[112:115], v[56:59]\n\t"
  "s_add_u32 s57, s57, 4096\n\t"
  "v_mfma_f32_16x16x32_bf16 v[52:55], v[72:75], v[108:111], v[52:55]\n\t"
  "v_mfma_f32_16x16x32_bf16 v[48:51], v[72:75], v[104:107], v[48:51]\n\t"
  "ds_read_b128 v[132:135], %[vb3] offset:2048\n\t"
  "buffer_load_dwordx4 a[112:115], %[vl16], %[srd], s57 offen offset:0 sc1\n\t"
  "v_mfma_f32_16x16x32_bf16 v[44:47], v[72:75], v[100:103], v[44:47]\n\t"
  "v_mfma_f32_16x16x32_bf16 v[40:43], v[72:75], v[96:99], v[40:43]\n\t"
  "ds_read_b64 v[72:73], %[va] offset:1472\n\t"
  "ds_read_b64 v[74:75], %[va] offset:1480\n\t"
  "v_mfma_f32_16x16x32_bf16 v[60:63], v[76:79], v[112:115], v[60:63]\n\t"
  "ds_read_b128 v[128:131], %[vb3] offset:0\n\t"
  "v_mfma_f32_16x16x32_bf16 v[56:59], v[76:79], v[108:111], v[56:59]\n\t"
  "buffer_load_dwordx4 a[116:119], %[vl16], %[srd], s57 offen offset:1024 sc1\n\t"
  "v_mfma_f32_16x16x32_bf16 v[52:55], v[76:79], v[104:107], v[52:55]\n\t"
  "v_mfma_f32_16x16x32_bf16 v[48:51], v[76:79], v[100:103], v[48:51]\n\t"
  "v_mfma_f32_16x16x32_bf16 v[44:47], v[76:79], v[96:99], v[44:47]\n\t"
  "ds_read_b64 v[76:77], %[va] offset:1984\n\t"
  "ds_read_b64 v[78:79], %[va] offset:1992\n\t"
  "v_mfma_f32_16x16x32_bf16 v[60:63], v[80:83], v[108:111], v[60:63]\n\t"
  "buffer_load_dwordx4 a[120:123], %[vl16], %[srd], s57 offen offset:2048 sc1\n\t"
  "v_mfma_f32_16x16x32_bf16 v[56:59], v[80:83], v[104:107], v[56:59]\n\t"
  "v_mfma_f32_16x16x32_bf16 v[52:55], v[80:83], v[100:103], v[52:55]\n\t"
  "v_mfma_f32_16x16x32_bf16 v[48:51], v[80:83], v[96:99], v[48:51]\n\t"
  "ds_read_b64 v[80:81], %[va] offset:2496\n\t"
  "ds_read_b64 v[82:83], %[va] offset:2504\n\t"
  "v_mfma_f32_16x16x32_bf16 v[60:63], v[84:87], v[104:107], v[60:63]\n\t"
  "buffer_load_dwordx4 a[124:127], %[vl16], %[srd], s57 offen offset:3072 sc1\n\t"
  "s_waitcnt vmcnt(20)\n\t"
  "v_add_f32_dpp v244, v244, v244 quad_perm:[1,0,3,2] row_mask:0xf bank_mask:0xf\n\t"
  "v_mfma_f32_16x16x32_bf16 v[56:59], v[84:87], v[100:103], v[56:59]\n\t"
  "v_mfma_f32_16x16x32_bf16 v[52:55], v[84:87], v[96:99], v[52:55]\n\t"
  "s_waitcnt lgkmcnt(15)\n\t"
  "ds_read_b64 v[84:85], %[va] offset:3008\n\t"
  "v_mfma_f32_16x16x32_bf16 v[60:63], v[88:91], v[100:103], v[60:63]\n\t"
  "ds_read_b64 v[86:87], %[va] offset:3016\n\t"
  "s_add_u32 s57, s57, 4096\n\t"
  "v_mfma_f32_16x16x32_bf16 v[56:59], v[88:91], v[96:99], v[56:59]\n\t"
  "ds_read_b64 v[88:89], %[va] offset:3520\n\t"
  "ds_read_b64 v[90:91], %[va] offset:3528\n\t"
  "v_mfma_f32_16x16x32_bf16 v[60:63], v[92:95], v[96:99], v[60:63]\n\t"
  "s_waitcnt lgkmcnt(8)\n\t"
  "ds_read_b64 v[92:93], %[va] offset:4032\n\t"
  "v_mfma_f32_16x16x32_bf16 v[60:63], v[64:67], v[156:159], v[60:63]\n\t"
  "ds_read_b64 v[94:95], %[va] offset:4040\n\t"
  "buffer_load_dwordx4 a[128:131], %[vl16], %[srd], s57 offen offset:0 sc1\n\t"
  "v_mfma_f32_16x16x32_bf16 v[56:59], v[64:67], v[152:155], v[56:59]\n\t"
  "v_add_f32_dpp v244, v244, v244 quad_perm:[2,3,0,1] row_mask:0xf bank_mask:0xf\n\t"
  "v_cvt_pk_bf16_f32 v244, 0, v244\n\t"
  "v_mfma_f32_16x16x32_bf16 v[52:55], v[64:67], v[148:151], v[52:55]\n\t"
  "v_add_f32 v245, 1.0, v244\n\t"
  "v_cvt_pk_bf16_f32 v245, 0, v245\n\t"
  "v_mfma_f32_16x16x32_bf16 v[48:51], v[64:67], v[144:147], v[48:51]\n\t"
  "buffer_load_dwordx4 a[132:135], %[vl16], %[srd], s57 offen offset:1024 sc1\n\t"
  "v_readlane_b32 s46, v244, 0\n\t"
  "v_mfma_f32_16x16x32_bf16 v[44:47], v[64:67], v[140:143], v[44:47]\n\t"
  "v_readlane_b32 s48, v244, 8\n\t"
  "v_readlane_b32 s47, v245, 4\n\t"
  "v_mfma_f32_16x16x32_bf16 v[40:43], v[64:67], v[136:139], v[40:43]\n\t"
  "v_readlane_b32 s51, v245, 12\n\t"
  "v_mfma_f32_16x16x32_bf16 v[36:39], v[64:67], v[132:135], v[36:39]\n\t"
  "buffer_load_dwordx4 a[136:139], %[vl16], %[srd], s57 offen offset:2048 sc1\n\t"
  "v_mfma_f32_16x16x32_bf16 v[32:35], v[64:67], v[128:131], v[32:35]\n\t"
  "s_nop 0\n\t"
  "s_and_b32 s52, s51, 0x7fffffff\n\t"
  "s_cselect_b32 s51, s51, 0x2b800000\n\t"
  "v_mfma_f32_16x16x32_bf16 v[60:63], v[68:71], v[152:155], v[60:63]\n\t"
  "v_mov_b32 v245, s48\n\t"
  "v_mul_f32 v245, s51, v245\n\t"
  "v_mfma_f32_16x16x32_bf16 v[56:59], v[68:71], v[148:151], v[56:59]\n\t"
  "buffer_load_dwordx4 a[140:143], %[vl16], %[srd], s57 offen offset:3072 sc1\n\t"
  "v_mul_f32 v244, s47, v245\n\t"
  "v_mfma_f32_16x16x32_bf16 v[52:55], v[68:71], v[144:147], v[52:55]\n\t"
  "v_mul_f32 v245, s46, v245\n\t"
  "v_mfma_f32_16x16x32_bf16 v[48:51], v[68:71], v[140:143], v[48:51]\n\t"
  "s_waitcnt vmcnt(16)\n\t"
  "s_barrier\n\t"
  "v_mfma_f32_16x16x32_bf16 v[44:47], v[68:71], v[136:139], v[44:47]\n\t"
  "v_mov_b32 v246, s51\n\t"
  "ds_read_b64 v[224:225], %[vx] offset:0\n\t"
  "v_mfma_f32_16x16x32_bf16 v[40:43], v[68:71], v[132:135], v[40:43]\n\t"
  "s_add_u32 s57, s57, 4096\n\t"
  "ds_read_b128 v[160:163], %[vs] offset:0\n\t"
  "v_mfma_f32_16x16x32_bf16 v[36:39], v[68:71], v[128:131], v[36:39]\n\t"
  "s_waitcnt lgkmcnt(6)\n\t"
  "ds_read_b128 v[164:167], %[vs2] offset:0\n\t"
  "v_mfma_f32_16x16x32_bf16 v[60:63], v[72:75], v[148:151], v[60:63]\n\t"
  "ds_read_b64 v[226:227], %[vx] offset:2048\n\t"
  "ds_read_b128 v[168:171], %[vs] offset:2048\n\t"
  "v_mfma_f32_16x16x32_bf16 v[56:59], v[72:75], v[144:147], v[56:59]\n\t"
  "buffer_load_dwordx4 a[144:147], %[vl16], %[srd], s57 offen offset:0 sc1\n\t"
  "ds_read_b128 v[172:175], %[vs2] offset:2048\n\t"
  "v_mfma_f32_16x16x32_bf16 v[52:55], v[72:75], v[140:143], v[52:55]\n\t"
  "ds_read_b64 v[228:229], %[vx] offset:4096\n\t"
  "ds_read_b128 v[176:179], %[vs] offset:4096\n\t"
  "v_mfma_f32_16x16x32_bf16 v[48:51], v[72:75], v[136:139], v[48:51]\n\t"
  "ds_read_b128 v[180:183], %[vs2] offset:4096\n\t"
  "ds_read_b64 v[230:231], %[vx] offset:6144\n\t"
  "v_mfma_f32_16x16x32_bf16 v[44:47], v[72:75], v[132:135], v[44:47]\n\t"
  "buffer_load_dwordx4 a[148:151], %[vl16], %[srd], s57 offen offset:1024 sc1\n\t"
  "ds_read_b128 v[184:187], %[vs] offset:6144\n\t"
  "v_mfma_f32_16x16x32_bf16 v[40:43], v[72:75], v[128:131], v[40:43]\n\t"
  "ds_read_b128 v[188:191], %[vs2] offset:6144\n\t"
  "ds_read_b64 v[232:233], %[vx] offset:8192\n\t"
  "v_mfma_f32_16x16x32_bf16 v[60:63], v[76:79], v[144:147], v[60:63]\n\t"
  "ds_read_b128 v[192:195], %[vs] offset:8192\n\t"
  "ds_read_b128 v[196:199], %[vs2] offset:8192\n\t"
  "v_mfma_f32_16x16x32_bf16 v[56:59], v[76:79], v[140:143], v[56:59]\n\t"
  "ds_read_b64 v[234:235], %[vx] offset:10240\n\t"
  "ds_read_b128 v[200:203], %[vs] offset:10240\n\t"
  "v_mfma_f32_16x16x32_bf16 v[52:55], v[76:79], v[136:139], v[52:55]\n\t"
  "buffer_load_dwordx4 a[152:155], %[vl16], %[srd], s57 offen offset:2048 sc1\n\t"
  "ds_read_b128 v[204:207], %[vs2] offset:10240\n\t"
  "v_mfma_f32_16x16x32_bf16 v[48:51], v[76:79], v[132:135], v[48:51]\n\t"
  "ds_read_b64 v[236:237], %[vx] offset:12288\n\t"
  "ds_read_b128 v[208:211], %[vs] offset:12288\n\t"
  "v_mfma_f32_16x16x32_bf16 v[44:47], v[76:79], v[128:131], v[44:47]\n\t"
  "ds_read_b128 v[212:215], %[vs2] offset:12288\n\t"
  "ds_read_b64 v[238:239], %[vx] offset:14336\n\t"
  "v_mfma_f32_16x16x32_bf16 v[60:63], v[80:83], v[140:143], v[60:63]\n\t"
  "buffer_load_dwordx4 a[156:159], %[vl16], %[srd], s57 offen offset:3072 sc1\n\t"
  "ds_read_b128 v[216:219], %[vs] offset:14336\n\t"
  "v_mfma_f32_16x16x32_bf16 v[56:59], v[80:83], v[136:139], v[56:59]\n\t"
  "ds_read_b128 v[220:223], %[vs2] offset:14336\n\t"
  "v_mfma_f32_16x16x32_bf16 v[52:55], v[80:83], v[132:135], v[52:55]\n\t"
  "v_mfma_f32_16x16x32_bf16 v[48:51], v[80:83], v[128:131], v[48:51]\n\t"
  "v_mfma_f32_16x16x32_bf16 v[60:63], v[84:87], v[136:139], v[60:63]\n\t"
  "v_mfma_f32_16x16x32_bf16 v[56:59], v[84:87], v[132:135], v[56:59]\n\t"
  "v_mfma_f32_16x16x32_bf16 v[52:55], v[84:87], v[128:131], v[52:55]\n\t"
  "s_waitcnt lgkmcnt(15)\n\t"
  "v_mfma_f32_16x16x32_bf16 v[60:63], v[88:91], v[132:135], v[60:63]\n\t"
  "v_mfma_f32_16x16x32_bf16 v[56:59], v[88:91], v[128:131], v[56:59]\n\t"
  "v_mfma_f32_16x16x32_bf16 v[60:63], v[92:95], v[128:131], v[60:63]\n\t"
  "v_lshlrev_b32 v96, 16, v224\n\t"
  "v_and_b32 v97, 0xffff0000, v224\n\t"
  "v_lshlrev_b32 v98, 16, v225\n\t"
  "v_and_b32 v99, 0xffff0000, v225\n\t"
  "v_lshlrev_b32 v100, 16, v226\n\t"
  "v_and_b32 v101, 0xffff0000, v226\n\t"
  "v_lshlrev_b32 v102, 16, v227\n\t"
  "v_and_b32 v103, 0xffff0000, v227\n\t"
  "v_pk_fma_f32 v[160:161], v[96:97], v[160:161], v[162:163]\n\t"
  "v_pk_fma_f32 v[164:165], v[98:99], v[164:165], v[166:167]\n\t"
  "v_pk_fma_f32 v[168:169], v[100:101], v[168:169], v[170:171]\n\t"
  "v_pk_fma_f32 v[172:173], v[102:103], v[172:173], v[174:175]\n\t"
  "v_pk_mul_f32 v[96:97], v[96:97], v[246:247] op_sel_hi:[1,0]\n\t"
  "v_pk_mul_f32 v[98:99], v[98:99], v[246:247] op_sel_hi:[1,0]\n\t"
  "v_pk_mul_f32 v[100:101], v[100:101], v[246:247] op_sel_hi:[1,0]\n\t"
  "v_pk_mul_f32 v[102:103], v[102:103], v[246:247] op_sel_hi:[1,0]\n\t"
  "v_pk_fma_f32 v[160:161], v[160:161], v[244:245], v[244:245] op_sel:[0,0,1] op_sel_hi:[1,0,1]\n\t"
  "v_pk_fma_f32 v[164:165], v[164:165], v[244:245], v[244:245] op_sel:[0,0,1] op_sel_hi:[1,0,1]\n\t"
  "v_pk_fma_f32 v[168:169], v[168:169], v[244:245], v[244:245] op_sel:[0,0,1] op_sel_hi:[1,0,1]\n\t"
  "v_pk_fma_f32 v[172:173], v[172:173], v[244:245], v[244:245] op_sel:[0,0,1] op_sel_hi:[1,0,1]\n\t"
  "v_pk_fma_f32 v[96:97], v[160:161], v[32:33], v[96:97]\n\t"
  "v_pk_fma_f32 v[98:99], v[164:165], v[34:35], v[98:99]\n\t"
  "v_pk_fma_f32 v[100:101], v[168:169], v[36:37], v[100:101]\n\t"
  "v_pk_fma_f32 v[102:103], v[172:173], v[38:39], v[102:103]\n\t"
  "v_cvt_pk_bf16_f32 v64, v96, v97\n\t"
  "v_cvt_pk_bf16_f32 v66, v100, v101\n\t"
  "v_cvt_pk_bf16_f32 v65, v98, v99\n\t"
  "v_cvt_pk_bf16_f32 v67, v102, v103\n\t"
  "s_add_u32 s44, %[sO], 0\n\t"
  "v_permlane16_swap_b32 v64, v66\n\t"
  "v_permlane16_swap_b32 v65, v67\n\t"
  "buffer_store_dwordx4 v[64:67], %[vo], %[srd], s44 offen sc1\n\t"
  "v_lshlrev_b32 v104, 16, v228\n\t"
  "v_and_b32 v105, 0xffff0000, v228\n\t"
  "v_lshlrev_b32 v106, 16, v229\n\t"
  "v_and_b32 v107, 0xffff0000, v229\n\t"
  "s_waitcnt lgkmcnt(14)\n\t"
  "v_lshlrev_b32 v108, 16, v230\n\t"
  "v_and_b32 v109, 0xffff0000, v230\n\t"
  "v_lshlrev_b32 v110, 16, v231\n\t"
  "v_and_b32 v111, 0xffff0000, v231\n\t"
  "v_pk_fma_f32 v[176:177], v[104:105], v[176:177], v[178:179]\n\t"
  "v_pk_fma_f32 v[180:181], v[106:107], v[180:181], v[182:183]\n\t"
  "s_waitcnt lgkmcnt(13)\n\t"
  "v_pk_fma_f32 v[184:185], v[108:109], v[184:185], v[186:187]\n\t"
  "s_waitcnt lgkmcnt(12)\n\t"
  "v_pk_fma_f32 v[188:189], v[110:111], v[188:189], v[190:191]\n\t"
  "v_pk_mul_f32 v[104:105], v[104:105], v[246:247] op_sel_hi:[1,0]\n\t"
  "v_pk_mul_f32 v[106:107], v[106:107], v[246:247] op_sel_hi:[1,0]\n\t"
  "v_pk_mul_f32 v[108:109], v[108:109], v[246:247] op_sel_hi:[1,0]\n\t"
  "v_pk_mul_f32 v[110:111], v[110:111], v[246:247] op_sel_hi:[1,0]\n\t"
  "v_pk_fma_f32 v[176:177], v[176:177], v[244:245], v[244:245] op_sel:[0,0,1] op_sel_hi:[1,0,1]\n\t"
  "v_pk_fma_f32 v[180:181], v[180:181], v[244:245], v[244:245] op_sel:[0,0,1] op_sel_hi:[1,0,1]\n\t"
  "v_pk_fma_f32 v[184:185], v[184:185], v[244:245], v[244:245] op_sel:[0,0,1] op_sel_hi:[1,0,1]\n\t"
  "v_pk_fma_f32 v[188:189], v[188:189], v[244:245], v[244:245] op_sel:[0,0,1] op_sel_hi:[1,0,1]\n\t"
  "v_pk_fma_f32 v[104:105], v[176:177], v[40:41], v[104:105]\n\t"
  "v_pk_fma_f32 v[106:107], v[180:181], v[42:43], v[106:107]\n\t"
  "v_pk_fma_f32 v[108:109], v[184:185], v[44:45], v[108:109]\n\t"
  "v_pk_fma_f32 v[110:111], v[188:189], v[46:47], v[110:111]\n\t"
  "v_cvt_pk_bf16_f32 v68, v104, v105\n\t"
  "v_cvt_pk_bf16_f32 v70, v108, v109\n\t"
  "v_cvt_pk_bf16_f32 v69, v106, v107\n\t"
  "v_cvt_pk_bf16_f32 v71, v110, v111\n\t"
  "s_add_u32 s44, %[sO], 655360\n\t"
  "v_permlane16_swap_b32 v68, v70\n\t"
  "v_permlane16_swap_b32 v69, v71\n\t"
  "buffer_store_dwordx4 v[68:71], %[vo], %[srd], s44 offen sc1\n\t"
  "s_waitcnt lgkmcnt(11)\n\t"
  "v_lshlrev_b32 v112, 16, v232\n\t"
  "v_and_b32 v113, 0xffff0000, v232\n\t"
  "v_lshlrev_b32 v114, 16, v233\n\t"
  "v_and_b32 v115, 0xffff0000, v233\n\t"
  "s_waitcnt lgkmcnt(8)\n\t"
  "v_lshlrev_b32 v116, 16, v234\n\t"
  "v_and_b32 v117, 0xffff0000, v234\n\t"
  "v_lshlrev_b32 v118, 16, v235\n\t"
  "v_and_b32 v119, 0xffff0000, v235\n\t"
  "v_pk_fma_f32 v[192:193], v[112:113], v[192:193], v[194:195]\n\t"
  "v_pk_fma_f32 v[196:197], v[114:115], v[196:197], v[198:199]\n\t"
  "s_waitcnt lgkmcnt(7)\n\t"
  "v_pk_fma_f32 v[200:201], v[116:117], v[200:201], v[202:203]\n\t"
  "s_waitcnt lgkmcnt(6)\n\t"
  "v_pk_fma_f32 v[204:205], v[118:119], v[204:205], v[206:207]\n\t"
  "v_pk_mul_f32 v[112:113], v[112:113], v[246:247] op_sel_hi:[1,0]\n\t"
  "v_pk_mul_f32 v[114:115], v[114:115], v[246:247] op_sel_hi:[1,0]\n\t"
  "v_pk_mul_f32 v[116:117], v[116:117], v[246:247] op_sel_hi:[1,0]\n\t"
  "v_pk_mul_f32 v[118:119], v[118:119], v[246:247] op_sel_hi:[1,0]\n\t"
  "v_pk_fma_f32 v[192:193], v[192:193], v[244:245], v[244:245] op_sel:[0,0,1] op_sel_hi:[1,0,1]\n\t"
  "v_pk_fma_f32 v[196:197], v[196:197], v[244:245], v[244:245] op_sel:[0,0,1] op_sel_hi:[1,0,1]\n\t"
  "v_pk_fma_f32 v[200:201], v[200:201], v[244:245], v[244:245] op_sel:[0,0,1] op_sel_hi:[1,0,1]\n\t"
  "v_pk_fma_f32 v[204:205], v[204:205], v[244:245], v[244:245] op_sel:[0,0,1] op_sel_hi:[1,0,1]\n\t"
  "v_pk_fma_f32 v[112:113], v[192:193], v[48:49], v[112:113]\n\t"
  "v_pk_fma_f32 v[114:115], v[196:197], v[50:51], v[114:115]\n\t"
  "v_pk_fma_f32 v[116:117], v[200:201], v[52:53], v[116:117]\n\t"
  "v_pk_fma_f32 v[118:119], v[204:205], v[54:55], v[118:119]\n\t"
  "v_cvt_pk_bf16_f32 v72, v112, v113\n\t"
  "v_cvt_pk_bf16_f32 v74, v116, v117\n\t"
  "v_cvt_pk_bf16_f32 v73, v114, v115\n\t"
  "v_cvt_pk_bf16_f32 v75, v118, v119\n\t"
  "s_add_u32 s44, %[sO], 1310720\n\t"
  "v_permlane16_swap_b32 v72, v74\n\t"
  "v_permlane16_swap_b32 v73, v75\n\t"
  "buffer_store_dwordx4 v[72:75], %[vo], %[srd], s44 offen sc1\n\t"
  "s_waitcnt lgkmcnt(5)\n\t"
  "v_lshlrev_b32 v120, 16, v236\n\t"
  "v_and_b32 v121, 0xffff0000, v236\n\t"
  "v_lshlrev_b32 v122, 16, v237\n\t"
  "v_and_b32 v123, 0xffff0000, v237\n\t"
  "s_waitcnt lgkmcnt(2)\n\t"
  "v_lshlrev_b32 v124, 16, v238\n\t"
  "v_and_b32 v125, 0xffff0000, v238\n\t"
  "v_lshlrev_b32 v126, 16, v239\n\t"
  "v_and_b32 v127, 0xffff0000, v239\n\t"
  "v_pk_fma_f32 v[208:209], v[120:121], v[208:209], v[210:211]\n\t"
  "v_pk_fma_f32 v[212:213], v[122:123], v[212:213], v[214:215]\n\t"
  "s_waitcnt lgkmcnt(1)\n\t"
  "v_pk_fma_f32 v[216:217], v[124:125], v[216:217], v[218:219]\n\t"
  "s_waitcnt lgkmcnt(0)\n\t"
  "v_pk_fma_f32 v[220:221], v[126:127], v[220:221], v[222:223]\n\t"
  "v_pk_mul_f32 v[120:121], v[120:121], v[246:247] op_sel_hi:[1,0]\n\t"
  "v_pk_mul_f32 v[122:123], v[122:123], v[246:247] op_sel_hi:[1,0]\n\t"
  "v_pk_mul_f32 v[124:125], v[124:125], v[246:247] op_sel_hi:[1,0]\n\t"
  "v_pk_mul_f32 v[126:127], v[126:127], v[246:247] op_sel_hi:[1,0]\n\t"
  "v_pk_fma_f32 v[208:209], v[208:209], v[244:245], v[244:245] op_sel:[0,0,1] op_sel_hi:[1,0,1]\n\t"
  "v_pk_fma_f32 v[212:213], v[212:213], v[244:245], v[244:245] op_sel:[0,0,1] op_sel_hi:[1,0,1]\n\t"
  "v_pk_fma_f32 v[216:217], v[216:217], v[244:245], v[244:245] op_sel:[0,0,1] op_sel_hi:[1,0,1]\n\t"
  "v_pk_fma_f32 v[220:221], v[220:221], v[244:245], v[244:245] op_sel:[0,0,1] op_sel_hi:[1,0,1]\n\t"
  "v_pk_fma_f32 v[120:121], v[208:209], v[56:57], v[120:121]\n\t"
  "v_pk_fma_f32 v[122:123], v[212:213], v[58:59], v[122:123]\n\t"
  "v_pk_fma_f32 v[124:125], v[216:217], v[60:61], v[124:125]\n\t"
  "v_pk_fma_f32 v[126:127], v[220:221], v[62:63], v[126:127]\n\t"
  "v_cvt_pk_bf16_f32 v76, v120, v121\n\t"
  "v_cvt_pk_bf16_f32 v78, v124, v125\n\t"
  "v_cvt_pk_bf16_f32 v77, v122, v123\n\t"
  "v_cvt_pk_bf16_f32 v79, v126, v127\n\t"
  "s_add_u32 s44, %[sO], 1966080\n\t"
  "v_permlane16_swap_b32 v76, v78\n\t"
  "v_permlane16_swap_b32 v77, v79\n\t"
  "buffer_store_dwordx4 v[76:79], %[vo], %[srd], s44 offen sc1\n\t"
  "s_nop 1\n\t"
  "s_branch Lcend%=\n\t"
  "La1_slow%=:\n\t"
  "s_nop 7\n\t"
  "s_nop 3\n\t"
  "s_mov_b32 s49, 0x40000\n\t"
  "La1_loop%=:\n\t"
  "s_sleep 3\n\t"
  "buffer_load_dwordx2 v[160:161], %[vf1], %[srd], 0 offen sc1\n\t"
  "s_waitcnt vmcnt(0)\n\t"
  "v_cmp_ne_u64 vcc, v[252:253], v[160:161]\n\t"
  "s_nop 3\n\t"
  "s_cmp_eq_u64 vcc, 0\n\t"
  "s_cbranch_scc1 La1_back%=\n\t"
  "s_sub_u32 s49, s49, 1\n\t"
  "s_cmp_lg_u32 s49, 0\n\t"
  "s_cbranch_scc1 La1_loop%=\n\t"
  "s_branch La1_back%=\n\t"
  "La2_slow%=:\n\t"
  "s_nop 7\n\t"
  "s_nop 3\n\t"
  "s_mov_b32 s49, 0x40000\n\t"
  "La2_loop%=:\n\t"
  "s_sleep 3\n\t"
  "buffer_load_dwordx2 v[162:163], %[vf2], %[srd], 0 offen sc1\n\t"
  "s_waitcnt vmcnt(0)\n\t"
  "v_cmp_ne_u64 vcc, v[252:253], v[162:163]\n\t"
  "s_nop 3\n\t"
  "s_cmp_eq_u64 vcc, 0\n\t"
  "s_cbranch_scc1 La2_back%=\n\t"
  "s_sub_u32 s49, s49, 1\n\t"
  "s_cmp_lg_u32 s49, 0\n\t"
  "s_cbranch_scc1 La2_loop%=\n\t"
  "s_branch La2_back%=\n\t"
  "Lcend%=:\n\t"
      : [wdone] "=&s"(wdone)
      : [srd] "s"(srd), [sO] "s"(OFF_X2P + d * 128u), [w] "s"(w), [sKV] "s"(OFF_KVT + cb * 128u + w * 655360u),
        [tlo] "s"((u32)tag), [thi] "s"((u32)(tag >> 32)), [sW1] "s"(OFF_W1F + (j * 4 + w) * 61440u),
        [vf2] "v"(lane < 32 ? OFF_FA3 + (32 * w + lane) * 128 : OFF_FA2 + ((lane & 3) * 60 + (mcol >> 6)) * 128),
        [vf1] "v"(OFF_FA1 + ((cb & ~31u) + 8 * w + (lane & 7)) * 128), [vf3] "v"(lane < 8 ? (cb < 128 ? OFF_FA3 + cb * 128 : OFF_FA2 + (kq * 60 + cgp) * 128) + lane * 16 : OOB), [vfw] "v"(OFF_FW1 + (j * 4 + (lane & 3)) * 128),
        [va] "v"(va), [vb0] "v"(vb[0]), [vb1] "v"(vb[1]), [vb2] "v"(vb[2]), [vb3] "v"(vb[3]), [vx] "v"(vx), [vs] "v"(vs), [vs2] "v"(vs ^ 16), [vo] "v"(vo), [vd] "v"(vd),
        [vl16] "v"(lane * 16), [vkv] "v"((lane >> 3) * 20480 + (lane & 7) * 16),
        [vmod] "v"(OFF_MODP + ((lane & 3) * 3840 + mcol) * 4)
      : "memory", "vcc", "scc",
"a0", "a1", "a2", "a3", "a4", "a5", "a6", "a7", "a8", "a9", "a10", "a11", "a12", "a13", "a14", "a15", "a16", "a17", "a18", "a19", "a20", "a21", "a22", "a23", "a24", "a25", "a26", "a27", "a28", "a29", "a30", "a31", "a32", "a33", "a34", "a35", "a36", "a37", "a38", "a39", "a40", "a41", "a42", "a43", "a44", "a45", "a46", "a47", "a48", "a49", "a50", "a51", "a52", "a53", "a54", "a55", "a56", "a57", "a58", "a59", "a60", "a61", "a62", "a63", "a64", "a65", "a66", "a67", "a68", "a69", "a70", "a71", "a72", "a73", "a74", "a75", "a76", "a77", "a78", "a79", "a80", "a81", "a82", "a83", "a84", "a85", "a86", "a87", "a88", "a89", "a90", "a91", "a92", "a93", "a94", "a95", "a96", "a97", "a98", "a99", "a100", "a101", "a102", "a103", "a104", "a105", "a106", "a107", "a108", "a109", "a110", "a111", "a112", "a113", "a114", "a115", "a116", "a117", "a118", "a119", "a120", "a121", "a122", "a123", "a124", "a125", "a126", "a127", "a128", "a129", "a130", "a131", "a132", "a133", "a134", "a135", "a136", "a137", "a138", "a139", "a140", "a141", "a142", "a143", "a144", "a145", "a146", "a147", "a148", "a149", "a150", "a151", "a152", "a153", "a154", "a155", "a156", "a157", "a158", "a159", "s44", "s45", "s46", "s47", "s48", "s49", "s51", "s52", "s53", "s54", "s55", "s56", "s57", "v32", "v33", "v34", "v35", "v36", "v37", "v38", "v39", "v40", "v41", "v42", "v43", "v44", "v45", "v46", "v47", "v48", "v49", "v50", "v51", "v52", "v53", "v54", "v55", "v56", "v57", "v58", "v59", "v60", "v61", "v62", "v63", "v64", "v65", "v66", "v67", "v68", "v69", "v70", "v71", "v72", "v73", "v74", "v75", "v76", "v77", "v78", "v79", "v80", "v81", "v82", "v83", "v84", "v85", "v86", "v87", "v88", "v89", "v90", "v91", "v92", "v93", "v94", "v95", "v96", "v97", "v98", "v99", "v100", "v101", "v102", "v103", "v104", "v105", "v106", "v107", "v108", "v109", "v110", "v111", "v112", "v113", "v114", "v115", "v116", "v117", "v118", "v119", "v120", "v121", "v122", "v123", "v124", "v125", "v126", "v127", "v128", "v129", "v130", "v131", "v132", "v133", "v134", "v135", "v136", "v137", "v138", "v139", "v140", "v141", "v142", "v143", "v144", "v145", "v146", "v147", "v148", "v149", "v150", "v151", "v152", "v153", "v154", "v155", "v156", "v157", "v158", "v159", "v160", "v161", "v162", "v163", "v164", "v165", "v166", "v167", "v168", "v169", "v170", "v171", "v172", "v173", "v174", "v175", "v176", "v177", "v178", "v179", "v180", "v181", "v182", "v183", "v184", "v185", "v186", "v187", "v188", "v189", "v190", "v191", "v192", "v193", "v194", "v195", "v196", "v197", "v198", "v199", "v200", "v201", "v202", "v203", "v204", "v205", "v206", "v207", "v208", "v209", "v210", "v211", "v212", "v213", "v214", "v215", "v216", "v217", "v218", "v219", "v220", "v221", "v222", "v223", "v224", "v225", "v226", "v227", "v228", "v229", "v230", "v231", "v232", "v233", "v234", "v235", "v236", "v237", "v238", "v239", "v244", "v245", "v246", "v247", "v250", "v251", "v252", "v253", "v254", "v255"
    );
  }
  STAMP(4);
  STAMP_OUT(1);
  const unsigned vpub = tid < 8 ? (role == 0 ? OFF_FX + cb * 128 : role == 1 ? OFF_FU + w1 * 128 : OFF_FW2 + w2 * 128) + tid * 16 : OOB;
  const unsigned vpw = lane < 4 ? OFF_FW1 + (j * 4 + lane) * 128 : lane < 8 ? OFF_FW2 + ((4 * w + (lane & 3)) * 2 + (ng >> 2)) * 128
                                                                            : OFF_FU + ((lane < 12 ? j * 4 : 0) + (lane & 3)) * 128;
  const unsigned vpx = lane < 40 ? OFF_FX + (32 * (lane >> 3) + 8 * w + (lane & 7)) * 128 : OFF_FW2 + min(lane - 40, 9u) * 128;
  const unsigned g = lane >> 4, q = (lane >> 2) & 3, pq = lane & 3;
  const unsigned row = 4 * g + q;
  const unsigned vtr = lb + row * 128 + ((row >> 1) & 3) * 32 + ((pq * 8) ^ ((row & 8) * 2));
  const unsigned vl16 = lane * 16;
  const unsigned vdma = OFF_X2P + p * 81920u + w * 4096u + lane * 16;
  const unsigned vh2st = lane < 48 ? OFF_H2F + (4 * p * 192 + 12 * j + 3 * w) * 256u + lane * 16 : OOB;
  const unsigned tk = 4 * j + w;
  const unsigned vstc = lb + lane * 128 + (((tk >> 4) ^ ((lane >> 1) & 3)) * 32) + (((tk & 15) * 2) ^ ((lane & 8) * 2));
  const unsigned vstw = lane == 63 ? tk * 64 : OOB;
  const unsigned vflw = lane < 8 ? OFF_FL + p * 32768u + (j * 4 + w) * 128u + lane * 16 : OOB;
  const unsigned vep = b == 0 && tid == 0 ? OFF_EP : OOB;
  const unsigned rs_ = 2 * (w & 1) + sub, rb = lb + (w >> 1) * 81920 + 80 * ng * 128;
  const unsigned r0 = 8 * g + q, r1 = 8 * g + 4 + q, r2 = 64 + 4 * g + q;
  const unsigned vres0 = rb + r0 * 128 + ((rs_ ^ ((r0 >> 1) & 3)) * 32) + ((pq * 8) ^ ((r0 & 8) * 2));
  const unsigned vres1 = rb + r1 * 128 + ((rs_ ^ ((r1 >> 1) & 3)) * 32) + ((pq * 8) ^ ((r1 & 8) * 2));
  const unsigned vres2 = rb + r2 * 128 + ((rs_ ^ ((r2 >> 1) & 3)) * 32) + ((pq * 8) ^ ((r2 & 8) * 2));
  const unsigned vwr = lb + w * 20480 + g * 256 + ((lane + 4 * g) & 15) * 16;
  const unsigned tq = lane < 32 ? q : (lane >> 1) & 3, tl = 4 * w + tq, g0 = lane < 32 ? pq : 2 * (lane & 1), g1 = lane < 32 ? pq : g0 + 1;
  const unsigned vrd0 = lb + (lane < 32 ? g * 2048 : 4096) + g0 * 256 + ((tl + 4 * g0) & 15) * 16;
  const unsigned vrd1 = lb + (lane < 32 ? g * 2048 + 1024 : 4096) + g1 * 256 + ((tl + 4 * g1) & 15) * 16;
  const unsigned vh1w = lane < 20 ? OFF_X2P + (16 + p) * 81920u + w * 4096u + (lane >> 2) * 16384 + ((lane & 3) * 8 + ng) * 128 : OOB;
  const unsigned vgb = OFF_GB - OFF_RT + (lane < 32 ? 32 * g + 8 * pq : 64 + 8 * (lane & 1)) * 4;
  const unsigned vout = lane < 40 ? ((64 * p + 16 * sub + tl) * D_ + 80 * ng + (lane < 32 ? 32 * g + 8 * pq : 64 + 8 * (lane & 1))) * 2 : OOB;
  asm volatile(
  "v_mov_b32 v132, %[tlo]\n\t"
  "v_mov_b32 v133, %[thi]\n\t"
  "v_mov_b32 v134, %[tlo]\n\t"
  "v_mov_b32 v135, %[thi]\n\t"
  "s_lshl_b32 s52, %[w], 12\n\t"
  "s_waitcnt vmcnt(0) lgkmcnt(0)\n\t"
  "s_barrier\n\t"
  "buffer_store_dwordx4 v[132:135], %[vpub], %[srd], 0 offen sc1\n\t"
  "s_bitcmp1_b32 %[wdone], 1\n\t"
  "s_cbranch_scc1 Lwskip2%=\n\t"
  "s_mov_b32 s49, 0x40000\n\t"
  "Lpw_loop%=:\n\t"
  "buffer_load_dwordx2 v[112:113], %[vpw], %[srd], 0 offen sc1\n\t"
  "s_waitcnt vmcnt(0)\n\t"
  "v_cmp_eq_u64 vcc, v[132:133], v[112:113]\n\t"
  "s_nop 3\n\t"
  "s_orn2_b64 vcc, vcc, 15\n\t"
  "s_cmp_eq_u64 vcc, -1\n\t"
  "s_cbranch_scc1 Lpw_done%=\n\t"
  "s_sleep 3\n\t"
  "s_sub_u32 s49, s49, 1\n\t"
  "s_cmp_lg_u32 s49, 0\n\t"
  "s_cbranch_scc1 Lpw_loop%=\n\t"
  "Lpw_done%=:\n\t"
  "s_add_u32 s44, %[sW1], 20480\n\t"
  "buffer_load_dwordx4 a[80:83], %[vl16], %[srd], s44 offen offset:0 sc1\n\t"
  "s_add_u32 s44, %[sW1], 20480\n\t"
  "buffer_load_dwordx4 a[84:87], %[vl16], %[srd], s44 offen offset:1024 sc1\n\t"
  "s_add_u32 s44, %[sW1], 20480\n\t"
  "buffer_load_dwordx4 a[88:91], %[vl16], %[srd], s44 offen offset:2048 sc1\n\t"
  "s_add_u32 s44, %[sW1], 20480\n\t"
  "buffer_load_dwordx4 a[92:95], %[vl16], %[srd], s44 offen offset:3072 sc1\n\t"
  "s_add_u32 s44, %[sW1], 24576\n\t"
  "buffer_load_dwordx4 a[96:99], %[vl16], %[srd], s44 offen offset:0 sc1\n\t"
  "s_add_u32 s44, %[sW1], 24576\n\t"
  "buffer_load_dwordx4 a[100:103], %[vl16], %[srd], s44 offen offset:1024 sc1\n\t"
  "s_add_u32 s44, %[sW1], 24576\n\t"
  "buffer_load_dwordx4 a[104:107], %[vl16], %[srd], s44 offen offset:2048 sc1\n\t"
  "s_add_u32 s44, %[sW1], 24576\n\t"
  "buffer_load_dwordx4 a[108:111], %[vl16], %[srd], s44 offen offset:3072 sc1\n\t"
  "s_add_u32 s44, %[sW1], 28672\n\t"
  "buffer_load_dwordx4 a[112:115], %[vl16], %[srd], s44 offen offset:0 sc1\n\t"
  "s_add_u32 s44, %[sW1], 28672\n\t"
  "buffer_load_dwordx4 a[116:119], %[vl16], %[srd], s44 offen offset:1024 sc1\n\t"
  "s_add_u32 s44, %[sW1], 28672\n\t"
  "buffer_load_dwordx4 a[120:123], %[vl16], %[srd], s44 offen offset:2048 sc1\n\t"
  "s_add_u32 s44, %[sW1], 28672\n\t"
  "buffer_load_dwordx4 a[124:127], %[vl16], %[srd], s44 offen offset:3072 sc1\n\t"
  "s_add_u32 s44, %[sW1], 32768\n\t"
  "buffer_load_dwordx4 a[128:131], %[vl16], %[srd], s44 offen offset:0 sc1\n\t"
  "s_add_u32 s44, %[sW1], 32768\n\t"
  "buffer_load_dwordx4 a[132:135], %[vl16], %[srd], s44 offen offset:1024 sc1\n\t"
  "s_add_u32 s44, %[sW1], 32768\n\t"
  "buffer_load_dwordx4 a[136:139], %[vl16], %[srd], s44 offen offset:2048 sc1\n\t"
  "s_add_u32 s44, %[sW1], 32768\n\t"
  "buffer_load_dwordx4 a[140:143], %[vl16], %[srd], s44 offen offset:3072 sc1\n\t"
  "s_add_u32 s44, %[sW1], 36864\n\t"
  "buffer_load_dwordx4 a[144:147], %[vl16], %[srd], s44 offen offset:0 sc1\n\t"
  "s_add_u32 s44, %[sW1], 36864\n\t"
  "buffer_load_dwordx4 a[148:151], %[vl16], %[srd], s44 offen offset:1024 sc1\n\t"
  "s_add_u32 s44, %[sW1], 36864\n\t"
  "buffer_load_dwordx4 a[152:155], %[vl16], %[srd], s44 offen offset:2048 sc1\n\t"
  "s_add_u32 s44, %[sW1], 36864\n\t"
  "buffer_load_dwordx4 a[156:159], %[vl16], %[srd], s44 offen offset:3072 sc1\n\t"
  "Lwskip2%=:\n\t"
  "s_bitcmp1_b32 %[wdone], 0\n\t"
  "s_cbranch_scc1 Lwskip%=\n\t"
  "s_add_u32 s44, %[sW1], 0\n\t"
  "buffer_load_dwordx4 a[0:3], %[vl16], %[srd], s44 offen offset:0 sc1\n\t"
  "s_add_u32 s44, %[sW1], 0\n\t"
  "buffer_load_dwordx4 a[4:7], %[vl16], %[srd], s44 offen offset:1024 sc1\n\t"
  "s_add_u32 s44, %[sW1], 0\n\t"
  "buffer_load_dwordx4 a[8:11], %[vl16], %[srd], s44 offen offset:2048 sc1\n\t"
  "s_add_u32 s44, %[sW1], 0\n\t"
  "buffer_load_dwordx4 a[12:15], %[vl16], %[srd], s44 offen offset:3072 sc1\n\t"
  "s_add_u32 s44, %[sW1], 4096\n\t"
  "buffer_load_dwordx4 a[16:19], %[vl16], %[srd], s44 offen offset:0 sc1\n\t"
  "s_add_u32 s44, %[sW1], 4096\n\t"
  "buffer_load_dwordx4 a[20:23], %[vl16], %[srd], s44 offen offset:1024 sc1\n\t"
  "s_add_u32 s44, %[sW1], 4096\n\t"
  "buffer_load_dwordx4 a[24:27], %[vl16], %[srd], s44 offen offset:2048 sc1\n\t"
  "s_add_u32 s44, %[sW1], 4096\n\t"
  "buffer_load_dwordx4 a[28:31], %[vl16], %[srd], s44 offen offset:3072 sc1\n\t"
  "s_add_u32 s44, %[sW1], 8192\n\t"
  "buffer_load_dwordx4 a[32:35], %[vl16], %[srd], s44 offen offset:0 sc1\n\t"
  "s_add_u32 s44, %[sW1], 8192\n\t"
  "buffer_load_dwordx4 a[36:39], %[vl16], %[srd], s44 offen offset:1024 sc1\n\t"
  "s_add_u32 s44, %[sW1], 8192\n\t"
  "buffer_load_dwordx4 a[40:43], %[vl16], %[srd], s44 offen offset:2048 sc1\n\t"
  "s_add_u32 s44, %[sW1], 8192\n\t"
  "buffer_load_dwordx4 a[44:47], %[vl16], %[srd], s44 offen offset:3072 sc1\n\t"
  "s_add_u32 s44, %[sW1], 12288\n\t"
  "buffer_load_dwordx4 a[48:51], %[vl16], %[srd], s44 offen offset:0 sc1\n\t"
  "s_add_u32 s44, %[sW1], 12288\n\t"
  "buffer_load_dwordx4 a[52:55], %[vl16], %[srd], s44 offen offset:1024 sc1\n\t"
  "s_add_u32 s44, %[sW1], 12288\n\t"
  "buffer_load_dwordx4 a[56:59], %[vl16], %[srd], s44 offen offset:2048 sc1\n\t"
  "s_add_u32 s44, %[sW1], 12288\n\t"
  "buffer_load_dwordx4 a[60:63], %[vl16], %[srd], s44 offen offset:3072 sc1\n\t"
  "s_add_u32 s44, %[sW1], 16384\n\t"
  "buffer_load_dwordx4 a[64:67], %[vl16], %[srd], s44 offen offset:0 sc1\n\t"
  "s_add_u32 s44, %[sW1], 16384\n\t"
  "buffer_load_dwordx4 a[68:71], %[vl16], %[srd], s44 offen offset:1024 sc1\n\t"
  "s_add_u32 s44, %[sW1], 16384\n\t"
  "buffer_load_dwordx4 a[72:75], %[vl16], %[srd], s44 offen offset:2048 sc1\n\t"
  "s_add_u32 s44, %[sW1], 16384\n\t"
  "buffer_load_dwordx4 a[76:79], %[vl16], %[srd], s44 offen offset:3072 sc1\n\t"
  "Lwskip%=:\n\t"
  "s_bitcmp1_b32 %[wdone], 2\n\t"
  "s_cbranch_scc1 Lwskip3%=\n\t"
  "s_add_u32 s44, %[sW1], 40960\n\t"
  "buffer_load_dwordx4 a[160:163], %[vl16], %[srd], s44 offen offset:0 sc1\n\t"
  "s_add_u32 s44, %[sW1], 40960\n\t"
  "buffer_load_dwordx4 a[164:167], %[vl16], %[srd], s44 offen offset:1024 sc1\n\t"
  "s_add_u32 s44, %[sW1], 40960\n\t"
  "buffer_load_dwordx4 a[168:171], %[vl16], %[srd], s44 offen offset:2048 sc1\n\t"
  "s_add_u32 s44, %[sW1], 40960\n\t"
  "buffer_load_dwordx4 a[172:175], %[vl16], %[srd], s44 offen offset:3072 sc1\n\t"
  "s_add_u32 s44, %[sW1], 45056\n\t"
  "buffer_load_dwordx4 a[176:179], %[vl16], %[srd], s44 offen offset:0 sc1\n\t"
  "s_add_u32 s44, %[sW1], 45056\n\t"
  "buffer_load_dwordx4 a[180:183], %[vl16], %[srd], s44 offen offset:1024 sc1\n\t"
  "s_add_u32 s44, %[sW1], 45056\n\t"
  "buffer_load_dwordx4 a[184:187], %[vl16], %[srd], s44 offen offset:2048 sc1\n\t"
  "s_add_u32 s44, %[sW1], 45056\n\t"
  "buffer_load_dwordx4 a[188:191], %[vl16], %[srd], s44 offen offset:3072 sc1\n\t"
  "s_add_u32 s44, %[sW1], 49152\n\t"
  "buffer_load_dwordx4 a[192:195], %[vl16], %[srd], s44 offen offset:0 sc1\n\t"
  "s_add_u32 s44, %[sW1], 49152\n\t"
  "buffer_load_dwordx4 a[196:199], %[vl16], %[srd], s44 offen offset:1024 sc1\n\t"
  "s_add_u32 s44, %[sW1], 49152\n\t"
  "buffer_load_dwordx4 a[200:203], %[vl16], %[srd], s44 offen offset:2048 sc1\n\t"
  "s_add_u32 s44, %[sW1], 49152\n\t"
  "buffer_load_dwordx4 a[204:207], %[vl16], %[srd], s44 offen offset:3072 sc1\n\t"
  "Lwskip3%=:\n\t"
  "v_mul_u32_u24 v139, 3, %[vl16]\n\t"
  "s_mov_b32 s47, 117760\n\t"
  "s_mov_b32 s49, 0x40000\n\t"
  "Lpx_loop%=:\n\t"
  "buffer_load_dwordx2 v[112:113], %[vpx], %[srd], 0 offen sc1\n\t"
  "s_waitcnt vmcnt(0)\n\t"
  "v_cmp_eq_u64 vcc, v[132:133], v[112:113]\n\t"
  "s_nop 3\n\t"
  "s_cmp_eq_u64 vcc, -1\n\t"
  "s_cbranch_scc1 Lpx_done%=\n\t"
  "s_sleep 3\n\t"
  "s_sub_u32 s49, s49, 1\n\t"
  "s_cmp_lg_u32 s49, 0\n\t"
  "s_cbranch_scc1 Lpx_loop%=\n\t"
  "Lpx_done%=:\n\t"
  "s_add_u32 m0, s52, 0\n\t"
  "s_mov_b32 s46, 0\n\t"
  "buffer_load_dwordx4 %[vdma], %[srd], s46 offen offset:0 sc1 lds\n\t"
  "buffer_load_dwordx4 %[vdma], %[srd], s46 offen offset:1024 sc1 lds\n\t"
  "buffer_load_dwordx4 %[vdma], %[srd], s46 offen offset:2048 sc1 lds\n\t"
  "buffer_load_dwordx4 %[vdma], %[srd], s46 offen offset:3072 sc1 lds\n\t"
  "s_add_u32 m0, s52, 16384\n\t"
  "s_mov_b32 s46, 16384\n\t"
  "buffer_load_dwordx4 %[vdma], %[srd], s46 offen offset:0 sc1 lds\n\t"
  "buffer_load_dwordx4 %[vdma], %[srd], s46 offen offset:1024 sc1 lds\n\t"
  "buffer_load_dwordx4 %[vdma], %[srd], s46 offen offset:2048 sc1 lds\n\t"
  "buffer_load_dwordx4 %[vdma], %[srd], s46 offen offset:3072 sc1 lds\n\t"
  "s_add_u32 m0, s52, 32768\n\t"
  "s_mov_b32 s46, 32768\n\t"
  "buffer_load_dwordx4 %[vdma], %[srd], s46 offen offset:0 sc1 lds\n\t"
  "buffer_load_dwordx4 %[vdma], %[srd], s46 offen offset:1024 sc1 lds\n\t"
  "buffer_load_dwordx4 %[vdma], %[srd], s46 offen offset:2048 sc1 lds\n\t"
  "buffer_load_dwordx4 %[vdma], %[srd], s46 offen offset:3072 sc1 lds\n\t"
  "s_add_u32 m0, s52, 49152\n\t"
  "s_mov_b32 s46, 49152\n\t"
  "buffer_load_dwordx4 %[vdma], %[srd], s46 offen offset:0 sc1 lds\n\t"
  "buffer_load_dwordx4 %[vdma], %[srd], s46 offen offset:1024 sc1 lds\n\t"
  "buffer_load_dwordx4 %[vdma], %[srd], s46 offen offset:2048 sc1 lds\n\t"
  "buffer_load_dwordx4 %[vdma], %[srd], s46 offen offset:3072 sc1 lds\n\t"
  "s_add_u32 m0, s52, 65536\n\t"
  "s_mov_b32 s46, 65536\n\t"
  "buffer_load_dwordx4 %[vdma], %[srd], s46 offen offset:0 sc1 lds\n\t"
  "buffer_load_dwordx4 %[vdma], %[srd], s46 offen offset:1024 sc1 lds\n\t"
  "buffer_load_dwordx4 %[vdma], %[srd], s46 offen offset:2048 sc1 lds\n\t"
  "buffer_load_dwordx4 %[vdma], %[srd], s46 offen offset:3072 sc1 lds\n\t"
  "buffer_load_dwordx2 v[142:143], v139, %[srd], s47 offen sc1\n\t"
  "buffer_load_dwordx4 v[248:251], v139, %[srd], s47 offen offset:16 sc1\n\t"
  "buffer_load_dwordx4 v[252:255], v139, %[srd], s47 offen offset:32 sc1\n\t"
  "s_add_u32 s44, %[sW1], 53248\n\t"
  "buffer_load_dwordx4 a[208:211], %[vl16], %[srd], s44 offen offset:0 sc1\n\t"
  "s_add_u32 s44, %[sW1], 53248\n\t"
  "buffer_load_dwordx4 a[212:215], %[vl16], %[srd], s44 offen offset:1024 sc1\n\t"
  "s_add_u32 s44, %[sW1], 53248\n\t"
  "buffer_load_dwordx4 a[216:219], %[vl16], %[srd], s44 offen offset:2048 sc1\n\t"
  "s_add_u32 s44, %[sW1], 53248\n\t"
  "buffer_load_dwordx4 a[220:223], %[vl16], %[srd], s44 offen offset:3072 sc1\n\t"
  "s_add_u32 s44, %[sW1], 57344\n\t"
  "buffer_load_dwordx4 a[224:227], %[vl16], %[srd], s44 offen offset:0 sc1\n\t"
  "s_add_u32 s44, %[sW1], 57344\n\t"
  "buffer_load_dwordx4 a[228:231], %[vl16], %[srd], s44 offen offset:1024 sc1\n\t"
  "s_add_u32 s44, %[sW1], 57344\n\t"
  "buffer_load_dwordx4 a[232:235], %[vl16], %[srd], s44 offen offset:2048 sc1\n\t"
  "s_add_u32 s44, %[sW1], 57344\n\t"
  "buffer_load_dwordx4 a[236:239], %[vl16], %[srd], s44 offen offset:3072 sc1\n\t"
  "v_add_u32 v124, 40960, %[vstc]\n\t"
  "v_mov_b32 v112, 0\n\t"
  "v_mov_b32 v113, 0\n\t"
  "v_mov_b32 v114, 0\n\t"
  "v_mov_b32 v115, 0\n\t"
  "v_mov_b32 v116, 0\n\t"
  "v_mov_b32 v117, 0\n\t"
  "v_mov_b32 v118, 0\n\t"
  "v_mov_b32 v119, 0\n\t"
  "v_mov_b32 v120, 0\n\t"
  "v_mov_b32 v121, 0\n\t"
  "v_and_b32 v136, 0xf0, %[vl16]\n\t"
  "v_lshlrev_b32 v137, 3, v136\n\t"
  "v_lshlrev_b32 v136, 2, v136\n\t"
  "s_add_u32 s54, %[sST], 65536\n\t"
  "s_mul_i32 s48, %[w], 20480\n\t"
  "v_add_u32 v141, s48, %[vl16]\n\t"
  "v_xor_b32 v109, 32, %[vtr]\n\t"
  "v_mov_b32 v108, %[vtr]\n\t"
  "s_waitcnt vmcnt(11)\n\t"
  "s_barrier\n\t"
  "ds_read_b64_tr_b16 v[68:69], v108 offset:0\n\t"
  "ds_read_b64_tr_b16 v[70:71], v108 offset:2048\n\t"
  "ds_read_b64_tr_b16 v[72:73], v109 offset:0\n\t"
  "ds_read_b64_tr_b16 v[74:75], v109 offset:2048\n\t"
  "ds_read_b64_tr_b16 v[76:77], v108 offset:4096\n\t"
  "ds_read_b64_tr_b16 v[78:79], v108 offset:6144\n\t"
  "ds_read_b64_tr_b16 v[80:81], v109 offset:4096\n\t"
  "ds_read_b64_tr_b16 v[82:83], v109 offset:6144\n\t"
  "ds_read_u16_d16_hi v112, %[vstc] offset:0\n\t"
  "ds_read_u16_d16_hi v113, %[vstc] offset:8192\n\t"
  "ds_read_u16_d16_hi v114, %[vstc] offset:16384\n\t"
  "ds_read_u16_d16_hi v115, %[vstc] offset:24576\n\t"
  "ds_read_u16_d16_hi v116, %[vstc] offset:32768\n\t"
  "ds_read_u16_d16_hi v117, v124 offset:0\n\t"
  "ds_read_u16_d16_hi v118, v124 offset:8192\n\t"
  "ds_read_u16_d16_hi v119, v124 offset:16384\n\t"
  "ds_read_u16_d16_hi v120, v124 offset:24576\n\t"
  "ds_read_u16_d16_hi v121, v124 offset:32768\n\t"
  "s_waitcnt lgkmcnt(15)\n\t"
  "v_mfma_f32_16x16x32_bf16 v[20:23], a[0:3], v[68:71], 0\n\t"
  "ds_read_b64_tr_b16 v[84:85], v108 offset:8192\n\t"
  "v_add_u32 v110, 40960, %[vtr]\n\t"
  "v_mfma_f32_16x16x32_bf16 v[24:27], a[4:7], v[68:71], 0\n\t"
  "ds_read_b64_tr_b16 v[86:87], v108 offset:10240\n\t"
  "v_xor_b32 v111, 32, %[vtr]\n\t"
  "v_mfma_f32_16x16x32_bf16 v[28:31], a[8:11], v[68:71], 0\n\t"
  "ds_read_b64_tr_b16 v[88:89], v109 offset:8192\n\t"
  "v_add_u32 v111, 40960, v111\n\t"
  "s_waitcnt lgkmcnt(15)\n\t"
  "v_mfma_f32_16x16x32_bf16 v[32:35], a[0:3], v[72:75], 0\n\t"
  "ds_read_b64_tr_b16 v[90:91], v109 offset:10240\n\t"
  "s_add_u32 m0, s52, 81920\n\t"
  "v_mfma_f32_16x16x32_bf16 v[36:39], a[4:7], v[72:75], 0\n\t"
  "s_mov_b32 s46, 1310720\n\t"
  "buffer_load_dwordx4 %[vdma], %[srd], s46 offen offset:0 sc1 lds\n\t"
  "v_mfma_f32_16x16x32_bf16 v[40:43], a[8:11], v[72:75], 0\n\t"
  "s_waitcnt lgkmcnt(14)\n\t"
  "v_mfma_f32_16x16x32_bf16 v[20:23], a[12:15], v[76:79], v[20:23]\n\t"
  "ds_read_b64_tr_b16 v[68:69], v108 offset:12288\n\t"
  "v_mfma_f32_16x16x32_bf16 v[24:27], a[16:19], v[76:79], v[24:27]\n\t"
  "ds_read_b64_tr_b16 v[70:71], v108 offset:14336\n\t"
  "buffer_load_dwordx4 %[vdma], %[srd], s46 offen offset:1024 sc1 lds\n\t"
  "v_mfma_f32_16x16x32_bf16 v[28:31], a[20:23], v[76:79], v[28:31]\n\t"
  "ds_read_b64_tr_b16 v[72:73], v109 offset:12288\n\t"
  "v_mfma_f32_16x16x32_bf16 v[32:35], a[12:15], v[80:83], v[32:35]\n\t"
  "ds_read_b64_tr_b16 v[74:75], v109 offset:14336\n\t"
  "s_waitcnt vmcnt(12) lgkmcnt(15)\n\t"
  "v_mul_f32 v112, v112, v142\n\t"
  "v_mul_f32 v113, v113, v143\n\t"
  "v_mfma_f32_16x16x32_bf16 v[36:39], a[16:19], v[80:83], v[36:39]\n\t"
  "s_waitcnt vmcnt(11)\n\t"
  "v_mul_f32 v114, v114, v248\n\t"
  "s_waitcnt lgkmcnt(14)\n\t"
  "v_mul_f32 v115, v115, v249\n\t"
  "v_mfma_f32_16x16x32_bf16 v[40:43], a[20:23], v[80:83], v[40:43]\n\t"
  "s_waitcnt lgkmcnt(4)\n\t"
  "v_mul_f32 v116, v116, v250\n\t"
  "v_mul_f32 v117, v117, v251\n\t"
  "s_waitcnt vmcnt(10)\n\t"
  "v_mul_f32 v118, v118, v252\n\t"
  "v_mfma_f32_16x16x32_bf16 v[20:23], a[24:27], v[84:87], v[20:23]\n\t"
  "ds_read_b64_tr_b16 v[76:77], v108 offset:16384\n\t"
  "v_mul_f32 v119, v119, v253\n\t"
  "v_mul_f32 v120, v120, v254\n\t"
  "v_mul_f32 v121, v121, v255\n\t"
  "v_mfma_f32_16x16x32_bf16 v[24:27], a[28:31], v[84:87], v[24:27]\n\t"
  "ds_read_b64_tr_b16 v[78:79], v108 offset:18432\n\t"
  "v_add_f32 v122, v112, v113\n\t"
  "v_mul_f32 v123, v112, v112\n\t"
  "v_fmac_f32 v123, v113, v113\n\t"
  "v_mfma_f32_16x16x32_bf16 v[28:31], a[32:35], v[84:87], v[28:31]\n\t"
  "ds_read_b64_tr_b16 v[80:81], v109 offset:16384\n\t"
  "v_add_f32 v122, v122, v114\n\t"
  "v_fmac_f32 v123, v114, v114\n\t"
  "v_add_f32 v122, v122, v115\n\t"
  "v_mfma_f32_16x16x32_bf16 v[32:35], a[24:27], v[88:91], v[32:35]\n\t"
  "ds_read_b64_tr_b16 v[82:83], v109 offset:18432\n\t"
  "v_fmac_f32 v123, v115, v115\n\t"
  "v_add_f32 v122, v122, v116\n\t"
  "v_fmac_f32 v123, v116, v116\n\t"
  "v_mfma_f32_16x16x32_bf16 v[36:39], a[28:31], v[88:91], v[36:39]\n\t"
  "v_add_f32 v122, v122, v117\n\t"
  "v_fmac_f32 v123, v117, v117\n\t"
  "v_add_f32 v122, v122, v118\n\t"
  "v_fmac_f32 v123, v118, v118\n\t"
  "v_mfma_f32_16x16x32_bf16 v[40:43], a[32:35], v[88:91], v[40:43]\n\t"
  "s_waitcnt lgkmcnt(4)\n\t"
  "v_add_f32 v122, v122, v119\n\t"
  "v_fmac_f32 v123, v119, v119\n\t"
  "v_add_f32 v122, v122, v120\n\t"
  "v_mfma_f32_16x16x32_bf16 v[20:23], a[36:39], v[68:71], v[20:23]\n\t"
  "ds_read_b64_tr_b16 v[84:85], v108 offset:20480\n\t"
  "v_fmac_f32 v123, v120, v120\n\t"
  "v_add_f32 v122, v122, v121\n\t"
  "v_fmac_f32 v123, v121, v121\n\t"
  "v_mfma_f32_16x16x32_bf16 v[24:27], a[40:43], v[68:71], v[24:27]\n\t"
  "ds_read_b64_tr_b16 v[86:87], v108 offset:22528\n\t"
  "v_add_f32_dpp v122, v122, v122 quad_perm:[1,0,3,2] row_mask:0xf bank_mask:0xf\n\t"
  "v_add_f32_dpp v123, v123, v123 quad_perm:[1,0,3,2] row_mask:0xf bank_mask:0xf\n\t"
  "buffer_load_dwordx4 %[vdma], %[srd], s46 offen offset:2048 sc1 lds\n\t"
  "v_mfma_f32_16x16x32_bf16 v[28:31], a[44:47], v[68:71], v[28:31]\n\t"
  "ds_read_b64_tr_b16 v[88:89], v109 offset:20480\n\t"
  "v_add_f32_dpp v122, v122, v122 quad_perm:[2,3,0,1] row_mask:0xf bank_mask:0xf\n\t"
  "v_add_f32_dpp v123, v123, v123 quad_perm:[2,3,0,1] row_mask:0xf bank_mask:0xf\n\t"
  "buffer_load_dwordx4 %[vdma], %[srd], s46 offen offset:3072 sc1 lds\n\t"
  "v_mfma_f32_16x16x32_bf16 v[32:35], a[36:39], v[72:75], v[32:35]\n\t"
  "ds_read_b64_tr_b16 v[90:91], v109 offset:22528\n\t"
  "v_add_f32_dpp v122, v122, v122 row_half_mirror row_mask:0xf bank_mask:0xf\n\t"
  "v_add_f32_dpp v123, v123, v123 row_half_mirror row_mask:0xf bank_mask:0xf\n\t"
  "s_add_u32 m0, s52, 98304\n\t"
  "v_mfma_f32_16x16x32_bf16 v[36:39], a[40:43], v[72:75], v[36:39]\n\t"
  "v_add_f32_dpp v122, v122, v122 row_mirror row_mask:0xf bank_mask:0xf\n\t"
  "v_add_f32_dpp v123, v123, v123 row_mirror row_mask:0xf bank_mask:0xf\n\t"
  "s_mov_b32 s46, 1327104\n\t"
  "v_add_f32_dpp v122, v122, v122 row_bcast:15 row_mask:0xa bank_mask:0xf\n\t"
  "v_mfma_f32_16x16x32_bf16 v[40:43], a[44:47], v[72:75], v[40:43]\n\t"
  "s_waitcnt lgkmcnt(4)\n\t"
  "v_add_f32_dpp v123, v123, v123 row_bcast:15 row_mask:0xa bank_mask:0xf\n\t"
  "v_add_f32_dpp v122, v122, v122 row_bcast:31 row_mask:0xc bank_mask:0xf\n\t"
  "buffer_load_dwordx4 %[vdma], %[srd], s46 offen offset:0 sc1 lds\n\t"
  "v_mfma_f32_16x16x32_bf16 v[20:23], a[48:51], v[76:79], v[20:23]\n\t"
  "ds_read_b64_tr_b16 v[68:69], v108 offset:24576\n\t"
  "v_add_f32_dpp v123, v123, v123 row_bcast:31 row_mask:0xc bank_mask:0xf\n\t"
  "v_mul_f32 v122, 0xbacccccd, v122\n\t"
  "v_mul_f32 v123, 0x3acccccd, v123\n\t"
  "v_mfma_f32_16x16x32_bf16 v[24:27], a[52:55], v[76:79], v[24:27]\n\t"
  "ds_read_b64_tr_b16 v[70:71], v108 offset:26624\n\t"
  "v_fma_f32 v123, -v122, v122, v123\n\t"
  "v_max_f32 v123, 0, v123\n\t"
  "v_cvt_pk_bf16_f32 v123, 0, v123\n\t"
  "v_mfma_f32_16x16x32_bf16 v[28:31], a[56:59], v[76:79], v[28:31]\n\t"
  "ds_read_b64_tr_b16 v[72:73], v109 offset:24576\n\t"
  "v_cvt_pk_bf16_f32 v126, v122, v122\n\t"
  "v_add_f32 v123, 0x3727c5ac, v123\n\t"
  "v_cvt_pk_bf16_f32 v123, 0, v123\n\t"
  "v_mfma_f32_16x16x32_bf16 v[32:35], a[48:51], v[80:83], v[32:35]\n\t"
  "ds_read_b64_tr_b16 v[74:75], v109 offset:26624\n\t"
  "v_sqrt_f32 v123, v123\n\t"
  "buffer_load_dwordx4 %[vdma], %[srd], s46 offen offset:1024 sc1 lds\n\t"
  "v_cvt_pk_bf16_f32 v127, v123, v123\n\t"
  "v_mfma_f32_16x16x32_bf16 v[36:39], a[52:55], v[80:83], v[36:39]\n\t"
  "buffer_store_dwordx2 v[126:127], %[vstw], %[srd], %[sST] offen sc1\n\t"
  "buffer_load_dwordx4 %[vdma], %[srd], s46 offen offset:2048 sc1 lds\n\t"
  "buffer_load_dwordx4 %[vdma], %[srd], s46 offen offset:3072 sc1 lds\n\t"
  "s_add_u32 m0, s52, 114688\n\t"
  "v_mfma_f32_16x16x32_bf16 v[40:43], a[56:59], v[80:83], v[40:43]\n\t"
  "s_waitcnt lgkmcnt(4)\n\t"
  "v_add_u32 v124, 122880, %[vstc]\n\t"
  "v_mfma_f32_16x16x32_bf16 v[20:23], a[60:63], v[84:87], v[20:23]\n\t"
  "ds_read_b64_tr_b16 v[76:77], v108 offset:28672\n\t"
  "v_add_u32 v125, 81920, %[vstc]\n\t"
  "v_mfma_f32_16x16x32_bf16 v[24:27], a[64:67], v[84:87], v[24:27]\n\t"
  "ds_read_b64_tr_b16 v[78:79], v108 offset:30720\n\t"
  "s_mov_b32 s46, 1343488\n\t"
  "v_mfma_f32_16x16x32_bf16 v[28:31], a[68:71], v[84:87], v[28:31]\n\t"
  "ds_read_b64_tr_b16 v[80:81], v109 offset:28672\n\t"
  "v_mov_b32 v112, 0\n\t"
  "v_mfma_f32_16x16x32_bf16 v[32:35], a[60:63], v[88:91], v[32:35]\n\t"
  "ds_read_b64_tr_b16 v[82:83], v109 offset:30720\n\t"
  "buffer_load_dwordx4 %[vdma], %[srd], s46 offen offset:0 sc1 lds\n\t"
  "v_mfma_f32_16x16x32_bf16 v[36:39], a[64:67], v[88:91], v[36:39]\n\t"
  "buffer_load_dwordx2 v[138:139], %[vpw], %[srd], 0 offen sc1\n\t"
  "v_mov_b32 v113, 0\n\t"
  "v_mfma_f32_16x16x32_bf16 v[40:43], a[68:71], v[88:91], v[40:43]\n\t"
  "s_waitcnt lgkmcnt(4)\n\t"
  "v_mov_b32 v114, 0\n\t"
  "v_mfma_f32_16x16x32_bf16 v[20:23], a[72:75], v[68:71], v[20:23]\n\t"
  "ds_read_b64_tr_b16 v[84:85], v108 offset:32768\n\t"
  "buffer_load_dwordx4 %[vdma], %[srd], s46 offen offset:1024 sc1 lds\n\t"
  "v_mfma_f32_16x16x32_bf16 v[24:27], a[76:79], v[68:71], v[24:27]\n\t"
  "ds_read_b64_tr_b16 v[86:87], v108 offset:34816\n\t"
  "v_mov_b32 v115, 0\n\t"
  "v_mfma_f32_16x16x32_bf16 v[28:31], a[80:83], v[68:71], v[28:31]\n\t"
  "ds_read_b64_tr_b16 v[88:89], v109 offset:32768\n\t"
  "buffer_load_dwordx4 %[vdma], %[srd], s46 offen offset:2048 sc1 lds\n\t"
  "v_mfma_f32_16x16x32_bf16 v[32:35], a[72:75], v[72:75], v[32:35]\n\t"
  "ds_read_b64_tr_b16 v[90:91], v109 offset:34816\n\t"
  "v_mov_b32 v116, 0\n\t"
  "v_mfma_f32_16x16x32_bf16 v[36:39], a[76:79], v[72:75], v[36:39]\n\t"
  "buffer_load_dwordx4 %[vdma], %[srd], s46 offen offset:3072 sc1 lds\n\t"
  "v_mov_b32 v117, 0\n\t"
  "v_mfma_f32_16x16x32_bf16 v[40:43], a[80:83], v[72:75], v[40:43]\n\t"
  "s_waitcnt lgkmcnt(4)\n\t"
  "v_mov_b32 v118, 0\n\t"
  "v_mfma_f32_16x16x32_bf16 v[20:23], a[84:87], v[76:79], v[20:23]\n\t"
  "ds_read_b64_tr_b16 v[68:69], v108 offset:36864\n\t"
  "v_mov_b32 v119, 0\n\t"
  "v_mfma_f32_16x16x32_bf16 v[24:27], a[88:91], v[76:79], v[24:27]\n\t"
  "ds_read_b64_tr_b16 v[70:71], v108 offset:38912\n\t"
  "s_add_u32 m0, s52, 131072\n\t"
  "v_mfma_f32_16x16x32_bf16 v[28:31], a[92:95], v[76:79], v[28:31]\n\t"
  "ds_read_b64_tr_b16 v[72:73], v109 offset:36864\n\t"
  "v_mov_b32 v120, 0\n\t"
  "v_mfma_f32_16x16x32_bf16 v[32:35], a[84:87], v[80:83], v[32:35]\n\t"
  "ds_read_b64_tr_b16 v[74:75], v109 offset:38912\n\t"
  "s_mov_b32 s46, 1359872\n\t"
  "v_mfma_f32_16x16x32_bf16 v[36:39], a[88:91], v[80:83], v[36:39]\n\t"
  "v_mov_b32 v121, 0\n\t"
  "v_mfma_f32_16x16x32_bf16 v[40:43], a[92:95], v[80:83], v[40:43]\n\t"
  "s_waitcnt lgkmcnt(4)\n\t"
  "v_mfma_f32_16x16x32_bf16 v[20:23], a[96:99], v[84:87], v[20:23]\n\t"
  "ds_read_b64_tr_b16 v[76:77], v110 offset:0\n\t"
  "buffer_load_dwordx4 %[vdma], %[srd], s46 offen offset:0 sc1 lds\n\t"
  "v_mfma_f32_16x16x32_bf16 v[24:27], a[100:103], v[84:87], v[24:27]\n\t"
  "ds_read_b64_tr_b16 v[78:79], v110 offset:2048\n\t"
  "v_mfma_f32_16x16x32_bf16 v[28:31], a[104:107], v[84:87], v[28:31]\n\t"
  "ds_read_b64_tr_b16 v[80:81], v111 offset:0\n\t"
  "buffer_load_dwordx4 %[vdma], %[srd], s46 offen offset:1024 sc1 lds\n\t"
  "v_mfma_f32_16x16x32_bf16 v[32:35], a[96:99], v[88:91], v[32:35]\n\t"
  "ds_read_b64_tr_b16 v[82:83], v111 offset:2048\n\t"
  "v_mfma_f32_16x16x32_bf16 v[36:39], a[100:103], v[88:91], v[36:39]\n\t"
  "buffer_load_dwordx4 %[vdma], %[srd], s46 offen offset:2048 sc1 lds\n\t"
  "v_mfma_f32_16x16x32_bf16 v[40:43], a[104:107], v[88:91], v[40:43]\n\t"
  "s_waitcnt lgkmcnt(4)\n\t"
  "v_mfma_f32_16x16x32_bf16 v[20:23], a[108:111], v[68:71], v[20:23]\n\t"
  "ds_read_b64_tr_b16 v[84:85], v110 offset:4096\n\t"
  "v_mfma_f32_16x16x32_bf16 v[24:27], a[112:115], v[68:71], v[24:27]\n\t"
  "ds_read_b64_tr_b16 v[86:87], v110 offset:6144\n\t"
  "buffer_load_dwordx4 %[vdma], %[srd], s46 offen offset:3072 sc1 lds\n\t"
  "v_mfma_f32_16x16x32_bf16 v[28:31], a[116:119], v[68:71], v[28:31]\n\t"
  "ds_read_b64_tr_b16 v[88:89], v111 offset:4096\n\t"
  "v_mfma_f32_16x16x32_bf16 v[32:35], a[108:111], v[72:75], v[32:35]\n\t"
  "ds_read_b64_tr_b16 v[90:91], v111 offset:6144\n\t"
  "s_add_u32 m0, s52, 147456\n\t"
  "v_mfma_f32_16x16x32_bf16 v[36:39], a[112:115], v[72:75], v[36:39]\n\t"
  "v_mfma_f32_16x16x32_bf16 v[40:43], a[116:119], v[72:75], v[40:43]\n\t"
  "s_waitcnt lgkmcnt(4)\n\t"
  "v_mfma_f32_16x16x32_bf16 v[20:23], a[120:123], v[76:79], v[20:23]\n\t"
  "ds_read_b64_tr_b16 v[68:69], v110 offset:8192\n\t"
  "v_xor_b32 v108, 64, %[vtr]\n\t"
  "v_mfma_f32_16x16x32_bf16 v[24:27], a[124:127], v[76:79], v[24:27]\n\t"
  "ds_read_b64_tr_b16 v[70:71], v110 offset:10240\n\t"
  "v_add_u32 v108, 0, v108\n\t"
  "v_mfma_f32_16x16x32_bf16 v[28:31], a[128:131], v[76:79], v[28:31]\n\t"
  "ds_read_b64_tr_b16 v[72:73], v111 offset:8192\n\t"
  "v_xor_b32 v109, 96, %[vtr]\n\t"
  "v_mfma_f32_16x16x32_bf16 v[32:35], a[120:123], v[80:83], v[32:35]\n\t"
  "ds_read_b64_tr_b16 v[74:75], v111 offset:10240\n\t"
  "v_add_u32 v109, 0, v109\n\t"
  "v_mfma_f32_16x16x32_bf16 v[36:39], a[124:127], v[80:83], v[36:39]\n\t"
  "s_mov_b32 s46, 1376256\n\t"
  "buffer_load_dwordx4 %[vdma], %[srd], s46 offen offset:0 sc1 lds\n\t"
  "v_mfma_f32_16x16x32_bf16 v[40:43], a[128:131], v[80:83], v[40:43]\n\t"
  "s_waitcnt lgkmcnt(4)\n\t"
  "buffer_load_dwordx4 %[vdma], %[srd], s46 offen offset:1024 sc1 lds\n\t"
  "v_mfma_f32_16x16x32_bf16 v[20:23], a[132:135], v[84:87], v[20:23]\n\t"
  "ds_read_b64_tr_b16 v[76:77], v110 offset:12288\n\t"
  "v_mfma_f32_16x16x32_bf16 v[24:27], a[136:139], v[84:87], v[24:27]\n\t"
  "ds_read_b64_tr_b16 v[78:79], v110 offset:14336\n\t"
  "buffer_load_dwordx4 %[vdma], %[srd], s46 offen offset:2048 sc1 lds\n\t"
  "v_mfma_f32_16x16x32_bf16 v[28:31], a[140:143], v[84:87], v[28:31]\n\t"
  "ds_read_b64_tr_b16 v[80:81], v111 offset:12288\n\t"
  "v_mfma_f32_16x16x32_bf16 v[32:35], a[132:135], v[88:91], v[32:35]\n\t"
  "ds_read_b64_tr_b16 v[82:83], v111 offset:14336\n\t"
  "buffer_load_dwordx4 %[vdma], %[srd], s46 offen offset:3072 sc1 lds\n\t"
  "v_mfma_f32_16x16x32_bf16 v[36:39], a[136:139], v[88:91], v[36:39]\n\t"
  "v_mfma_f32_16x16x32_bf16 v[40:43], a[140:143], v[88:91], v[40:43]\n\t"
  "s_waitcnt lgkmcnt(4)\n\t"
  "v_mfma_f32_16x16x32_bf16 v[20:23], a[144:147], v[68:71], v[20:23]\n\t"
  "ds_read_b64_tr_b16 v[84:85], v110 offset:16384\n\t"
  "v_mfma_f32_16x16x32_bf16 v[24:27], a[148:151], v[68:71], v[24:27]\n\t"
  "ds_read_b64_tr_b16 v[86:87], v110 offset:18432\n\t"
  "v_mfma_f32_16x16x32_bf16 v[28:31], a[152:155], v[68:71], v[28:31]\n\t"
  "ds_read_b64_tr_b16 v[88:89], v111 offset:16384\n\t"
  "v_mfma_f32_16x16x32_bf16 v[32:35], a[144:147], v[72:75], v[32:35]\n\t"
  "ds_read_b64_tr_b16 v[90:91], v111 offset:18432\n\t"
  "v_mfma_f32_16x16x32_bf16 v[36:39], a[148:151], v[72:75], v[36:39]\n\t"
  "v_mfma_f32_16x16x32_bf16 v[40:43], a[152:155], v[72:75], v[40:43]\n\t"
  "s_waitcnt lgkmcnt(4)\n\t"
  "v_mfma_f32_16x16x32_bf16 v[20:23], a[156:159], v[76:79], v[20:23]\n\t"
  "ds_read_b64_tr_b16 v[68:69], v110 offset:20480\n\t"
  "v_mfma_f32_16x16x32_bf16 v[24:27], a[160:163], v[76:79], v[24:27]\n\t"
  "ds_read_b64_tr_b16 v[70:71], v110 offset:22528\n\t"
  "v_mfma_f32_16x16x32_bf16 v[28:31], a[164:167], v[76:79], v[28:31]\n\t"
  "ds_read_b64_tr_b16 v[72:73], v111 offset:20480\n\t"
  "v_mfma_f32_16x16x32_bf16 v[32:35], a[156:159], v[80:83], v[32:35]\n\t"
  "ds_read_b64_tr_b16 v[74:75], v111 offset:22528\n\t"
  "v_mfma_f32_16x16x32_bf16 v[36:39], a[160:163], v[80:83], v[36:39]\n\t"
  "v_mfma_f32_16x16x32_bf16 v[40:43], a[164:167], v[80:83], v[40:43]\n\t"
  "s_waitcnt lgkmcnt(4)\n\t"
  "v_mfma_f32_16x16x32_bf16 v[20:23], a[168:171], v[84:87], v[20:23]\n\t"
  "ds_read_b64_tr_b16 v[76:77], v110 offset:24576\n\t"
  "s_waitcnt vmcnt(11)\n\t"
  "v_cmp_ne_u64 vcc, v[132:133], v[138:139]\n\t"
  "v_mfma_f32_16x16x32_bf16 v[24:27], a[172:175], v[84:87], v[24:27]\n\t"
  "ds_read_b64_tr_b16 v[78:79], v110 offset:26624\n\t"
  "v_mfma_f32_16x16x32_bf16 v[28:31], a[176:179], v[84:87], v[28:31]\n\t"
  "ds_read_b64_tr_b16 v[80:81], v111 offset:24576\n\t"
  "s_cbranch_vccnz Lw2_slow%=\n\t"
  "Lw2_back%=:\n\t"
  "v_mfma_f32_16x16x32_bf16 v[32:35], a[168:171], v[88:91], v[32:35]\n\t"
  "ds_read_b64_tr_b16 v[82:83], v111 offset:26624\n\t"
  "v_mfma_f32_16x16x32_bf16 v[36:39], a[172:175], v[88:91], v[36:39]\n\t"
  "v_mfma_f32_16x16x32_bf16 v[40:43], a[176:179], v[88:91], v[40:43]\n\t"
  "s_waitcnt lgkmcnt(4)\n\t"
  "v_mfma_f32_16x16x32_bf16 v[20:23], a[180:183], v[68:71], v[20:23]\n\t"
  "ds_read_b64_tr_b16 v[84:85], v110 offset:28672\n\t"
  "v_lshrrev_b32 v138, 1, %[vl16]\n\t"
  "v_mfma_f32_16x16x32_bf16 v[24:27], a[184:187], v[68:71], v[24:27]\n\t"
  "ds_read_b64_tr_b16 v[86:87], v110 offset:30720\n\t"
  "buffer_load_dwordx2 a[240:241], v138, %[srd], %[sUB] offen offset:0 sc1\n\t"
  "v_mfma_f32_16x16x32_bf16 v[28:31], a[188:191], v[68:71], v[28:31]\n\t"
  "ds_read_b64_tr_b16 v[88:89], v111 offset:28672\n\t"
  "buffer_load_dwordx2 a[242:243], v138, %[srd], %[sUB] offen offset:512 sc1\n\t"
  "v_mfma_f32_16x16x32_bf16 v[32:35], a[180:183], v[72:75], v[32:35]\n\t"
  "ds_read_b64_tr_b16 v[90:91], v111 offset:30720\n\t"
  "buffer_load_dwordx2 a[244:245], v138, %[srd], %[sUB] offen offset:1024 sc1\n\t"
  "v_mfma_f32_16x16x32_bf16 v[36:39], a[184:187], v[72:75], v[36:39]\n\t"
  "s_add_u32 s45, %[sW2], 0\n\t"
  "buffer_load_dwordx4 v[144:147], %[vl16], %[srd], s45 offen offset:0 sc1\n\t"
  "v_mfma_f32_16x16x32_bf16 v[40:43], a[188:191], v[72:75], v[40:43]\n\t"
  "s_waitcnt lgkmcnt(4)\n\t"
  "v_mfma_f32_16x16x32_bf16 v[20:23], a[192:195], v[76:79], v[20:23]\n\t"
  "ds_read_b64_tr_b16 v[68:69], v110 offset:32768\n\t"
  "v_mfma_f32_16x16x32_bf16 v[24:27], a[196:199], v[76:79], v[24:27]\n\t"
  "ds_read_b64_tr_b16 v[70:71], v110 offset:34816\n\t"
  "v_mfma_f32_16x16x32_bf16 v[28:31], a[200:203], v[76:79], v[28:31]\n\t"
  "ds_read_b64_tr_b16 v[72:73], v111 offset:32768\n\t"
  "v_mfma_f32_16x16x32_bf16 v[32:35], a[192:195], v[80:83], v[32:35]\n\t"
  "ds_read_b64_tr_b16 v[74:75], v111 offset:34816\n\t"
  "buffer_load_dwordx4 v[148:151], %[vl16], %[srd], s45 offen offset:1024 sc1\n\t"
  "v_mfma_f32_16x16x32_bf16 v[36:39], a[196:199], v[80:83], v[36:39]\n\t"
  "v_mfma_f32_16x16x32_bf16 v[40:43], a[200:203], v[80:83], v[40:43]\n\t"
  "s_waitcnt lgkmcnt(4)\n\t"
  "v_mfma_f32_16x16x32_bf16 v[20:23], a[204:207], v[84:87], v[20:23]\n\t"
  "ds_read_b64_tr_b16 v[76:77], v110 offset:36864\n\t"
  "v_mfma_f32_16x16x32_bf16 v[24:27], a[208:211], v[84:87], v[24:27]\n\t"
  "ds_read_b64_tr_b16 v[78:79], v110 offset:38912\n\t"
  "v_mfma_f32_16x16x32_bf16 v[28:31], a[212:215], v[84:87], v[28:31]\n\t"
  "ds_read_b64_tr_b16 v[80:81], v111 offset:36864\n\t"
  "buffer_load_dwordx4 v[152:155], %[vl16], %[srd], s45 offen offset:2048 sc1\n\t"
  "v_mfma_f32_16x16x32_bf16 v[32:35], a[204:207], v[88:91], v[32:35]\n\t"
  "ds_read_b64_tr_b16 v[82:83], v111 offset:38912\n\t"
  "v_mfma_f32_16x16x32_bf16 v[36:39], a[208:211], v[88:91], v[36:39]\n\t"
  "v_mfma_f32_16x16x32_bf16 v[40:43], a[212:215], v[88:91], v[40:43]\n\t"
  "s_waitcnt lgkmcnt(4)\n\t"
  "v_mfma_f32_16x16x32_bf16 v[20:23], a[216:219], v[68:71], v[20:23]\n\t"
  "ds_read_b64_tr_b16 v[84:85], v108 offset:0\n\t"
  "v_mfma_f32_16x16x32_bf16 v[24:27], a[220:223], v[68:71], v[24:27]\n\t"
  "ds_read_b64_tr_b16 v[86:87], v108 offset:2048\n\t"
  "buffer_load_dwordx4 v[156:159], %[vl16], %[srd], s45 offen offset:3072 sc1\n\t"
  "v_mfma_f32_16x16x32_bf16 v[28:31], a[224:227], v[68:71], v[28:31]\n\t"
  "ds_read_b64_tr_b16 v[88:89], v109 offset:0\n\t"
  "v_mfma_f32_16x16x32_bf16 v[32:35], a[216:219], v[72:75], v[32:35]\n\t"
  "ds_read_b64_tr_b16 v[90:91], v109 offset:2048\n\t"
  "v_mfma_f32_16x16x32_bf16 v[36:39], a[220:223], v[72:75], v[36:39]\n\t"
  "v_mfma_f32_16x16x32_bf16 v[40:43], a[224:227], v[72:75], v[40:43]\n\t"
  "s_waitcnt lgkmcnt(4)\n\t"
  "v_mfma_f32_16x16x32_bf16 v[20:23], a[228:231], v[76:79], v[20:23]\n\t"
  "ds_read_b64_tr_b16 v[68:69], v108 offset:4096\n\t"
  "s_add_u32 s45, %[sW2], 4096\n\t"
  "v_mfma_f32_16x16x32_bf16 v[24:27], a[232:235], v[76:79], v[24:27]\n\t"
  "ds_read_b64_tr_b16 v[70:71], v108 offset:6144\n\t"
  "v_mfma_f32_16x16x32_bf16 v[28:31], a[236:239], v[76:79], v[28:31]\n\t"
  "ds_read_b64_tr_b16 v[72:73], v109 offset:4096\n\t"
  "v_mfma_f32_16x16x32_bf16 v[32:35], a[228:231], v[80:83], v[32:35]\n\t"
  "ds_read_b64_tr_b16 v[74:75], v109 offset:6144\n\t"
  "v_mfma_f32_16x16x32_bf16 v[36:39], a[232:235], v[80:83], v[36:39]\n\t"
  "v_mfma_f32_16x16x32_bf16 v[40:43], a[236:239], v[80:83], v[40:43]\n\t"
  "s_waitcnt lgkmcnt(4)\n\t"
  "buffer_load_dwordx4 v[160:163], %[vl16], %[srd], s45 offen offset:0 sc1\n\t"
  "v_mfma_f32_16x16x32_bf16 v[44:47], a[0:3], v[84:87], 0\n\t"
  "ds_read_b64_tr_b16 v[76:77], v108 offset:8192\n\t"
  "v_xor_b32 v110, 64, %[vtr]\n\t"
  "v_mfma_f32_16x16x32_bf16 v[48:51], a[4:7], v[84:87], 0\n\t"
  "ds_read_b64_tr_b16 v[78:79], v108 offset:10240\n\t"
  "v_add_u32 v110, 40960, v110\n\t"
  "v_mfma_f32_16x16x32_bf16 v[52:55], a[8:11], v[84:87], 0\n\t"
  "ds_read_b64_tr_b16 v[80:81], v109 offset:8192\n\t"
  "v_xor_b32 v111, 96, %[vtr]\n\t"
  "v_mfma_f32_16x16x32_bf16 v[56:59], a[0:3], v[88:91], 0\n\t"
  "ds_read_b64_tr_b16 v[82:83], v109 offset:10240\n\t"
  "v_add_u32 v111, 40960, v111\n\t"
  "v_mfma_f32_16x16x32_bf16 v[60:63], a[4:7], v[88:91], 0\n\t"
  "buffer_load_dwordx4 v[164:167], %[vl16], %[srd], s45 offen offset:1024 sc1\n\t"
  "v_mfma_f32_16x16x32_bf16 v[64:67], a[8:11], v[88:91], 0\n\t"
  "s_waitcnt lgkmcnt(4)\n\t"
  "v_mfma_f32_16x16x32_bf16 v[44:47], a[12:15], v[68:71], v[44:47]\n\t"
  "ds_read_b64_tr_b16 v[84:85], v108 offset:12288\n\t"
  "buffer_load_dwordx2 v[92:93], v136, %[srd], %[sST] offen offset:0 sc1\n\t"
  "v_mfma_f32_16x16x32_bf16 v[48:51], a[16:19], v[68:71], v[48:51]\n\t"
  "ds_read_b64_tr_b16 v[86:87], v108 offset:14336\n\t"
  "buffer_load_dwordx2 v[94:95], v136, %[srd], %[sST] offen offset:1024 sc1\n\t"
  "v_mfma_f32_16x16x32_bf16 v[52:55], a[20:23], v[68:71], v[52:55]\n\t"
  "ds_read_b64_tr_b16 v[88:89], v109 offset:12288\n\t"
  "buffer_load_dwordx2 v[96:97], v136, %[srd], %[sST] offen offset:2048 sc1\n\t"
  "v_mfma_f32_16x16x32_bf16 v[56:59], a[12:15], v[72:75], v[56:59]\n\t"
  "ds_read_b64_tr_b16 v[90:91], v109 offset:14336\n\t"
  "buffer_load_dwordx2 v[98:99], v136, %[srd], %[sST] offen offset:3072 sc1\n\t"
  "v_mfma_f32_16x16x32_bf16 v[60:63], a[16:19], v[72:75], v[60:63]\n\t"
  "buffer_load_dwordx4 v[168:171], %[vl16], %[srd], s45 offen offset:2048 sc1\n\t"
  "v_mfma_f32_16x16x32_bf16 v[64:67], a[20:23], v[72:75], v[64:67]\n\t"
  "s_waitcnt lgkmcnt(4)\n\t"
  "v_mfma_f32_16x16x32_bf16 v[44:47], a[24:27], v[76:79], v[44:47]\n\t"
  "ds_read_b64_tr_b16 v[68:69], v108 offset:16384\n\t"
  "v_mfma_f32_16x16x32_bf16 v[48:51], a[28:31], v[76:79], v[48:51]\n\t"
  "ds_read_b64_tr_b16 v[70:71], v108 offset:18432\n\t"
  "v_mfma_f32_16x16x32_bf16 v[52:55], a[32:35], v[76:79], v[52:55]\n\t"
  "ds_read_b64_tr_b16 v[72:73], v109 offset:16384\n\t"
  "s_waitcnt vmcnt(14)\n\t"
  "s_barrier\n\t"
  "v_mfma_f32_16x16x32_bf16 v[56:59], a[24:27], v[80:83], v[56:59]\n\t"
  "ds_read_b64_tr_b16 v[74:75], v109 offset:18432\n\t"
  "buffer_load_dwordx4 v[172:175], %[vl16], %[srd], s45 offen offset:3072 sc1\n\t"
  "v_mfma_f32_16x16x32_bf16 v[60:63], a[28:31], v[80:83], v[60:63]\n\t"
  "ds_read_u16_d16_hi v112, v125 offset:0\n\t"
  "ds_read_b64_tr_b16 a[246:247], %[vres0] offset:0\n\t"
  "v_mfma_f32_16x16x32_bf16 v[64:67], a[32:35], v[80:83], v[64:67]\n\t"
  "s_waitcnt lgkmcnt(6)\n\t"
  "ds_read_u16_d16_hi v113, v125 offset:8192\n\t"
  "v_mfma_f32_16x16x32_bf16 v[44:47], a[36:39], v[84:87], v[44:47]\n\t"
  "ds_read_b64_tr_b16 v[76:77], v108 offset:20480\n\t"
  "ds_read_u16_d16_hi v114, v125 offset:16384\n\t"
  "v_mfma_f32_16x16x32_bf16 v[48:51], a[40:43], v[84:87], v[48:51]\n\t"
  "ds_read_b64_tr_b16 v[78:79], v108 offset:22528\n\t"
  "s_add_u32 s45, %[sW2], 8192\n\t"
  "v_mfma_f32_16x16x32_bf16 v[52:55], a[44:47], v[84:87], v[52:55]\n\t"
  "ds_read_b64_tr_b16 v[80:81], v109 offset:20480\n\t"
  "ds_read_u16_d16_hi v115, v125 offset:24576\n\t"
  "v_mfma_f32_16x16x32_bf16 v[56:59], a[36:39], v[88:91], v[56:59]\n\t"
  "ds_read_b64_tr_b16 v[82:83], v109 offset:22528\n\t"
  "ds_read_u16_d16_hi v116, v125 offset:32768\n\t"
  "v_mfma_f32_16x16x32_bf16 v[60:63], a[40:43], v[88:91], v[60:63]\n\t"
  "ds_read_u16_d16_hi v117, v124 offset:0\n\t"
  "ds_read_b64_tr_b16 a[248:249], %[vres1] offset:0\n\t"
  "v_mfma_f32_16x16x32_bf16 v[64:67], a[44:47], v[88:91], v[64:67]\n\t"
  "s_waitcnt lgkmcnt(12)\n\t"
  "ds_read_u16_d16_hi v118, v124 offset:8192\n\t"
  "v_mfma_f32_16x16x32_bf16 v[44:47], a[48:51], v[68:71], v[44:47]\n\t"
  "ds_read_b64_tr_b16 v[84:85], v108 offset:24576\n\t"
  "buffer_load_dwordx4 v[176:179], %[vl16], %[srd], s45 offen offset:0 sc1\n\t"
  "v_mfma_f32_16x16x32_bf16 v[48:51], a[52:55], v[68:71], v[48:51]\n\t"
  "ds_read_b64_tr_b16 v[86:87], v108 offset:26624\n\t"
  "ds_read_u16_d16_hi v119, v124 offset:16384\n\t"
  "v_mfma_f32_16x16x32_bf16 v[52:55], a[56:59], v[68:71], v[52:55]\n\t"
  "ds_read_b64_tr_b16 v[88:89], v109 offset:24576\n\t"
  "ds_read_u16_d16_hi v120, v124 offset:24576\n\t"
  "v_mfma_f32_16x16x32_bf16 v[56:59], a[48:51], v[72:75], v[56:59]\n\t"
  "ds_read_b64_tr_b16 v[90:91], v109 offset:26624\n\t"
  "ds_read_u16_d16_hi v121, v124 offset:32768\n\t"
  "v_mfma_f32_16x16x32_bf16 v[60:63], a[52:55], v[72:75], v[60:63]\n\t"
  "buffer_load_dwordx4 v[180:183], %[vl16], %[srd], s45 offen offset:1024 sc1\n\t"
  "ds_read_b64_tr_b16 a[250:251], %[vres0] offset:4096\n\t"
  "v_mfma_f32_16x16x32_bf16 v[64:67], a[56:59], v[72:75], v[64:67]\n\t"
  "s_waitcnt lgkmcnt(12)\n\t"
  "ds_read_b64_tr_b16 a[252:253], %[vres1] offset:4096\n\t"
  "v_mfma_f32_16x16x32_bf16 v[44:47], a[60:63], v[76:79], v[44:47]\n\t"
  "ds_read_b64_tr_b16 v[68:69], v108 offset:28672\n\t"
  "ds_read_b64_tr_b16 a[254:255], %[vres2] offset:0\n\t"
  "v_mfma_f32_16x16x32_bf16 v[48:51], a[64:67], v[76:79], v[48:51]\n\t"
  "ds_read_b64_tr_b16 v[70:71], v108 offset:30720\n\t"
  "v_mfma_f32_16x16x32_bf16 v[52:55], a[68:71], v[76:79], v[52:55]\n\t"
  "ds_read_b64_tr_b16 v[72:73], v109 offset:28672\n\t"
  "v_mfma_f32_16x16x32_bf16 v[56:59], a[60:63], v[80:83], v[56:59]\n\t"
  "ds_read_b64_tr_b16 v[74:75], v109 offset:30720\n\t"
  "buffer_load_dwordx4 v[184:187], %[vl16], %[srd], s45 offen offset:2048 sc1\n\t"
  "v_mfma_f32_16x16x32_bf16 v[60:63], a[64:67], v[80:83], v[60:63]\n\t"
  "v_mfma_f32_16x16x32_bf16 v[64:67], a[68:71], v[80:83], v[64:67]\n\t"
  "s_waitcnt lgkmcnt(8)\n\t"
  "v_mfma_f32_16x16x32_bf16 v[44:47], a[72:75], v[84:87], v[44:47]\n\t"
  "ds_read_b64_tr_b16 v[76:77], v108 offset:32768\n\t"
  "v_mfma_f32_16x16x32_bf16 v[48:51], a[76:79], v[84:87], v[48:51]\n\t"
  "ds_read_b64_tr_b16 v[78:79], v108 offset:34816\n\t"
  "v_mul_f32 v112, v112, v142\n\t"
  "v_mfma_f32_16x16x32_bf16 v[52:55], a[80:83], v[84:87], v[52:55]\n\t"
  "ds_read_b64_tr_b16 v[80:81], v109 offset:32768\n\t"
  "buffer_load_dwordx4 v[188:191], %[vl16], %[srd], s45 offen offset:3072 sc1\n\t"
  "v_mfma_f32_16x16x32_bf16 v[56:59], a[72:75], v[88:91], v[56:59]\n\t"
  "ds_read_b64_tr_b16 v[82:83], v109 offset:34816\n\t"
  "v_mul_f32 v113, v113, v143\n\t"
  "v_mfma_f32_16x16x32_bf16 v[60:63], a[76:79], v[88:91], v[60:63]\n\t"
  "v_mul_f32 v114, v114, v248\n\t"
  "v_mul_f32 v115, v115, v249\n\t"
  "v_mfma_f32_16x16x32_bf16 v[64:67], a[80:83], v[88:91], v[64:67]\n\t"
  "s_waitcnt lgkmcnt(4)\n\t"
  "v_mul_f32 v116, v116, v250\n\t"
  "v_mfma_f32_16x16x32_bf16 v[44:47], a[84:87], v[68:71], v[44:47]\n\t"
  "ds_read_b64_tr_b16 v[84:85], v108 offset:36864\n\t"
  "v_mul_f32 v117, v117, v251\n\t"
  "v_mfma_f32_16x16x32_bf16 v[48:51], a[88:91], v[68:71], v[48:51]\n\t"
  "ds_read_b64_tr_b16 v[86:87], v108 offset:38912\n\t"
  "s_add_u32 s45, %[sW2], 12288\n\t"
  "v_mfma_f32_16x16x32_bf16 v[52:55], a[92:95], v[68:71], v[52:55]\n\t"
  "ds_read_b64_tr_b16 v[88:89], v109 offset:36864\n\t"
  "v_mul_f32 v118, v118, v252\n\t"
  "v_mfma_f32_16x16x32_bf16 v[56:59], a[84:87], v[72:75], v[56:59]\n\t"
  "ds_read_b64_tr_b16 v[90:91], v109 offset:38912\n\t"
  "v_mul_f32 v119, v119, v253\n\t"
  "v_mfma_f32_16x16x32_bf16 v[60:63], a[88:91], v[72:75], v[60:63]\n\t"
  "v_mul_f32 v120, v120, v254\n\t"
  "v_mul_f32 v121, v121, v255\n\t"
  "v_mfma_f32_16x16x32_bf16 v[64:67], a[92:95], v[72:75], v[64:67]\n\t"
  "s_waitcnt lgkmcnt(4)\n\t"
  "v_add_f32 v122, v112, v113\n\t"
  "v_mfma_f32_16x16x32_bf16 v[44:47], a[96:99], v[76:79], v[44:47]\n\t"
  "ds_read_b64_tr_b16 v[68:69], v110 offset:0\n\t"
  "buffer_load_dwordx4 v[192:195], %[vl16], %[srd], s45 offen offset:0 sc1\n\t"
  "v_mfma_f32_16x16x32_bf16 v[48:51], a[100:103], v[76:79], v[48:51]\n\t"
  "ds_read_b64_tr_b16 v[70:71], v110 offset:2048\n\t"
  "v_mul_f32 v123, v112, v112\n\t"
  "v_mfma_f32_16x16x32_bf16 v[52:55], a[104:107], v[76:79], v[52:55]\n\t"
  "ds_read_b64_tr_b16 v[72:73], v111 offset:0\n\t"
  "v_fmac_f32 v123, v113, v113\n\t"
  "v_mfma_f32_16x16x32_bf16 v[56:59], a[96:99], v[80:83], v[56:59]\n\t"
  "ds_read_b64_tr_b16 v[74:75], v111 offset:2048\n\t"
  "v_add_f32 v122, v122, v114\n\t"
  "v_mfma_f32_16x16x32_bf16 v[60:63], a[100:103], v[80:83], v[60:63]\n\t"
  "v_fmac_f32 v123, v114, v114\n\t"
  "v_add_f32 v122, v122, v115\n\t"
  "v_mfma_f32_16x16x32_bf16 v[64:67], a[104:107], v[80:83], v[64:67]\n\t"
  "s_waitcnt lgkmcnt(4)\n\t"
  "buffer_load_dwordx4 v[196:199], %[vl16], %[srd], s45 offen offset:1024 sc1\n\t"
  "v_mfma_f32_16x16x32_bf16 v[44:47], a[108:111], v[84:87], v[44:47]\n\t"
  "ds_read_b64_tr_b16 v[76:77], v110 offset:4096\n\t"
  "v_fmac_f32 v123, v115, v115\n\t"
  "v_mfma_f32_16x16x32_bf16 v[48:51], a[112:115], v[84:87], v[48:51]\n\t"
  "ds_read_b64_tr_b16 v[78:79], v110 offset:6144\n\t"
  "v_add_f32 v122, v122, v116\n\t"
  "v_mfma_f32_16x16x32_bf16 v[52:55], a[116:119], v[84:87], v[52:55]\n\t"
  "ds_read_b64_tr_b16 v[80:81], v111 offset:4096\n\t"
  "v_fmac_f32 v123, v116, v116\n\t"
  "v_mfma_f32_16x16x32_bf16 v[56:59], a[108:111], v[88:91], v[56:59]\n\t"
  "ds_read_b64_tr_b16 v[82:83], v111 offset:6144\n\t"
  "v_add_f32 v122, v122, v117\n\t"
  "v_mfma_f32_16x16x32_bf16 v[60:63], a[112:115], v[88:91], v[60:63]\n\t"
  "buffer_load_dwordx4 v[200:203], %[vl16], %[srd], s45 offen offset:2048 sc1\n\t"
  "v_fmac_f32 v123, v117, v117\n\t"
  "v_mfma_f32_16x16x32_bf16 v[64:67], a[116:119], v[88:91], v[64:67]\n\t"
  "s_waitcnt lgkmcnt(4)\n\t"
  "s_waitcnt vmcnt(11)\n\t"
  "v_min_u32 v131, v93, v95\n\t"
  "v_mfma_f32_16x16x32_bf16 v[44:47], a[120:123], v[68:71], v[44:47]\n\t"
  "ds_read_b64_tr_b16 v[84:85], v110 offset:8192\n\t"
  "s_waitcnt vmcnt(9)\n\t"
  "v_min3_u32 v131, v131, v97, v99\n\t"
  "v_mfma_f32_16x16x32_bf16 v[48:51], a[124:127], v[68:71], v[48:51]\n\t"
  "ds_read_b64_tr_b16 v[86:87], v110 offset:10240\n\t"
  "v_cmp_eq_u32 vcc, 0, v131\n\t"
  "v_mfma_f32_16x16x32_bf16 v[52:55], a[128:131], v[68:71], v[52:55]\n\t"
  "ds_read_b64_tr_b16 v[88:89], v111 offset:8192\n\t"
  "v_add_u32 v108, 81920, %[vtr]\n\t"
  "v_mfma_f32_16x16x32_bf16 v[56:59], a[120:123], v[72:75], v[56:59]\n\t"
  "ds_read_b64_tr_b16 v[90:91], v111 offset:10240\n\t"
  "s_cbranch_vccnz Lst0_slow%=\n\t"
  "Lst0_back%=:\n\t"
  "v_mfma_f32_16x16x32_bf16 v[60:63], a[124:127], v[72:75], v[60:63]\n\t"
  "v_xor_b32 v109, 32, %[vtr]\n\t"
  "v_add_u32 v109, 81920, v109\n\t"
  "v_mfma_f32_16x16x32_bf16 v[64:67], a[128:131], v[72:75], v[64:67]\n\t"
  "s_waitcnt lgkmcnt(4)\n\t"
  "buffer_load_dwordx4 v[204:207], %[vl16], %[srd], s45 offen offset:3072 sc1\n\t"
  "v_mfma_f32_16x16x32_bf16 v[44:47], a[132:135], v[76:79], v[44:47]\n\t"
  "ds_read_b64_tr_b16 v[68:69], v110 offset:12288\n\t"
  "v_add_f32 v122, v122, v118\n\t"
  "v_mfma_f32_16x16x32_bf16 v[48:51], a[136:139], v[76:79], v[48:51]\n\t"
  "ds_read_b64_tr_b16 v[70:71], v110 offset:14336\n\t"
  "v_fmac_f32 v123, v118, v118\n\t"
  "v_mfma_f32_16x16x32_bf16 v[52:55], a[140:143], v[76:79], v[52:55]\n\t"
  "ds_read_b64_tr_b16 v[72:73], v111 offset:12288\n\t"
  "s_add_u32 s45, %[sW2], 16384\n\t"
  "v_mfma_f32_16x16x32_bf16 v[56:59], a[132:135], v[80:83], v[56:59]\n\t"
  "ds_read_b64_tr_b16 v[74:75], v111 offset:14336\n\t"
  "v_add_f32 v122, v122, v119\n\t"
  "v_mfma_f32_16x16x32_bf16 v[60:63], a[136:139], v[80:83], v[60:63]\n\t"
  "v_fmac_f32 v123, v119, v119\n\t"
  "v_add_f32 v122, v122, v120\n\t"
  "v_mfma_f32_16x16x32_bf16 v[64:67], a[140:143], v[80:83], v[64:67]\n\t"
  "v_fmac_f32 v123, v120, v120\n\t"
  "v_add_f32 v122, v122, v121\n\t"
  "v_mfma_f32_16x16x16_bf16 v[20:23], a[240:241], v[92:93], v[20:23]\n\t"
  "v_fmac_f32 v123, v121, v121\n\t"
  "v_add_f32_dpp v122, v122, v122 quad_perm:[1,0,3,2] row_mask:0xf bank_mask:0xf\n\t"
  "v_mfma_f32_16x16x16_bf16 v[24:27], a[242:243], v[92:93], v[24:27]\n\t"
  "buffer_load_dwordx4 v[208:211], %[vl16], %[srd], s45 offen offset:0 sc1\n\t"
  "v_add_f32_dpp v123, v123, v123 quad_perm:[1,0,3,2] row_mask:0xf bank_mask:0xf\n\t"
  "v_mfma_f32_16x16x16_bf16 v[28:31], a[244:245], v[92:93], v[28:31]\n\t"
  "v_add_f32_dpp v122, v122, v122 quad_perm:[2,3,0,1] row_mask:0xf bank_mask:0xf\n\t"
  "v_add_f32_dpp v123, v123, v123 quad_perm:[2,3,0,1] row_mask:0xf bank_mask:0xf\n\t"
  "v_mfma_f32_16x16x16_bf16 v[32:35], a[240:241], v[94:95], v[32:35]\n\t"
  "v_add_f32_dpp v122, v122, v122 row_half_mirror row_mask:0xf bank_mask:0xf\n\t"
  "v_add_f32_dpp v123, v123, v123 row_half_mirror row_mask:0xf bank_mask:0xf\n\t"
  "v_mfma_f32_16x16x16_bf16 v[36:39], a[242:243], v[94:95], v[36:39]\n\t"
  "v_add_f32_dpp v122, v122, v122 row_mirror row_mask:0xf bank_mask:0xf\n\t"
  "v_add_f32_dpp v123, v123, v123 row_mirror row_mask:0xf bank_mask:0xf\n\t"
  "v_mfma_f32_16x16x16_bf16 v[40:43], a[244:245], v[94:95], v[40:43]\n\t"
  "s_waitcnt lgkmcnt(4)\n\t"
  "v_add_f32_dpp v122, v122, v122 row_bcast:15 row_mask:0xa bank_mask:0xf\n\t"
  "v_mfma_f32_16x16x32_bf16 v[44:47], a[144:147], v[84:87], v[44:47]\n\t"
  "ds_read_b64_tr_b16 v[76:77], v110 offset:16384\n\t"
  "buffer_load_dwordx4 v[212:215], %[vl16], %[srd], s45 offen offset:1024 sc1\n\t"
  "v_mfma_f32_16x16x32_bf16 v[48:51], a[148:151], v[84:87], v[48:51]\n\t"
  "ds_read_b64_tr_b16 v[78:79], v110 offset:18432\n\t"
  "v_add_f32_dpp v123, v123, v123 row_bcast:15 row_mask:0xa bank_mask:0xf\n\t"
  "v_mfma_f32_16x16x32_bf16 v[52:55], a[152:155], v[84:87], v[52:55]\n\t"
  "ds_read_b64_tr_b16 v[80:81], v111 offset:16384\n\t"
  "v_add_f32_dpp v122, v122, v122 row_bcast:31 row_mask:0xc bank_mask:0xf\n\t"
  "v_mfma_f32_16x16x32_bf16 v[56:59], a[144:147], v[88:91], v[56:59]\n\t"
  "ds_read_b64_tr_b16 v[82:83], v111 offset:18432\n\t"
  "v_add_f32_dpp v123, v123, v123 row_bcast:31 row_mask:0xc bank_mask:0xf\n\t"
  "v_mfma_f32_16x16x32_bf16 v[60:63], a[148:151], v[88:91], v[60:63]\n\t"
  "buffer_load_dwordx4 v[216:219], %[vl16], %[srd], s45 offen offset:2048 sc1\n\t"
  "v_mul_f32 v122, 0xbacccccd, v122\n\t"
  "v_mfma_f32_16x16x32_bf16 v[64:67], a[152:155], v[88:91], v[64:67]\n\t"
  "s_waitcnt lgkmcnt(4)\n\t"
  "v_mul_f32 v123, 0x3acccccd, v123\n\t"
  "v_fma_f32 v123, -v122, v122, v123\n\t"
  "v_max_f32 v123, 0, v123\n\t"
  "v_cvt_pk_bf16_f32 v123, 0, v123\n\t"
  "v_cvt_pk_bf16_f32 v126, v122, v122\n\t"
  "v_add_f32 v123, 0x3727c5ac, v123\n\t"
  "v_cvt_pk_bf16_f32 v123, 0, v123\n\t"
  "v_sqrt_f32 v123, v123\n\t"
  "s_nop 0\n\t"
  "v_cvt_pk_bf16_f32 v127, v123, v123\n\t"
  "buffer_store_dwordx2 v[126:127], %[vstw], %[srd], s54 offen sc1\n\t"
  "v_mfma_f32_16x16x32_bf16 v[44:47], a[156:159], v[68:71], v[44:47]\n\t"
  "ds_read_b64_tr_b16 v[84:85], v110 offset:20480\n\t"
  "v_and_b32 v128, 0xffff0000, v93\n\t"
  "v_mfma_f32_16x16x32_bf16 v[48:51], a[160:163], v[68:71], v[48:51]\n\t"
  "ds_read_b64_tr_b16 v[86:87], v110 offset:22528\n\t"
  "v_and_b32 v129, 0xffff0000, v95\n\t"
  "v_mfma_f32_16x16x32_bf16 v[52:55], a[164:167], v[68:71], v[52:55]\n\t"
  "ds_read_b64_tr_b16 v[88:89], v111 offset:20480\n\t"
  "v_rcp_f32 v128, v128\n\t"
  "v_mfma_f32_16x16x32_bf16 v[56:59], a[156:159], v[72:75], v[56:59]\n\t"
  "ds_read_b64_tr_b16 v[90:91], v111 offset:22528\n\t"
  "buffer_load_dwordx4 v[220:223], %[vl16], %[srd], s45 offen offset:3072 sc1\n\t"
  "v_mfma_f32_16x16x32_bf16 v[60:63], a[160:163], v[72:75], v[60:63]\n\t"
  "v_rcp_f32 v129, v129\n\t"
  "v_mul_f32 v128, v128, v128\n\t"
  "v_mfma_f32_16x16x32_bf16 v[64:67], a[164:167], v[72:75], v[64:67]\n\t"
  "s_waitcnt lgkmcnt(4)\n\t"
  "v_mul_f32 v129, v129, v129\n\t"
  "v_mfma_f32_16x16x32_bf16 v[44:47], a[168:171], v[76:79], v[44:47]\n\t"
  "ds_read_b64_tr_b16 v[68:69], v110 offset:24576\n\t"
  "v_max_f32 v20, 0, v20\n\t"
  "v_mfma_f32_16x16x32_bf16 v[48:51], a[172:175], v[76:79], v[48:51]\n\t"
  "ds_read_b64_tr_b16 v[70:71], v110 offset:26624\n\t"
  "v_max_f32 v21, 0, v21\n\t"
  "v_mfma_f32_16x16x32_bf16 v[52:55], a[176:179], v[76:79], v[52:55]\n\t"
  "ds_read_b64_tr_b16 v[72:73], v111 offset:24576\n\t"
  "s_add_u32 s45, %[sW2], 20480\n\t"
  "v_mfma_f32_16x16x32_bf16 v[56:59], a[168:171], v[80:83], v[56:59]\n\t"
  "ds_read_b64_tr_b16 v[74:75], v111 offset:26624\n\t"
  "v_mul_f32 v22, v22, v128\n\t"
  "v_mfma_f32_16x16x32_bf16 v[60:63], a[172:175], v[80:83], v[60:63]\n\t"
  "v_mul_f32 v23, v23, v128\n\t"
  "v_mul_f32 v20, v20, v22\n\t"
  "v_mfma_f32_16x16x32_bf16 v[64:67], a[176:179], v[80:83], v[64:67]\n\t"
  "s_waitcnt lgkmcnt(4)\n\t"
  "v_mul_f32 v21, v21, v23\n\t"
  "v_mfma_f32_16x16x32_bf16 v[44:47], a[180:183], v[84:87], v[44:47]\n\t"
  "ds_read_b64_tr_b16 v[76:77], v110 offset:28672\n\t"
  "v_cvt_pk_bf16_f32 v116, v20, v21\n\t"
  "v_mfma_f32_16x16x32_bf16 v[48:51], a[184:187], v[84:87], v[48:51]\n\t"
  "ds_read_b64_tr_b16 v[78:79], v110 offset:30720\n\t"
  "buffer_load_dwordx4 v[224:227], %[vl16], %[srd], s45 offen offset:0 sc1\n\t"
  "v_mfma_f32_16x16x32_bf16 v[52:55], a[188:191], v[84:87], v[52:55]\n\t"
  "ds_read_b64_tr_b16 v[80:81], v111 offset:28672\n\t"
  "v_cvt_pk_bf16_f32 v112, v20, v21\n\t"
  "v_mfma_f32_16x16x32_bf16 v[56:59], a[180:183], v[88:91], v[56:59]\n\t"
  "ds_read_b64_tr_b16 v[82:83], v111 offset:30720\n\t"
  "v_max_f32 v24, 0, v24\n\t"
  "v_mfma_f32_16x16x32_bf16 v[60:63], a[184:187], v[88:91], v[60:63]\n\t"
  "v_max_f32 v25, 0, v25\n\t"
  "v_mul_f32 v26, v26, v128\n\t"
  "v_mfma_f32_16x16x32_bf16 v[64:67], a[188:191], v[88:91], v[64:67]\n\t"
  "s_waitcnt lgkmcnt(4)\n\t"
  "v_mul_f32 v27, v27, v128\n\t"
  "v_mfma_f32_16x16x32_bf16 v[44:47], a[192:195], v[68:71], v[44:47]\n\t"
  "ds_read_b64_tr_b16 v[84:85], v110 offset:32768\n\t"
  "buffer_load_dwordx4 v[228:231], %[vl16], %[srd], s45 offen offset:1024 sc1\n\t"
  "v_mfma_f32_16x16x32_bf16 v[48:51], a[196:199], v[68:71], v[48:51]\n\t"
  "ds_read_b64_tr_b16 v[86:87], v110 offset:34816\n\t"
  "v_mul_f32 v24, v24, v26\n\t"
  "v_mfma_f32_16x16x32_bf16 v[52:55], a[200:203], v[68:71], v[52:55]\n\t"
  "ds_read_b64_tr_b16 v[88:89], v111 offset:32768\n\t"
  "v_mul_f32 v25, v25, v27\n\t"
  "v_mfma_f32_16x16x32_bf16 v[56:59], a[192:195], v[72:75], v[56:59]\n\t"
  "ds_read_b64_tr_b16 v[90:91], v111 offset:34816\n\t"
  "v_cvt_pk_bf16_f32 v117, v24, v25\n\t"
  "v_mfma_f32_16x16x32_bf16 v[60:63], a[196:199], v[72:75], v[60:63]\n\t"
  "v_cvt_pk_bf16_f32 v113, v24, v25\n\t"
  "v_max_f32 v28, 0, v28\n\t"
  "v_mfma_f32_16x16x32_bf16 v[64:67], a[200:203], v[72:75], v[64:67]\n\t"
  "s_waitcnt lgkmcnt(4)\n\t"
  "buffer_load_dwordx4 v[232:235], %[vl16], %[srd], s45 offen offset:2048 sc1\n\t"
  "v_mfma_f32_16x16x32_bf16 v[44:47], a[204:207], v[76:79], v[44:47]\n\t"
  "ds_read_b64_tr_b16 v[68:69], v110 offset:36864\n\t"
  "v_max_f32 v29, 0, v29\n\t"
  "v_mfma_f32_16x16x32_bf16 v[48:51], a[208:211], v[76:79], v[48:51]\n\t"
  "ds_read_b64_tr_b16 v[70:71], v110 offset:38912\n\t"
  "v_mul_f32 v30, v30, v128\n\t"
  "v_mfma_f32_16x16x32_bf16 v[52:55], a[212:215], v[76:79], v[52:55]\n\t"
  "ds_read_b64_tr_b16 v[72:73], v111 offset:36864\n\t"
  "v_mul_f32 v31, v31, v128\n\t"
  "v_mfma_f32_16x16x32_bf16 v[56:59], a[204:207], v[80:83], v[56:59]\n\t"
  "ds_read_b64_tr_b16 v[74:75], v111 offset:38912\n\t"
  "v_mul_f32 v28, v28, v30\n\t"
  "v_mfma_f32_16x16x32_bf16 v[60:63], a[208:211], v[80:83], v[60:63]\n\t"
  "buffer_load_dwordx4 v[236:239], %[vl16], %[srd], s45 offen offset:3072 sc1\n\t"
  "v_permlane16_swap_b32 v116, v117\n\t"
  "v_mfma_f32_16x16x32_bf16 v[64:67], a[212:215], v[80:83], v[64:67]\n\t"
  "s_waitcnt lgkmcnt(4)\n\t"
  "v_mul_f32 v29, v29, v31\n\t"
  "v_mfma_f32_16x16x32_bf16 v[44:47], a[216:219], v[84:87], v[44:47]\n\t"
  "ds_read_b64_tr_b16 v[76:77], v108 offset:0\n\t"
  "v_cvt_pk_bf16_f32 v118, v28, v29\n\t"
  "v_mfma_f32_16x16x32_bf16 v[48:51], a[220:223], v[84:87], v[48:51]\n\t"
  "ds_read_b64_tr_b16 v[78:79], v108 offset:2048\n\t"
  "v_cvt_pk_bf16_f32 v114, v28, v29\n\t"
  "v_mfma_f32_16x16x32_bf16 v[52:55], a[224:227], v[84:87], v[52:55]\n\t"
  "ds_read_b64_tr_b16 v[80:81], v109 offset:0\n\t"
  "s_mov_b32 s55, 0\n\t"
  "v_mfma_f32_16x16x32_bf16 v[56:59], a[216:219], v[88:91], v[56:59]\n\t"
  "ds_read_b64_tr_b16 v[82:83], v109 offset:2048\n\t"
  "s_add_u32 s45, %[sW2], 24576\n\t"
  "v_mfma_f32_16x16x32_bf16 v[60:63], a[220:223], v[88:91], v[60:63]\n\t"
  "v_permlane16_swap_b32 v118, v115\n\t"
  "v_mfma_f32_16x16x32_bf16 v[64:67], a[224:227], v[88:91], v[64:67]\n\t"
  "s_waitcnt lgkmcnt(4)\n\t"
  "v_permlane32_swap_b32 v117, v115\n\t"
  "v_mfma_f32_16x16x32_bf16 v[44:47], a[228:231], v[68:71], v[44:47]\n\t"
  "ds_read_b64_tr_b16 v[84:85], v108 offset:4096\n\t"
  "buffer_store_dwordx4 v[112:115], %[vh2st], %[srd], s55 offen sc1\n\t"
  "v_mfma_f32_16x16x32_bf16 v[48:51], a[232:235], v[68:71], v[48:51]\n\t"
  "ds_read_b64_tr_b16 v[86:87], v108 offset:6144\n\t"
  "v_max_f32 v32, 0, v32\n\t"
  "v_mfma_f32_16x16x32_bf16 v[52:55], a[236:239], v[68:71], v[52:55]\n\t"
  "ds_read_b64_tr_b16 v[88:89], v109 offset:4096\n\t"
  "buffer_load_dwordx4 v[240:243], %[vl16], %[srd], s45 offen offset:0 sc1\n\t"
  "v_mfma_f32_16x16x32_bf16 v[56:59], a[228:231], v[72:75], v[56:59]\n\t"
  "ds_read_b64_tr_b16 v[90:91], v109 offset:6144\n\t"
  "v_max_f32 v33, 0, v33\n\t"
  "v_mfma_f32_16x16x32_bf16 v[60:63], a[232:235], v[72:75], v[60:63]\n\t"
  "v_mul_f32 v34, v34, v129\n\t"
  "v_mul_f32 v35, v35, v129\n\t"
  "v_mfma_f32_16x16x32_bf16 v[64:67], a[236:239], v[72:75], v[64:67]\n\t"
  "v_mul_f32 v32, v32, v34\n\t"
  "v_mul_f32 v33, v33, v35\n\t"
  "v_mfma_f32_16x16x16_bf16 v[44:47], a[240:241], v[96:97], v[44:47]\n\t"
  "v_cvt_pk_bf16_f32 v124, v32, v33\n\t"
  "v_cvt_pk_bf16_f32 v120, v32, v33\n\t"
  "v_mfma_f32_16x16x16_bf16 v[48:51], a[242:243], v[96:97], v[48:51]\n\t"
  "buffer_load_dwordx4 v[244:247], %[vl16], %[srd], s45 offen offset:1024 sc1\n\t"
  "v_max_f32 v36, 0, v36\n\t"
  "v_mfma_f32_16x16x16_bf16 v[52:55], a[244:245], v[96:97], v[52:55]\n\t"
  "v_max_f32 v37, 0, v37\n\t"
  "v_mul_f32 v38, v38, v129\n\t"
  "v_mfma_f32_16x16x16_bf16 v[56:59], a[240:241], v[98:99], v[56:59]\n\t"
  "v_mul_f32 v39, v39, v129\n\t"
  "v_mul_f32 v36, v36, v38\n\t"
  "v_mfma_f32_16x16x16_bf16 v[60:63], a[242:243], v[98:99], v[60:63]\n\t"
  "v_mul_f32 v37, v37, v39\n\t"
  "v_cvt_pk_bf16_f32 v125, v36, v37\n\t"
  "v_cvt_pk_bf16_f32 v121, v36, v37\n\t"
  "v_max_f32 v40, 0, v40\n\t"
  "v_max_f32 v41, 0, v41\n\t"
  "v_mul_f32 v42, v42, v129\n\t"
  "v_mul_f32 v43, v43, v129\n\t"
  "v_mul_f32 v40, v40, v42\n\t"
  "v_permlane16_swap_b32 v124, v125\n\t"
  "v_mul_f32 v41, v41, v43\n\t"
  "v_cvt_pk_bf16_f32 v126, v40, v41\n\t"
  "v_cvt_pk_bf16_f32 v122, v40, v41\n\t"
  "s_mov_b32 s55, 49152\n\t"
  "v_permlane16_swap_b32 v126, v123\n\t"
  "s_nop 1\n\t"
  "v_permlane32_swap_b32 v125, v123\n\t"
  "buffer_store_dwordx4 v[120:123], %[vh2st], %[srd], s55 offen sc1\n\t"
  "v_mfma_f32_16x16x16_bf16 v[64:67], a[244:245], v[98:99], v[64:67]\n\t"
  "s_waitcnt lgkmcnt(4)\n\t"
  "v_mfma_f32_16x16x32_bf16 v[20:23], a[0:3], v[76:79], 0\n\t"
  "ds_read_b64_tr_b16 v[68:69], v108 offset:8192\n\t"
  "v_add_u32 v110, 122880, %[vtr]\n\t"
  "v_mfma_f32_16x16x32_bf16 v[24:27], a[4:7], v[76:79], 0\n\t"
  "ds_read_b64_tr_b16 v[70:71], v108 offset:10240\n\t"
  "v_xor_b32 v111, 32, %[vtr]\n\t"
  "v_mfma_f32_16x16x32_bf16 v[28:31], a[8:11], v[76:79], 0\n\t"
  "ds_read_b64_tr_b16 v[72:73], v109 offset:8192\n\t"
  "v_add_u32 v111, 122880, v111\n\t"
  "v_mfma_f32_16x16x32_bf16 v[32:35], a[0:3], v[80:83], 0\n\t"
  "ds_read_b64_tr_b16 v[74:75], v109 offset:10240\n\t"
  "v_and_b32 v128, 0xffff0000, v97\n\t"
  "v_mfma_f32_16x16x32_bf16 v[36:39], a[4:7], v[80:83], 0\n\t"
  "v_and_b32 v129, 0xffff0000, v99\n\t"
  "v_rcp_f32 v128, v128\n\t"
  "v_mfma_f32_16x16x32_bf16 v[40:43], a[8:11], v[80:83], 0\n\t"
  "s_waitcnt lgkmcnt(4)\n\t"
  "v_rcp_f32 v129, v129\n\t"
  "v_mfma_f32_16x16x32_bf16 v[20:23], a[12:15], v[84:87], v[20:23]\n\t"
  "ds_read_b64_tr_b16 v[76:77], v108 offset:12288\n\t"
  "s_barrier\n\t"
  "v_mfma_f32_16x16x32_bf16 v[24:27], a[16:19], v[84:87], v[24:27]\n\t"
  "ds_read_b64_tr_b16 v[78:79], v108 offset:14336\n\t"
  "v_mul_f32 v128, v128, v128\n\t"
  "v_mfma_f32_16x16x32_bf16 v[28:31], a[20:23], v[84:87], v[28:31]\n\t"
  "ds_read_b64_tr_b16 v[80:81], v109 offset:12288\n\t"
  "v_mul_f32 v129, v129, v129\n\t"
  "v_mfma_f32_16x16x32_bf16 v[32:35], a[12:15], v[88:91], v[32:35]\n\t"
  "ds_read_b64_tr_b16 v[82:83], v109 offset:14336\n\t"
  "s_mul_i32 s52, %[w], 20480\n\t"
  "v_mfma_f32_16x16x32_bf16 v[36:39], a[16:19], v[88:91], v[36:39]\n\t"
  "v_max_f32 v44, 0, v44\n\t"
  "v_max_f32 v45, 0, v45\n\t"
  "v_mfma_f32_16x16x32_bf16 v[40:43], a[20:23], v[88:91], v[40:43]\n\t"
  "s_waitcnt lgkmcnt(4)\n\t"
  "v_mul_f32 v46, v46, v128\n\t"
  "v_mfma_f32_16x16x32_bf16 v[20:23], a[24:27], v[68:71], v[20:23]\n\t"
  "ds_read_b64_tr_b16 v[84:85], v108 offset:16384\n\t"
  "s_add_u32 m0, s52, 0\n\t"
  "v_mfma_f32_16x16x32_bf16 v[24:27], a[28:31], v[68:71], v[24:27]\n\t"
  "ds_read_b64_tr_b16 v[86:87], v108 offset:18432\n\t"
  "v_mul_f32 v47, v47, v128\n\t"
  "v_mfma_f32_16x16x32_bf16 v[28:31], a[32:35], v[68:71], v[28:31]\n\t"
  "ds_read_b64_tr_b16 v[88:89], v109 offset:16384\n\t"
  "v_mul_f32 v44, v44, v46\n\t"
  "v_mfma_f32_16x16x32_bf16 v[32:35], a[24:27], v[72:75], v[32:35]\n\t"
  "ds_read_b64_tr_b16 v[90:91], v109 offset:18432\n\t"
  "v_mul_f32 v45, v45, v47\n\t"
  "v_mfma_f32_16x16x32_bf16 v[36:39], a[28:31], v[72:75], v[36:39]\n\t"
  "s_add_u32 s46, %[sW2], 26624\n\t"
  "v_cvt_pk_bf16_f32 v116, v44, v45\n\t"
  "v_mfma_f32_16x16x32_bf16 v[40:43], a[32:35], v[72:75], v[40:43]\n\t"
  "s_waitcnt lgkmcnt(4)\n\t"
  "v_cvt_pk_bf16_f32 v112, v44, v45\n\t"
  "v_mfma_f32_16x16x32_bf16 v[20:23], a[36:39], v[76:79], v[20:23]\n\t"
  "ds_read_b64_tr_b16 v[68:69], v108 offset:20480\n\t"
  "v_max_f32 v48, 0, v48\n\t"
  "v_mfma_f32_16x16x32_bf16 v[24:27], a[40:43], v[76:79], v[24:27]\n\t"
  "ds_read_b64_tr_b16 v[70:71], v108 offset:22528\n\t"
  "buffer_load_dwordx4 %[vl16], %[srd], s46 offen offset:0 sc1 lds\n\t"
  "v_mfma_f32_16x16x32_bf16 v[28:31], a[44:47], v[76:79], v[28:31]\n\t"
  "ds_read_b64_tr_b16 v[72:73], v109 offset:20480\n\t"
  "v_max_f32 v49, 0, v49\n\t"
  "v_mfma_f32_16x16x32_bf16 v[32:35], a[36:39], v[80:83], v[32:35]\n\t"
  "ds_read_b64_tr_b16 v[74:75], v109 offset:22528\n\t"
  "v_mul_f32 v50, v50, v128\n\t"
  "v_mfma_f32_16x16x32_bf16 v[36:39], a[40:43], v[80:83], v[36:39]\n\t"
  "buffer_load_dwordx4 %[vl16], %[srd], s46 offen offset:1024 sc1 lds\n\t"
  "v_mul_f32 v51, v51, v128\n\t"
  "v_mfma_f32_16x16x32_bf16 v[40:43], a[44:47], v[80:83], v[40:43]\n\t"
  "s_waitcnt lgkmcnt(4)\n\t"
  "v_mul_f32 v48, v48, v50\n\t"
  "v_mfma_f32_16x16x32_bf16 v[20:23], a[48:51], v[84:87], v[20:23]\n\t"
  "ds_read_b64_tr_b16 v[76:77], v108 offset:24576\n\t"
  "v_mul_f32 v49, v49, v51\n\t"
  "v_mfma_f32_16x16x32_bf16 v[24:27], a[52:55], v[84:87], v[24:27]\n\t"
  "ds_read_b64_tr_b16 v[78:79], v108 offset:26624\n\t"
  "v_cvt_pk_bf16_f32 v117, v48, v49\n\t"
  "v_mfma_f32_16x16x32_bf16 v[28:31], a[56:59], v[84:87], v[28:31]\n\t"
  "ds_read_b64_tr_b16 v[80:81], v109 offset:24576\n\t"
  "buffer_load_dwordx4 %[vl16], %[srd], s46 offen offset:2048 sc1 lds\n\t"
  "v_mfma_f32_16x16x32_bf16 v[32:35], a[48:51], v[88:91], v[32:35]\n\t"
  "ds_read_b64_tr_b16 v[82:83], v109 offset:26624\n\t"
  "v_cvt_pk_bf16_f32 v113, v48, v49\n\t"
  "v_mfma_f32_16x16x32_bf16 v[36:39], a[52:55], v[88:91], v[36:39]\n\t"
  "v_max_f32 v52, 0, v52\n\t"
  "v_max_f32 v53, 0, v53\n\t"
  "v_mfma_f32_16x16x32_bf16 v[40:43], a[56:59], v[88:91], v[40:43]\n\t"
  "s_waitcnt lgkmcnt(4)\n\t"
  "buffer_load_dwordx4 %[vl16], %[srd], s46 offen offset:3072 sc1 lds\n\t"
  "v_mfma_f32_16x16x32_bf16 v[20:23], a[60:63], v[68:71], v[20:23]\n\t"
  "ds_read_b64_tr_b16 v[84:85], v108 offset:28672\n\t"
  "v_mul_f32 v54, v54, v128\n\t"
  "v_mfma_f32_16x16x32_bf16 v[24:27], a[64:67], v[68:71], v[24:27]\n\t"
  "ds_read_b64_tr_b16 v[86:87], v108 offset:30720\n\t"
  "v_mul_f32 v55, v55, v128\n\t"
  "v_mfma_f32_16x16x32_bf16 v[28:31], a[68:71], v[68:71], v[28:31]\n\t"
  "ds_read_b64_tr_b16 v[88:89], v109 offset:28672\n\t"
  "v_mul_f32 v52, v52, v54\n\t"
  "v_mfma_f32_16x16x32_bf16 v[32:35], a[60:63], v[72:75], v[32:35]\n\t"
  "ds_read_b64_tr_b16 v[90:91], v109 offset:30720\n\t"
  "s_add_u32 m0, s52, 4096\n\t"
  "v_mfma_f32_16x16x32_bf16 v[36:39], a[64:67], v[72:75], v[36:39]\n\t"
  "v_permlane16_swap_b32 v116, v117\n\t"
  "v_mul_f32 v53, v53, v55\n\t"
  "v_mfma_f32_16x16x32_bf16 v[40:43], a[68:71], v[72:75], v[40:43]\n\t"
  "s_waitcnt lgkmcnt(4)\n\t"
  "v_cvt_pk_bf16_f32 v118, v52, v53\n\t"
  "v_mfma_f32_16x16x32_bf16 v[20:23], a[72:75], v[76:79], v[20:23]\n\t"
  "ds_read_b64_tr_b16 v[68:69], v108 offset:32768\n\t"
  "s_add_u32 s46, %[sW2], 30720\n\t"
  "v_mfma_f32_16x16x32_bf16 v[24:27], a[76:79], v[76:79], v[24:27]\n\t"
  "ds_read_b64_tr_b16 v[70:71], v108 offset:34816\n\t"
  "v_cvt_pk_bf16_f32 v114, v52, v53\n\t"
  "v_mfma_f32_16x16x32_bf16 v[28:31], a[80:83], v[76:79], v[28:31]\n\t"
  "ds_read_b64_tr_b16 v[72:73], v109 offset:32768\n\t"
  "s_mov_b32 s55, 98304\n\t"
  "v_mfma_f32_16x16x32_bf16 v[32:35], a[72:75], v[80:83], v[32:35]\n\t"
  "ds_read_b64_tr_b16 v[74:75], v109 offset:34816\n\t"
  "buffer_load_dwordx4 %[vl16], %[srd], s46 offen offset:0 sc1 lds\n\t"
  "v_mfma_f32_16x16x32_bf16 v[36:39], a[76:79], v[80:83], v[36:39]\n\t"
  "v_permlane16_swap_b32 v118, v115\n\t"
  "v_mfma_f32_16x16x32_bf16 v[40:43], a[80:83], v[80:83], v[40:43]\n\t"
  "s_waitcnt lgkmcnt(4)\n\t"
  "v_permlane32_swap_b32 v117, v115\n\t"
  "v_mfma_f32_16x16x32_bf16 v[20:23], a[84:87], v[84:87], v[20:23]\n\t"
  "ds_read_b64_tr_b16 v[76:77], v108 offset:36864\n\t"
  "buffer_store_dwordx4 v[112:115], %[vh2st], %[srd], s55 offen sc1\n\t"
  "v_mfma_f32_16x16x32_bf16 v[24:27], a[88:91], v[84:87], v[24:27]\n\t"
  "ds_read_b64_tr_b16 v[78:79], v108 offset:38912\n\t"
  "buffer_load_dwordx4 %[vl16], %[srd], s46 offen offset:1024 sc1 lds\n\t"
  "v_mfma_f32_16x16x32_bf16 v[28:31], a[92:95], v[84:87], v[28:31]\n\t"
  "ds_read_b64_tr_b16 v[80:81], v109 offset:36864\n\t"
  "v_max_f32 v56, 0, v56\n\t"
  "v_mfma_f32_16x16x32_bf16 v[32:35], a[84:87], v[88:91], v[32:35]\n\t"
  "ds_read_b64_tr_b16 v[82:83], v109 offset:38912\n\t"
  "v_max_f32 v57, 0, v57\n\t"
  "v_mfma_f32_16x16x32_bf16 v[36:39], a[88:91], v[88:91], v[36:39]\n\t"
  "buffer_load_dwordx4 %[vl16], %[srd], s46 offen offset:2048 sc1 lds\n\t"
  "v_mul_f32 v58, v58, v129\n\t"
  "v_mfma_f32_16x16x32_bf16 v[40:43], a[92:95], v[88:91], v[40:43]\n\t"
  "s_waitcnt lgkmcnt(4)\n\t"
  "v_mul_f32 v59, v59, v129\n\t"
  "v_mfma_f32_16x16x32_bf16 v[20:23], a[96:99], v[68:71], v[20:23]\n\t"
  "ds_read_b64_tr_b16 v[84:85], v110 offset:0\n\t"
  "v_mul_f32 v56, v56, v58\n\t"
  "v_mfma_f32_16x16x32_bf16 v[24:27], a[100:103], v[68:71], v[24:27]\n\t"
  "ds_read_b64_tr_b16 v[86:87], v110 offset:2048\n\t"
  "buffer_load_dwordx4 %[vl16], %[srd], s46 offen offset:3072 sc1 lds\n\t"
  "v_mfma_f32_16x16x32_bf16 v[28:31], a[104:107], v[68:71], v[28:31]\n\t"
  "ds_read_b64_tr_b16 v[88:89], v111 offset:0\n\t"
  "v_mul_f32 v57, v57, v59\n\t"
  "v_mfma_f32_16x16x32_bf16 v[32:35], a[96:99], v[72:75], v[32:35]\n\t"
  "ds_read_b64_tr_b16 v[90:91], v111 offset:2048\n\t"
  "v_cvt_pk_bf16_f32 v124, v56, v57\n\t"
  "v_mfma_f32_16x16x32_bf16 v[36:39], a[100:103], v[72:75], v[36:39]\n\t"
  "buffer_load_dwordx2 v[100:101], v136, %[srd], s54 offen offset:0 sc1\n\t"
  "buffer_load_dwordx2 v[102:103], v136, %[srd], s54 offen offset:1024 sc1\n\t"
  "v_mfma_f32_16x16x32_bf16 v[40:43], a[104:107], v[72:75], v[40:43]\n\t"
  "s_waitcnt lgkmcnt(4)\n\t"
  "buffer_load_dwordx2 v[104:105], v136, %[srd], s54 offen offset:2048 sc1\n\t"
  "v_mfma_f32_16x16x32_bf16 v[20:23], a[108:111], v[76:79], v[20:23]\n\t"
  "ds_read_b64_tr_b16 v[68:69], v110 offset:4096\n\t"
  "s_movk_i32 s56, 0\n\t"
  "v_mfma_f32_16x16x32_bf16 v[24:27], a[112:115], v[76:79], v[24:27]\n\t"
  "ds_read_b64_tr_b16 v[70:71], v110 offset:6144\n\t"
  "s_waitcnt vmcnt(12)\n\t"
  "v_mfma_f32_16x16x32_bf16 v[28:31], a[116:119], v[76:79], v[28:31]\n\t"
  "ds_read_b64_tr_b16 v[72:73], v111 offset:4096\n\t"
  "buffer_load_dwordx2 v[106:107], v136, %[srd], s54 offen offset:3072 sc1\n\t"
  "v_mfma_f32_16x16x32_bf16 v[32:35], a[108:111], v[80:83], v[32:35]\n\t"
  "ds_read_b64_tr_b16 v[74:75], v111 offset:6144\n\t"
  "buffer_store_dwordx4 v[132:135], %[vflw], %[srd], s56 offen sc1\n\t"
  "v_mfma_f32_16x16x32_bf16 v[36:39], a[112:115], v[80:83], v[36:39]\n\t"
  "s_add_u32 m0, s52, 8192\n\t"
  "s_add_u32 s46, %[sW2], 34816\n\t"
  "v_mfma_f32_16x16x32_bf16 v[40:43], a[116:119], v[80:83], v[40:43]\n\t"
  "s_waitcnt lgkmcnt(4)\n\t"
  "v_cvt_pk_bf16_f32 v120, v56, v57\n\t"
  "v_mfma_f32_16x16x32_bf16 v[20:23], a[120:123], v[84:87], v[20:23]\n\t"
  "ds_read_b64_tr_b16 v[76:77], v110 offset:8192\n\t"
  "v_xor_b32 v108, 64, %[vtr]\n\t"
  "v_mfma_f32_16x16x32_bf16 v[24:27], a[124:127], v[84:87], v[24:27]\n\t"
  "ds_read_b64_tr_b16 v[78:79], v110 offset:10240\n\t"
  "v_add_u32 v108, 81920, v108\n\t"
  "v_mfma_f32_16x16x32_bf16 v[28:31], a[128:131], v[84:87], v[28:31]\n\t"
  "ds_read_b64_tr_b16 v[80:81], v111 offset:8192\n\t"
  "v_xor_b32 v109, 96, %[vtr]\n\t"
  "v_mfma_f32_16x16x32_bf16 v[32:35], a[120:123], v[88:91], v[32:35]\n\t"
  "ds_read_b64_tr_b16 v[82:83], v111 offset:10240\n\t"
  "v_add_u32 v109, 81920, v109\n\t"
  "v_mfma_f32_16x16x32_bf16 v[36:39], a[124:127], v[88:91], v[36:39]\n\t"
  "buffer_load_dwordx4 %[vl16], %[srd], s46 offen offset:0 sc1 lds\n\t"
  "buffer_load_dwordx4 %[vl16], %[srd], s46 offen offset:1024 sc1 lds\n\t"
  "v_mfma_f32_16x16x32_bf16 v[40:43], a[128:131], v[88:91], v[40:43]\n\t"
  "s_waitcnt lgkmcnt(4)\n\t"
  "v_max_f32 v60, 0, v60\n\t"
  "v_mfma_f32_16x16x32_bf16 v[20:23], a[132:135], v[68:71], v[20:23]\n\t"
  "ds_read_b64_tr_b16 v[84:85], v110 offset:12288\n\t"
  "buffer_load_dwordx4 %[vl16], %[srd], s46 offen offset:2048 sc1 lds\n\t"
  "v_mfma_f32_16x16x32_bf16 v[24:27], a[136:139], v[68:71], v[24:27]\n\t"
  "ds_read_b64_tr_b16 v[86:87], v110 offset:14336\n\t"
  "v_max_f32 v61, 0, v61\n\t"
  "v_mfma_f32_16x16x32_bf16 v[28:31], a[140:143], v[68:71], v[28:31]\n\t"
  "ds_read_b64_tr_b16 v[88:89], v111 offset:12288\n\t"
  "v_mul_f32 v62, v62, v129\n\t"
  "v_mfma_f32_16x16x32_bf16 v[32:35], a[132:135], v[72:75], v[32:35]\n\t"
  "ds_read_b64_tr_b16 v[90:91], v111 offset:14336\n\t"
  "v_mul_f32 v63, v63, v129\n\t"
  "v_mfma_f32_16x16x32_bf16 v[36:39], a[136:139], v[72:75], v[36:39]\n\t"
  "buffer_load_dwordx4 %[vl16], %[srd], s46 offen offset:3072 sc1 lds\n\t"
  "v_mul_f32 v60, v60, v62\n\t"
  "v_mfma_f32_16x16x32_bf16 v[40:43], a[140:143], v[72:75], v[40:43]\n\t"
  "s_waitcnt lgkmcnt(4)\n\t"
  "v_mul_f32 v61, v61, v63\n\t"
  "v_mfma_f32_16x16x32_bf16 v[20:23], a[144:147], v[76:79], v[20:23]\n\t"
  "ds_read_b64_tr_b16 v[68:69], v110 offset:16384\n\t"
  "v_cvt_pk_bf16_f32 v125, v60, v61\n\t"
  "v_mfma_f32_16x16x32_bf16 v[24:27], a[148:151], v[76:79], v[24:27]\n\t"
  "ds_read_b64_tr_b16 v[70:71], v110 offset:18432\n\t"
  "s_add_u32 m0, s52, 12288\n\t"
  "v_mfma_f32_16x16x32_bf16 v[28:31], a[152:155], v[76:79], v[28:31]\n\t"
  "ds_read_b64_tr_b16 v[72:73], v111 offset:16384\n\t"
  "v_cvt_pk_bf16_f32 v121, v60, v61\n\t"
  "v_mfma_f32_16x16x32_bf16 v[32:35], a[144:147], v[80:83], v[32:35]\n\t"
  "ds_read_b64_tr_b16 v[74:75], v111 offset:18432\n\t"
  "v_max_f32 v64, 0, v64\n\t"
  "v_mfma_f32_16x16x32_bf16 v[36:39], a[148:151], v[80:83], v[36:39]\n\t"
  "s_add_u32 s46, %[sW2], 38912\n\t"
  "v_max_f32 v65, 0, v65\n\t"
  "v_mfma_f32_16x16x32_bf16 v[40:43], a[152:155], v[80:83], v[40:43]\n\t"
  "s_waitcnt lgkmcnt(4)\n\t"
  "v_mul_f32 v66, v66, v129\n\t"
  "v_mfma_f32_16x16x32_bf16 v[20:23], a[156:159], v[84:87], v[20:23]\n\t"
  "ds_read_b64_tr_b16 v[76:77], v110 offset:20480\n\t"
  "v_mul_f32 v67, v67, v129\n\t"
  "v_mfma_f32_16x16x32_bf16 v[24:27], a[160:163], v[84:87], v[24:27]\n\t"
  "ds_read_b64_tr_b16 v[78:79], v110 offset:22528\n\t"
  "v_mul_f32 v64, v64, v66\n\t"
  "v_mfma_f32_16x16x32_bf16 v[28:31], a[164:167], v[84:87], v[28:31]\n\t"
  "ds_read_b64_tr_b16 v[80:81], v111 offset:20480\n\t"
  "buffer_load_dwordx4 %[vl16], %[srd], s46 offen offset:0 sc1 lds\n\t"
  "v_mfma_f32_16x16x32_bf16 v[32:35], a[156:159], v[88:91], v[32:35]\n\t"
  "ds_read_b64_tr_b16 v[82:83], v111 offset:22528\n\t"
  "v_permlane16_swap_b32 v124, v125\n\t"
  "v_mfma_f32_16x16x32_bf16 v[36:39], a[160:163], v[88:91], v[36:39]\n\t"
  "v_mul_f32 v65, v65, v67\n\t"
  "v_cvt_pk_bf16_f32 v126, v64, v65\n\t"
  "v_mfma_f32_16x16x32_bf16 v[40:43], a[164:167], v[88:91], v[40:43]\n\t"
  "s_waitcnt lgkmcnt(4)\n\t"
  "buffer_load_dwordx4 %[vl16], %[srd], s46 offen offset:1024 sc1 lds\n\t"
  "v_mfma_f32_16x16x32_bf16 v[20:23], a[168:171], v[68:71], v[20:23]\n\t"
  "ds_read_b64_tr_b16 v[84:85], v110 offset:24576\n\t"
  "v_cvt_pk_bf16_f32 v122, v64, v65\n\t"
  "v_mfma_f32_16x16x32_bf16 v[24:27], a[172:175], v[68:71], v[24:27]\n\t"
  "ds_read_b64_tr_b16 v[86:87], v110 offset:26624\n\t"
  "s_mov_b32 s55, 147456\n\t"
  "v_mfma_f32_16x16x32_bf16 v[28:31], a[176:179], v[68:71], v[28:31]\n\t"
  "ds_read_b64_tr_b16 v[88:89], v111 offset:24576\n\t"
  "v_permlane16_swap_b32 v126, v123\n\t"
  "v_mfma_f32_16x16x32_bf16 v[32:35], a[168:171], v[72:75], v[32:35]\n\t"
  "ds_read_b64_tr_b16 v[90:91], v111 offset:26624\n\t"
  "buffer_load_dwordx4 %[vl16], %[srd], s46 offen offset:2048 sc1 lds\n\t"
  "v_mfma_f32_16x16x32_bf16 v[36:39], a[172:175], v[72:75], v[36:39]\n\t"
  "v_permlane32_swap_b32 v125, v123\n\t"
  "buffer_store_dwordx4 v[120:123], %[vh2st], %[srd], s55 offen sc1\n\t"
  "v_mfma_f32_16x16x32_bf16 v[40:43], a[176:179], v[72:75], v[40:43]\n\t"
  "s_waitcnt lgkmcnt(4)\n\t"
  "v_mfma_f32_16x16x32_bf16 v[20:23], a[180:183], v[76:79], v[20:23]\n\t"
  "ds_read_b64_tr_b16 v[68:69], v110 offset:28672\n\t"
  "buffer_load_dwordx4 %[vl16], %[srd], s46 offen offset:3072 sc1 lds\n\t"
  "v_mfma_f32_16x16x32_bf16 v[24:27], a[184:187], v[76:79], v[24:27]\n\t"
  "ds_read_b64_tr_b16 v[70:71], v110 offset:30720\n\t"
  "v_mfma_f32_16x16x32_bf16 v[28:31], a[188:191], v[76:79], v[28:31]\n\t"
  "ds_read_b64_tr_b16 v[72:73], v111 offset:28672\n\t"
  "v_mfma_f32_16x16x32_bf16 v[32:35], a[180:183], v[80:83], v[32:35]\n\t"
  "ds_read_b64_tr_b16 v[74:75], v111 offset:30720\n\t"
  "s_add_u32 m0, s52, 16384\n\t"
  "v_mfma_f32_16x16x32_bf16 v[36:39], a[184:187], v[80:83], v[36:39]\n\t"
  "v_mfma_f32_16x16x32_bf16 v[40:43], a[188:191], v[80:83], v[40:43]\n\t"
  "s_waitcnt lgkmcnt(4)\n\t"
  "v_mfma_f32_16x16x32_bf16 v[20:23], a[192:195], v[84:87], v[20:23]\n\t"
  "ds_read_b64_tr_b16 v[76:77], v110 offset:32768\n\t"
  "v_mfma_f32_16x16x32_bf16 v[24:27], a[196:199], v[84:87], v[24:27]\n\t"
  "ds_read_b64_tr_b16 v[78:79], v110 offset:34816\n\t"
  "s_add_u32 s46, %[sW2], 43008\n\t"
  "v_mfma_f32_16x16x32_bf16 v[28:31], a[200:203], v[84:87], v[28:31]\n\t"
  "ds_read_b64_tr_b16 v[80:81], v111 offset:32768\n\t"
  "v_mfma_f32_16x16x32_bf16 v[32:35], a[192:195], v[88:91], v[32:35]\n\t"
  "ds_read_b64_tr_b16 v[82:83], v111 offset:34816\n\t"
  "v_mfma_f32_16x16x32_bf16 v[36:39], a[196:199], v[88:91], v[36:39]\n\t"
  "buffer_load_dwordx4 %[vl16], %[srd], s46 offen offset:0 sc1 lds\n\t"
  "v_mfma_f32_16x16x32_bf16 v[40:43], a[200:203], v[88:91], v[40:43]\n\t"
  "s_waitcnt lgkmcnt(4)\n\t"
  "v_mfma_f32_16x16x32_bf16 v[20:23], a[204:207], v[68:71], v[20:23]\n\t"
  "ds_read_b64_tr_b16 v[84:85], v110 offset:36864\n\t"
  "v_mfma_f32_16x16x32_bf16 v[24:27], a[208:211], v[68:71], v[24:27]\n\t"
  "ds_read_b64_tr_b16 v[86:87], v110 offset:38912\n\t"
  "buffer_load_dwordx4 %[vl16], %[srd], s46 offen offset:1024 sc1 lds\n\t"
  "v_mfma_f32_16x16x32_bf16 v[28:31], a[212:215], v[68:71], v[28:31]\n\t"
  "ds_read_b64_tr_b16 v[88:89], v111 offset:36864\n\t"
  "v_mfma_f32_16x16x32_bf16 v[32:35], a[204:207], v[72:75], v[32:35]\n\t"
  "ds_read_b64_tr_b16 v[90:91], v111 offset:38912\n\t"
  "v_mfma_f32_16x16x32_bf16 v[36:39], a[208:211], v[72:75], v[36:39]\n\t"
  "s_waitcnt vmcnt(14)\n\t"
  "v_min_u32 v131, v101, v103\n\t"
  "v_mfma_f32_16x16x32_bf16 v[40:43], a[212:215], v[72:75], v[40:43]\n\t"
  "s_waitcnt lgkmcnt(4)\n\t"
  "s_waitcnt vmcnt(12)\n\t"
  "v_min3_u32 v131, v131, v105, v107\n\t"
  "v_mfma_f32_16x16x32_bf16 v[20:23], a[216:219], v[76:79], v[20:23]\n\t"
  "ds_read_b64_tr_b16 v[68:69], v108 offset:0\n\t"
  "v_cmp_eq_u32 vcc, 0, v131\n\t"
  "v_mfma_f32_16x16x32_bf16 v[24:27], a[220:223], v[76:79], v[24:27]\n\t"
  "ds_read_b64_tr_b16 v[70:71], v108 offset:2048\n\t"
  "buffer_load_dwordx4 %[vl16], %[srd], s46 offen offset:2048 sc1 lds\n\t"
  "v_mfma_f32_16x16x32_bf16 v[28:31], a[224:227], v[76:79], v[28:31]\n\t"
  "ds_read_b64_tr_b16 v[72:73], v109 offset:0\n\t"
  "s_cbranch_vccnz Lst1_slow%=\n\t"
  "Lst1_back%=:\n\t"
  "v_mfma_f32_16x16x32_bf16 v[32:35], a[216:219], v[80:83], v[32:35]\n\t"
  "ds_read_b64_tr_b16 v[74:75], v109 offset:2048\n\t"
  "buffer_load_dwordx4 %[vl16], %[srd], s46 offen offset:3072 sc1 lds\n\t"
  "v_mfma_f32_16x16x32_bf16 v[36:39], a[220:223], v[80:83], v[36:39]\n\t"
  "v_mfma_f32_16x16x32_bf16 v[40:43], a[224:227], v[80:83], v[40:43]\n\t"
  "s_waitcnt lgkmcnt(4)\n\t"
  "v_mfma_f32_16x16x32_bf16 v[20:23], a[228:231], v[84:87], v[20:23]\n\t"
  "ds_read_b64_tr_b16 v[76:77], v108 offset:4096\n\t"
  "v_mfma_f32_16x16x32_bf16 v[24:27], a[232:235], v[84:87], v[24:27]\n\t"
  "ds_read_b64_tr_b16 v[78:79], v108 offset:6144\n\t"
  "v_mfma_f32_16x16x32_bf16 v[28:31], a[236:239], v[84:87], v[28:31]\n\t"
  "ds_read_b64_tr_b16 v[80:81], v109 offset:4096\n\t"
  "v_mfma_f32_16x16x32_bf16 v[32:35], a[228:231], v[88:91], v[32:35]\n\t"
  "ds_read_b64_tr_b16 v[82:83], v109 offset:6144\n\t"
  "v_mfma_f32_16x16x32_bf16 v[36:39], a[232:235], v[88:91], v[36:39]\n\t"
  "v_mfma_f32_16x16x32_bf16 v[40:43], a[236:239], v[88:91], v[40:43]\n\t"
  "v_mfma_f32_16x16x16_bf16 v[20:23], a[240:241], v[100:101], v[20:23]\n\t"
  "v_mfma_f32_16x16x16_bf16 v[24:27], a[242:243], v[100:101], v[24:27]\n\t"
  "v_mfma_f32_16x16x16_bf16 v[28:31], a[244:245], v[100:101], v[28:31]\n\t"
  "v_mfma_f32_16x16x16_bf16 v[32:35], a[240:241], v[102:103], v[32:35]\n\t"
  "v_mfma_f32_16x16x16_bf16 v[36:39], a[242:243], v[102:103], v[36:39]\n\t"
  "v_mfma_f32_16x16x16_bf16 v[40:43], a[244:245], v[102:103], v[40:43]\n\t"
  "s_waitcnt lgkmcnt(4)\n\t"
  "v_mfma_f32_16x16x32_bf16 v[44:47], a[0:3], v[68:71], 0\n\t"
  "ds_read_b64_tr_b16 v[84:85], v108 offset:8192\n\t"
  "v_xor_b32 v110, 64, %[vtr]\n\t"
  "v_mfma_f32_16x16x32_bf16 v[48:51], a[4:7], v[68:71], 0\n\t"
  "ds_read_b64_tr_b16 v[86:87], v108 offset:10240\n\t"
  "v_add_u32 v110, 122880, v110\n\t"
  "v_mfma_f32_16x16x32_bf16 v[52:55], a[8:11], v[68:71], 0\n\t"
  "ds_read_b64_tr_b16 v[88:89], v109 offset:8192\n\t"
  "v_xor_b32 v111, 96, %[vtr]\n\t"
  "v_mfma_f32_16x16x32_bf16 v[56:59], a[0:3], v[72:75], 0\n\t"
  "ds_read_b64_tr_b16 v[90:91], v109 offset:10240\n\t"
  "v_add_u32 v111, 122880, v111\n\t"
  "v_mfma_f32_16x16x32_bf16 v[60:63], a[4:7], v[72:75], 0\n\t"
  "v_and_b32 v128, 0xffff0000, v101\n\t"
  "v_and_b32 v129, 0xffff0000, v103\n\t"
  "v_mfma_f32_16x16x32_bf16 v[64:67], a[8:11], v[72:75], 0\n\t"
  "s_waitcnt lgkmcnt(4)\n\t"
  "v_rcp_f32 v128, v128\n\t"
  "v_mfma_f32_16x16x32_bf16 v[44:47], a[12:15], v[76:79], v[44:47]\n\t"
  "ds_read_b64_tr_b16 v[68:69], v108 offset:12288\n\t"
  "s_add_u32 s45, %[sW2], 47104\n\t"
  "v_mfma_f32_16x16x32_bf16 v[48:51], a[16:19], v[76:79], v[48:51]\n\t"
  "ds_read_b64_tr_b16 v[70:71], v108 offset:14336\n\t"
  "v_rcp_f32 v129, v129\n\t"
  "v_mfma_f32_16x16x32_bf16 v[52:55], a[20:23], v[76:79], v[52:55]\n\t"
  "ds_read_b64_tr_b16 v[72:73], v109 offset:12288\n\t"
  "v_mul_f32 v128, v128, v128\n\t"
  "v_mfma_f32_16x16x32_bf16 v[56:59], a[12:15], v[80:83], v[56:59]\n\t"
  "ds_read_b64_tr_b16 v[74:75], v109 offset:14336\n\t"
  "v_mul_f32 v129, v129, v129\n\t"
  "v_mfma_f32_16x16x32_bf16 v[60:63], a[16:19], v[80:83], v[60:63]\n\t"
  "buffer_load_dwordx4 a[0:3], %[vl16], %[srd], s45 offen offset:0 sc1\n\t"
  "v_max_f32 v20, 0, v20\n\t"
  "v_mfma_f32_16x16x32_bf16 v[64:67], a[20:23], v[80:83], v[64:67]\n\t"
  "s_waitcnt lgkmcnt(4)\n\t"
  "v_max_f32 v21, 0, v21\n\t"
  "v_mfma_f32_16x16x32_bf16 v[44:47], a[24:27], v[84:87], v[44:47]\n\t"
  "ds_read_b64_tr_b16 v[76:77], v108 offset:16384\n\t"
  "v_mul_f32 v22, v22, v128\n\t"
  "v_mfma_f32_16x16x32_bf16 v[48:51], a[28:31], v[84:87], v[48:51]\n\t"
  "ds_read_b64_tr_b16 v[78:79], v108 offset:18432\n\t"
  "v_mul_f32 v23, v23, v128\n\t"
  "v_mfma_f32_16x16x32_bf16 v[52:55], a[32:35], v[84:87], v[52:55]\n\t"
  "ds_read_b64_tr_b16 v[80:81], v109 offset:16384\n\t"
  "buffer_load_dwordx4 a[4:7], %[vl16], %[srd], s45 offen offset:1024 sc1\n\t"
  "v_mfma_f32_16x16x32_bf16 v[56:59], a[24:27], v[88:91], v[56:59]\n\t"
  "ds_read_b64_tr_b16 v[82:83], v109 offset:18432\n\t"
  "v_mul_f32 v20, v20, v22\n\t"
  "v_mfma_f32_16x16x32_bf16 v[60:63], a[28:31], v[88:91], v[60:63]\n\t"
  "v_mul_f32 v21, v21, v23\n\t"
  "v_cvt_pk_bf16_f32 v116, v20, v21\n\t"
  "v_mfma_f32_16x16x32_bf16 v[64:67], a[32:35], v[88:91], v[64:67]\n\t"
  "s_waitcnt lgkmcnt(4)\n\t"
  "v_cvt_pk_bf16_f32 v112, v20, v21\n\t"
  "v_mfma_f32_16x16x32_bf16 v[44:47], a[36:39], v[68:71], v[44:47]\n\t"
  "ds_read_b64_tr_b16 v[84:85], v108 offset:20480\n\t"
  "s_movk_i32 s56, 8192\n\t"
  "v_mfma_f32_16x16x32_bf16 v[48:51], a[40:43], v[68:71], v[48:51]\n\t"
  "ds_read_b64_tr_b16 v[86:87], v108 offset:22528\n\t"
  "s_waitcnt vmcnt(7)\n\t"
  "v_mfma_f32_16x16x32_bf16 v[52:55], a[44:47], v[68:71], v[52:55]\n\t"
  "ds_read_b64_tr_b16 v[88:89], v109 offset:20480\n\t"
  "buffer_store_dwordx4 v[132:135], %[vflw], %[srd], s56 offen sc1\n\t"
  "v_mfma_f32_16x16x32_bf16 v[56:59], a[36:39], v[72:75], v[56:59]\n\t"
  "ds_read_b64_tr_b16 v[90:91], v109 offset:22528\n\t"
  "buffer_load_dwordx4 a[8:11], %[vl16], %[srd], s45 offen offset:2048 sc1\n\t"
  "v_mfma_f32_16x16x32_bf16 v[60:63], a[40:43], v[72:75], v[60:63]\n\t"
  "buffer_load_dwordx4 a[12:15], %[vl16], %[srd], s45 offen offset:3072 sc1\n\t"
  "v_max_f32 v24, 0, v24\n\t"
  "v_mfma_f32_16x16x32_bf16 v[64:67], a[44:47], v[72:75], v[64:67]\n\t"
  "s_waitcnt lgkmcnt(4)\n\t"
  "v_max_f32 v25, 0, v25\n\t"
  "v_mfma_f32_16x16x32_bf16 v[44:47], a[48:51], v[76:79], v[44:47]\n\t"
  "ds_read_b64_tr_b16 v[68:69], v108 offset:24576\n\t"
  "v_mul_f32 v26, v26, v128\n\t"
  "v_mfma_f32_16x16x32_bf16 v[48:51], a[52:55], v[76:79], v[48:51]\n\t"
  "ds_read_b64_tr_b16 v[70:71], v108 offset:26624\n\t"
  "v_mul_f32 v27, v27, v128\n\t"
  "v_mfma_f32_16x16x32_bf16 v[52:55], a[56:59], v[76:79], v[52:55]\n\t"
  "ds_read_b64_tr_b16 v[72:73], v109 offset:24576\n\t"
  "s_add_u32 s45, %[sW2], 51200\n\t"
  "v_mfma_f32_16x16x32_bf16 v[56:59], a[48:51], v[80:83], v[56:59]\n\t"
  "ds_read_b64_tr_b16 v[74:75], v109 offset:26624\n\t"
  "v_mul_f32 v24, v24, v26\n\t"
  "v_mfma_f32_16x16x32_bf16 v[60:63], a[52:55], v[80:83], v[60:63]\n\t"
  "v_mul_f32 v25, v25, v27\n\t"
  "v_cvt_pk_bf16_f32 v117, v24, v25\n\t"
  "v_mfma_f32_16x16x32_bf16 v[64:67], a[56:59], v[80:83], v[64:67]\n\t"
  "s_waitcnt lgkmcnt(4)\n\t"
  "v_cvt_pk_bf16_f32 v113, v24, v25\n\t"
  "v_mfma_f32_16x16x32_bf16 v[44:47], a[60:63], v[84:87], v[44:47]\n\t"
  "ds_read_b64_tr_b16 v[76:77], v108 offset:28672\n\t"
  "s_add_u32 s57, %[sFL], 0\n\t"
  "v_mfma_f32_16x16x32_bf16 v[48:51], a[64:67], v[84:87], v[48:51]\n\t"
  "ds_read_b64_tr_b16 v[78:79], v108 offset:30720\n\t"
  "buffer_load_dwordx2 v[138:139], v137, %[srd], s57 offen sc1\n\t"
  "v_mfma_f32_16x16x32_bf16 v[52:55], a[68:71], v[84:87], v[52:55]\n\t"
  "ds_read_b64_tr_b16 v[80:81], v109 offset:28672\n\t"
  "buffer_load_dwordx4 a[16:19], %[vl16], %[srd], s45 offen offset:0 sc1\n\t"
  "v_mfma_f32_16x16x32_bf16 v[56:59], a[60:63], v[88:91], v[56:59]\n\t"
  "ds_read_b64_tr_b16 v[82:83], v109 offset:30720\n\t"
  "v_max_f32 v28, 0, v28\n\t"
  "v_mfma_f32_16x16x32_bf16 v[60:63], a[64:67], v[88:91], v[60:63]\n\t"
  "buffer_load_dwordx4 a[20:23], %[vl16], %[srd], s45 offen offset:1024 sc1\n\t"
  "v_max_f32 v29, 0, v29\n\t"
  "v_mfma_f32_16x16x32_bf16 v[64:67], a[68:71], v[88:91], v[64:67]\n\t"
  "s_waitcnt lgkmcnt(4)\n\t"
  "v_mul_f32 v30, v30, v128\n\t"
  "v_mfma_f32_16x16x32_bf16 v[44:47], a[72:75], v[68:71], v[44:47]\n\t"
  "ds_read_b64_tr_b16 v[84:85], v108 offset:32768\n\t"
  "v_mul_f32 v31, v31, v128\n\t"
  "v_mfma_f32_16x16x32_bf16 v[48:51], a[76:79], v[68:71], v[48:51]\n\t"
  "ds_read_b64_tr_b16 v[86:87], v108 offset:34816\n\t"
  "v_mul_f32 v28, v28, v30\n\t"
  "v_mfma_f32_16x16x32_bf16 v[52:55], a[80:83], v[68:71], v[52:55]\n\t"
  "ds_read_b64_tr_b16 v[88:89], v109 offset:32768\n\t"
  "buffer_load_dwordx4 a[24:27], %[vl16], %[srd], s45 offen offset:2048 sc1\n\t"
  "v_mfma_f32_16x16x32_bf16 v[56:59], a[72:75], v[72:75], v[56:59]\n\t"
  "ds_read_b64_tr_b16 v[90:91], v109 offset:34816\n\t"
  "v_permlane16_swap_b32 v116, v117\n\t"
  "v_mfma_f32_16x16x32_bf16 v[60:63], a[76:79], v[72:75], v[60:63]\n\t"
  "v_mul_f32 v29, v29, v31\n\t"
  "v_cvt_pk_bf16_f32 v118, v28, v29\n\t"
  "v_mfma_f32_16x16x32_bf16 v[64:67], a[80:83], v[72:75], v[64:67]\n\t"
  "s_waitcnt lgkmcnt(4)\n\t"
  "v_cvt_pk_bf16_f32 v114, v28, v29\n\t"
  "v_mfma_f32_16x16x32_bf16 v[44:47], a[84:87], v[76:79], v[44:47]\n\t"
  "ds_read_b64_tr_b16 v[68:69], v108 offset:36864\n\t"
  "s_mov_b32 s55, 3145728\n\t"
  "v_mfma_f32_16x16x32_bf16 v[48:51], a[88:91], v[76:79], v[48:51]\n\t"
  "ds_read_b64_tr_b16 v[70:71], v108 offset:38912\n\t"
  "buffer_load_dwordx4 a[28:31], %[vl16], %[srd], s45 offen offset:3072 sc1\n\t"
  "v_mfma_f32_16x16x32_bf16 v[52:55], a[92:95], v[76:79], v[52:55]\n\t"
  "ds_read_b64_tr_b16 v[72:73], v109 offset:36864\n\t"
  "v_permlane16_swap_b32 v118, v115\n\t"
  "v_mfma_f32_16x16x32_bf16 v[56:59], a[84:87], v[80:83], v[56:59]\n\t"
  "ds_read_b64_tr_b16 v[74:75], v109 offset:38912\n\t"
  "v_permlane32_swap_b32 v117, v115\n\t"
  "v_mfma_f32_16x16x32_bf16 v[60:63], a[88:91], v[80:83], v[60:63]\n\t"
  "buffer_store_dwordx4 v[112:115], %[vh2st], %[srd], s55 offen sc1\n\t"
  "v_max_f32 v32, 0, v32\n\t"
  "v_mfma_f32_16x16x32_bf16 v[64:67], a[92:95], v[80:83], v[64:67]\n\t"
  "s_waitcnt lgkmcnt(4)\n\t"
  "s_add_u32 s45, %[sW2], 55296\n\t"
  "v_mfma_f32_16x16x32_bf16 v[44:47], a[96:99], v[84:87], v[44:47]\n\t"
  "ds_read_b64_tr_b16 v[76:77], v110 offset:0\n\t"
  "v_max_f32 v33, 0, v33\n\t"
  "v_mfma_f32_16x16x32_bf16 v[48:51], a[100:103], v[84:87], v[48:51]\n\t"
  "ds_read_b64_tr_b16 v[78:79], v110 offset:2048\n\t"
  "v_mul_f32 v34, v34, v129\n\t"
  "v_mfma_f32_16x16x32_bf16 v[52:55], a[104:107], v[84:87], v[52:55]\n\t"
  "ds_read_b64_tr_b16 v[80:81], v111 offset:0\n\t"
  "v_mul_f32 v35, v35, v129\n\t"
  "v_mfma_f32_16x16x32_bf16 v[56:59], a[96:99], v[88:91], v[56:59]\n\t"
  "ds_read_b64_tr_b16 v[82:83], v111 offset:2048\n\t"
  "buffer_load_dwordx4 a[32:35], %[vl16], %[srd], s45 offen offset:0 sc1\n\t"
  "v_mfma_f32_16x16x32_bf16 v[60:63], a[100:103], v[88:91], v[60:63]\n\t"
  "v_mul_f32 v32, v32, v34\n\t"
  "v_mul_f32 v33, v33, v35\n\t"
  "v_mfma_f32_16x16x32_bf16 v[64:67], a[104:107], v[88:91], v[64:67]\n\t"
  "s_waitcnt lgkmcnt(4)\n\t"
  "v_cvt_pk_bf16_f32 v124, v32, v33\n\t"
  "v_mfma_f32_16x16x32_bf16 v[44:47], a[108:111], v[68:71], v[44:47]\n\t"
  "ds_read_b64_tr_b16 v[84:85], v110 offset:4096\n\t"
  "v_cvt_pk_bf16_f32 v120, v32, v33\n\t"
  "v_mfma_f32_16x16x32_bf16 v[48:51], a[112:115], v[68:71], v[48:51]\n\t"
  "ds_read_b64_tr_b16 v[86:87], v110 offset:6144\n\t"
  "buffer_load_dwordx4 a[36:39], %[vl16], %[srd], s45 offen offset:1024 sc1\n\t"
  "v_mfma_f32_16x16x32_bf16 v[52:55], a[116:119], v[68:71], v[52:55]\n\t"
  "ds_read_b64_tr_b16 v[88:89], v111 offset:4096\n\t"
  "v_max_f32 v36, 0, v36\n\t"
  "v_mfma_f32_16x16x32_bf16 v[56:59], a[108:111], v[72:75], v[56:59]\n\t"
  "ds_read_b64_tr_b16 v[90:91], v111 offset:6144\n\t"
  "v_max_f32 v37, 0, v37\n\t"
  "v_mfma_f32_16x16x32_bf16 v[60:63], a[112:115], v[72:75], v[60:63]\n\t"
  "v_mul_f32 v38, v38, v129\n\t"
  "v_mul_f32 v39, v39, v129\n\t"
  "v_mfma_f32_16x16x32_bf16 v[64:67], a[116:119], v[72:75], v[64:67]\n\t"
  "s_waitcnt lgkmcnt(4)\n\t"
  "buffer_load_dwordx4 a[40:43], %[vl16], %[srd], s45 offen offset:2048 sc1\n\t"
  "v_mfma_f32_16x16x32_bf16 v[44:47], a[120:123], v[76:79], v[44:47]\n\t"
  "ds_read_b64_tr_b16 v[68:69], v110 offset:8192\n\t"
  "v_mul_f32 v36, v36, v38\n\t"
  "v_mfma_f32_16x16x32_bf16 v[48:51], a[124:127], v[76:79], v[48:51]\n\t"
  "ds_read_b64_tr_b16 v[70:71], v110 offset:10240\n\t"
  "v_mul_f32 v37, v37, v39\n\t"
  "v_mfma_f32_16x16x32_bf16 v[52:55], a[128:131], v[76:79], v[52:55]\n\t"
  "ds_read_b64_tr_b16 v[72:73], v111 offset:8192\n\t"
  "v_cvt_pk_bf16_f32 v125, v36, v37\n\t"
  "v_mfma_f32_16x16x32_bf16 v[56:59], a[120:123], v[80:83], v[56:59]\n\t"
  "ds_read_b64_tr_b16 v[74:75], v111 offset:10240\n\t"
  "buffer_load_dwordx4 a[44:47], %[vl16], %[srd], s45 offen offset:3072 sc1\n\t"
  "v_mfma_f32_16x16x32_bf16 v[60:63], a[124:127], v[80:83], v[60:63]\n\t"
  "v_cvt_pk_bf16_f32 v121, v36, v37\n\t"
  "v_max_f32 v40, 0, v40\n\t"
  "v_mfma_f32_16x16x32_bf16 v[64:67], a[128:131], v[80:83], v[64:67]\n\t"
  "s_waitcnt lgkmcnt(4)\n\t"
  "v_max_f32 v41, 0, v41\n\t"
  "v_mfma_f32_16x16x32_bf16 v[44:47], a[132:135], v[84:87], v[44:47]\n\t"
  "ds_read_b64_tr_b16 v[76:77], v110 offset:12288\n\t"
  "v_mul_f32 v42, v42, v129\n\t"
  "v_mfma_f32_16x16x32_bf16 v[48:51], a[136:139], v[84:87], v[48:51]\n\t"
  "ds_read_b64_tr_b16 v[78:79], v110 offset:14336\n\t"
  "s_add_u32 s45, %[sW2], 59392\n\t"
  "v_mfma_f32_16x16x32_bf16 v[52:55], a[140:143], v[84:87], v[52:55]\n\t"
  "ds_read_b64_tr_b16 v[80:81], v111 offset:12288\n\t"
  "v_mul_f32 v43, v43, v129\n\t"
  "v_mfma_f32_16x16x32_bf16 v[56:59], a[132:135], v[88:91], v[56:59]\n\t"
  "ds_read_b64_tr_b16 v[82:83], v111 offset:14336\n\t"
  "v_mul_f32 v40, v40, v42\n\t"
  "v_mfma_f32_16x16x32_bf16 v[60:63], a[136:139], v[88:91], v[60:63]\n\t"
  "v_permlane16_swap_b32 v124, v125\n\t"
  "v_mul_f32 v41, v41, v43\n\t"
  "v_mfma_f32_16x16x32_bf16 v[64:67], a[140:143], v[88:91], v[64:67]\n\t"
  "s_waitcnt lgkmcnt(4)\n\t"
  "buffer_load_dwordx4 a[48:51], %[vl16], %[srd], s45 offen offset:0 sc1\n\t"
  "v_mfma_f32_16x16x32_bf16 v[44:47], a[144:147], v[68:71], v[44:47]\n\t"
  "ds_read_b64_tr_b16 v[84:85], v110 offset:16384\n\t"
  "v_cvt_pk_bf16_f32 v126, v40, v41\n\t"
  "v_mfma_f32_16x16x32_bf16 v[48:51], a[148:151], v[68:71], v[48:51]\n\t"
  "ds_read_b64_tr_b16 v[86:87], v110 offset:18432\n\t"
  "v_cvt_pk_bf16_f32 v122, v40, v41\n\t"
  "v_mfma_f32_16x16x32_bf16 v[52:55], a[152:155], v[68:71], v[52:55]\n\t"
  "ds_read_b64_tr_b16 v[88:89], v111 offset:16384\n\t"
  "s_mov_b32 s55, 3194880\n\t"
  "v_mfma_f32_16x16x32_bf16 v[56:59], a[144:147], v[72:75], v[56:59]\n\t"
  "ds_read_b64_tr_b16 v[90:91], v111 offset:18432\n\t"
  "buffer_load_dwordx4 a[52:55], %[vl16], %[srd], s45 offen offset:1024 sc1\n\t"
  "v_mfma_f32_16x16x32_bf16 v[60:63], a[148:151], v[72:75], v[60:63]\n\t"
  "v_permlane16_swap_b32 v126, v123\n\t"
  "v_mfma_f32_16x16x32_bf16 v[64:67], a[152:155], v[72:75], v[64:67]\n\t"
  "s_waitcnt lgkmcnt(4)\n\t"
  "v_permlane32_swap_b32 v125, v123\n\t"
  "v_mfma_f32_16x16x32_bf16 v[44:47], a[156:159], v[76:79], v[44:47]\n\t"
  "ds_read_b64_tr_b16 v[68:69], v110 offset:20480\n\t"
  "buffer_store_dwordx4 v[120:123], %[vh2st], %[srd], s55 offen sc1\n\t"
  "v_mfma_f32_16x16x32_bf16 v[48:51], a[160:163], v[76:79], v[48:51]\n\t"
  "ds_read_b64_tr_b16 v[70:71], v110 offset:22528\n\t"
  "v_and_b32 v128, 0xffff0000, v105\n\t"
  "v_mfma_f32_16x16x32_bf16 v[52:55], a[164:167], v[76:79], v[52:55]\n\t"
  "ds_read_b64_tr_b16 v[72:73], v111 offset:20480\n\t"
  "v_and_b32 v129, 0xffff0000, v107\n\t"
  "v_mfma_f32_16x16x32_bf16 v[56:59], a[156:159], v[80:83], v[56:59]\n\t"
  "ds_read_b64_tr_b16 v[74:75], v111 offset:22528\n\t"
  "s_waitcnt vmcnt(12)\n\t"
  "v_cmp_ne_u64 vcc, v[132:133], v[138:139]\n\t"
  "v_mfma_f32_16x16x32_bf16 v[60:63], a[160:163], v[80:83], v[60:63]\n\t"
  "v_rcp_f32 v128, v128\n\t"
  "v_rcp_f32 v129, v129\n\t"
  "v_mfma_f32_16x16x32_bf16 v[64:67], a[164:167], v[80:83], v[64:67]\n\t"
  "s_waitcnt lgkmcnt(4)\n\t"
  "s_cbranch_vccnz Lfl0_slow%=\n\t"
  "Lfl0_back%=:\n\t"
  "v_mfma_f32_16x16x32_bf16 v[44:47], a[168:171], v[84:87], v[44:47]\n\t"
  "ds_read_b64_tr_b16 v[76:77], v110 offset:24576\n\t"
  "s_add_u32 s44, %[sH2ld], 0\n\t"
  "v_mfma_f32_16x16x32_bf16 v[48:51], a[172:175], v[84:87], v[48:51]\n\t"
  "ds_read_b64_tr_b16 v[78:79], v110 offset:26624\n\t"
  "v_mul_f32 v128, v128, v128\n\t"
  "v_mfma_f32_16x16x32_bf16 v[52:55], a[176:179], v[84:87], v[52:55]\n\t"
  "ds_read_b64_tr_b16 v[80:81], v111 offset:24576\n\t"
  "buffer_load_dwordx4 a[56:59], %[vl16], %[srd], s44 offen offset:0 sc1\n\t"
  "v_mfma_f32_16x16x32_bf16 v[56:59], a[168:171], v[88:91], v[56:59]\n\t"
  "ds_read_b64_tr_b16 v[82:83], v111 offset:26624\n\t"
  "ds_read_b128 a[160:163], v141 offset:0\n\t"
  "v_mfma_f32_16x16x32_bf16 v[60:63], a[172:175], v[88:91], v[60:63]\n\t"
  "ds_read_b128 a[164:167], v141 offset:1024\n\t"
  "v_mul_f32 v129, v129, v129\n\t"
  "v_mfma_f32_16x16x32_bf16 v[64:67], a[176:179], v[88:91], v[64:67]\n\t"
  "s_waitcnt lgkmcnt(6)\n\t"
  "buffer_load_dwordx4 a[60:63], %[vl16], %[srd], s44 offen offset:1024 sc1\n\t"
  "v_mfma_f32_16x16x32_bf16 v[44:47], a[180:183], v[68:71], v[44:47]\n\t"
  "ds_read_b64_tr_b16 v[84:85], v110 offset:28672\n\t"
  "ds_read_b128 a[168:171], v141 offset:2048\n\t"
  "v_mfma_f32_16x16x32_bf16 v[48:51], a[184:187], v[68:71], v[48:51]\n\t"
  "ds_read_b64_tr_b16 v[86:87], v110 offset:30720\n\t"
  "ds_read_b128 a[172:175], v141 offset:3072\n\t"
  "v_mfma_f32_16x16x32_bf16 v[52:55], a[188:191], v[68:71], v[52:55]\n\t"
  "ds_read_b64_tr_b16 v[88:89], v111 offset:28672\n\t"
  "buffer_load_dwordx4 a[64:67], %[vl16], %[srd], s44 offen offset:2048 sc1\n\t"
  "v_mfma_f32_16x16x32_bf16 v[56:59], a[180:183], v[72:75], v[56:59]\n\t"
  "ds_read_b64_tr_b16 v[90:91], v111 offset:30720\n\t"
  "ds_read_b128 a[176:179], v141 offset:4096\n\t"
  "v_mfma_f32_16x16x32_bf16 v[60:63], a[184:187], v[72:75], v[60:63]\n\t"
  "v_mfma_f32_16x16x32_bf16 v[64:67], a[188:191], v[72:75], v[64:67]\n\t"
  "s_waitcnt lgkmcnt(9)\n\t"
  "buffer_load_dwordx4 a[68:71], %[vl16], %[srd], s44 offen offset:3072 sc1\n\t"
  "v_mfma_f32_16x16x32_bf16 v[44:47], a[192:195], v[76:79], v[44:47]\n\t"
  "ds_read_b64_tr_b16 v[68:69], v110 offset:32768\n\t"
  "ds_read_b128 a[180:183], v141 offset:5120\n\t"
  "v_mfma_f32_16x16x32_bf16 v[48:51], a[196:199], v[76:79], v[48:51]\n\t"
  "ds_read_b64_tr_b16 v[70:71], v110 offset:34816\n\t"
  "ds_read_b128 a[184:187], v141 offset:6144\n\t"
  "v_mfma_f32_16x16x32_bf16 v[52:55], a[200:203], v[76:79], v[52:55]\n\t"
  "ds_read_b64_tr_b16 v[72:73], v111 offset:32768\n\t"
  "s_add_u32 s44, %[sH2ld], 4096\n\t"
  "v_mfma_f32_16x16x32_bf16 v[56:59], a[192:195], v[80:83], v[56:59]\n\t"
  "ds_read_b64_tr_b16 v[74:75], v111 offset:34816\n\t"
  "ds_read_b128 a[188:191], v141 offset:7168\n\t"
  "v_mfma_f32_16x16x32_bf16 v[60:63], a[196:199], v[80:83], v[60:63]\n\t"
  "buffer_load_dwordx4 a[72:75], %[vl16], %[srd], s44 offen offset:0 sc1\n\t"
  "v_mfma_f32_16x16x32_bf16 v[64:67], a[200:203], v[80:83], v[64:67]\n\t"
  "s_waitcnt lgkmcnt(8)\n\t"
  "ds_read_b128 a[192:195], v141 offset:8192\n\t"
  "v_mfma_f32_16x16x32_bf16 v[44:47], a[204:207], v[84:87], v[44:47]\n\t"
  "ds_read_b64_tr_b16 v[76:77], v110 offset:36864\n\t"
  "ds_read_b128 a[196:199], v141 offset:9216\n\t"
  "v_mfma_f32_16x16x32_bf16 v[48:51], a[208:211], v[84:87], v[48:51]\n\t"
  "ds_read_b64_tr_b16 v[78:79], v110 offset:38912\n\t"
  "buffer_load_dwordx4 a[76:79], %[vl16], %[srd], s44 offen offset:1024 sc1\n\t"
  "v_mfma_f32_16x16x32_bf16 v[52:55], a[212:215], v[84:87], v[52:55]\n\t"
  "ds_read_b64_tr_b16 v[80:81], v111 offset:36864\n\t"
  "ds_read_b128 a[200:203], v141 offset:10240\n\t"
  "v_mfma_f32_16x16x32_bf16 v[56:59], a[204:207], v[88:91], v[56:59]\n\t"
  "ds_read_b64_tr_b16 v[82:83], v111 offset:38912\n\t"
  "v_mfma_f32_16x16x32_bf16 v[60:63], a[208:211], v[88:91], v[60:63]\n\t"
  "buffer_load_dwordx4 a[80:83], %[vl16], %[srd], s44 offen offset:2048 sc1\n\t"
  "v_mfma_f32_16x16x32_bf16 v[64:67], a[212:215], v[88:91], v[64:67]\n\t"
  "s_waitcnt lgkmcnt(8)\n\t"
  "ds_read_b128 a[204:207], v141 offset:11264\n\t"
  "v_mfma_f32_16x16x32_bf16 v[44:47], a[216:219], v[68:71], v[44:47]\n\t"
  "ds_read_b128 a[208:211], v141 offset:12288\n\t"
  "ds_read_b128 a[212:215], v141 offset:13312\n\t"
  "v_mfma_f32_16x16x32_bf16 v[48:51], a[220:223], v[68:71], v[48:51]\n\t"
  "buffer_load_dwordx4 a[84:87], %[vl16], %[srd], s44 offen offset:3072 sc1\n\t"
  "v_mfma_f32_16x16x32_bf16 v[52:55], a[224:227], v[68:71], v[52:55]\n\t"
  "v_mfma_f32_16x16x32_bf16 v[56:59], a[216:219], v[72:75], v[56:59]\n\t"
  "v_mfma_f32_16x16x32_bf16 v[60:63], a[220:223], v[72:75], v[60:63]\n\t"
  "s_add_u32 s44, %[sH2ld], 8192\n\t"
  "v_mfma_f32_16x16x32_bf16 v[64:67], a[224:227], v[72:75], v[64:67]\n\t"
  "s_waitcnt lgkmcnt(3)\n\t"
  "ds_read_b128 a[216:219], v141 offset:14336\n\t"
  "v_mfma_f32_16x16x32_bf16 v[44:47], a[228:231], v[76:79], v[44:47]\n\t"
  "s_add_u32 s57, %[sFL], 8192\n\t"
  "buffer_load_dwordx2 v[138:139], v137, %[srd], s57 offen sc1\n\t"
  "v_mfma_f32_16x16x32_bf16 v[48:51], a[232:235], v[76:79], v[48:51]\n\t"
  "buffer_load_dwordx4 a[88:91], %[vl16], %[srd], s44 offen offset:0 sc1\n\t"
  "ds_read_b128 a[220:223], v141 offset:15360\n\t"
  "v_mfma_f32_16x16x32_bf16 v[52:55], a[236:239], v[76:79], v[52:55]\n\t"
  "ds_read_b128 a[224:227], v141 offset:16384\n\t"
  "v_mfma_f32_16x16x32_bf16 v[56:59], a[228:231], v[80:83], v[56:59]\n\t"
  "buffer_load_dwordx4 a[92:95], %[vl16], %[srd], s44 offen offset:1024 sc1\n\t"
  "v_mfma_f32_16x16x32_bf16 v[60:63], a[232:235], v[80:83], v[60:63]\n\t"
  "v_mfma_f32_16x16x32_bf16 v[64:67], a[236:239], v[80:83], v[64:67]\n\t"
  "ds_read_b128 a[228:231], v141 offset:17408\n\t"
  "ds_read_b128 a[232:235], v141 offset:18432\n\t"
  "v_mfma_f32_16x16x16_bf16 v[44:47], a[240:241], v[104:105], v[44:47]\n\t"
  "buffer_load_dwordx4 a[96:99], %[vl16], %[srd], s44 offen offset:2048 sc1\n\t"
  "ds_read_b128 a[236:239], v141 offset:19456\n\t"
  "v_mfma_f32_16x16x16_bf16 v[48:51], a[242:243], v[104:105], v[48:51]\n\t"
  "v_mfma_f32_16x16x16_bf16 v[52:55], a[244:245], v[104:105], v[52:55]\n\t"
  "v_mfma_f32_16x16x16_bf16 v[56:59], a[240:241], v[106:107], v[56:59]\n\t"
  "buffer_load_dwordx4 a[100:103], %[vl16], %[srd], s44 offen offset:3072 sc1\n\t"
  "v_mfma_f32_16x16x16_bf16 v[60:63], a[242:243], v[106:107], v[60:63]\n\t"
  "v_mfma_f32_16x16x16_bf16 v[64:67], a[244:245], v[106:107], v[64:67]\n\t"
  "s_movk_i32 s56, 16384\n\t"
  "s_waitcnt vmcnt(13)\n\t"
  "buffer_store_dwordx4 v[132:135], %[vflw], %[srd], s56 offen sc1\n\t"
  "v_max_f32 v44, 0, v44\n\t"
  "v_max_f32 v45, 0, v45\n\t"
  "v_pk_mul_f32 v[46:47], v[46:47], v[128:129] op_sel:[0,0] op_sel_hi:[1,0]\n\t"
  "v_pk_mul_f32 v[44:45], v[44:45], v[46:47]\n\t"
  "v_cvt_pk_bf16_f32 v116, v44, v45\n\t"
  "v_cvt_pk_bf16_f32 v112, v44, v45\n\t"
  "v_max_f32 v48, 0, v48\n\t"
  "v_max_f32 v49, 0, v49\n\t"
  "v_pk_mul_f32 v[50:51], v[50:51], v[128:129] op_sel:[0,0] op_sel_hi:[1,0]\n\t"
  "v_pk_mul_f32 v[48:49], v[48:49], v[50:51]\n\t"
  "v_cvt_pk_bf16_f32 v117, v48, v49\n\t"
  "v_cvt_pk_bf16_f32 v113, v48, v49\n\t"
  "v_max_f32 v52, 0, v52\n\t"
  "v_max_f32 v53, 0, v53\n\t"
  "v_pk_mul_f32 v[54:55], v[54:55], v[128:129] op_sel:[0,0] op_sel_hi:[1,0]\n\t"
  "v_permlane16_swap_b32 v116, v117\n\t"
  "v_pk_mul_f32 v[52:53], v[52:53], v[54:55]\n\t"
  "v_cvt_pk_bf16_f32 v118, v52, v53\n\t"
  "v_cvt_pk_bf16_f32 v114, v52, v53\n\t"
  "s_mov_b32 s55, 3244032\n\t"
  "v_permlane16_swap_b32 v118, v115\n\t"
  "s_nop 1\n\t"
  "v_permlane32_swap_b32 v117, v115\n\t"
  "buffer_store_dwordx4 v[112:115], %[vh2st], %[srd], s55 offen sc1\n\t"
  "v_max_f32 v56, 0, v56\n\t"
  "v_max_f32 v57, 0, v57\n\t"
  "v_pk_mul_f32 v[58:59], v[58:59], v[128:129] op_sel:[0,1] op_sel_hi:[1,1]\n\t"
  "v_pk_mul_f32 v[56:57], v[56:57], v[58:59]\n\t"
  "v_cvt_pk_bf16_f32 v124, v56, v57\n\t"
  "v_cvt_pk_bf16_f32 v120, v56, v57\n\t"
  "v_max_f32 v60, 0, v60\n\t"
  "v_max_f32 v61, 0, v61\n\t"
  "v_pk_mul_f32 v[62:63], v[62:63], v[128:129] op_sel:[0,1] op_sel_hi:[1,1]\n\t"
  "v_pk_mul_f32 v[60:61], v[60:61], v[62:63]\n\t"
  "v_cvt_pk_bf16_f32 v125, v60, v61\n\t"
  "v_cvt_pk_bf16_f32 v121, v60, v61\n\t"
  "v_max_f32 v64, 0, v64\n\t"
  "v_max_f32 v65, 0, v65\n\t"
  "v_pk_mul_f32 v[66:67], v[66:67], v[128:129] op_sel:[0,1] op_sel_hi:[1,1]\n\t"
  "v_permlane16_swap_b32 v124, v125\n\t"
  "v_pk_mul_f32 v[64:65], v[64:65], v[66:67]\n\t"
  "v_cvt_pk_bf16_f32 v126, v64, v65\n\t"
  "v_cvt_pk_bf16_f32 v122, v64, v65\n\t"
  "s_mov_b32 s55, 3293184\n\t"
  "v_permlane16_swap_b32 v126, v123\n\t"
  "s_nop 1\n\t"
  "v_permlane32_swap_b32 v125, v123\n\t"
  "buffer_store_dwordx4 v[120:123], %[vh2st], %[srd], s55 offen sc1\n\t"
  "s_waitcnt lgkmcnt(0)\n\t"
  "s_barrier\n\t"
  "s_waitcnt vmcnt(15)\n\t"
  "v_mfma_f32_16x16x32_bf16 v[20:23], v[144:147], a[56:59], 0\n\t"
  "v_lshrrev_b32 v63, 3, %[vl16]\n\t"
  "v_and_b32 v63, 0x60, v63\n\t"
  "v_mfma_f32_16x16x32_bf16 v[24:27], v[148:151], a[56:59], 0\n\t"
  "v_lshrrev_b32 v62, 1, v63\n\t"
  "buffer_load_dwordx4 v[68:71], v63, %[srd], %[sRT] offen offset:0 sc1\n\t"
  "v_mfma_f32_16x16x32_bf16 v[28:31], v[152:155], a[56:59], 0\n\t"
  "buffer_load_dwordx4 v[72:75], v63, %[srd], %[sRT] offen offset:16 sc1\n\t"
  "buffer_load_dwordx4 v[76:79], v63, %[srd], %[sRT] offen offset:128 sc1\n\t"
  "v_mfma_f32_16x16x32_bf16 v[32:35], v[156:159], a[56:59], 0\n\t"
  "buffer_load_dwordx4 v[80:83], v63, %[srd], %[sRT] offen offset:144 sc1\n\t"
  "buffer_load_dwordx4 v[84:87], v62, %[srd], %[sRT] offen offset:256 sc1\n\t"
  "v_mfma_f32_16x16x32_bf16 v[36:39], v[160:163], a[56:59], 0\n\t"
  "buffer_load_dwordx4 v[64:67], %[vgb], %[srd], %[sRT] offen offset:0 sc1\n\t"
  "buffer_load_dwordx4 v[108:111], %[vgb], %[srd], %[sRT] offen offset:16 sc1\n\t"
  "s_waitcnt vmcnt(21)\n\t"
  "v_mfma_f32_16x16x32_bf16 v[20:23], v[164:167], a[60:63], v[20:23]\n\t"
  "s_mul_i32 s48, %[w], -15360\n\t"
  "s_add_u32 s48, s48, 81920\n\t"
  "v_mfma_f32_16x16x32_bf16 v[24:27], v[168:171], a[60:63], v[24:27]\n\t"
  "v_add_u32 v62, s48, %[vwr]\n\t"
  "v_add_u32 v60, 61440, %[vrd0]\n\t"
  "v_mfma_f32_16x16x32_bf16 v[28:31], v[172:175], a[60:63], v[28:31]\n\t"
  "v_add_u32 v61, 61440, %[vrd1]\n\t"
  "v_accvgpr_read_b32 v89, a246\n\t"
  "v_mfma_f32_16x16x32_bf16 v[32:35], v[176:179], a[60:63], v[32:35]\n\t"
  "s_waitcnt vmcnt(14)\n\t"
  "v_cmp_ne_u64 vcc, v[132:133], v[138:139]\n\t"
  "v_mfma_f32_16x16x32_bf16 v[36:39], v[180:183], a[60:63], v[36:39]\n\t"
  "v_lshlrev_b32 v88, 16, v89\n\t"
  "v_and_b32 v89, 0xffff0000, v89\n\t"
  "v_mfma_f32_16x16x32_bf16 v[20:23], v[184:187], a[64:67], v[20:23]\n\t"
  "s_cbranch_vccnz Lfl1_slow%=\n\t"
  "Lfl1_back%=:\n\t"
  "v_mfma_f32_16x16x32_bf16 v[24:27], v[188:191], a[64:67], v[24:27]\n\t"
  "v_accvgpr_read_b32 v91, a247\n\t"
  "v_lshlrev_b32 v90, 16, v91\n\t"
  "v_mfma_f32_16x16x32_bf16 v[28:31], v[192:195], a[64:67], v[28:31]\n\t"
  "s_add_u32 s45, %[sH2ld], 98304\n\t"
  "v_and_b32 v91, 0xffff0000, v91\n\t"
  "v_mfma_f32_16x16x32_bf16 v[32:35], v[196:199], a[64:67], v[32:35]\n\t"
  "v_accvgpr_read_b32 v93, a248\n\t"
  "v_lshlrev_b32 v92, 16, v93\n\t"
  "v_mfma_f32_16x16x32_bf16 v[36:39], v[200:203], a[64:67], v[36:39]\n\t"
  "v_and_b32 v93, 0xffff0000, v93\n\t"
  "v_accvgpr_read_b32 v95, a249\n\t"
  "v_mfma_f32_16x16x32_bf16 v[20:23], v[204:207], a[68:71], v[20:23]\n\t"
  "buffer_load_dwordx4 a[104:107], %[vl16], %[srd], s45 offen offset:0 sc1\n\t"
  "v_lshlrev_b32 v94, 16, v95\n\t"
  "v_mfma_f32_16x16x32_bf16 v[24:27], v[208:211], a[68:71], v[24:27]\n\t"
  "v_and_b32 v95, 0xffff0000, v95\n\t"
  "v_accvgpr_read_b32 v97, a250\n\t"
  "v_mfma_f32_16x16x32_bf16 v[28:31], v[212:215], a[68:71], v[28:31]\n\t"
  "v_lshlrev_b32 v96, 16, v97\n\t"
  "v_and_b32 v97, 0xffff0000, v97\n\t"
  "v_mfma_f32_16x16x32_bf16 v[32:35], v[216:219], a[68:71], v[32:35]\n\t"
  "buffer_load_dwordx4 a[108:111], %[vl16], %[srd], s45 offen offset:1024 sc1\n\t"
  "v_accvgpr_read_b32 v99, a251\n\t"
  "v_mfma_f32_16x16x32_bf16 v[36:39], v[220:223], a[68:71], v[36:39]\n\t"
  "v_lshlrev_b32 v98, 16, v99\n\t"
  "v_and_b32 v99, 0xffff0000, v99\n\t"
  "v_mfma_f32_16x16x32_bf16 v[20:23], v[224:227], a[72:75], v[20:23]\n\t"
  "v_accvgpr_read_b32 v101, a252\n\t"
  "v_lshlrev_b32 v100, 16, v101\n\t"
  "v_mfma_f32_16x16x32_bf16 v[24:27], v[228:231], a[72:75], v[24:27]\n\t"
  "buffer_load_dwordx4 a[112:115], %[vl16], %[srd], s45 offen offset:2048 sc1\n\t"
  "v_and_b32 v101, 0xffff0000, v101\n\t"
  "v_mfma_f32_16x16x32_bf16 v[28:31], v[232:235], a[72:75], v[28:31]\n\t"
  "v_accvgpr_read_b32 v103, a253\n\t"
  "v_lshlrev_b32 v102, 16, v103\n\t"
  "v_mfma_f32_16x16x32_bf16 v[32:35], v[236:239], a[72:75], v[32:35]\n\t"
  "v_and_b32 v103, 0xffff0000, v103\n\t"
  "v_accvgpr_read_b32 v105, a254\n\t"
  "v_mfma_f32_16x16x32_bf16 v[36:39], v[240:243], a[72:75], v[36:39]\n\t"
  "buffer_load_dwordx4 a[116:119], %[vl16], %[srd], s45 offen offset:3072 sc1\n\t"
  "s_add_u32 s57, %[sFL], 16384\n\t"
  "v_mfma_f32_16x16x32_bf16 v[20:23], v[244:247], a[76:79], v[20:23]\n\t"
  "buffer_load_dwordx2 v[138:139], v137, %[srd], s57 offen sc1\n\t"
  "v_lshlrev_b32 v104, 16, v105\n\t"
  "v_mfma_f32_16x16x32_bf16 v[24:27], a[160:163], a[76:79], v[24:27]\n\t"
  "s_movk_i32 s56, 24576\n\t"
  "s_waitcnt vmcnt(12)\n\t"
  "v_mfma_f32_16x16x32_bf16 v[28:31], a[164:167], a[76:79], v[28:31]\n\t"
  "buffer_store_dwordx4 v[132:135], %[vflw], %[srd], s56 offen sc1\n\t"
  "v_and_b32 v105, 0xffff0000, v105\n\t"
  "v_mfma_f32_16x16x32_bf16 v[32:35], a[168:171], a[76:79], v[32:35]\n\t"
  "s_add_u32 s45, %[sH2ld], 102400\n\t"
  "v_accvgpr_read_b32 v107, a255\n\t"
  "v_mfma_f32_16x16x32_bf16 v[36:39], a[172:175], a[76:79], v[36:39]\n\t"
  "v_lshlrev_b32 v106, 16, v107\n\t"
  "v_and_b32 v107, 0xffff0000, v107\n\t"
  "v_mfma_f32_16x16x32_bf16 v[20:23], a[176:179], a[80:83], v[20:23]\n\t"
  "v_mfma_f32_16x16x32_bf16 v[24:27], a[180:183], a[80:83], v[24:27]\n\t"
  "buffer_load_dwordx4 a[120:123], %[vl16], %[srd], s45 offen offset:0 sc1\n\t"
  "v_mfma_f32_16x16x32_bf16 v[28:31], a[184:187], a[80:83], v[28:31]\n\t"
  "v_mfma_f32_16x16x32_bf16 v[32:35], a[188:191], a[80:83], v[32:35]\n\t"
  "v_mfma_f32_16x16x32_bf16 v[36:39], a[192:195], a[80:83], v[36:39]\n\t"
  "buffer_load_dwordx4 a[124:127], %[vl16], %[srd], s45 offen offset:1024 sc1\n\t"
  "v_mfma_f32_16x16x32_bf16 v[20:23], a[196:199], a[84:87], v[20:23]\n\t"
  "v_mfma_f32_16x16x32_bf16 v[24:27], a[200:203], a[84:87], v[24:27]\n\t"
  "v_mfma_f32_16x16x32_bf16 v[28:31], a[204:207], a[84:87], v[28:31]\n\t"
  "buffer_load_dwordx4 a[128:131], %[vl16], %[srd], s45 offen offset:2048 sc1\n\t"
  "v_mfma_f32_16x16x32_bf16 v[32:35], a[208:211], a[84:87], v[32:35]\n\t"
  "v_mfma_f32_16x16x32_bf16 v[36:39], a[212:215], a[84:87], v[36:39]\n\t"
  "v_mfma_f32_16x16x32_bf16 v[20:23], a[216:219], a[88:91], v[20:23]\n\t"
  "buffer_load_dwordx4 a[132:135], %[vl16], %[srd], s45 offen offset:3072 sc1\n\t"
  "v_mfma_f32_16x16x32_bf16 v[24:27], a[220:223], a[88:91], v[24:27]\n\t"
  "v_mfma_f32_16x16x32_bf16 v[28:31], a[224:227], a[88:91], v[28:31]\n\t"
  "v_mfma_f32_16x16x32_bf16 v[32:35], a[228:231], a[88:91], v[32:35]\n\t"
  "v_mfma_f32_16x16x32_bf16 v[36:39], a[232:235], a[88:91], v[36:39]\n\t"
  "s_add_u32 s45, %[sH2ld], 106496\n\t"
  "v_mfma_f32_16x16x32_bf16 v[20:23], a[236:239], a[92:95], v[20:23]\n\t"
  "s_waitcnt vmcnt(16)\n\t"
  "v_mul_f32 v88, v88, v68\n\t"
  "v_mfma_f32_16x16x32_bf16 v[24:27], a[0:3], a[92:95], v[24:27]\n\t"
  "v_mul_f32 v89, v89, v69\n\t"
  "v_mul_f32 v90, v90, v70\n\t"
  "v_mfma_f32_16x16x32_bf16 v[28:31], a[4:7], a[92:95], v[28:31]\n\t"
  "buffer_load_dwordx4 a[136:139], %[vl16], %[srd], s45 offen offset:0 sc1\n\t"
  "v_mul_f32 v91, v91, v71\n\t"
  "v_mfma_f32_16x16x32_bf16 v[32:35], a[8:11], a[92:95], v[32:35]\n\t"
  "ds_write_b128 v62, v[88:91] offset:0\n\t"
  "s_waitcnt vmcnt(16)\n\t"
  "v_mul_f32 v92, v92, v72\n\t"
  "v_mfma_f32_16x16x32_bf16 v[36:39], a[12:15], a[92:95], v[36:39]\n\t"
  "v_mul_f32 v93, v93, v73\n\t"
  "v_mul_f32 v94, v94, v74\n\t"
  "v_mfma_f32_16x16x32_bf16 v[20:23], a[16:19], a[96:99], v[20:23]\n\t"
  "buffer_load_dwordx4 a[140:143], %[vl16], %[srd], s45 offen offset:1024 sc1\n\t"
  "v_mul_f32 v95, v95, v75\n\t"
  "v_mfma_f32_16x16x32_bf16 v[24:27], a[20:23], a[96:99], v[24:27]\n\t"
  "ds_write_b128 v62, v[92:95] offset:1024\n\t"
  "s_waitcnt vmcnt(16)\n\t"
  "v_mul_f32 v96, v96, v76\n\t"
  "v_mfma_f32_16x16x32_bf16 v[28:31], a[24:27], a[96:99], v[28:31]\n\t"
  "v_mul_f32 v97, v97, v77\n\t"
  "v_mul_f32 v98, v98, v78\n\t"
  "v_mfma_f32_16x16x32_bf16 v[32:35], a[28:31], a[96:99], v[32:35]\n\t"
  "buffer_load_dwordx4 a[144:147], %[vl16], %[srd], s45 offen offset:2048 sc1\n\t"
  "v_mul_f32 v99, v99, v79\n\t"
  "v_mfma_f32_16x16x32_bf16 v[36:39], a[32:35], a[96:99], v[36:39]\n\t"
  "ds_write_b128 v62, v[96:99] offset:2048\n\t"
  "s_waitcnt vmcnt(16)\n\t"
  "v_mul_f32 v100, v100, v80\n\t"
  "v_mfma_f32_16x16x32_bf16 v[20:23], a[36:39], a[100:103], v[20:23]\n\t"
  "v_mul_f32 v101, v101, v81\n\t"
  "v_mul_f32 v102, v102, v82\n\t"
  "v_mfma_f32_16x16x32_bf16 v[24:27], a[40:43], a[100:103], v[24:27]\n\t"
  "buffer_load_dwordx4 a[148:151], %[vl16], %[srd], s45 offen offset:3072 sc1\n\t"
  "v_mul_f32 v103, v103, v83\n\t"
  "v_mfma_f32_16x16x32_bf16 v[28:31], a[44:47], a[100:103], v[28:31]\n\t"
  "ds_write_b128 v62, v[100:103] offset:3072\n\t"
  "s_waitcnt vmcnt(16)\n\t"
  "v_mul_f32 v104, v104, v84\n\t"
  "v_mfma_f32_16x16x32_bf16 v[32:35], a[48:51], a[100:103], v[32:35]\n\t"
  "v_mul_f32 v105, v105, v85\n\t"
  "v_mul_f32 v106, v106, v86\n\t"
  "v_mfma_f32_16x16x32_bf16 v[36:39], a[52:55], a[100:103], v[36:39]\n\t"
  "s_waitcnt vmcnt(12)\n\t"
  "v_mul_f32 v107, v107, v87\n\t"
  "v_mfma_f32_16x16x32_bf16 v[40:43], v[144:147], a[104:107], 0\n\t"
  "ds_write_b128 v62, v[104:107] offset:4096\n\t"
  "v_mfma_f32_16x16x32_bf16 v[44:47], v[148:151], a[104:107], 0\n\t"
  "v_mfma_f32_16x16x32_bf16 v[48:51], v[152:155], a[104:107], 0\n\t"
  "v_mfma_f32_16x16x32_bf16 v[52:55], v[156:159], a[104:107], 0\n\t"
  "ds_write_b128 %[vwr], v[20:23] offset:0\n\t"
  "v_mfma_f32_16x16x32_bf16 v[56:59], v[160:163], a[104:107], 0\n\t"
  "v_mfma_f32_16x16x32_bf16 v[40:43], v[164:167], a[108:111], v[40:43]\n\t"
  "v_mfma_f32_16x16x32_bf16 v[44:47], v[168:171], a[108:111], v[44:47]\n\t"
  "ds_write_b128 %[vwr], v[24:27] offset:1024\n\t"
  "v_mfma_f32_16x16x32_bf16 v[48:51], v[172:175], a[108:111], v[48:51]\n\t"
  "v_mfma_f32_16x16x32_bf16 v[52:55], v[176:179], a[108:111], v[52:55]\n\t"
  "v_mfma_f32_16x16x32_bf16 v[56:59], v[180:183], a[108:111], v[56:59]\n\t"
  "s_waitcnt vmcnt(10)\n\t"
  "ds_write_b128 %[vwr], v[28:31] offset:2048\n\t"
  "v_mfma_f32_16x16x32_bf16 v[40:43], v[184:187], a[112:115], v[40:43]\n\t"
  "v_mfma_f32_16x16x32_bf16 v[44:47], v[188:191], a[112:115], v[44:47]\n\t"
  "v_mfma_f32_16x16x32_bf16 v[48:51], v[192:195], a[112:115], v[48:51]\n\t"
  "ds_write_b128 %[vwr], v[32:35] offset:3072\n\t"
  "v_mfma_f32_16x16x32_bf16 v[52:55], v[196:199], a[112:115], v[52:55]\n\t"
  "v_mfma_f32_16x16x32_bf16 v[56:59], v[200:203], a[112:115], v[56:59]\n\t"
  "v_mfma_f32_16x16x32_bf16 v[40:43], v[204:207], a[116:119], v[40:43]\n\t"
  "s_waitcnt vmcnt(9)\n\t"
  "v_cmp_ne_u64 vcc, v[132:133], v[138:139]\n\t"
  "v_mfma_f32_16x16x32_bf16 v[44:47], v[208:211], a[116:119], v[44:47]\n\t"
  "ds_write_b128 %[vwr], v[36:39] offset:4096\n\t"
  "v_mfma_f32_16x16x32_bf16 v[48:51], v[212:215], a[116:119], v[48:51]\n\t"
  "s_cbranch_vccnz Lfl2_slow%=\n\t"
  "Lfl2_back%=:\n\t"
  "v_mfma_f32_16x16x32_bf16 v[52:55], v[216:219], a[116:119], v[52:55]\n\t"
  "s_add_u32 s44, %[sH2ld], 3145728\n\t"
  "v_mfma_f32_16x16x32_bf16 v[56:59], v[220:223], a[116:119], v[56:59]\n\t"
  "s_waitcnt vmcnt(6)\n\t"
  "v_mfma_f32_16x16x32_bf16 v[40:43], v[224:227], a[120:123], v[40:43]\n\t"
  "v_mfma_f32_16x16x32_bf16 v[44:47], v[228:231], a[120:123], v[44:47]\n\t"
  "buffer_load_dwordx4 a[152:155], %[vl16], %[srd], s44 offen offset:0 sc1\n\t"
  "v_mfma_f32_16x16x32_bf16 v[48:51], v[232:235], a[120:123], v[48:51]\n\t"
  "v_mfma_f32_16x16x32_bf16 v[52:55], v[236:239], a[120:123], v[52:55]\n\t"
  "v_mfma_f32_16x16x32_bf16 v[56:59], v[240:243], a[120:123], v[56:59]\n\t"
  "buffer_load_dwordx4 a[156:159], %[vl16], %[srd], s44 offen offset:1024 sc1\n\t"
  "s_add_u32 s57, %[sFL], 24576\n\t"
  "v_mfma_f32_16x16x32_bf16 v[40:43], v[244:247], a[124:127], v[40:43]\n\t"
  "buffer_load_dwordx2 v[138:139], v137, %[srd], s57 offen sc1\n\t"
  "s_waitcnt lgkmcnt(0)\n\t"
  "s_barrier\n\t"
  "v_mfma_f32_16x16x32_bf16 v[44:47], a[160:163], a[124:127], v[44:47]\n\t"
  "ds_read_b128 v[68:71], %[vrd0] offset:0\n\t"
  "ds_read_b128 v[72:75], %[vrd1] offset:0\n\t"
  "v_mfma_f32_16x16x32_bf16 v[48:51], a[164:167], a[124:127], v[48:51]\n\t"
  "buffer_load_dwordx4 v[248:251], %[vl16], %[srd], s44 offen offset:2048 sc1\n\t"
  "ds_read_b128 v[76:79], %[vrd0] offset:20480\n\t"
  "v_mfma_f32_16x16x32_bf16 v[52:55], a[168:171], a[124:127], v[52:55]\n\t"
  "ds_read_b128 v[80:83], %[vrd1] offset:20480\n\t"
  "ds_read_b128 v[84:87], %[vrd0] offset:40960\n\t"
  "v_mfma_f32_16x16x32_bf16 v[56:59], a[172:175], a[124:127], v[56:59]\n\t"
  "s_waitcnt vmcnt(8)\n\t"
  "ds_read_b128 v[88:91], %[vrd1] offset:40960\n\t"
  "v_mfma_f32_16x16x32_bf16 v[40:43], a[176:179], a[128:131], v[40:43]\n\t"
  "buffer_load_dwordx4 v[252:255], %[vl16], %[srd], s44 offen offset:3072 sc1\n\t"
  "ds_read_b128 v[92:95], v60 offset:0\n\t"
  "v_mfma_f32_16x16x32_bf16 v[44:47], a[180:183], a[128:131], v[44:47]\n\t"
  "ds_read_b128 v[96:99], v61 offset:0\n\t"
  "ds_read_b128 v[100:103], v60 offset:20480\n\t"
  "v_mfma_f32_16x16x32_bf16 v[48:51], a[184:187], a[128:131], v[48:51]\n\t"
  "ds_read_b128 v[104:107], v61 offset:20480\n\t"
  "v_mfma_f32_16x16x32_bf16 v[52:55], a[188:191], a[128:131], v[52:55]\n\t"
  "v_mfma_f32_16x16x32_bf16 v[56:59], a[192:195], a[128:131], v[56:59]\n\t"
  "s_add_u32 s44, %[sH2ld], 3149824\n\t"
  "v_mfma_f32_16x16x32_bf16 v[40:43], a[196:199], a[132:135], v[40:43]\n\t"
  "v_mfma_f32_16x16x32_bf16 v[44:47], a[200:203], a[132:135], v[44:47]\n\t"
  "v_mfma_f32_16x16x32_bf16 v[48:51], a[204:207], a[132:135], v[48:51]\n\t"
  "buffer_load_dwordx4 v[112:115], %[vl16], %[srd], s44 offen offset:0 sc1\n\t"
  "v_mfma_f32_16x16x32_bf16 v[52:55], a[208:211], a[132:135], v[52:55]\n\t"
  "v_mfma_f32_16x16x32_bf16 v[56:59], a[212:215], a[132:135], v[56:59]\n\t"
  "s_waitcnt vmcnt(8)\n\t"
  "v_mfma_f32_16x16x32_bf16 v[40:43], a[216:219], a[136:139], v[40:43]\n\t"
  "buffer_load_dwordx4 v[116:119], %[vl16], %[srd], s44 offen offset:1024 sc1\n\t"
  "s_waitcnt lgkmcnt(0)\n\t"
  "v_mfma_f32_16x16x32_bf16 v[44:47], a[220:223], a[136:139], v[44:47]\n\t"
  "v_add_f32 v68, v68, v76\n\t"
  "v_add_f32 v69, v69, v77\n\t"
  "v_mfma_f32_16x16x32_bf16 v[48:51], a[224:227], a[136:139], v[48:51]\n\t"
  "v_add_f32 v70, v70, v78\n\t"
  "v_add_f32 v71, v71, v79\n\t"
  "v_mfma_f32_16x16x32_bf16 v[52:55], a[228:231], a[136:139], v[52:55]\n\t"
  "buffer_load_dwordx4 v[120:123], %[vl16], %[srd], s44 offen offset:2048 sc1\n\t"
  "v_add_f32 v72, v72, v80\n\t"
  "v_mfma_f32_16x16x32_bf16 v[56:59], a[232:235], a[136:139], v[56:59]\n\t"
  "v_add_f32 v73, v73, v81\n\t"
  "v_add_f32 v74, v74, v82\n\t"
  "v_mfma_f32_16x16x32_bf16 v[40:43], a[236:239], a[140:143], v[40:43]\n\t"
  "v_add_f32 v75, v75, v83\n\t"
  "v_add_f32 v84, v84, v92\n\t"
  "v_mfma_f32_16x16x32_bf16 v[44:47], a[0:3], a[140:143], v[44:47]\n\t"
  "buffer_load_dwordx4 v[124:127], %[vl16], %[srd], s44 offen offset:3072 sc1\n\t"
  "v_add_f32 v85, v85, v93\n\t"
  "v_mfma_f32_16x16x32_bf16 v[48:51], a[4:7], a[140:143], v[48:51]\n\t"
  "v_add_f32 v86, v86, v94\n\t"
  "v_add_f32 v87, v87, v95\n\t"
  "v_mfma_f32_16x16x32_bf16 v[52:55], a[8:11], a[140:143], v[52:55]\n\t"
  "v_add_f32 v88, v88, v96\n\t"
  "v_add_f32 v89, v89, v97\n\t"
  "v_mfma_f32_16x16x32_bf16 v[56:59], a[12:15], a[140:143], v[56:59]\n\t"
  "s_waitcnt vmcnt(9)\n\t"
  "v_add_f32 v90, v90, v98\n\t"
  "v_mfma_f32_16x16x32_bf16 v[40:43], a[16:19], a[144:147], v[40:43]\n\t"
  "s_add_u32 s44, %[sH2ld], 3153920\n\t"
  "v_add_f32 v91, v91, v99\n\t"
  "v_mfma_f32_16x16x32_bf16 v[44:47], a[20:23], a[144:147], v[44:47]\n\t"
  "v_add_f32 v68, v68, v84\n\t"
  "v_add_f32 v69, v69, v85\n\t"
  "v_mfma_f32_16x16x32_bf16 v[48:51], a[24:27], a[144:147], v[48:51]\n\t"
  "v_add_f32 v70, v70, v86\n\t"
  "v_add_f32 v71, v71, v87\n\t"
  "v_mfma_f32_16x16x32_bf16 v[52:55], a[28:31], a[144:147], v[52:55]\n\t"
  "buffer_load_dwordx4 v[128:131], %[vl16], %[srd], s44 offen offset:0 sc1\n\t"
  "v_add_f32 v72, v72, v88\n\t"
  "v_mfma_f32_16x16x32_bf16 v[56:59], a[32:35], a[144:147], v[56:59]\n\t"
  "v_add_f32 v73, v73, v89\n\t"
  "v_add_f32 v74, v74, v90\n\t"
  "v_mfma_f32_16x16x32_bf16 v[40:43], a[36:39], a[148:151], v[40:43]\n\t"
  "v_add_f32 v75, v75, v91\n\t"
  "v_fma_f32 v68, v68, v64, v100\n\t"
  "v_mfma_f32_16x16x32_bf16 v[44:47], a[40:43], a[148:151], v[44:47]\n\t"
  "buffer_load_dwordx4 a[240:243], %[vl16], %[srd], s44 offen offset:1024 sc1\n\t"
  "v_fma_f32 v69, v69, v65, v101\n\t"
  "v_mfma_f32_16x16x32_bf16 v[48:51], a[44:47], a[148:151], v[48:51]\n\t"
  "v_fma_f32 v70, v70, v66, v102\n\t"
  "v_fma_f32 v71, v71, v67, v103\n\t"
  "v_mfma_f32_16x16x32_bf16 v[52:55], a[48:51], a[148:151], v[52:55]\n\t"
  "v_fma_f32 v72, v72, v108, v104\n\t"
  "v_fma_f32 v73, v73, v109, v105\n\t"
  "v_mfma_f32_16x16x32_bf16 v[56:59], a[52:55], a[148:151], v[56:59]\n\t"
  "s_waitcnt vmcnt(9)\n\t"
  "buffer_load_dwordx4 a[56:59], %[vl16], %[srd], s44 offen offset:2048 sc1\n\t"
  "v_mfma_f32_16x16x32_bf16 v[20:23], v[144:147], a[152:155], 0\n\t"
  "v_fma_f32 v74, v74, v110, v106\n\t"
  "v_fma_f32 v75, v75, v111, v107\n\t"
  "v_mfma_f32_16x16x32_bf16 v[24:27], v[148:151], a[152:155], 0\n\t"
  "v_cvt_pk_bf16_f32 v76, v68, v69\n\t"
  "v_cvt_pk_bf16_f32 v77, v70, v71\n\t"
  "v_mfma_f32_16x16x32_bf16 v[28:31], v[152:155], a[152:155], 0\n\t"
  "buffer_load_dwordx4 a[60:63], %[vl16], %[srd], s44 offen offset:3072 sc1\n\t"
  "v_cvt_pk_bf16_f32 v78, v72, v73\n\t"
  "v_mfma_f32_16x16x32_bf16 v[32:35], v[156:159], a[152:155], 0\n\t"
  "v_cvt_pk_bf16_f32 v79, v74, v75\n\t"
  "s_mov_b32 s55, 0\n\t"
  "v_mfma_f32_16x16x32_bf16 v[36:39], v[160:163], a[152:155], 0\n\t"
  "buffer_store_dwordx4 v[76:79], %[vout], %[srdo], s55 offen sc1\n\t"
  "ds_write_b128 %[vwr], v[40:43] offset:5120\n\t"
  "v_mfma_f32_16x16x32_bf16 v[20:23], v[164:167], a[156:159], v[20:23]\n\t"
  "v_mfma_f32_16x16x32_bf16 v[24:27], v[168:171], a[156:159], v[24:27]\n\t"
  "v_mfma_f32_16x16x32_bf16 v[28:31], v[172:175], a[156:159], v[28:31]\n\t"
  "ds_write_b128 %[vwr], v[44:47] offset:6144\n\t"
  "v_mfma_f32_16x16x32_bf16 v[32:35], v[176:179], a[156:159], v[32:35]\n\t"
  "v_mfma_f32_16x16x32_bf16 v[36:39], v[180:183], a[156:159], v[36:39]\n\t"
  "s_waitcnt vmcnt(9)\n\t"
  "v_mfma_f32_16x16x32_bf16 v[20:23], v[184:187], v[248:251], v[20:23]\n\t"
  "ds_write_b128 %[vwr], v[48:51] offset:7168\n\t"
  "v_mfma_f32_16x16x32_bf16 v[24:27], v[188:191], v[248:251], v[24:27]\n\t"
  "v_mfma_f32_16x16x32_bf16 v[28:31], v[192:195], v[248:251], v[28:31]\n\t"
  "v_mfma_f32_16x16x32_bf16 v[32:35], v[196:199], v[248:251], v[32:35]\n\t"
  "ds_write_b128 %[vwr], v[52:55] offset:8192\n\t"
  "v_mfma_f32_16x16x32_bf16 v[36:39], v[200:203], v[248:251], v[36:39]\n\t"
  "v_mfma_f32_16x16x32_bf16 v[20:23], v[204:207], v[252:255], v[20:23]\n\t"
  "v_cmp_ne_u64 vcc, v[132:133], v[138:139]\n\t"
  "v_mfma_f32_16x16x32_bf16 v[24:27], v[208:211], v[252:255], v[24:27]\n\t"
  "ds_write_b128 %[vwr], v[56:59] offset:9216\n\t"
  "v_mfma_f32_16x16x32_bf16 v[28:31], v[212:215], v[252:255], v[28:31]\n\t"
  "s_cbranch_vccnz Lfl3_slow%=\n\t"
  "Lfl3_back%=:\n\t"
  "v_mfma_f32_16x16x32_bf16 v[32:35], v[216:219], v[252:255], v[32:35]\n\t"
  "s_add_u32 s45, %[sH2ld], 3244032\n\t"
  "v_mfma_f32_16x16x32_bf16 v[36:39], v[220:223], v[252:255], v[36:39]\n\t"
  "s_waitcnt vmcnt(7)\n\t"
  "v_mfma_f32_16x16x32_bf16 v[20:23], v[224:227], v[112:115], v[20:23]\n\t"
  "buffer_load_dwordx4 a[104:107], %[vl16], %[srd], s45 offen offset:0 sc1\n\t"
  "v_mfma_f32_16x16x32_bf16 v[24:27], v[228:231], v[112:115], v[24:27]\n\t"
  "v_mfma_f32_16x16x32_bf16 v[28:31], v[232:235], v[112:115], v[28:31]\n\t"
  "buffer_load_dwordx4 a[108:111], %[vl16], %[srd], s45 offen offset:1024 sc1\n\t"
  "v_mfma_f32_16x16x32_bf16 v[32:35], v[236:239], v[112:115], v[32:35]\n\t"
  "v_mfma_f32_16x16x32_bf16 v[36:39], v[240:243], v[112:115], v[36:39]\n\t"
  "s_waitcnt lgkmcnt(0)\n\t"
  "s_barrier\n\t"
  "v_mfma_f32_16x16x32_bf16 v[20:23], v[244:247], v[116:119], v[20:23]\n\t"
  "buffer_load_dwordx4 a[64:67], %[vl16], %[srd], s45 offen offset:2048 sc1\n\t"
  "ds_read_b128 v[68:71], %[vrd0] offset:5120\n\t"
  "v_mfma_f32_16x16x32_bf16 v[24:27], a[160:163], v[116:119], v[24:27]\n\t"
  "ds_read_b128 v[72:75], %[vrd1] offset:5120\n\t"
  "ds_read_b128 v[76:79], %[vrd0] offset:25600\n\t"
  "v_mfma_f32_16x16x32_bf16 v[28:31], a[164:167], v[116:119], v[28:31]\n\t"
  "buffer_load_dwordx4 a[68:71], %[vl16], %[srd], s45 offen offset:3072 sc1\n\t"
  "ds_read_b128 v[80:83], %[vrd1] offset:25600\n\t"
  "v_mfma_f32_16x16x32_bf16 v[32:35], a[168:171], v[116:119], v[32:35]\n\t"
  "ds_read_b128 v[84:87], %[vrd0] offset:46080\n\t"
  "ds_read_b128 v[88:91], %[vrd1] offset:46080\n\t"
  "v_mfma_f32_16x16x32_bf16 v[36:39], a[172:175], v[116:119], v[36:39]\n\t"
  "s_waitcnt vmcnt(9)\n\t"
  "ds_read_b128 v[92:95], v60 offset:5120\n\t"
  "v_mfma_f32_16x16x32_bf16 v[20:23], a[176:179], v[120:123], v[20:23]\n\t"
  "s_add_u32 s45, %[sH2ld], 3248128\n\t"
  "ds_read_b128 v[96:99], v61 offset:5120\n\t"
  "v_mfma_f32_16x16x32_bf16 v[24:27], a[180:183], v[120:123], v[24:27]\n\t"
  "ds_read_b128 v[100:103], v60 offset:25600\n\t"
  "ds_read_b128 v[104:107], v61 offset:25600\n\t"
  "v_mfma_f32_16x16x32_bf16 v[28:31], a[184:187], v[120:123], v[28:31]\n\t"
  "buffer_load_dwordx4 a[72:75], %[vl16], %[srd], s45 offen offset:0 sc1\n\t"
  "v_mfma_f32_16x16x32_bf16 v[32:35], a[188:191], v[120:123], v[32:35]\n\t"
  "v_mfma_f32_16x16x32_bf16 v[36:39], a[192:195], v[120:123], v[36:39]\n\t"
  "buffer_load_dwordx4 a[76:79], %[vl16], %[srd], s45 offen offset:1024 sc1\n\t"
  "v_mfma_f32_16x16x32_bf16 v[20:23], a[196:199], v[124:127], v[20:23]\n\t"
  "v_mfma_f32_16x16x32_bf16 v[24:27], a[200:203], v[124:127], v[24:27]\n\t"
  "v_mfma_f32_16x16x32_bf16 v[28:31], a[204:207], v[124:127], v[28:31]\n\t"
  "buffer_load_dwordx4 a[80:83], %[vl16], %[srd], s45 offen offset:2048 sc1\n\t"
  "v_mfma_f32_16x16x32_bf16 v[32:35], a[208:211], v[124:127], v[32:35]\n\t"
  "v_mfma_f32_16x16x32_bf16 v[36:39], a[212:215], v[124:127], v[36:39]\n\t"
  "s_waitcnt vmcnt(10)\n\t"
  "buffer_load_dwordx4 a[84:87], %[vl16], %[srd], s45 offen offset:3072 sc1\n\t"
  "v_mfma_f32_16x16x32_bf16 v[20:23], a[216:219], v[128:131], v[20:23]\n\t"
  "s_waitcnt lgkmcnt(0)\n\t"
  "v_add_f32 v68, v68, v76\n\t"
  "v_mfma_f32_16x16x32_bf16 v[24:27], a[220:223], v[128:131], v[24:27]\n\t"
  "v_add_f32 v69, v69, v77\n\t"
  "v_add_f32 v70, v70, v78\n\t"
  "v_mfma_f32_16x16x32_bf16 v[28:31], a[224:227], v[128:131], v[28:31]\n\t"
  "s_add_u32 s45, %[sH2ld], 3252224\n\t"
  "v_add_f32 v71, v71, v79\n\t"
  "v_mfma_f32_16x16x32_bf16 v[32:35], a[228:231], v[128:131], v[32:35]\n\t"
  "v_add_f32 v72, v72, v80\n\t"
  "v_add_f32 v73, v73, v81\n\t"
  "v_mfma_f32_16x16x32_bf16 v[36:39], a[232:235], v[128:131], v[36:39]\n\t"
  "buffer_load_dwordx4 a[88:91], %[vl16], %[srd], s45 offen offset:0 sc1\n\t"
  "v_add_f32 v74, v74, v82\n\t"
  "v_mfma_f32_16x16x32_bf16 v[20:23], a[236:239], a[240:243], v[20:23]\n\t"
  "v_add_f32 v75, v75, v83\n\t"
  "v_add_f32 v84, v84, v92\n\t"
  "v_mfma_f32_16x16x32_bf16 v[24:27], a[0:3], a[240:243], v[24:27]\n\t"
  "buffer_load_dwordx4 a[92:95], %[vl16], %[srd], s45 offen offset:1024 sc1\n\t"
  "v_add_f32 v85, v85, v93\n\t"
  "v_mfma_f32_16x16x32_bf16 v[28:31], a[4:7], a[240:243], v[28:31]\n\t"
  "v_add_f32 v86, v86, v94\n\t"
  "v_add_f32 v87, v87, v95\n\t"
  "v_mfma_f32_16x16x32_bf16 v[32:35], a[8:11], a[240:243], v[32:35]\n\t"
  "v_add_f32 v88, v88, v96\n\t"
  "v_add_f32 v89, v89, v97\n\t"
  "v_mfma_f32_16x16x32_bf16 v[36:39], a[12:15], a[240:243], v[36:39]\n\t"
  "s_waitcnt vmcnt(11)\n\t"
  "buffer_load_dwordx4 a[96:99], %[vl16], %[srd], s45 offen offset:2048 sc1\n\t"
  "v_mfma_f32_16x16x32_bf16 v[20:23], a[16:19], a[56:59], v[20:23]\n\t"
  "v_add_f32 v90, v90, v98\n\t"
  "v_add_f32 v91, v91, v99\n\t"
  "v_mfma_f32_16x16x32_bf16 v[24:27], a[20:23], a[56:59], v[24:27]\n\t"
  "buffer_load_dwordx4 a[100:103], %[vl16], %[srd], s45 offen offset:3072 sc1\n\t"
  "v_add_f32 v68, v68, v84\n\t"
  "v_mfma_f32_16x16x32_bf16 v[28:31], a[24:27], a[56:59], v[28:31]\n\t"
  "v_add_f32 v69, v69, v85\n\t"
  "v_add_f32 v70, v70, v86\n\t"
  "v_mfma_f32_16x16x32_bf16 v[32:35], a[28:31], a[56:59], v[32:35]\n\t"
  "v_add_f32 v71, v71, v87\n\t"
  "v_add_f32 v72, v72, v88\n\t"
  "v_mfma_f32_16x16x32_bf16 v[36:39], a[32:35], a[56:59], v[36:39]\n\t"
  "v_add_f32 v73, v73, v89\n\t"
  "v_add_f32 v74, v74, v90\n\t"
  "v_mfma_f32_16x16x32_bf16 v[20:23], a[36:39], a[60:63], v[20:23]\n\t"
  "v_add_f32 v75, v75, v91\n\t"
  "v_fma_f32 v68, v68, v64, v100\n\t"
  "v_mfma_f32_16x16x32_bf16 v[24:27], a[40:43], a[60:63], v[24:27]\n\t"
  "v_fma_f32 v69, v69, v65, v101\n\t"
  "v_fma_f32 v70, v70, v66, v102\n\t"
  "v_mfma_f32_16x16x32_bf16 v[28:31], a[44:47], a[60:63], v[28:31]\n\t"
  "v_fma_f32 v71, v71, v67, v103\n\t"
  "v_fma_f32 v72, v72, v108, v104\n\t"
  "v_mfma_f32_16x16x32_bf16 v[32:35], a[48:51], a[60:63], v[32:35]\n\t"
  "v_fma_f32 v73, v73, v109, v105\n\t"
  "v_fma_f32 v74, v74, v110, v106\n\t"
  "v_mfma_f32_16x16x32_bf16 v[36:39], a[52:55], a[60:63], v[36:39]\n\t"
  "s_waitcnt vmcnt(10)\n\t"
  "v_fma_f32 v75, v75, v111, v107\n\t"
  "v_mfma_f32_16x16x32_bf16 v[40:43], v[144:147], a[104:107], 0\n\t"
  "v_cvt_pk_bf16_f32 v76, v68, v69\n\t"
  "v_cvt_pk_bf16_f32 v77, v70, v71\n\t"
  "v_mfma_f32_16x16x32_bf16 v[44:47], v[148:151], a[104:107], 0\n\t"
  "v_cvt_pk_bf16_f32 v78, v72, v73\n\t"
  "v_cvt_pk_bf16_f32 v79, v74, v75\n\t"
  "v_mfma_f32_16x16x32_bf16 v[48:51], v[152:155], a[104:107], 0\n\t"
  "s_mov_b32 s55, 40960\n\t"
  "buffer_store_dwordx4 v[76:79], %[vout], %[srdo], s55 offen sc1\n\t"
  "v_mfma_f32_16x16x32_bf16 v[52:55], v[156:159], a[104:107], 0\n\t"
  "ds_write_b128 %[vwr], v[20:23] offset:10240\n\t"
  "v_mfma_f32_16x16x32_bf16 v[56:59], v[160:163], a[104:107], 0\n\t"
  "v_mfma_f32_16x16x32_bf16 v[40:43], v[164:167], a[108:111], v[40:43]\n\t"
  "v_mfma_f32_16x16x32_bf16 v[44:47], v[168:171], a[108:111], v[44:47]\n\t"
  "ds_write_b128 %[vwr], v[24:27] offset:11264\n\t"
  "v_mfma_f32_16x16x32_bf16 v[48:51], v[172:175], a[108:111], v[48:51]\n\t"
  "v_mfma_f32_16x16x32_bf16 v[52:55], v[176:179], a[108:111], v[52:55]\n\t"
  "v_mfma_f32_16x16x32_bf16 v[56:59], v[180:183], a[108:111], v[56:59]\n\t"
  "s_waitcnt vmcnt(9)\n\t"
  "ds_write_b128 %[vwr], v[28:31] offset:12288\n\t"
  "v_mfma_f32_16x16x32_bf16 v[40:43], v[184:187], a[64:67], v[40:43]\n\t"
  "v_mfma_f32_16x16x32_bf16 v[44:47], v[188:191], a[64:67], v[44:47]\n\t"
  "v_mfma_f32_16x16x32_bf16 v[48:51], v[192:195], a[64:67], v[48:51]\n\t"
  "ds_write_b128 %[vwr], v[32:35] offset:13312\n\t"
  "v_mfma_f32_16x16x32_bf16 v[52:55], v[196:199], a[64:67], v[52:55]\n\t"
  "v_mfma_f32_16x16x32_bf16 v[56:59], v[200:203], a[64:67], v[56:59]\n\t"
  "v_mfma_f32_16x16x32_bf16 v[40:43], v[204:207], a[68:71], v[40:43]\n\t"
  "ds_write_b128 %[vwr], v[36:39] offset:14336\n\t"
  "v_mfma_f32_16x16x32_bf16 v[44:47], v[208:211], a[68:71], v[44:47]\n\t"
  "v_mfma_f32_16x16x32_bf16 v[48:51], v[212:215], a[68:71], v[48:51]\n\t"
  "v_mfma_f32_16x16x32_bf16 v[52:55], v[216:219], a[68:71], v[52:55]\n\t"
  "v_mfma_f32_16x16x32_bf16 v[56:59], v[220:223], a[68:71], v[56:59]\n\t"
  "s_waitcnt vmcnt(7)\n\t"
  "v_mfma_f32_16x16x32_bf16 v[40:43], v[224:227], a[72:75], v[40:43]\n\t"
  "v_mfma_f32_16x16x32_bf16 v[44:47], v[228:231], a[72:75], v[44:47]\n\t"
  "v_mfma_f32_16x16x32_bf16 v[48:51], v[232:235], a[72:75], v[48:51]\n\t"
  "v_mfma_f32_16x16x32_bf16 v[52:55], v[236:239], a[72:75], v[52:55]\n\t"
  "s_waitcnt lgkmcnt(0)\n\t"
  "s_barrier\n\t"
  "v_mfma_f32_16x16x32_bf16 v[56:59], v[240:243], a[72:75], v[56:59]\n\t"
  "ds_read_b128 v[68:71], %[vrd0] offset:10240\n\t"
  "ds_read_b128 v[72:75], %[vrd1] offset:10240\n\t"
  "v_mfma_f32_16x16x32_bf16 v[40:43], v[244:247], a[76:79], v[40:43]\n\t"
  "ds_read_b128 v[76:79], %[vrd0] offset:30720\n\t"
  "ds_read_b128 v[80:83], %[vrd1] offset:30720\n\t"
  "v_mfma_f32_16x16x32_bf16 v[44:47], a[160:163], a[76:79], v[44:47]\n\t"
  "ds_read_b128 v[84:87], %[vrd0] offset:51200\n\t"
  "ds_read_b128 v[88:91], %[vrd1] offset:51200\n\t"
  "v_mfma_f32_16x16x32_bf16 v[48:51], a[164:167], a[76:79], v[48:51]\n\t"
  "ds_read_b128 v[92:95], v60 offset:10240\n\t"
  "ds_read_b128 v[96:99], v61 offset:10240\n\t"
  "v_mfma_f32_16x16x32_bf16 v[52:55], a[168:171], a[76:79], v[52:55]\n\t"
  "ds_read_b128 v[100:103], v60 offset:30720\n\t"
  "ds_read_b128 v[104:107], v61 offset:30720\n\t"
  "v_mfma_f32_16x16x32_bf16 v[56:59], a[172:175], a[76:79], v[56:59]\n\t"
  "s_waitcnt vmcnt(5)\n\t"
  "v_mfma_f32_16x16x32_bf16 v[40:43], a[176:179], a[80:83], v[40:43]\n\t"
  "v_mfma_f32_16x16x32_bf16 v[44:47], a[180:183], a[80:83], v[44:47]\n\t"
  "v_mfma_f32_16x16x32_bf16 v[48:51], a[184:187], a[80:83], v[48:51]\n\t"
  "v_mfma_f32_16x16x32_bf16 v[52:55], a[188:191], a[80:83], v[52:55]\n\t"
  "v_mfma_f32_16x16x32_bf16 v[56:59], a[192:195], a[80:83], v[56:59]\n\t"
  "v_mfma_f32_16x16x32_bf16 v[40:43], a[196:199], a[84:87], v[40:43]\n\t"
  "v_mfma_f32_16x16x32_bf16 v[44:47], a[200:203], a[84:87], v[44:47]\n\t"
  "s_waitcnt lgkmcnt(0)\n\t"
  "v_add_f32 v68, v68, v76\n\t"
  "v_mfma_f32_16x16x32_bf16 v[48:51], a[204:207], a[84:87], v[48:51]\n\t"
  "v_add_f32 v69, v69, v77\n\t"
  "v_add_f32 v70, v70, v78\n\t"
  "v_mfma_f32_16x16x32_bf16 v[52:55], a[208:211], a[84:87], v[52:55]\n\t"
  "v_add_f32 v71, v71, v79\n\t"
  "v_add_f32 v72, v72, v80\n\t"
  "v_mfma_f32_16x16x32_bf16 v[56:59], a[212:215], a[84:87], v[56:59]\n\t"
  "s_waitcnt vmcnt(3)\n\t"
  "v_add_f32 v73, v73, v81\n\t"
  "v_mfma_f32_16x16x32_bf16 v[40:43], a[216:219], a[88:91], v[40:43]\n\t"
  "v_add_f32 v74, v74, v82\n\t"
  "v_add_f32 v75, v75, v83\n\t"
  "v_mfma_f32_16x16x32_bf16 v[44:47], a[220:223], a[88:91], v[44:47]\n\t"
  "v_add_f32 v84, v84, v92\n\t"
  "v_add_f32 v85, v85, v93\n\t"
  "v_mfma_f32_16x16x32_bf16 v[48:51], a[224:227], a[88:91], v[48:51]\n\t"
  "v_add_f32 v86, v86, v94\n\t"
  "v_add_f32 v87, v87, v95\n\t"
  "v_mfma_f32_16x16x32_bf16 v[52:55], a[228:231], a[88:91], v[52:55]\n\t"
  "v_add_f32 v88, v88, v96\n\t"
  "v_add_f32 v89, v89, v97\n\t"
  "v_mfma_f32_16x16x32_bf16 v[56:59], a[232:235], a[88:91], v[56:59]\n\t"
  "v_add_f32 v90, v90, v98\n\t"
  "v_add_f32 v91, v91, v99\n\t"
  "v_mfma_f32_16x16x32_bf16 v[40:43], a[236:239], a[92:95], v[40:43]\n\t"
  "v_add_f32 v68, v68, v84\n\t"
  "v_add_f32 v69, v69, v85\n\t"
  "v_mfma_f32_16x16x32_bf16 v[44:47], a[0:3], a[92:95], v[44:47]\n\t"
  "v_add_f32 v70, v70, v86\n\t"
  "v_add_f32 v71, v71, v87\n\t"
  "v_mfma_f32_16x16x32_bf16 v[48:51], a[4:7], a[92:95], v[48:51]\n\t"
  "v_add_f32 v72, v72, v88\n\t"
  "v_add_f32 v73, v73, v89\n\t"
  "v_mfma_f32_16x16x32_bf16 v[52:55], a[8:11], a[92:95], v[52:55]\n\t"
  "v_add_f32 v74, v74, v90\n\t"
  "v_add_f32 v75, v75, v91\n\t"
  "v_mfma_f32_16x16x32_bf16 v[56:59], a[12:15], a[92:95], v[56:59]\n\t"
  "s_waitcnt vmcnt(1)\n\t"
  "v_fma_f32 v68, v68, v64, v100\n\t"
  "v_mfma_f32_16x16x32_bf16 v[40:43], a[16:19], a[96:99], v[40:43]\n\t"
  "v_fma_f32 v69, v69, v65, v101\n\t"
  "v_fma_f32 v70, v70, v66, v102\n\t"
  "v_mfma_f32_16x16x32_bf16 v[44:47], a[20:23], a[96:99], v[44:47]\n\t"
  "v_fma_f32 v71, v71, v67, v103\n\t"
  "v_fma_f32 v72, v72, v108, v104\n\t"
  "v_mfma_f32_16x16x32_bf16 v[48:51], a[24:27], a[96:99], v[48:51]\n\t"
  "v_fma_f32 v73, v73, v109, v105\n\t"
  "v_fma_f32 v74, v74, v110, v106\n\t"
  "v_mfma_f32_16x16x32_bf16 v[52:55], a[28:31], a[96:99], v[52:55]\n\t"
  "v_fma_f32 v75, v75, v111, v107\n\t"
  "v_cvt_pk_bf16_f32 v76, v68, v69\n\t"
  "v_mfma_f32_16x16x32_bf16 v[56:59], a[32:35], a[96:99], v[56:59]\n\t"
  "v_cvt_pk_bf16_f32 v77, v70, v71\n\t"
  "v_cvt_pk_bf16_f32 v78, v72, v73\n\t"
  "v_mfma_f32_16x16x32_bf16 v[40:43], a[36:39], a[100:103], v[40:43]\n\t"
  "v_cvt_pk_bf16_f32 v79, v74, v75\n\t"
  "s_mov_b32 s55, 1310720\n\t"
  "v_mfma_f32_16x16x32_bf16 v[44:47], a[40:43], a[100:103], v[44:47]\n\t"
  "buffer_store_dwordx4 v[76:79], %[vout], %[srdo], s55 offen sc1\n\t"
  "v_mfma_f32_16x16x32_bf16 v[48:51], a[44:47], a[100:103], v[48:51]\n\t"
  "v_mfma_f32_16x16x32_bf16 v[52:55], a[48:51], a[100:103], v[52:55]\n\t"
  "v_mfma_f32_16x16x32_bf16 v[56:59], a[52:55], a[100:103], v[56:59]\n\t"
  "s_nop 0\n\t"
  "ds_write_b128 %[vwr], v[40:43] offset:15360\n\t"
  "s_nop 1\n\t"
  "ds_write_b128 %[vwr], v[44:47] offset:16384\n\t"
  "s_nop 0\n\t"
  "ds_write_b128 %[vwr], v[48:51] offset:17408\n\t"
  "ds_write_b128 %[vwr], v[52:55] offset:18432\n\t"
  "ds_write_b128 %[vwr], v[56:59] offset:19456\n\t"
  "buffer_store_dwordx2 v[132:133], %[vep], %[srd], 0 offen sc1\n\t"
  "s_waitcnt lgkmcnt(0)\n\t"
  "s_barrier\n\t"
  "ds_read_b128 v[68:71], %[vrd0] offset:15360\n\t"
  "ds_read_b128 v[72:75], %[vrd1] offset:15360\n\t"
  "ds_read_b128 v[76:79], %[vrd0] offset:35840\n\t"
  "ds_read_b128 v[80:83], %[vrd1] offset:35840\n\t"
  "ds_read_b128 v[84:87], %[vrd0] offset:56320\n\t"
  "ds_read_b128 v[88:91], %[vrd1] offset:56320\n\t"
  "ds_read_b128 v[92:95], v60 offset:15360\n\t"
  "ds_read_b128 v[96:99], v61 offset:15360\n\t"
  "ds_read_b128 v[100:103], v60 offset:35840\n\t"
  "ds_read_b128 v[104:107], v61 offset:35840\n\t"
  "s_waitcnt lgkmcnt(7)\n\t"
  "v_pk_add_f32 v[68:69], v[68:69], v[76:77]\n\t"
  "v_pk_add_f32 v[70:71], v[70:71], v[78:79]\n\t"
  "s_waitcnt lgkmcnt(6)\n\t"
  "v_pk_add_f32 v[72:73], v[72:73], v[80:81]\n\t"
  "v_pk_add_f32 v[74:75], v[74:75], v[82:83]\n\t"
  "s_waitcnt lgkmcnt(3)\n\t"
  "v_pk_add_f32 v[84:85], v[84:85], v[92:93]\n\t"
  "v_pk_add_f32 v[86:87], v[86:87], v[94:95]\n\t"
  "s_waitcnt lgkmcnt(2)\n\t"
  "v_pk_add_f32 v[88:89], v[88:89], v[96:97]\n\t"
  "v_pk_add_f32 v[90:91], v[90:91], v[98:99]\n\t"
  "v_pk_add_f32 v[68:69], v[68:69], v[84:85]\n\t"
  "v_pk_add_f32 v[70:71], v[70:71], v[86:87]\n\t"
  "v_pk_add_f32 v[72:73], v[72:73], v[88:89]\n\t"
  "v_pk_add_f32 v[74:75], v[74:75], v[90:91]\n\t"
  "s_waitcnt lgkmcnt(1)\n\t"
  "v_pk_fma_f32 v[68:69], v[68:69], v[64:65], v[100:101]\n\t"
  "v_pk_fma_f32 v[70:71], v[70:71], v[66:67], v[102:103]\n\t"
  "s_waitcnt lgkmcnt(0)\n\t"
  "v_pk_fma_f32 v[72:73], v[72:73], v[108:109], v[104:105]\n\t"
  "v_pk_fma_f32 v[74:75], v[74:75], v[110:111], v[106:107]\n\t"
  "v_cvt_pk_bf16_f32 v76, v68, v69\n\t"
  "v_cvt_pk_bf16_f32 v77, v70, v71\n\t"
  "v_cvt_pk_bf16_f32 v78, v72, v73\n\t"
  "v_cvt_pk_bf16_f32 v79, v74, v75\n\t"
  "s_mov_b32 s55, 1351680\n\t"
  "buffer_store_dwordx4 v[76:79], %[vout], %[srdo], s55 offen sc1\n\t"
  "s_branch Lfend%=\n\t"
  "Lst0_slow%=:\n\t"
  "s_nop 7\n\t"
  "s_nop 3\n\t"
  "s_mov_b32 s49, 0x40000\n\t"
  "Lst0_loop%=:\n\t"
  "s_sleep 2\n\t"
  "buffer_load_dwordx2 v[92:93], v136, %[srd], %[sST] offen offset:0 sc1\n\t"
  "buffer_load_dwordx2 v[94:95], v136, %[srd], %[sST] offen offset:1024 sc1\n\t"
  "buffer_load_dwordx2 v[96:97], v136, %[srd], %[sST] offen offset:2048 sc1\n\t"
  "buffer_load_dwordx2 v[98:99], v136, %[srd], %[sST] offen offset:3072 sc1\n\t"
  "s_waitcnt vmcnt(0)\n\t"
  "v_min_u32 v131, v93, v95\n\t"
  "v_min3_u32 v131, v131, v97, v99\n\t"
  "v_cmp_eq_u32 vcc, 0, v131\n\t"
  "s_nop 3\n\t"
  "s_cmp_eq_u64 vcc, 0\n\t"
  "s_cbranch_scc1 Lst0_back%=\n\t"
  "s_sub_u32 s49, s49, 1\n\t"
  "s_cmp_lg_u32 s49, 0\n\t"
  "s_cbranch_scc1 Lst0_loop%=\n\t"
  "s_branch Lst0_back%=\n\t"
  "Lw2_slow%=:\n\t"
  "s_nop 7\n\t"
  "s_nop 3\n\t"
  "s_mov_b32 s49, 0x40000\n\t"
  "Lw2_loop%=:\n\t"
  "s_sleep 2\n\t"
  "buffer_load_dwordx2 v[138:139], %[vpw], %[srd], 0 offen sc1\n\t"
  "s_waitcnt vmcnt(0)\n\t"
  "v_cmp_ne_u64 vcc, v[132:133], v[138:139]\n\t"
  "s_nop 3\n\t"
  "s_cmp_eq_u64 vcc, 0\n\t"
  "s_cbranch_scc1 Lw2_back%=\n\t"
  "s_sub_u32 s49, s49, 1\n\t"
  "s_cmp_lg_u32 s49, 0\n\t"
  "s_cbranch_scc1 Lw2_loop%=\n\t"
  "s_branch Lw2_back%=\n\t"
  "Lst1_slow%=:\n\t"
  "s_nop 7\n\t"
  "s_nop 3\n\t"
  "s_mov_b32 s49, 0x40000\n\t"
  "Lst1_loop%=:\n\t"
  "s_sleep 2\n\t"
  "buffer_load_dwordx2 v[100:101], v136, %[srd], s54 offen offset:0 sc1\n\t"
  "buffer_load_dwordx2 v[102:103], v136, %[srd], s54 offen offset:1024 sc1\n\t"
  "buffer_load_dwordx2 v[104:105], v136, %[srd], s54 offen offset:2048 sc1\n\t"
  "buffer_load_dwordx2 v[106:107], v136, %[srd], s54 offen offset:3072 sc1\n\t"
  "s_waitcnt vmcnt(0)\n\t"
  "v_min_u32 v131, v101, v103\n\t"
  "v_min3_u32 v131, v131, v105, v107\n\t"
  "v_cmp_eq_u32 vcc, 0, v131\n\t"
  "s_nop 3\n\t"
  "s_cmp_eq_u64 vcc, 0\n\t"
  "s_cbranch_scc1 Lst1_back%=\n\t"
  "s_sub_u32 s49, s49, 1\n\t"
  "s_cmp_lg_u32 s49, 0\n\t"
  "s_cbranch_scc1 Lst1_loop%=\n\t"
  "s_branch Lst1_back%=\n\t"
  "Lfl0_slow%=:\n\t"
  "s_nop 7\n\t"
  "s_nop 3\n\t"
  "s_mov_b32 s49, 0x40000\n\t"
  "Lfl0_loop%=:\n\t"
  "s_sleep 2\n\t"
  "s_add_u32 s57, %[sFL], 0\n\t"
  "buffer_load_dwordx2 v[138:139], v137, %[srd], s57 offen sc1\n\t"
  "s_waitcnt vmcnt(0)\n\t"
  "v_cmp_ne_u64 vcc, v[132:133], v[138:139]\n\t"
  "s_nop 3\n\t"
  "s_cmp_eq_u64 vcc, 0\n\t"
  "s_cbranch_scc1 Lfl0_back%=\n\t"
  "s_sub_u32 s49, s49, 1\n\t"
  "s_cmp_lg_u32 s49, 0\n\t"
  "s_cbranch_scc1 Lfl0_loop%=\n\t"
  "s_branch Lfl0_back%=\n\t"
  "Lfl1_slow%=:\n\t"
  "s_nop 7\n\t"
  "s_nop 3\n\t"
  "s_mov_b32 s49, 0x40000\n\t"
  "Lfl1_loop%=:\n\t"
  "s_sleep 2\n\t"
  "s_add_u32 s57, %[sFL], 8192\n\t"
  "buffer_load_dwordx2 v[138:139], v137, %[srd], s57 offen sc1\n\t"
  "s_waitcnt vmcnt(0)\n\t"
  "v_cmp_ne_u64 vcc, v[132:133], v[138:139]\n\t"
  "s_nop 3\n\t"
  "s_cmp_eq_u64 vcc, 0\n\t"
  "s_cbranch_scc1 Lfl1_back%=\n\t"
  "s_sub_u32 s49, s49, 1\n\t"
  "s_cmp_lg_u32 s49, 0\n\t"
  "s_cbranch_scc1 Lfl1_loop%=\n\t"
  "s_branch Lfl1_back%=\n\t"
  "Lfl2_slow%=:\n\t"
  "s_nop 7\n\t"
  "s_nop 3\n\t"
  "s_mov_b32 s49, 0x40000\n\t"
  "Lfl2_loop%=:\n\t"
  "s_sleep 2\n\t"
  "s_add_u32 s57, %[sFL], 16384\n\t"
  "buffer_load_dwordx2 v[138:139], v137, %[srd], s57 offen sc1\n\t"
  "s_waitcnt vmcnt(0)\n\t"
  "v_cmp_ne_u64 vcc, v[132:133], v[138:139]\n\t"
  "s_nop 3\n\t"
  "s_cmp_eq_u64 vcc, 0\n\t"
  "s_cbranch_scc1 Lfl2_back%=\n\t"
  "s_sub_u32 s49, s49, 1\n\t"
  "s_cmp_lg_u32 s49, 0\n\t"
  "s_cbranch_scc1 Lfl2_loop%=\n\t"
  "s_branch Lfl2_back%=\n\t"
  "Lfl3_slow%=:\n\t"
  "s_nop 7\n\t"
  "s_nop 3\n\t"
  "s_mov_b32 s49, 0x40000\n\t"
  "Lfl3_loop%=:\n\t"
  "s_sleep 2\n\t"
  "s_add_u32 s57, %[sFL], 24576\n\t"
  "buffer_load_dwordx2 v[138:139], v137, %[srd], s57 offen sc1\n\t"
  "s_waitcnt vmcnt(0)\n\t"
  "v_cmp_ne_u64 vcc, v[132:133], v[138:139]\n\t"
  "s_nop 3\n\t"
  "s_cmp_eq_u64 vcc, 0\n\t"
  "s_cbranch_scc1 Lfl3_back%=\n\t"
  "s_sub_u32 s49, s49, 1\n\t"
  "s_cmp_lg_u32 s49, 0\n\t"
  "s_cbranch_scc1 Lfl3_loop%=\n\t"
  "s_branch Lfl3_back%=\n\t"
  "Lfend%=:\n\t"
  ".Lk_end:\n\t"
    :
    : [srd] "s"(srd), [srdo] "s"(srdo), [w] "s"(w),
      [tlo] "s"((u32)tag), [thi] "s"((u32)(tag >> 32)), [vep] "v"(vep), [sW1] "s"(OFF_W1F + (j * 4 + w) * 61440u), [sW2] "s"(OFF_W2F + (ng * 4 + w) * 61440u),
      [sUB] "s"(OFF_UB + (j * 4 + w) * 1536u),
      [sH2ld] "s"(OFF_H2F + ((4 * p + sub) * 48 + 12 * w) * 1024u), [sST] "s"(OFF_ST2 + p * 4096u), [sFL] "s"(OFF_FL + p * 32768u + w * 2048u),
      [sRT] "s"(OFF_RT + ng * 320u), [wdone] "s"(wdone), [vpub] "v"(vpub), [vpw] "v"(vpw), [vpx] "v"(vpx),
      [vtr] "v"(vtr), [vl16] "v"(vl16), [vdma] "v"(vdma), [vh2st] "v"(vh2st), [vstc] "v"(vstc), [vstw] "v"(vstw), [vflw] "v"(vflw),
      [vres0] "v"(vres0), [vres1] "v"(vres1), [vres2] "v"(vres2), [vwr] "v"(vwr), [vrd0] "v"(vrd0), [vrd1] "v"(vrd1), [vout] "v"(vout), [vgb] "v"(vgb), [vh1w] "v"(vh1w)
    : "memory", "vcc", "scc",
"a0", "a1", "a2", "a3", "a4", "a5", "a6", "a7", "a8", "a9", "a10", "a11", "a12", "a13", "a14", "a15", "a16", "a17", "a18", "a19", "a20", "a21", "a22", "a23", "a24", "a25", "a26", "a27", "a28", "a29", "a30", "a31", "a32", "a33", "a34", "a35", "a36", "a37", "a38", "a39", "a40", "a41", "a42", "a43", "a44", "a45", "a46", "a47", "a48", "a49", "a50", "a51", "a52", "a53", "a54", "a55", "a56", "a57", "a58", "a59", "a60", "a61", "a62", "a63", "a64", "a65", "a66", "a67", "a68", "a69", "a70", "a71", "a72", "a73", "a74", "a75", "a76", "a77", "a78", "a79", "a80", "a81", "a82", "a83", "a84", "a85", "a86", "a87", "a88", "a89", "a90", "a91", "a92", "a93", "a94", "a95", "a96", "a97", "a98", "a99", "a100", "a101", "a102", "a103", "a104", "a105", "a106", "a107", "a108", "a109", "a110", "a111", "a112", "a113", "a114", "a115", "a116", "a117", "a118", "a119", "a120", "a121", "a122", "a123", "a124", "a125", "a126", "a127", "a128", "a129", "a130", "a131", "a132", "a133", "a134", "a135", "a136", "a137", "a138", "a139", "a140", "a141", "a142", "a143", "a144", "a145", "a146", "a147", "a148", "a149", "a150", "a151", "a152", "a153", "a154", "a155", "a156", "a157", "a158", "a159", "a160", "a161", "a162", "a163", "a164", "a165", "a166", "a167", "a168", "a169", "a170", "a171", "a172", "a173", "a174", "a175", "a176", "a177", "a178", "a179", "a180", "a181", "a182", "a183", "a184", "a185", "a186", "a187", "a188", "a189", "a190", "a191", "a192", "a193", "a194", "a195", "a196", "a197", "a198", "a199", "a200", "a201", "a202", "a203", "a204", "a205", "a206", "a207", "a208", "a209", "a210", "a211", "a212", "a213", "a214", "a215", "a216", "a217", "a218", "a219", "a220", "a221", "a222", "a223", "a224", "a225", "a226", "a227", "a228", "a229", "a230", "a231", "a232", "a233", "a234", "a235", "a236", "a237", "a238", "a239", "a240", "a241", "a242", "a243", "a244", "a245", "a246", "a247", "a248", "a249", "a250", "a251", "a252", "a253", "a254", "a255", "s44", "s45", "s46", "s47", "s48", "s49", "s52", "s54", "s55", "s56", "s57", "v20", "v21", "v22", "v23", "v24", "v25", "v26", "v27", "v28", "v29", "v30", "v31", "v32", "v33", "v34", "v35", "v36", "v37", "v38", "v39", "v40", "v41", "v42", "v43", "v44", "v45", "v46", "v47", "v48", "v49", "v50", "v51", "v52", "v53", "v54", "v55", "v56", "v57", "v58", "v59", "v60", "v61", "v62", "v63", "v64", "v65", "v66", "v67", "v68", "v69", "v70", "v71", "v72", "v73", "v74", "v75", "v76", "v77", "v78", "v79", "v80", "v81", "v82", "v83", "v84", "v85", "v86", "v87", "v88", "v89", "v90", "v91", "v92", "v93", "v94", "v95", "v96", "v97", "v98", "v99", "v100", "v101", "v102", "v103", "v104", "v105", "v106", "v107", "v108", "v109", "v110", "v111", "v112", "v113", "v114", "v115", "v116", "v117", "v118", "v119", "v120", "v121", "v122", "v123", "v124", "v125", "v126", "v127", "v128", "v129", "v130", "v131", "v132", "v133", "v134", "v135", "v136", "v137", "v138", "v139", "v141", "v142", "v143", "v144", "v145", "v146", "v147", "v148", "v149", "v150", "v151", "v152", "v153", "v154", "v155", "v156", "v157", "v158", "v159", "v160", "v161", "v162", "v163", "v164", "v165", "v166", "v167", "v168", "v169", "v170", "v171", "v172", "v173", "v174", "v175", "v176", "v177", "v178", "v179", "v180", "v181", "v182", "v183", "v184", "v185", "v186", "v187", "v188", "v189", "v190", "v191", "v192", "v193", "v194", "v195", "v196", "v197", "v198", "v199", "v200", "v201", "v202", "v203", "v204", "v205", "v206", "v207", "v208", "v209", "v210", "v211", "v212", "v213", "v214", "v215", "v216", "v217", "v218", "v219", "v220", "v221", "v222", "v223", "v224", "v225", "v226", "v227", "v228", "v229", "v230", "v231", "v232", "v233", "v234", "v235", "v236", "v237", "v238", "v239", "v240", "v241", "v242", "v243", "v244", "v245", "v246", "v247", "v248", "v249", "v250", "v251", "v252", "v253", "v254", "v255"
  );
}

extern "C" void kernel_launch(void* const* d_in, const int* in_sizes, int n_in, void* d_out, int out_size,
                              void* d_ws, size_t ws_size, hipStream_t stream) {
  k_main<<<256, 256, 0, stream>>>((const u16*)d_in[0], (const u16*)d_in[1], (const u16*)d_in[2], (const u16*)d_in[3], (const u16*)d_in[4],
                                  (const u16*)d_in[5], (const u16*)d_in[7], (const u16*)d_in[8], (const u16*)d_in[9], (const u16*)d_in[10],
                                  (const u16*)d_in[11], (char*)d_ws, (u16*)d_out);
}
```

```cpp
#include <hip/hip_runtime.h>
#include <stdint.h>

typedef unsigned short u16;
typedef unsigned int u32;
typedef unsigned long long u64;
typedef __attribute__((ext_vector_type(8))) __bf16 bf16x8;
typedef __attribute__((ext_vector_type(2))) __bf16 bf16x2;
typedef __attribute__((ext_vector_type(4))) short s16x4;
typedef __attribute__((ext_vector_type(4))) float f32x4;
typedef __attribute__((ext_vector_type(2))) float f32x2;
typedef __attribute__((ext_vector_type(4))) u32 u32x4;
typedef __attribute__((ext_vector_type(2))) u32 u32x2;
#define LAS __attribute__((address_space(3)))
#define GAS __attribute__((address_space(1)))
#define DI __device__ __forceinline__

#define STAMP(id)
#define STAMP_DECL
#define STAMP_OUT(kid)
#define A1_R 4
#define A1_P 8
#define A1_S 2
#define A1_S0 0
#define A1_X 0
#define DBG_W 1
#define DBG_X 1
#define DBG_M 1
#define W_DELAY 12
#define ROLEMAP 0
#define SELF_BAR 0
#define CODEW 1
#define U_DELAY 16
#define PRE_ORDER 3
#define OPS_ORDER 0
constexpr int S_ = 2048, D_ = 640, F_ = 1536;
constexpr unsigned OFF_EP = 0u;
constexpr unsigned OFF_FR = 2048u;
constexpr unsigned OFF_FA1 = 4096u;
constexpr unsigned OFF_FA2 = 24576u;
constexpr unsigned OFF_FA3 = 55296u;
constexpr unsigned OFF_FX = 71680u;
constexpr unsigned OFF_FW1 = 92160u;
constexpr unsigned OFF_FU = 100352u;
constexpr unsigned OFF_FW2 = 108544u;
constexpr unsigned OFF_RT = 112640u;
constexpr unsigned OFF_GB = 115200u;
constexpr unsigned OFF_RL = 117760u;
constexpr unsigned OFF_ST2 = 131072u;
constexpr unsigned OFF_FL = 262144u;
constexpr unsigned OFF_MODP = 786432u;
constexpr unsigned OFF_ST1 = 847872u;
constexpr unsigned OFF_UB = 864256u;
constexpr unsigned OFF_KVT = 962560u;
constexpr unsigned OFF_XT = 3584000u;
constexpr unsigned OFF_X2P = 6205440u;
constexpr unsigned OFF_H2F = 8826880u;
constexpr unsigned OFF_W1F = 15118336u;
constexpr unsigned OFF_W2F = 19050496u;
constexpr unsigned WS_END = 21016576u;
constexpr unsigned TAG_A = 2654435769u;
constexpr unsigned TAG_B = 2246822507u;
constexpr unsigned TAG_C = 2135587861u;
constexpr unsigned TAG_D = 3266489909u;
constexpr unsigned FC_PITCH = 4160u;
constexpr unsigned FC_CH = 16640u;
constexpr unsigned L_FC = 0u;
constexpr unsigned L_KV = 66560u;
constexpr unsigned L_X = 84992u;
constexpr unsigned L_ST = 101376u;
constexpr unsigned L_XR = 117760u;
constexpr unsigned L_CNT = 140288u;
constexpr unsigned L_STASH = 143360u;
constexpr unsigned L_UN = 71680u;
constexpr unsigned UN_SZ = 22528u;

constexpr unsigned OOB = 0x80000000u;
DI unsigned rfl(unsigned x) { return __builtin_amdgcn_readfirstlane(x); }
DI unsigned stkey(unsigned r) { const unsigned k = (r >> 1) & 7; return (((k ^ (k >> 1)) >> 1) & 1) | ((k & 1) << 1) | (k & 4); }
DI float bflo(u32 w) { return __uint_as_float(w << 16); }
DI float bfhi(u32 w) { return __uint_as_float(w & 0xffff0000u); }
DI u32 pack_bf16(float lo, float hi) { bf16x2 v; v[0] = (__bf16)lo; v[1] = (__bf16)hi; return __builtin_bit_cast(u32, v); }
DI float rbf(float x) { __bf16 b = (__bf16)x; return (float)b; }
DI float dot2(u32 a, u32 b, float c) { return __builtin_amdgcn_fdot2_f32_bf16(__builtin_bit_cast(bf16x2, a), __builtin_bit_cast(bf16x2, b), c, false); }
DI f32x4 mfma(u32x4 a, u32x4 b, f32x4 c) { return __builtin_amdgcn_mfma_f32_16x16x32_bf16(__builtin_bit_cast(bf16x8, a), __builtin_bit_cast(bf16x8, b), c, 0, 0, 0); }
DI void mfma_v(f32x4& c, u32x4 a, u32x4 b) { asm volatile("s_nop 1\n\tv_mfma_f32_16x16x32_bf16 %0, %1, %2, %0" : "+v"(c) : "v"(a), "v"(b)); }
DI u32x2 tr_read(const char* p) { return __builtin_bit_cast(u32x2, __builtin_amdgcn_ds_read_tr16_b64_v4i16((LAS s16x4*)p)); }
typedef __amdgpu_buffer_rsrc_t rsrc_t;
DI rsrc_t mkrsrc(const void* p, unsigned bytes) { return __builtin_amdgcn_make_buffer_rsrc((void*)p, 0, bytes, 0x00020000); }
#define BAR() asm volatile("s_waitcnt lgkmcnt(0)\n\ts_barrier" ::: "memory")
#define FENCE() do { asm volatile("" ::: "memory"); __builtin_amdgcn_sched_barrier(0); } while (0)
constexpr int SC1 = 16, NT = 2, W_IDLE_SLEEP = 8;

DI u64 load_tag(const char* ws) {
  u64 epoch;
  asm volatile("s_load_dwordx2 %0, %1, 0\n\ts_waitcnt lgkmcnt(0)" : "=&s"(epoch) : "s"(ws) : "memory");
  const u32 tl = ((u32)epoch ^ TAG_A) * TAG_B, th = ((u32)(epoch >> 32) ^ TAG_C) * TAG_D + tl;
  return ((u64)th << 32) | tl;
}
template <int N, int SL = 2>
DI void poll_flags(rsrc_t srd, unsigned voff, u64 tag) {
  const u32 lo = (u32)tag, hi = (u32)(tag >> 32);
#pragma unroll 1
  for (int spin = 0; spin < (1 << 18); ++spin) {
    u32 bad = 0;
#pragma unroll
    for (int k = 0; k < N; ++k) { const u32x2 f = __builtin_amdgcn_raw_buffer_load_b64(srd, voff + k * 512, 0, SC1); bad |= (f[0] ^ lo) | (f[1] ^ hi); }
    asm volatile("" ::: "memory");
    if (__all(bad == 0)) break;
    __builtin_amdgcn_s_sleep(SL);
  }
  FENCE();
}
DI void put_flag(rsrc_t srd, unsigned off, unsigned lane, u64 tag) {
  __builtin_amdgcn_raw_buffer_store_b128(u32x4{(u32)tag, (u32)(tag >> 32), (u32)tag, (u32)(tag >> 32)}, srd, lane < 8 ? off + lane * 16 : OOB, 0, SC1);
  FENCE();
}
DI float modval(rsrc_t srd, unsigned col) {
  float m[4];
#pragma unroll
  for (int k = 0; k < 4; ++k) m[k] = __uint_as_float(__builtin_amdgcn_raw_buffer_load_b32(srd, col * 4, OFF_MODP + k * 15360, SC1));
  return rbf((m[0] + m[1]) + (m[2] + m[3]));
}

constexpr int RP1 = 400, RP2 = 656, L_WS = 69632;

__global__ __launch_bounds__(256, 1) void k_main(const u16* __restrict__ x, const u16* __restrict__ kIn, const u16* __restrict__ vIn,
                                                 const u16* __restrict__ shk, const u16* __restrict__ ssmk, const u16* __restrict__ ssmD,
                                                 const u16* __restrict__ wg, const u16* __restrict__ wu, const u16* __restrict__ wd,
                                                 const u16* __restrict__ cIn, const u16* __restrict__ wmod, char* ws, u16* __restrict__ out) {
  __shared__ __attribute__((aligned(16))) char smem[163840];
  const unsigned b = blockIdx.x, tid = threadIdx.x, w = rfl(tid >> 6), lane = tid & 63;
  const unsigned xcd = b & 7, loc = b >> 3, pl = loc & 3, p = 4 * (xcd >> 1) + pl, j = 8 * (xcd & 1) + (loc >> 2), ng = j & 7, sub = j >> 3;
  asm volatile("" :: "s"(x), "s"(kIn), "s"(vIn), "s"(shk), "s"(ssmk), "s"(ssmD), "s"(wg), "s"(wu), "s"(wd), "s"(cIn), "s"(wmod), "s"(ws), "s"(out));
  STAMP_DECL; STAMP(0);
  const rsrc_t srd = mkrsrc(ws, WS_END);
  const rsrc_t srdo = mkrsrc(out, S_ * D_ * 2);
  const unsigned lb = (unsigned)(size_t)smem;
  const unsigned role = loc < 20 ? 0 : loc < 28 ? 1 : 2;
  const unsigned cb = xcd * 20 + loc, w1 = xcd * 8 + loc - 20, w2 = xcd * 4 + loc - 28;
  const u32x4 zero4 = {0, 0, 0, 0};
  unsigned wdone = 0, dval = 0;
  if (w == 3 && loc >= 24) {
    u64 pc; unsigned len;
    asm volatile("s_getpc_b64 %0\n.Lk_here:\n\ts_mov_b32 %1, .Lk_end-.Lk_here" : "=s"(pc), "=s"(len));
    asm volatile("buffer_load_dword a255, %0, %1, 0 offen" :: "v"((lane * 8 + loc - 24) * 128), "s"(mkrsrc((const void*)pc, len)) : "a255", "memory");
  }
#define U_KQ(u_) ((u_) & 3)
#define U_CG(u_) (((u_) >> 2) < 30 || ((u_) >> 2) >= 50 ? (u_) >> 2 : ((u_) >> 2) < 40 ? ((u_) >> 2) + 10 : ((u_) >> 2) - 10)
#define U_SRC(u_) (U_KQ(u_) * 160u * 7680u + U_CG(u_) * 128u)
#define U_FLAG(u_) (OFF_FA2 + (U_KQ(u_) * 60 + U_CG(u_)) * 128)
  const unsigned vm = (lane >> 3) * 7680 + (((lane & 7) ^ (((lane >> 4) & 3) << 1)) * 16);
#define GEMV_LOAD(u_, lu_) asm volatile( \
  "s_nop 4\n\t" \
  "s_add_u32 m0, %[lu], 20480\n\t" \
  "s_nop 0\n\t" \
  "buffer_load_dwordx4 %[vl16], %[rc], %[sC] offen lds\n\t" \
  "s_add_u32 m0, %[lu], 0\n\t" \
  "s_add_u32 s44, %[sMod], 0\n\t" \
  "buffer_load_dwordx4 %[vm], %[rm], s44 offen lds\n\t" \
  "s_add_u32 m0, %[lu], 1024\n\t" \
  "s_add_u32 s44, %[sMod], 61440\n\t" \
  "buffer_load_dwordx4 %[vm], %[rm], s44 offen lds\n\t" \
  "s_add_u32 m0, %[lu], 2048\n\t" \
  "s_add_u32 s44, %[sMod], 122880\n\t" \
  "buffer_load_dwordx4 %[vm], %[rm], s44 offen lds\n\t" \
  "s_add_u32 m0, %[lu], 3072\n\t" \
  "s_add_u32 s44, %[sMod], 184320\n\t" \
  "buffer_load_dwordx4 %[vm], %[rm], s44 offen lds\n\t" \
  "s_add_u32 m0, %[lu], 4096\n\t" \
  "s_add_u32 s44, %[sMod], 245760\n\t" \
  "buffer_load_dwordx4 %[vm], %[rm], s44 offen lds\n\t" \
  "s_add_u32 m0, %[lu], 5120\n\t" \
  "s_add_u32 s44, %[sMod], 307200\n\t" \
  "buffer_load_dwordx4 %[vm], %[rm], s44 offen lds\n\t" \
  "s_add_u32 m0, %[lu], 6144\n\t" \
  "s_add_u32 s44, %[sMod], 368640\n\t" \
  "buffer_load_dwordx4 %[vm], %[rm], s44 offen lds\n\t" \
  "s_add_u32 m0, %[lu], 7168\n\t" \
  "s_add_u32 s44, %[sMod], 430080\n\t" \
  "buffer_load_dwordx4 %[vm], %[rm], s44 offen lds\n\t" \
  "s_add_u32 m0, %[lu], 8192\n\t" \
  "s_add_u32 s44, %[sMod], 491520\n\t" \
  "buffer_load_dwordx4 %[vm], %[rm], s44 offen lds\n\t" \
  "s_add_u32 m0, %[lu], 9216\n\t" \
  "s_add_u32 s44, %[sMod], 552960\n\t" \
  "buffer_load_dwordx4 %[vm], %[rm], s44 offen lds\n\t" \
  "s_add_u32 m0, %[lu], 10240\n\t" \
  "s_add_u32 s44, %[sMod], 614400\n\t" \
  "buffer_load_dwordx4 %[vm], %[rm], s44 offen lds\n\t" \
  "s_add_u32 m0, %[lu], 11264\n\t" \
  "s_add_u32 s44, %[sMod], 675840\n\t" \
  "buffer_load_dwordx4 %[vm], %[rm], s44 offen lds\n\t" \
  "s_add_u32 m0, %[lu], 12288\n\t" \
  "s_add_u32 s44, %[sMod], 737280\n\t" \
  "buffer_load_dwordx4 %[vm], %[rm], s44 offen lds\n\t" \
  "s_add_u32 m0, %[lu], 13312\n\t" \
  "s_add_u32 s44, %[sMod], 798720\n\t" \
  "buffer_load_dwordx4 %[vm], %[rm], s44 offen lds\n\t" \
  "s_add_u32 m0, %[lu], 14336\n\t" \
  "s_add_u32 s44, %[sMod], 860160\n\t" \
  "buffer_load_dwordx4 %[vm], %[rm], s44 offen lds\n\t" \
  "s_add_u32 m0, %[lu], 15360\n\t" \
  "s_add_u32 s44, %[sMod], 921600\n\t" \
  "buffer_load_dwordx4 %[vm], %[rm], s44 offen lds\n\t" \
  "s_add_u32 m0, %[lu], 16384\n\t" \
  "s_add_u32 s44, %[sMod], 983040\n\t" \
  "buffer_load_dwordx4 %[vm], %[rm], s44 offen lds\n\t" \
  "s_add_u32 m0, %[lu], 17408\n\t" \
  "s_add_u32 s44, %[sMod], 1044480\n\t" \
  "buffer_load_dwordx4 %[vm], %[rm], s44 offen lds\n\t" \
  "s_add_u32 m0, %[lu], 18432\n\t" \
  "s_add_u32 s44, %[sMod], 1105920\n\t" \
  "buffer_load_dwordx4 %[vm], %[rm], s44 offen lds\n\t" \
  "s_add_u32 m0, %[lu], 19456\n\t" \
  "s_add_u32 s44, %[sMod], 1167360\n\t" \
  "buffer_load_dwordx4 %[vm], %[rm], s44 offen lds\n\t" \
        :: [rm] "s"(mkrsrc(wmod, D_ * 3840 * 2)), [rc] "s"(mkrsrc(cIn, D_ * 2)), [lu] "s"(lu_), [sC] "s"(U_KQ(u_) * 320u), [sMod] "s"(U_SRC(u_)), \
           [vl16] "v"(lane * 16), [vm] "v"(vm) : "memory", "scc", "s44")
#define GEMV_UNIT(u_, lu_, younger_) asm volatile( \
  "s_nop 4\n\t" \
  "v_lshlrev_b32 v176, 1, %[vl]\n\t" \
  "v_add_u32 v176, %[lu], v176\n\t" \
  "v_mov_b32 v96, 0\n\t" \
  "v_mov_b32 v97, 0\n\t" \
  "v_mov_b32 v98, 0\n\t" \
  "s_waitcnt vmcnt(%c[wa])\n\t" \
  "ds_read_u16_d16_hi v96, v176 offset:20480\n\t" \
  "ds_read_u16_d16_hi v97, v176 offset:20608\n\t" \
  "ds_read_u16_d16_hi v98, v176 offset:20736\n\t" \
  "s_waitcnt lgkmcnt(2)\n\t" \
  "v_mul_f32 v177, 0xbfb8aa3b, v96\n\t" \
  "s_waitcnt lgkmcnt(1)\n\t" \
  "v_mul_f32 v178, 0xbfb8aa3b, v97\n\t" \
  "s_waitcnt lgkmcnt(0)\n\t" \
  "v_mul_f32 v179, 0xbfb8aa3b, v98\n\t" \
  "v_exp_f32 v177, v177\n\t" \
  "v_exp_f32 v178, v178\n\t" \
  "v_exp_f32 v179, v179\n\t" \
  "v_add_f32 v177, 1.0, v177\n\t" \
  "v_add_f32 v178, 1.0, v178\n\t" \
  "v_add_f32 v179, 1.0, v179\n\t" \
  "v_rcp_f32 v177, v177\n\t" \
  "v_rcp_f32 v178, v178\n\t" \
  "v_rcp_f32 v179, v179\n\t" \
  "v_mul_f32 v177, v177, v96\n\t" \
  "v_mul_f32 v178, v178, v97\n\t" \
  "v_mul_f32 v179, v179, v98\n\t" \
  "v_cvt_pk_bf16_f32 v177, v177, v177\n\t" \
  "v_cvt_pk_bf16_f32 v178, v178, v178\n\t" \
  "v_cvt_pk_bf16_f32 v179, v179, v179\n\t" \
  "ds_write_b16 v176, v177 offset:21504\n\t" \
  "ds_write_b16 v176, v178 offset:21632\n\t" \
  "ds_write_b16 v176, v179 offset:21760\n\t" \
  "v_lshrrev_b32 v180, 4, %[vl]\n\t" \
  "v_lshlrev_b32 v180, 3, v180\n\t" \
  "v_add_u32 v180, %[lu], v180\n\t" \
  "ds_read_b64 v[100:101], v180 offset:21504\n\t" \
  "ds_read_b64 v[102:103], v180 offset:21536\n\t" \
  "ds_read_b64 v[104:105], v180 offset:21568\n\t" \
  "ds_read_b64 v[106:107], v180 offset:21600\n\t" \
  "ds_read_b64 v[108:109], v180 offset:21632\n\t" \
  "ds_read_b64 v[110:111], v180 offset:21664\n\t" \
  "ds_read_b64 v[112:113], v180 offset:21696\n\t" \
  "ds_read_b64 v[114:115], v180 offset:21728\n\t" \
  "ds_read_b64 v[116:117], v180 offset:21760\n\t" \
  "ds_read_b64 v[118:119], v180 offset:21792\n\t" \
  "v_xor_b32 v181, 32, %[vtm]\n\t" \
  "v_xor_b32 v182, 64, %[vtm]\n\t" \
  "v_xor_b32 v183, 96, %[vtm]\n\t" \
  "s_waitcnt vmcnt(%c[wb])\n\t" \
  "ds_read_b64_tr_b16 v[120:121], %[vtm] offset:0\n\t" \
  "ds_read_b64_tr_b16 v[122:123], %[vtm] offset:2048\n\t" \
  "ds_read_b64_tr_b16 v[124:125], v181 offset:0\n\t" \
  "ds_read_b64_tr_b16 v[126:127], v181 offset:2048\n\t" \
  "ds_read_b64_tr_b16 v[128:129], v182 offset:0\n\t" \
  "ds_read_b64_tr_b16 v[130:131], v182 offset:2048\n\t" \
  "ds_read_b64_tr_b16 v[132:133], v183 offset:0\n\t" \
  "ds_read_b64_tr_b16 v[134:135], v183 offset:2048\n\t" \
  "ds_read_b64_tr_b16 v[136:137], %[vtm] offset:4096\n\t" \
  "ds_read_b64_tr_b16 v[138:139], %[vtm] offset:6144\n\t" \
  "ds_read_b64_tr_b16 v[140:141], v181 offset:4096\n\t" \
  "ds_read_b64_tr_b16 v[142:143], v181 offset:6144\n\t" \
  "ds_read_b64_tr_b16 v[144:145], v182 offset:4096\n\t" \
  "ds_read_b64_tr_b16 v[146:147], v182 offset:6144\n\t" \
  "ds_read_b64_tr_b16 v[148:149], v183 offset:4096\n\t" \
  "ds_read_b64_tr_b16 v[150:151], v183 offset:6144\n\t" \
  "s_waitcnt lgkmcnt(14)\n\t" \
  "v_mfma_f32_16x16x32_bf16 v[152:155], v[100:103], v[120:123], 0\n\t" \
  "s_waitcnt lgkmcnt(12)\n\t" \
  "v_mfma_f32_16x16x32_bf16 v[156:159], v[100:103], v[124:127], 0\n\t" \
  "s_waitcnt lgkmcnt(10)\n\t" \
  "v_mfma_f32_16x16x32_bf16 v[160:163], v[100:103], v[128:131], 0\n\t" \
  "s_waitcnt lgkmcnt(8)\n\t" \
  "v_mfma_f32_16x16x32_bf16 v[164:167], v[100:103], v[132:135], 0\n\t" \
  "ds_read_b64_tr_b16 v[120:121], %[vtm] offset:8192\n\t" \
  "ds_read_b64_tr_b16 v[122:123], %[vtm] offset:10240\n\t" \
  "ds_read_b64_tr_b16 v[124:125], v181 offset:8192\n\t" \
  "ds_read_b64_tr_b16 v[126:127], v181 offset:10240\n\t" \
  "ds_read_b64_tr_b16 v[128:129], v182 offset:8192\n\t" \
  "ds_read_b64_tr_b16 v[130:131], v182 offset:10240\n\t" \
  "ds_read_b64_tr_b16 v[132:133], v183 offset:8192\n\t" \
  "ds_read_b64_tr_b16 v[134:135], v183 offset:10240\n\t" \
  "s_waitcnt lgkmcnt(14)\n\t" \
  "v_mfma_f32_16x16x32_bf16 v[152:155], v[104:107], v[136:139], v[152:155]\n\t" \
  "s_waitcnt lgkmcnt(12)\n\t" \
  "v_mfma_f32_16x16x32_bf16 v[156:159], v[104:107], v[140:143], v[156:159]\n\t" \
  "s_waitcnt lgkmcnt(10)\n\t" \
  "v_mfma_f32_16x16x32_bf16 v[160:163], v[104:107], v[144:147], v[160:163]\n\t" \
  "s_waitcnt lgkmcnt(8)\n\t" \
  "v_mfma_f32_16x16x32_bf16 v[164:167], v[104:107], v[148:151], v[164:167]\n\t" \
  "ds_read_b64_tr_b16 v[136:137], %[vtm] offset:12288\n\t" \
  "ds_read_b64_tr_b16 v[138:139], %[vtm] offset:14336\n\t" \
  "ds_read_b64_tr_b16 v[140:141], v181 offset:12288\n\t" \
  "ds_read_b64_tr_b16 v[142:143], v181 offset:14336\n\t" \
  "ds_read_b64_tr_b16 v[144:145], v182 offset:12288\n\t" \
  "ds_read_b64_tr_b16 v[146:147], v182 offset:14336\n\t" \
  "ds_read_b64_tr_b16 v[148:149], v183 offset:12288\n\t" \
  "ds_read_b64_tr_b16 v[150:151], v183 offset:14336\n\t" \
  "s_waitcnt lgkmcnt(14)\n\t" \
  "v_mfma_f32_16x16x32_bf16 v[152:155], v[108:111], v[120:123], v[152:155]\n\t" \
  "s_waitcnt lgkmcnt(12)\n\t" \
  "v_mfma_f32_16x16x32_bf16 v[156:159], v[108:111], v[124:127], v[156:159]\n\t" \
  "s_waitcnt lgkmcnt(10)\n\t" \
  "v_mfma_f32_16x16x32_bf16 v[160:163], v[108:111], v[128:131], v[160:163]\n\t" \
  "s_waitcnt lgkmcnt(8)\n\t" \
  "v_mfma_f32_16x16x32_bf16 v[164:167], v[108:111], v[132:135], v[164:167]\n\t" \
  "ds_read_b64_tr_b16 v[120:121], %[vtm] offset:16384\n\t" \
  "ds_read_b64_tr_b16 v[122:123], %[vtm] offset:18432\n\t" \
  "ds_read_b64_tr_b16 v[124:125], v181 offset:16384\n\t" \
  "ds_read_b64_tr_b16 v[126:127], v181 offset:18432\n\t" \
  "ds_read_b64_tr_b16 v[128:129], v182 offset:16384\n\t" \
  "ds_read_b64_tr_b16 v[130:131], v182 offset:18432\n\t" \
  "ds_read_b64_tr_b16 v[132:133], v183 offset:16384\n\t" \
  "ds_read_b64_tr_b16 v[134:135], v183 offset:18432\n\t" \
  "s_waitcnt lgkmcnt(14)\n\t" \
  "v_mfma_f32_16x16x32_bf16 v[152:155], v[112:115], v[136:139], v[152:155]\n\t" \
  "s_waitcnt lgkmcnt(12)\n\t" \
  "v_mfma_f32_16x16x32_bf16 v[156:159], v[112:115], v[140:143], v[156:159]\n\t" \
  "s_waitcnt lgkmcnt(10)\n\t" \
  "v_mfma_f32_16x16x32_bf16 v[160:163], v[112:115], v[144:147], v[160:163]\n\t" \
  "s_waitcnt lgkmcnt(8)\n\t" \
  "v_mfma_f32_16x16x32_bf16 v[164:167], v[112:115], v[148:151], v[164:167]\n\t" \
  "s_waitcnt lgkmcnt(6)\n\t" \
  "v_mfma_f32_16x16x32_bf16 v[152:155], v[116:119], v[120:123], v[152:155]\n\t" \
  "s_waitcnt lgkmcnt(4)\n\t" \
  "v_mfma_f32_16x16x32_bf16 v[156:159], v[116:119], v[124:127], v[156:159]\n\t" \
  "s_waitcnt lgkmcnt(2)\n\t" \
  "v_mfma_f32_16x16x32_bf16 v[160:163], v[116:119], v[128:131], v[160:163]\n\t" \
  "s_waitcnt lgkmcnt(0)\n\t" \
  "v_mfma_f32_16x16x32_bf16 v[164:167], v[116:119], v[132:135], v[164:167]\n\t" \
  "v_lshlrev_b32 v176, 2, %[vl]\n\t" \
  "s_nop 2\n\t" \
  "v_mov_b32_dpp v152, v156 quad_perm:[0,1,2,3] row_mask:0x2 bank_mask:0xf\n\t" \
  "s_nop 1\n\t" \
  "v_mov_b32_dpp v152, v160 quad_perm:[0,1,2,3] row_mask:0x4 bank_mask:0xf\n\t" \
  "s_nop 1\n\t" \
  "v_mov_b32_dpp v152, v164 quad_perm:[0,1,2,3] row_mask:0x8 bank_mask:0xf\n\t" \
  "buffer_store_dword v152, v176, %[srd], %[sMo] offen sc1\n\t" \
  "s_nop 1\n\t" \
        :: [srd] "s"(srd), [lu] "s"(lu_), [wa] "i"((younger_) + 20), [wb] "i"(younger_), [sMo] "s"(OFF_MODP + U_KQ(u_) * 15360u + U_CG(u_) * 256u), [vl] "v"(lane), \
           [vtm] "v"((lu_) + (lane >> 2) * 128 + ((lane >> 3) & 3) * 32 + (lane & 3) * 8) : "memory", "vcc", "scc", \
"v96", "v97", "v98", "v100", "v101", "v102", "v103", "v104", "v105", "v106", "v107", "v108", "v109", "v110", "v111", "v112", "v113", "v114", "v115", "v116", "v117", "v118", "v119", "v120", "v121", "v122", "v123", "v124", "v125", "v126", "v127", "v128", "v129", "v130", "v131", "v132", "v133", "v134", "v135", "v136", "v137", "v138", "v139", "v140", "v141", "v142", "v143", "v144", "v145", "v146", "v147", "v148", "v149", "v150", "v151", "v152", "v153", "v154", "v155", "v156", "v157", "v158", "v159", "v160", "v161", "v162", "v163", "v164", "v165", "v166", "v167", "v176", "v177", "v178", "v179", "v180", "v181", "v182", "v183" \
      )
  auto stash = [&]() {
    const unsigned vpub = tid < 8 ? (role == 0 ? OFF_FX + cb * 128 : role == 1 ? OFF_FU + w1 * 128 : OFF_FW2 + w2 * 128) + tid * 16 : OOB;
    const unsigned vpw = lane < 4 ? OFF_FW1 + (j * 4 + lane) * 128 : lane < 8 ? OFF_FW2 + ((4 * w + (lane & 3)) * 2 + (ng >> 2)) * 128
                                                                              : OFF_FU + ((lane < 12 ? j * 4 : 0) + (lane & 3)) * 128;
    const unsigned vpx = lane < 40 ? OFF_FX + (32 * (lane >> 3) + 8 * w + (lane & 7)) * 128 : OFF_FW2 + min(lane - 40, 9u) * 128;
    const unsigned g = lane >> 4, q = (lane >> 2) & 3, pq = lane & 3;
    const unsigned row = 4 * g + q;
    const unsigned vtr = lb + row * 128 + ((row >> 1) & 3) * 32 + ((pq * 8) ^ ((row & 8) * 2));
    const unsigned vl16 = lane * 16;
    const unsigned vdma = OFF_X2P + p * 81920u + w * 4096u + lane * 16;
    const unsigned vh2st = lane < 48 ? OFF_H2F + (4 * p * 192 + 12 * j + 3 * w) * 256u + lane * 16 : OOB;
    const unsigned tk = 4 * j + w;
    const unsigned vstc = lb + lane * 128 + (((tk >> 4) ^ ((lane >> 1) & 3)) * 32) + (((tk & 15) * 2) ^ ((lane & 8) * 2));
    const unsigned vstw = lane == 63 ? tk * 64 : OOB;
    const unsigned vflw = lane < 8 ? OFF_FL + p * 32768u + (j * 4 + w) * 128u + lane * 16 : OOB;
    const unsigned vep = b == 0 && tid == 0 ? OFF_EP : OOB;
    const unsigned rs_ = 2 * (w & 1) + sub, rb = lb + (w >> 1) * 81920 + 80 * ng * 128;
    const unsigned r0 = 8 * g + q, r1 = 8 * g + 4 + q, r2 = 64 + 4 * g + q;
    const unsigned vres0 = rb + r0 * 128 + ((rs_ ^ ((r0 >> 1) & 3)) * 32) + ((pq * 8) ^ ((r0 & 8) * 2));
    const unsigned vres1 = rb + r1 * 128 + ((rs_ ^ ((r1 >> 1) & 3)) * 32) + ((pq * 8) ^ ((r1 & 8) * 2));
    const unsigned vres2 = rb + r2 * 128 + ((rs_ ^ ((r2 >> 1) & 3)) * 32) + ((pq * 8) ^ ((r2 & 8) * 2));
    const unsigned vwr = lb + w * 20480 + g * 256 + ((lane + 4 * g) & 15) * 16;
    const unsigned tq = lane < 32 ? q : (lane >> 1) & 3, tl = 4 * w + tq, g0 = lane < 32 ? pq : 2 * (lane & 1), g1 = lane < 32 ? pq : g0 + 1;
    const unsigned vrd0 = lb + (lane < 32 ? g * 2048 : 4096) + g0 * 256 + ((tl + 4 * g0) & 15) * 16;
    const unsigned vrd1 = lb + (lane < 32 ? g * 2048 + 1024 : 4096) + g1 * 256 + ((tl + 4 * g1) & 15) * 16;
    const unsigned vh1w = lane < 20 ? OFF_X2P + (16 + p) * 81920u + w * 4096u + (lane >> 2) * 16384 + ((lane & 3) * 8 + ng) * 128 : OOB;
    const unsigned vgb = OFF_GB - OFF_RT + (lane < 32 ? 32 * g + 8 * pq : 64 + 8 * (lane & 1)) * 4;
    const unsigned vout = lane < 40 ? ((64 * p + 16 * sub + tl) * D_ + 80 * ng + (lane < 32 ? 32 * g + 8 * pq : 64 + 8 * (lane & 1))) * 2 : OOB;
    *(u32x4*)(smem + L_STASH + 0 + tid * 16) = u32x4{vpub, vl16, vpw, vpx};
    *(u32x4*)(smem + L_STASH + 4096 + tid * 16) = u32x4{vdma, vtr, vstc, vstw};
    *(u32x4*)(smem + L_STASH + 8192 + tid * 16) = u32x4{vh2st, vflw, vres0, vres1};
    *(u32x4*)(smem + L_STASH + 12288 + tid * 16) = u32x4{vres2, vwr, vrd0, vrd1};
    *(u32x4*)(smem + L_STASH + 16384 + tid * 16) = u32x4{vout, vgb, vep, vh1w};
    FENCE();
  };
  unsigned c_vf2;
  unsigned c_vf1;
  unsigned c_vf3;
  unsigned c_vfw;
  unsigned c_va;
  unsigned c_vb0;
  unsigned c_vb1;
  unsigned c_vb2;
  unsigned c_vb3;
  unsigned c_vx;
  unsigned c_vs;
  unsigned c_vs2;
  unsigned c_vo;
  u32x4 c_vd;
  unsigned c_vl16;
  unsigned c_vkv;
  unsigned c_vmod;
  u64 tag;
  if (role == 0) {
    const rsrc_t rk = mkrsrc(kIn, S_ * D_ * 2), rv = mkrsrc(vIn, S_ * D_ * 2), rf = mkrsrc(ssmk, S_ * D_ * 2);
    const unsigned tb = 4 * (cb & 31) + w, d0 = 128 * (cb >> 5) + 2 * lane;
    u32x4 fa[4];
    const unsigned lkv = L_KV + 2048 + w * 9216;
    asm volatile(
  "s_mov_b32 m0, %[lk]\n\t"
  "s_nop 3\n\t"
  "buffer_load_dwordx4 %[vo], %[rk], %[sh] offen lds\n\t"
  "s_add_u32 m0, %[lk], 1024\n\t"
  "s_add_u32 s44, %[so], 0\n\t"
  "buffer_load_dwordx4 %[vo], %[rk], s44 offen lds\n\t"
  "s_add_u32 m0, %[lk], 5120\n\t"
  "s_nop 0\n\t"
  "buffer_load_dwordx4 %[vo], %[rv], s44 offen lds\n\t"
  "s_add_u32 m0, %[lk], 2048\n\t"
  "s_add_u32 s44, %[so], 5120\n\t"
  "buffer_load_dwordx4 %[vo], %[rk], s44 offen lds\n\t"
  "s_add_u32 m0, %[lk], 6144\n\t"
  "s_nop 0\n\t"
  "buffer_load_dwordx4 %[vo], %[rv], s44 offen lds\n\t"
  "s_add_u32 m0, %[lk], 3072\n\t"
  "s_add_u32 s44, %[so], 10240\n\t"
  "buffer_load_dwordx4 %[vo], %[rk], s44 offen lds\n\t"
  "s_add_u32 m0, %[lk], 7168\n\t"
  "s_nop 0\n\t"
  "buffer_load_dwordx4 %[vo], %[rv], s44 offen lds\n\t"
  "s_add_u32 m0, %[lk], 4096\n\t"
  "s_add_u32 s44, %[so], 15360\n\t"
  "buffer_load_dwordx4 %[vo], %[rk], s44 offen lds\n\t"
  "s_add_u32 m0, %[lk], 8192\n\t"
  "s_nop 0\n\t"
  "buffer_load_dwordx4 %[vo], %[rv], s44 offen lds\n\t"
      :: [rk] "s"(rk), [rv] "s"(rv), [lk] "s"(lb + lkv), [so] "s"(tb * 20480u + (cb >> 5) * 256u), [sh] "s"(tb ? tb * 20480u + (cb >> 5) * 256u - 5120u : OOB),
         [vo] "v"((lane >> 4) * 1280 + (lane & 15) * 16) : "memory", "scc", "s44");
    const u32x4 taps = *(const u32x4*)(shk + d0 * 4);
    dval = ssmD[8 * (cb >> 1) + 2 * w + (cb & 1)];
    FENCE();
    __builtin_amdgcn_raw_buffer_store_b128(u32x4{0, 0, 0, 0}, srd, tid < 64 && cb < 128 ? OFF_ST2 + cb * 1024 + tid * 16 : OOB, 0, SC1);
    FENCE();
#pragma unroll
    for (int n = 0; n < 4; ++n) fa[n] = __builtin_amdgcn_raw_buffer_load_b128(rf, lane * 16 + n * 1024, (8 * (cb >> 1) + 2 * w + (cb & 1)) * 4096u, NT);
    u32x4 xr[5];
    const bool isx = cb < 128;
    const rsrc_t rx = mkrsrc(isx ? x : wmod, isx ? S_ * D_ * 2 : D_ * 3840 * 2);
#pragma unroll
    for (int n = 0; n < 5; ++n)
      xr[n] = __builtin_amdgcn_raw_buffer_load_b128(rx, isx ? (lane >> 3) * 1280 + (lane & 7) * 16 : vm,
                                                    isx ? cb * 20480u + (w & 1) * 10240u + (w >> 1) * 128u + n * 256u : U_SRC(cb) + (4 * n + w) * 61440u, NT);
    const u32x4 cr = __builtin_amdgcn_raw_buffer_load_b128(mkrsrc(cIn, isx ? 0 : D_ * 2), lane * 16, U_KQ(cb) * 320u, 0);
    FENCE();
    STAMP(6);
    tag = load_tag(ws);
    if (w < 2) *(u32x4*)(smem + L_KV + tid * 16) = zero4;
    if (tid == 128) *(u32x4*)(smem + L_CNT) = zero4;
    {
      const unsigned d = 8 * (cb >> 1) + 2 * w + (cb & 1);
      const unsigned r = lane & 15, g = lane >> 4;
      const unsigned pp = 25 + r - 8 * g;
      const unsigned va = lb + L_FC + w * FC_CH + (pp & 3) * FC_PITCH + (pp & ~3u) * 2;
      unsigned vb[4];
  #pragma unroll
      for (int k = 0; k < 4; ++k) { const unsigned G = 14 - 4 * k + 2 * r + g; vb[k] = lb + L_KV + G * 64 + ((w ^ (G >> 2)) & 3) * 16; }
      const unsigned gx = (2 * r + (g >> 1)) * 64 + ((w ^ (r >> 1)) & 3) * 16;
      const unsigned vx = lb + L_X + gx + (g & 1) * 8, vs = lb + L_ST + r * 128 + ((2 * g) ^ stkey(r)) * 16;
      const unsigned to = 256 * (g & 1) + 16 * r + 8 * (g >> 1);
      const unsigned vo = (to >> 6) * 81920u + (((to & 63) * 2) ^ (((d >> 1) & 3) * 32 + (d & 8) * 2));
      const unsigned wh = (lane >> 2) & 3, mcol = (wh < 3 ? wh : 4) * 640 + d;
      const unsigned jd = 8 * g - 9 - r, dv = dval << (16 * (jd & 1));
      const u32x4 vd = {jd >> 1 == 0 ? dv : 0u, jd >> 1 == 1 ? dv : 0u, jd >> 1 == 2 ? dv : 0u, jd >> 1 == 3 ? dv : 0u};
      c_vf2 = lane < 32 ? OFF_FA3 + (32 * w + lane) * 128 : OFF_FA2 + ((lane & 3) * 60 + (mcol >> 6)) * 128;
      c_vf1 = OFF_FA1 + ((cb & ~31u) + 8 * w + (lane & 7)) * 128;
      c_vf3 = lane < 8 ? (cb < 128 ? OFF_FA3 + cb * 128 : U_FLAG(cb)) + lane * 16 : OOB;
      c_vfw = OFF_FW1 + (j * 4 + (lane & 3)) * 128;
      c_va = va;
      c_vb0 = vb[0];
      c_vb1 = vb[1];
      c_vb2 = vb[2];
      c_vb3 = vb[3];
      c_vx = vx;
      c_vs = vs;
      c_vs2 = vs ^ 16;
      c_vo = vo;
      c_vd = vd;
      c_vl16 = lane * 16;
      c_vkv = (lane >> 3) * 20480 + (lane & 7) * 16;
      c_vmod = OFF_MODP + ((lane & 3) * 3840 + mcol) * 4;
      asm volatile("" : "+v"(c_vf2), "+v"(c_vf1), "+v"(c_vf3), "+v"(c_vfw), "+v"(c_va), "+v"(c_vb0), "+v"(c_vb1), "+v"(c_vb2), "+v"(c_vb3), "+v"(c_vx), "+v"(c_vs), "+v"(c_vs2), "+v"(c_vo), "+v"(c_vd), "+v"(c_vl16), "+v"(c_vkv), "+v"(c_vmod));
    }
    FENCE();
    const f32x2 w0 = {bflo(taps[0]), bflo(taps[2])}, w1 = {bfhi(taps[0]), bfhi(taps[2])}, w2 = {bflo(taps[1]), bflo(taps[3])}, w3 = {bfhi(taps[1]), bfhi(taps[3])};
    asm volatile("s_waitcnt vmcnt(11)" ::: "memory");
    STAMP(7);
    u32 kk[20], vv[16];
    {
      unsigned la = lb + lkv + lane * 4;
      asm volatile("" : "+v"(la));
      const LAS u32* lp = (const LAS u32*)la;
#pragma unroll
      for (int i = 0; i < 12; ++i) kk[i] = lp[(i + 1) * 64];
#pragma unroll
      for (int i = 0; i < 8; ++i) vv[i] = lp[(20 + i) * 64];
      __builtin_amdgcn_sched_barrier(0);
#pragma unroll
      for (int i = 12; i < 19; ++i) kk[i] = lp[(i + 1) * 64];
#pragma unroll
      for (int i = 8; i < 16; ++i) vv[i] = lp[(20 + i) * 64];
      __builtin_amdgcn_sched_barrier(0);
    }
    f32x2 kf[19];
#pragma unroll
    for (int n = 0; n < 2; ++n) {
      f32x2 kv[8];
#pragma unroll
      for (int i = n ? 11 : 0; i < (n ? 19 : 11); ++i) kf[i] = f32x2{bflo(kk[i]), bfhi(kk[i])};
#pragma unroll
      for (int i = 0; i < 8; ++i) {
        f32x2 ks = kf[8 * n + i] * w3;
        ks = __builtin_elementwise_fma(kf[8 * n + i + 1], w2, ks);
        ks = __builtin_elementwise_fma(kf[8 * n + i + 2], w1, ks);
        ks = __builtin_elementwise_fma(kf[8 * n + i + 3], w0, ks);
        kv[i] = ks * f32x2{bflo(vv[8 * n + i]), bfhi(vv[8 * n + i])};
      }
      u32x4 o0, o1;
#pragma unroll
      for (int t = 0; t < 4; ++t) {
        o0[t] = pack_bf16(kv[7 - 2 * t][0], kv[6 - 2 * t][0]);
        o1[t] = pack_bf16(kv[7 - 2 * t][1], kv[6 - 2 * t][1]);
      }
      const unsigned off = OFF_KVT + (tb * 160 + 2 * (d0 >> 3)) * 128 + n * 64 + ((lane ^ (tb >> 1)) & 3) * 16;
      __builtin_amdgcn_raw_buffer_store_b128(o0, srd, off, 0, SC1);
      __builtin_amdgcn_raw_buffer_store_b128(o1, srd, off + 128, 0, SC1);
      FENCE();
    }
    STAMP(1);
    char* fcw = smem + L_FC + w * FC_CH;
    auto put = [&](unsigned u, u32x4 a, u32x2 h) {
      const u32 W[6] = {a[0], a[1], a[2], a[3], h[0], h[1]};
      u32 O[5];
#pragma unroll
      for (int i = 0; i < 5; ++i) O[i] = __builtin_amdgcn_alignbit(W[i + 1], W[i], 16);
      *(u32x4*)(fcw + 0 * FC_PITCH + u * 16) = a;
      *(u32x4*)(fcw + 1 * FC_PITCH + u * 16) = u32x4{O[0], O[1], O[2], O[3]};
      *(u32x4*)(fcw + 2 * FC_PITCH + u * 16) = u32x4{W[1], W[2], W[3], W[4]};
      *(u32x4*)(fcw + 3 * FC_PITCH + u * 16) = u32x4{O[1], O[2], O[3], O[4]};
    };
#pragma unroll
    for (int n = 0; n < 4; ++n) {
      u32x2 h;
#pragma unroll
      for (int e = 0; e < 2; ++e) h[e] = __builtin_amdgcn_update_dpp(n < 3 ? rfl(fa[n < 3 ? n + 1 : n][e]) : 0u, fa[n][e], 0x130, 0xf, 0xf, false);
      put(lane + 64 * n + 2, fa[n], h);
    }
    if (lane < 2) put(1 - lane, u32x4{0, 0, 0, 0}, lane == 0 ? u32x2{fa[0][0], fa[0][1]} : u32x2{0, 0});
    FENCE();
#pragma unroll
    for (int n = 0; n < 5; ++n) *(u32x4*)(smem + L_XR + n * 4096 + w * 1024 + lane * 16) = xr[n];
    if (w == 1) *(u32x4*)(smem + L_XR + 20480 + lane * 16) = cr;
    FENCE();
    asm volatile("s_waitcnt vmcnt(0) lgkmcnt(0)\n\ts_barrier" ::: "memory");
    __builtin_amdgcn_raw_buffer_store_b128(u32x4{(u32)tag, (u32)(tag >> 32), (u32)tag, (u32)(tag >> 32)}, srd, tid < 8 ? OFF_FA1 + cb * 128 + tid * 16 : OOB, 0, SC1);
    FENCE();
    STAMP(2);
    if (isx) {
      const unsigned g = lane >> 4, q = (lane >> 2) & 3, ch = 16 * (g >> 1) + (lane & 15);
      asm volatile(
  "s_nop 4\n\t"
  "s_add_u32 s44, %[w], 1\n\t"
  "s_and_b32 s44, s44, 2\n\t"
  "s_cbranch_scc0 Lxs%=\n\t"
  "ds_read_b64_tr_b16 v[96:97], %[vtx] offset:0\n\t"
  "ds_read_b64_tr_b16 v[98:99], %[vtx] offset:512\n\t"
  "ds_read_b64_tr_b16 v[100:101], %[vtx] offset:64\n\t"
  "ds_read_b64_tr_b16 v[102:103], %[vtx] offset:576\n\t"
  "ds_read_b64_tr_b16 v[104:105], %[vtx] offset:2048\n\t"
  "ds_read_b64_tr_b16 v[106:107], %[vtx] offset:2560\n\t"
  "ds_read_b64_tr_b16 v[108:109], %[vtx] offset:2112\n\t"
  "ds_read_b64_tr_b16 v[110:111], %[vtx] offset:2624\n\t"
  "ds_read_b64_tr_b16 v[112:113], %[vtx] offset:4096\n\t"
  "ds_read_b64_tr_b16 v[114:115], %[vtx] offset:4608\n\t"
  "ds_read_b64_tr_b16 v[116:117], %[vtx] offset:4160\n\t"
  "ds_read_b64_tr_b16 v[118:119], %[vtx] offset:4672\n\t"
  "ds_read_b64_tr_b16 v[120:121], %[vtx] offset:6144\n\t"
  "ds_read_b64_tr_b16 v[122:123], %[vtx] offset:6656\n\t"
  "ds_read_b64_tr_b16 v[124:125], %[vtx] offset:6208\n\t"
  "ds_read_b64_tr_b16 v[126:127], %[vtx] offset:6720\n\t"
  "s_add_u32 s44, %[sXo], 0\n\t"
  "s_waitcnt lgkmcnt(14)\n\t"
  "buffer_store_dwordx4 v[96:99], %[vxo], %[srd], s44 offen offset:0 sc1\n\t"
  "ds_read_b64_tr_b16 v[128:129], %[vtx] offset:8192\n\t"
  "ds_read_b64_tr_b16 v[130:131], %[vtx] offset:8704\n\t"
  "s_waitcnt lgkmcnt(14)\n\t"
  "buffer_store_dwordx4 v[100:103], %[vxo], %[srd], s44 offen offset:1024 sc1\n\t"
  "ds_read_b64_tr_b16 v[132:133], %[vtx] offset:8256\n\t"
  "ds_read_b64_tr_b16 v[134:135], %[vtx] offset:8768\n\t"
  "s_waitcnt lgkmcnt(14)\n\t"
  "buffer_store_dwordx4 v[104:107], %[vxo], %[srd], s44 offen offset:2048 sc1\n\t"
  "s_waitcnt lgkmcnt(12)\n\t"
  "buffer_store_dwordx4 v[108:111], %[vxo], %[srd], s44 offen offset:3072 sc1\n\t"
  "s_add_u32 s44, %[sXo], 4096\n\t"
  "s_waitcnt lgkmcnt(10)\n\t"
  "buffer_store_dwordx4 v[112:115], %[vxo], %[srd], s44 offen offset:0 sc1\n\t"
  "s_waitcnt lgkmcnt(8)\n\t"
  "buffer_store_dwordx4 v[116:119], %[vxo], %[srd], s44 offen offset:1024 sc1\n\t"
  "s_waitcnt lgkmcnt(6)\n\t"
  "buffer_store_dwordx4 v[120:123], %[vxo], %[srd], s44 offen offset:2048 sc1\n\t"
  "s_waitcnt lgkmcnt(4)\n\t"
  "buffer_store_dwordx4 v[124:127], %[vxo], %[srd], s44 offen offset:3072 sc1\n\t"
  "s_add_u32 s44, %[sXo], 8192\n\t"
  "s_waitcnt lgkmcnt(2)\n\t"
  "buffer_store_dwordx4 v[128:131], %[vxo], %[srd], s44 offen offset:0 sc1\n\t"
  "s_waitcnt lgkmcnt(0)\n\t"
  "buffer_store_dwordx4 v[132:135], %[vxo], %[srd], s44 offen offset:1024 sc1\n\t"
  "s_branch Lxe%=\n\t"
  "Lxs%=:\n\t"
  "ds_read_b128 v[96:99], %[vsx] offset:0\n\t"
  "ds_read_b128 v[100:103], %[vsx] offset:2048\n\t"
  "ds_read_b128 v[104:107], %[vsx] offset:4096\n\t"
  "ds_read_b128 v[108:111], %[vsx] offset:6144\n\t"
  "ds_read_b128 v[112:115], %[vsx] offset:8192\n\t"
  "ds_read_b128 v[116:119], %[vsx] offset:10240\n\t"
  "ds_read_b128 v[120:123], %[vsx] offset:12288\n\t"
  "ds_read_b128 v[124:127], %[vsx] offset:14336\n\t"
  "ds_read_b128 v[128:131], %[vsx] offset:16384\n\t"
  "ds_read_b128 v[132:135], %[vsx] offset:18432\n\t"
  "v_mov_b32 v184, 0\n\t"
  "v_mov_b32 v185, 0\n\t"
  "v_mov_b32 v186, 0x3f803f80\n\t"
  "s_waitcnt lgkmcnt(9)\n\t"
  "v_dot2c_f32_bf16 v184, v96, v186\n\t"
  "v_dot2c_f32_bf16 v185, v96, v96\n\t"
  "v_dot2c_f32_bf16 v184, v97, v186\n\t"
  "v_dot2c_f32_bf16 v185, v97, v97\n\t"
  "v_dot2c_f32_bf16 v184, v98, v186\n\t"
  "v_dot2c_f32_bf16 v185, v98, v98\n\t"
  "v_dot2c_f32_bf16 v184, v99, v186\n\t"
  "v_dot2c_f32_bf16 v185, v99, v99\n\t"
  "s_waitcnt lgkmcnt(8)\n\t"
  "v_dot2c_f32_bf16 v184, v100, v186\n\t"
  "v_dot2c_f32_bf16 v185, v100, v100\n\t"
  "v_dot2c_f32_bf16 v184, v101, v186\n\t"
  "v_dot2c_f32_bf16 v185, v101, v101\n\t"
  "v_dot2c_f32_bf16 v184, v102, v186\n\t"
  "v_dot2c_f32_bf16 v185, v102, v102\n\t"
  "v_dot2c_f32_bf16 v184, v103, v186\n\t"
  "v_dot2c_f32_bf16 v185, v103, v103\n\t"
  "s_waitcnt lgkmcnt(7)\n\t"
  "v_dot2c_f32_bf16 v184, v104, v186\n\t"
  "v_dot2c_f32_bf16 v185, v104, v104\n\t"
  "v_dot2c_f32_bf16 v184, v105, v186\n\t"
  "v_dot2c_f32_bf16 v185, v105, v105\n\t"
  "v_dot2c_f32_bf16 v184, v106, v186\n\t"
  "v_dot2c_f32_bf16 v185, v106, v106\n\t"
  "v_dot2c_f32_bf16 v184, v107, v186\n\t"
  "v_dot2c_f32_bf16 v185, v107, v107\n\t"
  "s_waitcnt lgkmcnt(6)\n\t"
  "v_dot2c_f32_bf16 v184, v108, v186\n\t"
  "v_dot2c_f32_bf16 v185, v108, v108\n\t"
  "v_dot2c_f32_bf16 v184, v109, v186\n\t"
  "v_dot2c_f32_bf16 v185, v109, v109\n\t"
  "v_dot2c_f32_bf16 v184, v110, v186\n\t"
  "v_dot2c_f32_bf16 v185, v110, v110\n\t"
  "v_dot2c_f32_bf16 v184, v111, v186\n\t"
  "v_dot2c_f32_bf16 v185, v111, v111\n\t"
  "s_waitcnt lgkmcnt(5)\n\t"
  "v_dot2c_f32_bf16 v184, v112, v186\n\t"
  "v_dot2c_f32_bf16 v185, v112, v112\n\t"
  "v_dot2c_f32_bf16 v184, v113, v186\n\t"
  "v_dot2c_f32_bf16 v185, v113, v113\n\t"
  "v_dot2c_f32_bf16 v184, v114, v186\n\t"
  "v_dot2c_f32_bf16 v185, v114, v114\n\t"
  "v_dot2c_f32_bf16 v184, v115, v186\n\t"
  "v_dot2c_f32_bf16 v185, v115, v115\n\t"
  "s_waitcnt lgkmcnt(4)\n\t"
  "v_dot2c_f32_bf16 v184, v116, v186\n\t"
  "v_dot2c_f32_bf16 v185, v116, v116\n\t"
  "v_dot2c_f32_bf16 v184, v117, v186\n\t"
  "v_dot2c_f32_bf16 v185, v117, v117\n\t"
  "v_dot2c_f32_bf16 v184, v118, v186\n\t"
  "v_dot2c_f32_bf16 v185, v118, v118\n\t"
  "v_dot2c_f32_bf16 v184, v119, v186\n\t"
  "v_dot2c_f32_bf16 v185, v119, v119\n\t"
  "s_waitcnt lgkmcnt(3)\n\t"
  "v_dot2c_f32_bf16 v184, v120, v186\n\t"
  "v_dot2c_f32_bf16 v185, v120, v120\n\t"
  "v_dot2c_f32_bf16 v184, v121, v186\n\t"
  "v_dot2c_f32_bf16 v185, v121, v121\n\t"
  "v_dot2c_f32_bf16 v184, v122, v186\n\t"
  "v_dot2c_f32_bf16 v185, v122, v122\n\t"
  "v_dot2c_f32_bf16 v184, v123, v186\n\t"
  "v_dot2c_f32_bf16 v185, v123, v123\n\t"
  "s_waitcnt lgkmcnt(2)\n\t"
  "v_dot2c_f32_bf16 v184, v124, v186\n\t"
  "v_dot2c_f32_bf16 v185, v124, v124\n\t"
  "v_dot2c_f32_bf16 v184, v125, v186\n\t"
  "v_dot2c_f32_bf16 v185, v125, v125\n\t"
  "v_dot2c_f32_bf16 v184, v126, v186\n\t"
  "v_dot2c_f32_bf16 v185, v126, v126\n\t"
  "v_dot2c_f32_bf16 v184, v127, v186\n\t"
  "v_dot2c_f32_bf16 v185, v127, v127\n\t"
  "s_waitcnt lgkmcnt(1)\n\t"
  "v_dot2c_f32_bf16 v184, v128, v186\n\t"
  "v_dot2c_f32_bf16 v185, v128, v128\n\t"
  "v_dot2c_f32_bf16 v184, v129, v186\n\t"
  "v_dot2c_f32_bf16 v185, v129, v129\n\t"
  "v_dot2c_f32_bf16 v184, v130, v186\n\t"
  "v_dot2c_f32_bf16 v185, v130, v130\n\t"
  "v_dot2c_f32_bf16 v184, v131, v186\n\t"
  "v_dot2c_f32_bf16 v185, v131, v131\n\t"
  "s_waitcnt lgkmcnt(0)\n\t"
  "v_dot2c_f32_bf16 v184, v132, v186\n\t"
  "v_dot2c_f32_bf16 v185, v132, v132\n\t"
  "v_dot2c_f32_bf16 v184, v133, v186\n\t"
  "v_dot2c_f32_bf16 v185, v133, v133\n\t"
  "v_dot2c_f32_bf16 v184, v134, v186\n\t"
  "v_dot2c_f32_bf16 v185, v134, v134\n\t"
  "v_dot2c_f32_bf16 v184, v135, v186\n\t"
  "v_dot2c_f32_bf16 v185, v135, v135\n\t"
  "s_nop 1\n\t"
  "v_add_f32_dpp v184, v184, v184 quad_perm:[1,0,3,2] row_mask:0xf bank_mask:0xf\n\t"
  "v_add_f32_dpp v185, v185, v185 quad_perm:[1,0,3,2] row_mask:0xf bank_mask:0xf\n\t"
  "s_nop 0\n\t"
  "v_add_f32_dpp v184, v184, v184 quad_perm:[2,3,0,1] row_mask:0xf bank_mask:0xf\n\t"
  "v_add_f32_dpp v185, v185, v185 quad_perm:[2,3,0,1] row_mask:0xf bank_mask:0xf\n\t"
  "s_nop 0\n\t"
  "v_add_f32_dpp v184, v184, v184 row_half_mirror row_mask:0xf bank_mask:0xf\n\t"
  "v_add_f32_dpp v185, v185, v185 row_half_mirror row_mask:0xf bank_mask:0xf\n\t"
  "v_mul_f32 v184, 0xbacccccd, v184\n\t"
  "v_mul_f32 v185, 0x3acccccd, v185\n\t"
  "v_fma_f32 v185, -v184, v184, v185\n\t"
  "v_max_f32 v185, 0, v185\n\t"
  "v_cvt_pk_bf16_f32 v185, 0, v185\n\t"
  "v_cvt_pk_bf16_f32 v184, 0, v184\n\t"
  "v_add_f32 v185, 0x3727c5ac, v185\n\t"
  "v_cvt_pk_bf16_f32 v185, 0, v185\n\t"
  "v_sqrt_f32 v185, v185\n\t"
  "s_nop 0\n\t"
  "v_cvt_pk_bf16_f32 v185, 0, v185\n\t"
  "v_rcp_f32 v188, v185\n\t"
  "s_nop 0\n\t"
  "v_mul_f32 v189, v184, v188\n\t"
  "s_nop 1\n\t"
  "v_mov_b32_dpp v188, v189 quad_perm:[0,1,2,3] row_mask:0xf bank_mask:0xa\n\t"
  "buffer_store_dword v188, %[vso], %[srd], %[sSo] offen sc1\n\t"
  "Lxe%=:\n\t"
  "s_nop 1\n\t"
        :: [srd] "s"(srd), [w] "s"(w), [sSo] "s"(OFF_ST1 + cb * 128u), [sXo] "s"(OFF_XT + cb * 20480u + (w - 1) * 10240u),
           [vtx] "v"(lb + L_XR + (w - 1) * 10240 + (8 * (g & 1) + q) * 128 + (g >> 1) * 32 + (lane & 3) * 8), [vsx] "v"(lb + L_XR + (w & 1) * 1024 + lane * 16),
           [vso] "v"((lane & 3) == 0 ? (((w & 1) * 4 + g) ^ stkey(cb)) * 16 + ((lane >> 3) & 1) * 4 + (lane & 4) * 2 : OOB), [vxo] "v"((2 * (ch >> 3) + (ch & 1)) * 128 + ((((ch ^ cb) >> 1) & 3) + 4 * (g & 1)) * 16)
        : "memory", "vcc", "scc",
"s44", "v96", "v97", "v98", "v99", "v100", "v101", "v102", "v103", "v104", "v105", "v106", "v107", "v108", "v109", "v110", "v111", "v112", "v113", "v114", "v115", "v116", "v117", "v118", "v119", "v120", "v121", "v122", "v123", "v124", "v125", "v126", "v127", "v128", "v129", "v130", "v131", "v132", "v133", "v134", "v135", "v184", "v185", "v186", "v188", "v189"
      );
    } else if (w == 1) GEMV_UNIT(cb, lb + L_XR, 8);
    stash();
    STAMP(5);
  } else {
    const unsigned lu = lb + L_UN + w * UN_SZ;
    const unsigned g = lane >> 4, q = (lane >> 2) & 3, p4 = lane & 3;
    if (W_DELAY) __builtin_amdgcn_s_sleep(W_DELAY);
    if (role == 2) {
      const unsigned ui = 4 * w2 + w;
      GEMV_LOAD(ui, lu);
      const rsrc_t rd = mkrsrc(wd, D_ * F_ * 2);
      const unsigned r8 = 24 * w + (lane >> 3), so = (96 * (w2 >> 1) * D_ + 320 * (w2 & 1)) * 2;
      u32x4 raw[15];
#pragma unroll
      for (int n = 0; n < 15; ++n) raw[n] = __builtin_amdgcn_raw_buffer_load_b128(rd, r8 * (D_ * 2) + (lane & 7) * 16 + (n % 5) * 128, so + (n / 5) * 8 * D_ * 2, NT);
      FENCE();
      tag = load_tag(ws);
      GEMV_UNIT(ui, lu, 15);
      STAMP(1);
#pragma unroll
      for (int n = 0; n < 15; ++n) *(u32x4*)(smem + r8 * RP2 + (lane & 7) * 16 + (n % 5) * 128 + (n / 5) * 8 * RP2) = raw[n];
      FENCE();
      asm volatile("s_waitcnt vmcnt(0)" ::: "memory");
      put_flag(srd, U_FLAG(ui), lane, tag);
      STAMP(2);
      BAR();
#pragma unroll
      for (int n = 0; n < 15; ++n) {
        const int i = n % 5;
        const char* a = smem + (32 * (n / 5) + 8 * g + q) * RP2 + (80 * w + (i < 4 ? 32 * (i >> 1) + 8 * p4 + 4 * (i & 1) : 64 + 4 * p4)) * 2;
        const u32x2 lo = tr_read(a), hi = tr_read(a + 4 * RP2);
        raw[n] = u32x4{lo[0], lo[1], hi[0], hi[1]};
      }
      FENCE();
#pragma unroll
      for (int n = 0; n < 15; ++n)
        __builtin_amdgcn_raw_buffer_store_b128(raw[n], srd, lane * 16 + (n & 3) * 1024, OFF_W2F + ((4 * (w2 & 1) + w) * 240 + 15 * (w2 >> 1) + (n & ~3)) * 1024, SC1);
      FENCE();
      STAMP(7);
      stash();
      if (w2 < 10 && w == 0) {
        poll_flags<1, 4>(srd, OFF_FA2 + ((lane & 3) * 60 + 40 + w2) * 128, tag);
        const float sc = rbf(1.f + modval(srd, 4 * 640 + lane + 64 * w2));
        const u32 rv = __float_as_uint(sc == 0.f ? 0x1p40f : 1.f / sc);
        __builtin_amdgcn_raw_buffer_store_b32(rv, srd, lane * 4, OFF_RT + w2 * 256, SC1);
        __builtin_amdgcn_raw_buffer_store_b32(rv, srd, lane * 48, OFF_RL + (w2 < 2 ? w2 : w2 + 2) * 4, SC1);
      }
    } else {
      const rsrc_t rg = mkrsrc(wg, D_ * F_ * 2), ru = mkrsrc(wu, D_ * F_ * 2);
      const unsigned rw = 5 * w + lane / 12, ck = lane % 12, ui = 160 + 64 * w + w1;
      const unsigned vo = lane < 60 ? rw * (F_ * 2) + ck * 16 : OOB, so = (160 * (w1 & 3) * F_ + 96 * (w1 >> 2)) * 2;
      u32x4 raw[16];
#pragma unroll
      for (int n = 0; n < 8; ++n) {
        raw[2 * n] = __builtin_amdgcn_raw_buffer_load_b128(rg, vo, so + n * 20 * F_ * 2, NT);
        raw[2 * n + 1] = __builtin_amdgcn_raw_buffer_load_b128(ru, vo, so + n * 20 * F_ * 2, NT);
      }
      FENCE();
      tag = load_tag(ws);
      char* o = smem + (lane < 60 ? rw * RP1 + ck * 32 : 384);
      u32x4 fr[15];
#pragma unroll
      for (int h = 0; h < 2; ++h) {
#pragma unroll
        for (int n = h ? 5 : 0; n < (h ? 8 : 5); ++n) {
          *(u32x4*)(o + n * 20 * RP1) = u32x4{raw[2 * n][0], raw[2 * n + 1][0], raw[2 * n][1], raw[2 * n + 1][1]};
          *(u32x4*)(o + n * 20 * RP1 + (lane < 60 ? 16 : 0)) = u32x4{raw[2 * n][2], raw[2 * n + 1][2], raw[2 * n][3], raw[2 * n + 1][3]};
        }
        BAR();
#pragma unroll
        for (int n = h ? 9 : 0; n < (h ? 15 : 9); ++n) {
          const int ks = n / 3, i = n % 3;
          const char* a = smem + (32 * ks + 4 * g + q) * RP1 + (12 * w + (p4 < 3 ? 4 * p4 + i : 4 * i + 3)) * 8;
          const u32x2 lo = tr_read(a), hi = tr_read(a + 16 * RP1);
          fr[n] = u32x4{lo[0], lo[1], hi[0], hi[1]};
        }
        FENCE();
#pragma unroll
        for (int n = h ? 9 : 0; n < (h ? 15 : 9); ++n)
          __builtin_amdgcn_raw_buffer_store_b128(fr[n], srd, lane * 16 + (n & 3) * 1024, OFF_W1F + (((w1 & ~3u) + w) * 60 + 15 * (w1 & 3) + (n & ~3)) * 1024, SC1);
        FENCE();
      }
      STAMP(6);
      if (ui < 240) { GEMV_LOAD(ui, lu); asm volatile("s_waitcnt vmcnt(21)" ::: "memory"); } else asm volatile("s_waitcnt vmcnt(0)" ::: "memory");
      asm volatile("s_barrier" ::: "memory");
      if (w == 3) put_flag(srd, OFF_FW1 + w1 * 128, lane, tag);
      STAMP(7);
      if (ui < 240) {
        GEMV_UNIT(ui, lu, 0);
        asm volatile("s_waitcnt vmcnt(0)" ::: "memory");
        put_flag(srd, U_FLAG(ui), lane, tag);
        STAMP(2);
      }
      poll_flags<1, 4>(srd, OFF_FW1 + (j * 4 + (lane & 3)) * 128, tag);
      asm volatile(
  "s_nop 4\n\t"
  "s_add_u32 s44, %[sW1], 0\n\t"
  "buffer_load_dwordx4 a[0:3], %[vl16], %[srd], s44 offen offset:0 sc1\n\t"
  "buffer_load_dwordx4 a[4:7], %[vl16], %[srd], s44 offen offset:1024 sc1\n\t"
  "buffer_load_dwordx4 a[8:11], %[vl16], %[srd], s44 offen offset:2048 sc1\n\t"
  "buffer_load_dwordx4 a[12:15], %[vl16], %[srd], s44 offen offset:3072 sc1\n\t"
  "s_add_u32 s44, %[sW1], 4096\n\t"
  "buffer_load_dwordx4 a[16:19], %[vl16], %[srd], s44 offen offset:0 sc1\n\t"
  "buffer_load_dwordx4 a[20:23], %[vl16], %[srd], s44 offen offset:1024 sc1\n\t"
  "buffer_load_dwordx4 a[24:27], %[vl16], %[srd], s44 offen offset:2048 sc1\n\t"
  "buffer_load_dwordx4 a[28:31], %[vl16], %[srd], s44 offen offset:3072 sc1\n\t"
  "s_add_u32 s44, %[sW1], 8192\n\t"
  "buffer_load_dwordx4 a[32:35], %[vl16], %[srd], s44 offen offset:0 sc1\n\t"
  "buffer_load_dwordx4 a[36:39], %[vl16], %[srd], s44 offen offset:1024 sc1\n\t"
  "buffer_load_dwordx4 a[40:43], %[vl16], %[srd], s44 offen offset:2048 sc1\n\t"
  "buffer_load_dwordx4 a[44:47], %[vl16], %[srd], s44 offen offset:3072 sc1\n\t"
  "s_add_u32 s44, %[sW1], 12288\n\t"
  "buffer_load_dwordx4 a[48:51], %[vl16], %[srd], s44 offen offset:0 sc1\n\t"
  "buffer_load_dwordx4 a[52:55], %[vl16], %[srd], s44 offen offset:1024 sc1\n\t"
  "buffer_load_dwordx4 a[56:59], %[vl16], %[srd], s44 offen offset:2048 sc1\n\t"
  "buffer_load_dwordx4 a[60:63], %[vl16], %[srd], s44 offen offset:3072 sc1\n\t"
  "s_add_u32 s44, %[sW1], 16384\n\t"
  "buffer_load_dwordx4 a[64:67], %[vl16], %[srd], s44 offen offset:0 sc1\n\t"
  "buffer_load_dwordx4 a[68:71], %[vl16], %[srd], s44 offen offset:1024 sc1\n\t"
  "buffer_load_dwordx4 a[72:75], %[vl16], %[srd], s44 offen offset:2048 sc1\n\t"
  "buffer_load_dwordx4 a[76:79], %[vl16], %[srd], s44 offen offset:3072 sc1\n\t"
  "s_add_u32 s44, %[sW1], 20480\n\t"
  "buffer_load_dwordx4 a[80:83], %[vl16], %[srd], s44 offen offset:0 sc1\n\t"
  "buffer_load_dwordx4 a[84:87], %[vl16], %[srd], s44 offen offset:1024 sc1\n\t"
  "buffer_load_dwordx4 a[88:91], %[vl16], %[srd], s44 offen offset:2048 sc1\n\t"
  "buffer_load_dwordx4 a[92:95], %[vl16], %[srd], s44 offen offset:3072 sc1\n\t"
  "s_add_u32 s44, %[sW1], 24576\n\t"
  "buffer_load_dwordx4 a[96:99], %[vl16], %[srd], s44 offen offset:0 sc1\n\t"
  "buffer_load_dwordx4 a[100:103], %[vl16], %[srd], s44 offen offset:1024 sc1\n\t"
  "buffer_load_dwordx4 a[104:107], %[vl16], %[srd], s44 offen offset:2048 sc1\n\t"
  "buffer_load_dwordx4 a[108:111], %[vl16], %[srd], s44 offen offset:3072 sc1\n\t"
  "s_add_u32 s44, %[sW1], 28672\n\t"
  "buffer_load_dwordx4 a[112:115], %[vl16], %[srd], s44 offen offset:0 sc1\n\t"
  "buffer_load_dwordx4 a[116:119], %[vl16], %[srd], s44 offen offset:1024 sc1\n\t"
  "buffer_load_dwordx4 a[120:123], %[vl16], %[srd], s44 offen offset:2048 sc1\n\t"
  "buffer_load_dwordx4 a[124:127], %[vl16], %[srd], s44 offen offset:3072 sc1\n\t"
  "s_add_u32 s44, %[sW1], 32768\n\t"
  "buffer_load_dwordx4 a[128:131], %[vl16], %[srd], s44 offen offset:0 sc1\n\t"
  "buffer_load_dwordx4 a[132:135], %[vl16], %[srd], s44 offen offset:1024 sc1\n\t"
  "buffer_load_dwordx4 a[136:139], %[vl16], %[srd], s44 offen offset:2048 sc1\n\t"
  "buffer_load_dwordx4 a[140:143], %[vl16], %[srd], s44 offen offset:3072 sc1\n\t"
  "s_add_u32 s44, %[sW1], 36864\n\t"
  "buffer_load_dwordx4 a[144:147], %[vl16], %[srd], s44 offen offset:0 sc1\n\t"
  "buffer_load_dwordx4 a[148:151], %[vl16], %[srd], s44 offen offset:1024 sc1\n\t"
  "buffer_load_dwordx4 a[152:155], %[vl16], %[srd], s44 offen offset:2048 sc1\n\t"
  "buffer_load_dwordx4 a[156:159], %[vl16], %[srd], s44 offen offset:3072 sc1\n\t"
  "s_add_u32 s44, %[sW1], 40960\n\t"
  "buffer_load_dwordx4 a[160:163], %[vl16], %[srd], s44 offen offset:0 sc1\n\t"
  "buffer_load_dwordx4 a[164:167], %[vl16], %[srd], s44 offen offset:1024 sc1\n\t"
  "buffer_load_dwordx4 a[168:171], %[vl16], %[srd], s44 offen offset:2048 sc1\n\t"
  "buffer_load_dwordx4 a[172:175], %[vl16], %[srd], s44 offen offset:3072 sc1\n\t"
  "s_add_u32 s44, %[sW1], 45056\n\t"
  "buffer_load_dwordx4 a[176:179], %[vl16], %[srd], s44 offen offset:0 sc1\n\t"
  "buffer_load_dwordx4 a[180:183], %[vl16], %[srd], s44 offen offset:1024 sc1\n\t"
  "buffer_load_dwordx4 a[184:187], %[vl16], %[srd], s44 offen offset:2048 sc1\n\t"
  "buffer_load_dwordx4 a[188:191], %[vl16], %[srd], s44 offen offset:3072 sc1\n\t"
  "s_add_u32 s44, %[sW1], 49152\n\t"
  "buffer_load_dwordx4 a[192:195], %[vl16], %[srd], s44 offen offset:0 sc1\n\t"
  "buffer_load_dwordx4 a[196:199], %[vl16], %[srd], s44 offen offset:1024 sc1\n\t"
  "buffer_load_dwordx4 a[200:203], %[vl16], %[srd], s44 offen offset:2048 sc1\n\t"
  "buffer_load_dwordx4 a[204:207], %[vl16], %[srd], s44 offen offset:3072 sc1\n\t"
        :: [srd] "s"(srd), [sW1] "s"(OFF_W1F + (j * 4 + w) * 61440u), [vl16] "v"(lane * 16) : "memory", "scc",
"a0", "a1", "a2", "a3", "a4", "a5", "a6", "a7", "a8", "a9", "a10", "a11", "a12", "a13", "a14", "a15", "a16", "a17", "a18", "a19", "a20", "a21", "a22", "a23", "a24", "a25", "a26", "a27", "a28", "a29", "a30", "a31", "a32", "a33", "a34", "a35", "a36", "a37", "a38", "a39", "a40", "a41", "a42", "a43", "a44", "a45", "a46", "a47", "a48", "a49", "a50", "a51", "a52", "a53", "a54", "a55", "a56", "a57", "a58", "a59", "a60", "a61", "a62", "a63", "a64", "a65", "a66", "a67", "a68", "a69", "a70", "a71", "a72", "a73", "a74", "a75", "a76", "a77", "a78", "a79", "a80", "a81", "a82", "a83", "a84", "a85", "a86", "a87", "a88", "a89", "a90", "a91", "a92", "a93", "a94", "a95", "a96", "a97", "a98", "a99", "a100", "a101", "a102", "a103", "a104", "a105", "a106", "a107", "a108", "a109", "a110", "a111", "a112", "a113", "a114", "a115", "a116", "a117", "a118", "a119", "a120", "a121", "a122", "a123", "a124", "a125", "a126", "a127", "a128", "a129", "a130", "a131", "a132", "a133", "a134", "a135", "a136", "a137", "a138", "a139", "a140", "a141", "a142", "a143", "a144", "a145", "a146", "a147", "a148", "a149", "a150", "a151", "a152", "a153", "a154", "a155", "a156", "a157", "a158", "a159", "a160", "a161", "a162", "a163", "a164", "a165", "a166", "a167", "a168", "a169", "a170", "a171", "a172", "a173", "a174", "a175", "a176", "a177", "a178", "a179", "a180", "a181", "a182", "a183", "a184", "a185", "a186", "a187", "a188", "a189", "a190", "a191", "a192", "a193", "a194", "a195", "a196", "a197", "a198", "a199", "a200", "a201", "a202", "a203", "a204", "a205", "a206", "a207", "s44"
      );
      wdone = 7;
      stash();
      if (w > 1 || (w == 1 && w1 < 4)) {
        const unsigned c0 = (6 - w) * 640 + 160 * (w1 & 3);
        poll_flags<1, 4>(srd, OFF_FA2 + ((lane & 3) * 60 + ((c0 + min(64 * ((lane >> 2) & 3), 159u)) >> 6)) * 128, tag);
        f32x4 m[4];
#pragma unroll
        for (int k = 0; k < 4; ++k) m[k] = __builtin_bit_cast(f32x4, __builtin_amdgcn_raw_buffer_load_b128(srd, lane < 40 ? lane * 16 : OOB, OFF_MODP + k * 15360 + c0 * 4, SC1));
        f32x4 v = (m[0] + m[1]) + (m[2] + m[3]);
#pragma unroll
        for (int e = 0; e < 4; ++e) v[e] = rbf(v[e]);
        if (w == 1) __builtin_amdgcn_raw_buffer_store_b128(__builtin_bit_cast(u32x4, v), srd, lane < 40 ? lane * 16 : OOB, OFF_GB + 640 * w1, SC1);
        else {
          const float of = w == 2 ? 1.f : 0.f;
          *(u32x2*)(smem + L_WS + (w - 2) * 1024 + lane * 8) = u32x2{pack_bf16(of + v[0], of + v[1]), pack_bf16(of + v[2], of + v[3])};
        }
      }
      STAMP(5);
      BAR();
      STAMP(3);
      f32x4 aU[3], aB[3];
#pragma unroll
      for (int i = 0; i < 3; ++i) { aU[i] = f32x4{0, 0, 0, 0}; aB[i] = f32x4{0, 0, 0, 0}; }
#pragma unroll
      for (int ks = 0; ks < 5; ++ks) {
        const char* t = smem + L_WS + (32 * ks + 4 * g) * 2;
        const u32x2 s0 = *(const u32x2*)t, s1 = *(const u32x2*)(t + 32), b0 = *(const u32x2*)(t + 1024), b1 = *(const u32x2*)(t + 1056);
#pragma unroll
        for (int i = 0; i < 3; ++i) {
          mfma_v(aU[i], u32x4{s0[0], s0[1], s1[0], s1[1]}, fr[3 * ks + i]);
          mfma_v(aB[i], u32x4{b0[0], b0[1], b1[0], b1[1]}, fr[3 * ks + i]);
        }
      }
      asm volatile("s_nop 7\n\ts_nop 1" : "+v"(aU[0]), "+v"(aU[1]), "+v"(aU[2]), "+v"(aB[0]), "+v"(aB[1]), "+v"(aB[2]));
      const float u = g == 0 ? aU[0][0] : g == 1 ? aU[1][0] : aU[2][0], bb = g == 0 ? aB[0][0] : g == 1 ? aB[1][0] : aB[2][0];
      const float uh = rbf(u), bh = rbf(bb);
      __builtin_amdgcn_raw_buffer_store_b64(u32x2{pack_bf16(uh, u - uh), pack_bf16(bh, bb - bh)}, srd,
                                            g < 3 ? OFF_UB + ((((w1 & ~3u) + w) * 3 + g) * 64 + (w1 & 3) * 16 + (lane & 15)) * 8 : OOB, 0, SC1);
    }
  }
  if (role == 0) {
    const unsigned d = 8 * (cb >> 1) + 2 * w + (cb & 1);
    asm volatile(
  "s_nop 4\n\t"
  "buffer_load_dwordx2 v[160:161], %[vf1], %[srd], 0 offen sc1\n\t"
  "s_lshl_b32 s45, %[w], 12\n\t"
  "v_mov_b32 v252, %[tlo]\n\t"
  "v_mov_b32 v253, %[thi]\n\t"
  "v_mov_b32 v254, %[tlo]\n\t"
  "v_mov_b32 v255, %[thi]\n\t"
  "v_mov_b32 v162, 140288\n\t"
  "v_mov_b32 v163, 1\n\t"
  "s_mov_b64 exec, 1\n\t"
  "s_waitcnt vmcnt(1)\n\t"
  "ds_add_rtn_u32 v164, v162, v163\n\t"
  "s_waitcnt lgkmcnt(0)\n\t"
  "s_mov_b64 exec, -1\n\t"
  "v_readfirstlane_b32 s44, v164\n\t"
  "s_nop 3\n\t"
  "s_cmp_lg_u32 s44, 3\n\t"
  "s_cbranch_scc1 Lnox%=\n\t"
  "buffer_store_dwordx4 v[252:255], %[vf3], %[srd], 0 offen sc1\n\t"
  "s_waitcnt vmcnt(1)\n\t"
  "s_branch Lx%=\n\t"
  "Lnox%=:\n\t"
  "s_waitcnt vmcnt(0)\n\t"
  "Lx%=:\n\t"
  "v_cmp_ne_u64 vcc, v[252:253], v[160:161]\n\t"
  "s_nop 1\n\t"
  "s_cbranch_vccnz La1_slow%=\n\t"
  "La1_back%=:\n\t"
  "s_add_u32 m0, s45, 68608\n\t"
  "s_add_u32 s44, %[sKV], 0\n\t"
  "buffer_load_dwordx4 %[vkv], %[srd], s44 offen sc1 lds\n\t"
  "s_add_u32 m0, s45, 69632\n\t"
  "s_add_u32 s44, %[sKV], 163840\n\t"
  "buffer_load_dwordx4 %[vkv], %[srd], s44 offen sc1 lds\n\t"
  "s_add_u32 m0, s45, 70656\n\t"
  "s_add_u32 s44, %[sKV], 327680\n\t"
  "buffer_load_dwordx4 %[vkv], %[srd], s44 offen sc1 lds\n\t"
  "s_add_u32 m0, s45, 71680\n\t"
  "s_add_u32 s44, %[sKV], 491520\n\t"
  "buffer_load_dwordx4 %[vkv], %[srd], s44 offen sc1 lds\n\t"
  "ds_read_b64 v[64:65], %[va] offset:0\n\t"
  "ds_read_b64 v[66:67], %[va] offset:8\n\t"
  "ds_read_b64 v[68:69], %[va] offset:512\n\t"
  "ds_read_b64 v[70:71], %[va] offset:520\n\t"
  "ds_read_b64 v[72:73], %[va] offset:1024\n\t"
  "ds_read_b64 v[74:75], %[va] offset:1032\n\t"
  "ds_read_b64 v[76:77], %[va] offset:1536\n\t"
  "ds_read_b64 v[78:79], %[va] offset:1544\n\t"
  "ds_read_b64 v[80:81], %[va] offset:2048\n\t"
  "ds_read_b64 v[82:83], %[va] offset:2056\n\t"
  "ds_read_b64 v[84:85], %[va] offset:2560\n\t"
  "ds_read_b64 v[86:87], %[va] offset:2568\n\t"
  "ds_read_b64 v[88:89], %[va] offset:3072\n\t"
  "ds_read_b64 v[90:91], %[va] offset:3080\n\t"
  "ds_read_b64 v[92:93], %[va] offset:3584\n\t"
  "ds_read_b64 v[94:95], %[va] offset:3592\n\t"
  "s_waitcnt vmcnt(0) lgkmcnt(0)\n\t"
  "s_barrier\n\t"
  "ds_read_b128 v[124:127], %[vb0] offset:15360\n\t"
  "ds_read_b128 v[120:123], %[vb0] offset:13312\n\t"
  "ds_read_b128 v[116:119], %[vb0] offset:11264\n\t"
  "ds_read_b128 v[112:115], %[vb0] offset:9216\n\t"
  "ds_read_b128 v[108:111], %[vb0] offset:7168\n\t"
  "ds_read_b128 v[104:107], %[vb0] offset:5120\n\t"
  "ds_read_b128 v[100:103], %[vb0] offset:3072\n\t"
  "ds_read_b128 v[96:99], %[vb0] offset:1024\n\t"
  "s_waitcnt lgkmcnt(7)\n\t"
  "v_mfma_f32_16x16x32_bf16 v[60:63], v[64:67], v[124:127], 0\n\t"
  "ds_read_b128 v[156:159], %[vb1] offset:15360\n\t"
  "s_waitcnt lgkmcnt(7)\n\t"
  "v_mfma_f32_16x16x32_bf16 v[56:59], v[64:67], v[120:123], 0\n\t"
  "s_waitcnt lgkmcnt(6)\n\t"
  "v_mfma_f32_16x16x32_bf16 v[52:55], v[64:67], v[116:119], 0\n\t"
  "s_waitcnt lgkmcnt(5)\n\t"
  "v_mfma_f32_16x16x32_bf16 v[48:51], v[64:67], v[112:115], 0\n\t"
  "s_waitcnt lgkmcnt(4)\n\t"
  "v_mfma_f32_16x16x32_bf16 v[44:47], v[64:67], v[108:111], 0\n\t"
  "ds_read_b128 v[152:155], %[vb1] offset:13312\n\t"
  "s_waitcnt lgkmcnt(4)\n\t"
  "v_mfma_f32_16x16x32_bf16 v[40:43], v[64:67], v[104:107], 0\n\t"
  "s_waitcnt lgkmcnt(3)\n\t"
  "v_mfma_f32_16x16x32_bf16 v[36:39], v[64:67], v[100:103], 0\n\t"
  "s_waitcnt lgkmcnt(2)\n\t"
  "v_mfma_f32_16x16x32_bf16 v[32:35], v[64:67], v[96:99], 0\n\t"
  "v_mfma_f32_16x16x32_bf16 v[60:63], %[vd], v[124:127], v[60:63]\n\t"
  "ds_read_b128 v[148:151], %[vb1] offset:11264\n\t"
  "v_mfma_f32_16x16x32_bf16 v[56:59], %[vd], v[120:123], v[56:59]\n\t"
  "v_mfma_f32_16x16x32_bf16 v[52:55], %[vd], v[116:119], v[52:55]\n\t"
  "v_mfma_f32_16x16x32_bf16 v[48:51], %[vd], v[112:115], v[48:51]\n\t"
  "v_mfma_f32_16x16x32_bf16 v[44:47], %[vd], v[108:111], v[44:47]\n\t"
  "ds_read_b128 v[144:147], %[vb1] offset:9216\n\t"
  "v_mfma_f32_16x16x32_bf16 v[40:43], %[vd], v[104:107], v[40:43]\n\t"
  "v_mfma_f32_16x16x32_bf16 v[36:39], %[vd], v[100:103], v[36:39]\n\t"
  "v_mfma_f32_16x16x32_bf16 v[32:35], %[vd], v[96:99], v[32:35]\n\t"
  "ds_read_b64 v[64:65], %[va] offset:64\n\t"
  "ds_read_b64 v[66:67], %[va] offset:72\n\t"
  "v_mfma_f32_16x16x32_bf16 v[60:63], v[68:71], v[120:123], v[60:63]\n\t"
  "ds_read_b128 v[140:143], %[vb1] offset:7168\n\t"
  "v_mfma_f32_16x16x32_bf16 v[56:59], v[68:71], v[116:119], v[56:59]\n\t"
  "v_mfma_f32_16x16x32_bf16 v[52:55], v[68:71], v[112:115], v[52:55]\n\t"
  "v_mfma_f32_16x16x32_bf16 v[48:51], v[68:71], v[108:111], v[48:51]\n\t"
  "v_mfma_f32_16x16x32_bf16 v[44:47], v[68:71], v[104:107], v[44:47]\n\t"
  "ds_read_b128 v[136:139], %[vb1] offset:5120\n\t"
  "v_mfma_f32_16x16x32_bf16 v[40:43], v[68:71], v[100:103], v[40:43]\n\t"
  "v_mfma_f32_16x16x32_bf16 v[36:39], v[68:71], v[96:99], v[36:39]\n\t"
  "ds_read_b64 v[68:69], %[va] offset:576\n\t"
  "ds_read_b64 v[70:71], %[va] offset:584\n\t"
  "v_mfma_f32_16x16x32_bf16 v[60:63], v[72:75], v[116:119], v[60:63]\n\t"
  "v_mfma_f32_16x16x32_bf16 v[56:59], v[72:75], v[112:115], v[56:59]\n\t"
  "ds_read_b128 v[132:135], %[vb1] offset:3072\n\t"
  "buffer_load_dwordx2 v[250:251], %[vfw], %[srd], 0 offen sc1\n\t"
  "v_mfma_f32_16x16x32_bf16 v[52:55], v[72:75], v[108:111], v[52:55]\n\t"
  "v_mfma_f32_16x16x32_bf16 v[48:51], v[72:75], v[104:107], v[48:51]\n\t"
  "v_mfma_f32_16x16x32_bf16 v[44:47], v[72:75], v[100:103], v[44:47]\n\t"
  "v_mfma_f32_16x16x32_bf16 v[40:43], v[72:75], v[96:99], v[40:43]\n\t"
  "ds_read_b128 v[128:131], %[vb1] offset:1024\n\t"
  "ds_read_b64 v[72:73], %[va] offset:1088\n\t"
  "v_mfma_f32_16x16x32_bf16 v[60:63], v[76:79], v[112:115], v[60:63]\n\t"
  "ds_read_b64 v[74:75], %[va] offset:1096\n\t"
  "v_mfma_f32_16x16x32_bf16 v[56:59], v[76:79], v[108:111], v[56:59]\n\t"
  "v_mfma_f32_16x16x32_bf16 v[52:55], v[76:79], v[104:107], v[52:55]\n\t"
  "v_mfma_f32_16x16x32_bf16 v[48:51], v[76:79], v[100:103], v[48:51]\n\t"
  "v_mfma_f32_16x16x32_bf16 v[44:47], v[76:79], v[96:99], v[44:47]\n\t"
  "ds_read_b64 v[76:77], %[va] offset:1600\n\t"
  "ds_read_b64 v[78:79], %[va] offset:1608\n\t"
  "v_mfma_f32_16x16x32_bf16 v[60:63], v[80:83], v[108:111], v[60:63]\n\t"
  "v_mfma_f32_16x16x32_bf16 v[56:59], v[80:83], v[104:107], v[56:59]\n\t"
  "v_mfma_f32_16x16x32_bf16 v[52:55], v[80:83], v[100:103], v[52:55]\n\t"
  "v_mfma_f32_16x16x32_bf16 v[48:51], v[80:83], v[96:99], v[48:51]\n\t"
  "ds_read_b64 v[80:81], %[va] offset:2112\n\t"
  "ds_read_b64 v[82:83], %[va] offset:2120\n\t"
  "v_mfma_f32_16x16x32_bf16 v[60:63], v[84:87], v[104:107], v[60:63]\n\t"
  "v_mfma_f32_16x16x32_bf16 v[56:59], v[84:87], v[100:103], v[56:59]\n\t"
  "v_mfma_f32_16x16x32_bf16 v[52:55], v[84:87], v[96:99], v[52:55]\n\t"
  "ds_read_b64 v[84:85], %[va] offset:2624\n\t"
  "ds_read_b64 v[86:87], %[va] offset:2632\n\t"
  "v_mfma_f32_16x16x32_bf16 v[60:63], v[88:91], v[100:103], v[60:63]\n\t"
  "v_mfma_f32_16x16x32_bf16 v[56:59], v[88:91], v[96:99], v[56:59]\n\t"
  "ds_read_b64 v[88:89], %[va] offset:3136\n\t"
  "ds_read_b64 v[90:91], %[va] offset:3144\n\t"
  "v_mfma_f32_16x16x32_bf16 v[60:63], v[92:95], v[96:99], v[60:63]\n\t"
  "s_waitcnt lgkmcnt(10)\n\t"
  "ds_read_b64 v[92:93], %[va] offset:3648\n\t"
  "v_mfma_f32_16x16x32_bf16 v[60:63], v[64:67], v[156:159], v[60:63]\n\t"
  "ds_read_b128 v[124:127], %[vb2] offset:15360\n\t"
  "ds_read_b64 v[94:95], %[va] offset:3656\n\t"
  "v_mfma_f32_16x16x32_bf16 v[56:59], v[64:67], v[152:155], v[56:59]\n\t"
  "v_mfma_f32_16x16x32_bf16 v[52:55], v[64:67], v[148:151], v[52:55]\n\t"
  "v_mfma_f32_16x16x32_bf16 v[48:51], v[64:67], v[144:147], v[48:51]\n\t"
  "ds_read_b128 v[120:123], %[vb2] offset:13312\n\t"
  "v_mfma_f32_16x16x32_bf16 v[44:47], v[64:67], v[140:143], v[44:47]\n\t"
  "v_mfma_f32_16x16x32_bf16 v[40:43], v[64:67], v[136:139], v[40:43]\n\t"
  "v_mfma_f32_16x16x32_bf16 v[36:39], v[64:67], v[132:135], v[36:39]\n\t"
  "ds_read_b128 v[116:119], %[vb2] offset:11264\n\t"
  "v_mfma_f32_16x16x32_bf16 v[32:35], v[64:67], v[128:131], v[32:35]\n\t"
  "ds_read_b64 v[64:65], %[va] offset:128\n\t"
  "ds_read_b64 v[66:67], %[va] offset:136\n\t"
  "v_mfma_f32_16x16x32_bf16 v[60:63], v[68:71], v[152:155], v[60:63]\n\t"
  "v_mfma_f32_16x16x32_bf16 v[56:59], v[68:71], v[148:151], v[56:59]\n\t"
  "ds_read_b128 v[112:115], %[vb2] offset:9216\n\t"
  "v_mfma_f32_16x16x32_bf16 v[52:55], v[68:71], v[144:147], v[52:55]\n\t"
  "v_mfma_f32_16x16x32_bf16 v[48:51], v[68:71], v[140:143], v[48:51]\n\t"
  "v_mfma_f32_16x16x32_bf16 v[44:47], v[68:71], v[136:139], v[44:47]\n\t"
  "ds_read_b128 v[108:111], %[vb2] offset:7168\n\t"
  "v_mfma_f32_16x16x32_bf16 v[40:43], v[68:71], v[132:135], v[40:43]\n\t"
  "v_mfma_f32_16x16x32_bf16 v[36:39], v[68:71], v[128:131], v[36:39]\n\t"
  "s_waitcnt lgkmcnt(11)\n\t"
  "ds_read_b64 v[68:69], %[va] offset:640\n\t"
  "v_mfma_f32_16x16x32_bf16 v[60:63], v[72:75], v[148:151], v[60:63]\n\t"
  "ds_read_b128 v[104:107], %[vb2] offset:5120\n\t"
  "ds_read_b64 v[70:71], %[va] offset:648\n\t"
  "v_mfma_f32_16x16x32_bf16 v[56:59], v[72:75], v[144:147], v[56:59]\n\t"
  "v_mfma_f32_16x16x32_bf16 v[52:55], v[72:75], v[140:143], v[52:55]\n\t"
  "v_mfma_f32_16x16x32_bf16 v[48:51], v[72:75], v[136:139], v[48:51]\n\t"
  "ds_read_b128 v[100:103], %[vb2] offset:3072\n\t"
  "v_mfma_f32_16x16x32_bf16 v[44:47], v[72:75], v[132:135], v[44:47]\n\t"
  "v_mfma_f32_16x16x32_bf16 v[40:43], v[72:75], v[128:131], v[40:43]\n\t"
  "ds_read_b64 v[72:73], %[va] offset:1152\n\t"
  "ds_read_b64 v[74:75], %[va] offset:1160\n\t"
  "v_mfma_f32_16x16x32_bf16 v[60:63], v[76:79], v[144:147], v[60:63]\n\t"
  "ds_read_b128 v[96:99], %[vb2] offset:1024\n\t"
  "s_waitcnt vmcnt(0)\n\t"
  "v_cmp_eq_u64 s[54:55], v[252:253], v[250:251]\n\t"
  "v_mfma_f32_16x16x32_bf16 v[56:59], v[76:79], v[140:143], v[56:59]\n\t"
  "v_mfma_f32_16x16x32_bf16 v[52:55], v[76:79], v[136:139], v[52:55]\n\t"
  "v_mfma_f32_16x16x32_bf16 v[48:51], v[76:79], v[132:135], v[48:51]\n\t"
  "s_nop 0\n\t"
  "s_cmp_eq_u64 s[54:55], -1\n\t"
  "s_cselect_b32 s53, 1, 0\n\t"
  "s_cselect_b32 s56, %[sW1], 0x70000000\n\t"
  "v_mfma_f32_16x16x32_bf16 v[44:47], v[76:79], v[128:131], v[44:47]\n\t"
  "ds_read_b64 v[76:77], %[va] offset:1664\n\t"
  "ds_read_b64 v[78:79], %[va] offset:1672\n\t"
  "v_mfma_f32_16x16x32_bf16 v[60:63], v[80:83], v[140:143], v[60:63]\n\t"
  "s_mov_b32 %[wdone], s53\n\t"
  "buffer_load_dwordx4 a[0:3], %[vl16], %[srd], s56 offen offset:0 sc1\n\t"
  "v_mfma_f32_16x16x32_bf16 v[56:59], v[80:83], v[136:139], v[56:59]\n\t"
  "v_mfma_f32_16x16x32_bf16 v[52:55], v[80:83], v[132:135], v[52:55]\n\t"
  "v_mfma_f32_16x16x32_bf16 v[48:51], v[80:83], v[128:131], v[48:51]\n\t"
  "ds_read_b64 v[80:81], %[va] offset:2176\n\t"
  "ds_read_b64 v[82:83], %[va] offset:2184\n\t"
  "v_mfma_f32_16x16x32_bf16 v[60:63], v[84:87], v[136:139], v[60:63]\n\t"
  "buffer_load_dwordx4 a[4:7], %[vl16], %[srd], s56 offen offset:1024 sc1\n\t"
  "v_mfma_f32_16x16x32_bf16 v[56:59], v[84:87], v[132:135], v[56:59]\n\t"
  "v_mfma_f32_16x16x32_bf16 v[52:55], v[84:87], v[128:131], v[52:55]\n\t"
  "s_waitcnt lgkmcnt(15)\n\t"
  "ds_read_b64 v[84:85], %[va] offset:2688\n\t"
  "v_mfma_f32_16x16x32_bf16 v[60:63], v[88:91], v[132:135], v[60:63]\n\t"
  "ds_read_b64 v[86:87], %[va] offset:2696\n\t"
  "buffer_load_dwordx4 a[8:11], %[vl16], %[srd], s56 offen offset:2048 sc1\n\t"
  "v_mfma_f32_16x16x32_bf16 v[56:59], v[88:91], v[128:131], v[56:59]\n\t"
  "ds_read_b64 v[88:89], %[va] offset:3200\n\t"
  "ds_read_b64 v[90:91], %[va] offset:3208\n\t"
  "v_mfma_f32_16x16x32_bf16 v[60:63], v[92:95], v[128:131], v[60:63]\n\t"
  "s_waitcnt lgkmcnt(8)\n\t"
  "ds_read_b64 v[92:93], %[va] offset:3712\n\t"
  "v_mfma_f32_16x16x32_bf16 v[60:63], v[64:67], v[124:127], v[60:63]\n\t"
  "ds_read_b128 v[156:159], %[vb3] offset:15360\n\t"
  "ds_read_b64 v[94:95], %[va] offset:3720\n\t"
  "v_mfma_f32_16x16x32_bf16 v[56:59], v[64:67], v[120:123], v[56:59]\n\t"
  "buffer_load_dwordx4 a[12:15], %[vl16], %[srd], s56 offen offset:3072 sc1\n\t"
  "v_mfma_f32_16x16x32_bf16 v[52:55], v[64:67], v[116:119], v[52:55]\n\t"
  "v_mfma_f32_16x16x32_bf16 v[48:51], v[64:67], v[112:115], v[48:51]\n\t"
  "ds_read_b128 v[152:155], %[vb3] offset:13312\n\t"
  "v_mfma_f32_16x16x32_bf16 v[44:47], v[64:67], v[108:111], v[44:47]\n\t"
  "s_add_u32 s56, s56, 4096\n\t"
  "v_mfma_f32_16x16x32_bf16 v[40:43], v[64:67], v[104:107], v[40:43]\n\t"
  "v_mfma_f32_16x16x32_bf16 v[36:39], v[64:67], v[100:103], v[36:39]\n\t"
  "ds_read_b128 v[148:151], %[vb3] offset:11264\n\t"
  "v_mfma_f32_16x16x32_bf16 v[32:35], v[64:67], v[96:99], v[32:35]\n\t"
  "ds_read_b64 v[64:65], %[va] offset:192\n\t"
  "ds_read_b64 v[66:67], %[va] offset:200\n\t"
  "v_mfma_f32_16x16x32_bf16 v[60:63], v[68:71], v[120:123], v[60:63]\n\t"
  "buffer_load_dwordx4 a[16:19], %[vl16], %[srd], s56 offen offset:0 sc1\n\t"
  "v_mfma_f32_16x16x32_bf16 v[56:59], v[68:71], v[116:119], v[56:59]\n\t"
  "ds_read_b128 v[144:147], %[vb3] offset:9216\n\t"
  "v_mfma_f32_16x16x32_bf16 v[52:55], v[68:71], v[112:115], v[52:55]\n\t"
  "v_mfma_f32_16x16x32_bf16 v[48:51], v[68:71], v[108:111], v[48:51]\n\t"
  "buffer_load_dwordx4 a[20:23], %[vl16], %[srd], s56 offen offset:1024 sc1\n\t"
  "v_mfma_f32_16x16x32_bf16 v[44:47], v[68:71], v[104:107], v[44:47]\n\t"
  "ds_read_b128 v[140:143], %[vb3] offset:7168\n\t"
  "v_mfma_f32_16x16x32_bf16 v[40:43], v[68:71], v[100:103], v[40:43]\n\t"
  "v_mfma_f32_16x16x32_bf16 v[36:39], v[68:71], v[96:99], v[36:39]\n\t"
  "s_waitcnt lgkmcnt(11)\n\t"
  "ds_read_b64 v[68:69], %[va] offset:704\n\t"
  "v_mfma_f32_16x16x32_bf16 v[60:63], v[72:75], v[116:119], v[60:63]\n\t"
  "ds_read_b128 v[136:139], %[vb3] offset:5120\n\t"
  "ds_read_b64 v[70:71], %[va] offset:712\n\t"
  "v_mfma_f32_16x16x32_bf16 v[56:59], v[72:75], v[112:115], v[56:59]\n\t"
  "buffer_load_dwordx4 a[24:27], %[vl16], %[srd], s56 offen offset:2048 sc1\n\t"
  "v_mfma_f32_16x16x32_bf16 v[52:55], v[72:75], v[108:111], v[52:55]\n\t"
  "v_mfma_f32_16x16x32_bf16 v[48:51], v[72:75], v[104:107], v[48:51]\n\t"
  "ds_read_b128 v[132:135], %[vb3] offset:3072\n\t"
  "buffer_load_dwordx4 a[28:31], %[vl16], %[srd], s56 offen offset:3072 sc1\n\t"
  "v_mfma_f32_16x16x32_bf16 v[44:47], v[72:75], v[100:103], v[44:47]\n\t"
  "v_mfma_f32_16x16x32_bf16 v[40:43], v[72:75], v[96:99], v[40:43]\n\t"
  "ds_read_b64 v[72:73], %[va] offset:1216\n\t"
  "ds_read_b64 v[74:75], %[va] offset:1224\n\t"
  "v_mfma_f32_16x16x32_bf16 v[60:63], v[76:79], v[112:115], v[60:63]\n\t"
  "ds_read_b128 v[128:131], %[vb3] offset:1024\n\t"
  "s_add_u32 s56, s56, 4096\n\t"
  "v_mfma_f32_16x16x32_bf16 v[56:59], v[76:79], v[108:111], v[56:59]\n\t"
  "v_mfma_f32_16x16x32_bf16 v[52:55], v[76:79], v[104:107], v[52:55]\n\t"
  "v_mfma_f32_16x16x32_bf16 v[48:51], v[76:79], v[100:103], v[48:51]\n\t"
  "v_mfma_f32_16x16x32_bf16 v[44:47], v[76:79], v[96:99], v[44:47]\n\t"
  "ds_read_b64 v[76:77], %[va] offset:1728\n\t"
  "ds_read_b64 v[78:79], %[va] offset:1736\n\t"
  "v_mfma_f32_16x16x32_bf16 v[60:63], v[80:83], v[108:111], v[60:63]\n\t"
  "buffer_load_dwordx4 a[32:35], %[vl16], %[srd], s56 offen offset:0 sc1\n\t"
  "v_mfma_f32_16x16x32_bf16 v[56:59], v[80:83], v[104:107], v[56:59]\n\t"
  "v_mfma_f32_16x16x32_bf16 v[52:55], v[80:83], v[100:103], v[52:55]\n\t"
  "buffer_load_dwordx4 a[36:39], %[vl16], %[srd], s56 offen offset:1024 sc1\n\t"
  "v_mfma_f32_16x16x32_bf16 v[48:51], v[80:83], v[96:99], v[48:51]\n\t"
  "ds_read_b64 v[80:81], %[va] offset:2240\n\t"
  "ds_read_b64 v[82:83], %[va] offset:2248\n\t"
  "v_mfma_f32_16x16x32_bf16 v[60:63], v[84:87], v[104:107], v[60:63]\n\t"
  "v_mfma_f32_16x16x32_bf16 v[56:59], v[84:87], v[100:103], v[56:59]\n\t"
  "v_mfma_f32_16x16x32_bf16 v[52:55], v[84:87], v[96:99], v[52:55]\n\t"
  "s_waitcnt lgkmcnt(15)\n\t"
  "ds_read_b64 v[84:85], %[va] offset:2752\n\t"
  "v_mfma_f32_16x16x32_bf16 v[60:63], v[88:91], v[100:103], v[60:63]\n\t"
  "ds_read_b64 v[86:87], %[va] offset:2760\n\t"
  "buffer_load_dwordx4 a[40:43], %[vl16], %[srd], s56 offen offset:2048 sc1\n\t"
  "v_mfma_f32_16x16x32_bf16 v[56:59], v[88:91], v[96:99], v[56:59]\n\t"
  "ds_read_b64 v[88:89], %[va] offset:3264\n\t"
  "ds_read_b64 v[90:91], %[va] offset:3272\n\t"
  "v_mfma_f32_16x16x32_bf16 v[60:63], v[92:95], v[96:99], v[60:63]\n\t"
  "s_waitcnt lgkmcnt(8)\n\t"
  "ds_read_b64 v[92:93], %[va] offset:3776\n\t"
  "v_mfma_f32_16x16x32_bf16 v[60:63], v[64:67], v[156:159], v[60:63]\n\t"
  "ds_read_b128 v[124:127], %[vb0] offset:14336\n\t"
  "ds_read_b64 v[94:95], %[va] offset:3784\n\t"
  "v_mfma_f32_16x16x32_bf16 v[56:59], v[64:67], v[152:155], v[56:59]\n\t"
  "buffer_load_dwordx4 a[44:47], %[vl16], %[srd], s56 offen offset:3072 sc1\n\t"
  "v_mfma_f32_16x16x32_bf16 v[52:55], v[64:67], v[148:151], v[52:55]\n\t"
  "v_mfma_f32_16x16x32_bf16 v[48:51], v[64:67], v[144:147], v[48:51]\n\t"
  "ds_read_b128 v[120:123], %[vb0] offset:12288\n\t"
  "s_add_u32 s56, s56, 4096\n\t"
  "v_mfma_f32_16x16x32_bf16 v[44:47], v[64:67], v[140:143], v[44:47]\n\t"
  "v_mfma_f32_16x16x32_bf16 v[40:43], v[64:67], v[136:139], v[40:43]\n\t"
  "v_mfma_f32_16x16x32_bf16 v[36:39], v[64:67], v[132:135], v[36:39]\n\t"
  "ds_read_b128 v[116:119], %[vb0] offset:10240\n\t"
  "buffer_load_dwordx4 a[48:51], %[vl16], %[srd], s56 offen offset:0 sc1\n\t"
  "v_mfma_f32_16x16x32_bf16 v[32:35], v[64:67], v[128:131], v[32:35]\n\t"
  "ds_read_b64 v[64:65], %[va] offset:256\n\t"
  "ds_read_b64 v[66:67], %[va] offset:264\n\t"
  "v_mfma_f32_16x16x32_bf16 v[60:63], v[68:71], v[152:155], v[60:63]\n\t"
  "v_mfma_f32_16x16x32_bf16 v[56:59], v[68:71], v[148:151], v[56:59]\n\t"
  "ds_read_b128 v[112:115], %[vb0] offset:8192\n\t"
  "v_mfma_f32_16x16x32_bf16 v[52:55], v[68:71], v[144:147], v[52:55]\n\t"
  "buffer_load_dwordx4 a[52:55], %[vl16], %[srd], s56 offen offset:1024 sc1\n\t"
  "v_mfma_f32_16x16x32_bf16 v[48:51], v[68:71], v[140:143], v[48:51]\n\t"
  "v_mfma_f32_16x16x32_bf16 v[44:47], v[68:71], v[136:139], v[44:47]\n\t"
  "ds_read_b128 v[108:111], %[vb0] offset:6144\n\t"
  "v_mfma_f32_16x16x32_bf16 v[40:43], v[68:71], v[132:135], v[40:43]\n\t"
  "v_mfma_f32_16x16x32_bf16 v[36:39], v[68:71], v[128:131], v[36:39]\n\t"
  "s_waitcnt lgkmcnt(11)\n\t"
  "ds_read_b64 v[68:69], %[va] offset:768\n\t"
  "v_mfma_f32_16x16x32_bf16 v[60:63], v[72:75], v[148:151], v[60:63]\n\t"
  "ds_read_b128 v[104:107], %[vb0] offset:4096\n\t"
  "ds_read_b64 v[70:71], %[va] offset:776\n\t"
  "v_mfma_f32_16x16x32_bf16 v[56:59], v[72:75], v[144:147], v[56:59]\n\t"
  "v_mfma_f32_16x16x32_bf16 v[52:55], v[72:75], v[140:143], v[52:55]\n\t"
  "v_mfma_f32_16x16x32_bf16 v[48:51], v[72:75], v[136:139], v[48:51]\n\t"
  "ds_read_b128 v[100:103], %[vb0] offset:2048\n\t"
  "v_mfma_f32_16x16x32_bf16 v[44:47], v[72:75], v[132:135], v[44:47]\n\t"
  "v_mfma_f32_16x16x32_bf16 v[40:43], v[72:75], v[128:131], v[40:43]\n\t"
  "ds_read_b64 v[72:73], %[va] offset:1280\n\t"
  "ds_read_b64 v[74:75], %[va] offset:1288\n\t"
  "v_mfma_f32_16x16x32_bf16 v[60:63], v[76:79], v[144:147], v[60:63]\n\t"
  "ds_read_b128 v[96:99], %[vb0] offset:0\n\t"
  "v_mfma_f32_16x16x32_bf16 v[56:59], v[76:79], v[140:143], v[56:59]\n\t"
  "v_mfma_f32_16x16x32_bf16 v[52:55], v[76:79], v[136:139], v[52:55]\n\t"
  "v_mfma_f32_16x16x32_bf16 v[48:51], v[76:79], v[132:135], v[48:51]\n\t"
  "buffer_load_dwordx2 v[162:163], %[vf2], %[srd], 0 offen sc1\n\t"
  "v_mfma_f32_16x16x32_bf16 v[44:47], v[76:79], v[128:131], v[44:47]\n\t"
  "ds_read_b64 v[76:77], %[va] offset:1792\n\t"
  "ds_read_b64 v[78:79], %[va] offset:1800\n\t"
  "v_mfma_f32_16x16x32_bf16 v[60:63], v[80:83], v[140:143], v[60:63]\n\t"
  "v_mfma_f32_16x16x32_bf16 v[56:59], v[80:83], v[136:139], v[56:59]\n\t"
  "v_mfma_f32_16x16x32_bf16 v[52:55], v[80:83], v[132:135], v[52:55]\n\t"
  "v_mfma_f32_16x16x32_bf16 v[48:51], v[80:83], v[128:131], v[48:51]\n\t"
  "ds_read_b64 v[80:81], %[va] offset:2304\n\t"
  "ds_read_b64 v[82:83], %[va] offset:2312\n\t"
  "v_mfma_f32_16x16x32_bf16 v[60:63], v[84:87], v[136:139], v[60:63]\n\t"
  "v_mfma_f32_16x16x32_bf16 v[56:59], v[84:87], v[132:135], v[56:59]\n\t"
  "v_mfma_f32_16x16x32_bf16 v[52:55], v[84:87], v[128:131], v[52:55]\n\t"
  "s_waitcnt lgkmcnt(15)\n\t"
  "ds_read_b64 v[84:85], %[va] offset:2816\n\t"
  "v_mfma_f32_16x16x32_bf16 v[60:63], v[88:91], v[132:135], v[60:63]\n\t"
  "ds_read_b64 v[86:87], %[va] offset:2824\n\t"
  "v_mfma_f32_16x16x32_bf16 v[56:59], v[88:91], v[128:131], v[56:59]\n\t"
  "ds_read_b64 v[88:89], %[va] offset:3328\n\t"
  "ds_read_b64 v[90:91], %[va] offset:3336\n\t"
  "v_mfma_f32_16x16x32_bf16 v[60:63], v[92:95], v[128:131], v[60:63]\n\t"
  "s_waitcnt lgkmcnt(8)\n\t"
  "ds_read_b64 v[92:93], %[va] offset:3840\n\t"
  "v_mfma_f32_16x16x32_bf16 v[60:63], v[64:67], v[124:127], v[60:63]\n\t"
  "ds_read_b128 v[156:159], %[vb1] offset:14336\n\t"
  "ds_read_b64 v[94:95], %[va] offset:3848\n\t"
  "v_mfma_f32_16x16x32_bf16 v[56:59], v[64:67], v[120:123], v[56:59]\n\t"
  "v_mfma_f32_16x16x32_bf16 v[52:55], v[64:67], v[116:119], v[52:55]\n\t"
  "v_mfma_f32_16x16x32_bf16 v[48:51], v[64:67], v[112:115], v[48:51]\n\t"
  "ds_read_b128 v[152:155], %[vb1] offset:12288\n\t"
  "v_mfma_f32_16x16x32_bf16 v[44:47], v[64:67], v[108:111], v[44:47]\n\t"
  "v_mfma_f32_16x16x32_bf16 v[40:43], v[64:67], v[104:107], v[40:43]\n\t"
  "v_mfma_f32_16x16x32_bf16 v[36:39], v[64:67], v[100:103], v[36:39]\n\t"
  "ds_read_b128 v[148:151], %[vb1] offset:10240\n\t"
  "v_mfma_f32_16x16x32_bf16 v[32:35], v[64:67], v[96:99], v[32:35]\n\t"
  "ds_read_b64 v[64:65], %[va] offset:320\n\t"
  "ds_read_b64 v[66:67], %[va] offset:328\n\t"
  "v_mfma_f32_16x16x32_bf16 v[60:63], v[68:71], v[120:123], v[60:63]\n\t"
  "buffer_load_dwordx2 v[250:251], %[vfw], %[srd], 0 offen sc1\n\t"
  "v_mfma_f32_16x16x32_bf16 v[56:59], v[68:71], v[116:119], v[56:59]\n\t"
  "ds_read_b128 v[144:147], %[vb1] offset:8192\n\t"
  "v_mfma_f32_16x16x32_bf16 v[52:55], v[68:71], v[112:115], v[52:55]\n\t"
  "v_mfma_f32_16x16x32_bf16 v[48:51], v[68:71], v[108:111], v[48:51]\n\t"
  "v_mfma_f32_16x16x32_bf16 v[44:47], v[68:71], v[104:107], v[44:47]\n\t"
  "ds_read_b128 v[140:143], %[vb1] offset:6144\n\t"
  "v_mfma_f32_16x16x32_bf16 v[40:43], v[68:71], v[100:103], v[40:43]\n\t"
  "v_mfma_f32_16x16x32_bf16 v[36:39], v[68:71], v[96:99], v[36:39]\n\t"
  "s_waitcnt lgkmcnt(11)\n\t"
  "ds_read_b64 v[68:69], %[va] offset:832\n\t"
  "v_mfma_f32_16x16x32_bf16 v[60:63], v[72:75], v[116:119], v[60:63]\n\t"
  "ds_read_b128 v[136:139], %[vb1] offset:4096\n\t"
  "ds_read_b64 v[70:71], %[va] offset:840\n\t"
  "v_mfma_f32_16x16x32_bf16 v[56:59], v[72:75], v[112:115], v[56:59]\n\t"
  "v_mfma_f32_16x16x32_bf16 v[52:55], v[72:75], v[108:111], v[52:55]\n\t"
  "v_mfma_f32_16x16x32_bf16 v[48:51], v[72:75], v[104:107], v[48:51]\n\t"
  "ds_read_b128 v[132:135], %[vb1] offset:2048\n\t"
  "v_mfma_f32_16x16x32_bf16 v[44:47], v[72:75], v[100:103], v[44:47]\n\t"
  "v_mfma_f32_16x16x32_bf16 v[40:43], v[72:75], v[96:99], v[40:43]\n\t"
  "ds_read_b64 v[72:73], %[va] offset:1344\n\t"
  "ds_read_b64 v[74:75], %[va] offset:1352\n\t"
  "v_mfma_f32_16x16x32_bf16 v[60:63], v[76:79], v[112:115], v[60:63]\n\t"
  "ds_read_b128 v[128:131], %[vb1] offset:0\n\t"
  "v_mfma_f32_16x16x32_bf16 v[56:59], v[76:79], v[108:111], v[56:59]\n\t"
  "v_mfma_f32_16x16x32_bf16 v[52:55], v[76:79], v[104:107], v[52:55]\n\t"
  "v_mfma_f32_16x16x32_bf16 v[48:51], v[76:79], v[100:103], v[48:51]\n\t"
  "v_mfma_f32_16x16x32_bf16 v[44:47], v[76:79], v[96:99], v[44:47]\n\t"
  "ds_read_b64 v[76:77], %[va] offset:1856\n\t"
  "ds_read_b64 v[78:79], %[va] offset:1864\n\t"
  "v_mfma_f32_16x16x32_bf16 v[60:63], v[80:83], v[108:111], v[60:63]\n\t"
  "v_mfma_f32_16x16x32_bf16 v[56:59], v[80:83], v[104:107], v[56:59]\n\t"
  "v_mfma_f32_16x16x32_bf16 v[52:55], v[80:83], v[100:103], v[52:55]\n\t"
  "s_waitcnt vmcnt(1)\n\t"
  "v_cmp_ne_u64 vcc, v[252:253], v[162:163]\n\t"
  "v_mfma_f32_16x16x32_bf16 v[48:51], v[80:83], v[96:99], v[48:51]\n\t"
  "ds_read_b64 v[80:81], %[va] offset:2368\n\t"
  "ds_read_b64 v[82:83], %[va] offset:2376\n\t"
  "v_mfma_f32_16x16x32_bf16 v[60:63], v[84:87], v[104:107], v[60:63]\n\t"
  "s_cbranch_vccnz La2_slow%=\n\t"
  "La2_back%=:\n\t"
  "v_mfma_f32_16x16x32_bf16 v[56:59], v[84:87], v[100:103], v[56:59]\n\t"
  "buffer_load_dword v244, %[vmod], %[srd], 0 offen sc1\n\t"
  "s_add_u32 m0, s45, 84992\n\t"
  "v_mfma_f32_16x16x32_bf16 v[52:55], v[84:87], v[96:99], v[52:55]\n\t"
  "s_waitcnt lgkmcnt(15)\n\t"
  "ds_read_b64 v[84:85], %[va] offset:2880\n\t"
  "v_mfma_f32_16x16x32_bf16 v[60:63], v[88:91], v[100:103], v[60:63]\n\t"
  "ds_read_b64 v[86:87], %[va] offset:2888\n\t"
  "s_add_u32 s44, %[sKV], 2621440\n\t"
  "v_mfma_f32_16x16x32_bf16 v[56:59], v[88:91], v[96:99], v[56:59]\n\t"
  "ds_read_b64 v[88:89], %[va] offset:3392\n\t"
  "ds_read_b64 v[90:91], %[va] offset:3400\n\t"
  "v_mfma_f32_16x16x32_bf16 v[60:63], v[92:95], v[96:99], v[60:63]\n\t"
  "s_waitcnt lgkmcnt(8)\n\t"
  "ds_read_b64 v[92:93], %[va] offset:3904\n\t"
  "v_mfma_f32_16x16x32_bf16 v[60:63], v[64:67], v[156:159], v[60:63]\n\t"
  "ds_read_b128 v[124:127], %[vb2] offset:14336\n\t"
  "ds_read_b64 v[94:95], %[va] offset:3912\n\t"
  "v_mfma_f32_16x16x32_bf16 v[56:59], v[64:67], v[152:155], v[56:59]\n\t"
  "buffer_load_dwordx4 %[vkv], %[srd], s44 offen sc1 lds\n\t"
  "s_add_u32 m0, s45, 86016\n\t"
  "v_mfma_f32_16x16x32_bf16 v[52:55], v[64:67], v[148:151], v[52:55]\n\t"
  "s_add_u32 s44, %[sKV], 2785280\n\t"
  "buffer_load_dwordx4 %[vkv], %[srd], s44 offen sc1 lds\n\t"
  "v_mfma_f32_16x16x32_bf16 v[48:51], v[64:67], v[144:147], v[48:51]\n\t"
  "ds_read_b128 v[120:123], %[vb2] offset:12288\n\t"
  "s_add_u32 m0, s45, 87040\n\t"
  "v_mfma_f32_16x16x32_bf16 v[44:47], v[64:67], v[140:143], v[44:47]\n\t"
  "s_add_u32 s44, %[sKV], 2949120\n\t"
  "buffer_load_dwordx4 %[vkv], %[srd], s44 offen sc1 lds\n\t"
  "v_mfma_f32_16x16x32_bf16 v[40:43], v[64:67], v[136:139], v[40:43]\n\t"
  "s_add_u32 m0, s45, 88064\n\t"
  "s_add_u32 s44, %[sKV], 3112960\n\t"
  "v_mfma_f32_16x16x32_bf16 v[36:39], v[64:67], v[132:135], v[36:39]\n\t"
  "ds_read_b128 v[116:119], %[vb2] offset:10240\n\t"
  "buffer_load_dwordx4 %[vkv], %[srd], s44 offen sc1 lds\n\t"
  "v_mfma_f32_16x16x32_bf16 v[32:35], v[64:67], v[128:131], v[32:35]\n\t"
  "ds_read_b64 v[64:65], %[va] offset:384\n\t"
  "ds_read_b64 v[66:67], %[va] offset:392\n\t"
  "v_mfma_f32_16x16x32_bf16 v[60:63], v[68:71], v[152:155], v[60:63]\n\t"
  "s_add_u32 m0, s45, 101376\n\t"
  "s_add_u32 s44, s45, 847872\n\t"
  "v_mfma_f32_16x16x32_bf16 v[56:59], v[68:71], v[148:151], v[56:59]\n\t"
  "ds_read_b128 v[112:115], %[vb2] offset:8192\n\t"
  "buffer_load_dwordx4 %[vl16], %[srd], s44 offen offset:0 sc1 lds\n\t"
  "v_mfma_f32_16x16x32_bf16 v[52:55], v[68:71], v[144:147], v[52:55]\n\t"
  "buffer_load_dwordx4 %[vl16], %[srd], s44 offen offset:1024 sc1 lds\n\t"
  "buffer_load_dwordx4 %[vl16], %[srd], s44 offen offset:2048 sc1 lds\n\t"
  "v_mfma_f32_16x16x32_bf16 v[48:51], v[68:71], v[140:143], v[48:51]\n\t"
  "buffer_load_dwordx4 %[vl16], %[srd], s44 offen offset:3072 sc1 lds\n\t"
  "v_mfma_f32_16x16x32_bf16 v[44:47], v[68:71], v[136:139], v[44:47]\n\t"
  "ds_read_b128 v[108:111], %[vb2] offset:6144\n\t"
  "s_waitcnt vmcnt(9)\n\t"
  "v_cmp_eq_u64 s[54:55], v[252:253], v[250:251]\n\t"
  "v_mfma_f32_16x16x32_bf16 v[40:43], v[68:71], v[132:135], v[40:43]\n\t"
  "v_mfma_f32_16x16x32_bf16 v[36:39], v[68:71], v[128:131], v[36:39]\n\t"
  "s_waitcnt lgkmcnt(11)\n\t"
  "ds_read_b64 v[68:69], %[va] offset:896\n\t"
  "v_mfma_f32_16x16x32_bf16 v[60:63], v[72:75], v[148:151], v[60:63]\n\t"
  "ds_read_b128 v[104:107], %[vb2] offset:4096\n\t"
  "ds_read_b64 v[70:71], %[va] offset:904\n\t"
  "v_mfma_f32_16x16x32_bf16 v[56:59], v[72:75], v[144:147], v[56:59]\n\t"
  "s_cmp_eq_u64 s[54:55], -1\n\t"
  "s_cselect_b32 s53, 2, 0\n\t"
  "s_cselect_b32 s57, %[sW1], 0x70000000\n\t"
  "v_mfma_f32_16x16x32_bf16 v[52:55], v[72:75], v[140:143], v[52:55]\n\t"
  "s_or_b32 %[wdone], %[wdone], s53\n\t"
  "v_mfma_f32_16x16x32_bf16 v[48:51], v[72:75], v[136:139], v[48:51]\n\t"
  "ds_read_b128 v[100:103], %[vb2] offset:2048\n\t"
  "s_add_u32 s57, s57, 14336\n\t"
  "v_mfma_f32_16x16x32_bf16 v[44:47], v[72:75], v[132:135], v[44:47]\n\t"
  "v_mfma_f32_16x16x32_bf16 v[40:43], v[72:75], v[128:131], v[40:43]\n\t"
  "ds_read_b64 v[72:73], %[va] offset:1408\n\t"
  "ds_read_b64 v[74:75], %[va] offset:1416\n\t"
  "v_mfma_f32_16x16x32_bf16 v[60:63], v[76:79], v[144:147], v[60:63]\n\t"
  "ds_read_b128 v[96:99], %[vb2] offset:0\n\t"
  "buffer_load_dwordx4 a[56:59], %[vl16], %[srd], s57 offen offset:0 sc1\n\t"
  "v_mfma_f32_16x16x32_bf16 v[56:59], v[76:79], v[140:143], v[56:59]\n\t"
  "v_mfma_f32_16x16x32_bf16 v[52:55], v[76:79], v[136:139], v[52:55]\n\t"
  "v_mfma_f32_16x16x32_bf16 v[48:51], v[76:79], v[132:135], v[48:51]\n\t"
  "buffer_load_dwordx4 a[60:63], %[vl16], %[srd], s57 offen offset:1024 sc1\n\t"
  "v_mfma_f32_16x16x32_bf16 v[44:47], v[76:79], v[128:131], v[44:47]\n\t"
  "ds_read_b64 v[76:77], %[va] offset:1920\n\t"
  "ds_read_b64 v[78:79], %[va] offset:1928\n\t"
  "v_mfma_f32_16x16x32_bf16 v[60:63], v[80:83], v[140:143], v[60:63]\n\t"
  "v_mfma_f32_16x16x32_bf16 v[56:59], v[80:83], v[136:139], v[56:59]\n\t"
  "v_mfma_f32_16x16x32_bf16 v[52:55], v[80:83], v[132:135], v[52:55]\n\t"
  "buffer_load_dwordx4 a[64:67], %[vl16], %[srd], s57 offen offset:2048 sc1\n\t"
  "v_mfma_f32_16x16x32_bf16 v[48:51], v[80:83], v[128:131], v[48:51]\n\t"
  "ds_read_b64 v[80:81], %[va] offset:2432\n\t"
  "ds_read_b64 v[82:83], %[va] offset:2440\n\t"
  "v_mfma_f32_16x16x32_bf16 v[60:63], v[84:87], v[136:139], v[60:63]\n\t"
  "v_mfma_f32_16x16x32_bf16 v[56:59], v[84:87], v[132:135], v[56:59]\n\t"
  "buffer_load_dwordx4 a[68:71], %[vl16], %[srd], s57 offen offset:3072 sc1\n\t"
  "v_mfma_f32_16x16x32_bf16 v[52:55], v[84:87], v[128:131], v[52:55]\n\t"
  "s_waitcnt lgkmcnt(15)\n\t"
  "ds_read_b64 v[84:85], %[va] offset:2944\n\t"
  "v_mfma_f32_16x16x32_bf16 v[60:63], v[88:91], v[132:135], v[60:63]\n\t"
  "ds_read_b64 v[86:87], %[va] offset:2952\n\t"
  "v_mfma_f32_16x16x32_bf16 v[56:59], v[88:91], v[128:131], v[56:59]\n\t"
  "ds_read_b64 v[88:89], %[va] offset:3456\n\t"
  "ds_read_b64 v[90:91], %[va] offset:3464\n\t"
  "v_mfma_f32_16x16x32_bf16 v[60:63], v[92:95], v[128:131], v[60:63]\n\t"
  "s_waitcnt lgkmcnt(8)\n\t"
  "ds_read_b64 v[92:93], %[va] offset:3968\n\t"
  "v_mfma_f32_16x16x32_bf16 v[60:63], v[64:67], v[124:127], v[60:63]\n\t"
  "ds_read_b128 v[156:159], %[vb3] offset:14336\n\t"
  "ds_read_b64 v[94:95], %[va] offset:3976\n\t"
  "v_mfma_f32_16x16x32_bf16 v[56:59], v[64:67], v[120:123], v[56:59]\n\t"
  "s_add_u32 s57, s57, 4096\n\t"
  "v_mfma_f32_16x16x32_bf16 v[52:55], v[64:67], v[116:119], v[52:55]\n\t"
  "buffer_load_dwordx4 a[72:75], %[vl16], %[srd], s57 offen offset:0 sc1\n\t"
  "v_mfma_f32_16x16x32_bf16 v[48:51], v[64:67], v[112:115], v[48:51]\n\t"
  "ds_read_b128 v[152:155], %[vb3] offset:12288\n\t"
  "v_mfma_f32_16x16x32_bf16 v[44:47], v[64:67], v[108:111], v[44:47]\n\t"
  "v_mfma_f32_16x16x32_bf16 v[40:43], v[64:67], v[104:107], v[40:43]\n\t"
  "buffer_load_dwordx4 a[76:79], %[vl16], %[srd], s57 offen offset:1024 sc1\n\t"
  "v_mfma_f32_16x16x32_bf16 v[36:39], v[64:67], v[100:103], v[36:39]\n\t"
  "ds_read_b128 v[148:151], %[vb3] offset:10240\n\t"
  "v_mfma_f32_16x16x32_bf16 v[32:35], v[64:67], v[96:99], v[32:35]\n\t"
  "ds_read_b64 v[64:65], %[va] offset:448\n\t"
  "ds_read_b64 v[66:67], %[va] offset:456\n\t"
  "v_mfma_f32_16x16x32_bf16 v[60:63], v[68:71], v[120:123], v[60:63]\n\t"
  "buffer_load_dwordx4 a[80:83], %[vl16], %[srd], s57 offen offset:2048 sc1\n\t"
  "v_mfma_f32_16x16x32_bf16 v[56:59], v[68:71], v[116:119], v[56:59]\n\t"
  "ds_read_b128 v[144:147], %[vb3] offset:8192\n\t"
  "v_mfma_f32_16x16x32_bf16 v[52:55], v[68:71], v[112:115], v[52:55]\n\t"
  "v_mfma_f32_16x16x32_bf16 v[48:51], v[68:71], v[108:111], v[48:51]\n\t"
  "v_mfma_f32_16x16x32_bf16 v[44:47], v[68:71], v[104:107], v[44:47]\n\t"
  "ds_read_b128 v[140:143], %[vb3] offset:6144\n\t"
  "buffer_load_dwordx4 a[84:87], %[vl16], %[srd], s57 offen offset:3072 sc1\n\t"
  "v_mfma_f32_16x16x32_bf16 v[40:43], v[68:71], v[100:103], v[40:43]\n\t"
  "v_mfma_f32_16x16x32_bf16 v[36:39], v[68:71], v[96:99], v[36:39]\n\t"
  "s_waitcnt lgkmcnt(11)\n\t"
  "ds_read_b64 v[68:69], %[va] offset:960\n\t"
  "v_mfma_f32_16x16x32_bf16 v[60:63], v[72:75], v[116:119], v[60:63]\n\t"
  "ds_read_b128 v[136:139], %[vb3] offset:4096\n\t"
  "ds_read_b64 v[70:71], %[va] offset:968\n\t"
  "v_mfma_f32_16x16x32_bf16 v[56:59], v[72:75], v[112:115], v[56:59]\n\t"
  "s_add_u32 s57, s57, 4096\n\t"
  "v_mfma_f32_16x16x32_bf16 v[52:55], v[72:75], v[108:111], v[52:55]\n\t"
  "v_mfma_f32_16x16x32_bf16 v[48:51], v[72:75], v[104:107], v[48:51]\n\t"
  "ds_read_b128 v[132:135], %[vb3] offset:2048\n\t"
  "buffer_load_dwordx4 a[88:91], %[vl16], %[srd], s57 offen offset:0 sc1\n\t"
  "v_mfma_f32_16x16x32_bf16 v[44:47], v[72:75], v[100:103], v[44:47]\n\t"
  "v_mfma_f32_16x16x32_bf16 v[40:43], v[72:75], v[96:99], v[40:43]\n\t"
  "ds_read_b64 v[72:73], %[va] offset:1472\n\t"
  "ds_read_b64 v[74:75], %[va] offset:1480\n\t"
  "v_mfma_f32_16x16x32_bf16 v[60:63], v[76:79], v[112:115], v[60:63]\n\t"
  "ds_read_b128 v[128:131], %[vb3] offset:0\n\t"
  "v_mfma_f32_16x16x32_bf16 v[56:59], v[76:79], v[108:111], v[56:59]\n\t"
  "buffer_load_dwordx4 a[92:95], %[vl16], %[srd], s57 offen offset:1024 sc1\n\t"
  "v_mfma_f32_16x16x32_bf16 v[52:55], v[76:79], v[104:107], v[52:55]\n\t"
  "v_mfma_f32_16x16x32_bf16 v[48:51], v[76:79], v[100:103], v[48:51]\n\t"
  "v_mfma_f32_16x16x32_bf16 v[44:47], v[76:79], v[96:99], v[44:47]\n\t"
  "ds_read_b64 v[76:77], %[va] offset:1984\n\t"
  "ds_read_b64 v[78:79], %[va] offset:1992\n\t"
  "v_mfma_f32_16x16x32_bf16 v[60:63], v[80:83], v[108:111], v[60:63]\n\t"
  "buffer_load_dwordx4 a[96:99], %[vl16], %[srd], s57 offen offset:2048 sc1\n\t"
  "v_mfma_f32_16x16x32_bf16 v[56:59], v[80:83], v[104:107], v[56:59]\n\t"
  "s_waitcnt vmcnt(19)\n\t"
  "v_add_f32_dpp v244, v244, v244 quad_perm:[1,0,3,2] row_mask:0xf bank_mask:0xf\n\t"
  "v_mfma_f32_16x16x32_bf16 v[52:55], v[80:83], v[100:103], v[52:55]\n\t"
  "v_mfma_f32_16x16x32_bf16 v[48:51], v[80:83], v[96:99], v[48:51]\n\t"
  "ds_read_b64 v[80:81], %[va] offset:2496\n\t"
  "ds_read_b64 v[82:83], %[va] offset:2504\n\t"
  "v_mfma_f32_16x16x32_bf16 v[60:63], v[84:87], v[104:107], v[60:63]\n\t"
  "buffer_load_dwordx4 a[100:103], %[vl16], %[srd], s57 offen offset:3072 sc1\n\t"
  "v_add_f32_dpp v244, v244, v244 quad_perm:[2,3,0,1] row_mask:0xf bank_mask:0xf\n\t"
  "v_mfma_f32_16x16x32_bf16 v[56:59], v[84:87], v[100:103], v[56:59]\n\t"
  "v_cvt_pk_bf16_f32 v244, 0, v244\n\t"
  "v_add_f32 v245, 1.0, v244\n\t"
  "v_mfma_f32_16x16x32_bf16 v[52:55], v[84:87], v[96:99], v[52:55]\n\t"
  "s_waitcnt lgkmcnt(15)\n\t"
  "ds_read_b64 v[84:85], %[va] offset:3008\n\t"
  "v_mfma_f32_16x16x32_bf16 v[60:63], v[88:91], v[100:103], v[60:63]\n\t"
  "ds_read_b64 v[86:87], %[va] offset:3016\n\t"
  "s_add_u32 s57, s57, 4096\n\t"
  "v_mfma_f32_16x16x32_bf16 v[56:59], v[88:91], v[96:99], v[56:59]\n\t"
  "ds_read_b64 v[88:89], %[va] offset:3520\n\t"
  "ds_read_b64 v[90:91], %[va] offset:3528\n\t"
  "v_mfma_f32_16x16x32_bf16 v[60:63], v[92:95], v[96:99], v[60:63]\n\t"
  "s_waitcnt lgkmcnt(8)\n\t"
  "ds_read_b64 v[92:93], %[va] offset:4032\n\t"
  "v_mfma_f32_16x16x32_bf16 v[60:63], v[64:67], v[156:159], v[60:63]\n\t"
  "ds_read_b64 v[94:95], %[va] offset:4040\n\t"
  "buffer_load_dwordx4 a[104:107], %[vl16], %[srd], s57 offen offset:0 sc1\n\t"
  "v_mfma_f32_16x16x32_bf16 v[56:59], v[64:67], v[152:155], v[56:59]\n\t"
  "v_cvt_pk_bf16_f32 v245, 0, v245\n\t"
  "v_readlane_b32 s46, v244, 0\n\t"
  "v_mfma_f32_16x16x32_bf16 v[52:55], v[64:67], v[148:151], v[52:55]\n\t"
  "v_readlane_b32 s48, v244, 8\n\t"
  "v_readlane_b32 s47, v245, 4\n\t"
  "v_mfma_f32_16x16x32_bf16 v[48:51], v[64:67], v[144:147], v[48:51]\n\t"
  "buffer_load_dwordx4 a[108:111], %[vl16], %[srd], s57 offen offset:1024 sc1\n\t"
  "v_readlane_b32 s51, v245, 12\n\t"
  "v_mfma_f32_16x16x32_bf16 v[44:47], v[64:67], v[140:143], v[44:47]\n\t"
  "v_mfma_f32_16x16x32_bf16 v[40:43], v[64:67], v[136:139], v[40:43]\n\t"
  "s_nop 1\n\t"
  "s_and_b32 s52, s51, 0x7fffffff\n\t"
  "s_cselect_b32 s51, s51, 0x2b800000\n\t"
  "v_mfma_f32_16x16x32_bf16 v[36:39], v[64:67], v[132:135], v[36:39]\n\t"
  "buffer_load_dwordx4 a[112:115], %[vl16], %[srd], s57 offen offset:2048 sc1\n\t"
  "v_mov_b32 v245, s48\n\t"
  "v_mfma_f32_16x16x32_bf16 v[32:35], v[64:67], v[128:131], v[32:35]\n\t"
  "v_mul_f32 v245, s51, v245\n\t"
  "v_mul_f32 v244, s47, v245\n\t"
  "v_mfma_f32_16x16x32_bf16 v[60:63], v[68:71], v[152:155], v[60:63]\n\t"
  "v_mul_f32 v245, s46, v245\n\t"
  "v_mfma_f32_16x16x32_bf16 v[56:59], v[68:71], v[148:151], v[56:59]\n\t"
  "buffer_load_dwordx4 a[116:119], %[vl16], %[srd], s57 offen offset:3072 sc1\n\t"
  "v_mfma_f32_16x16x32_bf16 v[52:55], v[68:71], v[144:147], v[52:55]\n\t"
  "s_waitcnt vmcnt(16)\n\t"
  "s_barrier\n\t"
  "v_mfma_f32_16x16x32_bf16 v[48:51], v[68:71], v[140:143], v[48:51]\n\t"
  "v_mov_b32 v246, s51\n\t"
  "ds_read_b64 v[224:225], %[vx] offset:0\n\t"
  "v_mfma_f32_16x16x32_bf16 v[44:47], v[68:71], v[136:139], v[44:47]\n\t"
  "ds_read_b128 v[160:163], %[vs] offset:0\n\t"
  "ds_read_b128 v[164:167], %[vs2] offset:0\n\t"
  "v_mfma_f32_16x16x32_bf16 v[40:43], v[68:71], v[132:135], v[40:43]\n\t"
  "s_add_u32 s57, s57, 4096\n\t"
  "ds_read_b64 v[226:227], %[vx] offset:2048\n\t"
  "v_mfma_f32_16x16x32_bf16 v[36:39], v[68:71], v[128:131], v[36:39]\n\t"
  "s_waitcnt lgkmcnt(8)\n\t"
  "ds_read_b128 v[168:171], %[vs] offset:2048\n\t"
  "v_mfma_f32_16x16x32_bf16 v[60:63], v[72:75], v[148:151], v[60:63]\n\t"
  "ds_read_b128 v[172:175], %[vs2] offset:2048\n\t"
  "ds_read_b64 v[228:229], %[vx] offset:4096\n\t"
  "v_mfma_f32_16x16x32_bf16 v[56:59], v[72:75], v[144:147], v[56:59]\n\t"
  "buffer_load_dwordx4 a[120:123], %[vl16], %[srd], s57 offen offset:0 sc1\n\t"
  "ds_read_b128 v[176:179], %[vs] offset:4096\n\t"
  "v_mfma_f32_16x16x32_bf16 v[52:55], v[72:75], v[140:143], v[52:55]\n\t"
  "ds_read_b128 v[180:183], %[vs2] offset:4096\n\t"
  "ds_read_b64 v[230:231], %[vx] offset:6144\n\t"
  "v_mfma_f32_16x16x32_bf16 v[48:51], v[72:75], v[136:139], v[48:51]\n\t"
  "ds_read_b128 v[184:187], %[vs] offset:6144\n\t"
  "ds_read_b128 v[188:191], %[vs2] offset:6144\n\t"
  "v_mfma_f32_16x16x32_bf16 v[44:47], v[72:75], v[132:135], v[44:47]\n\t"
  "buffer_load_dwordx4 a[124:127], %[vl16], %[srd], s57 offen offset:1024 sc1\n\t"
  "ds_read_b64 v[232:233], %[vx] offset:8192\n\t"
  "v_mfma_f32_16x16x32_bf16 v[40:43], v[72:75], v[128:131], v[40:43]\n\t"
  "ds_read_b128 v[192:195], %[vs] offset:8192\n\t"
  "ds_read_b128 v[196:199], %[vs2] offset:8192\n\t"
  "v_mfma_f32_16x16x32_bf16 v[60:63], v[76:79], v[144:147], v[60:63]\n\t"
  "ds_read_b64 v[234:235], %[vx] offset:10240\n\t"
  "ds_read_b128 v[200:203], %[vs] offset:10240\n\t"
  "v_mfma_f32_16x16x32_bf16 v[56:59], v[76:79], v[140:143], v[56:59]\n\t"
  "ds_read_b128 v[204:207], %[vs2] offset:10240\n\t"
  "ds_read_b64 v[236:237], %[vx] offset:12288\n\t"
  "v_mfma_f32_16x16x32_bf16 v[52:55], v[76:79], v[136:139], v[52:55]\n\t"
  "buffer_load_dwordx4 a[128:131], %[vl16], %[srd], s57 offen offset:2048 sc1\n\t"
  "ds_read_b128 v[208:211], %[vs] offset:12288\n\t"
  "v_mfma_f32_16x16x32_bf16 v[48:51], v[76:79], v[132:135], v[48:51]\n\t"
  "ds_read_b128 v[212:215], %[vs2] offset:12288\n\t"
  "ds_read_b64 v[238:239], %[vx] offset:14336\n\t"
  "v_mfma_f32_16x16x32_bf16 v[44:47], v[76:79], v[128:131], v[44:47]\n\t"
  "ds_read_b128 v[216:219], %[vs] offset:14336\n\t"
  "ds_read_b128 v[220:223], %[vs2] offset:14336\n\t"
  "v_mfma_f32_16x16x32_bf16 v[60:63], v[80:83], v[140:143], v[60:63]\n\t"
  "buffer_load_dwordx4 a[132:135], %[vl16], %[srd], s57 offen offset:3072 sc1\n\t"
  "v_mfma_f32_16x16x32_bf16 v[56:59], v[80:83], v[136:139], v[56:59]\n\t"
  "v_mfma_f32_16x16x32_bf16 v[52:55], v[80:83], v[132:135], v[52:55]\n\t"
  "v_mfma_f32_16x16x32_bf16 v[48:51], v[80:83], v[128:131], v[48:51]\n\t"
  "v_mfma_f32_16x16x32_bf16 v[60:63], v[84:87], v[136:139], v[60:63]\n\t"
  "v_mfma_f32_16x16x32_bf16 v[56:59], v[84:87], v[132:135], v[56:59]\n\t"
  "v_mfma_f32_16x16x32_bf16 v[52:55], v[84:87], v[128:131], v[52:55]\n\t"
  "s_waitcnt lgkmcnt(15)\n\t"
  "v_mfma_f32_16x16x32_bf16 v[60:63], v[88:91], v[132:135], v[60:63]\n\t"
  "v_mfma_f32_16x16x32_bf16 v[56:59], v[88:91], v[128:131], v[56:59]\n\t"
  "v_mfma_f32_16x16x32_bf16 v[60:63], v[92:95], v[128:131], v[60:63]\n\t"
  "v_lshlrev_b32 v96, 16, v224\n\t"
  "v_and_b32 v97, 0xffff0000, v224\n\t"
  "v_lshlrev_b32 v98, 16, v225\n\t"
  "v_and_b32 v99, 0xffff0000, v225\n\t"
  "v_lshlrev_b32 v100, 16, v226\n\t"
  "v_and_b32 v101, 0xffff0000, v226\n\t"
  "v_lshlrev_b32 v102, 16, v227\n\t"
  "v_and_b32 v103, 0xffff0000, v227\n\t"
  "v_pk_fma_f32 v[160:161], v[96:97], v[160:161], v[162:163]\n\t"
  "v_pk_fma_f32 v[164:165], v[98:99], v[164:165], v[166:167]\n\t"
  "v_pk_fma_f32 v[168:169], v[100:101], v[168:169], v[170:171]\n\t"
  "v_pk_fma_f32 v[172:173], v[102:103], v[172:173], v[174:175]\n\t"
  "v_pk_mul_f32 v[96:97], v[96:97], v[246:247] op_sel_hi:[1,0]\n\t"
  "v_pk_mul_f32 v[98:99], v[98:99], v[246:247] op_sel_hi:[1,0]\n\t"
  "v_pk_mul_f32 v[100:101], v[100:101], v[246:247] op_sel_hi:[1,0]\n\t"
  "v_pk_mul_f32 v[102:103], v[102:103], v[246:247] op_sel_hi:[1,0]\n\t"
  "v_pk_fma_f32 v[160:161], v[160:161], v[244:245], v[244:245] op_sel:[0,0,1] op_sel_hi:[1,0,1]\n\t"
  "v_pk_fma_f32 v[164:165], v[164:165], v[244:245], v[244:245] op_sel:[0,0,1] op_sel_hi:[1,0,1]\n\t"
  "v_pk_fma_f32 v[168:169], v[168:169], v[244:245], v[244:245] op_sel:[0,0,1] op_sel_hi:[1,0,1]\n\t"
  "v_pk_fma_f32 v[172:173], v[172:173], v[244:245], v[244:245] op_sel:[0,0,1] op_sel_hi:[1,0,1]\n\t"
  "v_pk_fma_f32 v[96:97], v[160:161], v[32:33], v[96:97]\n\t"
  "v_pk_fma_f32 v[98:99], v[164:165], v[34:35], v[98:99]\n\t"
  "v_pk_fma_f32 v[100:101], v[168:169], v[36:37], v[100:101]\n\t"
  "v_pk_fma_f32 v[102:103], v[172:173], v[38:39], v[102:103]\n\t"
  "v_cvt_pk_bf16_f32 v64, v96, v97\n\t"
  "v_cvt_pk_bf16_f32 v66, v100, v101\n\t"
  "v_cvt_pk_bf16_f32 v65, v98, v99\n\t"
  "v_cvt_pk_bf16_f32 v67, v102, v103\n\t"
  "s_add_u32 s44, %[sO], 0\n\t"
  "v_permlane16_swap_b32 v64, v66\n\t"
  "v_permlane16_swap_b32 v65, v67\n\t"
  "buffer_store_dwordx4 v[64:67], %[vo], %[srd], s44 offen sc1\n\t"
  "v_lshlrev_b32 v104, 16, v228\n\t"
  "v_and_b32 v105, 0xffff0000, v228\n\t"
  "v_lshlrev_b32 v106, 16, v229\n\t"
  "v_and_b32 v107, 0xffff0000, v229\n\t"
  "s_waitcnt lgkmcnt(14)\n\t"
  "v_lshlrev_b32 v108, 16, v230\n\t"
  "v_and_b32 v109, 0xffff0000, v230\n\t"
  "v_lshlrev_b32 v110, 16, v231\n\t"
  "v_and_b32 v111, 0xffff0000, v231\n\t"
  "v_pk_fma_f32 v[176:177], v[104:105], v[176:177], v[178:179]\n\t"
  "v_pk_fma_f32 v[180:181], v[106:107], v[180:181], v[182:183]\n\t"
  "s_waitcnt lgkmcnt(13)\n\t"
  "v_pk_fma_f32 v[184:185], v[108:109], v[184:185], v[186:187]\n\t"
  "s_waitcnt lgkmcnt(12)\n\t"
  "v_pk_fma_f32 v[188:189], v[110:111], v[188:189], v[190:191]\n\t"
  "v_pk_mul_f32 v[104:105], v[104:105], v[246:247] op_sel_hi:[1,0]\n\t"
  "v_pk_mul_f32 v[106:107], v[106:107], v[246:247] op_sel_hi:[1,0]\n\t"
  "v_pk_mul_f32 v[108:109], v[108:109], v[246:247] op_sel_hi:[1,0]\n\t"
  "v_pk_mul_f32 v[110:111], v[110:111], v[246:247] op_sel_hi:[1,0]\n\t"
  "v_pk_fma_f32 v[176:177], v[176:177], v[244:245], v[244:245] op_sel:[0,0,1] op_sel_hi:[1,0,1]\n\t"
  "v_pk_fma_f32 v[180:181], v[180:181], v[244:245], v[244:245] op_sel:[0,0,1] op_sel_hi:[1,0,1]\n\t"
  "v_pk_fma_f32 v[184:185], v[184:185], v[244:245], v[244:245] op_sel:[0,0,1] op_sel_hi:[1,0,1]\n\t"
  "v_pk_fma_f32 v[188:189], v[188:189], v[244:245], v[244:245] op_sel:[0,0,1] op_sel_hi:[1,0,1]\n\t"
  "v_pk_fma_f32 v[104:105], v[176:177], v[40:41], v[104:105]\n\t"
  "v_pk_fma_f32 v[106:107], v[180:181], v[42:43], v[106:107]\n\t"
  "v_pk_fma_f32 v[108:109], v[184:185], v[44:45], v[108:109]\n\t"
  "v_pk_fma_f32 v[110:111], v[188:189], v[46:47], v[110:111]\n\t"
  "v_cvt_pk_bf16_f32 v68, v104, v105\n\t"
  "v_cvt_pk_bf16_f32 v70, v108, v109\n\t"
  "v_cvt_pk_bf16_f32 v69, v106, v107\n\t"
  "v_cvt_pk_bf16_f32 v71, v110, v111\n\t"
  "s_add_u32 s44, %[sO], 655360\n\t"
  "v_permlane16_swap_b32 v68, v70\n\t"
  "v_permlane16_swap_b32 v69, v71\n\t"
  "buffer_store_dwordx4 v[68:71], %[vo], %[srd], s44 offen sc1\n\t"
  "s_waitcnt lgkmcnt(11)\n\t"
  "v_lshlrev_b32 v112, 16, v232\n\t"
  "v_and_b32 v113, 0xffff0000, v232\n\t"
  "v_lshlrev_b32 v114, 16, v233\n\t"
  "v_and_b32 v115, 0xffff0000, v233\n\t"
  "s_waitcnt lgkmcnt(8)\n\t"
  "v_lshlrev_b32 v116, 16, v234\n\t"
  "v_and_b32 v117, 0xffff0000, v234\n\t"
  "v_lshlrev_b32 v118, 16, v235\n\t"
  "v_and_b32 v119, 0xffff0000, v235\n\t"
  "v_pk_fma_f32 v[192:193], v[112:113], v[192:193], v[194:195]\n\t"
  "v_pk_fma_f32 v[196:197], v[114:115], v[196:197], v[198:199]\n\t"
  "s_waitcnt lgkmcnt(7)\n\t"
  "v_pk_fma_f32 v[200:201], v[116:117], v[200:201], v[202:203]\n\t"
  "s_waitcnt lgkmcnt(6)\n\t"
  "v_pk_fma_f32 v[204:205], v[118:119], v[204:205], v[206:207]\n\t"
  "v_pk_mul_f32 v[112:113], v[112:113], v[246:247] op_sel_hi:[1,0]\n\t"
  "v_pk_mul_f32 v[114:115], v[114:115], v[246:247] op_sel_hi:[1,0]\n\t"
  "v_pk_mul_f32 v[116:117], v[116:117], v[246:247] op_sel_hi:[1,0]\n\t"
  "v_pk_mul_f32 v[118:119], v[118:119], v[246:247] op_sel_hi:[1,0]\n\t"
  "v_pk_fma_f32 v[192:193], v[192:193], v[244:245], v[244:245] op_sel:[0,0,1] op_sel_hi:[1,0,1]\n\t"
  "v_pk_fma_f32 v[196:197], v[196:197], v[244:245], v[244:245] op_sel:[0,0,1] op_sel_hi:[1,0,1]\n\t"
  "v_pk_fma_f32 v[200:201], v[200:201], v[244:245], v[244:245] op_sel:[0,0,1] op_sel_hi:[1,0,1]\n\t"
  "v_pk_fma_f32 v[204:205], v[204:205], v[244:245], v[244:245] op_sel:[0,0,1] op_sel_hi:[1,0,1]\n\t"
  "v_pk_fma_f32 v[112:113], v[192:193], v[48:49], v[112:113]\n\t"
  "v_pk_fma_f32 v[114:115], v[196:197], v[50:51], v[114:115]\n\t"
  "v_pk_fma_f32 v[116:117], v[200:201], v[52:53], v[116:117]\n\t"
  "v_pk_fma_f32 v[118:119], v[204:205], v[54:55], v[118:119]\n\t"
  "v_cvt_pk_bf16_f32 v72, v112, v113\n\t"
  "v_cvt_pk_bf16_f32 v74, v116, v117\n\t"
  "v_cvt_pk_bf16_f32 v73, v114, v115\n\t"
  "v_cvt_pk_bf16_f32 v75, v118, v119\n\t"
  "s_add_u32 s44, %[sO], 1310720\n\t"
  "v_permlane16_swap_b32 v72, v74\n\t"
  "v_permlane16_swap_b32 v73, v75\n\t"
  "buffer_store_dwordx4 v[72:75], %[vo], %[srd], s44 offen sc1\n\t"
  "s_waitcnt lgkmcnt(5)\n\t"
  "v_lshlrev_b32 v120, 16, v236\n\t"
  "v_and_b32 v121, 0xffff0000, v236\n\t"
  "v_lshlrev_b32 v122, 16, v237\n\t"
  "v_and_b32 v123, 0xffff0000, v237\n\t"
  "s_waitcnt lgkmcnt(2)\n\t"
  "v_lshlrev_b32 v124, 16, v238\n\t"
  "v_and_b32 v125, 0xffff0000, v238\n\t"
  "v_lshlrev_b32 v126, 16, v239\n\t"
  "v_and_b32 v127, 0xffff0000, v239\n\t"
  "v_pk_fma_f32 v[208:209], v[120:121], v[208:209], v[210:211]\n\t"
  "v_pk_fma_f32 v[212:213], v[122:123], v[212:213], v[214:215]\n\t"
  "s_waitcnt lgkmcnt(1)\n\t"
  "v_pk_fma_f32 v[216:217], v[124:125], v[216:217], v[218:219]\n\t"
  "s_waitcnt lgkmcnt(0)\n\t"
  "v_pk_fma_f32 v[220:221], v[126:127], v[220:221], v[222:223]\n\t"
  "v_pk_mul_f32 v[120:121], v[120:121], v[246:247] op_sel_hi:[1,0]\n\t"
  "v_pk_mul_f32 v[122:123], v[122:123], v[246:247] op_sel_hi:[1,0]\n\t"
  "v_pk_mul_f32 v[124:125], v[124:125], v[246:247] op_sel_hi:[1,0]\n\t"
  "v_pk_mul_f32 v[126:127], v[126:127], v[246:247] op_sel_hi:[1,0]\n\t"
  "v_pk_fma_f32 v[208:209], v[208:209], v[244:245], v[244:245] op_sel:[0,0,1] op_sel_hi:[1,0,1]\n\t"
  "v_pk_fma_f32 v[212:213], v[212:213], v[244:245], v[244:245] op_sel:[0,0,1] op_sel_hi:[1,0,1]\n\t"
  "v_pk_fma_f32 v[216:217], v[216:217], v[244:245], v[244:245] op_sel:[0,0,1] op_sel_hi:[1,0,1]\n\t"
  "v_pk_fma_f32 v[220:221], v[220:221], v[244:245], v[244:245] op_sel:[0,0,1] op_sel_hi:[1,0,1]\n\t"
  "v_pk_fma_f32 v[120:121], v[208:209], v[56:57], v[120:121]\n\t"
  "v_pk_fma_f32 v[122:123], v[212:213], v[58:59], v[122:123]\n\t"
  "v_pk_fma_f32 v[124:125], v[216:217], v[60:61], v[124:125]\n\t"
  "v_pk_fma_f32 v[126:127], v[220:221], v[62:63], v[126:127]\n\t"
  "v_cvt_pk_bf16_f32 v76, v120, v121\n\t"
  "v_cvt_pk_bf16_f32 v78, v124, v125\n\t"
  "v_cvt_pk_bf16_f32 v77, v122, v123\n\t"
  "v_cvt_pk_bf16_f32 v79, v126, v127\n\t"
  "s_add_u32 s44, %[sO], 1966080\n\t"
  "v_permlane16_swap_b32 v76, v78\n\t"
  "v_permlane16_swap_b32 v77, v79\n\t"
  "buffer_store_dwordx4 v[76:79], %[vo], %[srd], s44 offen sc1\n\t"
  "s_nop 1\n\t"
  "s_branch Lcend%=\n\t"
  "La1_slow%=:\n\t"
  "s_nop 7\n\t"
  "s_nop 3\n\t"
  "s_mov_b32 s49, 0x40000\n\t"
  "La1_loop%=:\n\t"
  "s_sleep 3\n\t"
  "buffer_load_dwordx2 v[160:161], %[vf1], %[srd], 0 offen sc1\n\t"
  "s_waitcnt vmcnt(0)\n\t"
  "v_cmp_ne_u64 vcc, v[252:253], v[160:161]\n\t"
  "s_nop 3\n\t"
  "s_cmp_eq_u64 vcc, 0\n\t"
  "s_cbranch_scc1 La1_back%=\n\t"
  "s_sub_u32 s49, s49, 1\n\t"
  "s_cmp_lg_u32 s49, 0\n\t"
  "s_cbranch_scc1 La1_loop%=\n\t"
  "s_branch La1_back%=\n\t"
  "La2_slow%=:\n\t"
  "s_nop 7\n\t"
  "s_nop 3\n\t"
  "s_mov_b32 s49, 0x40000\n\t"
  "La2_loop%=:\n\t"
  "s_sleep 3\n\t"
  "buffer_load_dwordx2 v[162:163], %[vf2], %[srd], 0 offen sc1\n\t"
  "s_waitcnt vmcnt(0)\n\t"
  "v_cmp_ne_u64 vcc, v[252:253], v[162:163]\n\t"
  "s_nop 3\n\t"
  "s_cmp_eq_u64 vcc, 0\n\t"
  "s_cbranch_scc1 La2_back%=\n\t"
  "s_sub_u32 s49, s49, 1\n\t"
  "s_cmp_lg_u32 s49, 0\n\t"
  "s_cbranch_scc1 La2_loop%=\n\t"
  "s_branch La2_back%=\n\t"
  "Lcend%=:\n\t"
      : [wdone] "=&s"(wdone)
      : [srd] "s"(srd), [sO] "s"(OFF_X2P + d * 128u), [w] "s"(w), [sKV] "s"(OFF_KVT + cb * 128u + w * 655360u),
        [tlo] "s"((u32)tag), [thi] "s"((u32)(tag >> 32)), [sW1] "s"(OFF_W1F + (j * 4 + w) * 61440u),
        [vf2] "v"(c_vf2),
        [vf1] "v"(c_vf1), [vf3] "v"(c_vf3), [vfw] "v"(c_vfw),
        [va] "v"(c_va), [vb0] "v"(c_vb0), [vb1] "v"(c_vb1), [vb2] "v"(c_vb2), [vb3] "v"(c_vb3), [vx] "v"(c_vx), [vs] "v"(c_vs), [vs2] "v"(c_vs2), [vo] "v"(c_vo), [vd] "v"(c_vd),
        [vl16] "v"(c_vl16), [vkv] "v"(c_vkv),
        [vmod] "v"(c_vmod)
      : "memory", "vcc", "scc",
"a0", "a1", "a2", "a3", "a4", "a5", "a6", "a7", "a8", "a9", "a10", "a11", "a12", "a13", "a14", "a15", "a16", "a17", "a18", "a19", "a20", "a21", "a22", "a23", "a24", "a25", "a26", "a27", "a28", "a29", "a30", "a31", "a32", "a33", "a34", "a35", "a36", "a37", "a38", "a39", "a40", "a41", "a42", "a43", "a44", "a45", "a46", "a47", "a48", "a49", "a50", "a51", "a52", "a53", "a54", "a55", "a56", "a57", "a58", "a59", "a60", "a61", "a62", "a63", "a64", "a65", "a66", "a67", "a68", "a69", "a70", "a71", "a72", "a73", "a74", "a75", "a76", "a77", "a78", "a79", "a80", "a81", "a82", "a83", "a84", "a85", "a86", "a87", "a88", "a89", "a90", "a91", "a92", "a93", "a94", "a95", "a96", "a97", "a98", "a99", "a100", "a101", "a102", "a103", "a104", "a105", "a106", "a107", "a108", "a109", "a110", "a111", "a112", "a113", "a114", "a115", "a116", "a117", "a118", "a119", "a120", "a121", "a122", "a123", "a124", "a125", "a126", "a127", "a128", "a129", "a130", "a131", "a132", "a133", "a134", "a135", "s44", "s45", "s46", "s47", "s48", "s49", "s51", "s52", "s53", "s54", "s55", "s56", "s57", "v32", "v33", "v34", "v35", "v36", "v37", "v38", "v39", "v40", "v41", "v42", "v43", "v44", "v45", "v46", "v47", "v48", "v49", "v50", "v51", "v52", "v53", "v54", "v55", "v56", "v57", "v58", "v59", "v60", "v61", "v62", "v63", "v64", "v65", "v66", "v67", "v68", "v69", "v70", "v71", "v72", "v73", "v74", "v75", "v76", "v77", "v78", "v79", "v80", "v81", "v82", "v83", "v84", "v85", "v86", "v87", "v88", "v89", "v90", "v91", "v92", "v93", "v94", "v95", "v96", "v97", "v98", "v99", "v100", "v101", "v102", "v103", "v104", "v105", "v106", "v107", "v108", "v109", "v110", "v111", "v112", "v113", "v114", "v115", "v116", "v117", "v118", "v119", "v120", "v121", "v122", "v123", "v124", "v125", "v126", "v127", "v128", "v129", "v130", "v131", "v132", "v133", "v134", "v135", "v136", "v137", "v138", "v139", "v140", "v141", "v142", "v143", "v144", "v145", "v146", "v147", "v148", "v149", "v150", "v151", "v152", "v153", "v154", "v155", "v156", "v157", "v158", "v159", "v160", "v161", "v162", "v163", "v164", "v165", "v166", "v167", "v168", "v169", "v170", "v171", "v172", "v173", "v174", "v175", "v176", "v177", "v178", "v179", "v180", "v181", "v182", "v183", "v184", "v185", "v186", "v187", "v188", "v189", "v190", "v191", "v192", "v193", "v194", "v195", "v196", "v197", "v198", "v199", "v200", "v201", "v202", "v203", "v204", "v205", "v206", "v207", "v208", "v209", "v210", "v211", "v212", "v213", "v214", "v215", "v216", "v217", "v218", "v219", "v220", "v221", "v222", "v223", "v224", "v225", "v226", "v227", "v228", "v229", "v230", "v231", "v232", "v233", "v234", "v235", "v236", "v237", "v238", "v239", "v244", "v245", "v246", "v247", "v250", "v251", "v252", "v253", "v254", "v255"
    );
  }
  STAMP(4);
  STAMP_OUT(1);
  asm volatile(
  "v_mbcnt_lo_u32_b32 v20, -1, 0\n\t"
  "v_mbcnt_hi_u32_b32 v20, -1, v20\n\t"
  "s_lshl_b32 s52, %[w], 10\n\t"
  "s_add_u32 s52, s52, 143360\n\t"
  "v_lshl_add_u32 v20, v20, 4, s52\n\t"
  "ds_read_b128 v[0:3], v20 offset:0\n\t"
  "ds_read_b128 v[4:7], v20 offset:4096\n\t"
  "ds_read_b128 v[8:11], v20 offset:8192\n\t"
  "ds_read_b128 v[12:15], v20 offset:12288\n\t"
  "ds_read_b128 v[16:19], v20 offset:16384\n\t"
  "s_waitcnt lgkmcnt(0)\n\t"
  "v_mov_b32 v132, %[tlo]\n\t"
  "v_mov_b32 v133, %[thi]\n\t"
  "v_mov_b32 v134, %[tlo]\n\t"
  "v_mov_b32 v135, %[thi]\n\t"
  "s_lshl_b32 s52, %[w], 12\n\t"
  "s_waitcnt vmcnt(0) lgkmcnt(0)\n\t"
  "s_barrier\n\t"
  "buffer_store_dwordx4 v[132:135], v0, %[srd], 0 offen sc1\n\t"
  "s_bitcmp1_b32 %[wdone], 1\n\t"
  "s_cbranch_scc1 Lwskip2%=\n\t"
  "s_mov_b32 s49, 0x40000\n\t"
  "Lpw_loop%=:\n\t"
  "buffer_load_dwordx2 v[112:113], v2, %[srd], 0 offen sc1\n\t"
  "s_waitcnt vmcnt(0)\n\t"
  "v_cmp_eq_u64 vcc, v[132:133], v[112:113]\n\t"
  "s_nop 3\n\t"
  "s_orn2_b64 vcc, vcc, 15\n\t"
  "s_cmp_eq_u64 vcc, -1\n\t"
  "s_cbranch_scc1 Lpw_done%=\n\t"
  "s_sleep 3\n\t"
  "s_sub_u32 s49, s49, 1\n\t"
  "s_cmp_lg_u32 s49, 0\n\t"
  "s_cbranch_scc1 Lpw_loop%=\n\t"
  "Lpw_done%=:\n\t"
  "s_add_u32 s44, %[sW1], 12288\n\t"
  "buffer_load_dwordx4 a[56:59], v1, %[srd], s44 offen offset:2048 sc1\n\t"
  "s_add_u32 s44, %[sW1], 12288\n\t"
  "buffer_load_dwordx4 a[60:63], v1, %[srd], s44 offen offset:3072 sc1\n\t"
  "s_add_u32 s44, %[sW1], 16384\n\t"
  "buffer_load_dwordx4 a[64:67], v1, %[srd], s44 offen offset:0 sc1\n\t"
  "s_add_u32 s44, %[sW1], 16384\n\t"
  "buffer_load_dwordx4 a[68:71], v1, %[srd], s44 offen offset:1024 sc1\n\t"
  "s_add_u32 s44, %[sW1], 16384\n\t"
  "buffer_load_dwordx4 a[72:75], v1, %[srd], s44 offen offset:2048 sc1\n\t"
  "s_add_u32 s44, %[sW1], 16384\n\t"
  "buffer_load_dwordx4 a[76:79], v1, %[srd], s44 offen offset:3072 sc1\n\t"
  "s_add_u32 s44, %[sW1], 20480\n\t"
  "buffer_load_dwordx4 a[80:83], v1, %[srd], s44 offen offset:0 sc1\n\t"
  "s_add_u32 s44, %[sW1], 20480\n\t"
  "buffer_load_dwordx4 a[84:87], v1, %[srd], s44 offen offset:1024 sc1\n\t"
  "s_add_u32 s44, %[sW1], 20480\n\t"
  "buffer_load_dwordx4 a[88:91], v1, %[srd], s44 offen offset:2048 sc1\n\t"
  "s_add_u32 s44, %[sW1], 20480\n\t"
  "buffer_load_dwordx4 a[92:95], v1, %[srd], s44 offen offset:3072 sc1\n\t"
  "s_add_u32 s44, %[sW1], 24576\n\t"
  "buffer_load_dwordx4 a[96:99], v1, %[srd], s44 offen offset:0 sc1\n\t"
  "s_add_u32 s44, %[sW1], 24576\n\t"
  "buffer_load_dwordx4 a[100:103], v1, %[srd], s44 offen offset:1024 sc1\n\t"
  "s_add_u32 s44, %[sW1], 24576\n\t"
  "buffer_load_dwordx4 a[104:107], v1, %[srd], s44 offen offset:2048 sc1\n\t"
  "s_add_u32 s44, %[sW1], 24576\n\t"
  "buffer_load_dwordx4 a[108:111], v1, %[srd], s44 offen offset:3072 sc1\n\t"
  "s_add_u32 s44, %[sW1], 28672\n\t"
  "buffer_load_dwordx4 a[112:115], v1, %[srd], s44 offen offset:0 sc1\n\t"
  "s_add_u32 s44, %[sW1], 28672\n\t"
  "buffer_load_dwordx4 a[116:119], v1, %[srd], s44 offen offset:1024 sc1\n\t"
  "s_add_u32 s44, %[sW1], 28672\n\t"
  "buffer_load_dwordx4 a[120:123], v1, %[srd], s44 offen offset:2048 sc1\n\t"
  "s_add_u32 s44, %[sW1], 28672\n\t"
  "buffer_load_dwordx4 a[124:127], v1, %[srd], s44 offen offset:3072 sc1\n\t"
  "s_add_u32 s44, %[sW1], 32768\n\t"
  "buffer_load_dwordx4 a[128:131], v1, %[srd], s44 offen offset:0 sc1\n\t"
  "s_add_u32 s44, %[sW1], 32768\n\t"
  "buffer_load_dwordx4 a[132:135], v1, %[srd], s44 offen offset:1024 sc1\n\t"
  "Lwskip2%=:\n\t"
  "s_bitcmp1_b32 %[wdone], 0\n\t"
  "s_cbranch_scc1 Lwskip%=\n\t"
  "s_add_u32 s44, %[sW1], 0\n\t"
  "buffer_load_dwordx4 a[0:3], v1, %[srd], s44 offen offset:0 sc1\n\t"
  "s_add_u32 s44, %[sW1], 0\n\t"
  "buffer_load_dwordx4 a[4:7], v1, %[srd], s44 offen offset:1024 sc1\n\t"
  "s_add_u32 s44, %[sW1], 0\n\t"
  "buffer_load_dwordx4 a[8:11], v1, %[srd], s44 offen offset:2048 sc1\n\t"
  "s_add_u32 s44, %[sW1], 0\n\t"
  "buffer_load_dwordx4 a[12:15], v1, %[srd], s44 offen offset:3072 sc1\n\t"
  "s_add_u32 s44, %[sW1], 4096\n\t"
  "buffer_load_dwordx4 a[16:19], v1, %[srd], s44 offen offset:0 sc1\n\t"
  "s_add_u32 s44, %[sW1], 4096\n\t"
  "buffer_load_dwordx4 a[20:23], v1, %[srd], s44 offen offset:1024 sc1\n\t"
  "s_add_u32 s44, %[sW1], 4096\n\t"
  "buffer_load_dwordx4 a[24:27], v1, %[srd], s44 offen offset:2048 sc1\n\t"
  "s_add_u32 s44, %[sW1], 4096\n\t"
  "buffer_load_dwordx4 a[28:31], v1, %[srd], s44 offen offset:3072 sc1\n\t"
  "s_add_u32 s44, %[sW1], 8192\n\t"
  "buffer_load_dwordx4 a[32:35], v1, %[srd], s44 offen offset:0 sc1\n\t"
  "s_add_u32 s44, %[sW1], 8192\n\t"
  "buffer_load_dwordx4 a[36:39], v1, %[srd], s44 offen offset:1024 sc1\n\t"
  "s_add_u32 s44, %[sW1], 8192\n\t"
  "buffer_load_dwordx4 a[40:43], v1, %[srd], s44 offen offset:2048 sc1\n\t"
  "s_add_u32 s44, %[sW1], 8192\n\t"
  "buffer_load_dwordx4 a[44:47], v1, %[srd], s44 offen offset:3072 sc1\n\t"
  "s_add_u32 s44, %[sW1], 12288\n\t"
  "buffer_load_dwordx4 a[48:51], v1, %[srd], s44 offen offset:0 sc1\n\t"
  "s_add_u32 s44, %[sW1], 12288\n\t"
  "buffer_load_dwordx4 a[52:55], v1, %[srd], s44 offen offset:1024 sc1\n\t"
  "Lwskip%=:\n\t"
  "s_bitcmp1_b32 %[wdone], 2\n\t"
  "s_cbranch_scc1 Lwskip3%=\n\t"
  "s_add_u32 s44, %[sW1], 32768\n\t"
  "buffer_load_dwordx4 a[136:139], v1, %[srd], s44 offen offset:2048 sc1\n\t"
  "s_add_u32 s44, %[sW1], 32768\n\t"
  "buffer_load_dwordx4 a[140:143], v1, %[srd], s44 offen offset:3072 sc1\n\t"
  "s_add_u32 s44, %[sW1], 36864\n\t"
  "buffer_load_dwordx4 a[144:147], v1, %[srd], s44 offen offset:0 sc1\n\t"
  "s_add_u32 s44, %[sW1], 36864\n\t"
  "buffer_load_dwordx4 a[148:151], v1, %[srd], s44 offen offset:1024 sc1\n\t"
  "s_add_u32 s44, %[sW1], 36864\n\t"
  "buffer_load_dwordx4 a[152:155], v1, %[srd], s44 offen offset:2048 sc1\n\t"
  "s_add_u32 s44, %[sW1], 36864\n\t"
  "buffer_load_dwordx4 a[156:159], v1, %[srd], s44 offen offset:3072 sc1\n\t"
  "s_add_u32 s44, %[sW1], 40960\n\t"
  "buffer_load_dwordx4 a[160:163], v1, %[srd], s44 offen offset:0 sc1\n\t"
  "s_add_u32 s44, %[sW1], 40960\n\t"
  "buffer_load_dwordx4 a[164:167], v1, %[srd], s44 offen offset:1024 sc1\n\t"
  "s_add_u32 s44, %[sW1], 40960\n\t"
  "buffer_load_dwordx4 a[168:171], v1, %[srd], s44 offen offset:2048 sc1\n\t"
  "s_add_u32 s44, %[sW1], 40960\n\t"
  "buffer_load_dwordx4 a[172:175], v1, %[srd], s44 offen offset:3072 sc1\n\t"
  "s_add_u32 s44, %[sW1], 45056\n\t"
  "buffer_load_dwordx4 a[176:179], v1, %[srd], s44 offen offset:0 sc1\n\t"
  "s_add_u32 s44, %[sW1], 45056\n\t"
  "buffer_load_dwordx4 a[180:183], v1, %[srd], s44 offen offset:1024 sc1\n\t"
  "s_add_u32 s44, %[sW1], 45056\n\t"
  "buffer_load_dwordx4 a[184:187], v1, %[srd], s44 offen offset:2048 sc1\n\t"
  "s_add_u32 s44, %[sW1], 45056\n\t"
  "buffer_load_dwordx4 a[188:191], v1, %[srd], s44 offen offset:3072 sc1\n\t"
  "s_add_u32 s44, %[sW1], 49152\n\t"
  "buffer_load_dwordx4 a[192:195], v1, %[srd], s44 offen offset:0 sc1\n\t"
  "s_add_u32 s44, %[sW1], 49152\n\t"
  "buffer_load_dwordx4 a[196:199], v1, %[srd], s44 offen offset:1024 sc1\n\t"
  "s_add_u32 s44, %[sW1], 49152\n\t"
  "buffer_load_dwordx4 a[200:203], v1, %[srd], s44 offen offset:2048 sc1\n\t"
  "s_add_u32 s44, %[sW1], 49152\n\t"
  "buffer_load_dwordx4 a[204:207], v1, %[srd], s44 offen offset:3072 sc1\n\t"
  "Lwskip3%=:\n\t"
  "v_mul_u32_u24 v139, 3, v1\n\t"
  "s_mov_b32 s47, 117760\n\t"
  "s_mov_b32 s49, 0x40000\n\t"
  "Lpx_loop%=:\n\t"
  "buffer_load_dwordx2 v[112:113], v3, %[srd], 0 offen sc1\n\t"
  "s_waitcnt vmcnt(0)\n\t"
  "v_cmp_eq_u64 vcc, v[132:133], v[112:113]\n\t"
  "s_nop 3\n\t"
  "s_cmp_eq_u64 vcc, -1\n\t"
  "s_cbranch_scc1 Lpx_done%=\n\t"
  "s_sleep 3\n\t"
  "s_sub_u32 s49, s49, 1\n\t"
  "s_cmp_lg_u32 s49, 0\n\t"
  "s_cbranch_scc1 Lpx_loop%=\n\t"
  "Lpx_done%=:\n\t"
  "s_add_u32 m0, s52, 0\n\t"
  "s_mov_b32 s46, 0\n\t"
  "buffer_load_dwordx4 v4, %[srd], s46 offen offset:0 sc1 lds\n\t"
  "buffer_load_dwordx4 v4, %[srd], s46 offen offset:1024 sc1 lds\n\t"
  "buffer_load_dwordx4 v4, %[srd], s46 offen offset:2048 sc1 lds\n\t"
  "buffer_load_dwordx4 v4, %[srd], s46 offen offset:3072 sc1 lds\n\t"
  "s_add_u32 m0, s52, 16384\n\t"
  "s_mov_b32 s46, 16384\n\t"
  "buffer_load_dwordx4 v4, %[srd], s46 offen offset:0 sc1 lds\n\t"
  "buffer_load_dwordx4 v4, %[srd], s46 offen offset:1024 sc1 lds\n\t"
  "buffer_load_dwordx4 v4, %[srd], s46 offen offset:2048 sc1 lds\n\t"
  "buffer_load_dwordx4 v4, %[srd], s46 offen offset:3072 sc1 lds\n\t"
  "s_add_u32 m0, s52, 32768\n\t"
  "s_mov_b32 s46, 32768\n\t"
  "buffer_load_dwordx4 v4, %[srd], s46 offen offset:0 sc1 lds\n\t"
  "buffer_load_dwordx4 v4, %[srd], s46 offen offset:1024 sc1 lds\n\t"
  "buffer_load_dwordx4 v4, %[srd], s46 offen offset:2048 sc1 lds\n\t"
  "buffer_load_dwordx4 v4, %[srd], s46 offen offset:3072 sc1 lds\n\t"
  "s_add_u32 m0, s52, 49152\n\t"
  "s_mov_b32 s46, 49152\n\t"
  "buffer_load_dwordx4 v4, %[srd], s46 offen offset:0 sc1 lds\n\t"
  "buffer_load_dwordx4 v4, %[srd], s46 offen offset:1024 sc1 lds\n\t"
  "buffer_load_dwordx4 v4, %[srd], s46 offen offset:2048 sc1 lds\n\t"
  "buffer_load_dwordx4 v4, %[srd], s46 offen offset:3072 sc1 lds\n\t"
  "s_add_u32 m0, s52, 65536\n\t"
  "s_mov_b32 s46, 65536\n\t"
  "buffer_load_dwordx4 v4, %[srd], s46 offen offset:0 sc1 lds\n\t"
  "buffer_load_dwordx4 v4, %[srd], s46 offen offset:1024 sc1 lds\n\t"
  "buffer_load_dwordx4 v4, %[srd], s46 offen offset:2048 sc1 lds\n\t"
  "buffer_load_dwordx4 v4, %[srd], s46 offen offset:3072 sc1 lds\n\t"
  "buffer_load_dwordx2 v[142:143], v139, %[srd], s47 offen sc1\n\t"
  "buffer_load_dwordx4 v[248:251], v139, %[srd], s47 offen offset:16 sc1\n\t"
  "buffer_load_dwordx4 v[252:255], v139, %[srd], s47 offen offset:32 sc1\n\t"
  "s_add_u32 s44, %[sW1], 53248\n\t"
  "buffer_load_dwordx4 a[208:211], v1, %[srd], s44 offen offset:0 sc1\n\t"
  "s_add_u32 s44, %[sW1], 53248\n\t"
  "buffer_load_dwordx4 a[212:215], v1, %[srd], s44 offen offset:1024 sc1\n\t"
  "s_add_u32 s44, %[sW1], 53248\n\t"
  "buffer_load_dwordx4 a[216:219], v1, %[srd], s44 offen offset:2048 sc1\n\t"
  "s_add_u32 s44, %[sW1], 53248\n\t"
  "buffer_load_dwordx4 a[220:223], v1, %[srd], s44 offen offset:3072 sc1\n\t"
  "s_add_u32 s44, %[sW1], 57344\n\t"
  "buffer_load_dwordx4 a[224:227], v1, %[srd], s44 offen offset:0 sc1\n\t"
  "s_add_u32 s44, %[sW1], 57344\n\t"
  "buffer_load_dwordx4 a[228:231], v1, %[srd], s44 offen offset:1024 sc1\n\t"
  "s_add_u32 s44, %[sW1], 57344\n\t"
  "buffer_load_dwordx4 a[232:235], v1, %[srd], s44 offen offset:2048 sc1\n\t"
  "s_add_u32 s44, %[sW1], 57344\n\t"
  "buffer_load_dwordx4 a[236:239], v1, %[srd], s44 offen offset:3072 sc1\n\t"
  "v_add_u32 v124, 40960, v6\n\t"
  "v_mov_b32 v112, 0\n\t"
  "v_mov_b32 v113, 0\n\t"
  "v_mov_b32 v114, 0\n\t"
  "v_mov_b32 v115, 0\n\t"
  "v_mov_b32 v116, 0\n\t"
  "v_mov_b32 v117, 0\n\t"
  "v_mov_b32 v118, 0\n\t"
  "v_mov_b32 v119, 0\n\t"
  "v_mov_b32 v120, 0\n\t"
  "v_mov_b32 v121, 0\n\t"
  "v_and_b32 v136, 0xf0, v1\n\t"
  "v_lshlrev_b32 v137, 3, v136\n\t"
  "v_lshlrev_b32 v136, 2, v136\n\t"
  "s_add_u32 s54, %[sST], 65536\n\t"
  "s_mul_i32 s48, %[w], 20480\n\t"
  "v_add_u32 v141, s48, v1\n\t"
  "v_xor_b32 v109, 32, v5\n\t"
  "v_mov_b32 v108, v5\n\t"
  "s_waitcnt vmcnt(11)\n\t"
  "s_barrier\n\t"
  "ds_read_b64_tr_b16 v[68:69], v108 offset:0\n\t"
  "ds_read_b64_tr_b16 v[70:71], v108 offset:2048\n\t"
  "ds_read_b64_tr_b16 v[72:73], v109 offset:0\n\t"
  "ds_read_b64_tr_b16 v[74:75], v109 offset:2048\n\t"
  "ds_read_b64_tr_b16 v[76:77], v108 offset:4096\n\t"
  "ds_read_b64_tr_b16 v[78:79], v108 offset:6144\n\t"
  "ds_read_b64_tr_b16 v[80:81], v109 offset:4096\n\t"
  "ds_read_b64_tr_b16 v[82:83], v109 offset:6144\n\t"
  "ds_read_u16_d16_hi v112, v6 offset:0\n\t"
  "ds_read_u16_d16_hi v113, v6 offset:8192\n\t"
  "ds_read_u16_d16_hi v114, v6 offset:16384\n\t"
  "ds_read_u16_d16_hi v115, v6 offset:24576\n\t"
  "ds_read_u16_d16_hi v116, v6 offset:32768\n\t"
  "ds_read_u16_d16_hi v117, v124 offset:0\n\t"
  "ds_read_u16_d16_hi v118, v124 offset:8192\n\t"
  "ds_read_u16_d16_hi v119, v124 offset:16384\n\t"
  "ds_read_u16_d16_hi v120, v124 offset:24576\n\t"
  "ds_read_u16_d16_hi v121, v124 offset:32768\n\t"
  "s_waitcnt lgkmcnt(15)\n\t"
  "v_mfma_f32_16x16x32_bf16 v[20:23], a[0:3], v[68:71], 0\n\t"
  "ds_read_b64_tr_b16 v[84:85], v108 offset:8192\n\t"
  "v_add_u32 v110, 40960, v5\n\t"
  "v_mfma_f32_16x16x32_bf16 v[24:27], a[4:7], v[68:71], 0\n\t"
  "ds_read_b64_tr_b16 v[86:87], v108 offset:10240\n\t"
  "v_xor_b32 v111, 32, v5\n\t"
  "v_mfma_f32_16x16x32_bf16 v[28:31], a[8:11], v[68:71], 0\n\t"
  "ds_read_b64_tr_b16 v[88:89], v109 offset:8192\n\t"
  "v_add_u32 v111, 40960, v111\n\t"
  "s_waitcnt lgkmcnt(15)\n\t"
  "v_mfma_f32_16x16x32_bf16 v[32:35], a[0:3], v[72:75], 0\n\t"
  "ds_read_b64_tr_b16 v[90:91], v109 offset:10240\n\t"
  "s_add_u32 m0, s52, 81920\n\t"
  "v_mfma_f32_16x16x32_bf16 v[36:39], a[4:7], v[72:75], 0\n\t"
  "s_mov_b32 s46, 1310720\n\t"
  "buffer_load_dwordx4 v4, %[srd], s46 offen offset:0 sc1 lds\n\t"
  "v_mfma_f32_16x16x32_bf16 v[40:43], a[8:11], v[72:75], 0\n\t"
  "s_waitcnt lgkmcnt(14)\n\t"
  "v_mfma_f32_16x16x32_bf16 v[20:23], a[12:15], v[76:79], v[20:23]\n\t"
  "ds_read_b64_tr_b16 v[68:69], v108 offset:12288\n\t"
  "v_mfma_f32_16x16x32_bf16 v[24:27], a[16:19], v[76:79], v[24:27]\n\t"
  "ds_read_b64_tr_b16 v[70:71], v108 offset:14336\n\t"
  "buffer_load_dwordx4 v4, %[srd], s46 offen offset:1024 sc1 lds\n\t"
  "v_mfma_f32_16x16x32_bf16 v[28:31], a[20:23], v[76:79], v[28:31]\n\t"
  "ds_read_b64_tr_b16 v[72:73], v109 offset:12288\n\t"
  "v_mfma_f32_16x16x32_bf16 v[32:35], a[12:15], v[80:83], v[32:35]\n\t"
  "ds_read_b64_tr_b16 v[74:75], v109 offset:14336\n\t"
  "s_waitcnt vmcnt(12) lgkmcnt(15)\n\t"
  "v_mul_f32 v112, v112, v142\n\t"
  "v_mul_f32 v113, v113, v143\n\t"
  "v_mfma_f32_16x16x32_bf16 v[36:39], a[16:19], v[80:83], v[36:39]\n\t"
  "s_waitcnt vmcnt(11)\n\t"
  "v_mul_f32 v114, v114, v248\n\t"
  "s_waitcnt lgkmcnt(14)\n\t"
  "v_mul_f32 v115, v115, v249\n\t"
  "v_mfma_f32_16x16x32_bf16 v[40:43], a[20:23], v[80:83], v[40:43]\n\t"
  "s_waitcnt lgkmcnt(4)\n\t"
  "v_mul_f32 v116, v116, v250\n\t"
  "v_mul_f32 v117, v117, v251\n\t"
  "s_waitcnt vmcnt(10)\n\t"
  "v_mul_f32 v118, v118, v252\n\t"
  "v_mfma_f32_16x16x32_bf16 v[20:23], a[24:27], v[84:87], v[20:23]\n\t"
  "ds_read_b64_tr_b16 v[76:77], v108 offset:16384\n\t"
  "v_mul_f32 v119, v119, v253\n\t"
  "v_mul_f32 v120, v120, v254\n\t"
  "v_mul_f32 v121, v121, v255\n\t"
  "v_mfma_f32_16x16x32_bf16 v[24:27], a[28:31], v[84:87], v[24:27]\n\t"
  "ds_read_b64_tr_b16 v[78:79], v108 offset:18432\n\t"
  "v_add_f32 v122, v112, v113\n\t"
  "v_mul_f32 v123, v112, v112\n\t"
  "v_fmac_f32 v123, v113, v113\n\t"
  "v_mfma_f32_16x16x32_bf16 v[28:31], a[32:35], v[84:87], v[28:31]\n\t"
  "ds_read_b64_tr_b16 v[80:81], v109 offset:16384\n\t"
  "v_add_f32 v122, v122, v114\n\t"
  "v_fmac_f32 v123, v114, v114\n\t"
  "v_add_f32 v122, v122, v115\n\t"
  "v_mfma_f32_16x16x32_bf16 v[32:35], a[24:27], v[88:91], v[32:35]\n\t"
  "ds_read_b64_tr_b16 v[82:83], v109 offset:18432\n\t"
  "v_fmac_f32 v123, v115, v115\n\t"
  "v_add_f32 v122, v122, v116\n\t"
  "v_fmac_f32 v123, v116, v116\n\t"
  "v_mfma_f32_16x16x32_bf16 v[36:39], a[28:31], v[88:91], v[36:39]\n\t"
  "v_add_f32 v122, v122, v117\n\t"
  "v_fmac_f32 v123, v117, v117\n\t"
  "v_add_f32 v122, v122, v118\n\t"
  "v_fmac_f32 v123, v118, v118\n\t"
  "v_mfma_f32_16x16x32_bf16 v[40:43], a[32:35], v[88:91], v[40:43]\n\t"
  "s_waitcnt lgkmcnt(4)\n\t"
  "v_add_f32 v122, v122, v119\n\t"
  "v_fmac_f32 v123, v119, v119\n\t"
  "v_add_f32 v122, v122, v120\n\t"
  "v_mfma_f32_16x16x32_bf16 v[20:23], a[36:39], v[68:71], v[20:23]\n\t"
  "ds_read_b64_tr_b16 v[84:85], v108 offset:20480\n\t"
  "v_fmac_f32 v123, v120, v120\n\t"
  "v_add_f32 v122, v122, v121\n\t"
  "v_fmac_f32 v123, v121, v121\n\t"
  "v_mfma_f32_16x16x32_bf16 v[24:27], a[40:43], v[68:71], v[24:27]\n\t"
  "ds_read_b64_tr_b16 v[86:87], v108 offset:22528\n\t"
  "v_add_f32_dpp v122, v122, v122 quad_perm:[1,0,3,2] row_mask:0xf bank_mask:0xf\n\t"
  "v_add_f32_dpp v123, v123, v123 quad_perm:[1,0,3,2] row_mask:0xf bank_mask:0xf\n\t"
  "buffer_load_dwordx4 v4, %[srd], s46 offen offset:2048 sc1 lds\n\t"
  "v_mfma_f32_16x16x32_bf16 v[28:31], a[44:47], v[68:71], v[28:31]\n\t"
  "ds_read_b64_tr_b16 v[88:89], v109 offset:20480\n\t"
  "v_add_f32_dpp v122, v122, v122 quad_perm:[2,3,0,1] row_mask:0xf bank_mask:0xf\n\t"
  "v_add_f32_dpp v123, v123, v123 quad_perm:[2,3,0,1] row_mask:0xf bank_mask:0xf\n\t"
  "buffer_load_dwordx4 v4, %[srd], s46 offen offset:3072 sc1 lds\n\t"
  "v_mfma_f32_16x16x32_bf16 v[32:35], a[36:39], v[72:75], v[32:35]\n\t"
  "ds_read_b64_tr_b16 v[90:91], v109 offset:22528\n\t"
  "v_add_f32_dpp v122, v122, v122 row_half_mirror row_mask:0xf bank_mask:0xf\n\t"
  "v_add_f32_dpp v123, v123, v123 row_half_mirror row_mask:0xf bank_mask:0xf\n\t"
  "s_add_u32 m0, s52, 98304\n\t"
  "v_mfma_f32_16x16x32_bf16 v[36:39], a[40:43], v[72:75], v[36:39]\n\t"
  "v_add_f32_dpp v122, v122, v122 row_mirror row_mask:0xf bank_mask:0xf\n\t"
  "v_add_f32_dpp v123, v123, v123 row_mirror row_mask:0xf bank_mask:0xf\n\t"
  "s_mov_b32 s46, 1327104\n\t"
  "v_add_f32_dpp v122, v122, v122 row_bcast:15 row_mask:0xa bank_mask:0xf\n\t"
  "v_mfma_f32_16x16x32_bf16 v[40:43], a[44:47], v[72:75], v[40:43]\n\t"
  "s_waitcnt lgkmcnt(4)\n\t"
  "v_add_f32_dpp v123, v123, v123 row_bcast:15 row_mask:0xa bank_mask:0xf\n\t"
  "v_add_f32_dpp v122, v122, v122 row_bcast:31 row_mask:0xc bank_mask:0xf\n\t"
  "buffer_load_dwordx4 v4, %[srd], s46 offen offset:0 sc1 lds\n\t"
  "v_mfma_f32_16x16x32_bf16 v[20:23], a[48:51], v[76:79], v[20:23]\n\t"
  "ds_read_b64_tr_b16 v[68:69], v108 offset:24576\n\t"
  "v_add_f32_dpp v123, v123, v123 row_bcast:31 row_mask:0xc bank_mask:0xf\n\t"
  "v_mul_f32 v122, 0xbacccccd, v122\n\t"
  "v_mul_f32 v123, 0x3acccccd, v123\n\t"
  "v_mfma_f32_16x16x32_bf16 v[24:27], a[52:55], v[76:79], v[24:27]\n\t"
  "ds_read_b64_tr_b16 v[70:71], v108 offset:26624\n\t"
  "v_fma_f32 v123, -v122, v122, v123\n\t"
  "v_max_f32 v123, 0, v123\n\t"
  "v_cvt_pk_bf16_f32 v123, 0, v123\n\t"
  "v_mfma_f32_16x16x32_bf16 v[28:31], a[56:59], v[76:79], v[28:31]\n\t"
  "ds_read_b64_tr_b16 v[72:73], v109 offset:24576\n\t"
  "v_cvt_pk_bf16_f32 v126, v122, v122\n\t"
  "v_add_f32 v123, 0x3727c5ac, v123\n\t"
  "v_cvt_pk_bf16_f32 v123, 0, v123\n\t"
  "v_mfma_f32_16x16x32_bf16 v[32:35], a[48:51], v[80:83], v[32:35]\n\t"
  "ds_read_b64_tr_b16 v[74:75], v109 offset:26624\n\t"
  "v_sqrt_f32 v123, v123\n\t"
  "buffer_load_dwordx4 v4, %[srd], s46 offen offset:1024 sc1 lds\n\t"
  "v_cvt_pk_bf16_f32 v127, v123, v123\n\t"
  "v_mfma_f32_16x16x32_bf16 v[36:39], a[52:55], v[80:83], v[36:39]\n\t"
  "buffer_store_dwordx2 v[126:127], v7, %[srd], %[sST] offen sc1\n\t"
  "buffer_load_dwordx4 v4, %[srd], s46 offen offset:2048 sc1 lds\n\t"
  "buffer_load_dwordx4 v4, %[srd], s46 offen offset:3072 sc1 lds\n\t"
  "s_add_u32 m0, s52, 114688\n\t"
  "v_mfma_f32_16x16x32_bf16 v[40:43], a[56:59], v[80:83], v[40:43]\n\t"
  "s_waitcnt lgkmcnt(4)\n\t"
  "v_add_u32 v124, 122880, v6\n\t"
  "v_mfma_f32_16x16x32_bf16 v[20:23], a[60:63], v[84:87], v[20:23]\n\t"
  "ds_read_b64_tr_b16 v[76:77], v108 offset:28672\n\t"
  "v_add_u32 v125, 81920, v6\n\t"
  "v_mfma_f32_16x16x32_bf16 v[24:27], a[64:67], v[84:87], v[24:27]\n\t"
  "ds_read_b64_tr_b16 v[78:79], v108 offset:30720\n\t"
  "s_mov_b32 s46, 1343488\n\t"
  "v_mfma_f32_16x16x32_bf16 v[28:31], a[68:71], v[84:87], v[28:31]\n\t"
  "ds_read_b64_tr_b16 v[80:81], v109 offset:28672\n\t"
  "v_mov_b32 v112, 0\n\t"
  "v_mfma_f32_16x16x32_bf16 v[32:35], a[60:63], v[88:91], v[32:35]\n\t"
  "ds_read_b64_tr_b16 v[82:83], v109 offset:30720\n\t"
  "buffer_load_dwordx4 v4, %[srd], s46 offen offset:0 sc1 lds\n\t"
  "v_mfma_f32_16x16x32_bf16 v[36:39], a[64:67], v[88:91], v[36:39]\n\t"
  "buffer_load_dwordx2 v[138:139], v2, %[srd], 0 offen sc1\n\t"
  "v_mov_b32 v113, 0\n\t"
  "v_mfma_f32_16x16x32_bf16 v[40:43], a[68:71], v[88:91], v[40:43]\n\t"
  "s_waitcnt lgkmcnt(4)\n\t"
  "v_mov_b32 v114, 0\n\t"
  "v_mfma_f32_16x16x32_bf16 v[20:23], a[72:75], v[68:71], v[20:23]\n\t"
  "ds_read_b64_tr_b16 v[84:85], v108 offset:32768\n\t"
  "buffer_load_dwordx4 v4, %[srd], s46 offen offset:1024 sc1 lds\n\t"
  "v_mfma_f32_16x16x32_bf16 v[24:27], a[76:79], v[68:71], v[24:27]\n\t"
  "ds_read_b64_tr_b16 v[86:87], v108 offset:34816\n\t"
  "v_mov_b32 v115, 0\n\t"
  "v_mfma_f32_16x16x32_bf16 v[28:31], a[80:83], v[68:71], v[28:31]\n\t"
  "ds_read_b64_tr_b16 v[88:89], v109 offset:32768\n\t"
  "buffer_load_dwordx4 v4, %[srd], s46 offen offset:2048 sc1 lds\n\t"
  "v_mfma_f32_16x16x32_bf16 v[32:35], a[72:75], v[72:75], v[32:35]\n\t"
  "ds_read_b64_tr_b16 v[90:91], v109 offset:34816\n\t"
  "v_mov_b32 v116, 0\n\t"
  "v_mfma_f32_16x16x32_bf16 v[36:39], a[76:79], v[72:75], v[36:39]\n\t"
  "buffer_load_dwordx4 v4, %[srd], s46 offen offset:3072 sc1 lds\n\t"
  "v_mov_b32 v117, 0\n\t"
  "v_mfma_f32_16x16x32_bf16 v[40:43], a[80:83], v[72:75], v[40:43]\n\t"
  "s_waitcnt lgkmcnt(4)\n\t"
  "v_mov_b32 v118, 0\n\t"
  "v_mfma_f32_16x16x32_bf16 v[20:23], a[84:87], v[76:79], v[20:23]\n\t"
  "ds_read_b64_tr_b16 v[68:69], v108 offset:36864\n\t"
  "v_mov_b32 v119, 0\n\t"
  "v_mfma_f32_16x16x32_bf16 v[24:27], a[88:91], v[76:79], v[24:27]\n\t"
  "ds_read_b64_tr_b16 v[70:71], v108 offset:38912\n\t"
  "s_add_u32 m0, s52, 131072\n\t"
  "v_mfma_f32_16x16x32_bf16 v[28:31], a[92:95], v[76:79], v[28:31]\n\t"
  "ds_read_b64_tr_b16 v[72:73], v109 offset:36864\n\t"
  "v_mov_b32 v120, 0\n\t"
  "v_mfma_f32_16x16x32_bf16 v[32:35], a[84:87], v[80:83], v[32:35]\n\t"
  "ds_read_b64_tr_b16 v[74:75], v109 offset:38912\n\t"
  "s_mov_b32 s46, 1359872\n\t"
  "v_mfma_f32_16x16x32_bf16 v[36:39], a[88:91], v[80:83], v[36:39]\n\t"
  "v_mov_b32 v121, 0\n\t"
  "v_mfma_f32_16x16x32_bf16 v[40:43], a[92:95], v[80:83], v[40:43]\n\t"
  "s_waitcnt lgkmcnt(4)\n\t"
  "v_mfma_f32_16x16x32_bf16 v[20:23], a[96:99], v[84:87], v[20:23]\n\t"
  "ds_read_b64_tr_b16 v[76:77], v110 offset:0\n\t"
  "buffer_load_dwordx4 v4, %[srd], s46 offen offset:0 sc1 lds\n\t"
  "v_mfma_f32_16x16x32_bf16 v[24:27], a[100:103], v[84:87], v[24:27]\n\t"
  "ds_read_b64_tr_b16 v[78:79], v110 offset:2048\n\t"
  "v_mfma_f32_16x16x32_bf16 v[28:31], a[104:107], v[84:87], v[28:31]\n\t"
  "ds_read_b64_tr_b16 v[80:81], v111 offset:0\n\t"
  "buffer_load_dwordx4 v4, %[srd], s46 offen offset:1024 sc1 lds\n\t"
  "v_mfma_f32_16x16x32_bf16 v[32:35], a[96:99], v[88:91], v[32:35]\n\t"
  "ds_read_b64_tr_b16 v[82:83], v111 offset:2048\n\t"
  "v_mfma_f32_16x16x32_bf16 v[36:39], a[100:103], v[88:91], v[36:39]\n\t"
  "buffer_load_dwordx4 v4, %[srd], s46 offen offset:2048 sc1 lds\n\t"
  "v_mfma_f32_16x16x32_bf16 v[40:43], a[104:107], v[88:91], v[40:43]\n\t"
  "s_waitcnt lgkmcnt(4)\n\t"
  "v_mfma_f32_16x16x32_bf16 v[20:23], a[108:111], v[68:71], v[20:23]\n\t"
  "ds_read_b64_tr_b16 v[84:85], v110 offset:4096\n\t"
  "v_mfma_f32_16x16x32_bf16 v[24:27], a[112:115], v[68:71], v[24:27]\n\t"
  "ds_read_b64_tr_b16 v[86:87], v110 offset:6144\n\t"
  "buffer_load_dwordx4 v4, %[srd], s46 offen offset:3072 sc1 lds\n\t"
  "v_mfma_f32_16x16x32_bf16 v[28:31], a[116:119], v[68:71], v[28:31]\n\t"
  "ds_read_b64_tr_b16 v[88:89], v111 offset:4096\n\t"
  "v_mfma_f32_16x16x32_bf16 v[32:35], a[108:111], v[72:75], v[32:35]\n\t"
  "ds_read_b64_tr_b16 v[90:91], v111 offset:6144\n\t"
  "s_add_u32 m0, s52, 147456\n\t"
  "v_mfma_f32_16x16x32_bf16 v[36:39], a[112:115], v[72:75], v[36:39]\n\t"
  "v_mfma_f32_16x16x32_bf16 v[40:43], a[116:119], v[72:75], v[40:43]\n\t"
  "s_waitcnt lgkmcnt(4)\n\t"
  "v_mfma_f32_16x16x32_bf16 v[20:23], a[120:123], v[76:79], v[20:23]\n\t"
  "ds_read_b64_tr_b16 v[68:69], v110 offset:8192\n\t"
  "v_xor_b32 v108, 64, v5\n\t"
  "v_mfma_f32_16x16x32_bf16 v[24:27], a[124:127], v[76:79], v[24:27]\n\t"
  "ds_read_b64_tr_b16 v[70:71], v110 offset:10240\n\t"
  "v_add_u32 v108, 0, v108\n\t"
  "v_mfma_f32_16x16x32_bf16 v[28:31], a[128:131], v[76:79], v[28:31]\n\t"
  "ds_read_b64_tr_b16 v[72:73], v111 offset:8192\n\t"
  "v_xor_b32 v109, 96, v5\n\t"
  "v_mfma_f32_16x16x32_bf16 v[32:35], a[120:123], v[80:83], v[32:35]\n\t"
  "ds_read_b64_tr_b16 v[74:75], v111 offset:10240\n\t"
  "v_add_u32 v109, 0, v109\n\t"
  "v_mfma_f32_16x16x32_bf16 v[36:39], a[124:127], v[80:83], v[36:39]\n\t"
  "s_mov_b32 s46, 1376256\n\t"
  "buffer_load_dwordx4 v4, %[srd], s46 offen offset:0 sc1 lds\n\t"
  "v_mfma_f32_16x16x32_bf16 v[40:43], a[128:131], v[80:83], v[40:43]\n\t"
  "s_waitcnt lgkmcnt(4)\n\t"
  "buffer_load_dwordx4 v4, %[srd], s46 offen offset:1024 sc1 lds\n\t"
  "v_mfma_f32_16x16x32_bf16 v[20:23], a[132:135], v[84:87], v[20:23]\n\t"
  "ds_read_b64_tr_b16 v[76:77], v110 offset:12288\n\t"
  "v_mfma_f32_16x16x32_bf16 v[24:27], a[136:139], v[84:87], v[24:27]\n\t"
  "ds_read_b64_tr_b16 v[78:79], v110 offset:14336\n\t"
  "buffer_load_dwordx4 v4, %[srd], s46 offen offset:2048 sc1 lds\n\t"
  "v_mfma_f32_16x16x32_bf16 v[28:31], a[140:143], v[84:87], v[28:31]\n\t"
  "ds_read_b64_tr_b16 v[80:81], v111 offset:12288\n\t"
  "v_mfma_f32_16x16x32_bf16 v[32:35], a[132:135], v[88:91], v[32:35]\n\t"
  "ds_read_b64_tr_b16 v[82:83], v111 offset:14336\n\t"
  "buffer_load_dwordx4 v4, %[srd], s46 offen offset:3072 sc1 lds\n\t"
  "v_mfma_f32_16x16x32_bf16 v[36:39], a[136:139], v[88:91], v[36:39]\n\t"
  "v_mfma_f32_16x16x32_bf16 v[40:43], a[140:143], v[88:91], v[40:43]\n\t"
  "s_waitcnt lgkmcnt(4)\n\t"
  "v_mfma_f32_16x16x32_bf16 v[20:23], a[144:147], v[68:71], v[20:23]\n\t"
  "ds_read_b64_tr_b16 v[84:85], v110 offset:16384\n\t"
  "v_mfma_f32_16x16x32_bf16 v[24:27], a[148:151], v[68:71], v[24:27]\n\t"
  "ds_read_b64_tr_b16 v[86:87], v110 offset:18432\n\t"
  "v_mfma_f32_16x16x32_bf16 v[28:31], a[152:155], v[68:71], v[28:31]\n\t"
  "ds_read_b64_tr_b16 v[88:89], v111 offset:16384\n\t"
  "v_mfma_f32_16x16x32_bf16 v[32:35], a[144:147], v[72:75], v[32:35]\n\t"
  "ds_read_b64_tr_b16 v[90:91], v111 offset:18432\n\t"
  "v_mfma_f32_16x16x32_bf16 v[36:39], a[148:151], v[72:75], v[36:39]\n\t"
  "v_mfma_f32_16x16x32_bf16 v[40:43], a[152:155], v[72:75], v[40:43]\n\t"
  "s_waitcnt lgkmcnt(4)\n\t"
  "v_mfma_f32_16x16x32_bf16 v[20:23], a[156:159], v[76:79], v[20:23]\n\t"
  "ds_read_b64_tr_b16 v[68:69], v110 offset:20480\n\t"
  "v_mfma_f32_16x16x32_bf16 v[24:27], a[160:163], v[76:79], v[24:27]\n\t"
  "ds_read_b64_tr_b16 v[70:71], v110 offset:22528\n\t"
  "v_mfma_f32_16x16x32_bf16 v[28:31], a[164:167], v[76:79], v[28:31]\n\t"
  "ds_read_b64_tr_b16 v[72:73], v111 offset:20480\n\t"
  "v_mfma_f32_16x16x32_bf16 v[32:35], a[156:159], v[80:83], v[32:35]\n\t"
  "ds_read_b64_tr_b16 v[74:75], v111 offset:22528\n\t"
  "v_mfma_f32_16x16x32_bf16 v[36:39], a[160:163], v[80:83], v[36:39]\n\t"
  "v_mfma_f32_16x16x32_bf16 v[40:43], a[164:167], v[80:83], v[40:43]\n\t"
  "s_waitcnt lgkmcnt(4)\n\t"
  "v_mfma_f32_16x16x32_bf16 v[20:23], a[168:171], v[84:87], v[20:23]\n\t"
  "ds_read_b64_tr_b16 v[76:77], v110 offset:24576\n\t"
  "s_waitcnt vmcnt(11)\n\t"
  "v_cmp_ne_u64 vcc, v[132:133], v[138:139]\n\t"
  "v_mfma_f32_16x16x32_bf16 v[24:27], a[172:175], v[84:87], v[24:27]\n\t"
  "ds_read_b64_tr_b16 v[78:79], v110 offset:26624\n\t"
  "v_mfma_f32_16x16x32_bf16 v[28:31], a[176:179], v[84:87], v[28:31]\n\t"
  "ds_read_b64_tr_b16 v[80:81], v111 offset:24576\n\t"
  "s_cbranch_vccnz Lw2_slow%=\n\t"
  "Lw2_back%=:\n\t"
  "v_mfma_f32_16x16x32_bf16 v[32:35], a[168:171], v[88:91], v[32:35]\n\t"
  "ds_read_b64_tr_b16 v[82:83], v111 offset:26624\n\t"
  "v_mfma_f32_16x16x32_bf16 v[36:39], a[172:175], v[88:91], v[36:39]\n\t"
  "v_mfma_f32_16x16x32_bf16 v[40:43], a[176:179], v[88:91], v[40:43]\n\t"
  "s_waitcnt lgkmcnt(4)\n\t"
  "v_mfma_f32_16x16x32_bf16 v[20:23], a[180:183], v[68:71], v[20:23]\n\t"
  "ds_read_b64_tr_b16 v[84:85], v110 offset:28672\n\t"
  "v_lshrrev_b32 v138, 1, v1\n\t"
  "v_mfma_f32_16x16x32_bf16 v[24:27], a[184:187], v[68:71], v[24:27]\n\t"
  "ds_read_b64_tr_b16 v[86:87], v110 offset:30720\n\t"
  "buffer_load_dwordx2 a[240:241], v138, %[srd], %[sUB] offen offset:0 sc1\n\t"
  "v_mfma_f32_16x16x32_bf16 v[28:31], a[188:191], v[68:71], v[28:31]\n\t"
  "ds_read_b64_tr_b16 v[88:89], v111 offset:28672\n\t"
  "buffer_load_dwordx2 a[242:243], v138, %[srd], %[sUB] offen offset:512 sc1\n\t"
  "v_mfma_f32_16x16x32_bf16 v[32:35], a[180:183], v[72:75], v[32:35]\n\t"
  "ds_read_b64_tr_b16 v[90:91], v111 offset:30720\n\t"
  "buffer_load_dwordx2 a[244:245], v138, %[srd], %[sUB] offen offset:1024 sc1\n\t"
  "v_mfma_f32_16x16x32_bf16 v[36:39], a[184:187], v[72:75], v[36:39]\n\t"
  "s_add_u32 s45, %[sW2], 0\n\t"
  "buffer_load_dwordx4 v[144:147], v1, %[srd], s45 offen offset:0 sc1\n\t"
  "v_mfma_f32_16x16x32_bf16 v[40:43], a[188:191], v[72:75], v[40:43]\n\t"
  "s_waitcnt lgkmcnt(4)\n\t"
  "v_mfma_f32_16x16x32_bf16 v[20:23], a[192:195], v[76:79], v[20:23]\n\t"
  "ds_read_b64_tr_b16 v[68:69], v110 offset:32768\n\t"
  "v_mfma_f32_16x16x32_bf16 v[24:27], a[196:199], v[76:79], v[24:27]\n\t"
  "ds_read_b64_tr_b16 v[70:71], v110 offset:34816\n\t"
  "v_mfma_f32_16x16x32_bf16 v[28:31], a[200:203], v[76:79], v[28:31]\n\t"
  "ds_read_b64_tr_b16 v[72:73], v111 offset:32768\n\t"
  "v_mfma_f32_16x16x32_bf16 v[32:35], a[192:195], v[80:83], v[32:35]\n\t"
  "ds_read_b64_tr_b16 v[74:75], v111 offset:34816\n\t"
  "buffer_load_dwordx4 v[148:151], v1, %[srd], s45 offen offset:1024 sc1\n\t"
  "v_mfma_f32_16x16x32_bf16 v[36:39], a[196:199], v[80:83], v[36:39]\n\t"
  "v_mfma_f32_16x16x32_bf16 v[40:43], a[200:203], v[80:83], v[40:43]\n\t"
  "s_waitcnt lgkmcnt(4)\n\t"
  "v_mfma_f32_16x16x32_bf16 v[20:23], a[204:207], v[84:87], v[20:23]\n\t"
  "ds_read_b64_tr_b16 v[76:77], v110 offset:36864\n\t"
  "v_mfma_f32_16x16x32_bf16 v[24:27], a[208:211], v[84:87], v[24:27]\n\t"
  "ds_read_b64_tr_b16 v[78:79], v110 offset:38912\n\t"
  "v_mfma_f32_16x16x32_bf16 v[28:31], a[212:215], v[84:87], v[28:31]\n\t"
  "ds_read_b64_tr_b16 v[80:81], v111 offset:36864\n\t"
  "buffer_load_dwordx4 v[152:155], v1, %[srd], s45 offen offset:2048 sc1\n\t"
  "v_mfma_f32_16x16x32_bf16 v[32:35], a[204:207], v[88:91], v[32:35]\n\t"
  "ds_read_b64_tr_b16 v[82:83], v111 offset:38912\n\t"
  "v_mfma_f32_16x16x32_bf16 v[36:39], a[208:211], v[88:91], v[36:39]\n\t"
  "v_mfma_f32_16x16x32_bf16 v[40:43], a[212:215], v[88:91], v[40:43]\n\t"
  "s_waitcnt lgkmcnt(4)\n\t"
  "v_mfma_f32_16x16x32_bf16 v[20:23], a[216:219], v[68:71], v[20:23]\n\t"
  "ds_read_b64_tr_b16 v[84:85], v108 offset:0\n\t"
  "v_mfma_f32_16x16x32_bf16 v[24:27], a[220:223], v[68:71], v[24:27]\n\t"
  "ds_read_b64_tr_b16 v[86:87], v108 offset:2048\n\t"
  "buffer_load_dwordx4 v[156:159], v1, %[srd], s45 offen offset:3072 sc1\n\t"
  "v_mfma_f32_16x16x32_bf16 v[28:31], a[224:227], v[68:71], v[28:31]\n\t"
  "ds_read_b64_tr_b16 v[88:89], v109 offset:0\n\t"
  "v_mfma_f32_16x16x32_bf16 v[32:35], a[216:219], v[72:75], v[32:35]\n\t"
  "ds_read_b64_tr_b16 v[90:91], v109 offset:2048\n\t"
  "v_mfma_f32_16x16x32_bf16 v[36:39], a[220:223], v[72:75], v[36:39]\n\t"
  "v_mfma_f32_16x16x32_bf16 v[40:43], a[224:227], v[72:75], v[40:43]\n\t"
  "s_waitcnt lgkmcnt(4)\n\t"
  "v_mfma_f32_16x16x32_bf16 v[20:23], a[228:231], v[76:79], v[20:23]\n\t"
  "ds_read_b64_tr_b16 v[68:69], v108 offset:4096\n\t"
  "s_add_u32 s45, %[sW2], 4096\n\t"
  "v_mfma_f32_16x16x32_bf16 v[24:27], a[232:235], v[76:79], v[24:27]\n\t"
  "ds_read_b64_tr_b16 v[70:71], v108 offset:6144\n\t"
  "v_mfma_f32_16x16x32_bf16 v[28:31], a[236:239], v[76:79], v[28:31]\n\t"
  "ds_read_b64_tr_b16 v[72:73], v109 offset:4096\n\t"
  "v_mfma_f32_16x16x32_bf16 v[32:35], a[228:231], v[80:83], v[32:35]\n\t"
  "ds_read_b64_tr_b16 v[74:75], v109 offset:6144\n\t"
  "v_mfma_f32_16x16x32_bf16 v[36:39], a[232:235], v[80:83], v[36:39]\n\t"
  "v_mfma_f32_16x16x32_bf16 v[40:43], a[236:239], v[80:83], v[40:43]\n\t"
  "s_waitcnt lgkmcnt(4)\n\t"
  "buffer_load_dwordx4 v[160:163], v1, %[srd], s45 offen offset:0 sc1\n\t"
  "v_mfma_f32_16x16x32_bf16 v[44:47], a[0:3], v[84:87], 0\n\t"
  "ds_read_b64_tr_b16 v[76:77], v108 offset:8192\n\t"
  "v_xor_b32 v110, 64, v5\n\t"
  "v_mfma_f32_16x16x32_bf16 v[48:51], a[4:7], v[84:87], 0\n\t"
  "ds_read_b64_tr_b16 v[78:79], v108 offset:10240\n\t"
  "v_add_u32 v110, 40960, v110\n\t"
  "v_mfma_f32_16x16x32_bf16 v[52:55], a[8:11], v[84:87], 0\n\t"
  "ds_read_b64_tr_b16 v[80:81], v109 offset:8192\n\t"
  "v_xor_b32 v111, 96, v5\n\t"
  "v_mfma_f32_16x16x32_bf16 v[56:59], a[0:3], v[88:91], 0\n\t"
  "ds_read_b64_tr_b16 v[82:83], v109 offset:10240\n\t"
  "v_add_u32 v111, 40960, v111\n\t"
  "v_mfma_f32_16x16x32_bf16 v[60:63], a[4:7], v[88:91], 0\n\t"
  "buffer_load_dwordx4 v[164:167], v1, %[srd], s45 offen offset:1024 sc1\n\t"
  "v_mfma_f32_16x16x32_bf16 v[64:67], a[8:11], v[88:91], 0\n\t"
  "s_waitcnt lgkmcnt(4)\n\t"
  "v_mfma_f32_16x16x32_bf16 v[44:47], a[12:15], v[68:71], v[44:47]\n\t"
  "ds_read_b64_tr_b16 v[84:85], v108 offset:12288\n\t"
  "buffer_load_dwordx2 v[92:93], v136, %[srd], %[sST] offen offset:0 sc1\n\t"
  "v_mfma_f32_16x16x32_bf16 v[48:51], a[16:19], v[68:71], v[48:51]\n\t"
  "ds_read_b64_tr_b16 v[86:87], v108 offset:14336\n\t"
  "buffer_load_dwordx2 v[94:95], v136, %[srd], %[sST] offen offset:1024 sc1\n\t"
  "v_mfma_f32_16x16x32_bf16 v[52:55], a[20:23], v[68:71], v[52:55]\n\t"
  "ds_read_b64_tr_b16 v[88:89], v109 offset:12288\n\t"
  "buffer_load_dwordx2 v[96:97], v136, %[srd], %[sST] offen offset:2048 sc1\n\t"
  "v_mfma_f32_16x16x32_bf16 v[56:59], a[12:15], v[72:75], v[56:59]\n\t"
  "ds_read_b64_tr_b16 v[90:91], v109 offset:14336\n\t"
  "buffer_load_dwordx2 v[98:99], v136, %[srd], %[sST] offen offset:3072 sc1\n\t"
  "v_mfma_f32_16x16x32_bf16 v[60:63], a[16:19], v[72:75], v[60:63]\n\t"
  "buffer_load_dwordx4 v[168:171], v1, %[srd], s45 offen offset:2048 sc1\n\t"
  "v_mfma_f32_16x16x32_bf16 v[64:67], a[20:23], v[72:75], v[64:67]\n\t"
  "s_waitcnt lgkmcnt(4)\n\t"
  "v_mfma_f32_16x16x32_bf16 v[44:47], a[24:27], v[76:79], v[44:47]\n\t"
  "ds_read_b64_tr_b16 v[68:69], v108 offset:16384\n\t"
  "v_mfma_f32_16x16x32_bf16 v[48:51], a[28:31], v[76:79], v[48:51]\n\t"
  "ds_read_b64_tr_b16 v[70:71], v108 offset:18432\n\t"
  "v_mfma_f32_16x16x32_bf16 v[52:55], a[32:35], v[76:79], v[52:55]\n\t"
  "ds_read_b64_tr_b16 v[72:73], v109 offset:16384\n\t"
  "s_waitcnt vmcnt(14)\n\t"
  "s_barrier\n\t"
  "v_mfma_f32_16x16x32_bf16 v[56:59], a[24:27], v[80:83], v[56:59]\n\t"
  "ds_read_b64_tr_b16 v[74:75], v109 offset:18432\n\t"
  "buffer_load_dwordx4 v[172:175], v1, %[srd], s45 offen offset:3072 sc1\n\t"
  "v_mfma_f32_16x16x32_bf16 v[60:63], a[28:31], v[80:83], v[60:63]\n\t"
  "ds_read_u16_d16_hi v112, v125 offset:0\n\t"
  "ds_read_b64_tr_b16 a[246:247], v10 offset:0\n\t"
  "v_mfma_f32_16x16x32_bf16 v[64:67], a[32:35], v[80:83], v[64:67]\n\t"
  "s_waitcnt lgkmcnt(6)\n\t"
  "ds_read_u16_d16_hi v113, v125 offset:8192\n\t"
  "v_mfma_f32_16x16x32_bf16 v[44:47], a[36:39], v[84:87], v[44:47]\n\t"
  "ds_read_b64_tr_b16 v[76:77], v108 offset:20480\n\t"
  "ds_read_u16_d16_hi v114, v125 offset:16384\n\t"
  "v_mfma_f32_16x16x32_bf16 v[48:51], a[40:43], v[84:87], v[48:51]\n\t"
  "ds_read_b64_tr_b16 v[78:79], v108 offset:22528\n\t"
  "s_add_u32 s45, %[sW2], 8192\n\t"
  "v_mfma_f32_16x16x32_bf16 v[52:55], a[44:47], v[84:87], v[52:55]\n\t"
  "ds_read_b64_tr_b16 v[80:81], v109 offset:20480\n\t"
  "ds_read_u16_d16_hi v115, v125 offset:24576\n\t"
  "v_mfma_f32_16x16x32_bf16 v[56:59], a[36:39], v[88:91], v[56:59]\n\t"
  "ds_read_b64_tr_b16 v[82:83], v109 offset:22528\n\t"
  "ds_read_u16_d16_hi v116, v125 offset:32768\n\t"
  "v_mfma_f32_16x16x32_bf16 v[60:63], a[40:43], v[88:91], v[60:63]\n\t"
  "ds_read_u16_d16_hi v117, v124 offset:0\n\t"
  "ds_read_b64_tr_b16 a[248:249], v11 offset:0\n\t"
  "v_mfma_f32_16x16x32_bf16 v[64:67], a[44:47], v[88:91], v[64:67]\n\t"
  "s_waitcnt lgkmcnt(12)\n\t"
  "ds_read_u16_d16_hi v118, v124 offset:8192\n\t"
  "v_mfma_f32_16x16x32_bf16 v[44:47], a[48:51], v[68:71], v[44:47]\n\t"
  "ds_read_b64_tr_b16 v[84:85], v108 offset:24576\n\t"
  "buffer_load_dwordx4 v[176:179], v1, %[srd], s45 offen offset:0 sc1\n\t"
  "v_mfma_f32_16x16x32_bf16 v[48:51], a[52:55], v[68:71], v[48:51]\n\t"
  "ds_read_b64_tr_b16 v[86:87], v108 offset:26624\n\t"
  "ds_read_u16_d16_hi v119, v124 offset:16384\n\t"
  "v_mfma_f32_16x16x32_bf16 v[52:55], a[56:59], v[68:71], v[52:55]\n\t"
  "ds_read_b64_tr_b16 v[88:89], v109 offset:24576\n\t"
  "ds_read_u16_d16_hi v120, v124 offset:24576\n\t"
  "v_mfma_f32_16x16x32_bf16 v[56:59], a[48:51], v[72:75], v[56:59]\n\t"
  "ds_read_b64_tr_b16 v[90:91], v109 offset:26624\n\t"
  "ds_read_u16_d16_hi v121, v124 offset:32768\n\t"
  "v_mfma_f32_16x16x32_bf16 v[60:63], a[52:55], v[72:75], v[60:63]\n\t"
  "buffer_load_dwordx4 v[180:183], v1, %[srd], s45 offen offset:1024 sc1\n\t"
  "ds_read_b64_tr_b16 a[250:251], v10 offset:4096\n\t"
  "v_mfma_f32_16x16x32_bf16 v[64:67], a[56:59], v[72:75], v[64:67]\n\t"
  "s_waitcnt lgkmcnt(12)\n\t"
  "ds_read_b64_tr_b16 a[252:253], v11 offset:4096\n\t"
  "v_mfma_f32_16x16x32_bf16 v[44:47], a[60:63], v[76:79], v[44:47]\n\t"
  "ds_read_b64_tr_b16 v[68:69], v108 offset:28672\n\t"
  "ds_read_b64_tr_b16 a[254:255], v12 offset:0\n\t"
  "v_mfma_f32_16x16x32_bf16 v[48:51], a[64:67], v[76:79], v[48:51]\n\t"
  "ds_read_b64_tr_b16 v[70:71], v108 offset:30720\n\t"
  "v_mfma_f32_16x16x32_bf16 v[52:55], a[68:71], v[76:79], v[52:55]\n\t"
  "ds_read_b64_tr_b16 v[72:73], v109 offset:28672\n\t"
  "v_mfma_f32_16x16x32_bf16 v[56:59], a[60:63], v[80:83], v[56:59]\n\t"
  "ds_read_b64_tr_b16 v[74:75], v109 offset:30720\n\t"
  "buffer_load_dwordx4 v[184:187], v1, %[srd], s45 offen offset:2048 sc1\n\t"
  "v_mfma_f32_16x16x32_bf16 v[60:63], a[64:67], v[80:83], v[60:63]\n\t"
  "v_mfma_f32_16x16x32_bf16 v[64:67], a[68:71], v[80:83], v[64:67]\n\t"
  "s_waitcnt lgkmcnt(8)\n\t"
  "v_mfma_f32_16x16x32_bf16 v[44:47], a[72:75], v[84:87], v[44:47]\n\t"
  "ds_read_b64_tr_b16 v[76:77], v108 offset:32768\n\t"
  "v_mfma_f32_16x16x32_bf16 v[48:51], a[76:79], v[84:87], v[48:51]\n\t"
  "ds_read_b64_tr_b16 v[78:79], v108 offset:34816\n\t"
  "v_mul_f32 v112, v112, v142\n\t"
  "v_mfma_f32_16x16x32_bf16 v[52:55], a[80:83], v[84:87], v[52:55]\n\t"
  "ds_read_b64_tr_b16 v[80:81], v109 offset:32768\n\t"
  "buffer_load_dwordx4 v[188:191], v1, %[srd], s45 offen offset:3072 sc1\n\t"
  "v_mfma_f32_16x16x32_bf16 v[56:59], a[72:75], v[88:91], v[56:59]\n\t"
  "ds_read_b64_tr_b16 v[82:83], v109 offset:34816\n\t"
  "v_mul_f32 v113, v113, v143\n\t"
  "v_mfma_f32_16x16x32_bf16 v[60:63], a[76:79], v[88:91], v[60:63]\n\t"
  "v_mul_f32 v114, v114, v248\n\t"
  "v_mul_f32 v115, v115, v249\n\t"
  "v_mfma_f32_16x16x32_bf16 v[64:67], a[80:83], v[88:91], v[64:67]\n\t"
  "s_waitcnt lgkmcnt(4)\n\t"
  "v_mul_f32 v116, v116, v250\n\t"
  "v_mfma_f32_16x16x32_bf16 v[44:47], a[84:87], v[68:71], v[44:47]\n\t"
  "ds_read_b64_tr_b16 v[84:85], v108 offset:36864\n\t"
  "v_mul_f32 v117, v117, v251\n\t"
  "v_mfma_f32_16x16x32_bf16 v[48:51], a[88:91], v[68:71], v[48:51]\n\t"
  "ds_read_b64_tr_b16 v[86:87], v108 offset:38912\n\t"
  "s_add_u32 s45, %[sW2], 12288\n\t"
  "v_mfma_f32_16x16x32_bf16 v[52:55], a[92:95], v[68:71], v[52:55]\n\t"
  "ds_read_b64_tr_b16 v[88:89], v109 offset:36864\n\t"
  "v_mul_f32 v118, v118, v252\n\t"
  "v_mfma_f32_16x16x32_bf16 v[56:59], a[84:87], v[72:75], v[56:59]\n\t"
  "ds_read_b64_tr_b16 v[90:91], v109 offset:38912\n\t"
  "v_mul_f32 v119, v119, v253\n\t"
  "v_mfma_f32_16x16x32_bf16 v[60:63], a[88:91], v[72:75], v[60:63]\n\t"
  "v_mul_f32 v120, v120, v254\n\t"
  "v_mul_f32 v121, v121, v255\n\t"
  "v_mfma_f32_16x16x32_bf16 v[64:67], a[92:95], v[72:75], v[64:67]\n\t"
  "s_waitcnt lgkmcnt(4)\n\t"
  "v_add_f32 v122, v112, v113\n\t"
  "v_mfma_f32_16x16x32_bf16 v[44:47], a[96:99], v[76:79], v[44:47]\n\t"
  "ds_read_b64_tr_b16 v[68:69], v110 offset:0\n\t"
  "buffer_load_dwordx4 v[192:195], v1, %[srd], s45 offen offset:0 sc1\n\t"
  "v_mfma_f32_16x16x32_bf16 v[48:51], a[100:103], v[76:79], v[48:51]\n\t"
  "ds_read_b64_tr_b16 v[70:71], v110 offset:2048\n\t"
  "v_mul_f32 v123, v112, v112\n\t"
  "v_mfma_f32_16x16x32_bf16 v[52:55], a[104:107], v[76:79], v[52:55]\n\t"
  "ds_read_b64_tr_b16 v[72:73], v111 offset:0\n\t"
  "v_fmac_f32 v123, v113, v113\n\t"
  "v_mfma_f32_16x16x32_bf16 v[56:59], a[96:99], v[80:83], v[56:59]\n\t"
  "ds_read_b64_tr_b16 v[74:75], v111 offset:2048\n\t"
  "v_add_f32 v122, v122, v114\n\t"
  "v_mfma_f32_16x16x32_bf16 v[60:63], a[100:103], v[80:83], v[60:63]\n\t"
  "v_fmac_f32 v123, v114, v114\n\t"
  "v_add_f32 v122, v122, v115\n\t"
  "v_mfma_f32_16x16x32_bf16 v[64:67], a[104:107], v[80:83], v[64:67]\n\t"
  "s_waitcnt lgkmcnt(4)\n\t"
  "buffer_load_dwordx4 v[196:199], v1, %[srd], s45 offen offset:1024 sc1\n\t"
  "v_mfma_f32_16x16x32_bf16 v[44:47], a[108:111], v[84:87], v[44:47]\n\t"
  "ds_read_b64_tr_b16 v[76:77], v110 offset:4096\n\t"
  "v_fmac_f32 v123, v115, v115\n\t"
  "v_mfma_f32_16x16x32_bf16 v[48:51], a[112:115], v[84:87], v[48:51]\n\t"
  "ds_read_b64_tr_b16 v[78:79], v110 offset:6144\n\t"
  "v_add_f32 v122, v122, v116\n\t"
  "v_mfma_f32_16x16x32_bf16 v[52:55], a[116:119], v[84:87], v[52:55]\n\t"
  "ds_read_b64_tr_b16 v[80:81], v111 offset:4096\n\t"
  "v_fmac_f32 v123, v116, v116\n\t"
  "v_mfma_f32_16x16x32_bf16 v[56:59], a[108:111], v[88:91], v[56:59]\n\t"
  "ds_read_b64_tr_b16 v[82:83], v111 offset:6144\n\t"
  "v_add_f32 v122, v122, v117\n\t"
  "v_mfma_f32_16x16x32_bf16 v[60:63], a[112:115], v[88:91], v[60:63]\n\t"
  "buffer_load_dwordx4 v[200:203], v1, %[srd], s45 offen offset:2048 sc1\n\t"
  "v_fmac_f32 v123, v117, v117\n\t"
  "v_mfma_f32_16x16x32_bf16 v[64:67], a[116:119], v[88:91], v[64:67]\n\t"
  "s_waitcnt lgkmcnt(4)\n\t"
  "s_waitcnt vmcnt(11)\n\t"
  "v_min_u32 v131, v93, v95\n\t"
  "v_mfma_f32_16x16x32_bf16 v[44:47], a[120:123], v[68:71], v[44:47]\n\t"
  "ds_read_b64_tr_b16 v[84:85], v110 offset:8192\n\t"
  "s_waitcnt vmcnt(9)\n\t"
  "v_min3_u32 v131, v131, v97, v99\n\t"
  "v_mfma_f32_16x16x32_bf16 v[48:51], a[124:127], v[68:71], v[48:51]\n\t"
  "ds_read_b64_tr_b16 v[86:87], v110 offset:10240\n\t"
  "v_cmp_eq_u32 vcc, 0, v131\n\t"
  "v_mfma_f32_16x16x32_bf16 v[52:55], a[128:131], v[68:71], v[52:55]\n\t"
  "ds_read_b64_tr_b16 v[88:89], v111 offset:8192\n\t"
  "v_add_u32 v108, 81920, v5\n\t"
  "v_mfma_f32_16x16x32_bf16 v[56:59], a[120:123], v[72:75], v[56:59]\n\t"
  "ds_read_b64_tr_b16 v[90:91], v111 offset:10240\n\t"
  "s_cbranch_vccnz Lst0_slow%=\n\t"
  "Lst0_back%=:\n\t"
  "v_mfma_f32_16x16x32_bf16 v[60:63], a[124:127], v[72:75], v[60:63]\n\t"
  "v_xor_b32 v109, 32, v5\n\t"
  "v_add_u32 v109, 81920, v109\n\t"
  "v_mfma_f32_16x16x32_bf16 v[64:67], a[128:131], v[72:75], v[64:67]\n\t"
  "s_waitcnt lgkmcnt(4)\n\t"
  "buffer_load_dwordx4 v[204:207], v1, %[srd], s45 offen offset:3072 sc1\n\t"
  "v_mfma_f32_16x16x32_bf16 v[44:47], a[132:135], v[76:79], v[44:47]\n\t"
  "ds_read_b64_tr_b16 v[68:69], v110 offset:12288\n\t"
  "v_add_f32 v122, v122, v118\n\t"
  "v_mfma_f32_16x16x32_bf16 v[48:51], a[136:139], v[76:79], v[48:51]\n\t"
  "ds_read_b64_tr_b16 v[70:71], v110 offset:14336\n\t"
  "v_fmac_f32 v123, v118, v118\n\t"
  "v_mfma_f32_16x16x32_bf16 v[52:55], a[140:143], v[76:79], v[52:55]\n\t"
  "ds_read_b64_tr_b16 v[72:73], v111 offset:12288\n\t"
  "s_add_u32 s45, %[sW2], 16384\n\t"
  "v_mfma_f32_16x16x32_bf16 v[56:59], a[132:135], v[80:83], v[56:59]\n\t"
  "ds_read_b64_tr_b16 v[74:75], v111 offset:14336\n\t"
  "v_add_f32 v122, v122, v119\n\t"
  "v_mfma_f32_16x16x32_bf16 v[60:63], a[136:139], v[80:83], v[60:63]\n\t"
  "v_fmac_f32 v123, v119, v119\n\t"
  "v_add_f32 v122, v122, v120\n\t"
  "v_mfma_f32_16x16x32_bf16 v[64:67], a[140:143], v[80:83], v[64:67]\n\t"
  "v_fmac_f32 v123, v120, v120\n\t"
  "v_add_f32 v122, v122, v121\n\t"
  "v_mfma_f32_16x16x16_bf16 v[20:23], a[240:241], v[92:93], v[20:23]\n\t"
  "v_fmac_f32 v123, v121, v121\n\t"
  "v_add_f32_dpp v122, v122, v122 quad_perm:[1,0,3,2] row_mask:0xf bank_mask:0xf\n\t"
  "v_mfma_f32_16x16x16_bf16 v[24:27], a[242:243], v[92:93], v[24:27]\n\t"
  "buffer_load_dwordx4 v[208:211], v1, %[srd], s45 offen offset:0 sc1\n\t"
  "v_add_f32_dpp v123, v123, v123 quad_perm:[1,0,3,2] row_mask:0xf bank_mask:0xf\n\t"
  "v_mfma_f32_16x16x16_bf16 v[28:31], a[244:245], v[92:93], v[28:31]\n\t"
  "v_add_f32_dpp v122, v122, v122 quad_perm:[2,3,0,1] row_mask:0xf bank_mask:0xf\n\t"
  "v_add_f32_dpp v123, v123, v123 quad_perm:[2,3,0,1] row_mask:0xf bank_mask:0xf\n\t"
  "v_mfma_f32_16x16x16_bf16 v[32:35], a[240:241], v[94:95], v[32:35]\n\t"
  "v_add_f32_dpp v122, v122, v122 row_half_mirror row_mask:0xf bank_mask:0xf\n\t"
  "v_add_f32_dpp v123, v123, v123 row_half_mirror row_mask:0xf bank_mask:0xf\n\t"
  "v_mfma_f32_16x16x16_bf16 v[36:39], a[242:243], v[94:95], v[36:39]\n\t"
  "v_add_f32_dpp v122, v122, v122 row_mirror row_mask:0xf bank_mask:0xf\n\t"
  "v_add_f32_dpp v123, v123, v123 row_mirror row_mask:0xf bank_mask:0xf\n\t"
  "v_mfma_f32_16x16x16_bf16 v[40:43], a[244:245], v[94:95], v[40:43]\n\t"
  "s_waitcnt lgkmcnt(4)\n\t"
  "v_add_f32_dpp v122, v122, v122 row_bcast:15 row_mask:0xa bank_mask:0xf\n\t"
  "v_mfma_f32_16x16x32_bf16 v[44:47], a[144:147], v[84:87], v[44:47]\n\t"
  "ds_read_b64_tr_b16 v[76:77], v110 offset:16384\n\t"
  "buffer_load_dwordx4 v[212:215], v1, %[srd], s45 offen offset:1024 sc1\n\t"
  "v_mfma_f32_16x16x32_bf16 v[48:51], a[148:151], v[84:87], v[48:51]\n\t"
  "ds_read_b64_tr_b16 v[78:79], v110 offset:18432\n\t"
  "v_add_f32_dpp v123, v123, v123 row_bcast:15 row_mask:0xa bank_mask:0xf\n\t"
  "v_mfma_f32_16x16x32_bf16 v[52:55], a[152:155], v[84:87], v[52:55]\n\t"
  "ds_read_b64_tr_b16 v[80:81], v111 offset:16384\n\t"
  "v_add_f32_dpp v122, v122, v122 row_bcast:31 row_mask:0xc bank_mask:0xf\n\t"
  "v_mfma_f32_16x16x32_bf16 v[56:59], a[144:147], v[88:91], v[56:59]\n\t"
  "ds_read_b64_tr_b16 v[82:83], v111 offset:18432\n\t"
  "v_add_f32_dpp v123, v123, v123 row_bcast:31 row_mask:0xc bank_mask:0xf\n\t"
  "v_mfma_f32_16x16x32_bf16 v[60:63], a[148:151], v[88:91], v[60:63]\n\t"
  "buffer_load_dwordx4 v[216:219], v1, %[srd], s45 offen offset:2048 sc1\n\t"
  "v_mul_f32 v122, 0xbacccccd, v122\n\t"
  "v_mfma_f32_16x16x32_bf16 v[64:67], a[152:155], v[88:91], v[64:67]\n\t"
  "s_waitcnt lgkmcnt(4)\n\t"
  "v_mul_f32 v123, 0x3acccccd, v123\n\t"
  "v_fma_f32 v123, -v122, v122, v123\n\t"
  "v_max_f32 v123, 0, v123\n\t"
  "v_cvt_pk_bf16_f32 v123, 0, v123\n\t"
  "v_cvt_pk_bf16_f32 v126, v122, v122\n\t"
  "v_add_f32 v123, 0x3727c5ac, v123\n\t"
  "v_cvt_pk_bf16_f32 v123, 0, v123\n\t"
  "v_sqrt_f32 v123, v123\n\t"
  "s_nop 0\n\t"
  "v_cvt_pk_bf16_f32 v127, v123, v123\n\t"
  "buffer_store_dwordx2 v[126:127], v7, %[srd], s54 offen sc1\n\t"
  "v_mfma_f32_16x16x32_bf16 v[44:47], a[156:159], v[68:71], v[44:47]\n\t"
  "ds_read_b64_tr_b16 v[84:85], v110 offset:20480\n\t"
  "v_and_b32 v128, 0xffff0000, v93\n\t"
  "v_mfma_f32_16x16x32_bf16 v[48:51], a[160:163], v[68:71], v[48:51]\n\t"
  "ds_read_b64_tr_b16 v[86:87], v110 offset:22528\n\t"
  "v_and_b32 v129, 0xffff0000, v95\n\t"
  "v_mfma_f32_16x16x32_bf16 v[52:55], a[164:167], v[68:71], v[52:55]\n\t"
  "ds_read_b64_tr_b16 v[88:89], v111 offset:20480\n\t"
  "v_rcp_f32 v128, v128\n\t"
  "v_mfma_f32_16x16x32_bf16 v[56:59], a[156:159], v[72:75], v[56:59]\n\t"
  "ds_read_b64_tr_b16 v[90:91], v111 offset:22528\n\t"
  "buffer_load_dwordx4 v[220:223], v1, %[srd], s45 offen offset:3072 sc1\n\t"
  "v_mfma_f32_16x16x32_bf16 v[60:63], a[160:163], v[72:75], v[60:63]\n\t"
  "v_rcp_f32 v129, v129\n\t"
  "v_mul_f32 v128, v128, v128\n\t"
  "v_mfma_f32_16x16x32_bf16 v[64:67], a[164:167], v[72:75], v[64:67]\n\t"
  "s_waitcnt lgkmcnt(4)\n\t"
  "v_mul_f32 v129, v129, v129\n\t"
  "v_mfma_f32_16x16x32_bf16 v[44:47], a[168:171], v[76:79], v[44:47]\n\t"
  "ds_read_b64_tr_b16 v[68:69], v110 offset:24576\n\t"
  "v_max_f32 v20, 0, v20\n\t"
  "v_mfma_f32_16x16x32_bf16 v[48:51], a[172:175], v[76:79], v[48:51]\n\t"
  "ds_read_b64_tr_b16 v[70:71], v110 offset:26624\n\t"
  "v_max_f32 v21, 0, v21\n\t"
  "v_mfma_f32_16x16x32_bf16 v[52:55], a[176:179], v[76:79], v[52:55]\n\t"
  "ds_read_b64_tr_b16 v[72:73], v111 offset:24576\n\t"
  "s_add_u32 s45, %[sW2], 20480\n\t"
  "v_mfma_f32_16x16x32_bf16 v[56:59], a[168:171], v[80:83], v[56:59]\n\t"
  "ds_read_b64_tr_b16 v[74:75], v111 offset:26624\n\t"
  "v_mul_f32 v22, v22, v128\n\t"
  "v_mfma_f32_16x16x32_bf16 v[60:63], a[172:175], v[80:83], v[60:63]\n\t"
  "v_mul_f32 v23, v23, v128\n\t"
  "v_mul_f32 v20, v20, v22\n\t"
  "v_mfma_f32_16x16x32_bf16 v[64:67], a[176:179], v[80:83], v[64:67]\n\t"
  "s_waitcnt lgkmcnt(4)\n\t"
  "v_mul_f32 v21, v21, v23\n\t"
  "v_mfma_f32_16x16x32_bf16 v[44:47], a[180:183], v[84:87], v[44:47]\n\t"
  "ds_read_b64_tr_b16 v[76:77], v110 offset:28672\n\t"
  "v_cvt_pk_bf16_f32 v116, v20, v21\n\t"
  "v_mfma_f32_16x16x32_bf16 v[48:51], a[184:187], v[84:87], v[48:51]\n\t"
  "ds_read_b64_tr_b16 v[78:79], v110 offset:30720\n\t"
  "buffer_load_dwordx4 v[224:227], v1, %[srd], s45 offen offset:0 sc1\n\t"
  "v_mfma_f32_16x16x32_bf16 v[52:55], a[188:191], v[84:87], v[52:55]\n\t"
  "ds_read_b64_tr_b16 v[80:81], v111 offset:28672\n\t"
  "v_cvt_pk_bf16_f32 v112, v20, v21\n\t"
  "v_mfma_f32_16x16x32_bf16 v[56:59], a[180:183], v[88:91], v[56:59]\n\t"
  "ds_read_b64_tr_b16 v[82:83], v111 offset:30720\n\t"
  "v_max_f32 v24, 0, v24\n\t"
  "v_mfma_f32_16x16x32_bf16 v[60:63], a[184:187], v[88:91], v[60:63]\n\t"
  "v_max_f32 v25, 0, v25\n\t"
  "v_mul_f32 v26, v26, v128\n\t"
  "v_mfma_f32_16x16x32_bf16 v[64:67], a[188:191], v[88:91], v[64:67]\n\t"
  "s_waitcnt lgkmcnt(4)\n\t"
  "v_mul_f32 v27, v27, v128\n\t"
  "v_mfma_f32_16x16x32_bf16 v[44:47], a[192:195], v[68:71], v[44:47]\n\t"
  "ds_read_b64_tr_b16 v[84:85], v110 offset:32768\n\t"
  "buffer_load_dwordx4 v[228:231], v1, %[srd], s45 offen offset:1024 sc1\n\t"
  "v_mfma_f32_16x16x32_bf16 v[48:51], a[196:199], v[68:71], v[48:51]\n\t"
  "ds_read_b64_tr_b16 v[86:87], v110 offset:34816\n\t"
  "v_mul_f32 v24, v24, v26\n\t"
  "v_mfma_f32_16x16x32_bf16 v[52:55], a[200:203], v[68:71], v[52:55]\n\t"
  "ds_read_b64_tr_b16 v[88:89], v111 offset:32768\n\t"
  "v_mul_f32 v25, v25, v27\n\t"
  "v_mfma_f32_16x16x32_bf16 v[56:59], a[192:195], v[72:75], v[56:59]\n\t"
  "ds_read_b64_tr_b16 v[90:91], v111 offset:34816\n\t"
  "v_cvt_pk_bf16_f32 v117, v24, v25\n\t"
  "v_mfma_f32_16x16x32_bf16 v[60:63], a[196:199], v[72:75], v[60:63]\n\t"
  "v_cvt_pk_bf16_f32 v113, v24, v25\n\t"
  "v_max_f32 v28, 0, v28\n\t"
  "v_mfma_f32_16x16x32_bf16 v[64:67], a[200:203], v[72:75], v[64:67]\n\t"
  "s_waitcnt lgkmcnt(4)\n\t"
  "buffer_load_dwordx4 v[232:235], v1, %[srd], s45 offen offset:2048 sc1\n\t"
  "v_mfma_f32_16x16x32_bf16 v[44:47], a[204:207], v[76:79], v[44:47]\n\t"
  "ds_read_b64_tr_b16 v[68:69], v110 offset:36864\n\t"
  "v_max_f32 v29, 0, v29\n\t"
  "v_mfma_f32_16x16x32_bf16 v[48:51], a[208:211], v[76:79], v[48:51]\n\t"
  "ds_read_b64_tr_b16 v[70:71], v110 offset:38912\n\t"
  "v_mul_f32 v30, v30, v128\n\t"
  "v_mfma_f32_16x16x32_bf16 v[52:55], a[212:215], v[76:79], v[52:55]\n\t"
  "ds_read_b64_tr_b16 v[72:73], v111 offset:36864\n\t"
  "v_mul_f32 v31, v31, v128\n\t"
  "v_mfma_f32_16x16x32_bf16 v[56:59], a[204:207], v[80:83], v[56:59]\n\t"
  "ds_read_b64_tr_b16 v[74:75], v111 offset:38912\n\t"
  "v_mul_f32 v28, v28, v30\n\t"
  "v_mfma_f32_16x16x32_bf16 v[60:63], a[208:211], v[80:83], v[60:63]\n\t"
  "buffer_load_dwordx4 v[236:239], v1, %[srd], s45 offen offset:3072 sc1\n\t"
  "v_permlane16_swap_b32 v116, v117\n\t"
  "v_mfma_f32_16x16x32_bf16 v[64:67], a[212:215], v[80:83], v[64:67]\n\t"
  "s_waitcnt lgkmcnt(4)\n\t"
  "v_mul_f32 v29, v29, v31\n\t"
  "v_mfma_f32_16x16x32_bf16 v[44:47], a[216:219], v[84:87], v[44:47]\n\t"
  "ds_read_b64_tr_b16 v[76:77], v108 offset:0\n\t"
  "v_cvt_pk_bf16_f32 v118, v28, v29\n\t"
  "v_mfma_f32_16x16x32_bf16 v[48:51], a[220:223], v[84:87], v[48:51]\n\t"
  "ds_read_b64_tr_b16 v[78:79], v108 offset:2048\n\t"
  "v_cvt_pk_bf16_f32 v114, v28, v29\n\t"
  "v_mfma_f32_16x16x32_bf16 v[52:55], a[224:227], v[84:87], v[52:55]\n\t"
  "ds_read_b64_tr_b16 v[80:81], v109 offset:0\n\t"
  "s_mov_b32 s55, 0\n\t"
  "v_mfma_f32_16x16x32_bf16 v[56:59], a[216:219], v[88:91], v[56:59]\n\t"
  "ds_read_b64_tr_b16 v[82:83], v109 offset:2048\n\t"
  "s_add_u32 s45, %[sW2], 24576\n\t"
  "v_mfma_f32_16x16x32_bf16 v[60:63], a[220:223], v[88:91], v[60:63]\n\t"
  "v_permlane16_swap_b32 v118, v115\n\t"
  "v_mfma_f32_16x16x32_bf16 v[64:67], a[224:227], v[88:91], v[64:67]\n\t"
  "s_waitcnt lgkmcnt(4)\n\t"
  "v_permlane32_swap_b32 v117, v115\n\t"
  "v_mfma_f32_16x16x32_bf16 v[44:47], a[228:231], v[68:71], v[44:47]\n\t"
  "ds_read_b64_tr_b16 v[84:85], v108 offset:4096\n\t"
  "buffer_store_dwordx4 v[112:115], v8, %[srd], s55 offen sc1\n\t"
  "v_mfma_f32_16x16x32_bf16 v[48:51], a[232:235], v[68:71], v[48:51]\n\t"
  "ds_read_b64_tr_b16 v[86:87], v108 offset:6144\n\t"
  "v_max_f32 v32, 0, v32\n\t"
  "v_mfma_f32_16x16x32_bf16 v[52:55], a[236:239], v[68:71], v[52:55]\n\t"
  "ds_read_b64_tr_b16 v[88:89], v109 offset:4096\n\t"
  "buffer_load_dwordx4 v[240:243], v1, %[srd], s45 offen offset:0 sc1\n\t"
  "v_mfma_f32_16x16x32_bf16 v[56:59], a[228:231], v[72:75], v[56:59]\n\t"
  "ds_read_b64_tr_b16 v[90:91], v109 offset:6144\n\t"
  "v_max_f32 v33, 0, v33\n\t"
  "v_mfma_f32_16x16x32_bf16 v[60:63], a[232:235], v[72:75], v[60:63]\n\t"
  "v_mul_f32 v34, v34, v129\n\t"
  "v_mul_f32 v35, v35, v129\n\t"
  "v_mfma_f32_16x16x32_bf16 v[64:67], a[236:239], v[72:75], v[64:67]\n\t"
  "v_mul_f32 v32, v32, v34\n\t"
  "v_mul_f32 v33, v33, v35\n\t"
  "v_mfma_f32_16x16x16_bf16 v[44:47], a[240:241], v[96:97], v[44:47]\n\t"
  "v_cvt_pk_bf16_f32 v124, v32, v33\n\t"
  "v_cvt_pk_bf16_f32 v120, v32, v33\n\t"
  "v_mfma_f32_16x16x16_bf16 v[48:51], a[242:243], v[96:97], v[48:51]\n\t"
  "buffer_load_dwordx4 v[244:247], v1, %[srd], s45 offen offset:1024 sc1\n\t"
  "v_max_f32 v36, 0, v36\n\t"
  "v_mfma_f32_16x16x16_bf16 v[52:55], a[244:245], v[96:97], v[52:55]\n\t"
  "v_max_f32 v37, 0, v37\n\t"
  "v_mul_f32 v38, v38, v129\n\t"
  "v_mfma_f32_16x16x16_bf16 v[56:59], a[240:241], v[98:99], v[56:59]\n\t"
  "v_mul_f32 v39, v39, v129\n\t"
  "v_mul_f32 v36, v36, v38\n\t"
  "v_mfma_f32_16x16x16_bf16 v[60:63], a[242:243], v[98:99], v[60:63]\n\t"
  "v_mul_f32 v37, v37, v39\n\t"
  "v_cvt_pk_bf16_f32 v125, v36, v37\n\t"
  "v_cvt_pk_bf16_f32 v121, v36, v37\n\t"
  "v_max_f32 v40, 0, v40\n\t"
  "v_max_f32 v41, 0, v41\n\t"
  "v_mul_f32 v42, v42, v129\n\t"
  "v_mul_f32 v43, v43, v129\n\t"
  "v_mul_f32 v40, v40, v42\n\t"
  "v_permlane16_swap_b32 v124, v125\n\t"
  "v_mul_f32 v41, v41, v43\n\t"
  "v_cvt_pk_bf16_f32 v126, v40, v41\n\t"
  "v_cvt_pk_bf16_f32 v122, v40, v41\n\t"
  "s_mov_b32 s55, 49152\n\t"
  "v_permlane16_swap_b32 v126, v123\n\t"
  "s_nop 1\n\t"
  "v_permlane32_swap_b32 v125, v123\n\t"
  "buffer_store_dwordx4 v[120:123], v8, %[srd], s55 offen sc1\n\t"
  "v_mfma_f32_16x16x16_bf16 v[64:67], a[244:245], v[98:99], v[64:67]\n\t"
  "s_waitcnt lgkmcnt(4)\n\t"
  "v_mfma_f32_16x16x32_bf16 v[20:23], a[0:3], v[76:79], 0\n\t"
  "ds_read_b64_tr_b16 v[68:69], v108 offset:8192\n\t"
  "v_add_u32 v110, 122880, v5\n\t"
  "v_mfma_f32_16x16x32_bf16 v[24:27], a[4:7], v[76:79], 0\n\t"
  "ds_read_b64_tr_b16 v[70:71], v108 offset:10240\n\t"
  "v_xor_b32 v111, 32, v5\n\t"
  "v_mfma_f32_16x16x32_bf16 v[28:31], a[8:11], v[76:79], 0\n\t"
  "ds_read_b64_tr_b16 v[72:73], v109 offset:8192\n\t"
  "v_add_u32 v111, 122880, v111\n\t"
  "v_mfma_f32_16x16x32_bf16 v[32:35], a[0:3], v[80:83], 0\n\t"
  "ds_read_b64_tr_b16 v[74:75], v109 offset:10240\n\t"
  "v_and_b32 v128, 0xffff0000, v97\n\t"
  "v_mfma_f32_16x16x32_bf16 v[36:39], a[4:7], v[80:83], 0\n\t"
  "v_and_b32 v129, 0xffff0000, v99\n\t"
  "v_rcp_f32 v128, v128\n\t"
  "v_mfma_f32_16x16x32_bf16 v[40:43], a[8:11], v[80:83], 0\n\t"
  "s_waitcnt lgkmcnt(4)\n\t"
  "v_rcp_f32 v129, v129\n\t"
  "v_mfma_f32_16x16x32_bf16 v[20:23], a[12:15], v[84:87], v[20:23]\n\t"
  "ds_read_b64_tr_b16 v[76:77], v108 offset:12288\n\t"
  "s_barrier\n\t"
  "v_mfma_f32_16x16x32_bf16 v[24:27], a[16:19], v[84:87], v[24:27]\n\t"
  "ds_read_b64_tr_b16 v[78:79], v108 offset:14336\n\t"
  "v_mul_f32 v128, v128, v128\n\t"
  "v_mfma_f32_16x16x32_bf16 v[28:31], a[20:23], v[84:87], v[28:31]\n\t"
  "ds_read_b64_tr_b16 v[80:81], v109 offset:12288\n\t"
  "v_mul_f32 v129, v129, v129\n\t"
  "v_mfma_f32_16x16x32_bf16 v[32:35], a[12:15], v[88:91], v[32:35]\n\t"
  "ds_read_b64_tr_b16 v[82:83], v109 offset:14336\n\t"
  "s_mul_i32 s52, %[w], 20480\n\t"
  "v_mfma_f32_16x16x32_bf16 v[36:39], a[16:19], v[88:91], v[36:39]\n\t"
  "v_max_f32 v44, 0, v44\n\t"
  "v_max_f32 v45, 0, v45\n\t"
  "v_mfma_f32_16x16x32_bf16 v[40:43], a[20:23], v[88:91], v[40:43]\n\t"
  "s_waitcnt lgkmcnt(4)\n\t"
  "v_mul_f32 v46, v46, v128\n\t"
  "v_mfma_f32_16x16x32_bf16 v[20:23], a[24:27], v[68:71], v[20:23]\n\t"
  "ds_read_b64_tr_b16 v[84:85], v108 offset:16384\n\t"
  "s_add_u32 m0, s52, 0\n\t"
  "v_mfma_f32_16x16x32_bf16 v[24:27], a[28:31], v[68:71], v[24:27]\n\t"
  "ds_read_b64_tr_b16 v[86:87], v108 offset:18432\n\t"
  "v_mul_f32 v47, v47, v128\n\t"
  "v_mfma_f32_16x16x32_bf16 v[28:31], a[32:35], v[68:71], v[28:31]\n\t"
  "ds_read_b64_tr_b16 v[88:89], v109 offset:16384\n\t"
  "v_mul_f32 v44, v44, v46\n\t"
  "v_mfma_f32_16x16x32_bf16 v[32:35], a[24:27], v[72:75], v[32:35]\n\t"
  "ds_read_b64_tr_b16 v[90:91], v109 offset:18432\n\t"
  "v_mul_f32 v45, v45, v47\n\t"
  "v_mfma_f32_16x16x32_bf16 v[36:39], a[28:31], v[72:75], v[36:39]\n\t"
  "s_add_u32 s46, %[sW2], 26624\n\t"
  "v_cvt_pk_bf16_f32 v116, v44, v45\n\t"
  "v_mfma_f32_16x16x32_bf16 v[40:43], a[32:35], v[72:75], v[40:43]\n\t"
  "s_waitcnt lgkmcnt(4)\n\t"
  "v_cvt_pk_bf16_f32 v112, v44, v45\n\t"
  "v_mfma_f32_16x16x32_bf16 v[20:23], a[36:39], v[76:79], v[20:23]\n\t"
  "ds_read_b64_tr_b16 v[68:69], v108 offset:20480\n\t"
  "v_max_f32 v48, 0, v48\n\t"
  "v_mfma_f32_16x16x32_bf16 v[24:27], a[40:43], v[76:79], v[24:27]\n\t"
  "ds_read_b64_tr_b16 v[70:71], v108 offset:22528\n\t"
  "buffer_load_dwordx4 v1, %[srd], s46 offen offset:0 sc1 lds\n\t"
  "v_mfma_f32_16x16x32_bf16 v[28:31], a[44:47], v[76:79], v[28:31]\n\t"
  "ds_read_b64_tr_b16 v[72:73], v109 offset:20480\n\t"
  "v_max_f32 v49, 0, v49\n\t"
  "v_mfma_f32_16x16x32_bf16 v[32:35], a[36:39], v[80:83], v[32:35]\n\t"
  "ds_read_b64_tr_b16 v[74:75], v109 offset:22528\n\t"
  "v_mul_f32 v50, v50, v128\n\t"
  "v_mfma_f32_16x16x32_bf16 v[36:39], a[40:43], v[80:83], v[36:39]\n\t"
  "buffer_load_dwordx4 v1, %[srd], s46 offen offset:1024 sc1 lds\n\t"
  "v_mul_f32 v51, v51, v128\n\t"
  "v_mfma_f32_16x16x32_bf16 v[40:43], a[44:47], v[80:83], v[40:43]\n\t"
  "s_waitcnt lgkmcnt(4)\n\t"
  "v_mul_f32 v48, v48, v50\n\t"
  "v_mfma_f32_16x16x32_bf16 v[20:23], a[48:51], v[84:87], v[20:23]\n\t"
  "ds_read_b64_tr_b16 v[76:77], v108 offset:24576\n\t"
  "v_mul_f32 v49, v49, v51\n\t"
  "v_mfma_f32_16x16x32_bf16 v[24:27], a[52:55], v[84:87], v[24:27]\n\t"
  "ds_read_b64_tr_b16 v[78:79], v108 offset:26624\n\t"
  "v_cvt_pk_bf16_f32 v117, v48, v49\n\t"
  "v_mfma_f32_16x16x32_bf16 v[28:31], a[56:59], v[84:87], v[28:31]\n\t"
  "ds_read_b64_tr_b16 v[80:81], v109 offset:24576\n\t"
  "buffer_load_dwordx4 v1, %[srd], s46 offen offset:2048 sc1 lds\n\t"
  "v_mfma_f32_16x16x32_bf16 v[32:35], a[48:51], v[88:91], v[32:35]\n\t"
  "ds_read_b64_tr_b16 v[82:83], v109 offset:26624\n\t"
  "v_cvt_pk_bf16_f32 v113, v48, v49\n\t"
  "v_mfma_f32_16x16x32_bf16 v[36:39], a[52:55], v[88:91], v[36:39]\n\t"
  "v_max_f32 v52, 0, v52\n\t"
  "v_max_f32 v53, 0, v53\n\t"
  "v_mfma_f32_16x16x32_bf16 v[40:43], a[56:59], v[88:91], v[40:43]\n\t"
  "s_waitcnt lgkmcnt(4)\n\t"
  "buffer_load_dwordx4 v1, %[srd], s46 offen offset:3072 sc1 lds\n\t"
  "v_mfma_f32_16x16x32_bf16 v[20:23], a[60:63], v[68:71], v[20:23]\n\t"
  "ds_read_b64_tr_b16 v[84:85], v108 offset:28672\n\t"
  "v_mul_f32 v54, v54, v128\n\t"
  "v_mfma_f32_16x16x32_bf16 v[24:27], a[64:67], v[68:71], v[24:27]\n\t"
  "ds_read_b64_tr_b16 v[86:87], v108 offset:30720\n\t"
  "v_mul_f32 v55, v55, v128\n\t"
  "v_mfma_f32_16x16x32_bf16 v[28:31], a[68:71], v[68:71], v[28:31]\n\t"
  "ds_read_b64_tr_b16 v[88:89], v109 offset:28672\n\t"
  "v_mul_f32 v52, v52, v54\n\t"
  "v_mfma_f32_16x16x32_bf16 v[32:35], a[60:63], v[72:75], v[32:35]\n\t"
  "ds_read_b64_tr_b16 v[90:91], v109 offset:30720\n\t"
  "s_add_u32 m0, s52, 4096\n\t"
  "v_mfma_f32_16x16x32_bf16 v[36:39], a[64:67], v[72:75], v[36:39]\n\t"
  "v_permlane16_swap_b32 v116, v117\n\t"
  "v_mul_f32 v53, v53, v55\n\t"
  "v_mfma_f32_16x16x32_bf16 v[40:43], a[68:71], v[72:75], v[40:43]\n\t"
  "s_waitcnt lgkmcnt(4)\n\t"
  "v_cvt_pk_bf16_f32 v118, v52, v53\n\t"
  "v_mfma_f32_16x16x32_bf16 v[20:23], a[72:75], v[76:79], v[20:23]\n\t"
  "ds_read_b64_tr_b16 v[68:69], v108 offset:32768\n\t"
  "s_add_u32 s46, %[sW2], 30720\n\t"
  "v_mfma_f32_16x16x32_bf16 v[24:27], a[76:79], v[76:79], v[24:27]\n\t"
  "ds_read_b64_tr_b16 v[70:71], v108 offset:34816\n\t"
  "v_cvt_pk_bf16_f32 v114, v52, v53\n\t"
  "v_mfma_f32_16x16x32_bf16 v[28:31], a[80:83], v[76:79], v[28:31]\n\t"
  "ds_read_b64_tr_b16 v[72:73], v109 offset:32768\n\t"
  "s_mov_b32 s55, 98304\n\t"
  "v_mfma_f32_16x16x32_bf16 v[32:35], a[72:75], v[80:83], v[32:35]\n\t"
  "ds_read_b64_tr_b16 v[74:75], v109 offset:34816\n\t"
  "buffer_load_dwordx4 v1, %[srd], s46 offen offset:0 sc1 lds\n\t"
  "v_mfma_f32_16x16x32_bf16 v[36:39], a[76:79], v[80:83], v[36:39]\n\t"
  "v_permlane16_swap_b32 v118, v115\n\t"
  "v_mfma_f32_16x16x32_bf16 v[40:43], a[80:83], v[80:83], v[40:43]\n\t"
  "s_waitcnt lgkmcnt(4)\n\t"
  "v_permlane32_swap_b32 v117, v115\n\t"
  "v_mfma_f32_16x16x32_bf16 v[20:23], a[84:87], v[84:87], v[20:23]\n\t"
  "ds_read_b64_tr_b16 v[76:77], v108 offset:36864\n\t"
  "buffer_store_dwordx4 v[112:115], v8, %[srd], s55 offen sc1\n\t"
  "v_mfma_f32_16x16x32_bf16 v[24:27], a[88:91], v[84:87], v[24:27]\n\t"
  "ds_read_b64_tr_b16 v[78:79], v108 offset:38912\n\t"
  "buffer_load_dwordx4 v1, %[srd], s46 offen offset:1024 sc1 lds\n\t"
  "v_mfma_f32_16x16x32_bf16 v[28:31], a[92:95], v[84:87], v[28:31]\n\t"
  "ds_read_b64_tr_b16 v[80:81], v109 offset:36864\n\t"
  "v_max_f32 v56, 0, v56\n\t"
  "v_mfma_f32_16x16x32_bf16 v[32:35], a[84:87], v[88:91], v[32:35]\n\t"
  "ds_read_b64_tr_b16 v[82:83], v109 offset:38912\n\t"
  "v_max_f32 v57, 0, v57\n\t"
  "v_mfma_f32_16x16x32_bf16 v[36:39], a[88:91], v[88:91], v[36:39]\n\t"
  "buffer_load_dwordx4 v1, %[srd], s46 offen offset:2048 sc1 lds\n\t"
  "v_mul_f32 v58, v58, v129\n\t"
  "v_mfma_f32_16x16x32_bf16 v[40:43], a[92:95], v[88:91], v[40:43]\n\t"
  "s_waitcnt lgkmcnt(4)\n\t"
  "v_mul_f32 v59, v59, v129\n\t"
  "v_mfma_f32_16x16x32_bf16 v[20:23], a[96:99], v[68:71], v[20:23]\n\t"
  "ds_read_b64_tr_b16 v[84:85], v110 offset:0\n\t"
  "v_mul_f32 v56, v56, v58\n\t"
  "v_mfma_f32_16x16x32_bf16 v[24:27], a[100:103], v[68:71], v[24:27]\n\t"
  "ds_read_b64_tr_b16 v[86:87], v110 offset:2048\n\t"
  "buffer_load_dwordx4 v1, %[srd], s46 offen offset:3072 sc1 lds\n\t"
  "v_mfma_f32_16x16x32_bf16 v[28:31], a[104:107], v[68:71], v[28:31]\n\t"
  "ds_read_b64_tr_b16 v[88:89], v111 offset:0\n\t"
  "v_mul_f32 v57, v57, v59\n\t"
  "v_mfma_f32_16x16x32_bf16 v[32:35], a[96:99], v[72:75], v[32:35]\n\t"
  "ds_read_b64_tr_b16 v[90:91], v111 offset:2048\n\t"
  "v_cvt_pk_bf16_f32 v124, v56, v57\n\t"
  "v_mfma_f32_16x16x32_bf16 v[36:39], a[100:103], v[72:75], v[36:39]\n\t"
  "buffer_load_dwordx2 v[100:101], v136, %[srd], s54 offen offset:0 sc1\n\t"
  "buffer_load_dwordx2 v[102:103], v136, %[srd], s54 offen offset:1024 sc1\n\t"
  "v_mfma_f32_16x16x32_bf16 v[40:43], a[104:107], v[72:75], v[40:43]\n\t"
  "s_waitcnt lgkmcnt(4)\n\t"
  "buffer_load_dwordx2 v[104:105], v136, %[srd], s54 offen offset:2048 sc1\n\t"
  "v_mfma_f32_16x16x32_bf16 v[20:23], a[108:111], v[76:79], v[20:23]\n\t"
  "ds_read_b64_tr_b16 v[68:69], v110 offset:4096\n\t"
  "s_movk_i32 s56, 0\n\t"
  "v_mfma_f32_16x16x32_bf16 v[24:27], a[112:115], v[76:79], v[24:27]\n\t"
  "ds_read_b64_tr_b16 v[70:71], v110 offset:6144\n\t"
  "s_waitcnt vmcnt(12)\n\t"
  "v_mfma_f32_16x16x32_bf16 v[28:31], a[116:119], v[76:79], v[28:31]\n\t"
  "ds_read_b64_tr_b16 v[72:73], v111 offset:4096\n\t"
  "buffer_load_dwordx2 v[106:107], v136, %[srd], s54 offen offset:3072 sc1\n\t"
  "v_mfma_f32_16x16x32_bf16 v[32:35], a[108:111], v[80:83], v[32:35]\n\t"
  "ds_read_b64_tr_b16 v[74:75], v111 offset:6144\n\t"
  "buffer_store_dwordx4 v[132:135], v9, %[srd], s56 offen sc1\n\t"
  "v_mfma_f32_16x16x32_bf16 v[36:39], a[112:115], v[80:83], v[36:39]\n\t"
  "s_add_u32 m0, s52, 8192\n\t"
  "s_add_u32 s46, %[sW2], 34816\n\t"
  "v_mfma_f32_16x16x32_bf16 v[40:43], a[116:119], v[80:83], v[40:43]\n\t"
  "s_waitcnt lgkmcnt(4)\n\t"
  "v_cvt_pk_bf16_f32 v120, v56, v57\n\t"
  "v_mfma_f32_16x16x32_bf16 v[20:23], a[120:123], v[84:87], v[20:23]\n\t"
  "ds_read_b64_tr_b16 v[76:77], v110 offset:8192\n\t"
  "v_xor_b32 v108, 64, v5\n\t"
  "v_mfma_f32_16x16x32_bf16 v[24:27], a[124:127], v[84:87], v[24:27]\n\t"
  "ds_read_b64_tr_b16 v[78:79], v110 offset:10240\n\t"
  "v_add_u32 v108, 81920, v108\n\t"
  "v_mfma_f32_16x16x32_bf16 v[28:31], a[128:131], v[84:87], v[28:31]\n\t"
  "ds_read_b64_tr_b16 v[80:81], v111 offset:8192\n\t"
  "v_xor_b32 v109, 96, v5\n\t"
  "v_mfma_f32_16x16x32_bf16 v[32:35], a[120:123], v[88:91], v[32:35]\n\t"
  "ds_read_b64_tr_b16 v[82:83], v111 offset:10240\n\t"
  "v_add_u32 v109, 81920, v109\n\t"
  "v_mfma_f32_16x16x32_bf16 v[36:39], a[124:127], v[88:91], v[36:39]\n\t"
  "buffer_load_dwordx4 v1, %[srd], s46 offen offset:0 sc1 lds\n\t"
  "buffer_load_dwordx4 v1, %[srd], s46 offen offset:1024 sc1 lds\n\t"
  "v_mfma_f32_16x16x32_bf16 v[40:43], a[128:131], v[88:91], v[40:43]\n\t"
  "s_waitcnt lgkmcnt(4)\n\t"
  "v_max_f32 v60, 0, v60\n\t"
  "v_mfma_f32_16x16x32_bf16 v[20:23], a[132:135], v[68:71], v[20:23]\n\t"
  "ds_read_b64_tr_b16 v[84:85], v110 offset:12288\n\t"
  "buffer_load_dwordx4 v1, %[srd], s46 offen offset:2048 sc1 lds\n\t"
  "v_mfma_f32_16x16x32_bf16 v[24:27], a[136:139], v[68:71], v[24:27]\n\t"
  "ds_read_b64_tr_b16 v[86:87], v110 offset:14336\n\t"
  "v_max_f32 v61, 0, v61\n\t"
  "v_mfma_f32_16x16x32_bf16 v[28:31], a[140:143], v[68:71], v[28:31]\n\t"
  "ds_read_b64_tr_b16 v[88:89], v111 offset:12288\n\t"
  "v_mul_f32 v62, v62, v129\n\t"
  "v_mfma_f32_16x16x32_bf16 v[32:35], a[132:135], v[72:75], v[32:35]\n\t"
  "ds_read_b64_tr_b16 v[90:91], v111 offset:14336\n\t"
  "v_mul_f32 v63, v63, v129\n\t"
  "v_mfma_f32_16x16x32_bf16 v[36:39], a[136:139], v[72:75], v[36:39]\n\t"
  "buffer_load_dwordx4 v1, %[srd], s46 offen offset:3072 sc1 lds\n\t"
  "v_mul_f32 v60, v60, v62\n\t"
  "v_mfma_f32_16x16x32_bf16 v[40:43], a[140:143], v[72:75], v[40:43]\n\t"
  "s_waitcnt lgkmcnt(4)\n\t"
  "v_mul_f32 v61, v61, v63\n\t"
  "v_mfma_f32_16x16x32_bf16 v[20:23], a[144:147], v[76:79], v[20:23]\n\t"
  "ds_read_b64_tr_b16 v[68:69], v110 offset:16384\n\t"
  "v_cvt_pk_bf16_f32 v125, v60, v61\n\t"
  "v_mfma_f32_16x16x32_bf16 v[24:27], a[148:151], v[76:79], v[24:27]\n\t"
  "ds_read_b64_tr_b16 v[70:71], v110 offset:18432\n\t"
  "s_add_u32 m0, s52, 12288\n\t"
  "v_mfma_f32_16x16x32_bf16 v[28:31], a[152:155], v[76:79], v[28:31]\n\t"
  "ds_read_b64_tr_b16 v[72:73], v111 offset:16384\n\t"
  "v_cvt_pk_bf16_f32 v121, v60, v61\n\t"
  "v_mfma_f32_16x16x32_bf16 v[32:35], a[144:147], v[80:83], v[32:35]\n\t"
  "ds_read_b64_tr_b16 v[74:75], v111 offset:18432\n\t"
  "v_max_f32 v64, 0, v64\n\t"
  "v_mfma_f32_16x16x32_bf16 v[36:39], a[148:151], v[80:83], v[36:39]\n\t"
  "s_add_u32 s46, %[sW2], 38912\n\t"
  "v_max_f32 v65, 0, v65\n\t"
  "v_mfma_f32_16x16x32_bf16 v[40:43], a[152:155], v[80:83], v[40:43]\n\t"
  "s_waitcnt lgkmcnt(4)\n\t"
  "v_mul_f32 v66, v66, v129\n\t"
  "v_mfma_f32_16x16x32_bf16 v[20:23], a[156:159], v[84:87], v[20:23]\n\t"
  "ds_read_b64_tr_b16 v[76:77], v110 offset:20480\n\t"
  "v_mul_f32 v67, v67, v129\n\t"
  "v_mfma_f32_16x16x32_bf16 v[24:27], a[160:163], v[84:87], v[24:27]\n\t"
  "ds_read_b64_tr_b16 v[78:79], v110 offset:22528\n\t"
  "v_mul_f32 v64, v64, v66\n\t"
  "v_mfma_f32_16x16x32_bf16 v[28:31], a[164:167], v[84:87], v[28:31]\n\t"
  "ds_read_b64_tr_b16 v[80:81], v111 offset:20480\n\t"
  "buffer_load_dwordx4 v1, %[srd], s46 offen offset:0 sc1 lds\n\t"
  "v_mfma_f32_16x16x32_bf16 v[32:35], a[156:159], v[88:91], v[32:35]\n\t"
  "ds_read_b64_tr_b16 v[82:83], v111 offset:22528\n\t"
  "v_permlane16_swap_b32 v124, v125\n\t"
  "v_mfma_f32_16x16x32_bf16 v[36:39], a[160:163], v[88:91], v[36:39]\n\t"
  "v_mul_f32 v65, v65, v67\n\t"
  "v_cvt_pk_bf16_f32 v126, v64, v65\n\t"
  "v_mfma_f32_16x16x32_bf16 v[40:43], a[164:167], v[88:91], v[40:43]\n\t"
  "s_waitcnt lgkmcnt(4)\n\t"
  "buffer_load_dwordx4 v1, %[srd], s46 offen offset:1024 sc1 lds\n\t"
  "v_mfma_f32_16x16x32_bf16 v[20:23], a[168:171], v[68:71], v[20:23]\n\t"
  "ds_read_b64_tr_b16 v[84:85], v110 offset:24576\n\t"
  "v_cvt_pk_bf16_f32 v122, v64, v65\n\t"
  "v_mfma_f32_16x16x32_bf16 v[24:27], a[172:175], v[68:71], v[24:27]\n\t"
  "ds_read_b64_tr_b16 v[86:87], v110 offset:26624\n\t"
  "s_mov_b32 s55, 147456\n\t"
  "v_mfma_f32_16x16x32_bf16 v[28:31], a[176:179], v[68:71], v[28:31]\n\t"
  "ds_read_b64_tr_b16 v[88:89], v111 offset:24576\n\t"
  "v_permlane16_swap_b32 v126, v123\n\t"
  "v_mfma_f32_16x16x32_bf16 v[32:35], a[168:171], v[72:75], v[32:35]\n\t"
  "ds_read_b64_tr_b16 v[90:91], v111 offset:26624\n\t"
  "buffer_load_dwordx4 v1, %[srd], s46 offen offset:2048 sc1 lds\n\t"
  "v_mfma_f32_16x16x32_bf16 v[36:39], a[172:175], v[72:75], v[36:39]\n\t"
  "v_permlane32_swap_b32 v125, v123\n\t"
  "buffer_store_dwordx4 v[120:123], v8, %[srd], s55 offen sc1\n\t"
  "v_mfma_f32_16x16x32_bf16 v[40:43], a[176:179], v[72:75], v[40:43]\n\t"
  "s_waitcnt lgkmcnt(4)\n\t"
  "v_mfma_f32_16x16x32_bf16 v[20:23], a[180:183], v[76:79], v[20:23]\n\t"
  "ds_read_b64_tr_b16 v[68:69], v110 offset:28672\n\t"
  "buffer_load_dwordx4 v1, %[srd], s46 offen offset:3072 sc1 lds\n\t"
  "v_mfma_f32_16x16x32_bf16 v[24:27], a[184:187], v[76:79], v[24:27]\n\t"
  "ds_read_b64_tr_b16 v[70:71], v110 offset:30720\n\t"
  "v_mfma_f32_16x16x32_bf16 v[28:31], a[188:191], v[76:79], v[28:31]\n\t"
  "ds_read_b64_tr_b16 v[72:73], v111 offset:28672\n\t"
  "v_mfma_f32_16x16x32_bf16 v[32:35], a[180:183], v[80:83], v[32:35]\n\t"
  "ds_read_b64_tr_b16 v[74:75], v111 offset:30720\n\t"
  "s_add_u32 m0, s52, 16384\n\t"
  "v_mfma_f32_16x16x32_bf16 v[36:39], a[184:187], v[80:83], v[36:39]\n\t"
  "v_mfma_f32_16x16x32_bf16 v[40:43], a[188:191], v[80:83], v[40:43]\n\t"
  "s_waitcnt lgkmcnt(4)\n\t"
  "v_mfma_f32_16x16x32_bf16 v[20:23], a[192:195], v[84:87], v[20:23]\n\t"
  "ds_read_b64_tr_b16 v[76:77], v110 offset:32768\n\t"
  "v_mfma_f32_16x16x32_bf16 v[24:27], a[196:199], v[84:87], v[24:27]\n\t"
  "ds_read_b64_tr_b16 v[78:79], v110 offset:34816\n\t"
  "s_add_u32 s46, %[sW2], 43008\n\t"
  "v_mfma_f32_16x16x32_bf16 v[28:31], a[200:203], v[84:87], v[28:31]\n\t"
  "ds_read_b64_tr_b16 v[80:81], v111 offset:32768\n\t"
  "v_mfma_f32_16x16x32_bf16 v[32:35], a[192:195], v[88:91], v[32:35]\n\t"
  "ds_read_b64_tr_b16 v[82:83], v111 offset:34816\n\t"
  "v_mfma_f32_16x16x32_bf16 v[36:39], a[196:199], v[88:91], v[36:39]\n\t"
  "buffer_load_dwordx4 v1, %[srd], s46 offen offset:0 sc1 lds\n\t"
  "v_mfma_f32_16x16x32_bf16 v[40:43], a[200:203], v[88:91], v[40:43]\n\t"
  "s_waitcnt lgkmcnt(4)\n\t"
  "v_mfma_f32_16x16x32_bf16 v[20:23], a[204:207], v[68:71], v[20:23]\n\t"
  "ds_read_b64_tr_b16 v[84:85], v110 offset:36864\n\t"
  "v_mfma_f32_16x16x32_bf16 v[24:27], a[208:211], v[68:71], v[24:27]\n\t"
  "ds_read_b64_tr_b16 v[86:87], v110 offset:38912\n\t"
  "buffer_load_dwordx4 v1, %[srd], s46 offen offset:1024 sc1 lds\n\t"
  "v_mfma_f32_16x16x32_bf16 v[28:31], a[212:215], v[68:71], v[28:31]\n\t"
  "ds_read_b64_tr_b16 v[88:89], v111 offset:36864\n\t"
  "v_mfma_f32_16x16x32_bf16 v[32:35], a[204:207], v[72:75], v[32:35]\n\t"
  "ds_read_b64_tr_b16 v[90:91], v111 offset:38912\n\t"
  "v_mfma_f32_16x16x32_bf16 v[36:39], a[208:211], v[72:75], v[36:39]\n\t"
  "s_waitcnt vmcnt(14)\n\t"
  "v_min_u32 v131, v101, v103\n\t"
  "v_mfma_f32_16x16x32_bf16 v[40:43], a[212:215], v[72:75], v[40:43]\n\t"
  "s_waitcnt lgkmcnt(4)\n\t"
  "s_waitcnt vmcnt(12)\n\t"
  "v_min3_u32 v131, v131, v105, v107\n\t"
  "v_mfma_f32_16x16x32_bf16 v[20:23], a[216:219], v[76:79], v[20:23]\n\t"
  "ds_read_b64_tr_b16 v[68:69], v108 offset:0\n\t"
  "v_cmp_eq_u32 vcc, 0, v131\n\t"
  "v_mfma_f32_16x16x32_bf16 v[24:27], a[220:223], v[76:79], v[24:27]\n\t"
  "ds_read_b64_tr_b16 v[70:71], v108 offset:2048\n\t"
  "buffer_load_dwordx4 v1, %[srd], s46 offen offset:2048 sc1 lds\n\t"
  "v_mfma_f32_16x16x32_bf16 v[28:31], a[224:227], v[76:79], v[28:31]\n\t"
  "ds_read_b64_tr_b16 v[72:73], v109 offset:0\n\t"
  "s_cbranch_vccnz Lst1_slow%=\n\t"
  "Lst1_back%=:\n\t"
  "v_mfma_f32_16x16x32_bf16 v[32:35], a[216:219], v[80:83], v[32:35]\n\t"
  "ds_read_b64_tr_b16 v[74:75], v109 offset:2048\n\t"
  "buffer_load_dwordx4 v1, %[srd], s46 offen offset:3072 sc1 lds\n\t"
  "v_mfma_f32_16x16x32_bf16 v[36:39], a[220:223], v[80:83], v[36:39]\n\t"
  "v_mfma_f32_16x16x32_bf16 v[40:43], a[224:227], v[80:83], v[40:43]\n\t"
  "s_waitcnt lgkmcnt(4)\n\t"
  "v_mfma_f32_16x16x32_bf16 v[20:23], a[228:231], v[84:87], v[20:23]\n\t"
  "ds_read_b64_tr_b16 v[76:77], v108 offset:4096\n\t"
  "v_mfma_f32_16x16x32_bf16 v[24:27], a[232:235], v[84:87], v[24:27]\n\t"
  "ds_read_b64_tr_b16 v[78:79], v108 offset:6144\n\t"
  "v_mfma_f32_16x16x32_bf16 v[28:31], a[236:239], v[84:87], v[28:31]\n\t"
  "ds_read_b64_tr_b16 v[80:81], v109 offset:4096\n\t"
  "v_mfma_f32_16x16x32_bf16 v[32:35], a[228:231], v[88:91], v[32:35]\n\t"
  "ds_read_b64_tr_b16 v[82:83], v109 offset:6144\n\t"
  "v_mfma_f32_16x16x32_bf16 v[36:39], a[232:235], v[88:91], v[36:39]\n\t"
  "v_mfma_f32_16x16x32_bf16 v[40:43], a[236:239], v[88:91], v[40:43]\n\t"
  "v_mfma_f32_16x16x16_bf16 v[20:23], a[240:241], v[100:101], v[20:23]\n\t"
  "v_mfma_f32_16x16x16_bf16 v[24:27], a[242:243], v[100:101], v[24:27]\n\t"
  "v_mfma_f32_16x16x16_bf16 v[28:31], a[244:245], v[100:101], v[28:31]\n\t"
  "v_mfma_f32_16x16x16_bf16 v[32:35], a[240:241], v[102:103], v[32:35]\n\t"
  "v_mfma_f32_16x16x16_bf16 v[36:39], a[242:243], v[102:103], v[36:39]\n\t"
  "v_mfma_f32_16x16x16_bf16 v[40:43], a[244:245], v[102:103], v[40:43]\n\t"
  "s_waitcnt lgkmcnt(4)\n\t"
  "v_mfma_f32_16x16x32_bf16 v[44:47], a[0:3], v[68:71], 0\n\t"
  "ds_read_b64_tr_b16 v[84:85], v108 offset:8192\n\t"
  "v_xor_b32 v110, 64, v5\n\t"
  "v_mfma_f32_16x16x32_bf16 v[48:51], a[4:7], v[68:71], 0\n\t"
  "ds_read_b64_tr_b16 v[86:87], v108 offset:10240\n\t"
  "v_add_u32 v110, 122880, v110\n\t"
  "v_mfma_f32_16x16x32_bf16 v[52:55], a[8:11], v[68:71], 0\n\t"
  "ds_read_b64_tr_b16 v[88:89], v109 offset:8192\n\t"
  "v_xor_b32 v111, 96, v5\n\t"
  "v_mfma_f32_16x16x32_bf16 v[56:59], a[0:3], v[72:75], 0\n\t"
  "ds_read_b64_tr_b16 v[90:91], v109 offset:10240\n\t"
  "v_add_u32 v111, 122880, v111\n\t"
  "v_mfma_f32_16x16x32_bf16 v[60:63], a[4:7], v[72:75], 0\n\t"
  "v_and_b32 v128, 0xffff0000, v101\n\t"
  "v_and_b32 v129, 0xffff0000, v103\n\t"
  "v_mfma_f32_16x16x32_bf16 v[64:67], a[8:11], v[72:75], 0\n\t"
  "s_waitcnt lgkmcnt(4)\n\t"
  "v_rcp_f32 v128, v128\n\t"
  "v_mfma_f32_16x16x32_bf16 v[44:47], a[12:15], v[76:79], v[44:47]\n\t"
  "ds_read_b64_tr_b16 v[68:69], v108 offset:12288\n\t"
  "s_add_u32 s45, %[sW2], 47104\n\t"
  "v_mfma_f32_16x16x32_bf16 v[48:51], a[16:19], v[76:79], v[48:51]\n\t"
  "ds_read_b64_tr_b16 v[70:71], v108 offset:14336\n\t"
  "v_rcp_f32 v129, v129\n\t"
  "v_mfma_f32_16x16x32_bf16 v[52:55], a[20:23], v[76:79], v[52:55]\n\t"
  "ds_read_b64_tr_b16 v[72:73], v109 offset:12288\n\t"
  "v_mul_f32 v128, v128, v128\n\t"
  "v_mfma_f32_16x16x32_bf16 v[56:59], a[12:15], v[80:83], v[56:59]\n\t"
  "ds_read_b64_tr_b16 v[74:75], v109 offset:14336\n\t"
  "v_mul_f32 v129, v129, v129\n\t"
  "v_mfma_f32_16x16x32_bf16 v[60:63], a[16:19], v[80:83], v[60:63]\n\t"
  "buffer_load_dwordx4 a[0:3], v1, %[srd], s45 offen offset:0 sc1\n\t"
  "v_max_f32 v20, 0, v20\n\t"
  "v_mfma_f32_16x16x32_bf16 v[64:67], a[20:23], v[80:83], v[64:67]\n\t"
  "s_waitcnt lgkmcnt(4)\n\t"
  "v_max_f32 v21, 0, v21\n\t"
  "v_mfma_f32_16x16x32_bf16 v[44:47], a[24:27], v[84:87], v[44:47]\n\t"
  "ds_read_b64_tr_b16 v[76:77], v108 offset:16384\n\t"
  "v_mul_f32 v22, v22, v128\n\t"
  "v_mfma_f32_16x16x32_bf16 v[48:51], a[28:31], v[84:87], v[48:51]\n\t"
  "ds_read_b64_tr_b16 v[78:79], v108 offset:18432\n\t"
  "v_mul_f32 v23, v23, v128\n\t"
  "v_mfma_f32_16x16x32_bf16 v[52:55], a[32:35], v[84:87], v[52:55]\n\t"
  "ds_read_b64_tr_b16 v[80:81], v109 offset:16384\n\t"
  "buffer_load_dwordx4 a[4:7], v1, %[srd], s45 offen offset:1024 sc1\n\t"
  "v_mfma_f32_16x16x32_bf16 v[56:59], a[24:27], v[88:91], v[56:59]\n\t"
  "ds_read_b64_tr_b16 v[82:83], v109 offset:18432\n\t"
  "v_mul_f32 v20, v20, v22\n\t"
  "v_mfma_f32_16x16x32_bf16 v[60:63], a[28:31], v[88:91], v[60:63]\n\t"
  "v_mul_f32 v21, v21, v23\n\t"
  "v_cvt_pk_bf16_f32 v116, v20, v21\n\t"
  "v_mfma_f32_16x16x32_bf16 v[64:67], a[32:35], v[88:91], v[64:67]\n\t"
  "s_waitcnt lgkmcnt(4)\n\t"
  "v_cvt_pk_bf16_f32 v112, v20, v21\n\t"
  "v_mfma_f32_16x16x32_bf16 v[44:47], a[36:39], v[68:71], v[44:47]\n\t"
  "ds_read_b64_tr_b16 v[84:85], v108 offset:20480\n\t"
  "s_movk_i32 s56, 8192\n\t"
  "v_mfma_f32_16x16x32_bf16 v[48:51], a[40:43], v[68:71], v[48:51]\n\t"
  "ds_read_b64_tr_b16 v[86:87], v108 offset:22528\n\t"
  "s_waitcnt vmcnt(7)\n\t"
  "v_mfma_f32_16x16x32_bf16 v[52:55], a[44:47], v[68:71], v[52:55]\n\t"
  "ds_read_b64_tr_b16 v[88:89], v109 offset:20480\n\t"
  "buffer_store_dwordx4 v[132:135], v9, %[srd], s56 offen sc1\n\t"
  "v_mfma_f32_16x16x32_bf16 v[56:59], a[36:39], v[72:75], v[56:59]\n\t"
  "ds_read_b64_tr_b16 v[90:91], v109 offset:22528\n\t"
  "buffer_load_dwordx4 a[8:11], v1, %[srd], s45 offen offset:2048 sc1\n\t"
  "v_mfma_f32_16x16x32_bf16 v[60:63], a[40:43], v[72:75], v[60:63]\n\t"
  "buffer_load_dwordx4 a[12:15], v1, %[srd], s45 offen offset:3072 sc1\n\t"
  "v_max_f32 v24, 0, v24\n\t"
  "v_mfma_f32_16x16x32_bf16 v[64:67], a[44:47], v[72:75], v[64:67]\n\t"
  "s_waitcnt lgkmcnt(4)\n\t"
  "v_max_f32 v25, 0, v25\n\t"
  "v_mfma_f32_16x16x32_bf16 v[44:47], a[48:51], v[76:79], v[44:47]\n\t"
  "ds_read_b64_tr_b16 v[68:69], v108 offset:24576\n\t"
  "v_mul_f32 v26, v26, v128\n\t"
  "v_mfma_f32_16x16x32_bf16 v[48:51], a[52:55], v[76:79], v[48:51]\n\t"
  "ds_read_b64_tr_b16 v[70:71], v108 offset:26624\n\t"
  "v_mul_f32 v27, v27, v128\n\t"
  "v_mfma_f32_16x16x32_bf16 v[52:55], a[56:59], v[76:79], v[52:55]\n\t"
  "ds_read_b64_tr_b16 v[72:73], v109 offset:24576\n\t"
  "s_add_u32 s45, %[sW2], 51200\n\t"
  "v_mfma_f32_16x16x32_bf16 v[56:59], a[48:51], v[80:83], v[56:59]\n\t"
  "ds_read_b64_tr_b16 v[74:75], v109 offset:26624\n\t"
  "v_mul_f32 v24, v24, v26\n\t"
  "v_mfma_f32_16x16x32_bf16 v[60:63], a[52:55], v[80:83], v[60:63]\n\t"
  "v_mul_f32 v25, v25, v27\n\t"
  "v_cvt_pk_bf16_f32 v117, v24, v25\n\t"
  "v_mfma_f32_16x16x32_bf16 v[64:67], a[56:59], v[80:83], v[64:67]\n\t"
  "s_waitcnt lgkmcnt(4)\n\t"
  "v_cvt_pk_bf16_f32 v113, v24, v25\n\t"
  "v_mfma_f32_16x16x32_bf16 v[44:47], a[60:63], v[84:87], v[44:47]\n\t"
  "ds_read_b64_tr_b16 v[76:77], v108 offset:28672\n\t"
  "s_add_u32 s57, %[sFL], 0\n\t"
  "v_mfma_f32_16x16x32_bf16 v[48:51], a[64:67], v[84:87], v[48:51]\n\t"
  "ds_read_b64_tr_b16 v[78:79], v108 offset:30720\n\t"
  "buffer_load_dwordx2 v[138:139], v137, %[srd], s57 offen sc1\n\t"
  "v_mfma_f32_16x16x32_bf16 v[52:55], a[68:71], v[84:87], v[52:55]\n\t"
  "ds_read_b64_tr_b16 v[80:81], v109 offset:28672\n\t"
  "buffer_load_dwordx4 a[16:19], v1, %[srd], s45 offen offset:0 sc1\n\t"
  "v_mfma_f32_16x16x32_bf16 v[56:59], a[60:63], v[88:91], v[56:59]\n\t"
  "ds_read_b64_tr_b16 v[82:83], v109 offset:30720\n\t"
  "v_max_f32 v28, 0, v28\n\t"
  "v_mfma_f32_16x16x32_bf16 v[60:63], a[64:67], v[88:91], v[60:63]\n\t"
  "buffer_load_dwordx4 a[20:23], v1, %[srd], s45 offen offset:1024 sc1\n\t"
  "v_max_f32 v29, 0, v29\n\t"
  "v_mfma_f32_16x16x32_bf16 v[64:67], a[68:71], v[88:91], v[64:67]\n\t"
  "s_waitcnt lgkmcnt(4)\n\t"
  "v_mul_f32 v30, v30, v128\n\t"
  "v_mfma_f32_16x16x32_bf16 v[44:47], a[72:75], v[68:71], v[44:47]\n\t"
  "ds_read_b64_tr_b16 v[84:85], v108 offset:32768\n\t"
  "v_mul_f32 v31, v31, v128\n\t"
  "v_mfma_f32_16x16x32_bf16 v[48:51], a[76:79], v[68:71], v[48:51]\n\t"
  "ds_read_b64_tr_b16 v[86:87], v108 offset:34816\n\t"
  "v_mul_f32 v28, v28, v30\n\t"
  "v_mfma_f32_16x16x32_bf16 v[52:55], a[80:83], v[68:71], v[52:55]\n\t"
  "ds_read_b64_tr_b16 v[88:89], v109 offset:32768\n\t"
  "buffer_load_dwordx4 a[24:27], v1, %[srd], s45 offen offset:2048 sc1\n\t"
  "v_mfma_f32_16x16x32_bf16 v[56:59], a[72:75], v[72:75], v[56:59]\n\t"
  "ds_read_b64_tr_b16 v[90:91], v109 offset:34816\n\t"
  "v_permlane16_swap_b32 v116, v117\n\t"
  "v_mfma_f32_16x16x32_bf16 v[60:63], a[76:79], v[72:75], v[60:63]\n\t"
  "v_mul_f32 v29, v29, v31\n\t"
  "v_cvt_pk_bf16_f32 v118, v28, v29\n\t"
  "v_mfma_f32_16x16x32_bf16 v[64:67], a[80:83], v[72:75], v[64:67]\n\t"
  "s_waitcnt lgkmcnt(4)\n\t"
  "v_cvt_pk_bf16_f32 v114, v28, v29\n\t"
  "v_mfma_f32_16x16x32_bf16 v[44:47], a[84:87], v[76:79], v[44:47]\n\t"
  "ds_read_b64_tr_b16 v[68:69], v108 offset:36864\n\t"
  "s_mov_b32 s55, 3145728\n\t"
  "v_mfma_f32_16x16x32_bf16 v[48:51], a[88:91], v[76:79], v[48:51]\n\t"
  "ds_read_b64_tr_b16 v[70:71], v108 offset:38912\n\t"
  "buffer_load_dwordx4 a[28:31], v1, %[srd], s45 offen offset:3072 sc1\n\t"
  "v_mfma_f32_16x16x32_bf16 v[52:55], a[92:95], v[76:79], v[52:55]\n\t"
  "ds_read_b64_tr_b16 v[72:73], v109 offset:36864\n\t"
  "v_permlane16_swap_b32 v118, v115\n\t"
  "v_mfma_f32_16x16x32_bf16 v[56:59], a[84:87], v[80:83], v[56:59]\n\t"
  "ds_read_b64_tr_b16 v[74:75], v109 offset:38912\n\t"
  "v_permlane32_swap_b32 v117, v115\n\t"
  "v_mfma_f32_16x16x32_bf16 v[60:63], a[88:91], v[80:83], v[60:63]\n\t"
  "buffer_store_dwordx4 v[112:115], v8, %[srd], s55 offen sc1\n\t"
  "v_max_f32 v32, 0, v32\n\t"
  "v_mfma_f32_16x16x32_bf16 v[64:67], a[92:95], v[80:83], v[64:67]\n\t"
  "s_waitcnt lgkmcnt(4)\n\t"
  "s_add_u32 s45, %[sW2], 55296\n\t"
  "v_mfma_f32_16x16x32_bf16 v[44:47], a[96:99], v[84:87], v[44:47]\n\t"
  "ds_read_b64_tr_b16 v[76:77], v110 offset:0\n\t"
  "v_max_f32 v33, 0, v33\n\t"
  "v_mfma_f32_16x16x32_bf16 v[48:51], a[100:103], v[84:87], v[48:51]\n\t"
  "ds_read_b64_tr_b16 v[78:79], v110 offset:2048\n\t"
  "v_mul_f32 v34, v34, v129\n\t"
  "v_mfma_f32_16x16x32_bf16 v[52:55], a[104:107], v[84:87], v[52:55]\n\t"
  "ds_read_b64_tr_b16 v[80:81], v111 offset:0\n\t"
  "v_mul_f32 v35, v35, v129\n\t"
  "v_mfma_f32_16x16x32_bf16 v[56:59], a[96:99], v[88:91], v[56:59]\n\t"
  "ds_read_b64_tr_b16 v[82:83], v111 offset:2048\n\t"
  "buffer_load_dwordx4 a[32:35], v1, %[srd], s45 offen offset:0 sc1\n\t"
  "v_mfma_f32_16x16x32_bf16 v[60:63], a[100:103], v[88:91], v[60:63]\n\t"
  "v_mul_f32 v32, v32, v34\n\t"
  "v_mul_f32 v33, v33, v35\n\t"
  "v_mfma_f32_16x16x32_bf16 v[64:67], a[104:107], v[88:91], v[64:67]\n\t"
  "s_waitcnt lgkmcnt(4)\n\t"
  "v_cvt_pk_bf16_f32 v124, v32, v33\n\t"
  "v_mfma_f32_16x16x32_bf16 v[44:47], a[108:111], v[68:71], v[44:47]\n\t"
  "ds_read_b64_tr_b16 v[84:85], v110 offset:4096\n\t"
  "v_cvt_pk_bf16_f32 v120, v32, v33\n\t"
  "v_mfma_f32_16x16x32_bf16 v[48:51], a[112:115], v[68:71], v[48:51]\n\t"
  "ds_read_b64_tr_b16 v[86:87], v110 offset:6144\n\t"
  "buffer_load_dwordx4 a[36:39], v1, %[srd], s45 offen offset:1024 sc1\n\t"
  "v_mfma_f32_16x16x32_bf16 v[52:55], a[116:119], v[68:71], v[52:55]\n\t"
  "ds_read_b64_tr_b16 v[88:89], v111 offset:4096\n\t"
  "v_max_f32 v36, 0, v36\n\t"
  "v_mfma_f32_16x16x32_bf16 v[56:59], a[108:111], v[72:75], v[56:59]\n\t"
  "ds_read_b64_tr_b16 v[90:91], v111 offset:6144\n\t"
  "v_max_f32 v37, 0, v37\n\t"
  "v_mfma_f32_16x16x32_bf16 v[60:63], a[112:115], v[72:75], v[60:63]\n\t"
  "v_mul_f32 v38, v38, v129\n\t"
  "v_mul_f32 v39, v39, v129\n\t"
  "v_mfma_f32_16x16x32_bf16 v[64:67], a[116:119], v[72:75], v[64:67]\n\t"
  "s_waitcnt lgkmcnt(4)\n\t"
  "buffer_load_dwordx4 a[40:43], v1, %[srd], s45 offen offset:2048 sc1\n\t"
  "v_mfma_f32_16x16x32_bf16 v[44:47], a[120:123], v[76:79], v[44:47]\n\t"
  "ds_read_b64_tr_b16 v[68:69], v110 offset:8192\n\t"
  "v_mul_f32 v36, v36, v38\n\t"
  "v_mfma_f32_16x16x32_bf16 v[48:51], a[124:127], v[76:79], v[48:51]\n\t"
  "ds_read_b64_tr_b16 v[70:71], v110 offset:10240\n\t"
  "v_mul_f32 v37, v37, v39\n\t"
  "v_mfma_f32_16x16x32_bf16 v[52:55], a[128:131], v[76:79], v[52:55]\n\t"
  "ds_read_b64_tr_b16 v[72:73], v111 offset:8192\n\t"
  "v_cvt_pk_bf16_f32 v125, v36, v37\n\t"
  "v_mfma_f32_16x16x32_bf16 v[56:59], a[120:123], v[80:83], v[56:59]\n\t"
  "ds_read_b64_tr_b16 v[74:75], v111 offset:10240\n\t"
  "buffer_load_dwordx4 a[44:47], v1, %[srd], s45 offen offset:3072 sc1\n\t"
  "v_mfma_f32_16x16x32_bf16 v[60:63], a[124:127], v[80:83], v[60:63]\n\t"
  "v_cvt_pk_bf16_f32 v121, v36, v37\n\t"
  "v_max_f32 v40, 0, v40\n\t"
  "v_mfma_f32_16x16x32_bf16 v[64:67], a[128:131], v[80:83], v[64:67]\n\t"
  "s_waitcnt lgkmcnt(4)\n\t"
  "v_max_f32 v41, 0, v41\n\t"
  "v_mfma_f32_16x16x32_bf16 v[44:47], a[132:135], v[84:87], v[44:47]\n\t"
  "ds_read_b64_tr_b16 v[76:77], v110 offset:12288\n\t"
  "v_mul_f32 v42, v42, v129\n\t"
  "v_mfma_f32_16x16x32_bf16 v[48:51], a[136:139], v[84:87], v[48:51]\n\t"
  "ds_read_b64_tr_b16 v[78:79], v110 offset:14336\n\t"
  "s_add_u32 s45, %[sW2], 59392\n\t"
  "v_mfma_f32_16x16x32_bf16 v[52:55], a[140:143], v[84:87], v[52:55]\n\t"
  "ds_read_b64_tr_b16 v[80:81], v111 offset:12288\n\t"
  "v_mul_f32 v43, v43, v129\n\t"
  "v_mfma_f32_16x16x32_bf16 v[56:59], a[132:135], v[88:91], v[56:59]\n\t"
  "ds_read_b64_tr_b16 v[82:83], v111 offset:14336\n\t"
  "v_mul_f32 v40, v40, v42\n\t"
  "v_mfma_f32_16x16x32_bf16 v[60:63], a[136:139], v[88:91], v[60:63]\n\t"
  "v_permlane16_swap_b32 v124, v125\n\t"
  "v_mul_f32 v41, v41, v43\n\t"
  "v_mfma_f32_16x16x32_bf16 v[64:67], a[140:143], v[88:91], v[64:67]\n\t"
  "s_waitcnt lgkmcnt(4)\n\t"
  "buffer_load_dwordx4 a[48:51], v1, %[srd], s45 offen offset:0 sc1\n\t"
  "v_mfma_f32_16x16x32_bf16 v[44:47], a[144:147], v[68:71], v[44:47]\n\t"
  "ds_read_b64_tr_b16 v[84:85], v110 offset:16384\n\t"
  "v_cvt_pk_bf16_f32 v126, v40, v41\n\t"
  "v_mfma_f32_16x16x32_bf16 v[48:51], a[148:151], v[68:71], v[48:51]\n\t"
  "ds_read_b64_tr_b16 v[86:87], v110 offset:18432\n\t"
  "v_cvt_pk_bf16_f32 v122, v40, v41\n\t"
  "v_mfma_f32_16x16x32_bf16 v[52:55], a[152:155], v[68:71], v[52:55]\n\t"
  "ds_read_b64_tr_b16 v[88:89], v111 offset:16384\n\t"
  "s_mov_b32 s55, 3194880\n\t"
  "v_mfma_f32_16x16x32_bf16 v[56:59], a[144:147], v[72:75], v[56:59]\n\t"
  "ds_read_b64_tr_b16 v[90:91], v111 offset:18432\n\t"
  "buffer_load_dwordx4 a[52:55], v1, %[srd], s45 offen offset:1024 sc1\n\t"
  "v_mfma_f32_16x16x32_bf16 v[60:63], a[148:151], v[72:75], v[60:63]\n\t"
  "v_permlane16_swap_b32 v126, v123\n\t"
  "v_mfma_f32_16x16x32_bf16 v[64:67], a[152:155], v[72:75], v[64:67]\n\t"
  "s_waitcnt lgkmcnt(4)\n\t"
  "v_permlane32_swap_b32 v125, v123\n\t"
  "v_mfma_f32_16x16x32_bf16 v[44:47], a[156:159], v[76:79], v[44:47]\n\t"
  "ds_read_b64_tr_b16 v[68:69], v110 offset:20480\n\t"
  "buffer_store_dwordx4 v[120:123], v8, %[srd], s55 offen sc1\n\t"
  "v_mfma_f32_16x16x32_bf16 v[48:51], a[160:163], v[76:79], v[48:51]\n\t"
  "ds_read_b64_tr_b16 v[70:71], v110 offset:22528\n\t"
  "v_and_b32 v128, 0xffff0000, v105\n\t"
  "v_mfma_f32_16x16x32_bf16 v[52:55], a[164:167], v[76:79], v[52:55]\n\t"
  "ds_read_b64_tr_b16 v[72:73], v111 offset:20480\n\t"
  "v_and_b32 v129, 0xffff0000, v107\n\t"
  "v_mfma_f32_16x16x32_bf16 v[56:59], a[156:159], v[80:83], v[56:59]\n\t"
  "ds_read_b64_tr_b16 v[74:75], v111 offset:22528\n\t"
  "s_waitcnt vmcnt(12)\n\t"
  "v_cmp_ne_u64 vcc, v[132:133], v[138:139]\n\t"
  "v_mfma_f32_16x16x32_bf16 v[60:63], a[160:163], v[80:83], v[60:63]\n\t"
  "v_rcp_f32 v128, v128\n\t"
  "v_rcp_f32 v129, v129\n\t"
  "v_mfma_f32_16x16x32_bf16 v[64:67], a[164:167], v[80:83], v[64:67]\n\t"
  "s_waitcnt lgkmcnt(4)\n\t"
  "s_cbranch_vccnz Lfl0_slow%=\n\t"
  "Lfl0_back%=:\n\t"
  "v_mfma_f32_16x16x32_bf16 v[44:47], a[168:171], v[84:87], v[44:47]\n\t"
  "ds_read_b64_tr_b16 v[76:77], v110 offset:24576\n\t"
  "s_add_u32 s44, %[sH2ld], 0\n\t"
  "v_mfma_f32_16x16x32_bf16 v[48:51], a[172:175], v[84:87], v[48:51]\n\t"
  "ds_read_b64_tr_b16 v[78:79], v110 offset:26624\n\t"
  "v_mul_f32 v128, v128, v128\n\t"
  "v_mfma_f32_16x16x32_bf16 v[52:55], a[176:179], v[84:87], v[52:55]\n\t"
  "ds_read_b64_tr_b16 v[80:81], v111 offset:24576\n\t"
  "buffer_load_dwordx4 a[56:59], v1, %[srd], s44 offen offset:0 sc1\n\t"
  "v_mfma_f32_16x16x32_bf16 v[56:59], a[168:171], v[88:91], v[56:59]\n\t"
  "ds_read_b64_tr_b16 v[82:83], v111 offset:26624\n\t"
  "ds_read_b128 a[160:163], v141 offset:0\n\t"
  "v_mfma_f32_16x16x32_bf16 v[60:63], a[172:175], v[88:91], v[60:63]\n\t"
  "ds_read_b128 a[164:167], v141 offset:1024\n\t"
  "v_mul_f32 v129, v129, v129\n\t"
  "v_mfma_f32_16x16x32_bf16 v[64:67], a[176:179], v[88:91], v[64:67]\n\t"
  "s_waitcnt lgkmcnt(6)\n\t"
  "buffer_load_dwordx4 a[60:63], v1, %[srd], s44 offen offset:1024 sc1\n\t"
  "v_mfma_f32_16x16x32_bf16 v[44:47], a[180:183], v[68:71], v[44:47]\n\t"
  "ds_read_b64_tr_b16 v[84:85], v110 offset:28672\n\t"
  "ds_read_b128 a[168:171], v141 offset:2048\n\t"
  "v_mfma_f32_16x16x32_bf16 v[48:51], a[184:187], v[68:71], v[48:51]\n\t"
  "ds_read_b64_tr_b16 v[86:87], v110 offset:30720\n\t"
  "ds_read_b128 a[172:175], v141 offset:3072\n\t"
  "v_mfma_f32_16x16x32_bf16 v[52:55], a[188:191], v[68:71], v[52:55]\n\t"
  "ds_read_b64_tr_b16 v[88:89], v111 offset:28672\n\t"
  "buffer_load_dwordx4 a[64:67], v1, %[srd], s44 offen offset:2048 sc1\n\t"
  "v_mfma_f32_16x16x32_bf16 v[56:59], a[180:183], v[72:75], v[56:59]\n\t"
  "ds_read_b64_tr_b16 v[90:91], v111 offset:30720\n\t"
  "ds_read_b128 a[176:179], v141 offset:4096\n\t"
  "v_mfma_f32_16x16x32_bf16 v[60:63], a[184:187], v[72:75], v[60:63]\n\t"
  "v_mfma_f32_16x16x32_bf16 v[64:67], a[188:191], v[72:75], v[64:67]\n\t"
  "s_waitcnt lgkmcnt(9)\n\t"
  "buffer_load_dwordx4 a[68:71], v1, %[srd], s44 offen offset:3072 sc1\n\t"
  "v_mfma_f32_16x16x32_bf16 v[44:47], a[192:195], v[76:79], v[44:47]\n\t"
  "ds_read_b64_tr_b16 v[68:69], v110 offset:32768\n\t"
  "ds_read_b128 a[180:183], v141 offset:5120\n\t"
  "v_mfma_f32_16x16x32_bf16 v[48:51], a[196:199], v[76:79], v[48:51]\n\t"
  "ds_read_b64_tr_b16 v[70:71], v110 offset:34816\n\t"
  "ds_read_b128 a[184:187], v141 offset:6144\n\t"
  "v_mfma_f32_16x16x32_bf16 v[52:55], a[200:203], v[76:79], v[52:55]\n\t"
  "ds_read_b64_tr_b16 v[72:73], v111 offset:32768\n\t"
  "s_add_u32 s44, %[sH2ld], 4096\n\t"
  "v_mfma_f32_16x16x32_bf16 v[56:59], a[192:195], v[80:83], v[56:59]\n\t"
  "ds_read_b64_tr_b16 v[74:75], v111 offset:34816\n\t"
  "ds_read_b128 a[188:191], v141 offset:7168\n\t"
  "v_mfma_f32_16x16x32_bf16 v[60:63], a[196:199], v[80:83], v[60:63]\n\t"
  "buffer_load_dwordx4 a[72:75], v1, %[srd], s44 offen offset:0 sc1\n\t"
  "v_mfma_f32_16x16x32_bf16 v[64:67], a[200:203], v[80:83], v[64:67]\n\t"
  "s_waitcnt lgkmcnt(8)\n\t"
  "ds_read_b128 a[192:195], v141 offset:8192\n\t"
  "v_mfma_f32_16x16x32_bf16 v[44:47], a[204:207], v[84:87], v[44:47]\n\t"
  "ds_read_b64_tr_b16 v[76:77], v110 offset:36864\n\t"
  "ds_read_b128 a[196:199], v141 offset:9216\n\t"
  "v_mfma_f32_16x16x32_bf16 v[48:51], a[208:211], v[84:87], v[48:51]\n\t"
  "ds_read_b64_tr_b16 v[78:79], v110 offset:38912\n\t"
  "buffer_load_dwordx4 a[76:79], v1, %[srd], s44 offen offset:1024 sc1\n\t"
  "v_mfma_f32_16x16x32_bf16 v[52:55], a[212:215], v[84:87], v[52:55]\n\t"
  "ds_read_b64_tr_b16 v[80:81], v111 offset:36864\n\t"
  "ds_read_b128 a[200:203], v141 offset:10240\n\t"
  "v_mfma_f32_16x16x32_bf16 v[56:59], a[204:207], v[88:91], v[56:59]\n\t"
  "ds_read_b64_tr_b16 v[82:83], v111 offset:38912\n\t"
  "v_mfma_f32_16x16x32_bf16 v[60:63], a[208:211], v[88:91], v[60:63]\n\t"
  "buffer_load_dwordx4 a[80:83], v1, %[srd], s44 offen offset:2048 sc1\n\t"
  "v_mfma_f32_16x16x32_bf16 v[64:67], a[212:215], v[88:91], v[64:67]\n\t"
  "s_waitcnt lgkmcnt(8)\n\t"
  "ds_read_b128 a[204:207], v141 offset:11264\n\t"
  "v_mfma_f32_16x16x32_bf16 v[44:47], a[216:219], v[68:71], v[44:47]\n\t"
  "ds_read_b128 a[208:211], v141 offset:12288\n\t"
  "ds_read_b128 a[212:215], v141 offset:13312\n\t"
  "v_mfma_f32_16x16x32_bf16 v[48:51], a[220:223], v[68:71], v[48:51]\n\t"
  "buffer_load_dwordx4 a[84:87], v1, %[srd], s44 offen offset:3072 sc1\n\t"
  "v_mfma_f32_16x16x32_bf16 v[52:55], a[224:227], v[68:71], v[52:55]\n\t"
  "v_mfma_f32_16x16x32_bf16 v[56:59], a[216:219], v[72:75], v[56:59]\n\t"
  "v_mfma_f32_16x16x32_bf16 v[60:63], a[220:223], v[72:75], v[60:63]\n\t"
  "s_add_u32 s44, %[sH2ld], 8192\n\t"
  "v_mfma_f32_16x16x32_bf16 v[64:67], a[224:227], v[72:75], v[64:67]\n\t"
  "s_waitcnt lgkmcnt(3)\n\t"
  "ds_read_b128 a[216:219], v141 offset:14336\n\t"
  "v_mfma_f32_16x16x32_bf16 v[44:47], a[228:231], v[76:79], v[44:47]\n\t"
  "s_add_u32 s57, %[sFL], 8192\n\t"
  "buffer_load_dwordx2 v[138:139], v137, %[srd], s57 offen sc1\n\t"
  "v_mfma_f32_16x16x32_bf16 v[48:51], a[232:235], v[76:79], v[48:51]\n\t"
  "buffer_load_dwordx4 a[88:91], v1, %[srd], s44 offen offset:0 sc1\n\t"
  "ds_read_b128 a[220:223], v141 offset:15360\n\t"
  "v_mfma_f32_16x16x32_bf16 v[52:55], a[236:239], v[76:79], v[52:55]\n\t"
  "ds_read_b128 a[224:227], v141 offset:16384\n\t"
  "v_mfma_f32_16x16x32_bf16 v[56:59], a[228:231], v[80:83], v[56:59]\n\t"
  "buffer_load_dwordx4 a[92:95], v1, %[srd], s44 offen offset:1024 sc1\n\t"
  "v_mfma_f32_16x16x32_bf16 v[60:63], a[232:235], v[80:83], v[60:63]\n\t"
  "v_mfma_f32_16x16x32_bf16 v[64:67], a[236:239], v[80:83], v[64:67]\n\t"
  "ds_read_b128 a[228:231], v141 offset:17408\n\t"
  "ds_read_b128 a[232:235], v141 offset:18432\n\t"
  "v_mfma_f32_16x16x16_bf16 v[44:47], a[240:241], v[104:105], v[44:47]\n\t"
  "buffer_load_dwordx4 a[96:99], v1, %[srd], s44 offen offset:2048 sc1\n\t"
  "ds_read_b128 a[236:239], v141 offset:19456\n\t"
  "v_mfma_f32_16x16x16_bf16 v[48:51], a[242:243], v[104:105], v[48:51]\n\t"
  "v_mfma_f32_16x16x16_bf16 v[52:55], a[244:245], v[104:105], v[52:55]\n\t"
  "v_mfma_f32_16x16x16_bf16 v[56:59], a[240:241], v[106:107], v[56:59]\n\t"
  "buffer_load_dwordx4 a[100:103], v1, %[srd], s44 offen offset:3072 sc1\n\t"
  "v_mfma_f32_16x16x16_bf16 v[60:63], a[242:243], v[106:107], v[60:63]\n\t"
  "v_mfma_f32_16x16x16_bf16 v[64:67], a[244:245], v[106:107], v[64:67]\n\t"
  "s_movk_i32 s56, 16384\n\t"
  "s_waitcnt vmcnt(13)\n\t"
  "buffer_store_dwordx4 v[132:135], v9, %[srd], s56 offen sc1\n\t"
  "v_max_f32 v44, 0, v44\n\t"
  "v_max_f32 v45, 0, v45\n\t"
  "v_pk_mul_f32 v[46:47], v[46:47], v[128:129] op_sel:[0,0] op_sel_hi:[1,0]\n\t"
  "v_pk_mul_f32 v[44:45], v[44:45], v[46:47]\n\t"
  "v_cvt_pk_bf16_f32 v116, v44, v45\n\t"
  "v_cvt_pk_bf16_f32 v112, v44, v45\n\t"
  "v_max_f32 v48, 0, v48\n\t"
  "v_max_f32 v49, 0, v49\n\t"
  "v_pk_mul_f32 v[50:51], v[50:51], v[128:129] op_sel:[0,0] op_sel_hi:[1,0]\n\t"
  "v_pk_mul_f32 v[48:49], v[48:49], v[50:51]\n\t"
  "v_cvt_pk_bf16_f32 v117, v48, v49\n\t"
  "v_cvt_pk_bf16_f32 v113, v48, v49\n\t"
  "v_max_f32 v52, 0, v52\n\t"
  "v_max_f32 v53, 0, v53\n\t"
  "v_pk_mul_f32 v[54:55], v[54:55], v[128:129] op_sel:[0,0] op_sel_hi:[1,0]\n\t"
  "v_permlane16_swap_b32 v116, v117\n\t"
  "v_pk_mul_f32 v[52:53], v[52:53], v[54:55]\n\t"
  "v_cvt_pk_bf16_f32 v118, v52, v53\n\t"
  "v_cvt_pk_bf16_f32 v114, v52, v53\n\t"
  "s_mov_b32 s55, 3244032\n\t"
  "v_permlane16_swap_b32 v118, v115\n\t"
  "s_nop 1\n\t"
  "v_permlane32_swap_b32 v117, v115\n\t"
  "buffer_store_dwordx4 v[112:115], v8, %[srd], s55 offen sc1\n\t"
  "v_max_f32 v56, 0, v56\n\t"
  "v_max_f32 v57, 0, v57\n\t"
  "v_pk_mul_f32 v[58:59], v[58:59], v[128:129] op_sel:[0,1] op_sel_hi:[1,1]\n\t"
  "v_pk_mul_f32 v[56:57], v[56:57], v[58:59]\n\t"
  "v_cvt_pk_bf16_f32 v124, v56, v57\n\t"
  "v_cvt_pk_bf16_f32 v120, v56, v57\n\t"
  "v_max_f32 v60, 0, v60\n\t"
  "v_max_f32 v61, 0, v61\n\t"
  "v_pk_mul_f32 v[62:63], v[62:63], v[128:129] op_sel:[0,1] op_sel_hi:[1,1]\n\t"
  "v_pk_mul_f32 v[60:61], v[60:61], v[62:63]\n\t"
  "v_cvt_pk_bf16_f32 v125, v60, v61\n\t"
  "v_cvt_pk_bf16_f32 v121, v60, v61\n\t"
  "v_max_f32 v64, 0, v64\n\t"
  "v_max_f32 v65, 0, v65\n\t"
  "v_pk_mul_f32 v[66:67], v[66:67], v[128:129] op_sel:[0,1] op_sel_hi:[1,1]\n\t"
  "v_permlane16_swap_b32 v124, v125\n\t"
  "v_pk_mul_f32 v[64:65], v[64:65], v[66:67]\n\t"
  "v_cvt_pk_bf16_f32 v126, v64, v65\n\t"
  "v_cvt_pk_bf16_f32 v122, v64, v65\n\t"
  "s_mov_b32 s55, 3293184\n\t"
  "v_permlane16_swap_b32 v126, v123\n\t"
  "s_nop 1\n\t"
  "v_permlane32_swap_b32 v125, v123\n\t"
  "buffer_store_dwordx4 v[120:123], v8, %[srd], s55 offen sc1\n\t"
  "s_waitcnt lgkmcnt(0)\n\t"
  "s_barrier\n\t"
  "s_waitcnt vmcnt(15)\n\t"
  "v_mfma_f32_16x16x32_bf16 v[20:23], v[144:147], a[56:59], 0\n\t"
  "v_lshrrev_b32 v63, 3, v1\n\t"
  "v_and_b32 v63, 0x60, v63\n\t"
  "v_mfma_f32_16x16x32_bf16 v[24:27], v[148:151], a[56:59], 0\n\t"
  "v_lshrrev_b32 v62, 1, v63\n\t"
  "buffer_load_dwordx4 v[68:71], v63, %[srd], %[sRT] offen offset:0 sc1\n\t"
  "v_mfma_f32_16x16x32_bf16 v[28:31], v[152:155], a[56:59], 0\n\t"
  "buffer_load_dwordx4 v[72:75], v63, %[srd], %[sRT] offen offset:16 sc1\n\t"
  "buffer_load_dwordx4 v[76:79], v63, %[srd], %[sRT] offen offset:128 sc1\n\t"
  "v_mfma_f32_16x16x32_bf16 v[32:35], v[156:159], a[56:59], 0\n\t"
  "buffer_load_dwordx4 v[80:83], v63, %[srd], %[sRT] offen offset:144 sc1\n\t"
  "buffer_load_dwordx4 v[84:87], v62, %[srd], %[sRT] offen offset:256 sc1\n\t"
  "v_mfma_f32_16x16x32_bf16 v[36:39], v[160:163], a[56:59], 0\n\t"
  "buffer_load_dwordx4 v[64:67], v17, %[srd], %[sRT] offen offset:0 sc1\n\t"
  "buffer_load_dwordx4 v[108:111], v17, %[srd], %[sRT] offen offset:16 sc1\n\t"
  "s_waitcnt vmcnt(21)\n\t"
  "v_mfma_f32_16x16x32_bf16 v[20:23], v[164:167], a[60:63], v[20:23]\n\t"
  "s_mul_i32 s48, %[w], -15360\n\t"
  "s_add_u32 s48, s48, 81920\n\t"
  "v_mfma_f32_16x16x32_bf16 v[24:27], v[168:171], a[60:63], v[24:27]\n\t"
  "v_add_u32 v62, s48, v13\n\t"
  "v_add_u32 v60, 61440, v14\n\t"
  "v_mfma_f32_16x16x32_bf16 v[28:31], v[172:175], a[60:63], v[28:31]\n\t"
  "v_add_u32 v61, 61440, v15\n\t"
  "v_accvgpr_read_b32 v89, a246\n\t"
  "v_mfma_f32_16x16x32_bf16 v[32:35], v[176:179], a[60:63], v[32:35]\n\t"
  "s_waitcnt vmcnt(14)\n\t"
  "v_cmp_ne_u64 vcc, v[132:133], v[138:139]\n\t"
  "v_mfma_f32_16x16x32_bf16 v[36:39], v[180:183], a[60:63], v[36:39]\n\t"
  "v_lshlrev_b32 v88, 16, v89\n\t"
  "v_and_b32 v89, 0xffff0000, v89\n\t"
  "v_mfma_f32_16x16x32_bf16 v[20:23], v[184:187], a[64:67], v[20:23]\n\t"
  "s_cbranch_vccnz Lfl1_slow%=\n\t"
  "Lfl1_back%=:\n\t"
  "v_mfma_f32_16x16x32_bf16 v[24:27], v[188:191], a[64:67], v[24:27]\n\t"
  "v_accvgpr_read_b32 v91, a247\n\t"
  "v_lshlrev_b32 v90, 16, v91\n\t"
  "v_mfma_f32_16x16x32_bf16 v[28:31], v[192:195], a[64:67], v[28:31]\n\t"
  "s_add_u32 s45, %[sH2ld], 98304\n\t"
  "v_and_b32 v91, 0xffff0000, v91\n\t"
  "v_mfma_f32_16x16x32_bf16 v[32:35], v[196:199], a[64:67], v[32:35]\n\t"
  "v_accvgpr_read_b32 v93, a248\n\t"
  "v_lshlrev_b32 v92, 16, v93\n\t"
  "v_mfma_f32_16x16x32_bf16 v[36:39], v[200:203], a[64:67], v[36:39]\n\t"
  "v_and_b32 v93, 0xffff0000, v93\n\t"
  "v_accvgpr_read_b32 v95, a249\n\t"
  "v_mfma_f32_16x16x32_bf16 v[20:23], v[204:207], a[68:71], v[20:23]\n\t"
  "buffer_load_dwordx4 a[104:107], v1, %[srd], s45 offen offset:0 sc1\n\t"
  "v_lshlrev_b32 v94, 16, v95\n\t"
  "v_mfma_f32_16x16x32_bf16 v[24:27], v[208:211], a[68:71], v[24:27]\n\t"
  "v_and_b32 v95, 0xffff0000, v95\n\t"
  "v_accvgpr_read_b32 v97, a250\n\t"
  "v_mfma_f32_16x16x32_bf16 v[28:31], v[212:215], a[68:71], v[28:31]\n\t"
  "v_lshlrev_b32 v96, 16, v97\n\t"
  "v_and_b32 v97, 0xffff0000, v97\n\t"
  "v_mfma_f32_16x16x32_bf16 v[32:35], v[216:219], a[68:71], v[32:35]\n\t"
  "buffer_load_dwordx4 a[108:111], v1, %[srd], s45 offen offset:1024 sc1\n\t"
  "v_accvgpr_read_b32 v99, a251\n\t"
  "v_mfma_f32_16x16x32_bf16 v[36:39], v[220:223], a[68:71], v[36:39]\n\t"
  "v_lshlrev_b32 v98, 16, v99\n\t"
  "v_and_b32 v99, 0xffff0000, v99\n\t"
  "v_mfma_f32_16x16x32_bf16 v[20:23], v[224:227], a[72:75], v[20:23]\n\t"
  "v_accvgpr_read_b32 v101, a252\n\t"
  "v_lshlrev_b32 v100, 16, v101\n\t"
  "v_mfma_f32_16x16x32_bf16 v[24:27], v[228:231], a[72:75], v[24:27]\n\t"
  "buffer_load_dwordx4 a[112:115], v1, %[srd], s45 offen offset:2048 sc1\n\t"
  "v_and_b32 v101, 0xffff0000, v101\n\t"
  "v_mfma_f32_16x16x32_bf16 v[28:31], v[232:235], a[72:75], v[28:31]\n\t"
  "v_accvgpr_read_b32 v103, a253\n\t"
  "v_lshlrev_b32 v102, 16, v103\n\t"
  "v_mfma_f32_16x16x32_bf16 v[32:35], v[236:239], a[72:75], v[32:35]\n\t"
  "v_and_b32 v103, 0xffff0000, v103\n\t"
  "v_accvgpr_read_b32 v105, a254\n\t"
  "v_mfma_f32_16x16x32_bf16 v[36:39], v[240:243], a[72:75], v[36:39]\n\t"
  "buffer_load_dwordx4 a[116:119], v1, %[srd], s45 offen offset:3072 sc1\n\t"
  "s_add_u32 s57, %[sFL], 16384\n\t"
  "v_mfma_f32_16x16x32_bf16 v[20:23], v[244:247], a[76:79], v[20:23]\n\t"
  "buffer_load_dwordx2 v[138:139], v137, %[srd], s57 offen sc1\n\t"
  "v_lshlrev_b32 v104, 16, v105\n\t"
  "v_mfma_f32_16x16x32_bf16 v[24:27], a[160:163], a[76:79], v[24:27]\n\t"
  "s_movk_i32 s56, 24576\n\t"
  "s_waitcnt vmcnt(12)\n\t"
  "v_mfma_f32_16x16x32_bf16 v[28:31], a[164:167], a[76:79], v[28:31]\n\t"
  "buffer_store_dwordx4 v[132:135], v9, %[srd], s56 offen sc1\n\t"
  "v_and_b32 v105, 0xffff0000, v105\n\t"
  "v_mfma_f32_16x16x32_bf16 v[32:35], a[168:171], a[76:79], v[32:35]\n\t"
  "s_add_u32 s45, %[sH2ld], 102400\n\t"
  "v_accvgpr_read_b32 v107, a255\n\t"
  "v_mfma_f32_16x16x32_bf16 v[36:39], a[172:175], a[76:79], v[36:39]\n\t"
  "v_lshlrev_b32 v106, 16, v107\n\t"
  "v_and_b32 v107, 0xffff0000, v107\n\t"
  "v_mfma_f32_16x16x32_bf16 v[20:23], a[176:179], a[80:83], v[20:23]\n\t"
  "v_mfma_f32_16x16x32_bf16 v[24:27], a[180:183], a[80:83], v[24:27]\n\t"
  "buffer_load_dwordx4 a[120:123], v1, %[srd], s45 offen offset:0 sc1\n\t"
  "v_mfma_f32_16x16x32_bf16 v[28:31], a[184:187], a[80:83], v[28:31]\n\t"
  "v_mfma_f32_16x16x32_bf16 v[32:35], a[188:191], a[80:83], v[32:35]\n\t"
  "v_mfma_f32_16x16x32_bf16 v[36:39], a[192:195], a[80:83], v[36:39]\n\t"
  "buffer_load_dwordx4 a[124:127], v1, %[srd], s45 offen offset:1024 sc1\n\t"
  "v_mfma_f32_16x16x32_bf16 v[20:23], a[196:199], a[84:87], v[20:23]\n\t"
  "v_mfma_f32_16x16x32_bf16 v[24:27], a[200:203], a[84:87], v[24:27]\n\t"
  "v_mfma_f32_16x16x32_bf16 v[28:31], a[204:207], a[84:87], v[28:31]\n\t"
  "buffer_load_dwordx4 a[128:131], v1, %[srd], s45 offen offset:2048 sc1\n\t"
  "v_mfma_f32_16x16x32_bf16 v[32:35], a[208:211], a[84:87], v[32:35]\n\t"
  "v_mfma_f32_16x16x32_bf16 v[36:39], a[212:215], a[84:87], v[36:39]\n\t"
  "v_mfma_f32_16x16x32_bf16 v[20:23], a[216:219], a[88:91], v[20:23]\n\t"
  "buffer_load_dwordx4 a[132:135], v1, %[srd], s45 offen offset:3072 sc1\n\t"
  "v_mfma_f32_16x16x32_bf16 v[24:27], a[220:223], a[88:91], v[24:27]\n\t"
  "v_mfma_f32_16x16x32_bf16 v[28:31], a[224:227], a[88:91], v[28:31]\n\t"
  "v_mfma_f32_16x16x32_bf16 v[32:35], a[228:231], a[88:91], v[32:35]\n\t"
  "v_mfma_f32_16x16x32_bf16 v[36:39], a[232:235], a[88:91], v[36:39]\n\t"
  "s_add_u32 s45, %[sH2ld], 106496\n\t"
  "v_mfma_f32_16x16x32_bf16 v[20:23], a[236:239], a[92:95], v[20:23]\n\t"
  "s_waitcnt vmcnt(16)\n\t"
  "v_mul_f32 v88, v88, v68\n\t"
  "v_mfma_f32_16x16x32_bf16 v[24:27], a[0:3], a[92:95], v[24:27]\n\t"
  "v_mul_f32 v89, v89, v69\n\t"
  "v_mul_f32 v90, v90, v70\n\t"
  "v_mfma_f32_16x16x32_bf16 v[28:31], a[4:7], a[92:95], v[28:31]\n\t"
  "buffer_load_dwordx4 a[136:139], v1, %[srd], s45 offen offset:0 sc1\n\t"
  "v_mul_f32 v91, v91, v71\n\t"
  "v_mfma_f32_16x16x32_bf16 v[32:35], a[8:11], a[92:95], v[32:35]\n\t"
  "ds_write_b128 v62, v[88:91] offset:0\n\t"
  "s_waitcnt vmcnt(16)\n\t"
  "v_mul_f32 v92, v92, v72\n\t"
  "v_mfma_f32_16x16x32_bf16 v[36:39], a[12:15], a[92:95], v[36:39]\n\t"
  "v_mul_f32 v93, v93, v73\n\t"
  "v_mul_f32 v94, v94, v74\n\t"
  "v_mfma_f32_16x16x32_bf16 v[20:23], a[16:19], a[96:99], v[20:23]\n\t"
  "buffer_load_dwordx4 a[140:143], v1, %[srd], s45 offen offset:1024 sc1\n\t"
  "v_mul_f32 v95, v95, v75\n\t"
  "v_mfma_f32_16x16x32_bf16 v[24:27], a[20:23], a[96:99], v[24:27]\n\t"
  "ds_write_b128 v62, v[92:95] offset:1024\n\t"
  "s_waitcnt vmcnt(16)\n\t"
  "v_mul_f32 v96, v96, v76\n\t"
  "v_mfma_f32_16x16x32_bf16 v[28:31], a[24:27], a[96:99], v[28:31]\n\t"
  "v_mul_f32 v97, v97, v77\n\t"
  "v_mul_f32 v98, v98, v78\n\t"
  "v_mfma_f32_16x16x32_bf16 v[32:35], a[28:31], a[96:99], v[32:35]\n\t"
  "buffer_load_dwordx4 a[144:147], v1, %[srd], s45 offen offset:2048 sc1\n\t"
  "v_mul_f32 v99, v99, v79\n\t"
  "v_mfma_f32_16x16x32_bf16 v[36:39], a[32:35], a[96:99], v[36:39]\n\t"
  "ds_write_b128 v62, v[96:99] offset:2048\n\t"
  "s_waitcnt vmcnt(16)\n\t"
  "v_mul_f32 v100, v100, v80\n\t"
  "v_mfma_f32_16x16x32_bf16 v[20:23], a[36:39], a[100:103], v[20:23]\n\t"
  "v_mul_f32 v101, v101, v81\n\t"
  "v_mul_f32 v102, v102, v82\n\t"
  "v_mfma_f32_16x16x32_bf16 v[24:27], a[40:43], a[100:103], v[24:27]\n\t"
  "buffer_load_dwordx4 a[148:151], v1, %[srd], s45 offen offset:3072 sc1\n\t"
  "v_mul_f32 v103, v103, v83\n\t"
  "v_mfma_f32_16x16x32_bf16 v[28:31], a[44:47], a[100:103], v[28:31]\n\t"
  "ds_write_b128 v62, v[100:103] offset:3072\n\t"
  "s_waitcnt vmcnt(16)\n\t"
  "v_mul_f32 v104, v104, v84\n\t"
  "v_mfma_f32_16x16x32_bf16 v[32:35], a[48:51], a[100:103], v[32:35]\n\t"
  "v_mul_f32 v105, v105, v85\n\t"
  "v_mul_f32 v106, v106, v86\n\t"
  "v_mfma_f32_16x16x32_bf16 v[36:39], a[52:55], a[100:103], v[36:39]\n\t"
  "s_waitcnt vmcnt(12)\n\t"
  "v_mul_f32 v107, v107, v87\n\t"
  "v_mfma_f32_16x16x32_bf16 v[40:43], v[144:147], a[104:107], 0\n\t"
  "ds_write_b128 v62, v[104:107] offset:4096\n\t"
  "v_mfma_f32_16x16x32_bf16 v[44:47], v[148:151], a[104:107], 0\n\t"
  "v_mfma_f32_16x16x32_bf16 v[48:51], v[152:155], a[104:107], 0\n\t"
  "v_mfma_f32_16x16x32_bf16 v[52:55], v[156:159], a[104:107], 0\n\t"
  "ds_write_b128 v13, v[20:23] offset:0\n\t"
  "v_mfma_f32_16x16x32_bf16 v[56:59], v[160:163], a[104:107], 0\n\t"
  "v_mfma_f32_16x16x32_bf16 v[40:43], v[164:167], a[108:111], v[40:43]\n\t"
  "v_mfma_f32_16x16x32_bf16 v[44:47], v[168:171], a[108:111], v[44:47]\n\t"
  "ds_write_b128 v13, v[24:27] offset:1024\n\t"
  "v_mfma_f32_16x16x32_bf16 v[48:51], v[172:175], a[108:111], v[48:51]\n\t"
  "v_mfma_f32_16x16x32_bf16 v[52:55], v[176:179], a[108:111], v[52:55]\n\t"
  "v_mfma_f32_16x16x32_bf16 v[56:59], v[180:183], a[108:111], v[56:59]\n\t"
  "s_waitcnt vmcnt(10)\n\t"
  "ds_write_b128 v13, v[28:31] offset:2048\n\t"
  "v_mfma_f32_16x16x32_bf16 v[40:43], v[184:187], a[112:115], v[40:43]\n\t"
  "v_mfma_f32_16x16x32_bf16 v[44:47], v[188:191], a[112:115], v[44:47]\n\t"
  "v_mfma_f32_16x16x32_bf16 v[48:51], v[192:195], a[112:115], v[48:51]\n\t"
  "ds_write_b128 v13, v[32:35] offset:3072\n\t"
  "v_mfma_f32_16x16x32_bf16 v[52:55], v[196:199], a[112:115], v[52:55]\n\t"
  "v_mfma_f32_16x16x32_bf16 v[56:59], v[200:203], a[112:115], v[56:59]\n\t"
  "v_mfma_f32_16x16x32_bf16 v[40:43], v[204:207], a[116:119], v[40:43]\n\t"
  "s_waitcnt vmcnt(9)\n\t"
  "v_cmp_ne_u64 vcc, v[132:133], v[138:139]\n\t"
  "v_mfma_f32_16x16x32_bf16 v[44:47], v[208:211], a[116:119], v[44:47]\n\t"
  "ds_write_b128 v13, v[36:39] offset:4096\n\t"
  "v_mfma_f32_16x16x32_bf16 v[48:51], v[212:215], a[116:119], v[48:51]\n\t"
  "s_cbranch_vccnz Lfl2_slow%=\n\t"
  "Lfl2_back%=:\n\t"
  "v_mfma_f32_16x16x32_bf16 v[52:55], v[216:219], a[116:119], v[52:55]\n\t"
  "s_add_u32 s44, %[sH2ld], 3145728\n\t"
  "v_mfma_f32_16x16x32_bf16 v[56:59], v[220:223], a[116:119], v[56:59]\n\t"
  "s_waitcnt vmcnt(6)\n\t"
  "v_mfma_f32_16x16x32_bf16 v[40:43], v[224:227], a[120:123], v[40:43]\n\t"
  "v_mfma_f32_16x16x32_bf16 v[44:47], v[228:231], a[120:123], v[44:47]\n\t"
  "buffer_load_dwordx4 a[152:155], v1, %[srd], s44 offen offset:0 sc1\n\t"
  "v_mfma_f32_16x16x32_bf16 v[48:51], v[232:235], a[120:123], v[48:51]\n\t"
  "v_mfma_f32_16x16x32_bf16 v[52:55], v[236:239], a[120:123], v[52:55]\n\t"
  "v_mfma_f32_16x16x32_bf16 v[56:59], v[240:243], a[120:123], v[56:59]\n\t"
  "buffer_load_dwordx4 a[156:159], v1, %[srd], s44 offen offset:1024 sc1\n\t"
  "s_add_u32 s57, %[sFL], 24576\n\t"
  "v_mfma_f32_16x16x32_bf16 v[40:43], v[244:247], a[124:127], v[40:43]\n\t"
  "buffer_load_dwordx2 v[138:139], v137, %[srd], s57 offen sc1\n\t"
  "s_waitcnt lgkmcnt(0)\n\t"
  "s_barrier\n\t"
  "v_mfma_f32_16x16x32_bf16 v[44:47], a[160:163], a[124:127], v[44:47]\n\t"
  "ds_read_b128 v[68:71], v14 offset:0\n\t"
  "ds_read_b128 v[72:75], v15 offset:0\n\t"
  "v_mfma_f32_16x16x32_bf16 v[48:51], a[164:167], a[124:127], v[48:51]\n\t"
  "buffer_load_dwordx4 v[248:251], v1, %[srd], s44 offen offset:2048 sc1\n\t"
  "ds_read_b128 v[76:79], v14 offset:20480\n\t"
  "v_mfma_f32_16x16x32_bf16 v[52:55], a[168:171], a[124:127], v[52:55]\n\t"
  "ds_read_b128 v[80:83], v15 offset:20480\n\t"
  "ds_read_b128 v[84:87], v14 offset:40960\n\t"
  "v_mfma_f32_16x16x32_bf16 v[56:59], a[172:175], a[124:127], v[56:59]\n\t"
  "s_waitcnt vmcnt(8)\n\t"
  "ds_read_b128 v[88:91], v15 offset:40960\n\t"
  "v_mfma_f32_16x16x32_bf16 v[40:43], a[176:179], a[128:131], v[40:43]\n\t"
  "buffer_load_dwordx4 v[252:255], v1, %[srd], s44 offen offset:3072 sc1\n\t"
  "ds_read_b128 v[92:95], v60 offset:0\n\t"
  "v_mfma_f32_16x16x32_bf16 v[44:47], a[180:183], a[128:131], v[44:47]\n\t"
  "ds_read_b128 v[96:99], v61 offset:0\n\t"
  "ds_read_b128 v[100:103], v60 offset:20480\n\t"
  "v_mfma_f32_16x16x32_bf16 v[48:51], a[184:187], a[128:131], v[48:51]\n\t"
  "ds_read_b128 v[104:107], v61 offset:20480\n\t"
  "v_mfma_f32_16x16x32_bf16 v[52:55], a[188:191], a[128:131], v[52:55]\n\t"
  "v_mfma_f32_16x16x32_bf16 v[56:59], a[192:195], a[128:131], v[56:59]\n\t"
  "s_add_u32 s44, %[sH2ld], 3149824\n\t"
  "v_mfma_f32_16x16x32_bf16 v[40:43], a[196:199], a[132:135], v[40:43]\n\t"
  "v_mfma_f32_16x16x32_bf16 v[44:47], a[200:203], a[132:135], v[44:47]\n\t"
  "v_mfma_f32_16x16x32_bf16 v[48:51], a[204:207], a[132:135], v[48:51]\n\t"
  "buffer_load_dwordx4 v[112:115], v1, %[srd], s44 offen offset:0 sc1\n\t"
  "v_mfma_f32_16x16x32_bf16 v[52:55], a[208:211], a[132:135], v[52:55]\n\t"
  "v_mfma_f32_16x16x32_bf16 v[56:59], a[212:215], a[132:135], v[56:59]\n\t"
  "s_waitcnt vmcnt(8)\n\t"
  "v_mfma_f32_16x16x32_bf16 v[40:43], a[216:219], a[136:139], v[40:43]\n\t"
  "buffer_load_dwordx4 v[116:119], v1, %[srd], s44 offen offset:1024 sc1\n\t"
  "s_waitcnt lgkmcnt(0)\n\t"
  "v_mfma_f32_16x16x32_bf16 v[44:47], a[220:223], a[136:139], v[44:47]\n\t"
  "v_add_f32 v68, v68, v76\n\t"
  "v_add_f32 v69, v69, v77\n\t"
  "v_mfma_f32_16x16x32_bf16 v[48:51], a[224:227], a[136:139], v[48:51]\n\t"
  "v_add_f32 v70, v70, v78\n\t"
  "v_add_f32 v71, v71, v79\n\t"
  "v_mfma_f32_16x16x32_bf16 v[52:55], a[228:231], a[136:139], v[52:55]\n\t"
  "buffer_load_dwordx4 v[120:123], v1, %[srd], s44 offen offset:2048 sc1\n\t"
  "v_add_f32 v72, v72, v80\n\t"
  "v_mfma_f32_16x16x32_bf16 v[56:59], a[232:235], a[136:139], v[56:59]\n\t"
  "v_add_f32 v73, v73, v81\n\t"
  "v_add_f32 v74, v74, v82\n\t"
  "v_mfma_f32_16x16x32_bf16 v[40:43], a[236:239], a[140:143], v[40:43]\n\t"
  "v_add_f32 v75, v75, v83\n\t"
  "v_add_f32 v84, v84, v92\n\t"
  "v_mfma_f32_16x16x32_bf16 v[44:47], a[0:3], a[140:143], v[44:47]\n\t"
  "buffer_load_dwordx4 v[124:127], v1, %[srd], s44 offen offset:3072 sc1\n\t"
  "v_add_f32 v85, v85, v93\n\t"
  "v_mfma_f32_16x16x32_bf16 v[48:51], a[4:7], a[140:143], v[48:51]\n\t"
  "v_add_f32 v86, v86, v94\n\t"
  "v_add_f32 v87, v87, v95\n\t"
  "v_mfma_f32_16x16x32_bf16 v[52:55], a[8:11], a[140:143], v[52:55]\n\t"
  "v_add_f32 v88, v88, v96\n\t"
  "v_add_f32 v89, v89, v97\n\t"
  "v_mfma_f32_16x16x32_bf16 v[56:59], a[12:15], a[140:143], v[56:59]\n\t"
  "s_waitcnt vmcnt(9)\n\t"
  "v_add_f32 v90, v90, v98\n\t"
  "v_mfma_f32_16x16x32_bf16 v[40:43], a[16:19], a[144:147], v[40:43]\n\t"
  "s_add_u32 s44, %[sH2ld], 3153920\n\t"
  "v_add_f32 v91, v91, v99\n\t"
  "v_mfma_f32_16x16x32_bf16 v[44:47], a[20:23], a[144:147], v[44:47]\n\t"
  "v_add_f32 v68, v68, v84\n\t"
  "v_add_f32 v69, v69, v85\n\t"
  "v_mfma_f32_16x16x32_bf16 v[48:51], a[24:27], a[144:147], v[48:51]\n\t"
  "v_add_f32 v70, v70, v86\n\t"
  "v_add_f32 v71, v71, v87\n\t"
  "v_mfma_f32_16x16x32_bf16 v[52:55], a[28:31], a[144:147], v[52:55]\n\t"
  "buffer_load_dwordx4 v[128:131], v1, %[srd], s44 offen offset:0 sc1\n\t"
  "v_add_f32 v72, v72, v88\n\t"
  "v_mfma_f32_16x16x32_bf16 v[56:59], a[32:35], a[144:147], v[56:59]\n\t"
  "v_add_f32 v73, v73, v89\n\t"
  "v_add_f32 v74, v74, v90\n\t"
  "v_mfma_f32_16x16x32_bf16 v[40:43], a[36:39], a[148:151], v[40:43]\n\t"
  "v_add_f32 v75, v75, v91\n\t"
  "v_fma_f32 v68, v68, v64, v100\n\t"
  "v_mfma_f32_16x16x32_bf16 v[44:47], a[40:43], a[148:151], v[44:47]\n\t"
  "buffer_load_dwordx4 a[240:243], v1, %[srd], s44 offen offset:1024 sc1\n\t"
  "v_fma_f32 v69, v69, v65, v101\n\t"
  "v_mfma_f32_16x16x32_bf16 v[48:51], a[44:47], a[148:151], v[48:51]\n\t"
  "v_fma_f32 v70, v70, v66, v102\n\t"
  "v_fma_f32 v71, v71, v67, v103\n\t"
  "v_mfma_f32_16x16x32_bf16 v[52:55], a[48:51], a[148:151], v[52:55]\n\t"
  "v_fma_f32 v72, v72, v108, v104\n\t"
  "v_fma_f32 v73, v73, v109, v105\n\t"
  "v_mfma_f32_16x16x32_bf16 v[56:59], a[52:55], a[148:151], v[56:59]\n\t"
  "s_waitcnt vmcnt(9)\n\t"
  "buffer_load_dwordx4 a[56:59], v1, %[srd], s44 offen offset:2048 sc1\n\t"
  "v_mfma_f32_16x16x32_bf16 v[20:23], v[144:147], a[152:155], 0\n\t"
  "v_fma_f32 v74, v74, v110, v106\n\t"
  "v_fma_f32 v75, v75, v111, v107\n\t"
  "v_mfma_f32_16x16x32_bf16 v[24:27], v[148:151], a[152:155], 0\n\t"
  "v_cvt_pk_bf16_f32 v76, v68, v69\n\t"
  "v_cvt_pk_bf16_f32 v77, v70, v71\n\t"
  "v_mfma_f32_16x16x32_bf16 v[28:31], v[152:155], a[152:155], 0\n\t"
  "buffer_load_dwordx4 a[60:63], v1, %[srd], s44 offen offset:3072 sc1\n\t"
  "v_cvt_pk_bf16_f32 v78, v72, v73\n\t"
  "v_mfma_f32_16x16x32_bf16 v[32:35], v[156:159], a[152:155], 0\n\t"
  "v_cvt_pk_bf16_f32 v79, v74, v75\n\t"
  "s_mov_b32 s55, 0\n\t"
  "v_mfma_f32_16x16x32_bf16 v[36:39], v[160:163], a[152:155], 0\n\t"
  "buffer_store_dwordx4 v[76:79], v16, %[srdo], s55 offen sc1\n\t"
  "ds_write_b128 v13, v[40:43] offset:5120\n\t"
  "v_mfma_f32_16x16x32_bf16 v[20:23], v[164:167], a[156:159], v[20:23]\n\t"
  "v_mfma_f32_16x16x32_bf16 v[24:27], v[168:171], a[156:159], v[24:27]\n\t"
  "v_mfma_f32_16x16x32_bf16 v[28:31], v[172:175], a[156:159], v[28:31]\n\t"
  "ds_write_b128 v13, v[44:47] offset:6144\n\t"
  "v_mfma_f32_16x16x32_bf16 v[32:35], v[176:179], a[156:159], v[32:35]\n\t"
  "v_mfma_f32_16x16x32_bf16 v[36:39], v[180:183], a[156:159], v[36:39]\n\t"
  "s_waitcnt vmcnt(9)\n\t"
  "v_mfma_f32_16x16x32_bf16 v[20:23], v[184:187], v[248:251], v[20:23]\n\t"
  "ds_write_b128 v13, v[48:51] offset:7168\n\t"
  "v_mfma_f32_16x16x32_bf16 v[24:27], v[188:191], v[248:251], v[24:27]\n\t"
  "v_mfma_f32_16x16x32_bf16 v[28:31], v[192:195], v[248:251], v[28:31]\n\t"
  "v_mfma_f32_16x16x32_bf16 v[32:35], v[196:199], v[248:251], v[32:35]\n\t"
  "ds_write_b128 v13, v[52:55] offset:8192\n\t"
  "v_mfma_f32_16x16x32_bf16 v[36:39], v[200:203], v[248:251], v[36:39]\n\t"
  "v_mfma_f32_16x16x32_bf16 v[20:23], v[204:207], v[252:255], v[20:23]\n\t"
  "v_cmp_ne_u64 vcc, v[132:133], v[138:139]\n\t"
  "v_mfma_f32_16x16x32_bf16 v[24:27], v[208:211], v[252:255], v[24:27]\n\t"
  "ds_write_b128 v13, v[56:59] offset:9216\n\t"
  "v_mfma_f32_16x16x32_bf16 v[28:31], v[212:215], v[252:255], v[28:31]\n\t"
  "s_cbranch_vccnz Lfl3_slow%=\n\t"
  "Lfl3_back%=:\n\t"
  "v_mfma_f32_16x16x32_bf16 v[32:35], v[216:219], v[252:255], v[32:35]\n\t"
  "s_add_u32 s45, %[sH2ld], 3244032\n\t"
  "v_mfma_f32_16x16x32_bf16 v[36:39], v[220:223], v[252:255], v[36:39]\n\t"
  "s_waitcnt vmcnt(7)\n\t"
  "v_mfma_f32_16x16x32_bf16 v[20:23], v[224:227], v[112:115], v[20:23]\n\t"
  "buffer_load_dwordx4 a[104:107], v1, %[srd], s45 offen offset:0 sc1\n\t"
  "v_mfma_f32_16x16x32_bf16 v[24:27], v[228:231], v[112:115], v[24:27]\n\t"
  "v_mfma_f32_16x16x32_bf16 v[28:31], v[232:235], v[112:115], v[28:31]\n\t"
  "buffer_load_dwordx4 a[108:111], v1, %[srd], s45 offen offset:1024 sc1\n\t"
  "v_mfma_f32_16x16x32_bf16 v[32:35], v[236:239], v[112:115], v[32:35]\n\t"
  "v_mfma_f32_16x16x32_bf16 v[36:39], v[240:243], v[112:115], v[36:39]\n\t"
  "s_waitcnt lgkmcnt(0)\n\t"
  "s_barrier\n\t"
  "v_mfma_f32_16x16x32_bf16 v[20:23], v[244:247], v[116:119], v[20:23]\n\t"
  "buffer_load_dwordx4 a[64:67], v1, %[srd], s45 offen offset:2048 sc1\n\t"
  "ds_read_b128 v[68:71], v14 offset:5120\n\t"
  "v_mfma_f32_16x16x32_bf16 v[24:27], a[160:163], v[116:119], v[24:27]\n\t"
  "ds_read_b128 v[72:75], v15 offset:5120\n\t"
  "ds_read_b128 v[76:79], v14 offset:25600\n\t"
  "v_mfma_f32_16x16x32_bf16 v[28:31], a[164:167], v[116:119], v[28:31]\n\t"
  "buffer_load_dwordx4 a[68:71], v1, %[srd], s45 offen offset:3072 sc1\n\t"
  "ds_read_b128 v[80:83], v15 offset:25600\n\t"
  "v_mfma_f32_16x16x32_bf16 v[32:35], a[168:171], v[116:119], v[32:35]\n\t"
  "ds_read_b128 v[84:87], v14 offset:46080\n\t"
  "ds_read_b128 v[88:91], v15 offset:46080\n\t"
  "v_mfma_f32_16x16x32_bf16 v[36:39], a[172:175], v[116:119], v[36:39]\n\t"
  "s_waitcnt vmcnt(9)\n\t"
  "ds_read_b128 v[92:95], v60 offset:5120\n\t"
  "v_mfma_f32_16x16x32_bf16 v[20:23], a[176:179], v[120:123], v[20:23]\n\t"
  "s_add_u32 s45, %[sH2ld], 3248128\n\t"
  "ds_read_b128 v[96:99], v61 offset:5120\n\t"
  "v_mfma_f32_16x16x32_bf16 v[24:27], a[180:183], v[120:123], v[24:27]\n\t"
  "ds_read_b128 v[100:103], v60 offset:25600\n\t"
  "ds_read_b128 v[104:107], v61 offset:25600\n\t"
  "v_mfma_f32_16x16x32_bf16 v[28:31], a[184:187], v[120:123], v[28:31]\n\t"
  "buffer_load_dwordx4 a[72:75], v1, %[srd], s45 offen offset:0 sc1\n\t"
  "v_mfma_f32_16x16x32_bf16 v[32:35], a[188:191], v[120:123], v[32:35]\n\t"
  "v_mfma_f32_16x16x32_bf16 v[36:39], a[192:195], v[120:123], v[36:39]\n\t"
  "buffer_load_dwordx4 a[76:79], v1, %[srd], s45 offen offset:1024 sc1\n\t"
  "v_mfma_f32_16x16x32_bf16 v[20:23], a[196:199], v[124:127], v[20:23]\n\t"
  "v_mfma_f32_16x16x32_bf16 v[24:27], a[200:203], v[124:127], v[24:27]\n\t"
  "v_mfma_f32_16x16x32_bf16 v[28:31], a[204:207], v[124:127], v[28:31]\n\t"
  "buffer_load_dwordx4 a[80:83], v1, %[srd], s45 offen offset:2048 sc1\n\t"
  "v_mfma_f32_16x16x32_bf16 v[32:35], a[208:211], v[124:127], v[32:35]\n\t"
  "v_mfma_f32_16x16x32_bf16 v[36:39], a[212:215], v[124:127], v[36:39]\n\t"
  "s_waitcnt vmcnt(10)\n\t"
  "buffer_load_dwordx4 a[84:87], v1, %[srd], s45 offen offset:3072 sc1\n\t"
  "v_mfma_f32_16x16x32_bf16 v[20:23], a[216:219], v[128:131], v[20:23]\n\t"
  "s_waitcnt lgkmcnt(0)\n\t"
  "v_add_f32 v68, v68, v76\n\t"
  "v_mfma_f32_16x16x32_bf16 v[24:27], a[220:223], v[128:131], v[24:27]\n\t"
  "v_add_f32 v69, v69, v77\n\t"
  "v_add_f32 v70, v70, v78\n\t"
  "v_mfma_f32_16x16x32_bf16 v[28:31], a[224:227], v[128:131], v[28:31]\n\t"
  "s_add_u32 s45, %[sH2ld], 3252224\n\t"
  "v_add_f32 v71, v71, v79\n\t"
  "v_mfma_f32_16x16x32_bf16 v[32:35], a[228:231], v[128:131], v[32:35]\n\t"
  "v_add_f32 v72, v72, v80\n\t"
  "v_add_f32 v73, v73, v81\n\t"
  "v_mfma_f32_16x16x32_bf16 v[36:39], a[232:235], v[128:131], v[36:39]\n\t"
  "buffer_load_dwordx4 a[88:91], v1, %[srd], s45 offen offset:0 sc1\n\t"
  "v_add_f32 v74, v74, v82\n\t"
  "v_mfma_f32_16x16x32_bf16 v[20:23], a[236:239], a[240:243], v[20:23]\n\t"
  "v_add_f32 v75, v75, v83\n\t"
  "v_add_f32 v84, v84, v92\n\t"
  "v_mfma_f32_16x16x32_bf16 v[24:27], a[0:3], a[240:243], v[24:27]\n\t"
  "buffer_load_dwordx4 a[92:95], v1, %[srd], s45 offen offset:1024 sc1\n\t"
  "v_add_f32 v85, v85, v93\n\t"
  "v_mfma_f32_16x16x32_bf16 v[28:31], a[4:7], a[240:243], v[28:31]\n\t"
  "v_add_f32 v86, v86, v94\n\t"
  "v_add_f32 v87, v87, v95\n\t"
  "v_mfma_f32_16x16x32_bf16 v[32:35], a[8:11], a[240:243], v[32:35]\n\t"
  "v_add_f32 v88, v88, v96\n\t"
  "v_add_f32 v89, v89, v97\n\t"
  "v_mfma_f32_16x16x32_bf16 v[36:39], a[12:15], a[240:243], v[36:39]\n\t"
  "s_waitcnt vmcnt(11)\n\t"
  "buffer_load_dwordx4 a[96:99], v1, %[srd], s45 offen offset:2048 sc1\n\t"
  "v_mfma_f32_16x16x32_bf16 v[20:23], a[16:19], a[56:59], v[20:23]\n\t"
  "v_add_f32 v90, v90, v98\n\t"
  "v_add_f32 v91, v91, v99\n\t"
  "v_mfma_f32_16x16x32_bf16 v[24:27], a[20:23], a[56:59], v[24:27]\n\t"
  "buffer_load_dwordx4 a[100:103], v1, %[srd], s45 offen offset:3072 sc1\n\t"
  "v_add_f32 v68, v68, v84\n\t"
  "v_mfma_f32_16x16x32_bf16 v[28:31], a[24:27], a[56:59], v[28:31]\n\t"
  "v_add_f32 v69, v69, v85\n\t"
  "v_add_f32 v70, v70, v86\n\t"
  "v_mfma_f32_16x16x32_bf16 v[32:35], a[28:31], a[56:59], v[32:35]\n\t"
  "v_add_f32 v71, v71, v87\n\t"
  "v_add_f32 v72, v72, v88\n\t"
  "v_mfma_f32_16x16x32_bf16 v[36:39], a[32:35], a[56:59], v[36:39]\n\t"
  "v_add_f32 v73, v73, v89\n\t"
  "v_add_f32 v74, v74, v90\n\t"
  "v_mfma_f32_16x16x32_bf16 v[20:23], a[36:39], a[60:63], v[20:23]\n\t"
  "v_add_f32 v75, v75, v91\n\t"
  "v_fma_f32 v68, v68, v64, v100\n\t"
  "v_mfma_f32_16x16x32_bf16 v[24:27], a[40:43], a[60:63], v[24:27]\n\t"
  "v_fma_f32 v69, v69, v65, v101\n\t"
  "v_fma_f32 v70, v70, v66, v102\n\t"
  "v_mfma_f32_16x16x32_bf16 v[28:31], a[44:47], a[60:63], v[28:31]\n\t"
  "v_fma_f32 v71, v71, v67, v103\n\t"
  "v_fma_f32 v72, v72, v108, v104\n\t"
  "v_mfma_f32_16x16x32_bf16 v[32:35], a[48:51], a[60:63], v[32:35]\n\t"
  "v_fma_f32 v73, v73, v109, v105\n\t"
  "v_fma_f32 v74, v74, v110, v106\n\t"
  "v_mfma_f32_16x16x32_bf16 v[36:39], a[52:55], a[60:63], v[36:39]\n\t"
  "s_waitcnt vmcnt(10)\n\t"
  "v_fma_f32 v75, v75, v111, v107\n\t"
  "v_mfma_f32_16x16x32_bf16 v[40:43], v[144:147], a[104:107], 0\n\t"
  "v_cvt_pk_bf16_f32 v76, v68, v69\n\t"
  "v_cvt_pk_bf16_f32 v77, v70, v71\n\t"
  "v_mfma_f32_16x16x32_bf16 v[44:47], v[148:151], a[104:107], 0\n\t"
  "v_cvt_pk_bf16_f32 v78, v72, v73\n\t"
  "v_cvt_pk_bf16_f32 v79, v74, v75\n\t"
  "v_mfma_f32_16x16x32_bf16 v[48:51], v[152:155], a[104:107], 0\n\t"
  "s_mov_b32 s55, 40960\n\t"
  "buffer_store_dwordx4 v[76:79], v16, %[srdo], s55 offen sc1\n\t"
  "v_mfma_f32_16x16x32_bf16 v[52:55], v[156:159], a[104:107], 0\n\t"
  "ds_write_b128 v13, v[20:23] offset:10240\n\t"
  "v_mfma_f32_16x16x32_bf16 v[56:59], v[160:163], a[104:107], 0\n\t"
  "v_mfma_f32_16x16x32_bf16 v[40:43], v[164:167], a[108:111], v[40:43]\n\t"
  "v_mfma_f32_16x16x32_bf16 v[44:47], v[168:171], a[108:111], v[44:47]\n\t"
  "ds_write_b128 v13, v[24:27] offset:11264\n\t"
  "v_mfma_f32_16x16x32_bf16 v[48:51], v[172:175], a[108:111], v[48:51]\n\t"
  "v_mfma_f32_16x16x32_bf16 v[52:55], v[176:179], a[108:111], v[52:55]\n\t"
  "v_mfma_f32_16x16x32_bf16 v[56:59], v[180:183], a[108:111], v[56:59]\n\t"
  "s_waitcnt vmcnt(9)\n\t"
  "ds_write_b128 v13, v[28:31] offset:12288\n\t"
  "v_mfma_f32_16x16x32_bf16 v[40:43], v[184:187], a[64:67], v[40:43]\n\t"
  "v_mfma_f32_16x16x32_bf16 v[44:47], v[188:191], a[64:67], v[44:47]\n\t"
  "v_mfma_f32_16x16x32_bf16 v[48:51], v[192:195], a[64:67], v[48:51]\n\t"
  "ds_write_b128 v13, v[32:35] offset:13312\n\t"
  "v_mfma_f32_16x16x32_bf16 v[52:55], v[196:199], a[64:67], v[52:55]\n\t"
  "v_mfma_f32_16x16x32_bf16 v[56:59], v[200:203], a[64:67], v[56:59]\n\t"
  "v_mfma_f32_16x16x32_bf16 v[40:43], v[204:207], a[68:71], v[40:43]\n\t"
  "ds_write_b128 v13, v[36:39] offset:14336\n\t"
  "v_mfma_f32_16x16x32_bf16 v[44:47], v[208:211], a[68:71], v[44:47]\n\t"
  "v_mfma_f32_16x16x32_bf16 v[48:51], v[212:215], a[68:71], v[48:51]\n\t"
  "v_mfma_f32_16x16x32_bf16 v[52:55], v[216:219], a[68:71], v[52:55]\n\t"
  "v_mfma_f32_16x16x32_bf16 v[56:59], v[220:223], a[68:71], v[56:59]\n\t"
  "s_waitcnt vmcnt(7)\n\t"
  "v_mfma_f32_16x16x32_bf16 v[40:43], v[224:227], a[72:75], v[40:43]\n\t"
  "v_mfma_f32_16x16x32_bf16 v[44:47], v[228:231], a[72:75], v[44:47]\n\t"
  "v_mfma_f32_16x16x32_bf16 v[48:51], v[232:235], a[72:75], v[48:51]\n\t"
  "v_mfma_f32_16x16x32_bf16 v[52:55], v[236:239], a[72:75], v[52:55]\n\t"
  "s_waitcnt lgkmcnt(0)\n\t"
  "s_barrier\n\t"
  "v_mfma_f32_16x16x32_bf16 v[56:59], v[240:243], a[72:75], v[56:59]\n\t"
  "ds_read_b128 v[68:71], v14 offset:10240\n\t"
  "ds_read_b128 v[72:75], v15 offset:10240\n\t"
  "v_mfma_f32_16x16x32_bf16 v[40:43], v[244:247], a[76:79], v[40:43]\n\t"
  "ds_read_b128 v[76:79], v14 offset:30720\n\t"
  "ds_read_b128 v[80:83], v15 offset:30720\n\t"
  "v_mfma_f32_16x16x32_bf16 v[44:47], a[160:163], a[76:79], v[44:47]\n\t"
  "ds_read_b128 v[84:87], v14 offset:51200\n\t"
  "ds_read_b128 v[88:91], v15 offset:51200\n\t"
  "v_mfma_f32_16x16x32_bf16 v[48:51], a[164:167], a[76:79], v[48:51]\n\t"
  "ds_read_b128 v[92:95], v60 offset:10240\n\t"
  "ds_read_b128 v[96:99], v61 offset:10240\n\t"
  "v_mfma_f32_16x16x32_bf16 v[52:55], a[168:171], a[76:79], v[52:55]\n\t"
  "ds_read_b128 v[100:103], v60 offset:30720\n\t"
  "ds_read_b128 v[104:107], v61 offset:30720\n\t"
  "v_mfma_f32_16x16x32_bf16 v[56:59], a[172:175], a[76:79], v[56:59]\n\t"
  "s_waitcnt vmcnt(5)\n\t"
  "v_mfma_f32_16x16x32_bf16 v[40:43], a[176:179], a[80:83], v[40:43]\n\t"
  "v_mfma_f32_16x16x32_bf16 v[44:47], a[180:183], a[80:83], v[44:47]\n\t"
  "v_mfma_f32_16x16x32_bf16 v[48:51], a[184:187], a[80:83], v[48:51]\n\t"
  "v_mfma_f32_16x16x32_bf16 v[52:55], a[188:191], a[80:83], v[52:55]\n\t"
  "v_mfma_f32_16x16x32_bf16 v[56:59], a[192:195], a[80:83], v[56:59]\n\t"
  "v_mfma_f32_16x16x32_bf16 v[40:43], a[196:199], a[84:87], v[40:43]\n\t"
  "v_mfma_f32_16x16x32_bf16 v[44:47], a[200:203], a[84:87], v[44:47]\n\t"
  "s_waitcnt lgkmcnt(0)\n\t"
  "v_add_f32 v68, v68, v76\n\t"
  "v_mfma_f32_16x16x32_bf16 v[48:51], a[204:207], a[84:87], v[48:51]\n\t"
  "v_add_f32 v69, v69, v77\n\t"
  "v_add_f32 v70, v70, v78\n\t"
  "v_mfma_f32_16x16x32_bf16 v[52:55], a[208:211], a[84:87], v[52:55]\n\t"
  "v_add_f32 v71, v71, v79\n\t"
  "v_add_f32 v72, v72, v80\n\t"
  "v_mfma_f32_16x16x32_bf16 v[56:59], a[212:215], a[84:87], v[56:59]\n\t"
  "s_waitcnt vmcnt(3)\n\t"
  "v_add_f32 v73, v73, v81\n\t"
  "v_mfma_f32_16x16x32_bf16 v[40:43], a[216:219], a[88:91], v[40:43]\n\t"
  "v_add_f32 v74, v74, v82\n\t"
  "v_add_f32 v75, v75, v83\n\t"
  "v_mfma_f32_16x16x32_bf16 v[40:43], a[236:239], a[92:95], v[40:43]\n\t"
  "v_add_f32 v84, v84, v92\n\t"
  "v_add_f32 v85, v85, v93\n\t"
  "s_waitcnt vmcnt(2)\n\t"
  "v_mfma_f32_16x16x32_bf16 v[40:43], a[16:19], a[96:99], v[40:43]\n\t"
  "v_add_f32 v86, v86, v94\n\t"
  "v_add_f32 v87, v87, v95\n\t"
  "s_waitcnt vmcnt(1)\n\t"
  "v_mfma_f32_16x16x32_bf16 v[40:43], a[36:39], a[100:103], v[40:43]\n\t"
  "v_add_f32 v88, v88, v96\n\t"
  "v_add_f32 v89, v89, v97\n\t"
  "v_mfma_f32_16x16x32_bf16 v[44:47], a[220:223], a[88:91], v[44:47]\n\t"
  "v_add_f32 v90, v90, v98\n\t"
  "v_add_f32 v91, v91, v99\n\t"
  "v_mfma_f32_16x16x32_bf16 v[44:47], a[0:3], a[92:95], v[44:47]\n\t"
  "v_add_f32 v68, v68, v84\n\t"
  "v_add_f32 v69, v69, v85\n\t"
  "v_mfma_f32_16x16x32_bf16 v[44:47], a[20:23], a[96:99], v[44:47]\n\t"
  "v_add_f32 v70, v70, v86\n\t"
  "v_add_f32 v71, v71, v87\n\t"
  "v_mfma_f32_16x16x32_bf16 v[44:47], a[40:43], a[100:103], v[44:47]\n\t"
  "v_add_f32 v72, v72, v88\n\t"
  "v_add_f32 v73, v73, v89\n\t"
  "v_mfma_f32_16x16x32_bf16 v[48:51], a[224:227], a[88:91], v[48:51]\n\t"
  "v_add_f32 v74, v74, v90\n\t"
  "v_add_f32 v75, v75, v91\n\t"
  "v_mfma_f32_16x16x32_bf16 v[48:51], a[4:7], a[92:95], v[48:51]\n\t"
  "ds_write_b128 v13, v[40:43] offset:15360\n\t"
  "v_fma_f32 v68, v68, v64, v100\n\t"
  "v_mfma_f32_16x16x32_bf16 v[48:51], a[24:27], a[96:99], v[48:51]\n\t"
  "v_fma_f32 v69, v69, v65, v101\n\t"
  "v_fma_f32 v70, v70, v66, v102\n\t"
  "v_mfma_f32_16x16x32_bf16 v[48:51], a[44:47], a[100:103], v[48:51]\n\t"
  "v_fma_f32 v71, v71, v67, v103\n\t"
  "v_fma_f32 v72, v72, v108, v104\n\t"
  "v_mfma_f32_16x16x32_bf16 v[52:55], a[228:231], a[88:91], v[52:55]\n\t"
  "v_fma_f32 v73, v73, v109, v105\n\t"
  "v_fma_f32 v74, v74, v110, v106\n\t"
  "v_mfma_f32_16x16x32_bf16 v[52:55], a[8:11], a[92:95], v[52:55]\n\t"
  "ds_write_b128 v13, v[44:47] offset:16384\n\t"
  "v_fma_f32 v75, v75, v111, v107\n\t"
  "v_mfma_f32_16x16x32_bf16 v[52:55], a[28:31], a[96:99], v[52:55]\n\t"
  "v_cvt_pk_bf16_f32 v76, v68, v69\n\t"
  "v_cvt_pk_bf16_f32 v77, v70, v71\n\t"
  "v_mfma_f32_16x16x32_bf16 v[52:55], a[48:51], a[100:103], v[52:55]\n\t"
  "v_cvt_pk_bf16_f32 v78, v72, v73\n\t"
  "v_cvt_pk_bf16_f32 v79, v74, v75\n\t"
  "v_mfma_f32_16x16x32_bf16 v[56:59], a[232:235], a[88:91], v[56:59]\n\t"
  "s_mov_b32 s55, 1310720\n\t"
  "buffer_store_dwordx4 v[76:79], v16, %[srdo], s55 offen sc1\n\t"
  "v_mfma_f32_16x16x32_bf16 v[56:59], a[12:15], a[92:95], v[56:59]\n\t"
  "ds_write_b128 v13, v[48:51] offset:17408\n\t"
  "v_mfma_f32_16x16x32_bf16 v[56:59], a[32:35], a[96:99], v[56:59]\n\t"
  "v_mfma_f32_16x16x32_bf16 v[56:59], a[52:55], a[100:103], v[56:59]\n\t"
  "ds_write_b128 v13, v[52:55] offset:18432\n\t"
  "s_nop 6\n\t"
  "ds_write_b128 v13, v[56:59] offset:19456\n\t"
  "buffer_store_dwordx2 v[132:133], v18, %[srd], 0 offen sc1\n\t"
  "s_waitcnt lgkmcnt(0)\n\t"
  "s_barrier\n\t"
  "ds_read_b128 v[68:71], v14 offset:15360\n\t"
  "ds_read_b128 v[72:75], v15 offset:15360\n\t"
  "ds_read_b128 v[76:79], v14 offset:35840\n\t"
  "ds_read_b128 v[80:83], v15 offset:35840\n\t"
  "ds_read_b128 v[84:87], v14 offset:56320\n\t"
  "ds_read_b128 v[88:91], v15 offset:56320\n\t"
  "ds_read_b128 v[92:95], v60 offset:15360\n\t"
  "ds_read_b128 v[96:99], v61 offset:15360\n\t"
  "ds_read_b128 v[100:103], v60 offset:35840\n\t"
  "ds_read_b128 v[104:107], v61 offset:35840\n\t"
  "s_waitcnt lgkmcnt(7)\n\t"
  "v_pk_add_f32 v[68:69], v[68:69], v[76:77]\n\t"
  "v_pk_add_f32 v[70:71], v[70:71], v[78:79]\n\t"
  "s_waitcnt lgkmcnt(6)\n\t"
  "v_pk_add_f32 v[72:73], v[72:73], v[80:81]\n\t"
  "v_pk_add_f32 v[74:75], v[74:75], v[82:83]\n\t"
  "s_waitcnt lgkmcnt(3)\n\t"
  "v_pk_add_f32 v[84:85], v[84:85], v[92:93]\n\t"
  "v_pk_add_f32 v[86:87], v[86:87], v[94:95]\n\t"
  "s_waitcnt lgkmcnt(2)\n\t"
  "v_pk_add_f32 v[88:89], v[88:89], v[96:97]\n\t"
  "v_pk_add_f32 v[90:91], v[90:91], v[98:99]\n\t"
  "v_pk_add_f32 v[68:69], v[68:69], v[84:85]\n\t"
  "v_pk_add_f32 v[70:71], v[70:71], v[86:87]\n\t"
  "v_pk_add_f32 v[72:73], v[72:73], v[88:89]\n\t"
  "v_pk_add_f32 v[74:75], v[74:75], v[90:91]\n\t"
  "s_waitcnt lgkmcnt(1)\n\t"
  "v_pk_fma_f32 v[68:69], v[68:69], v[64:65], v[100:101]\n\t"
  "v_pk_fma_f32 v[70:71], v[70:71], v[66:67], v[102:103]\n\t"
  "s_waitcnt lgkmcnt(0)\n\t"
  "v_pk_fma_f32 v[72:73], v[72:73], v[108:109], v[104:105]\n\t"
  "v_pk_fma_f32 v[74:75], v[74:75], v[110:111], v[106:107]\n\t"
  "v_cvt_pk_bf16_f32 v76, v68, v69\n\t"
  "v_cvt_pk_bf16_f32 v77, v70, v71\n\t"
  "v_cvt_pk_bf16_f32 v78, v72, v73\n\t"
  "v_cvt_pk_bf16_f32 v79, v74, v75\n\t"
  "s_mov_b32 s55, 1351680\n\t"
  "buffer_store_dwordx4 v[76:79], v16, %[srdo], s55 offen sc1\n\t"
  "s_branch Lfend%=\n\t"
  "Lst0_slow%=:\n\t"
  "s_nop 7\n\t"
  "s_nop 3\n\t"
  "s_mov_b32 s49, 0x40000\n\t"
  "Lst0_loop%=:\n\t"
  "s_sleep 2\n\t"
  "buffer_load_dwordx2 v[92:93], v136, %[srd], %[sST] offen offset:0 sc1\n\t"
  "buffer_load_dwordx2 v[94:95], v136, %[srd], %[sST] offen offset:1024 sc1\n\t"
  "buffer_load_dwordx2 v[96:97], v136, %[srd], %[sST] offen offset:2048 sc1\n\t"
  "buffer_load_dwordx2 v[98:99], v136, %[srd], %[sST] offen offset:3072 sc1\n\t"
  "s_waitcnt vmcnt(0)\n\t"
  "v_min_u32 v131, v93, v95\n\t"
  "v_min3_u32 v131, v131, v97, v99\n\t"
  "v_cmp_eq_u32 vcc, 0, v131\n\t"
  "s_nop 3\n\t"
  "s_cmp_eq_u64 vcc, 0\n\t"
  "s_cbranch_scc1 Lst0_back%=\n\t"
  "s_sub_u32 s49, s49, 1\n\t"
  "s_cmp_lg_u32 s49, 0\n\t"
  "s_cbranch_scc1 Lst0_loop%=\n\t"
  "s_branch Lst0_back%=\n\t"
  "Lw2_slow%=:\n\t"
  "s_nop 7\n\t"
  "s_nop 3\n\t"
  "s_mov_b32 s49, 0x40000\n\t"
  "Lw2_loop%=:\n\t"
  "s_sleep 2\n\t"
  "buffer_load_dwordx2 v[138:139], v2, %[srd], 0 offen sc1\n\t"
  "s_waitcnt vmcnt(0)\n\t"
  "v_cmp_ne_u64 vcc, v[132:133], v[138:139]\n\t"
  "s_nop 3\n\t"
  "s_cmp_eq_u64 vcc, 0\n\t"
  "s_cbranch_scc1 Lw2_back%=\n\t"
  "s_sub_u32 s49, s49, 1\n\t"
  "s_cmp_lg_u32 s49, 0\n\t"
  "s_cbranch_scc1 Lw2_loop%=\n\t"
  "s_branch Lw2_back%=\n\t"
  "Lst1_slow%=:\n\t"
  "s_nop 7\n\t"
  "s_nop 3\n\t"
  "s_mov_b32 s49, 0x40000\n\t"
  "Lst1_loop%=:\n\t"
  "s_sleep 2\n\t"
  "buffer_load_dwordx2 v[100:101], v136, %[srd], s54 offen offset:0 sc1\n\t"
  "buffer_load_dwordx2 v[102:103], v136, %[srd], s54 offen offset:1024 sc1\n\t"
  "buffer_load_dwordx2 v[104:105], v136, %[srd], s54 offen offset:2048 sc1\n\t"
  "buffer_load_dwordx2 v[106:107], v136, %[srd], s54 offen offset:3072 sc1\n\t"
  "s_waitcnt vmcnt(0)\n\t"
  "v_min_u32 v131, v101, v103\n\t"
  "v_min3_u32 v131, v131, v105, v107\n\t"
  "v_cmp_eq_u32 vcc, 0, v131\n\t"
  "s_nop 3\n\t"
  "s_cmp_eq_u64 vcc, 0\n\t"
  "s_cbranch_scc1 Lst1_back%=\n\t"
  "s_sub_u32 s49, s49, 1\n\t"
  "s_cmp_lg_u32 s49, 0\n\t"
  "s_cbranch_scc1 Lst1_loop%=\n\t"
  "s_branch Lst1_back%=\n\t"
  "Lfl0_slow%=:\n\t"
  "s_nop 7\n\t"
  "s_nop 3\n\t"
  "s_mov_b32 s49, 0x40000\n\t"
  "Lfl0_loop%=:\n\t"
  "s_sleep 2\n\t"
  "s_add_u32 s57, %[sFL], 0\n\t"
  "buffer_load_dwordx2 v[138:139], v137, %[srd], s57 offen sc1\n\t"
  "s_waitcnt vmcnt(0)\n\t"
  "v_cmp_ne_u64 vcc, v[132:133], v[138:139]\n\t"
  "s_nop 3\n\t"
  "s_cmp_eq_u64 vcc, 0\n\t"
  "s_cbranch_scc1 Lfl0_back%=\n\t"
  "s_sub_u32 s49, s49, 1\n\t"
  "s_cmp_lg_u32 s49, 0\n\t"
  "s_cbranch_scc1 Lfl0_loop%=\n\t"
  "s_branch Lfl0_back%=\n\t"
  "Lfl1_slow%=:\n\t"
  "s_nop 7\n\t"
  "s_nop 3\n\t"
  "s_mov_b32 s49, 0x40000\n\t"
  "Lfl1_loop%=:\n\t"
  "s_sleep 2\n\t"
  "s_add_u32 s57, %[sFL], 8192\n\t"
  "buffer_load_dwordx2 v[138:139], v137, %[srd], s57 offen sc1\n\t"
  "s_waitcnt vmcnt(0)\n\t"
  "v_cmp_ne_u64 vcc, v[132:133], v[138:139]\n\t"
  "s_nop 3\n\t"
  "s_cmp_eq_u64 vcc, 0\n\t"
  "s_cbranch_scc1 Lfl1_back%=\n\t"
  "s_sub_u32 s49, s49, 1\n\t"
  "s_cmp_lg_u32 s49, 0\n\t"
  "s_cbranch_scc1 Lfl1_loop%=\n\t"
  "s_branch Lfl1_back%=\n\t"
  "Lfl2_slow%=:\n\t"
  "s_nop 7\n\t"
  "s_nop 3\n\t"
  "s_mov_b32 s49, 0x40000\n\t"
  "Lfl2_loop%=:\n\t"
  "s_sleep 2\n\t"
  "s_add_u32 s57, %[sFL], 16384\n\t"
  "buffer_load_dwordx2 v[138:139], v137, %[srd], s57 offen sc1\n\t"
  "s_waitcnt vmcnt(0)\n\t"
  "v_cmp_ne_u64 vcc, v[132:133], v[138:139]\n\t"
  "s_nop 3\n\t"
  "s_cmp_eq_u64 vcc, 0\n\t"
  "s_cbranch_scc1 Lfl2_back%=\n\t"
  "s_sub_u32 s49, s49, 1\n\t"
  "s_cmp_lg_u32 s49, 0\n\t"
  "s_cbranch_scc1 Lfl2_loop%=\n\t"
  "s_branch Lfl2_back%=\n\t"
  "Lfl3_slow%=:\n\t"
  "s_nop 7\n\t"
  "s_nop 3\n\t"
  "s_mov_b32 s49, 0x40000\n\t"
  "Lfl3_loop%=:\n\t"
  "s_sleep 2\n\t"
  "s_add_u32 s57, %[sFL], 24576\n\t"
  "buffer_load_dwordx2 v[138:139], v137, %[srd], s57 offen sc1\n\t"
  "s_waitcnt vmcnt(0)\n\t"
  "v_cmp_ne_u64 vcc, v[132:133], v[138:139]\n\t"
  "s_nop 3\n\t"
  "s_cmp_eq_u64 vcc, 0\n\t"
  "s_cbranch_scc1 Lfl3_back%=\n\t"
  "s_sub_u32 s49, s49, 1\n\t"
  "s_cmp_lg_u32 s49, 0\n\t"
  "s_cbranch_scc1 Lfl3_loop%=\n\t"
  "s_branch Lfl3_back%=\n\t"
  "Lfend%=:\n\t"
  ".Lk_end:\n\t"
    :
    : [srd] "s"(srd), [srdo] "s"(srdo), [w] "s"(w),
      [tlo] "s"((u32)tag), [thi] "s"((u32)(tag >> 32)), [sW1] "s"(OFF_W1F + (j * 4 + w) * 61440u), [sW2] "s"(OFF_W2F + (ng * 4 + w) * 61440u),
      [sUB] "s"(OFF_UB + (j * 4 + w) * 1536u),
      [sH2ld] "s"(OFF_H2F + ((4 * p + sub) * 48 + 12 * w) * 1024u), [sST] "s"(OFF_ST2 + p * 4096u), [sFL] "s"(OFF_FL + p * 32768u + w * 2048u),
      [sRT] "s"(OFF_RT + ng * 320u), [wdone] "s"(wdone)
    : "memory", "vcc", "scc",
"a0", "a1", "a2", "a3", "a4", "a5", "a6", "a7", "a8", "a9", "a10", "a11", "a12", "a13", "a14", "a15", "a16", "a17", "a18", "a19", "a20", "a21", "a22", "a23", "a24", "a25", "a26", "a27", "a28", "a29", "a30", "a31", "a32", "a33", "a34", "a35", "a36", "a37", "a38", "a39", "a40", "a41", "a42", "a43", "a44", "a45", "a46", "a47", "a48", "a49", "a50", "a51", "a52", "a53", "a54", "a55", "a56", "a57", "a58", "a59", "a60", "a61", "a62", "a63", "a64", "a65", "a66", "a67", "a68", "a69", "a70", "a71", "a72", "a73", "a74", "a75", "a76", "a77", "a78", "a79", "a80", "a81", "a82", "a83", "a84", "a85", "a86", "a87", "a88", "a89", "a90", "a91", "a92", "a93", "a94", "a95", "a96", "a97", "a98", "a99", "a100", "a101", "a102", "a103", "a104", "a105", "a106", "a107", "a108", "a109", "a110", "a111", "a112", "a113", "a114", "a115", "a116", "a117", "a118", "a119", "a120", "a121", "a122", "a123", "a124", "a125", "a126", "a127", "a128", "a129", "a130", "a131", "a132", "a133", "a134", "a135", "a136", "a137", "a138", "a139", "a140", "a141", "a142", "a143", "a144", "a145", "a146", "a147", "a148", "a149", "a150", "a151", "a152", "a153", "a154", "a155", "a156", "a157", "a158", "a159", "a160", "a161", "a162", "a163", "a164", "a165", "a166", "a167", "a168", "a169", "a170", "a171", "a172", "a173", "a174", "a175", "a176", "a177", "a178", "a179", "a180", "a181", "a182", "a183", "a184", "a185", "a186", "a187", "a188", "a189", "a190", "a191", "a192", "a193", "a194", "a195", "a196", "a197", "a198", "a199", "a200", "a201", "a202", "a203", "a204", "a205", "a206", "a207", "a208", "a209", "a210", "a211", "a212", "a213", "a214", "a215", "a216", "a217", "a218", "a219", "a220", "a221", "a222", "a223", "a224", "a225", "a226", "a227", "a228", "a229", "a230", "a231", "a232", "a233", "a234", "a235", "a236", "a237", "a238", "a239", "a240", "a241", "a242", "a243", "a244", "a245", "a246", "a247", "a248", "a249", "a250", "a251", "a252", "a253", "a254", "a255", "s44", "s45", "s46", "s47", "s48", "s49", "s52", "s54", "s55", "s56", "s57", "v0", "v1", "v2", "v3", "v4", "v5", "v6", "v7", "v8", "v9", "v10", "v11", "v12", "v13", "v14", "v15", "v16", "v17", "v18", "v19", "v20", "v21", "v22", "v23", "v24", "v25", "v26", "v27", "v28", "v29", "v30", "v31", "v32", "v33", "v34", "v35", "v36", "v37", "v38", "v39", "v40", "v41", "v42", "v43", "v44", "v45", "v46", "v47", "v48", "v49", "v50", "v51", "v52", "v53", "v54", "v55", "v56", "v57", "v58", "v59", "v60", "v61", "v62", "v63", "v64", "v65", "v66", "v67", "v68", "v69", "v70", "v71", "v72", "v73", "v74", "v75", "v76", "v77", "v78", "v79", "v80", "v81", "v82", "v83", "v84", "v85", "v86", "v87", "v88", "v89", "v90", "v91", "v92", "v93", "v94", "v95", "v96", "v97", "v98", "v99", "v100", "v101", "v102", "v103", "v104", "v105", "v106", "v107", "v108", "v109", "v110", "v111", "v112", "v113", "v114", "v115", "v116", "v117", "v118", "v119", "v120", "v121", "v122", "v123", "v124", "v125", "v126", "v127", "v128", "v129", "v130", "v131", "v132", "v133", "v134", "v135", "v136", "v137", "v138", "v139", "v141", "v142", "v143", "v144", "v145", "v146", "v147", "v148", "v149", "v150", "v151", "v152", "v153", "v154", "v155", "v156", "v157", "v158", "v159", "v160", "v161", "v162", "v163", "v164", "v165", "v166", "v167", "v168", "v169", "v170", "v171", "v172", "v173", "v174", "v175", "v176", "v177", "v178", "v179", "v180", "v181", "v182", "v183", "v184", "v185", "v186", "v187", "v188", "v189", "v190", "v191", "v192", "v193", "v194", "v195", "v196", "v197", "v198", "v199", "v200", "v201", "v202", "v203", "v204", "v205", "v206", "v207", "v208", "v209", "v210", "v211", "v212", "v213", "v214", "v215", "v216", "v217", "v218", "v219", "v220", "v221", "v222", "v223", "v224", "v225", "v226", "v227", "v228", "v229", "v230", "v231", "v232", "v233", "v234", "v235", "v236", "v237", "v238", "v239", "v240", "v241", "v242", "v243", "v244", "v245", "v246", "v247", "v248", "v249", "v250", "v251", "v252", "v253", "v254", "v255"
  );
}

extern "C" void kernel_launch(void* const* d_in, const int* in_sizes, int n_in, void* d_out, int out_size,
                              void* d_ws, size_t ws_size, hipStream_t stream) {
  k_main<<<256, 256, 0, stream>>>((const u16*)d_in[0], (const u16*)d_in[1], (const u16*)d_in[2], (const u16*)d_in[3], (const u16*)d_in[4],
                                  (const u16*)d_in[5], (const u16*)d_in[7], (const u16*)d_in[8], (const u16*)d_in[9], (const u16*)d_in[10],
                                  (const u16*)d_in[11], (char*)d_ws, (u16*)d_out);
}
```

```cpp
#include <hip/hip_runtime.h>
#include <stdint.h>

typedef unsigned short u16;
typedef unsigned int u32;
typedef unsigned long long u64;
typedef __attribute__((ext_vector_type(8))) __bf16 bf16x8;
typedef __attribute__((ext_vector_type(2))) __bf16 bf16x2;
typedef __attribute__((ext_vector_type(4))) short s16x4;
typedef __attribute__((ext_vector_type(4))) float f32x4;
typedef __attribute__((ext_vector_type(2))) float f32x2;
typedef __attribute__((ext_vector_type(4))) u32 u32x4;
typedef __attribute__((ext_vector_type(2))) u32 u32x2;
#define LAS __attribute__((address_space(3)))
#define GAS __attribute__((address_space(1)))
#define DI __device__ __forceinline__

#define STAMP(id)
#define STAMP_DECL
#define STAMP_OUT(kid)
#define A1_R 4
#define A1_P 8
#define A1_S 2
#define A1_S0 0
#define A1_X 0
#define DBG_W 1
#define DBG_X 1
#define DBG_M 1
#define W_DELAY 0
#define ROLEMAP 0
#define SELF_BAR 0
#define CODEW 1
#define U_DELAY 16
#define PRE_ORDER 3
#define OPS_ORDER 0
#define WP2_EARLY 1
#define KV_EXACT 0
#define FX_R 8
constexpr int S_ = 2048, D_ = 640, F_ = 1536;
constexpr unsigned OFF_EP = 0u;
constexpr unsigned OFF_FR = 2048u;
constexpr unsigned OFF_FA1 = 4096u;
constexpr unsigned OFF_FA2 = 24576u;
constexpr unsigned OFF_FA3 = 55296u;
constexpr unsigned OFF_FX = 71680u;
constexpr unsigned OFF_FW1 = 235520u;
constexpr unsigned OFF_FU = 243712u;
constexpr unsigned OFF_FW2 = 251904u;
constexpr unsigned OFF_RT = 256000u;
constexpr unsigned OFF_GB = 258560u;
constexpr unsigned OFF_RL = 261120u;
constexpr unsigned OFF_ST2 = 278528u;
constexpr unsigned OFF_FL = 409600u;
constexpr unsigned OFF_MODP = 933888u;
constexpr unsigned OFF_ST1 = 995328u;
constexpr unsigned OFF_UB = 1011712u;
constexpr unsigned OFF_KVT = 1110016u;
constexpr unsigned OFF_XT = 3731456u;
constexpr unsigned OFF_X2P = 6352896u;
constexpr unsigned OFF_H2F = 8974336u;
constexpr unsigned OFF_W1F = 15265792u;
constexpr unsigned OFF_W2F = 19197952u;
constexpr unsigned WS_END = 21164032u;
constexpr unsigned TAG_A = 2654435769u;
constexpr unsigned TAG_B = 2246822507u;
constexpr unsigned TAG_C = 2135587861u;
constexpr unsigned TAG_D = 3266489909u;
constexpr unsigned FC_PITCH = 4160u;
constexpr unsigned FC_CH = 16640u;
constexpr unsigned L_FC = 0u;
constexpr unsigned L_KV = 66560u;
constexpr unsigned L_X = 84992u;
constexpr unsigned L_ST = 101376u;
constexpr unsigned L_XR = 117760u;
constexpr unsigned L_CNT = 140288u;
constexpr unsigned L_STASH = 143360u;
constexpr unsigned L_UN = 71680u;
constexpr unsigned UN_SZ = 22528u;

constexpr unsigned OOB = 0x80000000u;
DI unsigned rfl(unsigned x) { return __builtin_amdgcn_readfirstlane(x); }
DI unsigned stkey(unsigned r) { const unsigned k = (r >> 1) & 7; return (((k ^ (k >> 1)) >> 1) & 1) | ((k & 1) << 1) | (k & 4); }
DI float bflo(u32 w) { return __uint_as_float(w << 16); }
DI float bfhi(u32 w) { return __uint_as_float(w & 0xffff0000u); }
DI u32 pack_bf16(float lo, float hi) { bf16x2 v; v[0] = (__bf16)lo; v[1] = (__bf16)hi; return __builtin_bit_cast(u32, v); }
DI float rbf(float x) { __bf16 b = (__bf16)x; return (float)b; }
DI float dot2(u32 a, u32 b, float c) { return __builtin_amdgcn_fdot2_f32_bf16(__builtin_bit_cast(bf16x2, a), __builtin_bit_cast(bf16x2, b), c, false); }
DI f32x4 mfma(u32x4 a, u32x4 b, f32x4 c) { return __builtin_amdgcn_mfma_f32_16x16x32_bf16(__builtin_bit_cast(bf16x8, a), __builtin_bit_cast(bf16x8, b), c, 0, 0, 0); }
DI void mfma_v(f32x4& c, u32x4 a, u32x4 b) { asm volatile("s_nop 1\n\tv_mfma_f32_16x16x32_bf16 %0, %1, %2, %0" : "+v"(c) : "v"(a), "v"(b)); }
DI u32x2 tr_read(const char* p) { return __builtin_bit_cast(u32x2, __builtin_amdgcn_ds_read_tr16_b64_v4i16((LAS s16x4*)p)); }
typedef __amdgpu_buffer_rsrc_t rsrc_t;
DI rsrc_t mkrsrc(const void* p, unsigned bytes) { return __builtin_amdgcn_make_buffer_rsrc((void*)p, 0, bytes, 0x00020000); }
#define BAR() asm volatile("s_waitcnt lgkmcnt(0)\n\ts_barrier" ::: "memory")
#define FENCE() do { asm volatile("" ::: "memory"); __builtin_amdgcn_sched_barrier(0); } while (0)
constexpr int SC1 = 16, NT = 2, W_IDLE_SLEEP = 8;

DI u64 load_tag(const char* ws) {
  u64 epoch;
  asm volatile("s_load_dwordx2 %0, %1, 0\n\ts_waitcnt lgkmcnt(0)" : "=&s"(epoch) : "s"(ws) : "memory");
  const u32 tl = ((u32)epoch ^ TAG_A) * TAG_B, th = ((u32)(epoch >> 32) ^ TAG_C) * TAG_D + tl;
  return ((u64)th << 32) | tl;
}
template <int N, int SL = 2>
DI void poll_flags(rsrc_t srd, unsigned voff, u64 tag) {
  const u32 lo = (u32)tag, hi = (u32)(tag >> 32);
#pragma unroll 1
  for (int spin = 0; spin < (1 << 18); ++spin) {
    u32 bad = 0;
#pragma unroll
    for (int k = 0; k < N; ++k) { const u32x2 f = __builtin_amdgcn_raw_buffer_load_b64(srd, voff + k * 512, 0, SC1); bad |= (f[0] ^ lo) | (f[1] ^ hi); }
    asm volatile("" ::: "memory");
    if (__all(bad == 0)) break;
    __builtin_amdgcn_s_sleep(SL);
  }
  FENCE();
}
DI void put_flag(rsrc_t srd, unsigned off, unsigned lane, u64 tag) {
  __builtin_amdgcn_raw_buffer_store_b128(u32x4{(u32)tag, (u32)(tag >> 32), (u32)tag, (u32)(tag >> 32)}, srd, lane < 8 ? off + lane * 16 : OOB, 0, SC1);
  FENCE();
}
DI float modval(rsrc_t srd, unsigned col) {
  float m[4];
#pragma unroll
  for (int k = 0; k < 4; ++k) m[k] = __uint_as_float(__builtin_amdgcn_raw_buffer_load_b32(srd, col * 4, OFF_MODP + k * 15360, SC1));
  return rbf((m[0] + m[1]) + (m[2] + m[3]));
}

constexpr int RP1 = 400, RP2 = 656, L_WS = 69632;

__global__ __launch_bounds__(256, 1) void k_main(const u16* __restrict__ x, const u16* __restrict__ kIn, const u16* __restrict__ vIn,
                                                 const u16* __restrict__ shk, const u16* __restrict__ ssmk, const u16* __restrict__ ssmD,
                                                 const u16* __restrict__ wg, const u16* __restrict__ wu, const u16* __restrict__ wd,
                                                 const u16* __restrict__ cIn, const u16* __restrict__ wmod, char* ws, u16* __restrict__ out) {
  __shared__ __attribute__((aligned(16))) char smem[163840];
  const unsigned b = blockIdx.x, tid = threadIdx.x, w = rfl(tid >> 6), lane = tid & 63;
  const unsigned xcd = b & 7, loc = b >> 3, pl = loc & 3, p = 4 * (xcd >> 1) + pl, j = 8 * (xcd & 1) + (loc >> 2), ng = j & 7, sub = j >> 3;
  asm volatile("" :: "s"(x), "s"(kIn), "s"(vIn), "s"(shk), "s"(ssmk), "s"(ssmD), "s"(wg), "s"(wu), "s"(wd), "s"(cIn), "s"(wmod), "s"(ws), "s"(out));
  STAMP_DECL; STAMP(0);
  const rsrc_t srd = mkrsrc(ws, WS_END);
  const rsrc_t srdo = mkrsrc(out, S_ * D_ * 2);
  const unsigned lb = (unsigned)(size_t)smem;
  const unsigned role = loc < 20 ? 0 : loc < 28 ? 1 : 2;
  const unsigned cb = xcd * 20 + loc, w1 = xcd * 8 + loc - 20, w2 = xcd * 4 + loc - 28;
  const u32x4 zero4 = {0, 0, 0, 0};
  unsigned wdone = 8, dval = 0;
  if (w == 3 && loc >= 24) {
    u64 pc; unsigned len;
    asm volatile("s_getpc_b64 %0\n.Lk_here:\n\ts_mov_b32 %1, .Lk_end-.Lk_here" : "=s"(pc), "=s"(len));
    asm volatile("buffer_load_dword a255, %0, %1, 0 offen" :: "v"((lane * 8 + loc - 24) * 128), "s"(mkrsrc((const void*)pc, len)) : "a255", "memory");
  }
#define U_KQ(u_) ((u_) & 3)
#define U_CG(u_) (((u_) >> 2) < 30 || ((u_) >> 2) >= 50 ? (u_) >> 2 : ((u_) >> 2) < 40 ? ((u_) >> 2) + 10 : ((u_) >> 2) - 10)
#define U_SRC(u_) (U_KQ(u_) * 160u * 7680u + U_CG(u_) * 128u)
#define U_FLAG(u_) (OFF_FA2 + (U_KQ(u_) * 60 + U_CG(u_)) * 128)
  const unsigned vm = (lane >> 3) * 7680 + (((lane & 7) ^ (((lane >> 4) & 3) << 1)) * 16);
#define GEMV_LOAD(u_, lu_) asm volatile( \
  "s_nop 4\n\t" \
  "s_add_u32 m0, %[lu], 20480\n\t" \
  "s_nop 0\n\t" \
  "buffer_load_dwordx4 %[vl16], %[rc], %[sC] offen lds\n\t" \
  "s_add_u32 m0, %[lu], 0\n\t" \
  "s_add_u32 s44, %[sMod], 0\n\t" \
  "buffer_load_dwordx4 %[vm], %[rm], s44 offen nt lds\n\t" \
  "s_add_u32 m0, %[lu], 1024\n\t" \
  "s_add_u32 s44, %[sMod], 61440\n\t" \
  "buffer_load_dwordx4 %[vm], %[rm], s44 offen nt lds\n\t" \
  "s_add_u32 m0, %[lu], 2048\n\t" \
  "s_add_u32 s44, %[sMod], 122880\n\t" \
  "buffer_load_dwordx4 %[vm], %[rm], s44 offen nt lds\n\t" \
  "s_add_u32 m0, %[lu], 3072\n\t" \
  "s_add_u32 s44, %[sMod], 184320\n\t" \
  "buffer_load_dwordx4 %[vm], %[rm], s44 offen nt lds\n\t" \
  "s_add_u32 m0, %[lu], 4096\n\t" \
  "s_add_u32 s44, %[sMod], 245760\n\t" \
  "buffer_load_dwordx4 %[vm], %[rm], s44 offen nt lds\n\t" \
  "s_add_u32 m0, %[lu], 5120\n\t" \
  "s_add_u32 s44, %[sMod], 307200\n\t" \
  "buffer_load_dwordx4 %[vm], %[rm], s44 offen nt lds\n\t" \
  "s_add_u32 m0, %[lu], 6144\n\t" \
  "s_add_u32 s44, %[sMod], 368640\n\t" \
  "buffer_load_dwordx4 %[vm], %[rm], s44 offen nt lds\n\t" \
  "s_add_u32 m0, %[lu], 7168\n\t" \
  "s_add_u32 s44, %[sMod], 430080\n\t" \
  "buffer_load_dwordx4 %[vm], %[rm], s44 offen nt lds\n\t" \
  "s_add_u32 m0, %[lu], 8192\n\t" \
  "s_add_u32 s44, %[sMod], 491520\n\t" \
  "buffer_load_dwordx4 %[vm], %[rm], s44 offen nt lds\n\t" \
  "s_add_u32 m0, %[lu], 9216\n\t" \
  "s_add_u32 s44, %[sMod], 552960\n\t" \
  "buffer_load_dwordx4 %[vm], %[rm], s44 offen nt lds\n\t" \
  "s_add_u32 m0, %[lu], 10240\n\t" \
  "s_add_u32 s44, %[sMod], 614400\n\t" \
  "buffer_load_dwordx4 %[vm], %[rm], s44 offen nt lds\n\t" \
  "s_add_u32 m0, %[lu], 11264\n\t" \
  "s_add_u32 s44, %[sMod], 675840\n\t" \
  "buffer_load_dwordx4 %[vm], %[rm], s44 offen nt lds\n\t" \
  "s_add_u32 m0, %[lu], 12288\n\t" \
  "s_add_u32 s44, %[sMod], 737280\n\t" \
  "buffer_load_dwordx4 %[vm], %[rm], s44 offen nt lds\n\t" \
  "s_add_u32 m0, %[lu], 13312\n\t" \
  "s_add_u32 s44, %[sMod], 798720\n\t" \
  "buffer_load_dwordx4 %[vm], %[rm], s44 offen nt lds\n\t" \
  "s_add_u32 m0, %[lu], 14336\n\t" \
  "s_add_u32 s44, %[sMod], 860160\n\t" \
  "buffer_load_dwordx4 %[vm], %[rm], s44 offen nt lds\n\t" \
  "s_add_u32 m0, %[lu], 15360\n\t" \
  "s_add_u32 s44, %[sMod], 921600\n\t" \
  "buffer_load_dwordx4 %[vm], %[rm], s44 offen nt lds\n\t" \
  "s_add_u32 m0, %[lu], 16384\n\t" \
  "s_add_u32 s44, %[sMod], 983040\n\t" \
  "buffer_load_dwordx4 %[vm], %[rm], s44 offen nt lds\n\t" \
  "s_add_u32 m0, %[lu], 17408\n\t" \
  "s_add_u32 s44, %[sMod], 1044480\n\t" \
  "buffer_load_dwordx4 %[vm], %[rm], s44 offen nt lds\n\t" \
  "s_add_u32 m0, %[lu], 18432\n\t" \
  "s_add_u32 s44, %[sMod], 1105920\n\t" \
  "buffer_load_dwordx4 %[vm], %[rm], s44 offen nt lds\n\t" \
  "s_add_u32 m0, %[lu], 19456\n\t" \
  "s_add_u32 s44, %[sMod], 1167360\n\t" \
  "buffer_load_dwordx4 %[vm], %[rm], s44 offen nt lds\n\t" \
        :: [rm] "s"(mkrsrc(wmod, D_ * 3840 * 2)), [rc] "s"(mkrsrc(cIn, D_ * 2)), [lu] "s"(lu_), [sC] "s"(U_KQ(u_) * 320u), [sMod] "s"(U_SRC(u_)), \
           [vl16] "v"(lane * 16), [vm] "v"(vm) : "memory", "scc", "s44")
#define GEMV_UNIT(u_, lu_, younger_) asm volatile( \
  "s_nop 4\n\t" \
  "v_lshlrev_b32 v176, 1, %[vl]\n\t" \
  "v_add_u32 v176, %[lu], v176\n\t" \
  "v_mov_b32 v96, 0\n\t" \
  "v_mov_b32 v97, 0\n\t" \
  "v_mov_b32 v98, 0\n\t" \
  "s_waitcnt vmcnt(%c[wa])\n\t" \
  "ds_read_u16_d16_hi v96, v176 offset:20480\n\t" \
  "ds_read_u16_d16_hi v97, v176 offset:20608\n\t" \
  "ds_read_u16_d16_hi v98, v176 offset:20736\n\t" \
  "s_waitcnt lgkmcnt(2)\n\t" \
  "v_mul_f32 v177, 0xbfb8aa3b, v96\n\t" \
  "s_waitcnt lgkmcnt(1)\n\t" \
  "v_mul_f32 v178, 0xbfb8aa3b, v97\n\t" \
  "s_waitcnt lgkmcnt(0)\n\t" \
  "v_mul_f32 v179, 0xbfb8aa3b, v98\n\t" \
  "v_exp_f32 v177, v177\n\t" \
  "v_exp_f32 v178, v178\n\t" \
  "v_exp_f32 v179, v179\n\t" \
  "v_add_f32 v177, 1.0, v177\n\t" \
  "v_add_f32 v178, 1.0, v178\n\t" \
  "v_add_f32 v179, 1.0, v179\n\t" \
  "v_rcp_f32 v177, v177\n\t" \
  "v_rcp_f32 v178, v178\n\t" \
  "v_rcp_f32 v179, v179\n\t" \
  "v_mul_f32 v177, v177, v96\n\t" \
  "v_mul_f32 v178, v178, v97\n\t" \
  "v_mul_f32 v179, v179, v98\n\t" \
  "v_cvt_pk_bf16_f32 v177, v177, v177\n\t" \
  "v_cvt_pk_bf16_f32 v178, v178, v178\n\t" \
  "v_cvt_pk_bf16_f32 v179, v179, v179\n\t" \
  "ds_write_b16 v176, v177 offset:21504\n\t" \
  "ds_write_b16 v176, v178 offset:21632\n\t" \
  "ds_write_b16 v176, v179 offset:21760\n\t" \
  "v_lshrrev_b32 v180, 4, %[vl]\n\t" \
  "v_lshlrev_b32 v180, 3, v180\n\t" \
  "v_add_u32 v180, %[lu], v180\n\t" \
  "ds_read_b64 v[100:101], v180 offset:21504\n\t" \
  "ds_read_b64 v[102:103], v180 offset:21536\n\t" \
  "ds_read_b64 v[104:105], v180 offset:21568\n\t" \
  "ds_read_b64 v[106:107], v180 offset:21600\n\t" \
  "ds_read_b64 v[108:109], v180 offset:21632\n\t" \
  "ds_read_b64 v[110:111], v180 offset:21664\n\t" \
  "ds_read_b64 v[112:113], v180 offset:21696\n\t" \
  "ds_read_b64 v[114:115], v180 offset:21728\n\t" \
  "ds_read_b64 v[116:117], v180 offset:21760\n\t" \
  "ds_read_b64 v[118:119], v180 offset:21792\n\t" \
  "v_xor_b32 v181, 32, %[vtm]\n\t" \
  "v_xor_b32 v182, 64, %[vtm]\n\t" \
  "v_xor_b32 v183, 96, %[vtm]\n\t" \
  "s_waitcnt vmcnt(%c[wb])\n\t" \
  "ds_read_b64_tr_b16 v[120:121], %[vtm] offset:0\n\t" \
  "ds_read_b64_tr_b16 v[122:123], %[vtm] offset:2048\n\t" \
  "ds_read_b64_tr_b16 v[124:125], v181 offset:0\n\t" \
  "ds_read_b64_tr_b16 v[126:127], v181 offset:2048\n\t" \
  "ds_read_b64_tr_b16 v[128:129], v182 offset:0\n\t" \
  "ds_read_b64_tr_b16 v[130:131], v182 offset:2048\n\t" \
  "ds_read_b64_tr_b16 v[132:133], v183 offset:0\n\t" \
  "ds_read_b64_tr_b16 v[134:135], v183 offset:2048\n\t" \
  "ds_read_b64_tr_b16 v[136:137], %[vtm] offset:4096\n\t" \
  "ds_read_b64_tr_b16 v[138:139], %[vtm] offset:6144\n\t" \
  "ds_read_b64_tr_b16 v[140:141], v181 offset:4096\n\t" \
  "ds_read_b64_tr_b16 v[142:143], v181 offset:6144\n\t" \
  "ds_read_b64_tr_b16 v[144:145], v182 offset:4096\n\t" \
  "ds_read_b64_tr_b16 v[146:147], v182 offset:6144\n\t" \
  "ds_read_b64_tr_b16 v[148:149], v183 offset:4096\n\t" \
  "ds_read_b64_tr_b16 v[150:151], v183 offset:6144\n\t" \
  "s_waitcnt lgkmcnt(14)\n\t" \
  "v_mfma_f32_16x16x32_bf16 v[152:155], v[100:103], v[120:123], 0\n\t" \
  "s_waitcnt lgkmcnt(12)\n\t" \
  "v_mfma_f32_16x16x32_bf16 v[156:159], v[100:103], v[124:127], 0\n\t" \
  "s_waitcnt lgkmcnt(10)\n\t" \
  "v_mfma_f32_16x16x32_bf16 v[160:163], v[100:103], v[128:131], 0\n\t" \
  "s_waitcnt lgkmcnt(8)\n\t" \
  "v_mfma_f32_16x16x32_bf16 v[164:167], v[100:103], v[132:135], 0\n\t" \
  "ds_read_b64_tr_b16 v[120:121], %[vtm] offset:8192\n\t" \
  "ds_read_b64_tr_b16 v[122:123], %[vtm] offset:10240\n\t" \
  "ds_read_b64_tr_b16 v[124:125], v181 offset:8192\n\t" \
  "ds_read_b64_tr_b16 v[126:127], v181 offset:10240\n\t" \
  "ds_read_b64_tr_b16 v[128:129], v182 offset:8192\n\t" \
  "ds_read_b64_tr_b16 v[130:131], v182 offset:10240\n\t" \
  "ds_read_b64_tr_b16 v[132:133], v183 offset:8192\n\t" \
  "ds_read_b64_tr_b16 v[134:135], v183 offset:10240\n\t" \
  "s_waitcnt lgkmcnt(14)\n\t" \
  "v_mfma_f32_16x16x32_bf16 v[152:155], v[104:107], v[136:139], v[152:155]\n\t" \
  "s_waitcnt lgkmcnt(12)\n\t" \
  "v_mfma_f32_16x16x32_bf16 v[156:159], v[104:107], v[140:143], v[156:159]\n\t" \
  "s_waitcnt lgkmcnt(10)\n\t" \
  "v_mfma_f32_16x16x32_bf16 v[160:163], v[104:107], v[144:147], v[160:163]\n\t" \
  "s_waitcnt lgkmcnt(8)\n\t" \
  "v_mfma_f32_16x16x32_bf16 v[164:167], v[104:107], v[148:151], v[164:167]\n\t" \
  "ds_read_b64_tr_b16 v[136:137], %[vtm] offset:12288\n\t" \
  "ds_read_b64_tr_b16 v[138:139], %[vtm] offset:14336\n\t" \
  "ds_read_b64_tr_b16 v[140:141], v181 offset:12288\n\t" \
  "ds_read_b64_tr_b16 v[142:143], v181 offset:14336\n\t" \
  "ds_read_b64_tr_b16 v[144:145], v182 offset:12288\n\t" \
  "ds_read_b64_tr_b16 v[146:147], v182 offset:14336\n\t" \
  "ds_read_b64_tr_b16 v[148:149], v183 offset:12288\n\t" \
  "ds_read_b64_tr_b16 v[150:151], v183 offset:14336\n\t" \
  "s_waitcnt lgkmcnt(14)\n\t" \
  "v_mfma_f32_16x16x32_bf16 v[152:155], v[108:111], v[120:123], v[152:155]\n\t" \
  "s_waitcnt lgkmcnt(12)\n\t" \
  "v_mfma_f32_16x16x32_bf16 v[156:159], v[108:111], v[124:127], v[156:159]\n\t" \
  "s_waitcnt lgkmcnt(10)\n\t" \
  "v_mfma_f32_16x16x32_bf16 v[160:163], v[108:111], v[128:131], v[160:163]\n\t" \
  "s_waitcnt lgkmcnt(8)\n\t" \
  "v_mfma_f32_16x16x32_bf16 v[164:167], v[108:111], v[132:135], v[164:167]\n\t" \
  "ds_read_b64_tr_b16 v[120:121], %[vtm] offset:16384\n\t" \
  "ds_read_b64_tr_b16 v[122:123], %[vtm] offset:18432\n\t" \
  "ds_read_b64_tr_b16 v[124:125], v181 offset:16384\n\t" \
  "ds_read_b64_tr_b16 v[126:127], v181 offset:18432\n\t" \
  "ds_read_b64_tr_b16 v[128:129], v182 offset:16384\n\t" \
  "ds_read_b64_tr_b16 v[130:131], v182 offset:18432\n\t" \
  "ds_read_b64_tr_b16 v[132:133], v183 offset:16384\n\t" \
  "ds_read_b64_tr_b16 v[134:135], v183 offset:18432\n\t" \
  "s_waitcnt lgkmcnt(14)\n\t" \
  "v_mfma_f32_16x16x32_bf16 v[152:155], v[112:115], v[136:139], v[152:155]\n\t" \
  "s_waitcnt lgkmcnt(12)\n\t" \
  "v_mfma_f32_16x16x32_bf16 v[156:159], v[112:115], v[140:143], v[156:159]\n\t" \
  "s_waitcnt lgkmcnt(10)\n\t" \
  "v_mfma_f32_16x16x32_bf16 v[160:163], v[112:115], v[144:147], v[160:163]\n\t" \
  "s_waitcnt lgkmcnt(8)\n\t" \
  "v_mfma_f32_16x16x32_bf16 v[164:167], v[112:115], v[148:151], v[164:167]\n\t" \
  "s_waitcnt lgkmcnt(6)\n\t" \
  "v_mfma_f32_16x16x32_bf16 v[152:155], v[116:119], v[120:123], v[152:155]\n\t" \
  "s_waitcnt lgkmcnt(4)\n\t" \
  "v_mfma_f32_16x16x32_bf16 v[156:159], v[116:119], v[124:127], v[156:159]\n\t" \
  "s_waitcnt lgkmcnt(2)\n\t" \
  "v_mfma_f32_16x16x32_bf16 v[160:163], v[116:119], v[128:131], v[160:163]\n\t" \
  "s_waitcnt lgkmcnt(0)\n\t" \
  "v_mfma_f32_16x16x32_bf16 v[164:167], v[116:119], v[132:135], v[164:167]\n\t" \
  "v_lshlrev_b32 v176, 2, %[vl]\n\t" \
  "s_nop 2\n\t" \
  "v_mov_b32_dpp v152, v156 quad_perm:[0,1,2,3] row_mask:0x2 bank_mask:0xf\n\t" \
  "s_nop 1\n\t" \
  "v_mov_b32_dpp v152, v160 quad_perm:[0,1,2,3] row_mask:0x4 bank_mask:0xf\n\t" \
  "s_nop 1\n\t" \
  "v_mov_b32_dpp v152, v164 quad_perm:[0,1,2,3] row_mask:0x8 bank_mask:0xf\n\t" \
  "buffer_store_dword v152, v176, %[srd], %[sMo] offen sc1\n\t" \
  "s_nop 1\n\t" \
        :: [srd] "s"(srd), [lu] "s"(lu_), [wa] "i"((younger_) + 20), [wb] "i"(younger_), [sMo] "s"(OFF_MODP + U_KQ(u_) * 15360u + U_CG(u_) * 256u), [vl] "v"(lane), \
           [vtm] "v"((lu_) + (lane >> 2) * 128 + ((lane >> 3) & 3) * 32 + (lane & 3) * 8) : "memory", "vcc", "scc", \
"v96", "v97", "v98", "v100", "v101", "v102", "v103", "v104", "v105", "v106", "v107", "v108", "v109", "v110", "v111", "v112", "v113", "v114", "v115", "v116", "v117", "v118", "v119", "v120", "v121", "v122", "v123", "v124", "v125", "v126", "v127", "v128", "v129", "v130", "v131", "v132", "v133", "v134", "v135", "v136", "v137", "v138", "v139", "v140", "v141", "v142", "v143", "v144", "v145", "v146", "v147", "v148", "v149", "v150", "v151", "v152", "v153", "v154", "v155", "v156", "v157", "v158", "v159", "v160", "v161", "v162", "v163", "v164", "v165", "v166", "v167", "v176", "v177", "v178", "v179", "v180", "v181", "v182", "v183" \
      )
  auto stash = [&]() {
    const unsigned vpub = role == 0 ? (tid < 8 * FX_R ? OFF_FX + ((tid >> 3) * 160 + cb) * 128 + (tid & 7) * 16 : OOB) : tid < 8 ? (role == 1 ? OFF_FU + w1 * 128 : OFF_FW2 + w2 * 128) + tid * 16 : OOB;
    const unsigned vpw = lane < 4 ? OFF_FW1 + (j * 4 + lane) * 128 : lane < 8 ? OFF_FW2 + ((4 * w + (lane & 3)) * 2 + (ng >> 2)) * 128
                                                                              : OFF_FU + ((lane < 12 ? j * 4 : 0) + (lane & 3)) * 128;
    const unsigned pb = 32 * (lane >> 3) + 8 * w + (lane & 7);
    const unsigned vpx = lane < 40 && !(role == 0 && pb == cb) ? OFF_FX + (xcd % FX_R * 160 + pb) * 128 : OFF_FW2 + min(lane - 40, 9u) * 128;
    const unsigned g = lane >> 4, q = (lane >> 2) & 3, pq = lane & 3;
    const unsigned row = 4 * g + q;
    const unsigned vtr = lb + row * 128 + ((row >> 1) & 3) * 32 + ((pq * 8) ^ ((row & 8) * 2));
    const unsigned vl16 = lane * 16;
    const unsigned vdma = OFF_X2P + p * 81920u + w * 4096u + lane * 16;
    const unsigned vh2st = lane < 48 ? OFF_H2F + (4 * p * 192 + 12 * j + 3 * w) * 256u + lane * 16 : OOB;
    const unsigned tk = 4 * j + w;
    const unsigned vstc = lb + lane * 128 + (((tk >> 4) ^ ((lane >> 1) & 3)) * 32) + (((tk & 15) * 2) ^ ((lane & 8) * 2));
    const unsigned vstw = lane == 63 ? tk * 64 : OOB;
    const unsigned vflw = lane < 8 ? OFF_FL + p * 32768u + (j * 4 + w) * 128u + lane * 16 : OOB;
    const unsigned vep = b == 0 && tid == 0 ? OFF_EP : OOB;
    const unsigned rs_ = 2 * (w & 1) + sub, rb = lb + (w >> 1) * 81920 + 80 * ng * 128;
    const unsigned r0 = 8 * g + q, r1 = 8 * g + 4 + q, r2 = 64 + 4 * g + q;
    const unsigned vres0 = rb + r0 * 128 + ((rs_ ^ ((r0 >> 1) & 3)) * 32) + ((pq * 8) ^ ((r0 & 8) * 2));
    const unsigned vres1 = rb + r1 * 128 + ((rs_ ^ ((r1 >> 1) & 3)) * 32) + ((pq * 8) ^ ((r1 & 8) * 2));
    const unsigned vres2 = rb + r2 * 128 + ((rs_ ^ ((r2 >> 1) & 3)) * 32) + ((pq * 8) ^ ((r2 & 8) * 2));
    const unsigned vwr = lb + w * 20480 + g * 256 + ((lane + 4 * g) & 15) * 16;
    const unsigned tq = lane < 32 ? q : (lane >> 1) & 3, tl = 4 * w + tq, g0 = lane < 32 ? pq : 2 * (lane & 1), g1 = lane < 32 ? pq : g0 + 1;
    const unsigned vrd0 = lb + (lane < 32 ? g * 2048 : 4096) + g0 * 256 + ((tl + 4 * g0) & 15) * 16;
    const unsigned vrd1 = lb + (lane < 32 ? g * 2048 + 1024 : 4096) + g1 * 256 + ((tl + 4 * g1) & 15) * 16;
    const unsigned vh1w = lane < 20 ? OFF_X2P + (16 + p) * 81920u + w * 4096u + (lane >> 2) * 16384 + ((lane & 3) * 8 + ng) * 128 : OOB;
    const unsigned vgb = OFF_GB - OFF_RT + (lane < 32 ? 32 * g + 8 * pq : 64 + 8 * (lane & 1)) * 4;
    const unsigned vout = lane < 40 ? ((64 * p + 16 * sub + tl) * D_ + 80 * ng + (lane < 32 ? 32 * g + 8 * pq : 64 + 8 * (lane & 1))) * 2 : OOB;
    *(u32x4*)(smem + L_STASH + 0 + tid * 16) = u32x4{vpub, vl16, vpw, vpx};
    *(u32x4*)(smem + L_STASH + 4096 + tid * 16) = u32x4{vdma, vtr, vstc, vstw};
    *(u32x4*)(smem + L_STASH + 8192 + tid * 16) = u32x4{vh2st, vflw, vres0, vres1};
    *(u32x4*)(smem + L_STASH + 12288 + tid * 16) = u32x4{vres2, vwr, vrd0, vrd1};
    *(u32x4*)(smem + L_STASH + 16384 + tid * 16) = u32x4{vout, vgb, vep, vh1w};
    FENCE();
  };
  unsigned c_vf2;
  unsigned c_vf1;
  unsigned c_vf3;
  unsigned c_vfw;
  unsigned c_va;
  unsigned c_vb0;
  unsigned c_vb1;
  unsigned c_vb2;
  unsigned c_vb3;
  unsigned c_vx;
  unsigned c_vs;
  unsigned c_vs2;
  unsigned c_vo;
  u32x4 c_vd;
  unsigned c_vl16;
  unsigned c_vkv;
  unsigned c_vmod;
  u64 tag;
  if (role == 0) {
    const rsrc_t rk = mkrsrc(kIn, S_ * D_ * 2), rv = mkrsrc(vIn, S_ * D_ * 2), rf = mkrsrc(ssmk, S_ * D_ * 2);
    const unsigned tb = 4 * (cb & 31) + w, d0 = 128 * (cb >> 5) + 2 * lane;
    u32x4 fa[4];
    const unsigned lkv = L_KV + 2048 + w * 9216;
    asm volatile(
  "s_mov_b32 m0, %[lk]\n\t"
  "s_nop 3\n\t"
  "buffer_load_dwordx4 %[vo], %[rk], %[sh] offen nt lds\n\t"
  "s_add_u32 m0, %[lk], 1024\n\t"
  "s_add_u32 s44, %[so], 0\n\t"
  "buffer_load_dwordx4 %[vo], %[rk], s44 offen nt lds\n\t"
  "s_add_u32 m0, %[lk], 5120\n\t"
  "s_nop 0\n\t"
  "buffer_load_dwordx4 %[vo], %[rv], s44 offen nt lds\n\t"
  "s_add_u32 m0, %[lk], 2048\n\t"
  "s_add_u32 s44, %[so], 5120\n\t"
  "buffer_load_dwordx4 %[vo], %[rk], s44 offen nt lds\n\t"
  "s_add_u32 m0, %[lk], 6144\n\t"
  "s_nop 0\n\t"
  "buffer_load_dwordx4 %[vo], %[rv], s44 offen nt lds\n\t"
  "s_add_u32 m0, %[lk], 3072\n\t"
  "s_add_u32 s44, %[so], 10240\n\t"
  "buffer_load_dwordx4 %[vo], %[rk], s44 offen nt lds\n\t"
  "s_add_u32 m0, %[lk], 7168\n\t"
  "s_nop 0\n\t"
  "buffer_load_dwordx4 %[vo], %[rv], s44 offen nt lds\n\t"
  "s_add_u32 m0, %[lk], 4096\n\t"
  "s_add_u32 s44, %[so], 15360\n\t"
  "buffer_load_dwordx4 %[vo], %[rk], s44 offen nt lds\n\t"
  "s_add_u32 m0, %[lk], 8192\n\t"
  "s_nop 0\n\t"
  "buffer_load_dwordx4 %[vo], %[rv], s44 offen nt lds\n\t"
      :: [rk] "s"(rk), [rv] "s"(rv), [lk] "s"(lb + lkv), [so] "s"(tb * 20480u + (cb >> 5) * 256u), [sh] "s"(tb ? tb * 20480u + (cb >> 5) * 256u - 5120u : OOB),
         [vo] "v"((lane >> 4) * 1280 + (lane & 15) * 16) : "memory", "scc", "s44");
    const u32x4 taps = *(const u32x4*)(shk + d0 * 4);
    dval = ssmD[8 * (cb >> 1) + 2 * w + (cb & 1)];
    FENCE();
    __builtin_amdgcn_raw_buffer_store_b128(u32x4{0, 0, 0, 0}, srd, tid < 64 && cb < 128 ? OFF_ST2 + cb * 1024 + tid * 16 : OOB, 0, SC1);
    FENCE();
#pragma unroll
    for (int n = 0; n < 4; ++n) fa[n] = __builtin_amdgcn_raw_buffer_load_b128(rf, lane * 16 + n * 1024, (8 * (cb >> 1) + 2 * w + (cb & 1)) * 4096u, NT);
    u32x4 xr[5];
    const bool isx = cb < 128;
    const rsrc_t rx = mkrsrc(isx ? x : wmod, isx ? S_ * D_ * 2 : D_ * 3840 * 2);
#pragma unroll
    for (int n = 0; n < 5; ++n)
      xr[n] = __builtin_amdgcn_raw_buffer_load_b128(rx, isx ? (lane >> 3) * 1280 + (lane & 7) * 16 : vm,
                                                    isx ? cb * 20480u + (w & 1) * 10240u + (w >> 1) * 128u + n * 256u : U_SRC(cb) + (4 * n + w) * 61440u, NT);
    const u32x4 cr = __builtin_amdgcn_raw_buffer_load_b128(mkrsrc(cIn, isx ? 0 : D_ * 2), lane * 16, U_KQ(cb) * 320u, 0);
    FENCE();
    STAMP(6);
    tag = load_tag(ws);
    if (w < 2) *(u32x4*)(smem + L_KV + tid * 16) = zero4;
    if (tid == 128) *(u32x4*)(smem + L_CNT) = zero4;
    {
      const unsigned d = 8 * (cb >> 1) + 2 * w + (cb & 1);
      const unsigned r = lane & 15, g = lane >> 4;
      const unsigned pp = 25 + r - 8 * g;
      const unsigned va = lb + L_FC + w * FC_CH + (pp & 3) * FC_PITCH + (pp & ~3u) * 2;
      unsigned vb[4];
  #pragma unroll
      for (int k = 0; k < 4; ++k) { const unsigned G = 14 - 4 * k + 2 * r + g; vb[k] = lb + L_KV + G * 64 + ((w ^ (G >> 2)) & 3) * 16; }
      const unsigned gx = (2 * r + (g >> 1)) * 64 + ((w ^ (r >> 1)) & 3) * 16;
      const unsigned vx = lb + L_X + gx + (g & 1) * 8, vs = lb + L_ST + r * 128 + ((2 * g) ^ stkey(r)) * 16;
      const unsigned to = 256 * (g & 1) + 16 * r + 8 * (g >> 1);
      const unsigned vo = (to >> 6) * 81920u + (((to & 63) * 2) ^ (((d >> 1) & 3) * 32 + (d & 8) * 2));
      const unsigned wh = (lane >> 2) & 3, mcol = (wh < 3 ? wh : 4) * 640 + d;
      const unsigned jd = 8 * g - 9 - r, dv = dval << (16 * (jd & 1));
      const u32x4 vd = {jd >> 1 == 0 ? dv : 0u, jd >> 1 == 1 ? dv : 0u, jd >> 1 == 2 ? dv : 0u, jd >> 1 == 3 ? dv : 0u};
      c_vf2 = lane < 32 ? OFF_FA3 + (32 * w + lane) * 128 : OFF_FA2 + ((lane & 3) * 60 + (mcol >> 6)) * 128;
      c_vf1 = OFF_FA1 + ((cb & ~31u) + 8 * w + (lane & 7)) * 128;
      c_vf3 = lane < 8 ? (cb < 128 ? OFF_FA3 + cb * 128 : U_FLAG(cb)) + lane * 16 : OOB;
      c_vfw = OFF_FW1 + (j * 4 + (lane & 3)) * 128;
      c_va = va;
      c_vb0 = vb[0];
      c_vb1 = vb[1];
      c_vb2 = vb[2];
      c_vb3 = vb[3];
      c_vx = vx;
      c_vs = vs;
      c_vs2 = vs ^ 16;
      c_vo = vo;
      c_vd = vd;
      c_vl16 = lane * 16;
      c_vkv = (lane >> 3) * 20480 + (lane & 7) * 16;
      c_vmod = OFF_MODP + ((lane & 3) * 3840 + mcol) * 4;
      asm volatile("" : "+v"(c_vf2), "+v"(c_vf1), "+v"(c_vf3), "+v"(c_vfw), "+v"(c_va), "+v"(c_vb0), "+v"(c_vb1), "+v"(c_vb2), "+v"(c_vb3), "+v"(c_vx), "+v"(c_vs), "+v"(c_vs2), "+v"(c_vo), "+v"(c_vd), "+v"(c_vl16), "+v"(c_vkv), "+v"(c_vmod));
    }
    FENCE();
    asm volatile("s_waitcnt vmcnt(11)" ::: "memory");
    STAMP(7);
    asm volatile(
  "s_nop 1\n\t"
  "ds_read2st64_b32 v[112:113], %[la] offset0:0 offset1:1\n\t"
  "ds_read2st64_b32 v[114:115], %[la] offset0:2 offset1:3\n\t"
  "ds_read2st64_b32 v[116:117], %[la] offset0:4 offset1:5\n\t"
  "ds_read2st64_b32 v[118:119], %[la] offset0:6 offset1:7\n\t"
  "ds_read2st64_b32 v[120:121], %[la] offset0:8 offset1:9\n\t"
  "ds_read2st64_b32 v[122:123], %[la] offset0:10 offset1:11\n\t"
  "ds_read2st64_b32 v[132:133], %[la] offset0:20 offset1:21\n\t"
  "ds_read2st64_b32 v[134:135], %[la] offset0:22 offset1:23\n\t"
  "ds_read2st64_b32 v[136:137], %[la] offset0:24 offset1:25\n\t"
  "ds_read2st64_b32 v[138:139], %[la] offset0:26 offset1:27\n\t"
  "ds_read2st64_b32 v[124:125], %[la] offset0:12 offset1:13\n\t"
  "ds_read2st64_b32 v[126:127], %[la] offset0:14 offset1:15\n\t"
  "ds_read2st64_b32 v[128:129], %[la] offset0:16 offset1:17\n\t"
  "ds_read2st64_b32 v[130:131], %[la] offset0:18 offset1:19\n\t"
  "ds_read2st64_b32 v[140:141], %[la] offset0:28 offset1:29\n\t"
  "ds_read2st64_b32 v[142:143], %[la] offset0:30 offset1:31\n\t"
  "ds_read2st64_b32 v[144:145], %[la] offset0:32 offset1:33\n\t"
  "ds_read2st64_b32 v[146:147], %[la] offset0:34 offset1:35\n\t"
  "v_lshlrev_b32 v186, 16, %[t0]\n\t"
  "v_lshlrev_b32 v187, 16, %[t2]\n\t"
  "v_and_b32 v188, 0xffff0000, %[t0]\n\t"
  "v_and_b32 v189, 0xffff0000, %[t2]\n\t"
  "v_lshlrev_b32 v190, 16, %[t1]\n\t"
  "v_lshlrev_b32 v191, 16, %[t3]\n\t"
  "v_and_b32 v192, 0xffff0000, %[t1]\n\t"
  "v_and_b32 v193, 0xffff0000, %[t3]\n\t"
  "v_mov_b32 v234, 0x3f803f80\n\t"
  "s_waitcnt lgkmcnt(15)\n\t"
  "v_lshlrev_b32 v148, 16, v113\n\t"
  "v_and_b32 v149, 0xffff0000, v113\n\t"
  "v_lshlrev_b32 v150, 16, v114\n\t"
  "v_and_b32 v151, 0xffff0000, v114\n\t"
  "v_lshlrev_b32 v152, 16, v115\n\t"
  "v_and_b32 v153, 0xffff0000, v115\n\t"
  "v_lshlrev_b32 v154, 16, v116\n\t"
  "v_and_b32 v155, 0xffff0000, v116\n\t"
  "v_lshlrev_b32 v156, 16, v117\n\t"
  "v_and_b32 v157, 0xffff0000, v117\n\t"
  "s_waitcnt lgkmcnt(14)\n\t"
  "v_lshlrev_b32 v158, 16, v118\n\t"
  "v_and_b32 v159, 0xffff0000, v118\n\t"
  "v_lshlrev_b32 v160, 16, v119\n\t"
  "v_and_b32 v161, 0xffff0000, v119\n\t"
  "s_waitcnt lgkmcnt(13)\n\t"
  "v_lshlrev_b32 v162, 16, v120\n\t"
  "v_and_b32 v163, 0xffff0000, v120\n\t"
  "v_lshlrev_b32 v164, 16, v121\n\t"
  "v_and_b32 v165, 0xffff0000, v121\n\t"
  "s_waitcnt lgkmcnt(12)\n\t"
  "v_lshlrev_b32 v166, 16, v122\n\t"
  "v_and_b32 v167, 0xffff0000, v122\n\t"
  "v_lshlrev_b32 v168, 16, v123\n\t"
  "v_and_b32 v169, 0xffff0000, v123\n\t"
  "v_pk_mul_f32 v[194:195], v[148:149], v[192:193]\n\t"
  "v_pk_mul_f32 v[196:197], v[150:151], v[192:193]\n\t"
  "v_pk_mul_f32 v[198:199], v[152:153], v[192:193]\n\t"
  "v_pk_mul_f32 v[200:201], v[154:155], v[192:193]\n\t"
  "v_pk_mul_f32 v[202:203], v[156:157], v[192:193]\n\t"
  "v_pk_mul_f32 v[204:205], v[158:159], v[192:193]\n\t"
  "v_pk_mul_f32 v[206:207], v[160:161], v[192:193]\n\t"
  "v_pk_mul_f32 v[208:209], v[162:163], v[192:193]\n\t"
  "v_pk_mul_f32 v[210:211], v[150:151], v[190:191]\n\t"
  "v_pk_mul_f32 v[212:213], v[152:153], v[190:191]\n\t"
  "v_pk_mul_f32 v[214:215], v[154:155], v[190:191]\n\t"
  "v_pk_mul_f32 v[216:217], v[156:157], v[190:191]\n\t"
  "v_pk_mul_f32 v[218:219], v[158:159], v[190:191]\n\t"
  "v_pk_mul_f32 v[220:221], v[160:161], v[190:191]\n\t"
  "v_pk_mul_f32 v[222:223], v[162:163], v[190:191]\n\t"
  "v_pk_mul_f32 v[224:225], v[164:165], v[190:191]\n\t"
  "v_cvt_pk_bf16_f32 v194, v194, v210\n\t"
  "v_cvt_pk_bf16_f32 v195, v195, v211\n\t"
  "v_cvt_pk_bf16_f32 v196, v196, v212\n\t"
  "v_cvt_pk_bf16_f32 v197, v197, v213\n\t"
  "v_cvt_pk_bf16_f32 v198, v198, v214\n\t"
  "v_cvt_pk_bf16_f32 v199, v199, v215\n\t"
  "v_cvt_pk_bf16_f32 v200, v200, v216\n\t"
  "v_cvt_pk_bf16_f32 v201, v201, v217\n\t"
  "v_cvt_pk_bf16_f32 v202, v202, v218\n\t"
  "v_cvt_pk_bf16_f32 v203, v203, v219\n\t"
  "v_cvt_pk_bf16_f32 v204, v204, v220\n\t"
  "v_cvt_pk_bf16_f32 v205, v205, v221\n\t"
  "v_cvt_pk_bf16_f32 v206, v206, v222\n\t"
  "v_cvt_pk_bf16_f32 v207, v207, v223\n\t"
  "v_cvt_pk_bf16_f32 v208, v208, v224\n\t"
  "v_cvt_pk_bf16_f32 v209, v209, v225\n\t"
  "v_dot2_f32_bf16 v194, v194, v234, 0\n\t"
  "v_dot2_f32_bf16 v195, v195, v234, 0\n\t"
  "v_dot2_f32_bf16 v196, v196, v234, 0\n\t"
  "v_dot2_f32_bf16 v197, v197, v234, 0\n\t"
  "v_dot2_f32_bf16 v198, v198, v234, 0\n\t"
  "v_dot2_f32_bf16 v199, v199, v234, 0\n\t"
  "v_dot2_f32_bf16 v200, v200, v234, 0\n\t"
  "v_dot2_f32_bf16 v201, v201, v234, 0\n\t"
  "v_dot2_f32_bf16 v202, v202, v234, 0\n\t"
  "v_dot2_f32_bf16 v203, v203, v234, 0\n\t"
  "v_dot2_f32_bf16 v204, v204, v234, 0\n\t"
  "v_dot2_f32_bf16 v205, v205, v234, 0\n\t"
  "v_dot2_f32_bf16 v206, v206, v234, 0\n\t"
  "v_dot2_f32_bf16 v207, v207, v234, 0\n\t"
  "v_dot2_f32_bf16 v208, v208, v234, 0\n\t"
  "v_dot2_f32_bf16 v209, v209, v234, 0\n\t"
  "v_pk_mul_f32 v[210:211], v[152:153], v[188:189]\n\t"
  "v_pk_mul_f32 v[212:213], v[154:155], v[188:189]\n\t"
  "v_pk_mul_f32 v[214:215], v[156:157], v[188:189]\n\t"
  "v_pk_mul_f32 v[216:217], v[158:159], v[188:189]\n\t"
  "v_pk_mul_f32 v[218:219], v[160:161], v[188:189]\n\t"
  "v_pk_mul_f32 v[220:221], v[162:163], v[188:189]\n\t"
  "v_pk_mul_f32 v[222:223], v[164:165], v[188:189]\n\t"
  "v_pk_mul_f32 v[224:225], v[166:167], v[188:189]\n\t"
  "v_cvt_pk_bf16_f32 v194, v194, v210\n\t"
  "v_cvt_pk_bf16_f32 v195, v195, v211\n\t"
  "v_cvt_pk_bf16_f32 v196, v196, v212\n\t"
  "v_cvt_pk_bf16_f32 v197, v197, v213\n\t"
  "v_cvt_pk_bf16_f32 v198, v198, v214\n\t"
  "v_cvt_pk_bf16_f32 v199, v199, v215\n\t"
  "v_cvt_pk_bf16_f32 v200, v200, v216\n\t"
  "v_cvt_pk_bf16_f32 v201, v201, v217\n\t"
  "v_cvt_pk_bf16_f32 v202, v202, v218\n\t"
  "v_cvt_pk_bf16_f32 v203, v203, v219\n\t"
  "v_cvt_pk_bf16_f32 v204, v204, v220\n\t"
  "v_cvt_pk_bf16_f32 v205, v205, v221\n\t"
  "v_cvt_pk_bf16_f32 v206, v206, v222\n\t"
  "v_cvt_pk_bf16_f32 v207, v207, v223\n\t"
  "v_cvt_pk_bf16_f32 v208, v208, v224\n\t"
  "v_cvt_pk_bf16_f32 v209, v209, v225\n\t"
  "v_dot2_f32_bf16 v194, v194, v234, 0\n\t"
  "v_dot2_f32_bf16 v195, v195, v234, 0\n\t"
  "v_dot2_f32_bf16 v196, v196, v234, 0\n\t"
  "v_dot2_f32_bf16 v197, v197, v234, 0\n\t"
  "v_dot2_f32_bf16 v198, v198, v234, 0\n\t"
  "v_dot2_f32_bf16 v199, v199, v234, 0\n\t"
  "v_dot2_f32_bf16 v200, v200, v234, 0\n\t"
  "v_dot2_f32_bf16 v201, v201, v234, 0\n\t"
  "v_dot2_f32_bf16 v202, v202, v234, 0\n\t"
  "v_dot2_f32_bf16 v203, v203, v234, 0\n\t"
  "v_dot2_f32_bf16 v204, v204, v234, 0\n\t"
  "v_dot2_f32_bf16 v205, v205, v234, 0\n\t"
  "v_dot2_f32_bf16 v206, v206, v234, 0\n\t"
  "v_dot2_f32_bf16 v207, v207, v234, 0\n\t"
  "v_dot2_f32_bf16 v208, v208, v234, 0\n\t"
  "v_dot2_f32_bf16 v209, v209, v234, 0\n\t"
  "v_pk_mul_f32 v[210:211], v[154:155], v[186:187]\n\t"
  "v_pk_mul_f32 v[212:213], v[156:157], v[186:187]\n\t"
  "v_pk_mul_f32 v[214:215], v[158:159], v[186:187]\n\t"
  "v_pk_mul_f32 v[216:217], v[160:161], v[186:187]\n\t"
  "v_pk_mul_f32 v[218:219], v[162:163], v[186:187]\n\t"
  "v_pk_mul_f32 v[220:221], v[164:165], v[186:187]\n\t"
  "v_pk_mul_f32 v[222:223], v[166:167], v[186:187]\n\t"
  "v_pk_mul_f32 v[224:225], v[168:169], v[186:187]\n\t"
  "v_cvt_pk_bf16_f32 v194, v194, v210\n\t"
  "v_cvt_pk_bf16_f32 v195, v195, v211\n\t"
  "v_cvt_pk_bf16_f32 v196, v196, v212\n\t"
  "v_cvt_pk_bf16_f32 v197, v197, v213\n\t"
  "v_cvt_pk_bf16_f32 v198, v198, v214\n\t"
  "v_cvt_pk_bf16_f32 v199, v199, v215\n\t"
  "v_cvt_pk_bf16_f32 v200, v200, v216\n\t"
  "v_cvt_pk_bf16_f32 v201, v201, v217\n\t"
  "v_cvt_pk_bf16_f32 v202, v202, v218\n\t"
  "v_cvt_pk_bf16_f32 v203, v203, v219\n\t"
  "v_cvt_pk_bf16_f32 v204, v204, v220\n\t"
  "v_cvt_pk_bf16_f32 v205, v205, v221\n\t"
  "v_cvt_pk_bf16_f32 v206, v206, v222\n\t"
  "v_cvt_pk_bf16_f32 v207, v207, v223\n\t"
  "v_cvt_pk_bf16_f32 v208, v208, v224\n\t"
  "v_cvt_pk_bf16_f32 v209, v209, v225\n\t"
  "v_dot2_f32_bf16 v194, v194, v234, 0\n\t"
  "v_dot2_f32_bf16 v195, v195, v234, 0\n\t"
  "v_dot2_f32_bf16 v196, v196, v234, 0\n\t"
  "v_dot2_f32_bf16 v197, v197, v234, 0\n\t"
  "v_dot2_f32_bf16 v198, v198, v234, 0\n\t"
  "v_dot2_f32_bf16 v199, v199, v234, 0\n\t"
  "v_dot2_f32_bf16 v200, v200, v234, 0\n\t"
  "v_dot2_f32_bf16 v201, v201, v234, 0\n\t"
  "v_dot2_f32_bf16 v202, v202, v234, 0\n\t"
  "v_dot2_f32_bf16 v203, v203, v234, 0\n\t"
  "v_dot2_f32_bf16 v204, v204, v234, 0\n\t"
  "v_dot2_f32_bf16 v205, v205, v234, 0\n\t"
  "v_dot2_f32_bf16 v206, v206, v234, 0\n\t"
  "v_dot2_f32_bf16 v207, v207, v234, 0\n\t"
  "v_dot2_f32_bf16 v208, v208, v234, 0\n\t"
  "v_dot2_f32_bf16 v209, v209, v234, 0\n\t"
  "s_waitcnt lgkmcnt(11)\n\t"
  "v_and_b32 v210, 0xffff, v132\n\t"
  "v_and_b32 v211, 0xffff0000, v132\n\t"
  "v_and_b32 v212, 0xffff, v133\n\t"
  "v_and_b32 v213, 0xffff0000, v133\n\t"
  "s_waitcnt lgkmcnt(10)\n\t"
  "v_and_b32 v214, 0xffff, v134\n\t"
  "v_and_b32 v215, 0xffff0000, v134\n\t"
  "v_and_b32 v216, 0xffff, v135\n\t"
  "v_and_b32 v217, 0xffff0000, v135\n\t"
  "s_waitcnt lgkmcnt(9)\n\t"
  "v_and_b32 v218, 0xffff, v136\n\t"
  "v_and_b32 v219, 0xffff0000, v136\n\t"
  "v_and_b32 v220, 0xffff, v137\n\t"
  "v_and_b32 v221, 0xffff0000, v137\n\t"
  "s_waitcnt lgkmcnt(8)\n\t"
  "v_and_b32 v222, 0xffff, v138\n\t"
  "v_and_b32 v223, 0xffff0000, v138\n\t"
  "v_and_b32 v224, 0xffff, v139\n\t"
  "v_and_b32 v225, 0xffff0000, v139\n\t"
  "v_cvt_pk_bf16_f32 v194, v194, v195\n\t"
  "v_cvt_pk_bf16_f32 v196, v196, v197\n\t"
  "v_cvt_pk_bf16_f32 v198, v198, v199\n\t"
  "v_cvt_pk_bf16_f32 v200, v200, v201\n\t"
  "v_cvt_pk_bf16_f32 v202, v202, v203\n\t"
  "v_cvt_pk_bf16_f32 v204, v204, v205\n\t"
  "v_cvt_pk_bf16_f32 v206, v206, v207\n\t"
  "v_cvt_pk_bf16_f32 v208, v208, v209\n\t"
  "v_dot2_f32_bf16 v210, v194, v210, 0\n\t"
  "v_dot2_f32_bf16 v211, v194, v211, 0\n\t"
  "v_dot2_f32_bf16 v212, v196, v212, 0\n\t"
  "v_dot2_f32_bf16 v213, v196, v213, 0\n\t"
  "v_dot2_f32_bf16 v214, v198, v214, 0\n\t"
  "v_dot2_f32_bf16 v215, v198, v215, 0\n\t"
  "v_dot2_f32_bf16 v216, v200, v216, 0\n\t"
  "v_dot2_f32_bf16 v217, v200, v217, 0\n\t"
  "v_dot2_f32_bf16 v218, v202, v218, 0\n\t"
  "v_dot2_f32_bf16 v219, v202, v219, 0\n\t"
  "v_dot2_f32_bf16 v220, v204, v220, 0\n\t"
  "v_dot2_f32_bf16 v221, v204, v221, 0\n\t"
  "v_dot2_f32_bf16 v222, v206, v222, 0\n\t"
  "v_dot2_f32_bf16 v223, v206, v223, 0\n\t"
  "v_dot2_f32_bf16 v224, v208, v224, 0\n\t"
  "v_dot2_f32_bf16 v225, v208, v225, 0\n\t"
  "v_cvt_pk_bf16_f32 v229, v212, v210\n\t"
  "v_cvt_pk_bf16_f32 v228, v216, v214\n\t"
  "v_cvt_pk_bf16_f32 v227, v220, v218\n\t"
  "v_cvt_pk_bf16_f32 v226, v224, v222\n\t"
  "buffer_store_dwordx4 v[226:229], %[vo], %[srd], 0 offen offset:0 sc1\n\t"
  "v_cvt_pk_bf16_f32 v233, v213, v211\n\t"
  "v_cvt_pk_bf16_f32 v232, v217, v215\n\t"
  "v_cvt_pk_bf16_f32 v231, v221, v219\n\t"
  "v_cvt_pk_bf16_f32 v230, v225, v223\n\t"
  "buffer_store_dwordx4 v[230:233], %[vo], %[srd], 0 offen offset:128 sc1\n\t"
  "s_waitcnt lgkmcnt(7)\n\t"
  "v_lshlrev_b32 v170, 16, v124\n\t"
  "v_and_b32 v171, 0xffff0000, v124\n\t"
  "v_lshlrev_b32 v172, 16, v125\n\t"
  "v_and_b32 v173, 0xffff0000, v125\n\t"
  "s_waitcnt lgkmcnt(6)\n\t"
  "v_lshlrev_b32 v174, 16, v126\n\t"
  "v_and_b32 v175, 0xffff0000, v126\n\t"
  "v_lshlrev_b32 v176, 16, v127\n\t"
  "v_and_b32 v177, 0xffff0000, v127\n\t"
  "s_waitcnt lgkmcnt(5)\n\t"
  "v_lshlrev_b32 v178, 16, v128\n\t"
  "v_and_b32 v179, 0xffff0000, v128\n\t"
  "v_lshlrev_b32 v180, 16, v129\n\t"
  "v_and_b32 v181, 0xffff0000, v129\n\t"
  "s_waitcnt lgkmcnt(4)\n\t"
  "v_lshlrev_b32 v182, 16, v130\n\t"
  "v_and_b32 v183, 0xffff0000, v130\n\t"
  "v_lshlrev_b32 v184, 16, v131\n\t"
  "v_and_b32 v185, 0xffff0000, v131\n\t"
  "v_pk_mul_f32 v[194:195], v[164:165], v[192:193]\n\t"
  "v_pk_mul_f32 v[196:197], v[166:167], v[192:193]\n\t"
  "v_pk_mul_f32 v[198:199], v[168:169], v[192:193]\n\t"
  "v_pk_mul_f32 v[200:201], v[170:171], v[192:193]\n\t"
  "v_pk_mul_f32 v[202:203], v[172:173], v[192:193]\n\t"
  "v_pk_mul_f32 v[204:205], v[174:175], v[192:193]\n\t"
  "v_pk_mul_f32 v[206:207], v[176:177], v[192:193]\n\t"
  "v_pk_mul_f32 v[208:209], v[178:179], v[192:193]\n\t"
  "v_pk_mul_f32 v[210:211], v[166:167], v[190:191]\n\t"
  "v_pk_mul_f32 v[212:213], v[168:169], v[190:191]\n\t"
  "v_pk_mul_f32 v[214:215], v[170:171], v[190:191]\n\t"
  "v_pk_mul_f32 v[216:217], v[172:173], v[190:191]\n\t"
  "v_pk_mul_f32 v[218:219], v[174:175], v[190:191]\n\t"
  "v_pk_mul_f32 v[220:221], v[176:177], v[190:191]\n\t"
  "v_pk_mul_f32 v[222:223], v[178:179], v[190:191]\n\t"
  "v_pk_mul_f32 v[224:225], v[180:181], v[190:191]\n\t"
  "v_cvt_pk_bf16_f32 v194, v194, v210\n\t"
  "v_cvt_pk_bf16_f32 v195, v195, v211\n\t"
  "v_cvt_pk_bf16_f32 v196, v196, v212\n\t"
  "v_cvt_pk_bf16_f32 v197, v197, v213\n\t"
  "v_cvt_pk_bf16_f32 v198, v198, v214\n\t"
  "v_cvt_pk_bf16_f32 v199, v199, v215\n\t"
  "v_cvt_pk_bf16_f32 v200, v200, v216\n\t"
  "v_cvt_pk_bf16_f32 v201, v201, v217\n\t"
  "v_cvt_pk_bf16_f32 v202, v202, v218\n\t"
  "v_cvt_pk_bf16_f32 v203, v203, v219\n\t"
  "v_cvt_pk_bf16_f32 v204, v204, v220\n\t"
  "v_cvt_pk_bf16_f32 v205, v205, v221\n\t"
  "v_cvt_pk_bf16_f32 v206, v206, v222\n\t"
  "v_cvt_pk_bf16_f32 v207, v207, v223\n\t"
  "v_cvt_pk_bf16_f32 v208, v208, v224\n\t"
  "v_cvt_pk_bf16_f32 v209, v209, v225\n\t"
  "v_dot2_f32_bf16 v194, v194, v234, 0\n\t"
  "v_dot2_f32_bf16 v195, v195, v234, 0\n\t"
  "v_dot2_f32_bf16 v196, v196, v234, 0\n\t"
  "v_dot2_f32_bf16 v197, v197, v234, 0\n\t"
  "v_dot2_f32_bf16 v198, v198, v234, 0\n\t"
  "v_dot2_f32_bf16 v199, v199, v234, 0\n\t"
  "v_dot2_f32_bf16 v200, v200, v234, 0\n\t"
  "v_dot2_f32_bf16 v201, v201, v234, 0\n\t"
  "v_dot2_f32_bf16 v202, v202, v234, 0\n\t"
  "v_dot2_f32_bf16 v203, v203, v234, 0\n\t"
  "v_dot2_f32_bf16 v204, v204, v234, 0\n\t"
  "v_dot2_f32_bf16 v205, v205, v234, 0\n\t"
  "v_dot2_f32_bf16 v206, v206, v234, 0\n\t"
  "v_dot2_f32_bf16 v207, v207, v234, 0\n\t"
  "v_dot2_f32_bf16 v208, v208, v234, 0\n\t"
  "v_dot2_f32_bf16 v209, v209, v234, 0\n\t"
  "v_pk_mul_f32 v[210:211], v[168:169], v[188:189]\n\t"
  "v_pk_mul_f32 v[212:213], v[170:171], v[188:189]\n\t"
  "v_pk_mul_f32 v[214:215], v[172:173], v[188:189]\n\t"
  "v_pk_mul_f32 v[216:217], v[174:175], v[188:189]\n\t"
  "v_pk_mul_f32 v[218:219], v[176:177], v[188:189]\n\t"
  "v_pk_mul_f32 v[220:221], v[178:179], v[188:189]\n\t"
  "v_pk_mul_f32 v[222:223], v[180:181], v[188:189]\n\t"
  "v_pk_mul_f32 v[224:225], v[182:183], v[188:189]\n\t"
  "v_cvt_pk_bf16_f32 v194, v194, v210\n\t"
  "v_cvt_pk_bf16_f32 v195, v195, v211\n\t"
  "v_cvt_pk_bf16_f32 v196, v196, v212\n\t"
  "v_cvt_pk_bf16_f32 v197, v197, v213\n\t"
  "v_cvt_pk_bf16_f32 v198, v198, v214\n\t"
  "v_cvt_pk_bf16_f32 v199, v199, v215\n\t"
  "v_cvt_pk_bf16_f32 v200, v200, v216\n\t"
  "v_cvt_pk_bf16_f32 v201, v201, v217\n\t"
  "v_cvt_pk_bf16_f32 v202, v202, v218\n\t"
  "v_cvt_pk_bf16_f32 v203, v203, v219\n\t"
  "v_cvt_pk_bf16_f32 v204, v204, v220\n\t"
  "v_cvt_pk_bf16_f32 v205, v205, v221\n\t"
  "v_cvt_pk_bf16_f32 v206, v206, v222\n\t"
  "v_cvt_pk_bf16_f32 v207, v207, v223\n\t"
  "v_cvt_pk_bf16_f32 v208, v208, v224\n\t"
  "v_cvt_pk_bf16_f32 v209, v209, v225\n\t"
  "v_dot2_f32_bf16 v194, v194, v234, 0\n\t"
  "v_dot2_f32_bf16 v195, v195, v234, 0\n\t"
  "v_dot2_f32_bf16 v196, v196, v234, 0\n\t"
  "v_dot2_f32_bf16 v197, v197, v234, 0\n\t"
  "v_dot2_f32_bf16 v198, v198, v234, 0\n\t"
  "v_dot2_f32_bf16 v199, v199, v234, 0\n\t"
  "v_dot2_f32_bf16 v200, v200, v234, 0\n\t"
  "v_dot2_f32_bf16 v201, v201, v234, 0\n\t"
  "v_dot2_f32_bf16 v202, v202, v234, 0\n\t"
  "v_dot2_f32_bf16 v203, v203, v234, 0\n\t"
  "v_dot2_f32_bf16 v204, v204, v234, 0\n\t"
  "v_dot2_f32_bf16 v205, v205, v234, 0\n\t"
  "v_dot2_f32_bf16 v206, v206, v234, 0\n\t"
  "v_dot2_f32_bf16 v207, v207, v234, 0\n\t"
  "v_dot2_f32_bf16 v208, v208, v234, 0\n\t"
  "v_dot2_f32_bf16 v209, v209, v234, 0\n\t"
  "v_pk_mul_f32 v[210:211], v[170:171], v[186:187]\n\t"
  "v_pk_mul_f32 v[212:213], v[172:173], v[186:187]\n\t"
  "v_pk_mul_f32 v[214:215], v[174:175], v[186:187]\n\t"
  "v_pk_mul_f32 v[216:217], v[176:177], v[186:187]\n\t"
  "v_pk_mul_f32 v[218:219], v[178:179], v[186:187]\n\t"
  "v_pk_mul_f32 v[220:221], v[180:181], v[186:187]\n\t"
  "v_pk_mul_f32 v[222:223], v[182:183], v[186:187]\n\t"
  "v_pk_mul_f32 v[224:225], v[184:185], v[186:187]\n\t"
  "v_cvt_pk_bf16_f32 v194, v194, v210\n\t"
  "v_cvt_pk_bf16_f32 v195, v195, v211\n\t"
  "v_cvt_pk_bf16_f32 v196, v196, v212\n\t"
  "v_cvt_pk_bf16_f32 v197, v197, v213\n\t"
  "v_cvt_pk_bf16_f32 v198, v198, v214\n\t"
  "v_cvt_pk_bf16_f32 v199, v199, v215\n\t"
  "v_cvt_pk_bf16_f32 v200, v200, v216\n\t"
  "v_cvt_pk_bf16_f32 v201, v201, v217\n\t"
  "v_cvt_pk_bf16_f32 v202, v202, v218\n\t"
  "v_cvt_pk_bf16_f32 v203, v203, v219\n\t"
  "v_cvt_pk_bf16_f32 v204, v204, v220\n\t"
  "v_cvt_pk_bf16_f32 v205, v205, v221\n\t"
  "v_cvt_pk_bf16_f32 v206, v206, v222\n\t"
  "v_cvt_pk_bf16_f32 v207, v207, v223\n\t"
  "v_cvt_pk_bf16_f32 v208, v208, v224\n\t"
  "v_cvt_pk_bf16_f32 v209, v209, v225\n\t"
  "v_dot2_f32_bf16 v194, v194, v234, 0\n\t"
  "v_dot2_f32_bf16 v195, v195, v234, 0\n\t"
  "v_dot2_f32_bf16 v196, v196, v234, 0\n\t"
  "v_dot2_f32_bf16 v197, v197, v234, 0\n\t"
  "v_dot2_f32_bf16 v198, v198, v234, 0\n\t"
  "v_dot2_f32_bf16 v199, v199, v234, 0\n\t"
  "v_dot2_f32_bf16 v200, v200, v234, 0\n\t"
  "v_dot2_f32_bf16 v201, v201, v234, 0\n\t"
  "v_dot2_f32_bf16 v202, v202, v234, 0\n\t"
  "v_dot2_f32_bf16 v203, v203, v234, 0\n\t"
  "v_dot2_f32_bf16 v204, v204, v234, 0\n\t"
  "v_dot2_f32_bf16 v205, v205, v234, 0\n\t"
  "v_dot2_f32_bf16 v206, v206, v234, 0\n\t"
  "v_dot2_f32_bf16 v207, v207, v234, 0\n\t"
  "v_dot2_f32_bf16 v208, v208, v234, 0\n\t"
  "v_dot2_f32_bf16 v209, v209, v234, 0\n\t"
  "s_waitcnt lgkmcnt(3)\n\t"
  "v_and_b32 v210, 0xffff, v140\n\t"
  "v_and_b32 v211, 0xffff0000, v140\n\t"
  "v_and_b32 v212, 0xffff, v141\n\t"
  "v_and_b32 v213, 0xffff0000, v141\n\t"
  "s_waitcnt lgkmcnt(2)\n\t"
  "v_and_b32 v214, 0xffff, v142\n\t"
  "v_and_b32 v215, 0xffff0000, v142\n\t"
  "v_and_b32 v216, 0xffff, v143\n\t"
  "v_and_b32 v217, 0xffff0000, v143\n\t"
  "s_waitcnt lgkmcnt(1)\n\t"
  "v_and_b32 v218, 0xffff, v144\n\t"
  "v_and_b32 v219, 0xffff0000, v144\n\t"
  "v_and_b32 v220, 0xffff, v145\n\t"
  "v_and_b32 v221, 0xffff0000, v145\n\t"
  "s_waitcnt lgkmcnt(0)\n\t"
  "v_and_b32 v222, 0xffff, v146\n\t"
  "v_and_b32 v223, 0xffff0000, v146\n\t"
  "v_and_b32 v224, 0xffff, v147\n\t"
  "v_and_b32 v225, 0xffff0000, v147\n\t"
  "v_cvt_pk_bf16_f32 v194, v194, v195\n\t"
  "v_cvt_pk_bf16_f32 v196, v196, v197\n\t"
  "v_cvt_pk_bf16_f32 v198, v198, v199\n\t"
  "v_cvt_pk_bf16_f32 v200, v200, v201\n\t"
  "v_cvt_pk_bf16_f32 v202, v202, v203\n\t"
  "v_cvt_pk_bf16_f32 v204, v204, v205\n\t"
  "v_cvt_pk_bf16_f32 v206, v206, v207\n\t"
  "v_cvt_pk_bf16_f32 v208, v208, v209\n\t"
  "v_dot2_f32_bf16 v210, v194, v210, 0\n\t"
  "v_dot2_f32_bf16 v211, v194, v211, 0\n\t"
  "v_dot2_f32_bf16 v212, v196, v212, 0\n\t"
  "v_dot2_f32_bf16 v213, v196, v213, 0\n\t"
  "v_dot2_f32_bf16 v214, v198, v214, 0\n\t"
  "v_dot2_f32_bf16 v215, v198, v215, 0\n\t"
  "v_dot2_f32_bf16 v216, v200, v216, 0\n\t"
  "v_dot2_f32_bf16 v217, v200, v217, 0\n\t"
  "v_dot2_f32_bf16 v218, v202, v218, 0\n\t"
  "v_dot2_f32_bf16 v219, v202, v219, 0\n\t"
  "v_dot2_f32_bf16 v220, v204, v220, 0\n\t"
  "v_dot2_f32_bf16 v221, v204, v221, 0\n\t"
  "v_dot2_f32_bf16 v222, v206, v222, 0\n\t"
  "v_dot2_f32_bf16 v223, v206, v223, 0\n\t"
  "v_dot2_f32_bf16 v224, v208, v224, 0\n\t"
  "v_dot2_f32_bf16 v225, v208, v225, 0\n\t"
  "v_cvt_pk_bf16_f32 v229, v212, v210\n\t"
  "v_cvt_pk_bf16_f32 v228, v216, v214\n\t"
  "v_cvt_pk_bf16_f32 v227, v220, v218\n\t"
  "v_cvt_pk_bf16_f32 v226, v224, v222\n\t"
  "buffer_store_dwordx4 v[226:229], %[vo], %[srd], 0 offen offset:64 sc1\n\t"
  "v_cvt_pk_bf16_f32 v233, v213, v211\n\t"
  "v_cvt_pk_bf16_f32 v232, v217, v215\n\t"
  "v_cvt_pk_bf16_f32 v231, v221, v219\n\t"
  "v_cvt_pk_bf16_f32 v230, v225, v223\n\t"
  "buffer_store_dwordx4 v[230:233], %[vo], %[srd], 0 offen offset:192 sc1\n\t"
  "s_nop 1\n\t"
      :: [srd] "s"(srd), [la] "v"(lb + lkv + lane * 4), [t0] "v"(taps[0]), [t1] "v"(taps[1]), [t2] "v"(taps[2]), [t3] "v"(taps[3]),
         [vo] "v"(OFF_KVT + (tb * 160 + 2 * (d0 >> 3)) * 128 + ((lane ^ (tb >> 1)) & 3) * 16)
      : "memory",
"v112", "v113", "v114", "v115", "v116", "v117", "v118", "v119", "v120", "v121", "v122", "v123", "v124", "v125", "v126", "v127", "v128", "v129", "v130", "v131", "v132", "v133", "v134", "v135", "v136", "v137", "v138", "v139", "v140", "v141", "v142", "v143", "v144", "v145", "v146", "v147", "v148", "v149", "v150", "v151", "v152", "v153", "v154", "v155", "v156", "v157", "v158", "v159", "v160", "v161", "v162", "v163", "v164", "v165", "v166", "v167", "v168", "v169", "v170", "v171", "v172", "v173", "v174", "v175", "v176", "v177", "v178", "v179", "v180", "v181", "v182", "v183", "v184", "v185", "v186", "v187", "v188", "v189", "v190", "v191", "v192", "v193", "v194", "v195", "v196", "v197", "v198", "v199", "v200", "v201", "v202", "v203", "v204", "v205", "v206", "v207", "v208", "v209", "v210", "v211", "v212", "v213", "v214", "v215", "v216", "v217", "v218", "v219", "v220", "v221", "v222", "v223", "v224", "v225", "v226", "v227", "v228", "v229", "v230", "v231", "v232", "v233", "v234"
    );
    STAMP(1);
    char* fcw = smem + L_FC + w * FC_CH;
    auto put = [&](unsigned u, u32x4 a, u32x2 h) {
      const u32 W[6] = {a[0], a[1], a[2], a[3], h[0], h[1]};
      u32 O[5];
#pragma unroll
      for (int i = 0; i < 5; ++i) O[i] = __builtin_amdgcn_alignbit(W[i + 1], W[i], 16);
      *(u32x4*)(fcw + 0 * FC_PITCH + u * 16) = a;
      *(u32x4*)(fcw + 1 * FC_PITCH + u * 16) = u32x4{O[0], O[1], O[2], O[3]};
      *(u32x4*)(fcw + 2 * FC_PITCH + u * 16) = u32x4{W[1], W[2], W[3], W[4]};
      *(u32x4*)(fcw + 3 * FC_PITCH + u * 16) = u32x4{O[1], O[2], O[3], O[4]};
    };
#pragma unroll
    for (int n = 0; n < 4; ++n) {
      u32x2 h;
#pragma unroll
      for (int e = 0; e < 2; ++e) h[e] = __builtin_amdgcn_update_dpp(n < 3 ? rfl(fa[n < 3 ? n + 1 : n][e]) : 0u, fa[n][e], 0x130, 0xf, 0xf, false);
      put(lane + 64 * n + 2, fa[n], h);
    }
    if (lane < 2) put(1 - lane, u32x4{0, 0, 0, 0}, lane == 0 ? u32x2{fa[0][0], fa[0][1]} : u32x2{0, 0});
    FENCE();
#pragma unroll
    for (int n = 0; n < 5; ++n) *(u32x4*)(smem + L_XR + n * 4096 + w * 1024 + lane * 16) = xr[n];
    if (w == 1) *(u32x4*)(smem + L_XR + 20480 + lane * 16) = cr;
    FENCE();
    asm volatile("s_waitcnt vmcnt(0) lgkmcnt(0)\n\ts_barrier" ::: "memory");
    __builtin_amdgcn_raw_buffer_store_b128(u32x4{(u32)tag, (u32)(tag >> 32), (u32)tag, (u32)(tag >> 32)}, srd, tid < 8 ? OFF_FA1 + cb * 128 + tid * 16 : OOB, 0, SC1);
    FENCE();
    STAMP(2);
    if (isx) {
      const unsigned g = lane >> 4, q = (lane >> 2) & 3, ch = 16 * (g >> 1) + (lane & 15);
      asm volatile(
  "s_nop 4\n\t"
  "s_add_u32 s44, %[w], 1\n\t"
  "s_and_b32 s44, s44, 2\n\t"
  "s_cbranch_scc0 Lxs%=\n\t"
  "ds_read_b64_tr_b16 v[96:97], %[vtx] offset:0\n\t"
  "ds_read_b64_tr_b16 v[98:99], %[vtx] offset:512\n\t"
  "ds_read_b64_tr_b16 v[100:101], %[vtx] offset:64\n\t"
  "ds_read_b64_tr_b16 v[102:103], %[vtx] offset:576\n\t"
  "ds_read_b64_tr_b16 v[104:105], %[vtx] offset:2048\n\t"
  "ds_read_b64_tr_b16 v[106:107], %[vtx] offset:2560\n\t"
  "ds_read_b64_tr_b16 v[108:109], %[vtx] offset:2112\n\t"
  "ds_read_b64_tr_b16 v[110:111], %[vtx] offset:2624\n\t"
  "ds_read_b64_tr_b16 v[112:113], %[vtx] offset:4096\n\t"
  "ds_read_b64_tr_b16 v[114:115], %[vtx] offset:4608\n\t"
  "ds_read_b64_tr_b16 v[116:117], %[vtx] offset:4160\n\t"
  "ds_read_b64_tr_b16 v[118:119], %[vtx] offset:4672\n\t"
  "ds_read_b64_tr_b16 v[120:121], %[vtx] offset:6144\n\t"
  "ds_read_b64_tr_b16 v[122:123], %[vtx] offset:6656\n\t"
  "ds_read_b64_tr_b16 v[124:125], %[vtx] offset:6208\n\t"
  "ds_read_b64_tr_b16 v[126:127], %[vtx] offset:6720\n\t"
  "s_add_u32 s44, %[sXo], 0\n\t"
  "s_waitcnt lgkmcnt(14)\n\t"
  "buffer_store_dwordx4 v[96:99], %[vxo], %[srd], s44 offen offset:0 sc1\n\t"
  "ds_read_b64_tr_b16 v[128:129], %[vtx] offset:8192\n\t"
  "ds_read_b64_tr_b16 v[130:131], %[vtx] offset:8704\n\t"
  "s_waitcnt lgkmcnt(14)\n\t"
  "buffer_store_dwordx4 v[100:103], %[vxo], %[srd], s44 offen offset:1024 sc1\n\t"
  "ds_read_b64_tr_b16 v[132:133], %[vtx] offset:8256\n\t"
  "ds_read_b64_tr_b16 v[134:135], %[vtx] offset:8768\n\t"
  "s_waitcnt lgkmcnt(14)\n\t"
  "buffer_store_dwordx4 v[104:107], %[vxo], %[srd], s44 offen offset:2048 sc1\n\t"
  "s_waitcnt lgkmcnt(12)\n\t"
  "buffer_store_dwordx4 v[108:111], %[vxo], %[srd], s44 offen offset:3072 sc1\n\t"
  "s_add_u32 s44, %[sXo], 4096\n\t"
  "s_waitcnt lgkmcnt(10)\n\t"
  "buffer_store_dwordx4 v[112:115], %[vxo], %[srd], s44 offen offset:0 sc1\n\t"
  "s_waitcnt lgkmcnt(8)\n\t"
  "buffer_store_dwordx4 v[116:119], %[vxo], %[srd], s44 offen offset:1024 sc1\n\t"
  "s_waitcnt lgkmcnt(6)\n\t"
  "buffer_store_dwordx4 v[120:123], %[vxo], %[srd], s44 offen offset:2048 sc1\n\t"
  "s_waitcnt lgkmcnt(4)\n\t"
  "buffer_store_dwordx4 v[124:127], %[vxo], %[srd], s44 offen offset:3072 sc1\n\t"
  "s_add_u32 s44, %[sXo], 8192\n\t"
  "s_waitcnt lgkmcnt(2)\n\t"
  "buffer_store_dwordx4 v[128:131], %[vxo], %[srd], s44 offen offset:0 sc1\n\t"
  "s_waitcnt lgkmcnt(0)\n\t"
  "buffer_store_dwordx4 v[132:135], %[vxo], %[srd], s44 offen offset:1024 sc1\n\t"
  "s_branch Lxe%=\n\t"
  "Lxs%=:\n\t"
  "ds_read_b128 v[96:99], %[vsx] offset:0\n\t"
  "ds_read_b128 v[100:103], %[vsx] offset:2048\n\t"
  "ds_read_b128 v[104:107], %[vsx] offset:4096\n\t"
  "ds_read_b128 v[108:111], %[vsx] offset:6144\n\t"
  "ds_read_b128 v[112:115], %[vsx] offset:8192\n\t"
  "ds_read_b128 v[116:119], %[vsx] offset:10240\n\t"
  "ds_read_b128 v[120:123], %[vsx] offset:12288\n\t"
  "ds_read_b128 v[124:127], %[vsx] offset:14336\n\t"
  "ds_read_b128 v[128:131], %[vsx] offset:16384\n\t"
  "ds_read_b128 v[132:135], %[vsx] offset:18432\n\t"
  "v_mov_b32 v184, 0\n\t"
  "v_mov_b32 v185, 0\n\t"
  "v_mov_b32 v186, 0x3f803f80\n\t"
  "s_waitcnt lgkmcnt(9)\n\t"
  "v_dot2c_f32_bf16 v184, v96, v186\n\t"
  "v_dot2c_f32_bf16 v185, v96, v96\n\t"
  "v_dot2c_f32_bf16 v184, v97, v186\n\t"
  "v_dot2c_f32_bf16 v185, v97, v97\n\t"
  "v_dot2c_f32_bf16 v184, v98, v186\n\t"
  "v_dot2c_f32_bf16 v185, v98, v98\n\t"
  "v_dot2c_f32_bf16 v184, v99, v186\n\t"
  "v_dot2c_f32_bf16 v185, v99, v99\n\t"
  "s_waitcnt lgkmcnt(8)\n\t"
  "v_dot2c_f32_bf16 v184, v100, v186\n\t"
  "v_dot2c_f32_bf16 v185, v100, v100\n\t"
  "v_dot2c_f32_bf16 v184, v101, v186\n\t"
  "v_dot2c_f32_bf16 v185, v101, v101\n\t"
  "v_dot2c_f32_bf16 v184, v102, v186\n\t"
  "v_dot2c_f32_bf16 v185, v102, v102\n\t"
  "v_dot2c_f32_bf16 v184, v103, v186\n\t"
  "v_dot2c_f32_bf16 v185, v103, v103\n\t"
  "s_waitcnt lgkmcnt(7)\n\t"
  "v_dot2c_f32_bf16 v184, v104, v186\n\t"
  "v_dot2c_f32_bf16 v185, v104, v104\n\t"
  "v_dot2c_f32_bf16 v184, v105, v186\n\t"
  "v_dot2c_f32_bf16 v185, v105, v105\n\t"
  "v_dot2c_f32_bf16 v184, v106, v186\n\t"
  "v_dot2c_f32_bf16 v185, v106, v106\n\t"
  "v_dot2c_f32_bf16 v184, v107, v186\n\t"
  "v_dot2c_f32_bf16 v185, v107, v107\n\t"
  "s_waitcnt lgkmcnt(6)\n\t"
  "v_dot2c_f32_bf16 v184, v108, v186\n\t"
  "v_dot2c_f32_bf16 v185, v108, v108\n\t"
  "v_dot2c_f32_bf16 v184, v109, v186\n\t"
  "v_dot2c_f32_bf16 v185, v109, v109\n\t"
  "v_dot2c_f32_bf16 v184, v110, v186\n\t"
  "v_dot2c_f32_bf16 v185, v110, v110\n\t"
  "v_dot2c_f32_bf16 v184, v111, v186\n\t"
  "v_dot2c_f32_bf16 v185, v111, v111\n\t"
  "s_waitcnt lgkmcnt(5)\n\t"
  "v_dot2c_f32_bf16 v184, v112, v186\n\t"
  "v_dot2c_f32_bf16 v185, v112, v112\n\t"
  "v_dot2c_f32_bf16 v184, v113, v186\n\t"
  "v_dot2c_f32_bf16 v185, v113, v113\n\t"
  "v_dot2c_f32_bf16 v184, v114, v186\n\t"
  "v_dot2c_f32_bf16 v185, v114, v114\n\t"
  "v_dot2c_f32_bf16 v184, v115, v186\n\t"
  "v_dot2c_f32_bf16 v185, v115, v115\n\t"
  "s_waitcnt lgkmcnt(4)\n\t"
  "v_dot2c_f32_bf16 v184, v116, v186\n\t"
  "v_dot2c_f32_bf16 v185, v116, v116\n\t"
  "v_dot2c_f32_bf16 v184, v117, v186\n\t"
  "v_dot2c_f32_bf16 v185, v117, v117\n\t"
  "v_dot2c_f32_bf16 v184, v118, v186\n\t"
  "v_dot2c_f32_bf16 v185, v118, v118\n\t"
  "v_dot2c_f32_bf16 v184, v119, v186\n\t"
  "v_dot2c_f32_bf16 v185, v119, v119\n\t"
  "s_waitcnt lgkmcnt(3)\n\t"
  "v_dot2c_f32_bf16 v184, v120, v186\n\t"
  "v_dot2c_f32_bf16 v185, v120, v120\n\t"
  "v_dot2c_f32_bf16 v184, v121, v186\n\t"
  "v_dot2c_f32_bf16 v185, v121, v121\n\t"
  "v_dot2c_f32_bf16 v184, v122, v186\n\t"
  "v_dot2c_f32_bf16 v185, v122, v122\n\t"
  "v_dot2c_f32_bf16 v184, v123, v186\n\t"
  "v_dot2c_f32_bf16 v185, v123, v123\n\t"
  "s_waitcnt lgkmcnt(2)\n\t"
  "v_dot2c_f32_bf16 v184, v124, v186\n\t"
  "v_dot2c_f32_bf16 v185, v124, v124\n\t"
  "v_dot2c_f32_bf16 v184, v125, v186\n\t"
  "v_dot2c_f32_bf16 v185, v125, v125\n\t"
  "v_dot2c_f32_bf16 v184, v126, v186\n\t"
  "v_dot2c_f32_bf16 v185, v126, v126\n\t"
  "v_dot2c_f32_bf16 v184, v127, v186\n\t"
  "v_dot2c_f32_bf16 v185, v127, v127\n\t"
  "s_waitcnt lgkmcnt(1)\n\t"
  "v_dot2c_f32_bf16 v184, v128, v186\n\t"
  "v_dot2c_f32_bf16 v185, v128, v128\n\t"
  "v_dot2c_f32_bf16 v184, v129, v186\n\t"
  "v_dot2c_f32_bf16 v185, v129, v129\n\t"
  "v_dot2c_f32_bf16 v184, v130, v186\n\t"
  "v_dot2c_f32_bf16 v185, v130, v130\n\t"
  "v_dot2c_f32_bf16 v184, v131, v186\n\t"
  "v_dot2c_f32_bf16 v185, v131, v131\n\t"
  "s_waitcnt lgkmcnt(0)\n\t"
  "v_dot2c_f32_bf16 v184, v132, v186\n\t"
  "v_dot2c_f32_bf16 v185, v132, v132\n\t"
  "v_dot2c_f32_bf16 v184, v133, v186\n\t"
  "v_dot2c_f32_bf16 v185, v133, v133\n\t"
  "v_dot2c_f32_bf16 v184, v134, v186\n\t"
  "v_dot2c_f32_bf16 v185, v134, v134\n\t"
  "v_dot2c_f32_bf16 v184, v135, v186\n\t"
  "v_dot2c_f32_bf16 v185, v135, v135\n\t"
  "s_nop 1\n\t"
  "v_add_f32_dpp v184, v184, v184 quad_perm:[1,0,3,2] row_mask:0xf bank_mask:0xf\n\t"
  "v_add_f32_dpp v185, v185, v185 quad_perm:[1,0,3,2] row_mask:0xf bank_mask:0xf\n\t"
  "s_nop 0\n\t"
  "v_add_f32_dpp v184, v184, v184 quad_perm:[2,3,0,1] row_mask:0xf bank_mask:0xf\n\t"
  "v_add_f32_dpp v185, v185, v185 quad_perm:[2,3,0,1] row_mask:0xf bank_mask:0xf\n\t"
  "s_nop 0\n\t"
  "v_add_f32_dpp v184, v184, v184 row_half_mirror row_mask:0xf bank_mask:0xf\n\t"
  "v_add_f32_dpp v185, v185, v185 row_half_mirror row_mask:0xf bank_mask:0xf\n\t"
  "v_mul_f32 v184, 0xbacccccd, v184\n\t"
  "v_mul_f32 v185, 0x3acccccd, v185\n\t"
  "v_fma_f32 v185, -v184, v184, v185\n\t"
  "v_max_f32 v185, 0, v185\n\t"
  "v_cvt_pk_bf16_f32 v185, 0, v185\n\t"
  "v_cvt_pk_bf16_f32 v184, 0, v184\n\t"
  "v_add_f32 v185, 0x3727c5ac, v185\n\t"
  "v_cvt_pk_bf16_f32 v185, 0, v185\n\t"
  "v_sqrt_f32 v185, v185\n\t"
  "s_nop 0\n\t"
  "v_cvt_pk_bf16_f32 v185, 0, v185\n\t"
  "v_rcp_f32 v188, v185\n\t"
  "s_nop 0\n\t"
  "v_mul_f32 v189, v184, v188\n\t"
  "s_nop 1\n\t"
  "v_mov_b32_dpp v188, v189 quad_perm:[0,1,2,3] row_mask:0xf bank_mask:0xa\n\t"
  "buffer_store_dword v188, %[vso], %[srd], %[sSo] offen sc1\n\t"
  "Lxe%=:\n\t"
  "s_nop 1\n\t"
        :: [srd] "s"(srd), [w] "s"(w), [sSo] "s"(OFF_ST1 + cb * 128u), [sXo] "s"(OFF_XT + cb * 20480u + (w - 1) * 10240u),
           [vtx] "v"(lb + L_XR + (w - 1) * 10240 + (8 * (g & 1) + q) * 128 + (g >> 1) * 32 + (lane & 3) * 8), [vsx] "v"(lb + L_XR + (w & 1) * 1024 + lane * 16),
           [vso] "v"((lane & 3) == 0 ? (((w & 1) * 4 + g) ^ stkey(cb)) * 16 + ((lane >> 3) & 1) * 4 + (lane & 4) * 2 : OOB), [vxo] "v"((2 * (ch >> 3) + (ch & 1)) * 128 + ((((ch ^ cb) >> 1) & 3) + 4 * (g & 1)) * 16)
        : "memory", "vcc", "scc",
"s44", "v96", "v97", "v98", "v99", "v100", "v101", "v102", "v103", "v104", "v105", "v106", "v107", "v108", "v109", "v110", "v111", "v112", "v113", "v114", "v115", "v116", "v117", "v118", "v119", "v120", "v121", "v122", "v123", "v124", "v125", "v126", "v127", "v128", "v129", "v130", "v131", "v132", "v133", "v134", "v135", "v184", "v185", "v186", "v188", "v189"
      );
    } else if (w == 1) GEMV_UNIT(cb, lb + L_XR, 8);
    stash();
    STAMP(5);
  } else {
    const unsigned lu = lb + L_UN + w * UN_SZ;
    const unsigned g = lane >> 4, q = (lane >> 2) & 3, p4 = lane & 3;
    if (W_DELAY) __builtin_amdgcn_s_sleep(W_DELAY);
    if (role == 2) {
      const unsigned ui = 4 * w2 + w;
      GEMV_LOAD(ui, lu);
      const rsrc_t rd = mkrsrc(wd, D_ * F_ * 2);
      const unsigned r8 = 24 * w + (lane >> 3), so = (96 * (w2 >> 1) * D_ + 320 * (w2 & 1)) * 2;
      u32x4 raw[15];
#pragma unroll
      for (int n = 0; n < 15; ++n) raw[n] = __builtin_amdgcn_raw_buffer_load_b128(rd, r8 * (D_ * 2) + (lane & 7) * 16 + (n % 5) * 128, so + (n / 5) * 8 * D_ * 2, NT);
      FENCE();
      tag = load_tag(ws);
      GEMV_UNIT(ui, lu, 15);
      STAMP(1);
#pragma unroll
      for (int n = 0; n < 15; ++n) *(u32x4*)(smem + r8 * RP2 + (lane & 7) * 16 + (n % 5) * 128 + (n / 5) * 8 * RP2) = raw[n];
      FENCE();
      asm volatile("s_waitcnt vmcnt(0)" ::: "memory");
      put_flag(srd, U_FLAG(ui), lane, tag);
      STAMP(2);
      BAR();
#pragma unroll
      for (int n = 0; n < 15; ++n) {
        const int i = n % 5;
        const char* a = smem + (32 * (n / 5) + 8 * g + q) * RP2 + (80 * w + (i < 4 ? 32 * (i >> 1) + 8 * p4 + 4 * (i & 1) : 64 + 4 * p4)) * 2;
        const u32x2 lo = tr_read(a), hi = tr_read(a + 4 * RP2);
        raw[n] = u32x4{lo[0], lo[1], hi[0], hi[1]};
      }
      FENCE();
#pragma unroll
      for (int n = 0; n < 15; ++n)
        __builtin_amdgcn_raw_buffer_store_b128(raw[n], srd, lane * 16 + (n & 3) * 1024, OFF_W2F + ((4 * (w2 & 1) + w) * 240 + 15 * (w2 >> 1) + (n & ~3)) * 1024, SC1);
      FENCE();
      STAMP(7);
      stash();
      if (w2 < 10 && w == 0) {
        poll_flags<1, 4>(srd, OFF_FA2 + ((lane & 3) * 60 + 40 + w2) * 128, tag);
        const float sc = rbf(1.f + modval(srd, 4 * 640 + lane + 64 * w2));
        const u32 rv = __float_as_uint(sc == 0.f ? 0x1p40f : 1.f / sc);
        __builtin_amdgcn_raw_buffer_store_b32(rv, srd, lane * 4, OFF_RT + w2 * 256, SC1);
        __builtin_amdgcn_raw_buffer_store_b32(rv, srd, lane * 48, OFF_RL + (w2 < 2 ? w2 : w2 + 2) * 4, SC1);
      }
    } else {
      const rsrc_t rg = mkrsrc(wg, D_ * F_ * 2), ru = mkrsrc(wu, D_ * F_ * 2);
      const unsigned rw = 5 * w + lane / 12, ck = lane % 12, ui = 160 + 64 * w + w1;
      const unsigned vo = lane < 60 ? rw * (F_ * 2) + ck * 16 : OOB, so = (160 * (w1 & 3) * F_ + 96 * (w1 >> 2)) * 2;
      u32x4 raw[16];
#pragma unroll
      for (int n = 0; n < 8; ++n) {
        raw[2 * n] = __builtin_amdgcn_raw_buffer_load_b128(rg, vo, so + n * 20 * F_ * 2, NT);
        raw[2 * n + 1] = __builtin_amdgcn_raw_buffer_load_b128(ru, vo, so + n * 20 * F_ * 2, NT);
      }
      FENCE();
      tag = load_tag(ws);
      char* o = smem + (lane < 60 ? rw * RP1 + ck * 32 : 384);
      u32x4 fr[15];
#pragma unroll
      for (int h = 0; h < 2; ++h) {
#pragma unroll
        for (int n = h ? 5 : 0; n < (h ? 8 : 5); ++n) {
          *(u32x4*)(o + n * 20 * RP1) = u32x4{raw[2 * n][0], raw[2 * n + 1][0], raw[2 * n][1], raw[2 * n + 1][1]};
          *(u32x4*)(o + n * 20 * RP1 + (lane < 60 ? 16 : 0)) = u32x4{raw[2 * n][2], raw[2 * n + 1][2], raw[2 * n][3], raw[2 * n + 1][3]};
        }
        BAR();
#pragma unroll
        for (int n = h ? 9 : 0; n < (h ? 15 : 9); ++n) {
          const int ks = n / 3, i = n % 3;
          const char* a = smem + (32 * ks + 4 * g + q) * RP1 + (12 * w + (p4 < 3 ? 4 * p4 + i : 4 * i + 3)) * 8;
          const u32x2 lo = tr_read(a), hi = tr_read(a + 16 * RP1);
          fr[n] = u32x4{lo[0], lo[1], hi[0], hi[1]};
        }
        FENCE();
#pragma unroll
        for (int n = h ? 9 : 0; n < (h ? 15 : 9); ++n)
          __builtin_amdgcn_raw_buffer_store_b128(fr[n], srd, lane * 16 + (n & 3) * 1024, OFF_W1F + (((w1 & ~3u) + w) * 60 + 15 * (w1 & 3) + (n & ~3)) * 1024, SC1);
        FENCE();
      }
      STAMP(6);
      if (ui < 240) { GEMV_LOAD(ui, lu); asm volatile("s_waitcnt vmcnt(21)" ::: "memory"); } else asm volatile("s_waitcnt vmcnt(0)" ::: "memory");
      asm volatile("s_barrier" ::: "memory");
      if (w == 3) put_flag(srd, OFF_FW1 + w1 * 128, lane, tag);
      STAMP(7);
      if (ui < 240) {
        GEMV_UNIT(ui, lu, 0);
        asm volatile("s_waitcnt vmcnt(0)" ::: "memory");
        put_flag(srd, U_FLAG(ui), lane, tag);
        STAMP(2);
      }
      poll_flags<1, 4>(srd, OFF_FW1 + (j * 4 + (lane & 3)) * 128, tag);
      asm volatile(
  "s_nop 4\n\t"
  "s_add_u32 s44, %[sW1], 0\n\t"
  "buffer_load_dwordx4 a[0:3], %[vl16], %[srd], s44 offen offset:0 sc1\n\t"
  "buffer_load_dwordx4 a[4:7], %[vl16], %[srd], s44 offen offset:1024 sc1\n\t"
  "buffer_load_dwordx4 a[8:11], %[vl16], %[srd], s44 offen offset:2048 sc1\n\t"
  "buffer_load_dwordx4 a[12:15], %[vl16], %[srd], s44 offen offset:3072 sc1\n\t"
  "s_add_u32 s44, %[sW1], 4096\n\t"
  "buffer_load_dwordx4 a[16:19], %[vl16], %[srd], s44 offen offset:0 sc1\n\t"
  "buffer_load_dwordx4 a[20:23], %[vl16], %[srd], s44 offen offset:1024 sc1\n\t"
  "buffer_load_dwordx4 a[24:27], %[vl16], %[srd], s44 offen offset:2048 sc1\n\t"
  "buffer_load_dwordx4 a[28:31], %[vl16], %[srd], s44 offen offset:3072 sc1\n\t"
  "s_add_u32 s44, %[sW1], 8192\n\t"
  "buffer_load_dwordx4 a[32:35], %[vl16], %[srd], s44 offen offset:0 sc1\n\t"
  "buffer_load_dwordx4 a[36:39], %[vl16], %[srd], s44 offen offset:1024 sc1\n\t"
  "buffer_load_dwordx4 a[40:43], %[vl16], %[srd], s44 offen offset:2048 sc1\n\t"
  "buffer_load_dwordx4 a[44:47], %[vl16], %[srd], s44 offen offset:3072 sc1\n\t"
  "s_add_u32 s44, %[sW1], 12288\n\t"
  "buffer_load_dwordx4 a[48:51], %[vl16], %[srd], s44 offen offset:0 sc1\n\t"
  "buffer_load_dwordx4 a[52:55], %[vl16], %[srd], s44 offen offset:1024 sc1\n\t"
  "buffer_load_dwordx4 a[56:59], %[vl16], %[srd], s44 offen offset:2048 sc1\n\t"
  "buffer_load_dwordx4 a[60:63], %[vl16], %[srd], s44 offen offset:3072 sc1\n\t"
  "s_add_u32 s44, %[sW1], 16384\n\t"
  "buffer_load_dwordx4 a[64:67], %[vl16], %[srd], s44 offen offset:0 sc1\n\t"
  "buffer_load_dwordx4 a[68:71], %[vl16], %[srd], s44 offen offset:1024 sc1\n\t"
  "buffer_load_dwordx4 a[72:75], %[vl16], %[srd], s44 offen offset:2048 sc1\n\t"
  "buffer_load_dwordx4 a[76:79], %[vl16], %[srd], s44 offen offset:3072 sc1\n\t"
  "s_add_u32 s44, %[sW1], 20480\n\t"
  "buffer_load_dwordx4 a[80:83], %[vl16], %[srd], s44 offen offset:0 sc1\n\t"
  "buffer_load_dwordx4 a[84:87], %[vl16], %[srd], s44 offen offset:1024 sc1\n\t"
  "buffer_load_dwordx4 a[88:91], %[vl16], %[srd], s44 offen offset:2048 sc1\n\t"
  "buffer_load_dwordx4 a[92:95], %[vl16], %[srd], s44 offen offset:3072 sc1\n\t"
  "s_add_u32 s44, %[sW1], 24576\n\t"
  "buffer_load_dwordx4 a[96:99], %[vl16], %[srd], s44 offen offset:0 sc1\n\t"
  "buffer_load_dwordx4 a[100:103], %[vl16], %[srd], s44 offen offset:1024 sc1\n\t"
  "buffer_load_dwordx4 a[104:107], %[vl16], %[srd], s44 offen offset:2048 sc1\n\t"
  "buffer_load_dwordx4 a[108:111], %[vl16], %[srd], s44 offen offset:3072 sc1\n\t"
  "s_add_u32 s44, %[sW1], 28672\n\t"
  "buffer_load_dwordx4 a[112:115], %[vl16], %[srd], s44 offen offset:0 sc1\n\t"
  "buffer_load_dwordx4 a[116:119], %[vl16], %[srd], s44 offen offset:1024 sc1\n\t"
  "buffer_load_dwordx4 a[120:123], %[vl16], %[srd], s44 offen offset:2048 sc1\n\t"
  "buffer_load_dwordx4 a[124:127], %[vl16], %[srd], s44 offen offset:3072 sc1\n\t"
  "s_add_u32 s44, %[sW1], 32768\n\t"
  "buffer_load_dwordx4 a[128:131], %[vl16], %[srd], s44 offen offset:0 sc1\n\t"
  "buffer_load_dwordx4 a[132:135], %[vl16], %[srd], s44 offen offset:1024 sc1\n\t"
  "buffer_load_dwordx4 a[136:139], %[vl16], %[srd], s44 offen offset:2048 sc1\n\t"
  "buffer_load_dwordx4 a[140:143], %[vl16], %[srd], s44 offen offset:3072 sc1\n\t"
  "s_add_u32 s44, %[sW1], 36864\n\t"
  "buffer_load_dwordx4 a[144:147], %[vl16], %[srd], s44 offen offset:0 sc1\n\t"
  "buffer_load_dwordx4 a[148:151], %[vl16], %[srd], s44 offen offset:1024 sc1\n\t"
  "buffer_load_dwordx4 a[152:155], %[vl16], %[srd], s44 offen offset:2048 sc1\n\t"
  "buffer_load_dwordx4 a[156:159], %[vl16], %[srd], s44 offen offset:3072 sc1\n\t"
  "s_add_u32 s44, %[sW1], 40960\n\t"
  "buffer_load_dwordx4 a[160:163], %[vl16], %[srd], s44 offen offset:0 sc1\n\t"
  "buffer_load_dwordx4 a[164:167], %[vl16], %[srd], s44 offen offset:1024 sc1\n\t"
  "buffer_load_dwordx4 a[168:171], %[vl16], %[srd], s44 offen offset:2048 sc1\n\t"
  "buffer_load_dwordx4 a[172:175], %[vl16], %[srd], s44 offen offset:3072 sc1\n\t"
  "s_add_u32 s44, %[sW1], 45056\n\t"
  "buffer_load_dwordx4 a[176:179], %[vl16], %[srd], s44 offen offset:0 sc1\n\t"
  "buffer_load_dwordx4 a[180:183], %[vl16], %[srd], s44 offen offset:1024 sc1\n\t"
  "buffer_load_dwordx4 a[184:187], %[vl16], %[srd], s44 offen offset:2048 sc1\n\t"
  "buffer_load_dwordx4 a[188:191], %[vl16], %[srd], s44 offen offset:3072 sc1\n\t"
  "s_add_u32 s44, %[sW1], 49152\n\t"
  "buffer_load_dwordx4 a[192:195], %[vl16], %[srd], s44 offen offset:0 sc1\n\t"
  "buffer_load_dwordx4 a[196:199], %[vl16], %[srd], s44 offen offset:1024 sc1\n\t"
  "buffer_load_dwordx4 a[200:203], %[vl16], %[srd], s44 offen offset:2048 sc1\n\t"
  "buffer_load_dwordx4 a[204:207], %[vl16], %[srd], s44 offen offset:3072 sc1\n\t"
  "s_add_u32 s44, %[sW1], 53248\n\t"
  "buffer_load_dwordx4 a[208:211], %[vl16], %[srd], s44 offen offset:0 sc1\n\t"
  "buffer_load_dwordx4 a[212:215], %[vl16], %[srd], s44 offen offset:1024 sc1\n\t"
  "buffer_load_dwordx4 a[216:219], %[vl16], %[srd], s44 offen offset:2048 sc1\n\t"
  "buffer_load_dwordx4 a[220:223], %[vl16], %[srd], s44 offen offset:3072 sc1\n\t"
  "s_add_u32 s44, %[sW1], 57344\n\t"
  "buffer_load_dwordx4 a[224:227], %[vl16], %[srd], s44 offen offset:0 sc1\n\t"
  "buffer_load_dwordx4 a[228:231], %[vl16], %[srd], s44 offen offset:1024 sc1\n\t"
  "buffer_load_dwordx4 a[232:235], %[vl16], %[srd], s44 offen offset:2048 sc1\n\t"
  "buffer_load_dwordx4 a[236:239], %[vl16], %[srd], s44 offen offset:3072 sc1\n\t"
        :: [srd] "s"(srd), [sW1] "s"(OFF_W1F + (j * 4 + w) * 61440u), [vl16] "v"(lane * 16) : "memory", "scc",
"a0", "a1", "a2", "a3", "a4", "a5", "a6", "a7", "a8", "a9", "a10", "a11", "a12", "a13", "a14", "a15", "a16", "a17", "a18", "a19", "a20", "a21", "a22", "a23", "a24", "a25", "a26", "a27", "a28", "a29", "a30", "a31", "a32", "a33", "a34", "a35", "a36", "a37", "a38", "a39", "a40", "a41", "a42", "a43", "a44", "a45", "a46", "a47", "a48", "a49", "a50", "a51", "a52", "a53", "a54", "a55", "a56", "a57", "a58", "a59", "a60", "a61", "a62", "a63", "a64", "a65", "a66", "a67", "a68", "a69", "a70", "a71", "a72", "a73", "a74", "a75", "a76", "a77", "a78", "a79", "a80", "a81", "a82", "a83", "a84", "a85", "a86", "a87", "a88", "a89", "a90", "a91", "a92", "a93", "a94", "a95", "a96", "a97", "a98", "a99", "a100", "a101", "a102", "a103", "a104", "a105", "a106", "a107", "a108", "a109", "a110", "a111", "a112", "a113", "a114", "a115", "a116", "a117", "a118", "a119", "a120", "a121", "a122", "a123", "a124", "a125", "a126", "a127", "a128", "a129", "a130", "a131", "a132", "a133", "a134", "a135", "a136", "a137", "a138", "a139", "a140", "a141", "a142", "a143", "a144", "a145", "a146", "a147", "a148", "a149", "a150", "a151", "a152", "a153", "a154", "a155", "a156", "a157", "a158", "a159", "a160", "a161", "a162", "a163", "a164", "a165", "a166", "a167", "a168", "a169", "a170", "a171", "a172", "a173", "a174", "a175", "a176", "a177", "a178", "a179", "a180", "a181", "a182", "a183", "a184", "a185", "a186", "a187", "a188", "a189", "a190", "a191", "a192", "a193", "a194", "a195", "a196", "a197", "a198", "a199", "a200", "a201", "a202", "a203", "a204", "a205", "a206", "a207", "a208", "a209", "a210", "a211", "a212", "a213", "a214", "a215", "a216", "a217", "a218", "a219", "a220", "a221", "a222", "a223", "a224", "a225", "a226", "a227", "a228", "a229", "a230", "a231", "a232", "a233", "a234", "a235", "a236", "a237", "a238", "a239", "s44"
      );
      wdone = 7;
      stash();
      if (w > 1 || (w == 1 && w1 < 4)) {
        const unsigned c0 = (6 - w) * 640 + 160 * (w1 & 3);
        poll_flags<1, 4>(srd, OFF_FA2 + ((lane & 3) * 60 + ((c0 + min(64 * ((lane >> 2) & 3), 159u)) >> 6)) * 128, tag);
        f32x4 m[4];
#pragma unroll
        for (int k = 0; k < 4; ++k) m[k] = __builtin_bit_cast(f32x4, __builtin_amdgcn_raw_buffer_load_b128(srd, lane < 40 ? lane * 16 : OOB, OFF_MODP + k * 15360 + c0 * 4, SC1));
        f32x4 v = (m[0] + m[1]) + (m[2] + m[3]);
#pragma unroll
        for (int e = 0; e < 4; ++e) v[e] = rbf(v[e]);
        if (w == 1) __builtin_amdgcn_raw_buffer_store_b128(__builtin_bit_cast(u32x4, v), srd, lane < 40 ? lane * 16 : OOB, OFF_GB + 640 * w1, SC1);
        else {
          const float of = w == 2 ? 1.f : 0.f;
          *(u32x2*)(smem + L_WS + (w - 2) * 1024 + lane * 8) = u32x2{pack_bf16(of + v[0], of + v[1]), pack_bf16(of + v[2], of + v[3])};
        }
      }
      STAMP(5);
      BAR();
      STAMP(3);
      f32x4 aU[3], aB[3];
#pragma unroll
      for (int i = 0; i < 3; ++i) { aU[i] = f32x4{0, 0, 0, 0}; aB[i] = f32x4{0, 0, 0, 0}; }
#pragma unroll
      for (int ks = 0; ks < 5; ++ks) {
        const char* t = smem + L_WS + (32 * ks + 4 * g) * 2;
        const u32x2 s0 = *(const u32x2*)t, s1 = *(const u32x2*)(t + 32), b0 = *(const u32x2*)(t + 1024), b1 = *(const u32x2*)(t + 1056);
#pragma unroll
        for (int i = 0; i < 3; ++i) {
          mfma_v(aU[i], u32x4{s0[0], s0[1], s1[0], s1[1]}, fr[3 * ks + i]);
          mfma_v(aB[i], u32x4{b0[0], b0[1], b1[0], b1[1]}, fr[3 * ks + i]);
        }
      }
      asm volatile("s_nop 7\n\ts_nop 1" : "+v"(aU[0]), "+v"(aU[1]), "+v"(aU[2]), "+v"(aB[0]), "+v"(aB[1]), "+v"(aB[2]));
      const float u = g == 0 ? aU[0][0] : g == 1 ? aU[1][0] : aU[2][0], bb = g == 0 ? aB[0][0] : g == 1 ? aB[1][0] : aB[2][0];
      const float uh = rbf(u), bh = rbf(bb);
      __builtin_amdgcn_raw_buffer_store_b64(u32x2{pack_bf16(uh, u - uh), pack_bf16(bh, bb - bh)}, srd,
                                            g < 3 ? OFF_UB + ((((w1 & ~3u) + w) * 3 + g) * 64 + (w1 & 3) * 16 + (lane & 15)) * 8 : OOB, 0, SC1);
    }
  }
  if (role == 0) {
    const unsigned d = 8 * (cb >> 1) + 2 * w + (cb & 1);
    asm volatile(
  "s_nop 4\n\t"
  "buffer_load_dwordx2 v[160:161], %[vf1], %[srd], 0 offen sc1\n\t"
  "s_lshl_b32 s45, %[w], 12\n\t"
  "v_mov_b32 v252, %[tlo]\n\t"
  "v_mov_b32 v253, %[thi]\n\t"
  "v_mov_b32 v254, %[tlo]\n\t"
  "v_mov_b32 v255, %[thi]\n\t"
  "v_mov_b32 v162, 140288\n\t"
  "v_mov_b32 v163, 1\n\t"
  "s_mov_b64 exec, 1\n\t"
  "s_waitcnt vmcnt(1)\n\t"
  "ds_add_rtn_u32 v164, v162, v163\n\t"
  "s_waitcnt lgkmcnt(0)\n\t"
  "s_mov_b64 exec, -1\n\t"
  "v_readfirstlane_b32 s44, v164\n\t"
  "s_nop 3\n\t"
  "s_cmp_lg_u32 s44, 3\n\t"
  "s_cbranch_scc1 Lnox%=\n\t"
  "buffer_store_dwordx4 v[252:255], %[vf3], %[srd], 0 offen sc1\n\t"
  "s_waitcnt vmcnt(1)\n\t"
  "s_branch Lx%=\n\t"
  "Lnox%=:\n\t"
  "s_waitcnt vmcnt(0)\n\t"
  "Lx%=:\n\t"
  "v_cmp_ne_u64 vcc, v[252:253], v[160:161]\n\t"
  "s_nop 1\n\t"
  "s_cbranch_vccnz La1_slow%=\n\t"
  "La1_back%=:\n\t"
  "s_add_u32 m0, s45, 68608\n\t"
  "s_add_u32 s44, %[sKV], 0\n\t"
  "buffer_load_dwordx4 %[vkv], %[srd], s44 offen sc1 lds\n\t"
  "s_add_u32 m0, s45, 69632\n\t"
  "s_add_u32 s44, %[sKV], 163840\n\t"
  "buffer_load_dwordx4 %[vkv], %[srd], s44 offen sc1 lds\n\t"
  "s_add_u32 m0, s45, 70656\n\t"
  "s_add_u32 s44, %[sKV], 327680\n\t"
  "buffer_load_dwordx4 %[vkv], %[srd], s44 offen sc1 lds\n\t"
  "s_add_u32 m0, s45, 71680\n\t"
  "s_add_u32 s44, %[sKV], 491520\n\t"
  "buffer_load_dwordx4 %[vkv], %[srd], s44 offen sc1 lds\n\t"
  "ds_read_b64 v[64:65], %[va] offset:0\n\t"
  "ds_read_b64 v[66:67], %[va] offset:8\n\t"
  "ds_read_b64 v[68:69], %[va] offset:512\n\t"
  "ds_read_b64 v[70:71], %[va] offset:520\n\t"
  "ds_read_b64 v[72:73], %[va] offset:1024\n\t"
  "ds_read_b64 v[74:75], %[va] offset:1032\n\t"
  "ds_read_b64 v[76:77], %[va] offset:1536\n\t"
  "ds_read_b64 v[78:79], %[va] offset:1544\n\t"
  "ds_read_b64 v[80:81], %[va] offset:2048\n\t"
  "ds_read_b64 v[82:83], %[va] offset:2056\n\t"
  "ds_read_b64 v[84:85], %[va] offset:2560\n\t"
  "ds_read_b64 v[86:87], %[va] offset:2568\n\t"
  "ds_read_b64 v[88:89], %[va] offset:3072\n\t"
  "ds_read_b64 v[90:91], %[va] offset:3080\n\t"
  "ds_read_b64 v[92:93], %[va] offset:3584\n\t"
  "ds_read_b64 v[94:95], %[va] offset:3592\n\t"
  "s_waitcnt vmcnt(0) lgkmcnt(0)\n\t"
  "s_barrier\n\t"
  "ds_read_b128 v[124:127], %[vb0] offset:15360\n\t"
  "ds_read_b128 v[120:123], %[vb0] offset:13312\n\t"
  "ds_read_b128 v[116:119], %[vb0] offset:11264\n\t"
  "ds_read_b128 v[112:115], %[vb0] offset:9216\n\t"
  "ds_read_b128 v[108:111], %[vb0] offset:7168\n\t"
  "ds_read_b128 v[104:107], %[vb0] offset:5120\n\t"
  "ds_read_b128 v[100:103], %[vb0] offset:3072\n\t"
  "ds_read_b128 v[96:99], %[vb0] offset:1024\n\t"
  "s_waitcnt lgkmcnt(7)\n\t"
  "v_mfma_f32_16x16x32_bf16 v[60:63], v[64:67], v[124:127], 0\n\t"
  "ds_read_b128 v[156:159], %[vb1] offset:15360\n\t"
  "s_waitcnt lgkmcnt(7)\n\t"
  "v_mfma_f32_16x16x32_bf16 v[56:59], v[64:67], v[120:123], 0\n\t"
  "s_waitcnt lgkmcnt(6)\n\t"
  "v_mfma_f32_16x16x32_bf16 v[52:55], v[64:67], v[116:119], 0\n\t"
  "s_waitcnt lgkmcnt(5)\n\t"
  "v_mfma_f32_16x16x32_bf16 v[48:51], v[64:67], v[112:115], 0\n\t"
  "s_waitcnt lgkmcnt(4)\n\t"
  "v_mfma_f32_16x16x32_bf16 v[44:47], v[64:67], v[108:111], 0\n\t"
  "ds_read_b128 v[152:155], %[vb1] offset:13312\n\t"
  "s_waitcnt lgkmcnt(4)\n\t"
  "v_mfma_f32_16x16x32_bf16 v[40:43], v[64:67], v[104:107], 0\n\t"
  "s_waitcnt lgkmcnt(3)\n\t"
  "v_mfma_f32_16x16x32_bf16 v[36:39], v[64:67], v[100:103], 0\n\t"
  "s_waitcnt lgkmcnt(2)\n\t"
  "v_mfma_f32_16x16x32_bf16 v[32:35], v[64:67], v[96:99], 0\n\t"
  "v_mfma_f32_16x16x32_bf16 v[60:63], %[vd], v[124:127], v[60:63]\n\t"
  "ds_read_b128 v[148:151], %[vb1] offset:11264\n\t"
  "v_mfma_f32_16x16x32_bf16 v[56:59], %[vd], v[120:123], v[56:59]\n\t"
  "v_mfma_f32_16x16x32_bf16 v[52:55], %[vd], v[116:119], v[52:55]\n\t"
  "v_mfma_f32_16x16x32_bf16 v[48:51], %[vd], v[112:115], v[48:51]\n\t"
  "v_mfma_f32_16x16x32_bf16 v[44:47], %[vd], v[108:111], v[44:47]\n\t"
  "ds_read_b128 v[144:147], %[vb1] offset:9216\n\t"
  "v_mfma_f32_16x16x32_bf16 v[40:43], %[vd], v[104:107], v[40:43]\n\t"
  "v_mfma_f32_16x16x32_bf16 v[36:39], %[vd], v[100:103], v[36:39]\n\t"
  "v_mfma_f32_16x16x32_bf16 v[32:35], %[vd], v[96:99], v[32:35]\n\t"
  "ds_read_b64 v[64:65], %[va] offset:64\n\t"
  "ds_read_b64 v[66:67], %[va] offset:72\n\t"
  "v_mfma_f32_16x16x32_bf16 v[60:63], v[68:71], v[120:123], v[60:63]\n\t"
  "ds_read_b128 v[140:143], %[vb1] offset:7168\n\t"
  "v_mfma_f32_16x16x32_bf16 v[56:59], v[68:71], v[116:119], v[56:59]\n\t"
  "v_mfma_f32_16x16x32_bf16 v[52:55], v[68:71], v[112:115], v[52:55]\n\t"
  "v_mfma_f32_16x16x32_bf16 v[48:51], v[68:71], v[108:111], v[48:51]\n\t"
  "v_mfma_f32_16x16x32_bf16 v[44:47], v[68:71], v[104:107], v[44:47]\n\t"
  "ds_read_b128 v[136:139], %[vb1] offset:5120\n\t"
  "v_mfma_f32_16x16x32_bf16 v[40:43], v[68:71], v[100:103], v[40:43]\n\t"
  "v_mfma_f32_16x16x32_bf16 v[36:39], v[68:71], v[96:99], v[36:39]\n\t"
  "ds_read_b64 v[68:69], %[va] offset:576\n\t"
  "ds_read_b64 v[70:71], %[va] offset:584\n\t"
  "v_mfma_f32_16x16x32_bf16 v[60:63], v[72:75], v[116:119], v[60:63]\n\t"
  "v_mfma_f32_16x16x32_bf16 v[56:59], v[72:75], v[112:115], v[56:59]\n\t"
  "ds_read_b128 v[132:135], %[vb1] offset:3072\n\t"
  "buffer_load_dwordx2 v[250:251], %[vfw], %[srd], 0 offen sc1\n\t"
  "v_mfma_f32_16x16x32_bf16 v[52:55], v[72:75], v[108:111], v[52:55]\n\t"
  "v_mfma_f32_16x16x32_bf16 v[48:51], v[72:75], v[104:107], v[48:51]\n\t"
  "v_mfma_f32_16x16x32_bf16 v[44:47], v[72:75], v[100:103], v[44:47]\n\t"
  "v_mfma_f32_16x16x32_bf16 v[40:43], v[72:75], v[96:99], v[40:43]\n\t"
  "ds_read_b64 v[72:73], %[va] offset:1088\n\t"
  "ds_read_b128 v[128:131], %[vb1] offset:1024\n\t"
  "v_mfma_f32_16x16x32_bf16 v[60:63], v[76:79], v[112:115], v[60:63]\n\t"
  "ds_read_b64 v[74:75], %[va] offset:1096\n\t"
  "v_mfma_f32_16x16x32_bf16 v[56:59], v[76:79], v[108:111], v[56:59]\n\t"
  "v_mfma_f32_16x16x32_bf16 v[52:55], v[76:79], v[104:107], v[52:55]\n\t"
  "v_mfma_f32_16x16x32_bf16 v[48:51], v[76:79], v[100:103], v[48:51]\n\t"
  "v_mfma_f32_16x16x32_bf16 v[44:47], v[76:79], v[96:99], v[44:47]\n\t"
  "ds_read_b64 v[76:77], %[va] offset:1600\n\t"
  "ds_read_b64 v[78:79], %[va] offset:1608\n\t"
  "v_mfma_f32_16x16x32_bf16 v[60:63], v[80:83], v[108:111], v[60:63]\n\t"
  "v_mfma_f32_16x16x32_bf16 v[56:59], v[80:83], v[104:107], v[56:59]\n\t"
  "v_mfma_f32_16x16x32_bf16 v[52:55], v[80:83], v[100:103], v[52:55]\n\t"
  "v_mfma_f32_16x16x32_bf16 v[48:51], v[80:83], v[96:99], v[48:51]\n\t"
  "ds_read_b64 v[80:81], %[va] offset:2112\n\t"
  "ds_read_b64 v[82:83], %[va] offset:2120\n\t"
  "v_mfma_f32_16x16x32_bf16 v[60:63], v[84:87], v[104:107], v[60:63]\n\t"
  "v_mfma_f32_16x16x32_bf16 v[56:59], v[84:87], v[100:103], v[56:59]\n\t"
  "v_mfma_f32_16x16x32_bf16 v[52:55], v[84:87], v[96:99], v[52:55]\n\t"
  "ds_read_b64 v[84:85], %[va] offset:2624\n\t"
  "ds_read_b64 v[86:87], %[va] offset:2632\n\t"
  "v_mfma_f32_16x16x32_bf16 v[60:63], v[88:91], v[100:103], v[60:63]\n\t"
  "v_mfma_f32_16x16x32_bf16 v[56:59], v[88:91], v[96:99], v[56:59]\n\t"
  "ds_read_b64 v[88:89], %[va] offset:3136\n\t"
  "ds_read_b64 v[90:91], %[va] offset:3144\n\t"
  "v_mfma_f32_16x16x32_bf16 v[60:63], v[92:95], v[96:99], v[60:63]\n\t"
  "s_waitcnt lgkmcnt(9)\n\t"
  "ds_read_b64 v[92:93], %[va] offset:3648\n\t"
  "v_mfma_f32_16x16x32_bf16 v[60:63], v[64:67], v[156:159], v[60:63]\n\t"
  "ds_read_b128 v[124:127], %[vb2] offset:15360\n\t"
  "ds_read_b64 v[94:95], %[va] offset:3656\n\t"
  "v_mfma_f32_16x16x32_bf16 v[56:59], v[64:67], v[152:155], v[56:59]\n\t"
  "v_mfma_f32_16x16x32_bf16 v[52:55], v[64:67], v[148:151], v[52:55]\n\t"
  "v_mfma_f32_16x16x32_bf16 v[48:51], v[64:67], v[144:147], v[48:51]\n\t"
  "ds_read_b128 v[120:123], %[vb2] offset:13312\n\t"
  "v_mfma_f32_16x16x32_bf16 v[44:47], v[64:67], v[140:143], v[44:47]\n\t"
  "v_mfma_f32_16x16x32_bf16 v[40:43], v[64:67], v[136:139], v[40:43]\n\t"
  "v_mfma_f32_16x16x32_bf16 v[36:39], v[64:67], v[132:135], v[36:39]\n\t"
  "ds_read_b128 v[116:119], %[vb2] offset:11264\n\t"
  "v_mfma_f32_16x16x32_bf16 v[32:35], v[64:67], v[128:131], v[32:35]\n\t"
  "ds_read_b64 v[64:65], %[va] offset:128\n\t"
  "ds_read_b64 v[66:67], %[va] offset:136\n\t"
  "v_mfma_f32_16x16x32_bf16 v[60:63], v[68:71], v[152:155], v[60:63]\n\t"
  "v_mfma_f32_16x16x32_bf16 v[56:59], v[68:71], v[148:151], v[56:59]\n\t"
  "ds_read_b128 v[112:115], %[vb2] offset:9216\n\t"
  "v_mfma_f32_16x16x32_bf16 v[52:55], v[68:71], v[144:147], v[52:55]\n\t"
  "v_mfma_f32_16x16x32_bf16 v[48:51], v[68:71], v[140:143], v[48:51]\n\t"
  "v_mfma_f32_16x16x32_bf16 v[44:47], v[68:71], v[136:139], v[44:47]\n\t"
  "ds_read_b128 v[108:111], %[vb2] offset:7168\n\t"
  "v_mfma_f32_16x16x32_bf16 v[40:43], v[68:71], v[132:135], v[40:43]\n\t"
  "v_mfma_f32_16x16x32_bf16 v[36:39], v[68:71], v[128:131], v[36:39]\n\t"
  "s_waitcnt lgkmcnt(11)\n\t"
  "ds_read_b64 v[68:69], %[va] offset:640\n\t"
  "v_mfma_f32_16x16x32_bf16 v[60:63], v[72:75], v[148:151], v[60:63]\n\t"
  "ds_read_b128 v[104:107], %[vb2] offset:5120\n\t"
  "ds_read_b64 v[70:71], %[va] offset:648\n\t"
  "v_mfma_f32_16x16x32_bf16 v[56:59], v[72:75], v[144:147], v[56:59]\n\t"
  "v_mfma_f32_16x16x32_bf16 v[52:55], v[72:75], v[140:143], v[52:55]\n\t"
  "v_mfma_f32_16x16x32_bf16 v[48:51], v[72:75], v[136:139], v[48:51]\n\t"
  "ds_read_b128 v[100:103], %[vb2] offset:3072\n\t"
  "v_mfma_f32_16x16x32_bf16 v[44:47], v[72:75], v[132:135], v[44:47]\n\t"
  "v_mfma_f32_16x16x32_bf16 v[40:43], v[72:75], v[128:131], v[40:43]\n\t"
  "ds_read_b64 v[72:73], %[va] offset:1152\n\t"
  "ds_read_b64 v[74:75], %[va] offset:1160\n\t"
  "v_mfma_f32_16x16x32_bf16 v[60:63], v[76:79], v[144:147], v[60:63]\n\t"
  "ds_read_b128 v[96:99], %[vb2] offset:1024\n\t"
  "s_waitcnt vmcnt(0)\n\t"
  "v_cmp_eq_u64 s[54:55], v[252:253], v[250:251]\n\t"
  "v_mfma_f32_16x16x32_bf16 v[56:59], v[76:79], v[140:143], v[56:59]\n\t"
  "v_mfma_f32_16x16x32_bf16 v[52:55], v[76:79], v[136:139], v[52:55]\n\t"
  "v_mfma_f32_16x16x32_bf16 v[48:51], v[76:79], v[132:135], v[48:51]\n\t"
  "s_nop 0\n\t"
  "s_cmp_eq_u64 s[54:55], -1\n\t"
  "s_cselect_b32 s53, 1, 0\n\t"
  "s_cselect_b32 s56, %[sW1], 0x70000000\n\t"
  "v_mfma_f32_16x16x32_bf16 v[44:47], v[76:79], v[128:131], v[44:47]\n\t"
  "ds_read_b64 v[76:77], %[va] offset:1664\n\t"
  "ds_read_b64 v[78:79], %[va] offset:1672\n\t"
  "v_mfma_f32_16x16x32_bf16 v[60:63], v[80:83], v[140:143], v[60:63]\n\t"
  "s_mov_b32 %[wdone], s53\n\t"
  "buffer_load_dwordx4 a[0:3], %[vl16], %[srd], s56 offen offset:0 sc1\n\t"
  "v_mfma_f32_16x16x32_bf16 v[56:59], v[80:83], v[136:139], v[56:59]\n\t"
  "v_mfma_f32_16x16x32_bf16 v[52:55], v[80:83], v[132:135], v[52:55]\n\t"
  "v_mfma_f32_16x16x32_bf16 v[48:51], v[80:83], v[128:131], v[48:51]\n\t"
  "ds_read_b64 v[80:81], %[va] offset:2176\n\t"
  "ds_read_b64 v[82:83], %[va] offset:2184\n\t"
  "v_mfma_f32_16x16x32_bf16 v[60:63], v[84:87], v[136:139], v[60:63]\n\t"
  "buffer_load_dwordx4 a[4:7], %[vl16], %[srd], s56 offen offset:1024 sc1\n\t"
  "v_mfma_f32_16x16x32_bf16 v[56:59], v[84:87], v[132:135], v[56:59]\n\t"
  "v_mfma_f32_16x16x32_bf16 v[52:55], v[84:87], v[128:131], v[52:55]\n\t"
  "s_waitcnt lgkmcnt(15)\n\t"
  "ds_read_b64 v[84:85], %[va] offset:2688\n\t"
  "v_mfma_f32_16x16x32_bf16 v[60:63], v[88:91], v[132:135], v[60:63]\n\t"
  "ds_read_b64 v[86:87], %[va] offset:2696\n\t"
  "buffer_load_dwordx4 a[8:11], %[vl16], %[srd], s56 offen offset:2048 sc1\n\t"
  "v_mfma_f32_16x16x32_bf16 v[56:59], v[88:91], v[128:131], v[56:59]\n\t"
  "ds_read_b64 v[88:89], %[va] offset:3200\n\t"
  "ds_read_b64 v[90:91], %[va] offset:3208\n\t"
  "v_mfma_f32_16x16x32_bf16 v[60:63], v[92:95], v[128:131], v[60:63]\n\t"
  "s_waitcnt lgkmcnt(8)\n\t"
  "ds_read_b64 v[92:93], %[va] offset:3712\n\t"
  "v_mfma_f32_16x16x32_bf16 v[60:63], v[64:67], v[124:127], v[60:63]\n\t"
  "ds_read_b128 v[156:159], %[vb3] offset:15360\n\t"
  "ds_read_b64 v[94:95], %[va] offset:3720\n\t"
  "v_mfma_f32_16x16x32_bf16 v[56:59], v[64:67], v[120:123], v[56:59]\n\t"
  "buffer_load_dwordx4 a[12:15], %[vl16], %[srd], s56 offen offset:3072 sc1\n\t"
  "v_mfma_f32_16x16x32_bf16 v[52:55], v[64:67], v[116:119], v[52:55]\n\t"
  "v_mfma_f32_16x16x32_bf16 v[48:51], v[64:67], v[112:115], v[48:51]\n\t"
  "ds_read_b128 v[152:155], %[vb3] offset:13312\n\t"
  "v_mfma_f32_16x16x32_bf16 v[44:47], v[64:67], v[108:111], v[44:47]\n\t"
  "s_add_u32 s56, s56, 4096\n\t"
  "v_mfma_f32_16x16x32_bf16 v[40:43], v[64:67], v[104:107], v[40:43]\n\t"
  "v_mfma_f32_16x16x32_bf16 v[36:39], v[64:67], v[100:103], v[36:39]\n\t"
  "ds_read_b128 v[148:151], %[vb3] offset:11264\n\t"
  "v_mfma_f32_16x16x32_bf16 v[32:35], v[64:67], v[96:99], v[32:35]\n\t"
  "ds_read_b64 v[64:65], %[va] offset:192\n\t"
  "ds_read_b64 v[66:67], %[va] offset:200\n\t"
  "v_mfma_f32_16x16x32_bf16 v[60:63], v[68:71], v[120:123], v[60:63]\n\t"
  "buffer_load_dwordx4 a[16:19], %[vl16], %[srd], s56 offen offset:0 sc1\n\t"
  "v_mfma_f32_16x16x32_bf16 v[56:59], v[68:71], v[116:119], v[56:59]\n\t"
  "ds_read_b128 v[144:147], %[vb3] offset:9216\n\t"
  "v_mfma_f32_16x16x32_bf16 v[52:55], v[68:71], v[112:115], v[52:55]\n\t"
  "v_mfma_f32_16x16x32_bf16 v[48:51], v[68:71], v[108:111], v[48:51]\n\t"
  "buffer_load_dwordx4 a[20:23], %[vl16], %[srd], s56 offen offset:1024 sc1\n\t"
  "v_mfma_f32_16x16x32_bf16 v[44:47], v[68:71], v[104:107], v[44:47]\n\t"
  "ds_read_b128 v[140:143], %[vb3] offset:7168\n\t"
  "v_mfma_f32_16x16x32_bf16 v[40:43], v[68:71], v[100:103], v[40:43]\n\t"
  "v_mfma_f32_16x16x32_bf16 v[36:39], v[68:71], v[96:99], v[36:39]\n\t"
  "s_waitcnt lgkmcnt(11)\n\t"
  "ds_read_b64 v[68:69], %[va] offset:704\n\t"
  "v_mfma_f32_16x16x32_bf16 v[60:63], v[72:75], v[116:119], v[60:63]\n\t"
  "ds_read_b128 v[136:139], %[vb3] offset:5120\n\t"
  "ds_read_b64 v[70:71], %[va] offset:712\n\t"
  "v_mfma_f32_16x16x32_bf16 v[56:59], v[72:75], v[112:115], v[56:59]\n\t"
  "buffer_load_dwordx4 a[24:27], %[vl16], %[srd], s56 offen offset:2048 sc1\n\t"
  "v_mfma_f32_16x16x32_bf16 v[52:55], v[72:75], v[108:111], v[52:55]\n\t"
  "v_mfma_f32_16x16x32_bf16 v[48:51], v[72:75], v[104:107], v[48:51]\n\t"
  "ds_read_b128 v[132:135], %[vb3] offset:3072\n\t"
  "buffer_load_dwordx4 a[28:31], %[vl16], %[srd], s56 offen offset:3072 sc1\n\t"
  "v_mfma_f32_16x16x32_bf16 v[44:47], v[72:75], v[100:103], v[44:47]\n\t"
  "v_mfma_f32_16x16x32_bf16 v[40:43], v[72:75], v[96:99], v[40:43]\n\t"
  "ds_read_b64 v[72:73], %[va] offset:1216\n\t"
  "ds_read_b64 v[74:75], %[va] offset:1224\n\t"
  "v_mfma_f32_16x16x32_bf16 v[60:63], v[76:79], v[112:115], v[60:63]\n\t"
  "ds_read_b128 v[128:131], %[vb3] offset:1024\n\t"
  "s_add_u32 s56, s56, 4096\n\t"
  "v_mfma_f32_16x16x32_bf16 v[56:59], v[76:79], v[108:111], v[56:59]\n\t"
  "v_mfma_f32_16x16x32_bf16 v[52:55], v[76:79], v[104:107], v[52:55]\n\t"
  "v_mfma_f32_16x16x32_bf16 v[48:51], v[76:79], v[100:103], v[48:51]\n\t"
  "v_mfma_f32_16x16x32_bf16 v[44:47], v[76:79], v[96:99], v[44:47]\n\t"
  "ds_read_b64 v[76:77], %[va] offset:1728\n\t"
  "ds_read_b64 v[78:79], %[va] offset:1736\n\t"
  "v_mfma_f32_16x16x32_bf16 v[60:63], v[80:83], v[108:111], v[60:63]\n\t"
  "buffer_load_dwordx4 a[32:35], %[vl16], %[srd], s56 offen offset:0 sc1\n\t"
  "v_mfma_f32_16x16x32_bf16 v[56:59], v[80:83], v[104:107], v[56:59]\n\t"
  "v_mfma_f32_16x16x32_bf16 v[52:55], v[80:83], v[100:103], v[52:55]\n\t"
  "buffer_load_dwordx4 a[36:39], %[vl16], %[srd], s56 offen offset:1024 sc1\n\t"
  "v_mfma_f32_16x16x32_bf16 v[48:51], v[80:83], v[96:99], v[48:51]\n\t"
  "ds_read_b64 v[80:81], %[va] offset:2240\n\t"
  "ds_read_b64 v[82:83], %[va] offset:2248\n\t"
  "v_mfma_f32_16x16x32_bf16 v[60:63], v[84:87], v[104:107], v[60:63]\n\t"
  "v_mfma_f32_16x16x32_bf16 v[56:59], v[84:87], v[100:103], v[56:59]\n\t"
  "v_mfma_f32_16x16x32_bf16 v[52:55], v[84:87], v[96:99], v[52:55]\n\t"
  "s_waitcnt lgkmcnt(15)\n\t"
  "ds_read_b64 v[84:85], %[va] offset:2752\n\t"
  "v_mfma_f32_16x16x32_bf16 v[60:63], v[88:91], v[100:103], v[60:63]\n\t"
  "ds_read_b64 v[86:87], %[va] offset:2760\n\t"
  "buffer_load_dwordx4 a[40:43], %[vl16], %[srd], s56 offen offset:2048 sc1\n\t"
  "v_mfma_f32_16x16x32_bf16 v[56:59], v[88:91], v[96:99], v[56:59]\n\t"
  "ds_read_b64 v[88:89], %[va] offset:3264\n\t"
  "ds_read_b64 v[90:91], %[va] offset:3272\n\t"
  "v_mfma_f32_16x16x32_bf16 v[60:63], v[92:95], v[96:99], v[60:63]\n\t"
  "s_waitcnt lgkmcnt(8)\n\t"
  "ds_read_b64 v[92:93], %[va] offset:3776\n\t"
  "v_mfma_f32_16x16x32_bf16 v[60:63], v[64:67], v[156:159], v[60:63]\n\t"
  "ds_read_b128 v[124:127], %[vb0] offset:14336\n\t"
  "ds_read_b64 v[94:95], %[va] offset:3784\n\t"
  "v_mfma_f32_16x16x32_bf16 v[56:59], v[64:67], v[152:155], v[56:59]\n\t"
  "buffer_load_dwordx4 a[44:47], %[vl16], %[srd], s56 offen offset:3072 sc1\n\t"
  "v_mfma_f32_16x16x32_bf16 v[52:55], v[64:67], v[148:151], v[52:55]\n\t"
  "v_mfma_f32_16x16x32_bf16 v[48:51], v[64:67], v[144:147], v[48:51]\n\t"
  "ds_read_b128 v[120:123], %[vb0] offset:12288\n\t"
  "s_add_u32 s56, s56, 4096\n\t"
  "v_mfma_f32_16x16x32_bf16 v[44:47], v[64:67], v[140:143], v[44:47]\n\t"
  "v_mfma_f32_16x16x32_bf16 v[40:43], v[64:67], v[136:139], v[40:43]\n\t"
  "v_mfma_f32_16x16x32_bf16 v[36:39], v[64:67], v[132:135], v[36:39]\n\t"
  "ds_read_b128 v[116:119], %[vb0] offset:10240\n\t"
  "buffer_load_dwordx4 a[48:51], %[vl16], %[srd], s56 offen offset:0 sc1\n\t"
  "v_mfma_f32_16x16x32_bf16 v[32:35], v[64:67], v[128:131], v[32:35]\n\t"
  "ds_read_b64 v[64:65], %[va] offset:256\n\t"
  "ds_read_b64 v[66:67], %[va] offset:264\n\t"
  "v_mfma_f32_16x16x32_bf16 v[60:63], v[68:71], v[152:155], v[60:63]\n\t"
  "v_mfma_f32_16x16x32_bf16 v[56:59], v[68:71], v[148:151], v[56:59]\n\t"
  "ds_read_b128 v[112:115], %[vb0] offset:8192\n\t"
  "v_mfma_f32_16x16x32_bf16 v[52:55], v[68:71], v[144:147], v[52:55]\n\t"
  "buffer_load_dwordx4 a[52:55], %[vl16], %[srd], s56 offen offset:1024 sc1\n\t"
  "v_mfma_f32_16x16x32_bf16 v[48:51], v[68:71], v[140:143], v[48:51]\n\t"
  "v_mfma_f32_16x16x32_bf16 v[44:47], v[68:71], v[136:139], v[44:47]\n\t"
  "ds_read_b128 v[108:111], %[vb0] offset:6144\n\t"
  "v_mfma_f32_16x16x32_bf16 v[40:43], v[68:71], v[132:135], v[40:43]\n\t"
  "v_mfma_f32_16x16x32_bf16 v[36:39], v[68:71], v[128:131], v[36:39]\n\t"
  "s_waitcnt lgkmcnt(11)\n\t"
  "ds_read_b64 v[68:69], %[va] offset:768\n\t"
  "v_mfma_f32_16x16x32_bf16 v[60:63], v[72:75], v[148:151], v[60:63]\n\t"
  "ds_read_b128 v[104:107], %[vb0] offset:4096\n\t"
  "ds_read_b64 v[70:71], %[va] offset:776\n\t"
  "v_mfma_f32_16x16x32_bf16 v[56:59], v[72:75], v[144:147], v[56:59]\n\t"
  "v_mfma_f32_16x16x32_bf16 v[52:55], v[72:75], v[140:143], v[52:55]\n\t"
  "v_mfma_f32_16x16x32_bf16 v[48:51], v[72:75], v[136:139], v[48:51]\n\t"
  "ds_read_b128 v[100:103], %[vb0] offset:2048\n\t"
  "v_mfma_f32_16x16x32_bf16 v[44:47], v[72:75], v[132:135], v[44:47]\n\t"
  "v_mfma_f32_16x16x32_bf16 v[40:43], v[72:75], v[128:131], v[40:43]\n\t"
  "ds_read_b64 v[72:73], %[va] offset:1280\n\t"
  "ds_read_b64 v[74:75], %[va] offset:1288\n\t"
  "v_mfma_f32_16x16x32_bf16 v[60:63], v[76:79], v[144:147], v[60:63]\n\t"
  "ds_read_b128 v[96:99], %[vb0] offset:0\n\t"
  "v_mfma_f32_16x16x32_bf16 v[56:59], v[76:79], v[140:143], v[56:59]\n\t"
  "v_mfma_f32_16x16x32_bf16 v[52:55], v[76:79], v[136:139], v[52:55]\n\t"
  "v_mfma_f32_16x16x32_bf16 v[48:51], v[76:79], v[132:135], v[48:51]\n\t"
  "buffer_load_dwordx2 v[162:163], %[vf2], %[srd], 0 offen sc1\n\t"
  "v_mfma_f32_16x16x32_bf16 v[44:47], v[76:79], v[128:131], v[44:47]\n\t"
  "ds_read_b64 v[76:77], %[va] offset:1792\n\t"
  "ds_read_b64 v[78:79], %[va] offset:1800\n\t"
  "v_mfma_f32_16x16x32_bf16 v[60:63], v[80:83], v[140:143], v[60:63]\n\t"
  "v_mfma_f32_16x16x32_bf16 v[56:59], v[80:83], v[136:139], v[56:59]\n\t"
  "v_mfma_f32_16x16x32_bf16 v[52:55], v[80:83], v[132:135], v[52:55]\n\t"
  "v_mfma_f32_16x16x32_bf16 v[48:51], v[80:83], v[128:131], v[48:51]\n\t"
  "ds_read_b64 v[80:81], %[va] offset:2304\n\t"
  "ds_read_b64 v[82:83], %[va] offset:2312\n\t"
  "v_mfma_f32_16x16x32_bf16 v[60:63], v[84:87], v[136:139], v[60:63]\n\t"
  "v_mfma_f32_16x16x32_bf16 v[56:59], v[84:87], v[132:135], v[56:59]\n\t"
  "v_mfma_f32_16x16x32_bf16 v[52:55], v[84:87], v[128:131], v[52:55]\n\t"
  "s_waitcnt lgkmcnt(15)\n\t"
  "ds_read_b64 v[84:85], %[va] offset:2816\n\t"
  "v_mfma_f32_16x16x32_bf16 v[60:63], v[88:91], v[132:135], v[60:63]\n\t"
  "ds_read_b64 v[86:87], %[va] offset:2824\n\t"
  "v_mfma_f32_16x16x32_bf16 v[56:59], v[88:91], v[128:131], v[56:59]\n\t"
  "ds_read_b64 v[88:89], %[va] offset:3328\n\t"
  "ds_read_b64 v[90:91], %[va] offset:3336\n\t"
  "v_mfma_f32_16x16x32_bf16 v[60:63], v[92:95], v[128:131], v[60:63]\n\t"
  "s_waitcnt lgkmcnt(8)\n\t"
  "ds_read_b64 v[92:93], %[va] offset:3840\n\t"
  "v_mfma_f32_16x16x32_bf16 v[60:63], v[64:67], v[124:127], v[60:63]\n\t"
  "ds_read_b128 v[156:159], %[vb1] offset:14336\n\t"
  "ds_read_b64 v[94:95], %[va] offset:3848\n\t"
  "v_mfma_f32_16x16x32_bf16 v[56:59], v[64:67], v[120:123], v[56:59]\n\t"
  "v_mfma_f32_16x16x32_bf16 v[52:55], v[64:67], v[116:119], v[52:55]\n\t"
  "v_mfma_f32_16x16x32_bf16 v[48:51], v[64:67], v[112:115], v[48:51]\n\t"
  "ds_read_b128 v[152:155], %[vb1] offset:12288\n\t"
  "v_mfma_f32_16x16x32_bf16 v[44:47], v[64:67], v[108:111], v[44:47]\n\t"
  "v_mfma_f32_16x16x32_bf16 v[40:43], v[64:67], v[104:107], v[40:43]\n\t"
  "v_mfma_f32_16x16x32_bf16 v[36:39], v[64:67], v[100:103], v[36:39]\n\t"
  "ds_read_b128 v[148:151], %[vb1] offset:10240\n\t"
  "v_mfma_f32_16x16x32_bf16 v[32:35], v[64:67], v[96:99], v[32:35]\n\t"
  "ds_read_b64 v[64:65], %[va] offset:320\n\t"
  "ds_read_b64 v[66:67], %[va] offset:328\n\t"
  "v_mfma_f32_16x16x32_bf16 v[60:63], v[68:71], v[120:123], v[60:63]\n\t"
  "buffer_load_dwordx2 v[250:251], %[vfw], %[srd], 0 offen sc1\n\t"
  "v_mfma_f32_16x16x32_bf16 v[56:59], v[68:71], v[116:119], v[56:59]\n\t"
  "ds_read_b128 v[144:147], %[vb1] offset:8192\n\t"
  "v_mfma_f32_16x16x32_bf16 v[52:55], v[68:71], v[112:115], v[52:55]\n\t"
  "v_mfma_f32_16x16x32_bf16 v[48:51], v[68:71], v[108:111], v[48:51]\n\t"
  "v_mfma_f32_16x16x32_bf16 v[44:47], v[68:71], v[104:107], v[44:47]\n\t"
  "ds_read_b128 v[140:143], %[vb1] offset:6144\n\t"
  "v_mfma_f32_16x16x32_bf16 v[40:43], v[68:71], v[100:103], v[40:43]\n\t"
  "v_mfma_f32_16x16x32_bf16 v[36:39], v[68:71], v[96:99], v[36:39]\n\t"
  "s_waitcnt lgkmcnt(11)\n\t"
  "ds_read_b64 v[68:69], %[va] offset:832\n\t"
  "v_mfma_f32_16x16x32_bf16 v[60:63], v[72:75], v[116:119], v[60:63]\n\t"
  "ds_read_b128 v[136:139], %[vb1] offset:4096\n\t"
  "ds_read_b64 v[70:71], %[va] offset:840\n\t"
  "v_mfma_f32_16x16x32_bf16 v[56:59], v[72:75], v[112:115], v[56:59]\n\t"
  "v_mfma_f32_16x16x32_bf16 v[52:55], v[72:75], v[108:111], v[52:55]\n\t"
  "v_mfma_f32_16x16x32_bf16 v[48:51], v[72:75], v[104:107], v[48:51]\n\t"
  "ds_read_b128 v[132:135], %[vb1] offset:2048\n\t"
  "v_mfma_f32_16x16x32_bf16 v[44:47], v[72:75], v[100:103], v[44:47]\n\t"
  "v_mfma_f32_16x16x32_bf16 v[40:43], v[72:75], v[96:99], v[40:43]\n\t"
  "ds_read_b64 v[72:73], %[va] offset:1344\n\t"
  "ds_read_b64 v[74:75], %[va] offset:1352\n\t"
  "v_mfma_f32_16x16x32_bf16 v[60:63], v[76:79], v[112:115], v[60:63]\n\t"
  "ds_read_b128 v[128:131], %[vb1] offset:0\n\t"
  "v_mfma_f32_16x16x32_bf16 v[56:59], v[76:79], v[108:111], v[56:59]\n\t"
  "v_mfma_f32_16x16x32_bf16 v[52:55], v[76:79], v[104:107], v[52:55]\n\t"
  "v_mfma_f32_16x16x32_bf16 v[48:51], v[76:79], v[100:103], v[48:51]\n\t"
  "v_mfma_f32_16x16x32_bf16 v[44:47], v[76:79], v[96:99], v[44:47]\n\t"
  "ds_read_b64 v[76:77], %[va] offset:1856\n\t"
  "ds_read_b64 v[78:79], %[va] offset:1864\n\t"
  "v_mfma_f32_16x16x32_bf16 v[60:63], v[80:83], v[108:111], v[60:63]\n\t"
  "v_mfma_f32_16x16x32_bf16 v[56:59], v[80:83], v[104:107], v[56:59]\n\t"
  "v_mfma_f32_16x16x32_bf16 v[52:55], v[80:83], v[100:103], v[52:55]\n\t"
  "s_waitcnt vmcnt(1)\n\t"
  "v_cmp_ne_u64 vcc, v[252:253], v[162:163]\n\t"
  "v_mfma_f32_16x16x32_bf16 v[48:51], v[80:83], v[96:99], v[48:51]\n\t"
  "ds_read_b64 v[80:81], %[va] offset:2368\n\t"
  "ds_read_b64 v[82:83], %[va] offset:2376\n\t"
  "v_mfma_f32_16x16x32_bf16 v[60:63], v[84:87], v[104:107], v[60:63]\n\t"
  "s_cbranch_vccnz La2_slow%=\n\t"
  "La2_back%=:\n\t"
  "v_mfma_f32_16x16x32_bf16 v[56:59], v[84:87], v[100:103], v[56:59]\n\t"
  "buffer_load_dword v244, %[vmod], %[srd], 0 offen sc1\n\t"
  "s_add_u32 m0, s45, 84992\n\t"
  "v_mfma_f32_16x16x32_bf16 v[52:55], v[84:87], v[96:99], v[52:55]\n\t"
  "s_waitcnt lgkmcnt(15)\n\t"
  "ds_read_b64 v[84:85], %[va] offset:2880\n\t"
  "v_mfma_f32_16x16x32_bf16 v[60:63], v[88:91], v[100:103], v[60:63]\n\t"
  "ds_read_b64 v[86:87], %[va] offset:2888\n\t"
  "s_add_u32 s44, %[sKV], 2621440\n\t"
  "v_mfma_f32_16x16x32_bf16 v[56:59], v[88:91], v[96:99], v[56:59]\n\t"
  "ds_read_b64 v[88:89], %[va] offset:3392\n\t"
  "ds_read_b64 v[90:91], %[va] offset:3400\n\t"
  "v_mfma_f32_16x16x32_bf16 v[60:63], v[92:95], v[96:99], v[60:63]\n\t"
  "s_waitcnt lgkmcnt(8)\n\t"
  "ds_read_b64 v[92:93], %[va] offset:3904\n\t"
  "v_mfma_f32_16x16x32_bf16 v[60:63], v[64:67], v[156:159], v[60:63]\n\t"
  "ds_read_b128 v[124:127], %[vb2] offset:14336\n\t"
  "ds_read_b64 v[94:95], %[va] offset:3912\n\t"
  "v_mfma_f32_16x16x32_bf16 v[56:59], v[64:67], v[152:155], v[56:59]\n\t"
  "buffer_load_dwordx4 %[vkv], %[srd], s44 offen sc1 lds\n\t"
  "s_add_u32 m0, s45, 86016\n\t"
  "v_mfma_f32_16x16x32_bf16 v[52:55], v[64:67], v[148:151], v[52:55]\n\t"
  "s_add_u32 s44, %[sKV], 2785280\n\t"
  "buffer_load_dwordx4 %[vkv], %[srd], s44 offen sc1 lds\n\t"
  "v_mfma_f32_16x16x32_bf16 v[48:51], v[64:67], v[144:147], v[48:51]\n\t"
  "ds_read_b128 v[120:123], %[vb2] offset:12288\n\t"
  "s_add_u32 m0, s45, 87040\n\t"
  "v_mfma_f32_16x16x32_bf16 v[44:47], v[64:67], v[140:143], v[44:47]\n\t"
  "s_add_u32 s44, %[sKV], 2949120\n\t"
  "buffer_load_dwordx4 %[vkv], %[srd], s44 offen sc1 lds\n\t"
  "v_mfma_f32_16x16x32_bf16 v[40:43], v[64:67], v[136:139], v[40:43]\n\t"
  "s_add_u32 m0, s45, 88064\n\t"
  "s_add_u32 s44, %[sKV], 3112960\n\t"
  "v_mfma_f32_16x16x32_bf16 v[36:39], v[64:67], v[132:135], v[36:39]\n\t"
  "ds_read_b128 v[116:119], %[vb2] offset:10240\n\t"
  "buffer_load_dwordx4 %[vkv], %[srd], s44 offen sc1 lds\n\t"
  "v_mfma_f32_16x16x32_bf16 v[32:35], v[64:67], v[128:131], v[32:35]\n\t"
  "ds_read_b64 v[64:65], %[va] offset:384\n\t"
  "ds_read_b64 v[66:67], %[va] offset:392\n\t"
  "v_mfma_f32_16x16x32_bf16 v[60:63], v[68:71], v[152:155], v[60:63]\n\t"
  "s_add_u32 m0, s45, 101376\n\t"
  "s_add_u32 s44, s45, 995328\n\t"
  "v_mfma_f32_16x16x32_bf16 v[56:59], v[68:71], v[148:151], v[56:59]\n\t"
  "ds_read_b128 v[112:115], %[vb2] offset:8192\n\t"
  "buffer_load_dwordx4 %[vl16], %[srd], s44 offen offset:0 sc1 lds\n\t"
  "v_mfma_f32_16x16x32_bf16 v[52:55], v[68:71], v[144:147], v[52:55]\n\t"
  "buffer_load_dwordx4 %[vl16], %[srd], s44 offen offset:1024 sc1 lds\n\t"
  "buffer_load_dwordx4 %[vl16], %[srd], s44 offen offset:2048 sc1 lds\n\t"
  "v_mfma_f32_16x16x32_bf16 v[48:51], v[68:71], v[140:143], v[48:51]\n\t"
  "buffer_load_dwordx4 %[vl16], %[srd], s44 offen offset:3072 sc1 lds\n\t"
  "v_mfma_f32_16x16x32_bf16 v[44:47], v[68:71], v[136:139], v[44:47]\n\t"
  "ds_read_b128 v[108:111], %[vb2] offset:6144\n\t"
  "s_waitcnt vmcnt(9)\n\t"
  "v_cmp_eq_u64 s[54:55], v[252:253], v[250:251]\n\t"
  "v_mfma_f32_16x16x32_bf16 v[40:43], v[68:71], v[132:135], v[40:43]\n\t"
  "v_mfma_f32_16x16x32_bf16 v[36:39], v[68:71], v[128:131], v[36:39]\n\t"
  "s_waitcnt lgkmcnt(11)\n\t"
  "ds_read_b64 v[68:69], %[va] offset:896\n\t"
  "v_mfma_f32_16x16x32_bf16 v[60:63], v[72:75], v[148:151], v[60:63]\n\t"
  "ds_read_b128 v[104:107], %[vb2] offset:4096\n\t"
  "ds_read_b64 v[70:71], %[va] offset:904\n\t"
  "v_mfma_f32_16x16x32_bf16 v[56:59], v[72:75], v[144:147], v[56:59]\n\t"
  "s_cmp_eq_u64 s[54:55], -1\n\t"
  "s_cselect_b32 s53, 2, 0\n\t"
  "s_cselect_b32 s57, %[sW1], 0x70000000\n\t"
  "v_mfma_f32_16x16x32_bf16 v[52:55], v[72:75], v[140:143], v[52:55]\n\t"
  "s_or_b32 %[wdone], %[wdone], s53\n\t"
  "v_mfma_f32_16x16x32_bf16 v[48:51], v[72:75], v[136:139], v[48:51]\n\t"
  "ds_read_b128 v[100:103], %[vb2] offset:2048\n\t"
  "s_add_u32 s57, s57, 14336\n\t"
  "v_mfma_f32_16x16x32_bf16 v[44:47], v[72:75], v[132:135], v[44:47]\n\t"
  "v_mfma_f32_16x16x32_bf16 v[40:43], v[72:75], v[128:131], v[40:43]\n\t"
  "ds_read_b64 v[72:73], %[va] offset:1408\n\t"
  "ds_read_b64 v[74:75], %[va] offset:1416\n\t"
  "v_mfma_f32_16x16x32_bf16 v[60:63], v[76:79], v[144:147], v[60:63]\n\t"
  "ds_read_b128 v[96:99], %[vb2] offset:0\n\t"
  "buffer_load_dwordx4 a[56:59], %[vl16], %[srd], s57 offen offset:0 sc1\n\t"
  "v_mfma_f32_16x16x32_bf16 v[56:59], v[76:79], v[140:143], v[56:59]\n\t"
  "v_mfma_f32_16x16x32_bf16 v[52:55], v[76:79], v[136:139], v[52:55]\n\t"
  "v_mfma_f32_16x16x32_bf16 v[48:51], v[76:79], v[132:135], v[48:51]\n\t"
  "buffer_load_dwordx4 a[60:63], %[vl16], %[srd], s57 offen offset:1024 sc1\n\t"
  "v_mfma_f32_16x16x32_bf16 v[44:47], v[76:79], v[128:131], v[44:47]\n\t"
  "ds_read_b64 v[76:77], %[va] offset:1920\n\t"
  "ds_read_b64 v[78:79], %[va] offset:1928\n\t"
  "v_mfma_f32_16x16x32_bf16 v[60:63], v[80:83], v[140:143], v[60:63]\n\t"
  "v_mfma_f32_16x16x32_bf16 v[56:59], v[80:83], v[136:139], v[56:59]\n\t"
  "v_mfma_f32_16x16x32_bf16 v[52:55], v[80:83], v[132:135], v[52:55]\n\t"
  "buffer_load_dwordx4 a[64:67], %[vl16], %[srd], s57 offen offset:2048 sc1\n\t"
  "v_mfma_f32_16x16x32_bf16 v[48:51], v[80:83], v[128:131], v[48:51]\n\t"
  "ds_read_b64 v[80:81], %[va] offset:2432\n\t"
  "ds_read_b64 v[82:83], %[va] offset:2440\n\t"
  "v_mfma_f32_16x16x32_bf16 v[60:63], v[84:87], v[136:139], v[60:63]\n\t"
  "v_mfma_f32_16x16x32_bf16 v[56:59], v[84:87], v[132:135], v[56:59]\n\t"
  "buffer_load_dwordx4 a[68:71], %[vl16], %[srd], s57 offen offset:3072 sc1\n\t"
  "v_mfma_f32_16x16x32_bf16 v[52:55], v[84:87], v[128:131], v[52:55]\n\t"
  "s_waitcnt lgkmcnt(15)\n\t"
  "ds_read_b64 v[84:85], %[va] offset:2944\n\t"
  "v_mfma_f32_16x16x32_bf16 v[60:63], v[88:91], v[132:135], v[60:63]\n\t"
  "ds_read_b64 v[86:87], %[va] offset:2952\n\t"
  "v_mfma_f32_16x16x32_bf16 v[56:59], v[88:91], v[128:131], v[56:59]\n\t"
  "ds_read_b64 v[88:89], %[va] offset:3456\n\t"
  "ds_read_b64 v[90:91], %[va] offset:3464\n\t"
  "v_mfma_f32_16x16x32_bf16 v[60:63], v[92:95], v[128:131], v[60:63]\n\t"
  "s_waitcnt lgkmcnt(8)\n\t"
  "ds_read_b64 v[92:93], %[va] offset:3968\n\t"
  "v_mfma_f32_16x16x32_bf16 v[60:63], v[64:67], v[124:127], v[60:63]\n\t"
  "ds_read_b128 v[156:159], %[vb3] offset:14336\n\t"
  "ds_read_b64 v[94:95], %[va] offset:3976\n\t"
  "v_mfma_f32_16x16x32_bf16 v[56:59], v[64:67], v[120:123], v[56:59]\n\t"
  "s_add_u32 s57, s57, 4096\n\t"
  "v_mfma_f32_16x16x32_bf16 v[52:55], v[64:67], v[116:119], v[52:55]\n\t"
  "buffer_load_dwordx4 a[72:75], %[vl16], %[srd], s57 offen offset:0 sc1\n\t"
  "v_mfma_f32_16x16x32_bf16 v[48:51], v[64:67], v[112:115], v[48:51]\n\t"
  "ds_read_b128 v[152:155], %[vb3] offset:12288\n\t"
  "v_mfma_f32_16x16x32_bf16 v[44:47], v[64:67], v[108:111], v[44:47]\n\t"
  "v_mfma_f32_16x16x32_bf16 v[40:43], v[64:67], v[104:107], v[40:43]\n\t"
  "buffer_load_dwordx4 a[76:79], %[vl16], %[srd], s57 offen offset:1024 sc1\n\t"
  "v_mfma_f32_16x16x32_bf16 v[36:39], v[64:67], v[100:103], v[36:39]\n\t"
  "ds_read_b128 v[148:151], %[vb3] offset:10240\n\t"
  "v_mfma_f32_16x16x32_bf16 v[32:35], v[64:67], v[96:99], v[32:35]\n\t"
  "ds_read_b64 v[64:65], %[va] offset:448\n\t"
  "ds_read_b64 v[66:67], %[va] offset:456\n\t"
  "v_mfma_f32_16x16x32_bf16 v[60:63], v[68:71], v[120:123], v[60:63]\n\t"
  "buffer_load_dwordx4 a[80:83], %[vl16], %[srd], s57 offen offset:2048 sc1\n\t"
  "v_mfma_f32_16x16x32_bf16 v[56:59], v[68:71], v[116:119], v[56:59]\n\t"
  "ds_read_b128 v[144:147], %[vb3] offset:8192\n\t"
  "v_mfma_f32_16x16x32_bf16 v[52:55], v[68:71], v[112:115], v[52:55]\n\t"
  "v_mfma_f32_16x16x32_bf16 v[48:51], v[68:71], v[108:111], v[48:51]\n\t"
  "v_mfma_f32_16x16x32_bf16 v[44:47], v[68:71], v[104:107], v[44:47]\n\t"
  "ds_read_b128 v[140:143], %[vb3] offset:6144\n\t"
  "buffer_load_dwordx4 a[84:87], %[vl16], %[srd], s57 offen offset:3072 sc1\n\t"
  "v_mfma_f32_16x16x32_bf16 v[40:43], v[68:71], v[100:103], v[40:43]\n\t"
  "v_mfma_f32_16x16x32_bf16 v[36:39], v[68:71], v[96:99], v[36:39]\n\t"
  "s_waitcnt lgkmcnt(11)\n\t"
  "ds_read_b64 v[68:69], %[va] offset:960\n\t"
  "v_mfma_f32_16x16x32_bf16 v[60:63], v[72:75], v[116:119], v[60:63]\n\t"
  "ds_read_b128 v[136:139], %[vb3] offset:4096\n\t"
  "ds_read_b64 v[70:71], %[va] offset:968\n\t"
  "v_mfma_f32_16x16x32_bf16 v[56:59], v[72:75], v[112:115], v[56:59]\n\t"
  "s_add_u32 s57, s57, 4096\n\t"
  "v_mfma_f32_16x16x32_bf16 v[52:55], v[72:75], v[108:111], v[52:55]\n\t"
  "v_mfma_f32_16x16x32_bf16 v[48:51], v[72:75], v[104:107], v[48:51]\n\t"
  "ds_read_b128 v[132:135], %[vb3] offset:2048\n\t"
  "buffer_load_dwordx4 a[88:91], %[vl16], %[srd], s57 offen offset:0 sc1\n\t"
  "v_mfma_f32_16x16x32_bf16 v[44:47], v[72:75], v[100:103], v[44:47]\n\t"
  "v_mfma_f32_16x16x32_bf16 v[40:43], v[72:75], v[96:99], v[40:43]\n\t"
  "ds_read_b64 v[72:73], %[va] offset:1472\n\t"
  "ds_read_b64 v[74:75], %[va] offset:1480\n\t"
  "v_mfma_f32_16x16x32_bf16 v[60:63], v[76:79], v[112:115], v[60:63]\n\t"
  "ds_read_b128 v[128:131], %[vb3] offset:0\n\t"
  "v_mfma_f32_16x16x32_bf16 v[56:59], v[76:79], v[108:111], v[56:59]\n\t"
  "buffer_load_dwordx4 a[92:95], %[vl16], %[srd], s57 offen offset:1024 sc1\n\t"
  "v_mfma_f32_16x16x32_bf16 v[52:55], v[76:79], v[104:107], v[52:55]\n\t"
  "v_mfma_f32_16x16x32_bf16 v[48:51], v[76:79], v[100:103], v[48:51]\n\t"
  "v_mfma_f32_16x16x32_bf16 v[44:47], v[76:79], v[96:99], v[44:47]\n\t"
  "ds_read_b64 v[76:77], %[va] offset:1984\n\t"
  "ds_read_b64 v[78:79], %[va] offset:1992\n\t"
  "v_mfma_f32_16x16x32_bf16 v[60:63], v[80:83], v[108:111], v[60:63]\n\t"
  "buffer_load_dwordx4 a[96:99], %[vl16], %[srd], s57 offen offset:2048 sc1\n\t"
  "v_mfma_f32_16x16x32_bf16 v[56:59], v[80:83], v[104:107], v[56:59]\n\t"
  "s_waitcnt vmcnt(19)\n\t"
  "v_add_f32_dpp v244, v244, v244 quad_perm:[1,0,3,2] row_mask:0xf bank_mask:0xf\n\t"
  "v_mfma_f32_16x16x32_bf16 v[52:55], v[80:83], v[100:103], v[52:55]\n\t"
  "v_mfma_f32_16x16x32_bf16 v[48:51], v[80:83], v[96:99], v[48:51]\n\t"
  "ds_read_b64 v[80:81], %[va] offset:2496\n\t"
  "ds_read_b64 v[82:83], %[va] offset:2504\n\t"
  "v_mfma_f32_16x16x32_bf16 v[60:63], v[84:87], v[104:107], v[60:63]\n\t"
  "buffer_load_dwordx4 a[100:103], %[vl16], %[srd], s57 offen offset:3072 sc1\n\t"
  "v_add_f32_dpp v244, v244, v244 quad_perm:[2,3,0,1] row_mask:0xf bank_mask:0xf\n\t"
  "v_mfma_f32_16x16x32_bf16 v[56:59], v[84:87], v[100:103], v[56:59]\n\t"
  "v_cvt_pk_bf16_f32 v244, 0, v244\n\t"
  "v_add_f32 v245, 1.0, v244\n\t"
  "v_mfma_f32_16x16x32_bf16 v[52:55], v[84:87], v[96:99], v[52:55]\n\t"
  "s_waitcnt lgkmcnt(15)\n\t"
  "ds_read_b64 v[84:85], %[va] offset:3008\n\t"
  "v_mfma_f32_16x16x32_bf16 v[60:63], v[88:91], v[100:103], v[60:63]\n\t"
  "ds_read_b64 v[86:87], %[va] offset:3016\n\t"
  "s_add_u32 s57, s57, 4096\n\t"
  "v_mfma_f32_16x16x32_bf16 v[56:59], v[88:91], v[96:99], v[56:59]\n\t"
  "ds_read_b64 v[88:89], %[va] offset:3520\n\t"
  "ds_read_b64 v[90:91], %[va] offset:3528\n\t"
  "v_mfma_f32_16x16x32_bf16 v[60:63], v[92:95], v[96:99], v[60:63]\n\t"
  "s_waitcnt lgkmcnt(8)\n\t"
  "ds_read_b64 v[92:93], %[va] offset:4032\n\t"
  "v_mfma_f32_16x16x32_bf16 v[60:63], v[64:67], v[156:159], v[60:63]\n\t"
  "ds_read_b64 v[94:95], %[va] offset:4040\n\t"
  "buffer_load_dwordx4 a[104:107], %[vl16], %[srd], s57 offen offset:0 sc1\n\t"
  "v_mfma_f32_16x16x32_bf16 v[56:59], v[64:67], v[152:155], v[56:59]\n\t"
  "v_cvt_pk_bf16_f32 v245, 0, v245\n\t"
  "v_readlane_b32 s46, v244, 0\n\t"
  "v_mfma_f32_16x16x32_bf16 v[52:55], v[64:67], v[148:151], v[52:55]\n\t"
  "v_readlane_b32 s48, v244, 8\n\t"
  "v_readlane_b32 s47, v245, 4\n\t"
  "v_mfma_f32_16x16x32_bf16 v[48:51], v[64:67], v[144:147], v[48:51]\n\t"
  "buffer_load_dwordx4 a[108:111], %[vl16], %[srd], s57 offen offset:1024 sc1\n\t"
  "v_readlane_b32 s51, v245, 12\n\t"
  "v_mfma_f32_16x16x32_bf16 v[44:47], v[64:67], v[140:143], v[44:47]\n\t"
  "v_mfma_f32_16x16x32_bf16 v[40:43], v[64:67], v[136:139], v[40:43]\n\t"
  "s_nop 1\n\t"
  "s_and_b32 s52, s51, 0x7fffffff\n\t"
  "s_cselect_b32 s51, s51, 0x2b800000\n\t"
  "v_mfma_f32_16x16x32_bf16 v[36:39], v[64:67], v[132:135], v[36:39]\n\t"
  "buffer_load_dwordx4 a[112:115], %[vl16], %[srd], s57 offen offset:2048 sc1\n\t"
  "v_mov_b32 v245, s48\n\t"
  "v_mfma_f32_16x16x32_bf16 v[32:35], v[64:67], v[128:131], v[32:35]\n\t"
  "v_mul_f32 v245, s51, v245\n\t"
  "v_mul_f32 v244, s47, v245\n\t"
  "v_mfma_f32_16x16x32_bf16 v[60:63], v[68:71], v[152:155], v[60:63]\n\t"
  "v_mul_f32 v245, s46, v245\n\t"
  "v_mfma_f32_16x16x32_bf16 v[56:59], v[68:71], v[148:151], v[56:59]\n\t"
  "buffer_load_dwordx4 a[116:119], %[vl16], %[srd], s57 offen offset:3072 sc1\n\t"
  "v_mfma_f32_16x16x32_bf16 v[52:55], v[68:71], v[144:147], v[52:55]\n\t"
  "s_waitcnt vmcnt(16)\n\t"
  "s_barrier\n\t"
  "v_mfma_f32_16x16x32_bf16 v[48:51], v[68:71], v[140:143], v[48:51]\n\t"
  "v_mov_b32 v246, s51\n\t"
  "ds_read_b64 v[224:225], %[vx] offset:0\n\t"
  "v_mfma_f32_16x16x32_bf16 v[44:47], v[68:71], v[136:139], v[44:47]\n\t"
  "ds_read_b128 v[160:163], %[vs] offset:0\n\t"
  "ds_read_b128 v[164:167], %[vs2] offset:0\n\t"
  "v_mfma_f32_16x16x32_bf16 v[40:43], v[68:71], v[132:135], v[40:43]\n\t"
  "s_add_u32 s57, s57, 4096\n\t"
  "ds_read_b64 v[226:227], %[vx] offset:2048\n\t"
  "v_mfma_f32_16x16x32_bf16 v[36:39], v[68:71], v[128:131], v[36:39]\n\t"
  "s_waitcnt lgkmcnt(8)\n\t"
  "ds_read_b128 v[168:171], %[vs] offset:2048\n\t"
  "v_mfma_f32_16x16x32_bf16 v[60:63], v[72:75], v[148:151], v[60:63]\n\t"
  "ds_read_b128 v[172:175], %[vs2] offset:2048\n\t"
  "ds_read_b64 v[228:229], %[vx] offset:4096\n\t"
  "v_mfma_f32_16x16x32_bf16 v[56:59], v[72:75], v[144:147], v[56:59]\n\t"
  "buffer_load_dwordx4 a[120:123], %[vl16], %[srd], s57 offen offset:0 sc1\n\t"
  "ds_read_b128 v[176:179], %[vs] offset:4096\n\t"
  "v_mfma_f32_16x16x32_bf16 v[52:55], v[72:75], v[140:143], v[52:55]\n\t"
  "ds_read_b128 v[180:183], %[vs2] offset:4096\n\t"
  "ds_read_b64 v[230:231], %[vx] offset:6144\n\t"
  "v_mfma_f32_16x16x32_bf16 v[48:51], v[72:75], v[136:139], v[48:51]\n\t"
  "ds_read_b128 v[184:187], %[vs] offset:6144\n\t"
  "ds_read_b128 v[188:191], %[vs2] offset:6144\n\t"
  "v_mfma_f32_16x16x32_bf16 v[44:47], v[72:75], v[132:135], v[44:47]\n\t"
  "buffer_load_dwordx4 a[124:127], %[vl16], %[srd], s57 offen offset:1024 sc1\n\t"
  "ds_read_b64 v[232:233], %[vx] offset:8192\n\t"
  "v_mfma_f32_16x16x32_bf16 v[40:43], v[72:75], v[128:131], v[40:43]\n\t"
  "ds_read_b128 v[192:195], %[vs] offset:8192\n\t"
  "ds_read_b128 v[196:199], %[vs2] offset:8192\n\t"
  "v_mfma_f32_16x16x32_bf16 v[60:63], v[76:79], v[144:147], v[60:63]\n\t"
  "ds_read_b64 v[234:235], %[vx] offset:10240\n\t"
  "ds_read_b128 v[200:203], %[vs] offset:10240\n\t"
  "v_mfma_f32_16x16x32_bf16 v[56:59], v[76:79], v[140:143], v[56:59]\n\t"
  "ds_read_b128 v[204:207], %[vs2] offset:10240\n\t"
  "ds_read_b64 v[236:237], %[vx] offset:12288\n\t"
  "v_mfma_f32_16x16x32_bf16 v[52:55], v[76:79], v[136:139], v[52:55]\n\t"
  "buffer_load_dwordx4 a[128:131], %[vl16], %[srd], s57 offen offset:2048 sc1\n\t"
  "ds_read_b128 v[208:211], %[vs] offset:12288\n\t"
  "v_mfma_f32_16x16x32_bf16 v[48:51], v[76:79], v[132:135], v[48:51]\n\t"
  "ds_read_b128 v[212:215], %[vs2] offset:12288\n\t"
  "ds_read_b64 v[238:239], %[vx] offset:14336\n\t"
  "v_mfma_f32_16x16x32_bf16 v[44:47], v[76:79], v[128:131], v[44:47]\n\t"
  "ds_read_b128 v[216:219], %[vs] offset:14336\n\t"
  "ds_read_b128 v[220:223], %[vs2] offset:14336\n\t"
  "v_mfma_f32_16x16x32_bf16 v[60:63], v[80:83], v[140:143], v[60:63]\n\t"
  "buffer_load_dwordx4 a[132:135], %[vl16], %[srd], s57 offen offset:3072 sc1\n\t"
  "v_mfma_f32_16x16x32_bf16 v[56:59], v[80:83], v[136:139], v[56:59]\n\t"
  "v_mfma_f32_16x16x32_bf16 v[52:55], v[80:83], v[132:135], v[52:55]\n\t"
  "v_mfma_f32_16x16x32_bf16 v[48:51], v[80:83], v[128:131], v[48:51]\n\t"
  "v_mfma_f32_16x16x32_bf16 v[60:63], v[84:87], v[136:139], v[60:63]\n\t"
  "v_mfma_f32_16x16x32_bf16 v[56:59], v[84:87], v[132:135], v[56:59]\n\t"
  "v_mfma_f32_16x16x32_bf16 v[52:55], v[84:87], v[128:131], v[52:55]\n\t"
  "s_waitcnt lgkmcnt(15)\n\t"
  "v_mfma_f32_16x16x32_bf16 v[60:63], v[88:91], v[132:135], v[60:63]\n\t"
  "v_mfma_f32_16x16x32_bf16 v[56:59], v[88:91], v[128:131], v[56:59]\n\t"
  "v_mfma_f32_16x16x32_bf16 v[60:63], v[92:95], v[128:131], v[60:63]\n\t"
  "v_lshlrev_b32 v96, 16, v224\n\t"
  "v_and_b32 v97, 0xffff0000, v224\n\t"
  "v_lshlrev_b32 v98, 16, v225\n\t"
  "v_and_b32 v99, 0xffff0000, v225\n\t"
  "v_lshlrev_b32 v100, 16, v226\n\t"
  "v_and_b32 v101, 0xffff0000, v226\n\t"
  "v_lshlrev_b32 v102, 16, v227\n\t"
  "v_and_b32 v103, 0xffff0000, v227\n\t"
  "v_pk_fma_f32 v[160:161], v[96:97], v[160:161], v[162:163]\n\t"
  "v_pk_fma_f32 v[164:165], v[98:99], v[164:165], v[166:167]\n\t"
  "v_pk_fma_f32 v[168:169], v[100:101], v[168:169], v[170:171]\n\t"
  "v_pk_fma_f32 v[172:173], v[102:103], v[172:173], v[174:175]\n\t"
  "v_pk_mul_f32 v[96:97], v[96:97], v[246:247] op_sel_hi:[1,0]\n\t"
  "v_pk_mul_f32 v[98:99], v[98:99], v[246:247] op_sel_hi:[1,0]\n\t"
  "v_pk_mul_f32 v[100:101], v[100:101], v[246:247] op_sel_hi:[1,0]\n\t"
  "v_pk_mul_f32 v[102:103], v[102:103], v[246:247] op_sel_hi:[1,0]\n\t"
  "v_pk_fma_f32 v[160:161], v[160:161], v[244:245], v[244:245] op_sel:[0,0,1] op_sel_hi:[1,0,1]\n\t"
  "v_pk_fma_f32 v[164:165], v[164:165], v[244:245], v[244:245] op_sel:[0,0,1] op_sel_hi:[1,0,1]\n\t"
  "v_pk_fma_f32 v[168:169], v[168:169], v[244:245], v[244:245] op_sel:[0,0,1] op_sel_hi:[1,0,1]\n\t"
  "v_pk_fma_f32 v[172:173], v[172:173], v[244:245], v[244:245] op_sel:[0,0,1] op_sel_hi:[1,0,1]\n\t"
  "v_pk_fma_f32 v[96:97], v[160:161], v[32:33], v[96:97]\n\t"
  "v_pk_fma_f32 v[98:99], v[164:165], v[34:35], v[98:99]\n\t"
  "v_pk_fma_f32 v[100:101], v[168:169], v[36:37], v[100:101]\n\t"
  "v_pk_fma_f32 v[102:103], v[172:173], v[38:39], v[102:103]\n\t"
  "v_cvt_pk_bf16_f32 v64, v96, v97\n\t"
  "v_cvt_pk_bf16_f32 v66, v100, v101\n\t"
  "v_cvt_pk_bf16_f32 v65, v98, v99\n\t"
  "v_cvt_pk_bf16_f32 v67, v102, v103\n\t"
  "s_add_u32 s44, %[sO], 0\n\t"
  "v_permlane16_swap_b32 v64, v66\n\t"
  "v_permlane16_swap_b32 v65, v67\n\t"
  "buffer_store_dwordx4 v[64:67], %[vo], %[srd], s44 offen sc1\n\t"
  "v_lshlrev_b32 v104, 16, v228\n\t"
  "v_and_b32 v105, 0xffff0000, v228\n\t"
  "v_lshlrev_b32 v106, 16, v229\n\t"
  "v_and_b32 v107, 0xffff0000, v229\n\t"
  "s_waitcnt lgkmcnt(14)\n\t"
  "v_lshlrev_b32 v108, 16, v230\n\t"
  "v_and_b32 v109, 0xffff0000, v230\n\t"
  "v_lshlrev_b32 v110, 16, v231\n\t"
  "v_and_b32 v111, 0xffff0000, v231\n\t"
  "v_pk_fma_f32 v[176:177], v[104:105], v[176:177], v[178:179]\n\t"
  "v_pk_fma_f32 v[180:181], v[106:107], v[180:181], v[182:183]\n\t"
  "s_waitcnt lgkmcnt(13)\n\t"
  "v_pk_fma_f32 v[184:185], v[108:109], v[184:185], v[186:187]\n\t"
  "s_waitcnt lgkmcnt(12)\n\t"
  "v_pk_fma_f32 v[188:189], v[110:111], v[188:189], v[190:191]\n\t"
  "v_pk_mul_f32 v[104:105], v[104:105], v[246:247] op_sel_hi:[1,0]\n\t"
  "v_pk_mul_f32 v[106:107], v[106:107], v[246:247] op_sel_hi:[1,0]\n\t"
  "v_pk_mul_f32 v[108:109], v[108:109], v[246:247] op_sel_hi:[1,0]\n\t"
  "v_pk_mul_f32 v[110:111], v[110:111], v[246:247] op_sel_hi:[1,0]\n\t"
  "v_pk_fma_f32 v[176:177], v[176:177], v[244:245], v[244:245] op_sel:[0,0,1] op_sel_hi:[1,0,1]\n\t"
  "v_pk_fma_f32 v[180:181], v[180:181], v[244:245], v[244:245] op_sel:[0,0,1] op_sel_hi:[1,0,1]\n\t"
  "v_pk_fma_f32 v[184:185], v[184:185], v[244:245], v[244:245] op_sel:[0,0,1] op_sel_hi:[1,0,1]\n\t"
  "v_pk_fma_f32 v[188:189], v[188:189], v[244:245], v[244:245] op_sel:[0,0,1] op_sel_hi:[1,0,1]\n\t"
  "v_pk_fma_f32 v[104:105], v[176:177], v[40:41], v[104:105]\n\t"
  "v_pk_fma_f32 v[106:107], v[180:181], v[42:43], v[106:107]\n\t"
  "v_pk_fma_f32 v[108:109], v[184:185], v[44:45], v[108:109]\n\t"
  "v_pk_fma_f32 v[110:111], v[188:189], v[46:47], v[110:111]\n\t"
  "v_cvt_pk_bf16_f32 v68, v104, v105\n\t"
  "v_cvt_pk_bf16_f32 v70, v108, v109\n\t"
  "v_cvt_pk_bf16_f32 v69, v106, v107\n\t"
  "v_cvt_pk_bf16_f32 v71, v110, v111\n\t"
  "s_add_u32 s44, %[sO], 655360\n\t"
  "v_permlane16_swap_b32 v68, v70\n\t"
  "v_permlane16_swap_b32 v69, v71\n\t"
  "buffer_store_dwordx4 v[68:71], %[vo], %[srd], s44 offen sc1\n\t"
  "s_waitcnt lgkmcnt(11)\n\t"
  "v_lshlrev_b32 v112, 16, v232\n\t"
  "v_and_b32 v113, 0xffff0000, v232\n\t"
  "v_lshlrev_b32 v114, 16, v233\n\t"
  "v_and_b32 v115, 0xffff0000, v233\n\t"
  "s_waitcnt lgkmcnt(8)\n\t"
  "v_lshlrev_b32 v116, 16, v234\n\t"
  "v_and_b32 v117, 0xffff0000, v234\n\t"
  "v_lshlrev_b32 v118, 16, v235\n\t"
  "v_and_b32 v119, 0xffff0000, v235\n\t"
  "v_pk_fma_f32 v[192:193], v[112:113], v[192:193], v[194:195]\n\t"
  "v_pk_fma_f32 v[196:197], v[114:115], v[196:197], v[198:199]\n\t"
  "s_waitcnt lgkmcnt(7)\n\t"
  "v_pk_fma_f32 v[200:201], v[116:117], v[200:201], v[202:203]\n\t"
  "s_waitcnt lgkmcnt(6)\n\t"
  "v_pk_fma_f32 v[204:205], v[118:119], v[204:205], v[206:207]\n\t"
  "v_pk_mul_f32 v[112:113], v[112:113], v[246:247] op_sel_hi:[1,0]\n\t"
  "v_pk_mul_f32 v[114:115], v[114:115], v[246:247] op_sel_hi:[1,0]\n\t"
  "v_pk_mul_f32 v[116:117], v[116:117], v[246:247] op_sel_hi:[1,0]\n\t"
  "v_pk_mul_f32 v[118:119], v[118:119], v[246:247] op_sel_hi:[1,0]\n\t"
  "v_pk_fma_f32 v[192:193], v[192:193], v[244:245], v[244:245] op_sel:[0,0,1] op_sel_hi:[1,0,1]\n\t"
  "v_pk_fma_f32 v[196:197], v[196:197], v[244:245], v[244:245] op_sel:[0,0,1] op_sel_hi:[1,0,1]\n\t"
  "v_pk_fma_f32 v[200:201], v[200:201], v[244:245], v[244:245] op_sel:[0,0,1] op_sel_hi:[1,0,1]\n\t"
  "v_pk_fma_f32 v[204:205], v[204:205], v[244:245], v[244:245] op_sel:[0,0,1] op_sel_hi:[1,0,1]\n\t"
  "v_pk_fma_f32 v[112:113], v[192:193], v[48:49], v[112:113]\n\t"
  "v_pk_fma_f32 v[114:115], v[196:197], v[50:51], v[114:115]\n\t"
  "v_pk_fma_f32 v[116:117], v[200:201], v[52:53], v[116:117]\n\t"
  "v_pk_fma_f32 v[118:119], v[204:205], v[54:55], v[118:119]\n\t"
  "v_cvt_pk_bf16_f32 v72, v112, v113\n\t"
  "v_cvt_pk_bf16_f32 v74, v116, v117\n\t"
  "v_cvt_pk_bf16_f32 v73, v114, v115\n\t"
  "v_cvt_pk_bf16_f32 v75, v118, v119\n\t"
  "s_add_u32 s44, %[sO], 1310720\n\t"
  "v_permlane16_swap_b32 v72, v74\n\t"
  "v_permlane16_swap_b32 v73, v75\n\t"
  "buffer_store_dwordx4 v[72:75], %[vo], %[srd], s44 offen sc1\n\t"
  "s_waitcnt lgkmcnt(5)\n\t"
  "v_lshlrev_b32 v120, 16, v236\n\t"
  "v_and_b32 v121, 0xffff0000, v236\n\t"
  "v_lshlrev_b32 v122, 16, v237\n\t"
  "v_and_b32 v123, 0xffff0000, v237\n\t"
  "s_waitcnt lgkmcnt(2)\n\t"
  "v_lshlrev_b32 v124, 16, v238\n\t"
  "v_and_b32 v125, 0xffff0000, v238\n\t"
  "v_lshlrev_b32 v126, 16, v239\n\t"
  "v_and_b32 v127, 0xffff0000, v239\n\t"
  "v_pk_fma_f32 v[208:209], v[120:121], v[208:209], v[210:211]\n\t"
  "v_pk_fma_f32 v[212:213], v[122:123], v[212:213], v[214:215]\n\t"
  "s_waitcnt lgkmcnt(1)\n\t"
  "v_pk_fma_f32 v[216:217], v[124:125], v[216:217], v[218:219]\n\t"
  "s_waitcnt lgkmcnt(0)\n\t"
  "v_pk_fma_f32 v[220:221], v[126:127], v[220:221], v[222:223]\n\t"
  "v_pk_mul_f32 v[120:121], v[120:121], v[246:247] op_sel_hi:[1,0]\n\t"
  "v_pk_mul_f32 v[122:123], v[122:123], v[246:247] op_sel_hi:[1,0]\n\t"
  "v_pk_mul_f32 v[124:125], v[124:125], v[246:247] op_sel_hi:[1,0]\n\t"
  "v_pk_mul_f32 v[126:127], v[126:127], v[246:247] op_sel_hi:[1,0]\n\t"
  "v_pk_fma_f32 v[208:209], v[208:209], v[244:245], v[244:245] op_sel:[0,0,1] op_sel_hi:[1,0,1]\n\t"
  "v_pk_fma_f32 v[212:213], v[212:213], v[244:245], v[244:245] op_sel:[0,0,1] op_sel_hi:[1,0,1]\n\t"
  "v_pk_fma_f32 v[216:217], v[216:217], v[244:245], v[244:245] op_sel:[0,0,1] op_sel_hi:[1,0,1]\n\t"
  "v_pk_fma_f32 v[220:221], v[220:221], v[244:245], v[244:245] op_sel:[0,0,1] op_sel_hi:[1,0,1]\n\t"
  "v_pk_fma_f32 v[120:121], v[208:209], v[56:57], v[120:121]\n\t"
  "v_pk_fma_f32 v[122:123], v[212:213], v[58:59], v[122:123]\n\t"
  "v_pk_fma_f32 v[124:125], v[216:217], v[60:61], v[124:125]\n\t"
  "v_pk_fma_f32 v[126:127], v[220:221], v[62:63], v[126:127]\n\t"
  "v_cvt_pk_bf16_f32 v76, v120, v121\n\t"
  "v_cvt_pk_bf16_f32 v78, v124, v125\n\t"
  "v_cvt_pk_bf16_f32 v77, v122, v123\n\t"
  "v_cvt_pk_bf16_f32 v79, v126, v127\n\t"
  "s_add_u32 s44, %[sO], 1966080\n\t"
  "v_permlane16_swap_b32 v76, v78\n\t"
  "v_permlane16_swap_b32 v77, v79\n\t"
  "buffer_store_dwordx4 v[76:79], %[vo], %[srd], s44 offen sc1\n\t"
  "s_nop 1\n\t"
  "s_branch Lcend%=\n\t"
  "La1_slow%=:\n\t"
  "s_nop 7\n\t"
  "s_nop 3\n\t"
  "s_mov_b32 s49, 0x40000\n\t"
  "La1_loop%=:\n\t"
  "s_sleep 3\n\t"
  "buffer_load_dwordx2 v[160:161], %[vf1], %[srd], 0 offen sc1\n\t"
  "s_waitcnt vmcnt(0)\n\t"
  "v_cmp_ne_u64 vcc, v[252:253], v[160:161]\n\t"
  "s_nop 3\n\t"
  "s_cmp_eq_u64 vcc, 0\n\t"
  "s_cbranch_scc1 La1_back%=\n\t"
  "s_sub_u32 s49, s49, 1\n\t"
  "s_cmp_lg_u32 s49, 0\n\t"
  "s_cbranch_scc1 La1_loop%=\n\t"
  "s_branch La1_back%=\n\t"
  "La2_slow%=:\n\t"
  "s_nop 7\n\t"
  "s_nop 3\n\t"
  "s_mov_b32 s49, 0x40000\n\t"
  "La2_loop%=:\n\t"
  "s_sleep 3\n\t"
  "buffer_load_dwordx2 v[162:163], %[vf2], %[srd], 0 offen sc1\n\t"
  "s_waitcnt vmcnt(0)\n\t"
  "v_cmp_ne_u64 vcc, v[252:253], v[162:163]\n\t"
  "s_nop 3\n\t"
  "s_cmp_eq_u64 vcc, 0\n\t"
  "s_cbranch_scc1 La2_back%=\n\t"
  "s_sub_u32 s49, s49, 1\n\t"
  "s_cmp_lg_u32 s49, 0\n\t"
  "s_cbranch_scc1 La2_loop%=\n\t"
  "s_branch La2_back%=\n\t"
  "Lcend%=:\n\t"
      : [wdone] "=&s"(wdone)
      : [srd] "s"(srd), [sO] "s"(OFF_X2P + d * 128u), [w] "s"(w), [sKV] "s"(OFF_KVT + cb * 128u + w * 655360u),
        [tlo] "s"((u32)tag), [thi] "s"((u32)(tag >> 32)), [sW1] "s"(OFF_W1F + (j * 4 + w) * 61440u),
        [vf2] "v"(c_vf2),
        [vf1] "v"(c_vf1), [vf3] "v"(c_vf3), [vfw] "v"(c_vfw),
        [va] "v"(c_va), [vb0] "v"(c_vb0), [vb1] "v"(c_vb1), [vb2] "v"(c_vb2), [vb3] "v"(c_vb3), [vx] "v"(c_vx), [vs] "v"(c_vs), [vs2] "v"(c_vs2), [vo] "v"(c_vo), [vd] "v"(c_vd),
        [vl16] "v"(c_vl16), [vkv] "v"(c_vkv),
        [vmod] "v"(c_vmod)
      : "memory", "vcc", "scc",
"a0", "a1", "a2", "a3", "a4", "a5", "a6", "a7", "a8", "a9", "a10", "a11", "a12", "a13", "a14", "a15", "a16", "a17", "a18", "a19", "a20", "a21", "a22", "a23", "a24", "a25", "a26", "a27", "a28", "a29", "a30", "a31", "a32", "a33", "a34", "a35", "a36", "a37", "a38", "a39", "a40", "a41", "a42", "a43", "a44", "a45", "a46", "a47", "a48", "a49", "a50", "a51", "a52", "a53", "a54", "a55", "a56", "a57", "a58", "a59", "a60", "a61", "a62", "a63", "a64", "a65", "a66", "a67", "a68", "a69", "a70", "a71", "a72", "a73", "a74", "a75", "a76", "a77", "a78", "a79", "a80", "a81", "a82", "a83", "a84", "a85", "a86", "a87", "a88", "a89", "a90", "a91", "a92", "a93", "a94", "a95", "a96", "a97", "a98", "a99", "a100", "a101", "a102", "a103", "a104", "a105", "a106", "a107", "a108", "a109", "a110", "a111", "a112", "a113", "a114", "a115", "a116", "a117", "a118", "a119", "a120", "a121", "a122", "a123", "a124", "a125", "a126", "a127", "a128", "a129", "a130", "a131", "a132", "a133", "a134", "a135", "s44", "s45", "s46", "s47", "s48", "s49", "s51", "s52", "s53", "s54", "s55", "s56", "s57", "v32", "v33", "v34", "v35", "v36", "v37", "v38", "v39", "v40", "v41", "v42", "v43", "v44", "v45", "v46", "v47", "v48", "v49", "v50", "v51", "v52", "v53", "v54", "v55", "v56", "v57", "v58", "v59", "v60", "v61", "v62", "v63", "v64", "v65", "v66", "v67", "v68", "v69", "v70", "v71", "v72", "v73", "v74", "v75", "v76", "v77", "v78", "v79", "v80", "v81", "v82", "v83", "v84", "v85", "v86", "v87", "v88", "v89", "v90", "v91", "v92", "v93", "v94", "v95", "v96", "v97", "v98", "v99", "v100", "v101", "v102", "v103", "v104", "v105", "v106", "v107", "v108", "v109", "v110", "v111", "v112", "v113", "v114", "v115", "v116", "v117", "v118", "v119", "v120", "v121", "v122", "v123", "v124", "v125", "v126", "v127", "v128", "v129", "v130", "v131", "v132", "v133", "v134", "v135", "v136", "v137", "v138", "v139", "v140", "v141", "v142", "v143", "v144", "v145", "v146", "v147", "v148", "v149", "v150", "v151", "v152", "v153", "v154", "v155", "v156", "v157", "v158", "v159", "v160", "v161", "v162", "v163", "v164", "v165", "v166", "v167", "v168", "v169", "v170", "v171", "v172", "v173", "v174", "v175", "v176", "v177", "v178", "v179", "v180", "v181", "v182", "v183", "v184", "v185", "v186", "v187", "v188", "v189", "v190", "v191", "v192", "v193", "v194", "v195", "v196", "v197", "v198", "v199", "v200", "v201", "v202", "v203", "v204", "v205", "v206", "v207", "v208", "v209", "v210", "v211", "v212", "v213", "v214", "v215", "v216", "v217", "v218", "v219", "v220", "v221", "v222", "v223", "v224", "v225", "v226", "v227", "v228", "v229", "v230", "v231", "v232", "v233", "v234", "v235", "v236", "v237", "v238", "v239", "v244", "v245", "v246", "v247", "v250", "v251", "v252", "v253", "v254", "v255"
    );
  }
  STAMP(4);
  STAMP_OUT(1);
  asm volatile(
  "v_mbcnt_lo_u32_b32 v20, -1, 0\n\t"
  "v_mbcnt_hi_u32_b32 v20, -1, v20\n\t"
  "s_lshl_b32 s52, %[w], 10\n\t"
  "s_add_u32 s52, s52, 143360\n\t"
  "v_lshl_add_u32 v20, v20, 4, s52\n\t"
  "ds_read_b128 v[0:3], v20 offset:0\n\t"
  "ds_read_b128 v[4:7], v20 offset:4096\n\t"
  "ds_read_b128 v[8:11], v20 offset:8192\n\t"
  "ds_read_b128 v[12:15], v20 offset:12288\n\t"
  "ds_read_b128 v[16:19], v20 offset:16384\n\t"
  "s_waitcnt lgkmcnt(0)\n\t"
  "v_mov_b32 v132, %[tlo]\n\t"
  "v_mov_b32 v133, %[thi]\n\t"
  "v_mov_b32 v134, %[tlo]\n\t"
  "v_mov_b32 v135, %[thi]\n\t"
  "s_lshl_b32 s52, %[w], 12\n\t"
  "s_mov_b64 s[60:61], 0\n\t"
  "v_bfrev_b32 v128, 1\n\t"
  "v_add_u32 v124, 40960, v6\n\t"
  "v_mov_b32 v112, 0\n\t"
  "v_mov_b32 v113, 0\n\t"
  "v_mov_b32 v114, 0\n\t"
  "v_mov_b32 v115, 0\n\t"
  "v_mov_b32 v116, 0\n\t"
  "v_mov_b32 v117, 0\n\t"
  "v_mov_b32 v118, 0\n\t"
  "v_mov_b32 v119, 0\n\t"
  "v_mov_b32 v120, 0\n\t"
  "v_mov_b32 v121, 0\n\t"
  "v_and_b32 v136, 0xf0, v1\n\t"
  "v_lshlrev_b32 v137, 3, v136\n\t"
  "v_lshlrev_b32 v136, 2, v136\n\t"
  "s_add_u32 s54, %[sST], 65536\n\t"
  "s_mul_i32 s48, %[w], 20480\n\t"
  "v_add_u32 v141, s48, v1\n\t"
  "v_xor_b32 v109, 32, v5\n\t"
  "v_mov_b32 v108, v5\n\t"
  "v_mul_u32_u24 v139, 3, v1\n\t"
  "s_mov_b32 s47, 261120\n\t"
  "v_not_b32 v126, v132\n\t"
  "v_mov_b32 v127, 0\n\t"
  "s_waitcnt vmcnt(0) lgkmcnt(0)\n\t"
  "s_barrier\n\t"
  "buffer_store_dwordx4 v[132:135], v0, %[srd], 0 offen sc1\n\t"
  "s_movk_i32 s59, 0\n\t"
  "buffer_store_dwordx4 v[112:115], v9, %[srd], s59 offen sc1\n\t"
  "s_movk_i32 s59, 8192\n\t"
  "buffer_store_dwordx4 v[112:115], v9, %[srd], s59 offen sc1\n\t"
  "s_movk_i32 s59, 16384\n\t"
  "buffer_store_dwordx4 v[112:115], v9, %[srd], s59 offen sc1\n\t"
  "s_movk_i32 s59, 24576\n\t"
  "buffer_store_dwordx4 v[112:115], v9, %[srd], s59 offen sc1\n\t"
  "s_bitcmp1_b32 %[wdone], 1\n\t"
  "s_cbranch_scc1 Lw2%=\n\t"
  "s_mov_b32 s49, 0x40000\n\t"
  "Lpw_loop%=:\n\t"
  "buffer_load_dwordx2 v[130:131], v2, %[srd], 0 offen sc1\n\t"
  "s_waitcnt vmcnt(0)\n\t"
  "v_cmp_eq_u64 vcc, v[132:133], v[130:131]\n\t"
  "s_nop 3\n\t"
  "s_orn2_b64 vcc, vcc, 15\n\t"
  "s_cmp_eq_u64 vcc, -1\n\t"
  "s_cbranch_scc1 Lpw_done%=\n\t"
  "s_sleep 3\n\t"
  "s_sub_u32 s49, s49, 1\n\t"
  "s_cmp_lg_u32 s49, 0\n\t"
  "s_cbranch_scc1 Lpw_loop%=\n\t"
  "Lpw_done%=:\n\t"
  "s_add_u32 s44, %[sW1], 12288\n\t"
  "buffer_load_dwordx4 a[56:59], v1, %[srd], s44 offen offset:2048 sc1\n\t"
  "s_add_u32 s44, %[sW1], 12288\n\t"
  "buffer_load_dwordx4 a[60:63], v1, %[srd], s44 offen offset:3072 sc1\n\t"
  "s_add_u32 s44, %[sW1], 16384\n\t"
  "buffer_load_dwordx4 a[64:67], v1, %[srd], s44 offen offset:0 sc1\n\t"
  "s_add_u32 s44, %[sW1], 16384\n\t"
  "buffer_load_dwordx4 a[68:71], v1, %[srd], s44 offen offset:1024 sc1\n\t"
  "s_add_u32 s44, %[sW1], 16384\n\t"
  "buffer_load_dwordx4 a[72:75], v1, %[srd], s44 offen offset:2048 sc1\n\t"
  "s_add_u32 s44, %[sW1], 16384\n\t"
  "buffer_load_dwordx4 a[76:79], v1, %[srd], s44 offen offset:3072 sc1\n\t"
  "s_add_u32 s44, %[sW1], 20480\n\t"
  "buffer_load_dwordx4 a[80:83], v1, %[srd], s44 offen offset:0 sc1\n\t"
  "s_add_u32 s44, %[sW1], 20480\n\t"
  "buffer_load_dwordx4 a[84:87], v1, %[srd], s44 offen offset:1024 sc1\n\t"
  "s_add_u32 s44, %[sW1], 20480\n\t"
  "buffer_load_dwordx4 a[88:91], v1, %[srd], s44 offen offset:2048 sc1\n\t"
  "s_add_u32 s44, %[sW1], 20480\n\t"
  "buffer_load_dwordx4 a[92:95], v1, %[srd], s44 offen offset:3072 sc1\n\t"
  "s_add_u32 s44, %[sW1], 24576\n\t"
  "buffer_load_dwordx4 a[96:99], v1, %[srd], s44 offen offset:0 sc1\n\t"
  "s_add_u32 s44, %[sW1], 24576\n\t"
  "buffer_load_dwordx4 a[100:103], v1, %[srd], s44 offen offset:1024 sc1\n\t"
  "s_add_u32 s44, %[sW1], 24576\n\t"
  "buffer_load_dwordx4 a[104:107], v1, %[srd], s44 offen offset:2048 sc1\n\t"
  "s_add_u32 s44, %[sW1], 24576\n\t"
  "buffer_load_dwordx4 a[108:111], v1, %[srd], s44 offen offset:3072 sc1\n\t"
  "s_add_u32 s44, %[sW1], 28672\n\t"
  "buffer_load_dwordx4 a[112:115], v1, %[srd], s44 offen offset:0 sc1\n\t"
  "s_add_u32 s44, %[sW1], 28672\n\t"
  "buffer_load_dwordx4 a[116:119], v1, %[srd], s44 offen offset:1024 sc1\n\t"
  "s_add_u32 s44, %[sW1], 28672\n\t"
  "buffer_load_dwordx4 a[120:123], v1, %[srd], s44 offen offset:2048 sc1\n\t"
  "s_add_u32 s44, %[sW1], 28672\n\t"
  "buffer_load_dwordx4 a[124:127], v1, %[srd], s44 offen offset:3072 sc1\n\t"
  "s_add_u32 s44, %[sW1], 32768\n\t"
  "buffer_load_dwordx4 a[128:131], v1, %[srd], s44 offen offset:0 sc1\n\t"
  "s_add_u32 s44, %[sW1], 32768\n\t"
  "buffer_load_dwordx4 a[132:135], v1, %[srd], s44 offen offset:1024 sc1\n\t"
  "Lw2%=:\n\t"
  "s_bitcmp1_b32 %[wdone], 0\n\t"
  "s_cbranch_scc1 Lw1%=\n\t"
  "s_add_u32 s44, %[sW1], 0\n\t"
  "buffer_load_dwordx4 a[0:3], v1, %[srd], s44 offen offset:0 sc1\n\t"
  "s_add_u32 s44, %[sW1], 0\n\t"
  "buffer_load_dwordx4 a[4:7], v1, %[srd], s44 offen offset:1024 sc1\n\t"
  "s_add_u32 s44, %[sW1], 0\n\t"
  "buffer_load_dwordx4 a[8:11], v1, %[srd], s44 offen offset:2048 sc1\n\t"
  "s_add_u32 s44, %[sW1], 0\n\t"
  "buffer_load_dwordx4 a[12:15], v1, %[srd], s44 offen offset:3072 sc1\n\t"
  "s_add_u32 s44, %[sW1], 4096\n\t"
  "buffer_load_dwordx4 a[16:19], v1, %[srd], s44 offen offset:0 sc1\n\t"
  "s_add_u32 s44, %[sW1], 4096\n\t"
  "buffer_load_dwordx4 a[20:23], v1, %[srd], s44 offen offset:1024 sc1\n\t"
  "s_add_u32 s44, %[sW1], 4096\n\t"
  "buffer_load_dwordx4 a[24:27], v1, %[srd], s44 offen offset:2048 sc1\n\t"
  "s_add_u32 s44, %[sW1], 4096\n\t"
  "buffer_load_dwordx4 a[28:31], v1, %[srd], s44 offen offset:3072 sc1\n\t"
  "s_add_u32 s44, %[sW1], 8192\n\t"
  "buffer_load_dwordx4 a[32:35], v1, %[srd], s44 offen offset:0 sc1\n\t"
  "s_add_u32 s44, %[sW1], 8192\n\t"
  "buffer_load_dwordx4 a[36:39], v1, %[srd], s44 offen offset:1024 sc1\n\t"
  "s_add_u32 s44, %[sW1], 8192\n\t"
  "buffer_load_dwordx4 a[40:43], v1, %[srd], s44 offen offset:2048 sc1\n\t"
  "s_add_u32 s44, %[sW1], 8192\n\t"
  "buffer_load_dwordx4 a[44:47], v1, %[srd], s44 offen offset:3072 sc1\n\t"
  "s_add_u32 s44, %[sW1], 12288\n\t"
  "buffer_load_dwordx4 a[48:51], v1, %[srd], s44 offen offset:0 sc1\n\t"
  "s_add_u32 s44, %[sW1], 12288\n\t"
  "buffer_load_dwordx4 a[52:55], v1, %[srd], s44 offen offset:1024 sc1\n\t"
  "Lw1%=:\n\t"
  "s_mov_b32 s58, 4\n\t"
  "s_bitcmp1_b32 %[wdone], 2\n\t"
  "s_cbranch_scc1 Lpx_chk%=\n\t"
  "s_mov_b32 s58, 0\n\t"
  "v_cmp_eq_u64 vcc, v[132:133], v[126:127]\n\t"
  "s_nop 3\n\t"
  "s_or_b64 s[60:61], s[60:61], vcc\n\t"
  "s_cmp_eq_u64 s[60:61], -1\n\t"
  "s_cbranch_scc1 Lpx_done%=\n\t"
  "v_cndmask_b32 v3, v3, v128, vcc\n\t"
  "buffer_load_dwordx2 v[126:127], v3, %[srd], 0 offen sc1\n\t"
  "s_add_u32 s44, %[sW1], 32768\n\t"
  "buffer_load_dwordx4 a[136:139], v1, %[srd], s44 offen offset:2048 sc1\n\t"
  "s_add_u32 s44, %[sW1], 32768\n\t"
  "buffer_load_dwordx4 a[140:143], v1, %[srd], s44 offen offset:3072 sc1\n\t"
  "s_add_u32 s44, %[sW1], 36864\n\t"
  "buffer_load_dwordx4 a[144:147], v1, %[srd], s44 offen offset:0 sc1\n\t"
  "s_add_u32 s44, %[sW1], 36864\n\t"
  "buffer_load_dwordx4 a[148:151], v1, %[srd], s44 offen offset:1024 sc1\n\t"
  "s_add_u32 s44, %[sW1], 36864\n\t"
  "buffer_load_dwordx4 a[152:155], v1, %[srd], s44 offen offset:2048 sc1\n\t"
  "s_add_u32 s44, %[sW1], 36864\n\t"
  "buffer_load_dwordx4 a[156:159], v1, %[srd], s44 offen offset:3072 sc1\n\t"
  "s_waitcnt vmcnt(6)\n\t"
  "s_mov_b32 s58, 1\n\t"
  "v_cmp_eq_u64 vcc, v[132:133], v[126:127]\n\t"
  "s_nop 3\n\t"
  "s_or_b64 s[60:61], s[60:61], vcc\n\t"
  "s_cmp_eq_u64 s[60:61], -1\n\t"
  "s_cbranch_scc1 Lpx_done%=\n\t"
  "v_cndmask_b32 v3, v3, v128, vcc\n\t"
  "buffer_load_dwordx2 v[126:127], v3, %[srd], 0 offen sc1\n\t"
  "s_add_u32 s44, %[sW1], 40960\n\t"
  "buffer_load_dwordx4 a[160:163], v1, %[srd], s44 offen offset:0 sc1\n\t"
  "s_add_u32 s44, %[sW1], 40960\n\t"
  "buffer_load_dwordx4 a[164:167], v1, %[srd], s44 offen offset:1024 sc1\n\t"
  "s_add_u32 s44, %[sW1], 40960\n\t"
  "buffer_load_dwordx4 a[168:171], v1, %[srd], s44 offen offset:2048 sc1\n\t"
  "s_add_u32 s44, %[sW1], 40960\n\t"
  "buffer_load_dwordx4 a[172:175], v1, %[srd], s44 offen offset:3072 sc1\n\t"
  "s_add_u32 s44, %[sW1], 45056\n\t"
  "buffer_load_dwordx4 a[176:179], v1, %[srd], s44 offen offset:0 sc1\n\t"
  "s_add_u32 s44, %[sW1], 45056\n\t"
  "buffer_load_dwordx4 a[180:183], v1, %[srd], s44 offen offset:1024 sc1\n\t"
  "s_add_u32 s44, %[sW1], 45056\n\t"
  "buffer_load_dwordx4 a[184:187], v1, %[srd], s44 offen offset:2048 sc1\n\t"
  "s_waitcnt vmcnt(7)\n\t"
  "s_mov_b32 s58, 2\n\t"
  "v_cmp_eq_u64 vcc, v[132:133], v[126:127]\n\t"
  "s_nop 3\n\t"
  "s_or_b64 s[60:61], s[60:61], vcc\n\t"
  "s_cmp_eq_u64 s[60:61], -1\n\t"
  "s_cbranch_scc1 Lpx_done%=\n\t"
  "v_cndmask_b32 v3, v3, v128, vcc\n\t"
  "buffer_load_dwordx2 v[126:127], v3, %[srd], 0 offen sc1\n\t"
  "s_add_u32 s44, %[sW1], 45056\n\t"
  "buffer_load_dwordx4 a[188:191], v1, %[srd], s44 offen offset:3072 sc1\n\t"
  "s_add_u32 s44, %[sW1], 49152\n\t"
  "buffer_load_dwordx4 a[192:195], v1, %[srd], s44 offen offset:0 sc1\n\t"
  "s_add_u32 s44, %[sW1], 49152\n\t"
  "buffer_load_dwordx4 a[196:199], v1, %[srd], s44 offen offset:1024 sc1\n\t"
  "s_add_u32 s44, %[sW1], 49152\n\t"
  "buffer_load_dwordx4 a[200:203], v1, %[srd], s44 offen offset:2048 sc1\n\t"
  "s_add_u32 s44, %[sW1], 49152\n\t"
  "buffer_load_dwordx4 a[204:207], v1, %[srd], s44 offen offset:3072 sc1\n\t"
  "s_add_u32 s44, %[sW1], 53248\n\t"
  "buffer_load_dwordx4 a[208:211], v1, %[srd], s44 offen offset:0 sc1\n\t"
  "s_add_u32 s44, %[sW1], 53248\n\t"
  "buffer_load_dwordx4 a[212:215], v1, %[srd], s44 offen offset:1024 sc1\n\t"
  "s_waitcnt vmcnt(7)\n\t"
  "s_mov_b32 s58, 3\n\t"
  "v_cmp_eq_u64 vcc, v[132:133], v[126:127]\n\t"
  "s_nop 3\n\t"
  "s_or_b64 s[60:61], s[60:61], vcc\n\t"
  "s_cmp_eq_u64 s[60:61], -1\n\t"
  "s_cbranch_scc1 Lpx_done%=\n\t"
  "v_cndmask_b32 v3, v3, v128, vcc\n\t"
  "buffer_load_dwordx2 v[126:127], v3, %[srd], 0 offen sc1\n\t"
  "s_add_u32 s44, %[sW1], 53248\n\t"
  "buffer_load_dwordx4 a[216:219], v1, %[srd], s44 offen offset:2048 sc1\n\t"
  "s_add_u32 s44, %[sW1], 53248\n\t"
  "buffer_load_dwordx4 a[220:223], v1, %[srd], s44 offen offset:3072 sc1\n\t"
  "s_add_u32 s44, %[sW1], 57344\n\t"
  "buffer_load_dwordx4 a[224:227], v1, %[srd], s44 offen offset:0 sc1\n\t"
  "s_add_u32 s44, %[sW1], 57344\n\t"
  "buffer_load_dwordx4 a[228:231], v1, %[srd], s44 offen offset:1024 sc1\n\t"
  "s_add_u32 s44, %[sW1], 57344\n\t"
  "buffer_load_dwordx4 a[232:235], v1, %[srd], s44 offen offset:2048 sc1\n\t"
  "s_add_u32 s44, %[sW1], 57344\n\t"
  "buffer_load_dwordx4 a[236:239], v1, %[srd], s44 offen offset:3072 sc1\n\t"
  "s_waitcnt vmcnt(6)\n\t"
  "s_mov_b32 s58, 4\n\t"
  "Lpx_chk%=:\n\t"
  "s_mov_b32 s49, 0x40000\n\t"
  "Lpx_loop%=:\n\t"
  "v_cmp_eq_u64 vcc, v[132:133], v[126:127]\n\t"
  "s_nop 3\n\t"
  "s_or_b64 s[60:61], s[60:61], vcc\n\t"
  "s_cmp_eq_u64 s[60:61], -1\n\t"
  "s_cbranch_scc1 Lpx_done%=\n\t"
  "v_cndmask_b32 v3, v3, v128, vcc\n\t"
  "s_sleep 2\n\t"
  "buffer_load_dwordx2 v[126:127], v3, %[srd], 0 offen sc1\n\t"
  "s_waitcnt vmcnt(0)\n\t"
  "s_sub_u32 s49, s49, 1\n\t"
  "s_cmp_lg_u32 s49, 0\n\t"
  "s_cbranch_scc1 Lpx_loop%=\n\t"
  "Lpx_done%=:\n\t"
  "s_add_u32 m0, s52, 0\n\t"
  "s_mov_b32 s46, 0\n\t"
  "buffer_load_dwordx4 v4, %[srd], s46 offen offset:0 sc1 lds\n\t"
  "buffer_load_dwordx4 v4, %[srd], s46 offen offset:1024 sc1 lds\n\t"
  "buffer_load_dwordx4 v4, %[srd], s46 offen offset:2048 sc1 lds\n\t"
  "buffer_load_dwordx4 v4, %[srd], s46 offen offset:3072 sc1 lds\n\t"
  "s_add_u32 m0, s52, 16384\n\t"
  "s_mov_b32 s46, 16384\n\t"
  "buffer_load_dwordx4 v4, %[srd], s46 offen offset:0 sc1 lds\n\t"
  "buffer_load_dwordx4 v4, %[srd], s46 offen offset:1024 sc1 lds\n\t"
  "buffer_load_dwordx4 v4, %[srd], s46 offen offset:2048 sc1 lds\n\t"
  "buffer_load_dwordx4 v4, %[srd], s46 offen offset:3072 sc1 lds\n\t"
  "s_add_u32 m0, s52, 32768\n\t"
  "s_mov_b32 s46, 32768\n\t"
  "buffer_load_dwordx4 v4, %[srd], s46 offen offset:0 sc1 lds\n\t"
  "buffer_load_dwordx4 v4, %[srd], s46 offen offset:1024 sc1 lds\n\t"
  "buffer_load_dwordx4 v4, %[srd], s46 offen offset:2048 sc1 lds\n\t"
  "buffer_load_dwordx4 v4, %[srd], s46 offen offset:3072 sc1 lds\n\t"
  "s_add_u32 m0, s52, 49152\n\t"
  "s_mov_b32 s46, 49152\n\t"
  "buffer_load_dwordx4 v4, %[srd], s46 offen offset:0 sc1 lds\n\t"
  "buffer_load_dwordx4 v4, %[srd], s46 offen offset:1024 sc1 lds\n\t"
  "buffer_load_dwordx4 v4, %[srd], s46 offen offset:2048 sc1 lds\n\t"
  "buffer_load_dwordx4 v4, %[srd], s46 offen offset:3072 sc1 lds\n\t"
  "s_add_u32 m0, s52, 65536\n\t"
  "s_mov_b32 s46, 65536\n\t"
  "buffer_load_dwordx4 v4, %[srd], s46 offen offset:0 sc1 lds\n\t"
  "buffer_load_dwordx4 v4, %[srd], s46 offen offset:1024 sc1 lds\n\t"
  "buffer_load_dwordx4 v4, %[srd], s46 offen offset:2048 sc1 lds\n\t"
  "buffer_load_dwordx4 v4, %[srd], s46 offen offset:3072 sc1 lds\n\t"
  "buffer_load_dwordx2 v[142:143], v139, %[srd], s47 offen sc1\n\t"
  "buffer_load_dwordx4 v[248:251], v139, %[srd], s47 offen offset:16 sc1\n\t"
  "buffer_load_dwordx4 v[252:255], v139, %[srd], s47 offen offset:32 sc1\n\t"
  "s_cmp_gt_u32 s58, 0\n\t"
  "s_cbranch_scc1 Lc0%=\n\t"
  "s_add_u32 s44, %[sW1], 32768\n\t"
  "buffer_load_dwordx4 a[136:139], v1, %[srd], s44 offen offset:2048 sc1\n\t"
  "s_add_u32 s44, %[sW1], 32768\n\t"
  "buffer_load_dwordx4 a[140:143], v1, %[srd], s44 offen offset:3072 sc1\n\t"
  "s_add_u32 s44, %[sW1], 36864\n\t"
  "buffer_load_dwordx4 a[144:147], v1, %[srd], s44 offen offset:0 sc1\n\t"
  "s_add_u32 s44, %[sW1], 36864\n\t"
  "buffer_load_dwordx4 a[148:151], v1, %[srd], s44 offen offset:1024 sc1\n\t"
  "s_add_u32 s44, %[sW1], 36864\n\t"
  "buffer_load_dwordx4 a[152:155], v1, %[srd], s44 offen offset:2048 sc1\n\t"
  "s_add_u32 s44, %[sW1], 36864\n\t"
  "buffer_load_dwordx4 a[156:159], v1, %[srd], s44 offen offset:3072 sc1\n\t"
  "Lc0%=:\n\t"
  "s_cmp_gt_u32 s58, 1\n\t"
  "s_cbranch_scc1 Lc1%=\n\t"
  "s_add_u32 s44, %[sW1], 40960\n\t"
  "buffer_load_dwordx4 a[160:163], v1, %[srd], s44 offen offset:0 sc1\n\t"
  "s_add_u32 s44, %[sW1], 40960\n\t"
  "buffer_load_dwordx4 a[164:167], v1, %[srd], s44 offen offset:1024 sc1\n\t"
  "s_add_u32 s44, %[sW1], 40960\n\t"
  "buffer_load_dwordx4 a[168:171], v1, %[srd], s44 offen offset:2048 sc1\n\t"
  "s_add_u32 s44, %[sW1], 40960\n\t"
  "buffer_load_dwordx4 a[172:175], v1, %[srd], s44 offen offset:3072 sc1\n\t"
  "s_add_u32 s44, %[sW1], 45056\n\t"
  "buffer_load_dwordx4 a[176:179], v1, %[srd], s44 offen offset:0 sc1\n\t"
  "s_add_u32 s44, %[sW1], 45056\n\t"
  "buffer_load_dwordx4 a[180:183], v1, %[srd], s44 offen offset:1024 sc1\n\t"
  "s_add_u32 s44, %[sW1], 45056\n\t"
  "buffer_load_dwordx4 a[184:187], v1, %[srd], s44 offen offset:2048 sc1\n\t"
  "Lc1%=:\n\t"
  "s_cmp_gt_u32 s58, 2\n\t"
  "s_cbranch_scc1 Lc2%=\n\t"
  "s_add_u32 s44, %[sW1], 45056\n\t"
  "buffer_load_dwordx4 a[188:191], v1, %[srd], s44 offen offset:3072 sc1\n\t"
  "s_add_u32 s44, %[sW1], 49152\n\t"
  "buffer_load_dwordx4 a[192:195], v1, %[srd], s44 offen offset:0 sc1\n\t"
  "s_add_u32 s44, %[sW1], 49152\n\t"
  "buffer_load_dwordx4 a[196:199], v1, %[srd], s44 offen offset:1024 sc1\n\t"
  "s_add_u32 s44, %[sW1], 49152\n\t"
  "buffer_load_dwordx4 a[200:203], v1, %[srd], s44 offen offset:2048 sc1\n\t"
  "s_add_u32 s44, %[sW1], 49152\n\t"
  "buffer_load_dwordx4 a[204:207], v1, %[srd], s44 offen offset:3072 sc1\n\t"
  "s_add_u32 s44, %[sW1], 53248\n\t"
  "buffer_load_dwordx4 a[208:211], v1, %[srd], s44 offen offset:0 sc1\n\t"
  "s_add_u32 s44, %[sW1], 53248\n\t"
  "buffer_load_dwordx4 a[212:215], v1, %[srd], s44 offen offset:1024 sc1\n\t"
  "Lc2%=:\n\t"
  "s_cmp_gt_u32 s58, 3\n\t"
  "s_cbranch_scc1 Lc3%=\n\t"
  "s_add_u32 s44, %[sW1], 53248\n\t"
  "buffer_load_dwordx4 a[216:219], v1, %[srd], s44 offen offset:2048 sc1\n\t"
  "s_add_u32 s44, %[sW1], 53248\n\t"
  "buffer_load_dwordx4 a[220:223], v1, %[srd], s44 offen offset:3072 sc1\n\t"
  "s_add_u32 s44, %[sW1], 57344\n\t"
  "buffer_load_dwordx4 a[224:227], v1, %[srd], s44 offen offset:0 sc1\n\t"
  "s_add_u32 s44, %[sW1], 57344\n\t"
  "buffer_load_dwordx4 a[228:231], v1, %[srd], s44 offen offset:1024 sc1\n\t"
  "s_add_u32 s44, %[sW1], 57344\n\t"
  "buffer_load_dwordx4 a[232:235], v1, %[srd], s44 offen offset:2048 sc1\n\t"
  "s_add_u32 s44, %[sW1], 57344\n\t"
  "buffer_load_dwordx4 a[236:239], v1, %[srd], s44 offen offset:3072 sc1\n\t"
  "Lc3%=:\n\t"
  "s_waitcnt vmcnt(26)\n\t"
  "s_cmp_eq_u32 s58, 0\n\t"
  "s_cbranch_scc1 Lcw%=\n\t"
  "s_waitcnt vmcnt(20)\n\t"
  "s_cmp_eq_u32 s58, 1\n\t"
  "s_cbranch_scc1 Lcw%=\n\t"
  "s_waitcnt vmcnt(13)\n\t"
  "s_cmp_eq_u32 s58, 2\n\t"
  "s_cbranch_scc1 Lcw%=\n\t"
  "s_waitcnt vmcnt(6)\n\t"
  "s_cmp_eq_u32 s58, 3\n\t"
  "s_cbranch_scc1 Lcw%=\n\t"
  "s_waitcnt vmcnt(0)\n\t"
  "Lcw%=:\n\t"
  "s_barrier\n\t"
  "ds_read_b64_tr_b16 v[68:69], v108 offset:0\n\t"
  "ds_read_b64_tr_b16 v[70:71], v108 offset:2048\n\t"
  "ds_read_b64_tr_b16 v[72:73], v109 offset:0\n\t"
  "ds_read_b64_tr_b16 v[74:75], v109 offset:2048\n\t"
  "ds_read_b64_tr_b16 v[76:77], v108 offset:4096\n\t"
  "ds_read_b64_tr_b16 v[78:79], v108 offset:6144\n\t"
  "ds_read_b64_tr_b16 v[80:81], v109 offset:4096\n\t"
  "ds_read_b64_tr_b16 v[82:83], v109 offset:6144\n\t"
  "ds_read_u16_d16_hi v112, v6 offset:0\n\t"
  "ds_read_u16_d16_hi v113, v6 offset:8192\n\t"
  "ds_read_u16_d16_hi v114, v6 offset:16384\n\t"
  "ds_read_u16_d16_hi v115, v6 offset:24576\n\t"
  "ds_read_u16_d16_hi v116, v6 offset:32768\n\t"
  "ds_read_u16_d16_hi v117, v124 offset:0\n\t"
  "ds_read_u16_d16_hi v118, v124 offset:8192\n\t"
  "ds_read_u16_d16_hi v119, v124 offset:16384\n\t"
  "ds_read_u16_d16_hi v120, v124 offset:24576\n\t"
  "ds_read_u16_d16_hi v121, v124 offset:32768\n\t"
  "s_waitcnt lgkmcnt(15)\n\t"
  "v_mfma_f32_16x16x32_bf16 v[20:23], a[0:3], v[68:71], 0\n\t"
  "ds_read_b64_tr_b16 v[84:85], v108 offset:8192\n\t"
  "v_add_u32 v110, 40960, v5\n\t"
  "v_mfma_f32_16x16x32_bf16 v[24:27], a[4:7], v[68:71], 0\n\t"
  "ds_read_b64_tr_b16 v[86:87], v108 offset:10240\n\t"
  "v_xor_b32 v111, 32, v5\n\t"
  "v_mfma_f32_16x16x32_bf16 v[28:31], a[8:11], v[68:71], 0\n\t"
  "ds_read_b64_tr_b16 v[88:89], v109 offset:8192\n\t"
  "v_add_u32 v111, 40960, v111\n\t"
  "s_waitcnt lgkmcnt(15)\n\t"
  "v_mfma_f32_16x16x32_bf16 v[32:35], a[0:3], v[72:75], 0\n\t"
  "ds_read_b64_tr_b16 v[90:91], v109 offset:10240\n\t"
  "s_add_u32 m0, s52, 81920\n\t"
  "v_mfma_f32_16x16x32_bf16 v[36:39], a[4:7], v[72:75], 0\n\t"
  "s_mov_b32 s46, 1310720\n\t"
  "buffer_load_dwordx4 v4, %[srd], s46 offen offset:0 sc1 lds\n\t"
  "v_mfma_f32_16x16x32_bf16 v[40:43], a[8:11], v[72:75], 0\n\t"
  "s_waitcnt lgkmcnt(14)\n\t"
  "v_mfma_f32_16x16x32_bf16 v[20:23], a[12:15], v[76:79], v[20:23]\n\t"
  "ds_read_b64_tr_b16 v[68:69], v108 offset:12288\n\t"
  "v_mfma_f32_16x16x32_bf16 v[24:27], a[16:19], v[76:79], v[24:27]\n\t"
  "ds_read_b64_tr_b16 v[70:71], v108 offset:14336\n\t"
  "buffer_load_dwordx4 v4, %[srd], s46 offen offset:1024 sc1 lds\n\t"
  "v_mfma_f32_16x16x32_bf16 v[28:31], a[20:23], v[76:79], v[28:31]\n\t"
  "ds_read_b64_tr_b16 v[72:73], v109 offset:12288\n\t"
  "v_mfma_f32_16x16x32_bf16 v[32:35], a[12:15], v[80:83], v[32:35]\n\t"
  "ds_read_b64_tr_b16 v[74:75], v109 offset:14336\n\t"
  "s_waitcnt lgkmcnt(15)\n\t"
  "v_mul_f32 v112, v112, v142\n\t"
  "v_mul_f32 v113, v113, v143\n\t"
  "v_mfma_f32_16x16x32_bf16 v[36:39], a[16:19], v[80:83], v[36:39]\n\t"
  "v_mul_f32 v114, v114, v248\n\t"
  "s_waitcnt lgkmcnt(14)\n\t"
  "v_mul_f32 v115, v115, v249\n\t"
  "s_waitcnt lgkmcnt(13)\n\t"
  "v_mul_f32 v116, v116, v250\n\t"
  "v_mfma_f32_16x16x32_bf16 v[40:43], a[20:23], v[80:83], v[40:43]\n\t"
  "s_waitcnt lgkmcnt(4)\n\t"
  "v_mul_f32 v117, v117, v251\n\t"
  "v_mul_f32 v118, v118, v252\n\t"
  "v_mul_f32 v119, v119, v253\n\t"
  "v_mfma_f32_16x16x32_bf16 v[20:23], a[24:27], v[84:87], v[20:23]\n\t"
  "ds_read_b64_tr_b16 v[76:77], v108 offset:16384\n\t"
  "v_mul_f32 v120, v120, v254\n\t"
  "v_mul_f32 v121, v121, v255\n\t"
  "v_add_f32 v122, v112, v113\n\t"
  "v_mfma_f32_16x16x32_bf16 v[24:27], a[28:31], v[84:87], v[24:27]\n\t"
  "ds_read_b64_tr_b16 v[78:79], v108 offset:18432\n\t"
  "v_mul_f32 v123, v112, v112\n\t"
  "v_fmac_f32 v123, v113, v113\n\t"
  "v_add_f32 v122, v122, v114\n\t"
  "v_mfma_f32_16x16x32_bf16 v[28:31], a[32:35], v[84:87], v[28:31]\n\t"
  "ds_read_b64_tr_b16 v[80:81], v109 offset:16384\n\t"
  "v_fmac_f32 v123, v114, v114\n\t"
  "v_add_f32 v122, v122, v115\n\t"
  "v_fmac_f32 v123, v115, v115\n\t"
  "v_mfma_f32_16x16x32_bf16 v[32:35], a[24:27], v[88:91], v[32:35]\n\t"
  "ds_read_b64_tr_b16 v[82:83], v109 offset:18432\n\t"
  "v_add_f32 v122, v122, v116\n\t"
  "v_fmac_f32 v123, v116, v116\n\t"
  "v_add_f32 v122, v122, v117\n\t"
  "v_mfma_f32_16x16x32_bf16 v[36:39], a[28:31], v[88:91], v[36:39]\n\t"
  "v_fmac_f32 v123, v117, v117\n\t"
  "v_add_f32 v122, v122, v118\n\t"
  "v_fmac_f32 v123, v118, v118\n\t"
  "v_add_f32 v122, v122, v119\n\t"
  "v_mfma_f32_16x16x32_bf16 v[40:43], a[32:35], v[88:91], v[40:43]\n\t"
  "s_waitcnt lgkmcnt(4)\n\t"
  "v_fmac_f32 v123, v119, v119\n\t"
  "v_add_f32 v122, v122, v120\n\t"
  "v_fmac_f32 v123, v120, v120\n\t"
  "v_mfma_f32_16x16x32_bf16 v[20:23], a[36:39], v[68:71], v[20:23]\n\t"
  "ds_read_b64_tr_b16 v[84:85], v108 offset:20480\n\t"
  "v_add_f32 v122, v122, v121\n\t"
  "v_fmac_f32 v123, v121, v121\n\t"
  "buffer_load_dwordx4 v4, %[srd], s46 offen offset:2048 sc1 lds\n\t"
  "v_mfma_f32_16x16x32_bf16 v[24:27], a[40:43], v[68:71], v[24:27]\n\t"
  "ds_read_b64_tr_b16 v[86:87], v108 offset:22528\n\t"
  "v_add_f32_dpp v122, v122, v122 quad_perm:[1,0,3,2] row_mask:0xf bank_mask:0xf\n\t"
  "v_add_f32_dpp v123, v123, v123 quad_perm:[1,0,3,2] row_mask:0xf bank_mask:0xf\n\t"
  "buffer_load_dwordx4 v4, %[srd], s46 offen offset:3072 sc1 lds\n\t"
  "v_mfma_f32_16x16x32_bf16 v[28:31], a[44:47], v[68:71], v[28:31]\n\t"
  "ds_read_b64_tr_b16 v[88:89], v109 offset:20480\n\t"
  "v_add_f32_dpp v122, v122, v122 quad_perm:[2,3,0,1] row_mask:0xf bank_mask:0xf\n\t"
  "v_add_f32_dpp v123, v123, v123 quad_perm:[2,3,0,1] row_mask:0xf bank_mask:0xf\n\t"
  "s_add_u32 m0, s52, 98304\n\t"
  "v_mfma_f32_16x16x32_bf16 v[32:35], a[36:39], v[72:75], v[32:35]\n\t"
  "ds_read_b64_tr_b16 v[90:91], v109 offset:22528\n\t"
  "v_add_f32_dpp v122, v122, v122 row_half_mirror row_mask:0xf bank_mask:0xf\n\t"
  "v_add_f32_dpp v123, v123, v123 row_half_mirror row_mask:0xf bank_mask:0xf\n\t"
  "s_mov_b32 s46, 1327104\n\t"
  "v_mfma_f32_16x16x32_bf16 v[36:39], a[40:43], v[72:75], v[36:39]\n\t"
  "v_add_f32_dpp v122, v122, v122 row_mirror row_mask:0xf bank_mask:0xf\n\t"
  "v_add_f32_dpp v123, v123, v123 row_mirror row_mask:0xf bank_mask:0xf\n\t"
  "buffer_load_dwordx4 v4, %[srd], s46 offen offset:0 sc1 lds\n\t"
  "v_add_f32_dpp v122, v122, v122 row_bcast:15 row_mask:0xa bank_mask:0xf\n\t"
  "v_mfma_f32_16x16x32_bf16 v[40:43], a[44:47], v[72:75], v[40:43]\n\t"
  "s_waitcnt lgkmcnt(4)\n\t"
  "v_add_f32_dpp v123, v123, v123 row_bcast:15 row_mask:0xa bank_mask:0xf\n\t"
  "v_add_f32_dpp v122, v122, v122 row_bcast:31 row_mask:0xc bank_mask:0xf\n\t"
  "buffer_load_dwordx4 v4, %[srd], s46 offen offset:1024 sc1 lds\n\t"
  "v_mfma_f32_16x16x32_bf16 v[20:23], a[48:51], v[76:79], v[20:23]\n\t"
  "ds_read_b64_tr_b16 v[68:69], v108 offset:24576\n\t"
  "v_add_f32_dpp v123, v123, v123 row_bcast:31 row_mask:0xc bank_mask:0xf\n\t"
  "v_mul_f32 v122, 0xbacccccd, v122\n\t"
  "v_mul_f32 v123, 0x3acccccd, v123\n\t"
  "v_mfma_f32_16x16x32_bf16 v[24:27], a[52:55], v[76:79], v[24:27]\n\t"
  "ds_read_b64_tr_b16 v[70:71], v108 offset:26624\n\t"
  "v_fma_f32 v123, -v122, v122, v123\n\t"
  "v_max_f32 v123, 0, v123\n\t"
  "v_cvt_pk_bf16_f32 v123, 0, v123\n\t"
  "v_mfma_f32_16x16x32_bf16 v[28:31], a[56:59], v[76:79], v[28:31]\n\t"
  "ds_read_b64_tr_b16 v[72:73], v109 offset:24576\n\t"
  "v_cvt_pk_bf16_f32 v126, v122, v122\n\t"
  "v_add_f32 v123, 0x3727c5ac, v123\n\t"
  "v_cvt_pk_bf16_f32 v123, 0, v123\n\t"
  "v_mfma_f32_16x16x32_bf16 v[32:35], a[48:51], v[80:83], v[32:35]\n\t"
  "ds_read_b64_tr_b16 v[74:75], v109 offset:26624\n\t"
  "v_sqrt_f32 v123, v123\n\t"
  "buffer_load_dwordx4 v4, %[srd], s46 offen offset:2048 sc1 lds\n\t"
  "v_cvt_pk_bf16_f32 v127, v123, v123\n\t"
  "v_mfma_f32_16x16x32_bf16 v[36:39], a[52:55], v[80:83], v[36:39]\n\t"
  "buffer_store_dwordx2 v[126:127], v7, %[srd], %[sST] offen sc1\n\t"
  "buffer_load_dwordx4 v4, %[srd], s46 offen offset:3072 sc1 lds\n\t"
  "s_add_u32 m0, s52, 114688\n\t"
  "v_add_u32 v124, 122880, v6\n\t"
  "v_mfma_f32_16x16x32_bf16 v[40:43], a[56:59], v[80:83], v[40:43]\n\t"
  "s_waitcnt lgkmcnt(4)\n\t"
  "v_add_u32 v125, 81920, v6\n\t"
  "v_mfma_f32_16x16x32_bf16 v[20:23], a[60:63], v[84:87], v[20:23]\n\t"
  "ds_read_b64_tr_b16 v[76:77], v108 offset:28672\n\t"
  "v_mov_b32 v112, 0\n\t"
  "v_mfma_f32_16x16x32_bf16 v[24:27], a[64:67], v[84:87], v[24:27]\n\t"
  "ds_read_b64_tr_b16 v[78:79], v108 offset:30720\n\t"
  "s_mov_b32 s46, 1343488\n\t"
  "v_mfma_f32_16x16x32_bf16 v[28:31], a[68:71], v[84:87], v[28:31]\n\t"
  "ds_read_b64_tr_b16 v[80:81], v109 offset:28672\n\t"
  "v_mov_b32 v113, 0\n\t"
  "v_mfma_f32_16x16x32_bf16 v[32:35], a[60:63], v[88:91], v[32:35]\n\t"
  "ds_read_b64_tr_b16 v[82:83], v109 offset:30720\n\t"
  "buffer_load_dwordx4 v4, %[srd], s46 offen offset:0 sc1 lds\n\t"
  "v_mfma_f32_16x16x32_bf16 v[36:39], a[64:67], v[88:91], v[36:39]\n\t"
  "buffer_load_dwordx2 v[138:139], v2, %[srd], 0 offen sc1\n\t"
  "v_mov_b32 v114, 0\n\t"
  "v_mfma_f32_16x16x32_bf16 v[40:43], a[68:71], v[88:91], v[40:43]\n\t"
  "s_waitcnt lgkmcnt(4)\n\t"
  "v_mov_b32 v115, 0\n\t"
  "v_mfma_f32_16x16x32_bf16 v[20:23], a[72:75], v[68:71], v[20:23]\n\t"
  "ds_read_b64_tr_b16 v[84:85], v108 offset:32768\n\t"
  "buffer_load_dwordx4 v4, %[srd], s46 offen offset:1024 sc1 lds\n\t"
  "v_mfma_f32_16x16x32_bf16 v[24:27], a[76:79], v[68:71], v[24:27]\n\t"
  "ds_read_b64_tr_b16 v[86:87], v108 offset:34816\n\t"
  "v_mov_b32 v116, 0\n\t"
  "v_mfma_f32_16x16x32_bf16 v[28:31], a[80:83], v[68:71], v[28:31]\n\t"
  "ds_read_b64_tr_b16 v[88:89], v109 offset:32768\n\t"
  "buffer_load_dwordx4 v4, %[srd], s46 offen offset:2048 sc1 lds\n\t"
  "v_mfma_f32_16x16x32_bf16 v[32:35], a[72:75], v[72:75], v[32:35]\n\t"
  "ds_read_b64_tr_b16 v[90:91], v109 offset:34816\n\t"
  "v_mov_b32 v117, 0\n\t"
  "v_mfma_f32_16x16x32_bf16 v[36:39], a[76:79], v[72:75], v[36:39]\n\t"
  "buffer_load_dwordx4 v4, %[srd], s46 offen offset:3072 sc1 lds\n\t"
  "v_mov_b32 v118, 0\n\t"
  "v_mfma_f32_16x16x32_bf16 v[40:43], a[80:83], v[72:75], v[40:43]\n\t"
  "s_waitcnt lgkmcnt(4)\n\t"
  "v_mov_b32 v119, 0\n\t"
  "v_mfma_f32_16x16x32_bf16 v[20:23], a[84:87], v[76:79], v[20:23]\n\t"
  "ds_read_b64_tr_b16 v[68:69], v108 offset:36864\n\t"
  "v_mov_b32 v120, 0\n\t"
  "v_mfma_f32_16x16x32_bf16 v[24:27], a[88:91], v[76:79], v[24:27]\n\t"
  "ds_read_b64_tr_b16 v[70:71], v108 offset:38912\n\t"
  "s_add_u32 m0, s52, 131072\n\t"
  "v_mfma_f32_16x16x32_bf16 v[28:31], a[92:95], v[76:79], v[28:31]\n\t"
  "ds_read_b64_tr_b16 v[72:73], v109 offset:36864\n\t"
  "v_mov_b32 v121, 0\n\t"
  "v_mfma_f32_16x16x32_bf16 v[32:35], a[84:87], v[80:83], v[32:35]\n\t"
  "ds_read_b64_tr_b16 v[74:75], v109 offset:38912\n\t"
  "s_mov_b32 s46, 1359872\n\t"
  "v_mfma_f32_16x16x32_bf16 v[36:39], a[88:91], v[80:83], v[36:39]\n\t"
  "v_mfma_f32_16x16x32_bf16 v[40:43], a[92:95], v[80:83], v[40:43]\n\t"
  "s_waitcnt lgkmcnt(4)\n\t"
  "v_mfma_f32_16x16x32_bf16 v[20:23], a[96:99], v[84:87], v[20:23]\n\t"
  "ds_read_b64_tr_b16 v[76:77], v110 offset:0\n\t"
  "buffer_load_dwordx4 v4, %[srd], s46 offen offset:0 sc1 lds\n\t"
  "v_mfma_f32_16x16x32_bf16 v[24:27], a[100:103], v[84:87], v[24:27]\n\t"
  "ds_read_b64_tr_b16 v[78:79], v110 offset:2048\n\t"
  "v_mfma_f32_16x16x32_bf16 v[28:31], a[104:107], v[84:87], v[28:31]\n\t"
  "ds_read_b64_tr_b16 v[80:81], v111 offset:0\n\t"
  "buffer_load_dwordx4 v4, %[srd], s46 offen offset:1024 sc1 lds\n\t"
  "v_mfma_f32_16x16x32_bf16 v[32:35], a[96:99], v[88:91], v[32:35]\n\t"
  "ds_read_b64_tr_b16 v[82:83], v111 offset:2048\n\t"
  "v_mfma_f32_16x16x32_bf16 v[36:39], a[100:103], v[88:91], v[36:39]\n\t"
  "buffer_load_dwordx4 v4, %[srd], s46 offen offset:2048 sc1 lds\n\t"
  "v_mfma_f32_16x16x32_bf16 v[40:43], a[104:107], v[88:91], v[40:43]\n\t"
  "s_waitcnt lgkmcnt(4)\n\t"
  "v_mfma_f32_16x16x32_bf16 v[20:23], a[108:111], v[68:71], v[20:23]\n\t"
  "ds_read_b64_tr_b16 v[84:85], v110 offset:4096\n\t"
  "v_mfma_f32_16x16x32_bf16 v[24:27], a[112:115], v[68:71], v[24:27]\n\t"
  "ds_read_b64_tr_b16 v[86:87], v110 offset:6144\n\t"
  "buffer_load_dwordx4 v4, %[srd], s46 offen offset:3072 sc1 lds\n\t"
  "v_mfma_f32_16x16x32_bf16 v[28:31], a[116:119], v[68:71], v[28:31]\n\t"
  "ds_read_b64_tr_b16 v[88:89], v111 offset:4096\n\t"
  "v_mfma_f32_16x16x32_bf16 v[32:35], a[108:111], v[72:75], v[32:35]\n\t"
  "ds_read_b64_tr_b16 v[90:91], v111 offset:6144\n\t"
  "s_add_u32 m0, s52, 147456\n\t"
  "v_mfma_f32_16x16x32_bf16 v[36:39], a[112:115], v[72:75], v[36:39]\n\t"
  "v_mfma_f32_16x16x32_bf16 v[40:43], a[116:119], v[72:75], v[40:43]\n\t"
  "s_waitcnt lgkmcnt(4)\n\t"
  "v_mfma_f32_16x16x32_bf16 v[20:23], a[120:123], v[76:79], v[20:23]\n\t"
  "ds_read_b64_tr_b16 v[68:69], v110 offset:8192\n\t"
  "v_xor_b32 v108, 64, v5\n\t"
  "v_mfma_f32_16x16x32_bf16 v[24:27], a[124:127], v[76:79], v[24:27]\n\t"
  "ds_read_b64_tr_b16 v[70:71], v110 offset:10240\n\t"
  "v_add_u32 v108, 0, v108\n\t"
  "v_mfma_f32_16x16x32_bf16 v[28:31], a[128:131], v[76:79], v[28:31]\n\t"
  "ds_read_b64_tr_b16 v[72:73], v111 offset:8192\n\t"
  "v_xor_b32 v109, 96, v5\n\t"
  "v_mfma_f32_16x16x32_bf16 v[32:35], a[120:123], v[80:83], v[32:35]\n\t"
  "ds_read_b64_tr_b16 v[74:75], v111 offset:10240\n\t"
  "v_add_u32 v109, 0, v109\n\t"
  "v_mfma_f32_16x16x32_bf16 v[36:39], a[124:127], v[80:83], v[36:39]\n\t"
  "s_mov_b32 s46, 1376256\n\t"
  "buffer_load_dwordx4 v4, %[srd], s46 offen offset:0 sc1 lds\n\t"
  "v_mfma_f32_16x16x32_bf16 v[40:43], a[128:131], v[80:83], v[40:43]\n\t"
  "s_waitcnt lgkmcnt(4)\n\t"
  "buffer_load_dwordx4 v4, %[srd], s46 offen offset:1024 sc1 lds\n\t"
  "v_mfma_f32_16x16x32_bf16 v[20:23], a[132:135], v[84:87], v[20:23]\n\t"
  "ds_read_b64_tr_b16 v[76:77], v110 offset:12288\n\t"
  "s_waitcnt vmcnt(45)\n\t"
  "v_mfma_f32_16x16x32_bf16 v[24:27], a[136:139], v[84:87], v[24:27]\n\t"
  "ds_read_b64_tr_b16 v[78:79], v110 offset:14336\n\t"
  "buffer_load_dwordx4 v4, %[srd], s46 offen offset:2048 sc1 lds\n\t"
  "s_waitcnt vmcnt(45)\n\t"
  "v_mfma_f32_16x16x32_bf16 v[28:31], a[140:143], v[84:87], v[28:31]\n\t"
  "ds_read_b64_tr_b16 v[80:81], v111 offset:12288\n\t"
  "v_mfma_f32_16x16x32_bf16 v[32:35], a[132:135], v[88:91], v[32:35]\n\t"
  "ds_read_b64_tr_b16 v[82:83], v111 offset:14336\n\t"
  "buffer_load_dwordx4 v4, %[srd], s46 offen offset:3072 sc1 lds\n\t"
  "v_mfma_f32_16x16x32_bf16 v[36:39], a[136:139], v[88:91], v[36:39]\n\t"
  "v_mfma_f32_16x16x32_bf16 v[40:43], a[140:143], v[88:91], v[40:43]\n\t"
  "s_waitcnt lgkmcnt(4)\n\t"
  "s_waitcnt vmcnt(45)\n\t"
  "v_mfma_f32_16x16x32_bf16 v[20:23], a[144:147], v[68:71], v[20:23]\n\t"
  "ds_read_b64_tr_b16 v[84:85], v110 offset:16384\n\t"
  "s_waitcnt vmcnt(44)\n\t"
  "v_mfma_f32_16x16x32_bf16 v[24:27], a[148:151], v[68:71], v[24:27]\n\t"
  "ds_read_b64_tr_b16 v[86:87], v110 offset:18432\n\t"
  "s_waitcnt vmcnt(43)\n\t"
  "v_mfma_f32_16x16x32_bf16 v[28:31], a[152:155], v[68:71], v[28:31]\n\t"
  "ds_read_b64_tr_b16 v[88:89], v111 offset:16384\n\t"
  "v_mfma_f32_16x16x32_bf16 v[32:35], a[144:147], v[72:75], v[32:35]\n\t"
  "ds_read_b64_tr_b16 v[90:91], v111 offset:18432\n\t"
  "v_mfma_f32_16x16x32_bf16 v[36:39], a[148:151], v[72:75], v[36:39]\n\t"
  "v_mfma_f32_16x16x32_bf16 v[40:43], a[152:155], v[72:75], v[40:43]\n\t"
  "s_waitcnt lgkmcnt(4)\n\t"
  "s_waitcnt vmcnt(42)\n\t"
  "v_mfma_f32_16x16x32_bf16 v[20:23], a[156:159], v[76:79], v[20:23]\n\t"
  "ds_read_b64_tr_b16 v[68:69], v110 offset:20480\n\t"
  "s_waitcnt vmcnt(41)\n\t"
  "v_mfma_f32_16x16x32_bf16 v[24:27], a[160:163], v[76:79], v[24:27]\n\t"
  "ds_read_b64_tr_b16 v[70:71], v110 offset:22528\n\t"
  "s_waitcnt vmcnt(40)\n\t"
  "v_mfma_f32_16x16x32_bf16 v[28:31], a[164:167], v[76:79], v[28:31]\n\t"
  "ds_read_b64_tr_b16 v[72:73], v111 offset:20480\n\t"
  "v_mfma_f32_16x16x32_bf16 v[32:35], a[156:159], v[80:83], v[32:35]\n\t"
  "ds_read_b64_tr_b16 v[74:75], v111 offset:22528\n\t"
  "v_mfma_f32_16x16x32_bf16 v[36:39], a[160:163], v[80:83], v[36:39]\n\t"
  "v_mfma_f32_16x16x32_bf16 v[40:43], a[164:167], v[80:83], v[40:43]\n\t"
  "s_waitcnt lgkmcnt(4)\n\t"
  "s_waitcnt vmcnt(39)\n\t"
  "v_mfma_f32_16x16x32_bf16 v[20:23], a[168:171], v[84:87], v[20:23]\n\t"
  "ds_read_b64_tr_b16 v[76:77], v110 offset:24576\n\t"
  "s_waitcnt vmcnt(11)\n\t"
  "v_cmp_ne_u64 vcc, v[132:133], v[138:139]\n\t"
  "v_mfma_f32_16x16x32_bf16 v[24:27], a[172:175], v[84:87], v[24:27]\n\t"
  "ds_read_b64_tr_b16 v[78:79], v110 offset:26624\n\t"
  "v_mfma_f32_16x16x32_bf16 v[28:31], a[176:179], v[84:87], v[28:31]\n\t"
  "ds_read_b64_tr_b16 v[80:81], v111 offset:24576\n\t"
  "s_cbranch_vccnz Lw2_slow%=\n\t"
  "Lw2_back%=:\n\t"
  "v_mfma_f32_16x16x32_bf16 v[32:35], a[168:171], v[88:91], v[32:35]\n\t"
  "ds_read_b64_tr_b16 v[82:83], v111 offset:26624\n\t"
  "v_mfma_f32_16x16x32_bf16 v[36:39], a[172:175], v[88:91], v[36:39]\n\t"
  "v_mfma_f32_16x16x32_bf16 v[40:43], a[176:179], v[88:91], v[40:43]\n\t"
  "s_waitcnt lgkmcnt(4)\n\t"
  "v_mfma_f32_16x16x32_bf16 v[20:23], a[180:183], v[68:71], v[20:23]\n\t"
  "ds_read_b64_tr_b16 v[84:85], v110 offset:28672\n\t"
  "v_lshrrev_b32 v138, 1, v1\n\t"
  "v_mfma_f32_16x16x32_bf16 v[24:27], a[184:187], v[68:71], v[24:27]\n\t"
  "ds_read_b64_tr_b16 v[86:87], v110 offset:30720\n\t"
  "buffer_load_dwordx2 a[240:241], v138, %[srd], %[sUB] offen offset:0 sc1\n\t"
  "v_mfma_f32_16x16x32_bf16 v[28:31], a[188:191], v[68:71], v[28:31]\n\t"
  "ds_read_b64_tr_b16 v[88:89], v111 offset:28672\n\t"
  "buffer_load_dwordx2 a[242:243], v138, %[srd], %[sUB] offen offset:512 sc1\n\t"
  "v_mfma_f32_16x16x32_bf16 v[32:35], a[180:183], v[72:75], v[32:35]\n\t"
  "ds_read_b64_tr_b16 v[90:91], v111 offset:30720\n\t"
  "buffer_load_dwordx2 a[244:245], v138, %[srd], %[sUB] offen offset:1024 sc1\n\t"
  "v_mfma_f32_16x16x32_bf16 v[36:39], a[184:187], v[72:75], v[36:39]\n\t"
  "s_add_u32 s45, %[sW2], 0\n\t"
  "buffer_load_dwordx4 v[144:147], v1, %[srd], s45 offen offset:0 sc1\n\t"
  "v_mfma_f32_16x16x32_bf16 v[40:43], a[188:191], v[72:75], v[40:43]\n\t"
  "s_waitcnt lgkmcnt(4)\n\t"
  "v_mfma_f32_16x16x32_bf16 v[20:23], a[192:195], v[76:79], v[20:23]\n\t"
  "ds_read_b64_tr_b16 v[68:69], v110 offset:32768\n\t"
  "v_mfma_f32_16x16x32_bf16 v[24:27], a[196:199], v[76:79], v[24:27]\n\t"
  "ds_read_b64_tr_b16 v[70:71], v110 offset:34816\n\t"
  "v_mfma_f32_16x16x32_bf16 v[28:31], a[200:203], v[76:79], v[28:31]\n\t"
  "ds_read_b64_tr_b16 v[72:73], v111 offset:32768\n\t"
  "v_mfma_f32_16x16x32_bf16 v[32:35], a[192:195], v[80:83], v[32:35]\n\t"
  "ds_read_b64_tr_b16 v[74:75], v111 offset:34816\n\t"
  "buffer_load_dwordx4 v[148:151], v1, %[srd], s45 offen offset:1024 sc1\n\t"
  "v_mfma_f32_16x16x32_bf16 v[36:39], a[196:199], v[80:83], v[36:39]\n\t"
  "v_mfma_f32_16x16x32_bf16 v[40:43], a[200:203], v[80:83], v[40:43]\n\t"
  "s_waitcnt lgkmcnt(4)\n\t"
  "v_mfma_f32_16x16x32_bf16 v[20:23], a[204:207], v[84:87], v[20:23]\n\t"
  "ds_read_b64_tr_b16 v[76:77], v110 offset:36864\n\t"
  "v_mfma_f32_16x16x32_bf16 v[24:27], a[208:211], v[84:87], v[24:27]\n\t"
  "ds_read_b64_tr_b16 v[78:79], v110 offset:38912\n\t"
  "v_mfma_f32_16x16x32_bf16 v[28:31], a[212:215], v[84:87], v[28:31]\n\t"
  "ds_read_b64_tr_b16 v[80:81], v111 offset:36864\n\t"
  "buffer_load_dwordx4 v[152:155], v1, %[srd], s45 offen offset:2048 sc1\n\t"
  "v_mfma_f32_16x16x32_bf16 v[32:35], a[204:207], v[88:91], v[32:35]\n\t"
  "ds_read_b64_tr_b16 v[82:83], v111 offset:38912\n\t"
  "v_mfma_f32_16x16x32_bf16 v[36:39], a[208:211], v[88:91], v[36:39]\n\t"
  "v_mfma_f32_16x16x32_bf16 v[40:43], a[212:215], v[88:91], v[40:43]\n\t"
  "s_waitcnt lgkmcnt(4)\n\t"
  "v_mfma_f32_16x16x32_bf16 v[20:23], a[216:219], v[68:71], v[20:23]\n\t"
  "ds_read_b64_tr_b16 v[84:85], v108 offset:0\n\t"
  "v_mfma_f32_16x16x32_bf16 v[24:27], a[220:223], v[68:71], v[24:27]\n\t"
  "ds_read_b64_tr_b16 v[86:87], v108 offset:2048\n\t"
  "buffer_load_dwordx4 v[156:159], v1, %[srd], s45 offen offset:3072 sc1\n\t"
  "v_mfma_f32_16x16x32_bf16 v[28:31], a[224:227], v[68:71], v[28:31]\n\t"
  "ds_read_b64_tr_b16 v[88:89], v109 offset:0\n\t"
  "v_mfma_f32_16x16x32_bf16 v[32:35], a[216:219], v[72:75], v[32:35]\n\t"
  "ds_read_b64_tr_b16 v[90:91], v109 offset:2048\n\t"
  "v_mfma_f32_16x16x32_bf16 v[36:39], a[220:223], v[72:75], v[36:39]\n\t"
  "v_mfma_f32_16x16x32_bf16 v[40:43], a[224:227], v[72:75], v[40:43]\n\t"
  "s_waitcnt lgkmcnt(4)\n\t"
  "v_mfma_f32_16x16x32_bf16 v[20:23], a[228:231], v[76:79], v[20:23]\n\t"
  "ds_read_b64_tr_b16 v[68:69], v108 offset:4096\n\t"
  "s_add_u32 s45, %[sW2], 4096\n\t"
  "v_mfma_f32_16x16x32_bf16 v[24:27], a[232:235], v[76:79], v[24:27]\n\t"
  "ds_read_b64_tr_b16 v[70:71], v108 offset:6144\n\t"
  "v_mfma_f32_16x16x32_bf16 v[28:31], a[236:239], v[76:79], v[28:31]\n\t"
  "ds_read_b64_tr_b16 v[72:73], v109 offset:4096\n\t"
  "v_mfma_f32_16x16x32_bf16 v[32:35], a[228:231], v[80:83], v[32:35]\n\t"
  "ds_read_b64_tr_b16 v[74:75], v109 offset:6144\n\t"
  "v_mfma_f32_16x16x32_bf16 v[36:39], a[232:235], v[80:83], v[36:39]\n\t"
  "v_mfma_f32_16x16x32_bf16 v[40:43], a[236:239], v[80:83], v[40:43]\n\t"
  "s_waitcnt lgkmcnt(4)\n\t"
  "buffer_load_dwordx4 v[160:163], v1, %[srd], s45 offen offset:0 sc1\n\t"
  "v_mfma_f32_16x16x32_bf16 v[44:47], a[0:3], v[84:87], 0\n\t"
  "ds_read_b64_tr_b16 v[76:77], v108 offset:8192\n\t"
  "v_xor_b32 v110, 64, v5\n\t"
  "v_mfma_f32_16x16x32_bf16 v[48:51], a[4:7], v[84:87], 0\n\t"
  "ds_read_b64_tr_b16 v[78:79], v108 offset:10240\n\t"
  "v_add_u32 v110, 40960, v110\n\t"
  "v_mfma_f32_16x16x32_bf16 v[52:55], a[8:11], v[84:87], 0\n\t"
  "ds_read_b64_tr_b16 v[80:81], v109 offset:8192\n\t"
  "v_xor_b32 v111, 96, v5\n\t"
  "v_mfma_f32_16x16x32_bf16 v[56:59], a[0:3], v[88:91], 0\n\t"
  "ds_read_b64_tr_b16 v[82:83], v109 offset:10240\n\t"
  "v_add_u32 v111, 40960, v111\n\t"
  "v_mfma_f32_16x16x32_bf16 v[60:63], a[4:7], v[88:91], 0\n\t"
  "buffer_load_dwordx4 v[164:167], v1, %[srd], s45 offen offset:1024 sc1\n\t"
  "v_mfma_f32_16x16x32_bf16 v[64:67], a[8:11], v[88:91], 0\n\t"
  "s_waitcnt lgkmcnt(4)\n\t"
  "v_mfma_f32_16x16x32_bf16 v[44:47], a[12:15], v[68:71], v[44:47]\n\t"
  "ds_read_b64_tr_b16 v[84:85], v108 offset:12288\n\t"
  "buffer_load_dwordx2 v[92:93], v136, %[srd], %[sST] offen offset:0 sc1\n\t"
  "v_mfma_f32_16x16x32_bf16 v[48:51], a[16:19], v[68:71], v[48:51]\n\t"
  "ds_read_b64_tr_b16 v[86:87], v108 offset:14336\n\t"
  "buffer_load_dwordx2 v[94:95], v136, %[srd], %[sST] offen offset:1024 sc1\n\t"
  "v_mfma_f32_16x16x32_bf16 v[52:55], a[20:23], v[68:71], v[52:55]\n\t"
  "ds_read_b64_tr_b16 v[88:89], v109 offset:12288\n\t"
  "buffer_load_dwordx2 v[96:97], v136, %[srd], %[sST] offen offset:2048 sc1\n\t"
  "v_mfma_f32_16x16x32_bf16 v[56:59], a[12:15], v[72:75], v[56:59]\n\t"
  "ds_read_b64_tr_b16 v[90:91], v109 offset:14336\n\t"
  "buffer_load_dwordx2 v[98:99], v136, %[srd], %[sST] offen offset:3072 sc1\n\t"
  "v_mfma_f32_16x16x32_bf16 v[60:63], a[16:19], v[72:75], v[60:63]\n\t"
  "buffer_load_dwordx4 v[168:171], v1, %[srd], s45 offen offset:2048 sc1\n\t"
  "v_mfma_f32_16x16x32_bf16 v[64:67], a[20:23], v[72:75], v[64:67]\n\t"
  "s_waitcnt lgkmcnt(4)\n\t"
  "v_mfma_f32_16x16x32_bf16 v[44:47], a[24:27], v[76:79], v[44:47]\n\t"
  "ds_read_b64_tr_b16 v[68:69], v108 offset:16384\n\t"
  "v_mfma_f32_16x16x32_bf16 v[48:51], a[28:31], v[76:79], v[48:51]\n\t"
  "ds_read_b64_tr_b16 v[70:71], v108 offset:18432\n\t"
  "v_mfma_f32_16x16x32_bf16 v[52:55], a[32:35], v[76:79], v[52:55]\n\t"
  "ds_read_b64_tr_b16 v[72:73], v109 offset:16384\n\t"
  "s_waitcnt vmcnt(14)\n\t"
  "s_barrier\n\t"
  "v_mfma_f32_16x16x32_bf16 v[56:59], a[24:27], v[80:83], v[56:59]\n\t"
  "ds_read_b64_tr_b16 v[74:75], v109 offset:18432\n\t"
  "buffer_load_dwordx4 v[172:175], v1, %[srd], s45 offen offset:3072 sc1\n\t"
  "v_mfma_f32_16x16x32_bf16 v[60:63], a[28:31], v[80:83], v[60:63]\n\t"
  "ds_read_u16_d16_hi v112, v125 offset:0\n\t"
  "ds_read_b64_tr_b16 a[246:247], v10 offset:0\n\t"
  "v_mfma_f32_16x16x32_bf16 v[64:67], a[32:35], v[80:83], v[64:67]\n\t"
  "s_waitcnt lgkmcnt(6)\n\t"
  "ds_read_u16_d16_hi v113, v125 offset:8192\n\t"
  "v_mfma_f32_16x16x32_bf16 v[44:47], a[36:39], v[84:87], v[44:47]\n\t"
  "ds_read_b64_tr_b16 v[76:77], v108 offset:20480\n\t"
  "ds_read_u16_d16_hi v114, v125 offset:16384\n\t"
  "v_mfma_f32_16x16x32_bf16 v[48:51], a[40:43], v[84:87], v[48:51]\n\t"
  "ds_read_b64_tr_b16 v[78:79], v108 offset:22528\n\t"
  "s_add_u32 s45, %[sW2], 8192\n\t"
  "v_mfma_f32_16x16x32_bf16 v[52:55], a[44:47], v[84:87], v[52:55]\n\t"
  "ds_read_b64_tr_b16 v[80:81], v109 offset:20480\n\t"
  "ds_read_u16_d16_hi v115, v125 offset:24576\n\t"
  "v_mfma_f32_16x16x32_bf16 v[56:59], a[36:39], v[88:91], v[56:59]\n\t"
  "ds_read_b64_tr_b16 v[82:83], v109 offset:22528\n\t"
  "ds_read_u16_d16_hi v116, v125 offset:32768\n\t"
  "v_mfma_f32_16x16x32_bf16 v[60:63], a[40:43], v[88:91], v[60:63]\n\t"
  "ds_read_u16_d16_hi v117, v124 offset:0\n\t"
  "ds_read_b64_tr_b16 a[248:249], v11 offset:0\n\t"
  "v_mfma_f32_16x16x32_bf16 v[64:67], a[44:47], v[88:91], v[64:67]\n\t"
  "s_waitcnt lgkmcnt(12)\n\t"
  "ds_read_u16_d16_hi v118, v124 offset:8192\n\t"
  "v_mfma_f32_16x16x32_bf16 v[44:47], a[48:51], v[68:71], v[44:47]\n\t"
  "ds_read_b64_tr_b16 v[84:85], v108 offset:24576\n\t"
  "buffer_load_dwordx4 v[176:179], v1, %[srd], s45 offen offset:0 sc1\n\t"
  "v_mfma_f32_16x16x32_bf16 v[48:51], a[52:55], v[68:71], v[48:51]\n\t"
  "ds_read_b64_tr_b16 v[86:87], v108 offset:26624\n\t"
  "ds_read_u16_d16_hi v119, v124 offset:16384\n\t"
  "v_mfma_f32_16x16x32_bf16 v[52:55], a[56:59], v[68:71], v[52:55]\n\t"
  "ds_read_b64_tr_b16 v[88:89], v109 offset:24576\n\t"
  "ds_read_u16_d16_hi v120, v124 offset:24576\n\t"
  "v_mfma_f32_16x16x32_bf16 v[56:59], a[48:51], v[72:75], v[56:59]\n\t"
  "ds_read_b64_tr_b16 v[90:91], v109 offset:26624\n\t"
  "ds_read_u16_d16_hi v121, v124 offset:32768\n\t"
  "v_mfma_f32_16x16x32_bf16 v[60:63], a[52:55], v[72:75], v[60:63]\n\t"
  "buffer_load_dwordx4 v[180:183], v1, %[srd], s45 offen offset:1024 sc1\n\t"
  "ds_read_b64_tr_b16 a[250:251], v10 offset:4096\n\t"
  "v_mfma_f32_16x16x32_bf16 v[64:67], a[56:59], v[72:75], v[64:67]\n\t"
  "s_waitcnt lgkmcnt(12)\n\t"
  "ds_read_b64_tr_b16 a[252:253], v11 offset:4096\n\t"
  "v_mfma_f32_16x16x32_bf16 v[44:47], a[60:63], v[76:79], v[44:47]\n\t"
  "ds_read_b64_tr_b16 v[68:69], v108 offset:28672\n\t"
  "ds_read_b64_tr_b16 a[254:255], v12 offset:0\n\t"
  "v_mfma_f32_16x16x32_bf16 v[48:51], a[64:67], v[76:79], v[48:51]\n\t"
  "ds_read_b64_tr_b16 v[70:71], v108 offset:30720\n\t"
  "v_mfma_f32_16x16x32_bf16 v[52:55], a[68:71], v[76:79], v[52:55]\n\t"
  "ds_read_b64_tr_b16 v[72:73], v109 offset:28672\n\t"
  "v_mfma_f32_16x16x32_bf16 v[56:59], a[60:63], v[80:83], v[56:59]\n\t"
  "ds_read_b64_tr_b16 v[74:75], v109 offset:30720\n\t"
  "buffer_load_dwordx4 v[184:187], v1, %[srd], s45 offen offset:2048 sc1\n\t"
  "v_mfma_f32_16x16x32_bf16 v[60:63], a[64:67], v[80:83], v[60:63]\n\t"
  "v_mfma_f32_16x16x32_bf16 v[64:67], a[68:71], v[80:83], v[64:67]\n\t"
  "s_waitcnt lgkmcnt(8)\n\t"
  "v_mfma_f32_16x16x32_bf16 v[44:47], a[72:75], v[84:87], v[44:47]\n\t"
  "ds_read_b64_tr_b16 v[76:77], v108 offset:32768\n\t"
  "v_mfma_f32_16x16x32_bf16 v[48:51], a[76:79], v[84:87], v[48:51]\n\t"
  "ds_read_b64_tr_b16 v[78:79], v108 offset:34816\n\t"
  "v_mul_f32 v112, v112, v142\n\t"
  "v_mfma_f32_16x16x32_bf16 v[52:55], a[80:83], v[84:87], v[52:55]\n\t"
  "ds_read_b64_tr_b16 v[80:81], v109 offset:32768\n\t"
  "buffer_load_dwordx4 v[188:191], v1, %[srd], s45 offen offset:3072 sc1\n\t"
  "v_mfma_f32_16x16x32_bf16 v[56:59], a[72:75], v[88:91], v[56:59]\n\t"
  "ds_read_b64_tr_b16 v[82:83], v109 offset:34816\n\t"
  "v_mul_f32 v113, v113, v143\n\t"
  "v_mfma_f32_16x16x32_bf16 v[60:63], a[76:79], v[88:91], v[60:63]\n\t"
  "v_mul_f32 v114, v114, v248\n\t"
  "v_mul_f32 v115, v115, v249\n\t"
  "v_mfma_f32_16x16x32_bf16 v[64:67], a[80:83], v[88:91], v[64:67]\n\t"
  "s_waitcnt lgkmcnt(4)\n\t"
  "v_mul_f32 v116, v116, v250\n\t"
  "v_mfma_f32_16x16x32_bf16 v[44:47], a[84:87], v[68:71], v[44:47]\n\t"
  "ds_read_b64_tr_b16 v[84:85], v108 offset:36864\n\t"
  "v_mul_f32 v117, v117, v251\n\t"
  "v_mfma_f32_16x16x32_bf16 v[48:51], a[88:91], v[68:71], v[48:51]\n\t"
  "ds_read_b64_tr_b16 v[86:87], v108 offset:38912\n\t"
  "s_add_u32 s45, %[sW2], 12288\n\t"
  "v_mfma_f32_16x16x32_bf16 v[52:55], a[92:95], v[68:71], v[52:55]\n\t"
  "ds_read_b64_tr_b16 v[88:89], v109 offset:36864\n\t"
  "v_mul_f32 v118, v118, v252\n\t"
  "v_mfma_f32_16x16x32_bf16 v[56:59], a[84:87], v[72:75], v[56:59]\n\t"
  "ds_read_b64_tr_b16 v[90:91], v109 offset:38912\n\t"
  "v_mul_f32 v119, v119, v253\n\t"
  "v_mfma_f32_16x16x32_bf16 v[60:63], a[88:91], v[72:75], v[60:63]\n\t"
  "v_mul_f32 v120, v120, v254\n\t"
  "v_mul_f32 v121, v121, v255\n\t"
  "v_mfma_f32_16x16x32_bf16 v[64:67], a[92:95], v[72:75], v[64:67]\n\t"
  "s_waitcnt lgkmcnt(4)\n\t"
  "v_add_f32 v122, v112, v113\n\t"
  "v_mfma_f32_16x16x32_bf16 v[44:47], a[96:99], v[76:79], v[44:47]\n\t"
  "ds_read_b64_tr_b16 v[68:69], v110 offset:0\n\t"
  "buffer_load_dwordx4 v[192:195], v1, %[srd], s45 offen offset:0 sc1\n\t"
  "v_mfma_f32_16x16x32_bf16 v[48:51], a[100:103], v[76:79], v[48:51]\n\t"
  "ds_read_b64_tr_b16 v[70:71], v110 offset:2048\n\t"
  "v_mul_f32 v123, v112, v112\n\t"
  "v_mfma_f32_16x16x32_bf16 v[52:55], a[104:107], v[76:79], v[52:55]\n\t"
  "ds_read_b64_tr_b16 v[72:73], v111 offset:0\n\t"
  "v_fmac_f32 v123, v113, v113\n\t"
  "v_mfma_f32_16x16x32_bf16 v[56:59], a[96:99], v[80:83], v[56:59]\n\t"
  "ds_read_b64_tr_b16 v[74:75], v111 offset:2048\n\t"
  "v_add_f32 v122, v122, v114\n\t"
  "v_mfma_f32_16x16x32_bf16 v[60:63], a[100:103], v[80:83], v[60:63]\n\t"
  "v_fmac_f32 v123, v114, v114\n\t"
  "v_add_f32 v122, v122, v115\n\t"
  "v_mfma_f32_16x16x32_bf16 v[64:67], a[104:107], v[80:83], v[64:67]\n\t"
  "s_waitcnt lgkmcnt(4)\n\t"
  "buffer_load_dwordx4 v[196:199], v1, %[srd], s45 offen offset:1024 sc1\n\t"
  "v_mfma_f32_16x16x32_bf16 v[44:47], a[108:111], v[84:87], v[44:47]\n\t"
  "ds_read_b64_tr_b16 v[76:77], v110 offset:4096\n\t"
  "v_fmac_f32 v123, v115, v115\n\t"
  "v_mfma_f32_16x16x32_bf16 v[48:51], a[112:115], v[84:87], v[48:51]\n\t"
  "ds_read_b64_tr_b16 v[78:79], v110 offset:6144\n\t"
  "v_add_f32 v122, v122, v116\n\t"
  "v_mfma_f32_16x16x32_bf16 v[52:55], a[116:119], v[84:87], v[52:55]\n\t"
  "ds_read_b64_tr_b16 v[80:81], v111 offset:4096\n\t"
  "v_fmac_f32 v123, v116, v116\n\t"
  "v_mfma_f32_16x16x32_bf16 v[56:59], a[108:111], v[88:91], v[56:59]\n\t"
  "ds_read_b64_tr_b16 v[82:83], v111 offset:6144\n\t"
  "v_add_f32 v122, v122, v117\n\t"
  "v_mfma_f32_16x16x32_bf16 v[60:63], a[112:115], v[88:91], v[60:63]\n\t"
  "buffer_load_dwordx4 v[200:203], v1, %[srd], s45 offen offset:2048 sc1\n\t"
  "v_fmac_f32 v123, v117, v117\n\t"
  "v_mfma_f32_16x16x32_bf16 v[64:67], a[116:119], v[88:91], v[64:67]\n\t"
  "s_waitcnt lgkmcnt(4)\n\t"
  "s_waitcnt vmcnt(11)\n\t"
  "v_min_u32 v131, v93, v95\n\t"
  "v_mfma_f32_16x16x32_bf16 v[44:47], a[120:123], v[68:71], v[44:47]\n\t"
  "ds_read_b64_tr_b16 v[84:85], v110 offset:8192\n\t"
  "s_waitcnt vmcnt(9)\n\t"
  "v_min3_u32 v131, v131, v97, v99\n\t"
  "v_mfma_f32_16x16x32_bf16 v[48:51], a[124:127], v[68:71], v[48:51]\n\t"
  "ds_read_b64_tr_b16 v[86:87], v110 offset:10240\n\t"
  "v_cmp_eq_u32 vcc, 0, v131\n\t"
  "v_mfma_f32_16x16x32_bf16 v[52:55], a[128:131], v[68:71], v[52:55]\n\t"
  "ds_read_b64_tr_b16 v[88:89], v111 offset:8192\n\t"
  "v_add_u32 v108, 81920, v5\n\t"
  "v_mfma_f32_16x16x32_bf16 v[56:59], a[120:123], v[72:75], v[56:59]\n\t"
  "ds_read_b64_tr_b16 v[90:91], v111 offset:10240\n\t"
  "s_cbranch_vccnz Lst0_slow%=\n\t"
  "Lst0_back%=:\n\t"
  "v_mfma_f32_16x16x32_bf16 v[60:63], a[124:127], v[72:75], v[60:63]\n\t"
  "v_xor_b32 v109, 32, v5\n\t"
  "v_add_u32 v109, 81920, v109\n\t"
  "v_mfma_f32_16x16x32_bf16 v[64:67], a[128:131], v[72:75], v[64:67]\n\t"
  "s_waitcnt lgkmcnt(4)\n\t"
  "buffer_load_dwordx4 v[204:207], v1, %[srd], s45 offen offset:3072 sc1\n\t"
  "v_mfma_f32_16x16x32_bf16 v[44:47], a[132:135], v[76:79], v[44:47]\n\t"
  "ds_read_b64_tr_b16 v[68:69], v110 offset:12288\n\t"
  "v_add_f32 v122, v122, v118\n\t"
  "v_mfma_f32_16x16x32_bf16 v[48:51], a[136:139], v[76:79], v[48:51]\n\t"
  "ds_read_b64_tr_b16 v[70:71], v110 offset:14336\n\t"
  "v_fmac_f32 v123, v118, v118\n\t"
  "v_mfma_f32_16x16x32_bf16 v[52:55], a[140:143], v[76:79], v[52:55]\n\t"
  "ds_read_b64_tr_b16 v[72:73], v111 offset:12288\n\t"
  "s_add_u32 s45, %[sW2], 16384\n\t"
  "v_mfma_f32_16x16x32_bf16 v[56:59], a[132:135], v[80:83], v[56:59]\n\t"
  "ds_read_b64_tr_b16 v[74:75], v111 offset:14336\n\t"
  "v_add_f32 v122, v122, v119\n\t"
  "v_mfma_f32_16x16x32_bf16 v[60:63], a[136:139], v[80:83], v[60:63]\n\t"
  "v_fmac_f32 v123, v119, v119\n\t"
  "v_add_f32 v122, v122, v120\n\t"
  "v_mfma_f32_16x16x32_bf16 v[64:67], a[140:143], v[80:83], v[64:67]\n\t"
  "v_fmac_f32 v123, v120, v120\n\t"
  "v_add_f32 v122, v122, v121\n\t"
  "v_mfma_f32_16x16x16_bf16 v[20:23], a[240:241], v[92:93], v[20:23]\n\t"
  "v_fmac_f32 v123, v121, v121\n\t"
  "v_add_f32_dpp v122, v122, v122 quad_perm:[1,0,3,2] row_mask:0xf bank_mask:0xf\n\t"
  "v_mfma_f32_16x16x16_bf16 v[24:27], a[242:243], v[92:93], v[24:27]\n\t"
  "buffer_load_dwordx4 v[208:211], v1, %[srd], s45 offen offset:0 sc1\n\t"
  "v_add_f32_dpp v123, v123, v123 quad_perm:[1,0,3,2] row_mask:0xf bank_mask:0xf\n\t"
  "v_mfma_f32_16x16x16_bf16 v[28:31], a[244:245], v[92:93], v[28:31]\n\t"
  "v_add_f32_dpp v122, v122, v122 quad_perm:[2,3,0,1] row_mask:0xf bank_mask:0xf\n\t"
  "v_add_f32_dpp v123, v123, v123 quad_perm:[2,3,0,1] row_mask:0xf bank_mask:0xf\n\t"
  "v_mfma_f32_16x16x16_bf16 v[32:35], a[240:241], v[94:95], v[32:35]\n\t"
  "v_add_f32_dpp v122, v122, v122 row_half_mirror row_mask:0xf bank_mask:0xf\n\t"
  "v_add_f32_dpp v123, v123, v123 row_half_mirror row_mask:0xf bank_mask:0xf\n\t"
  "v_mfma_f32_16x16x16_bf16 v[36:39], a[242:243], v[94:95], v[36:39]\n\t"
  "v_add_f32_dpp v122, v122, v122 row_mirror row_mask:0xf bank_mask:0xf\n\t"
  "v_add_f32_dpp v123, v123, v123 row_mirror row_mask:0xf bank_mask:0xf\n\t"
  "v_mfma_f32_16x16x16_bf16 v[40:43], a[244:245], v[94:95], v[40:43]\n\t"
  "s_waitcnt lgkmcnt(4)\n\t"
  "v_add_f32_dpp v122, v122, v122 row_bcast:15 row_mask:0xa bank_mask:0xf\n\t"
  "v_mfma_f32_16x16x32_bf16 v[44:47], a[144:147], v[84:87], v[44:47]\n\t"
  "ds_read_b64_tr_b16 v[76:77], v110 offset:16384\n\t"
  "buffer_load_dwordx4 v[212:215], v1, %[srd], s45 offen offset:1024 sc1\n\t"
  "v_mfma_f32_16x16x32_bf16 v[48:51], a[148:151], v[84:87], v[48:51]\n\t"
  "ds_read_b64_tr_b16 v[78:79], v110 offset:18432\n\t"
  "v_add_f32_dpp v123, v123, v123 row_bcast:15 row_mask:0xa bank_mask:0xf\n\t"
  "v_mfma_f32_16x16x32_bf16 v[52:55], a[152:155], v[84:87], v[52:55]\n\t"
  "ds_read_b64_tr_b16 v[80:81], v111 offset:16384\n\t"
  "v_add_f32_dpp v122, v122, v122 row_bcast:31 row_mask:0xc bank_mask:0xf\n\t"
  "v_mfma_f32_16x16x32_bf16 v[56:59], a[144:147], v[88:91], v[56:59]\n\t"
  "ds_read_b64_tr_b16 v[82:83], v111 offset:18432\n\t"
  "v_add_f32_dpp v123, v123, v123 row_bcast:31 row_mask:0xc bank_mask:0xf\n\t"
  "v_mfma_f32_16x16x32_bf16 v[60:63], a[148:151], v[88:91], v[60:63]\n\t"
  "buffer_load_dwordx4 v[216:219], v1, %[srd], s45 offen offset:2048 sc1\n\t"
  "v_mul_f32 v122, 0xbacccccd, v122\n\t"
  "v_mfma_f32_16x16x32_bf16 v[64:67], a[152:155], v[88:91], v[64:67]\n\t"
  "s_waitcnt lgkmcnt(4)\n\t"
  "v_mul_f32 v123, 0x3acccccd, v123\n\t"
  "v_fma_f32 v123, -v122, v122, v123\n\t"
  "v_max_f32 v123, 0, v123\n\t"
  "v_cvt_pk_bf16_f32 v123, 0, v123\n\t"
  "v_cvt_pk_bf16_f32 v126, v122, v122\n\t"
  "v_add_f32 v123, 0x3727c5ac, v123\n\t"
  "v_cvt_pk_bf16_f32 v123, 0, v123\n\t"
  "v_sqrt_f32 v123, v123\n\t"
  "s_nop 0\n\t"
  "v_cvt_pk_bf16_f32 v127, v123, v123\n\t"
  "buffer_store_dwordx2 v[126:127], v7, %[srd], s54 offen sc1\n\t"
  "v_mfma_f32_16x16x32_bf16 v[44:47], a[156:159], v[68:71], v[44:47]\n\t"
  "ds_read_b64_tr_b16 v[84:85], v110 offset:20480\n\t"
  "v_and_b32 v128, 0xffff0000, v93\n\t"
  "v_mfma_f32_16x16x32_bf16 v[48:51], a[160:163], v[68:71], v[48:51]\n\t"
  "ds_read_b64_tr_b16 v[86:87], v110 offset:22528\n\t"
  "v_and_b32 v129, 0xffff0000, v95\n\t"
  "v_mfma_f32_16x16x32_bf16 v[52:55], a[164:167], v[68:71], v[52:55]\n\t"
  "ds_read_b64_tr_b16 v[88:89], v111 offset:20480\n\t"
  "v_rcp_f32 v128, v128\n\t"
  "v_mfma_f32_16x16x32_bf16 v[56:59], a[156:159], v[72:75], v[56:59]\n\t"
  "ds_read_b64_tr_b16 v[90:91], v111 offset:22528\n\t"
  "buffer_load_dwordx4 v[220:223], v1, %[srd], s45 offen offset:3072 sc1\n\t"
  "v_mfma_f32_16x16x32_bf16 v[60:63], a[160:163], v[72:75], v[60:63]\n\t"
  "v_rcp_f32 v129, v129\n\t"
  "v_mul_f32 v128, v128, v128\n\t"
  "v_mfma_f32_16x16x32_bf16 v[64:67], a[164:167], v[72:75], v[64:67]\n\t"
  "s_waitcnt lgkmcnt(4)\n\t"
  "v_mul_f32 v129, v129, v129\n\t"
  "v_mfma_f32_16x16x32_bf16 v[44:47], a[168:171], v[76:79], v[44:47]\n\t"
  "ds_read_b64_tr_b16 v[68:69], v110 offset:24576\n\t"
  "v_max_f32 v20, 0, v20\n\t"
  "v_mfma_f32_16x16x32_bf16 v[48:51], a[172:175], v[76:79], v[48:51]\n\t"
  "ds_read_b64_tr_b16 v[70:71], v110 offset:26624\n\t"
  "v_max_f32 v21, 0, v21\n\t"
  "v_mfma_f32_16x16x32_bf16 v[52:55], a[176:179], v[76:79], v[52:55]\n\t"
  "ds_read_b64_tr_b16 v[72:73], v111 offset:24576\n\t"
  "s_add_u32 s45, %[sW2], 20480\n\t"
  "v_mfma_f32_16x16x32_bf16 v[56:59], a[168:171], v[80:83], v[56:59]\n\t"
  "ds_read_b64_tr_b16 v[74:75], v111 offset:26624\n\t"
  "v_mul_f32 v22, v22, v128\n\t"
  "v_mfma_f32_16x16x32_bf16 v[60:63], a[172:175], v[80:83], v[60:63]\n\t"
  "v_mul_f32 v23, v23, v128\n\t"
  "v_mul_f32 v20, v20, v22\n\t"
  "v_mfma_f32_16x16x32_bf16 v[64:67], a[176:179], v[80:83], v[64:67]\n\t"
  "s_waitcnt lgkmcnt(4)\n\t"
  "v_mul_f32 v21, v21, v23\n\t"
  "v_mfma_f32_16x16x32_bf16 v[44:47], a[180:183], v[84:87], v[44:47]\n\t"
  "ds_read_b64_tr_b16 v[76:77], v110 offset:28672\n\t"
  "v_cvt_pk_bf16_f32 v116, v20, v21\n\t"
  "v_mfma_f32_16x16x32_bf16 v[48:51], a[184:187], v[84:87], v[48:51]\n\t"
  "ds_read_b64_tr_b16 v[78:79], v110 offset:30720\n\t"
  "buffer_load_dwordx4 v[224:227], v1, %[srd], s45 offen offset:0 sc1\n\t"
  "v_mfma_f32_16x16x32_bf16 v[52:55], a[188:191], v[84:87], v[52:55]\n\t"
  "ds_read_b64_tr_b16 v[80:81], v111 offset:28672\n\t"
  "v_cvt_pk_bf16_f32 v112, v20, v21\n\t"
  "v_mfma_f32_16x16x32_bf16 v[56:59], a[180:183], v[88:91], v[56:59]\n\t"
  "ds_read_b64_tr_b16 v[82:83], v111 offset:30720\n\t"
  "v_max_f32 v24, 0, v24\n\t"
  "v_mfma_f32_16x16x32_bf16 v[60:63], a[184:187], v[88:91], v[60:63]\n\t"
  "v_max_f32 v25, 0, v25\n\t"
  "v_mul_f32 v26, v26, v128\n\t"
  "v_mfma_f32_16x16x32_bf16 v[64:67], a[188:191], v[88:91], v[64:67]\n\t"
  "s_waitcnt lgkmcnt(4)\n\t"
  "v_mul_f32 v27, v27, v128\n\t"
  "v_mfma_f32_16x16x32_bf16 v[44:47], a[192:195], v[68:71], v[44:47]\n\t"
  "ds_read_b64_tr_b16 v[84:85], v110 offset:32768\n\t"
  "buffer_load_dwordx4 v[228:231], v1, %[srd], s45 offen offset:1024 sc1\n\t"
  "v_mfma_f32_16x16x32_bf16 v[48:51], a[196:199], v[68:71], v[48:51]\n\t"
  "ds_read_b64_tr_b16 v[86:87], v110 offset:34816\n\t"
  "v_mul_f32 v24, v24, v26\n\t"
  "v_mfma_f32_16x16x32_bf16 v[52:55], a[200:203], v[68:71], v[52:55]\n\t"
  "ds_read_b64_tr_b16 v[88:89], v111 offset:32768\n\t"
  "v_mul_f32 v25, v25, v27\n\t"
  "v_mfma_f32_16x16x32_bf16 v[56:59], a[192:195], v[72:75], v[56:59]\n\t"
  "ds_read_b64_tr_b16 v[90:91], v111 offset:34816\n\t"
  "v_cvt_pk_bf16_f32 v117, v24, v25\n\t"
  "v_mfma_f32_16x16x32_bf16 v[60:63], a[196:199], v[72:75], v[60:63]\n\t"
  "v_cvt_pk_bf16_f32 v113, v24, v25\n\t"
  "v_max_f32 v28, 0, v28\n\t"
  "v_mfma_f32_16x16x32_bf16 v[64:67], a[200:203], v[72:75], v[64:67]\n\t"
  "s_waitcnt lgkmcnt(4)\n\t"
  "buffer_load_dwordx4 v[232:235], v1, %[srd], s45 offen offset:2048 sc1\n\t"
  "v_mfma_f32_16x16x32_bf16 v[44:47], a[204:207], v[76:79], v[44:47]\n\t"
  "ds_read_b64_tr_b16 v[68:69], v110 offset:36864\n\t"
  "v_max_f32 v29, 0, v29\n\t"
  "v_mfma_f32_16x16x32_bf16 v[48:51], a[208:211], v[76:79], v[48:51]\n\t"
  "ds_read_b64_tr_b16 v[70:71], v110 offset:38912\n\t"
  "v_mul_f32 v30, v30, v128\n\t"
  "v_mfma_f32_16x16x32_bf16 v[52:55], a[212:215], v[76:79], v[52:55]\n\t"
  "ds_read_b64_tr_b16 v[72:73], v111 offset:36864\n\t"
  "v_mul_f32 v31, v31, v128\n\t"
  "v_mfma_f32_16x16x32_bf16 v[56:59], a[204:207], v[80:83], v[56:59]\n\t"
  "ds_read_b64_tr_b16 v[74:75], v111 offset:38912\n\t"
  "v_mul_f32 v28, v28, v30\n\t"
  "v_mfma_f32_16x16x32_bf16 v[60:63], a[208:211], v[80:83], v[60:63]\n\t"
  "buffer_load_dwordx4 v[236:239], v1, %[srd], s45 offen offset:3072 sc1\n\t"
  "v_permlane16_swap_b32 v116, v117\n\t"
  "v_mfma_f32_16x16x32_bf16 v[64:67], a[212:215], v[80:83], v[64:67]\n\t"
  "s_waitcnt lgkmcnt(4)\n\t"
  "v_mul_f32 v29, v29, v31\n\t"
  "v_mfma_f32_16x16x32_bf16 v[44:47], a[216:219], v[84:87], v[44:47]\n\t"
  "ds_read_b64_tr_b16 v[76:77], v108 offset:0\n\t"
  "v_cvt_pk_bf16_f32 v118, v28, v29\n\t"
  "v_mfma_f32_16x16x32_bf16 v[48:51], a[220:223], v[84:87], v[48:51]\n\t"
  "ds_read_b64_tr_b16 v[78:79], v108 offset:2048\n\t"
  "v_cvt_pk_bf16_f32 v114, v28, v29\n\t"
  "v_mfma_f32_16x16x32_bf16 v[52:55], a[224:227], v[84:87], v[52:55]\n\t"
  "ds_read_b64_tr_b16 v[80:81], v109 offset:0\n\t"
  "s_mov_b32 s55, 0\n\t"
  "v_mfma_f32_16x16x32_bf16 v[56:59], a[216:219], v[88:91], v[56:59]\n\t"
  "ds_read_b64_tr_b16 v[82:83], v109 offset:2048\n\t"
  "s_add_u32 s45, %[sW2], 24576\n\t"
  "v_mfma_f32_16x16x32_bf16 v[60:63], a[220:223], v[88:91], v[60:63]\n\t"
  "v_permlane16_swap_b32 v118, v115\n\t"
  "v_mfma_f32_16x16x32_bf16 v[64:67], a[224:227], v[88:91], v[64:67]\n\t"
  "s_waitcnt lgkmcnt(4)\n\t"
  "v_permlane32_swap_b32 v117, v115\n\t"
  "v_mfma_f32_16x16x32_bf16 v[44:47], a[228:231], v[68:71], v[44:47]\n\t"
  "ds_read_b64_tr_b16 v[84:85], v108 offset:4096\n\t"
  "buffer_store_dwordx4 v[112:115], v8, %[srd], s55 offen sc1\n\t"
  "v_mfma_f32_16x16x32_bf16 v[48:51], a[232:235], v[68:71], v[48:51]\n\t"
  "ds_read_b64_tr_b16 v[86:87], v108 offset:6144\n\t"
  "v_max_f32 v32, 0, v32\n\t"
  "v_mfma_f32_16x16x32_bf16 v[52:55], a[236:239], v[68:71], v[52:55]\n\t"
  "ds_read_b64_tr_b16 v[88:89], v109 offset:4096\n\t"
  "buffer_load_dwordx4 v[240:243], v1, %[srd], s45 offen offset:0 sc1\n\t"
  "v_mfma_f32_16x16x32_bf16 v[56:59], a[228:231], v[72:75], v[56:59]\n\t"
  "ds_read_b64_tr_b16 v[90:91], v109 offset:6144\n\t"
  "v_max_f32 v33, 0, v33\n\t"
  "v_mfma_f32_16x16x32_bf16 v[60:63], a[232:235], v[72:75], v[60:63]\n\t"
  "v_mul_f32 v34, v34, v129\n\t"
  "v_mul_f32 v35, v35, v129\n\t"
  "v_mfma_f32_16x16x32_bf16 v[64:67], a[236:239], v[72:75], v[64:67]\n\t"
  "v_mul_f32 v32, v32, v34\n\t"
  "v_mul_f32 v33, v33, v35\n\t"
  "v_mfma_f32_16x16x16_bf16 v[44:47], a[240:241], v[96:97], v[44:47]\n\t"
  "v_cvt_pk_bf16_f32 v124, v32, v33\n\t"
  "v_cvt_pk_bf16_f32 v120, v32, v33\n\t"
  "v_mfma_f32_16x16x16_bf16 v[48:51], a[242:243], v[96:97], v[48:51]\n\t"
  "buffer_load_dwordx4 v[244:247], v1, %[srd], s45 offen offset:1024 sc1\n\t"
  "v_max_f32 v36, 0, v36\n\t"
  "v_mfma_f32_16x16x16_bf16 v[52:55], a[244:245], v[96:97], v[52:55]\n\t"
  "v_max_f32 v37, 0, v37\n\t"
  "v_mul_f32 v38, v38, v129\n\t"
  "v_mfma_f32_16x16x16_bf16 v[56:59], a[240:241], v[98:99], v[56:59]\n\t"
  "v_mul_f32 v39, v39, v129\n\t"
  "v_mul_f32 v36, v36, v38\n\t"
  "v_mfma_f32_16x16x16_bf16 v[60:63], a[242:243], v[98:99], v[60:63]\n\t"
  "v_mul_f32 v37, v37, v39\n\t"
  "v_cvt_pk_bf16_f32 v125, v36, v37\n\t"
  "v_cvt_pk_bf16_f32 v121, v36, v37\n\t"
  "v_max_f32 v40, 0, v40\n\t"
  "v_max_f32 v41, 0, v41\n\t"
  "v_mul_f32 v42, v42, v129\n\t"
  "v_mul_f32 v43, v43, v129\n\t"
  "v_mul_f32 v40, v40, v42\n\t"
  "v_permlane16_swap_b32 v124, v125\n\t"
  "v_mul_f32 v41, v41, v43\n\t"
  "v_cvt_pk_bf16_f32 v126, v40, v41\n\t"
  "v_cvt_pk_bf16_f32 v122, v40, v41\n\t"
  "s_mov_b32 s55, 49152\n\t"
  "v_permlane16_swap_b32 v126, v123\n\t"
  "s_nop 1\n\t"
  "v_permlane32_swap_b32 v125, v123\n\t"
  "buffer_store_dwordx4 v[120:123], v8, %[srd], s55 offen sc1\n\t"
  "v_mfma_f32_16x16x16_bf16 v[64:67], a[244:245], v[98:99], v[64:67]\n\t"
  "s_waitcnt lgkmcnt(4)\n\t"
  "v_mfma_f32_16x16x32_bf16 v[20:23], a[0:3], v[76:79], 0\n\t"
  "ds_read_b64_tr_b16 v[68:69], v108 offset:8192\n\t"
  "v_add_u32 v110, 122880, v5\n\t"
  "v_mfma_f32_16x16x32_bf16 v[24:27], a[4:7], v[76:79], 0\n\t"
  "ds_read_b64_tr_b16 v[70:71], v108 offset:10240\n\t"
  "v_xor_b32 v111, 32, v5\n\t"
  "v_mfma_f32_16x16x32_bf16 v[28:31], a[8:11], v[76:79], 0\n\t"
  "ds_read_b64_tr_b16 v[72:73], v109 offset:8192\n\t"
  "v_add_u32 v111, 122880, v111\n\t"
  "v_mfma_f32_16x16x32_bf16 v[32:35], a[0:3], v[80:83], 0\n\t"
  "ds_read_b64_tr_b16 v[74:75], v109 offset:10240\n\t"
  "v_and_b32 v128, 0xffff0000, v97\n\t"
  "v_mfma_f32_16x16x32_bf16 v[36:39], a[4:7], v[80:83], 0\n\t"
  "v_and_b32 v129, 0xffff0000, v99\n\t"
  "v_rcp_f32 v128, v128\n\t"
  "v_mfma_f32_16x16x32_bf16 v[40:43], a[8:11], v[80:83], 0\n\t"
  "s_waitcnt lgkmcnt(4)\n\t"
  "v_rcp_f32 v129, v129\n\t"
  "v_mfma_f32_16x16x32_bf16 v[20:23], a[12:15], v[84:87], v[20:23]\n\t"
  "ds_read_b64_tr_b16 v[76:77], v108 offset:12288\n\t"
  "s_barrier\n\t"
  "v_mfma_f32_16x16x32_bf16 v[24:27], a[16:19], v[84:87], v[24:27]\n\t"
  "ds_read_b64_tr_b16 v[78:79], v108 offset:14336\n\t"
  "v_mul_f32 v128, v128, v128\n\t"
  "v_mfma_f32_16x16x32_bf16 v[28:31], a[20:23], v[84:87], v[28:31]\n\t"
  "ds_read_b64_tr_b16 v[80:81], v109 offset:12288\n\t"
  "v_mul_f32 v129, v129, v129\n\t"
  "v_mfma_f32_16x16x32_bf16 v[32:35], a[12:15], v[88:91], v[32:35]\n\t"
  "ds_read_b64_tr_b16 v[82:83], v109 offset:14336\n\t"
  "s_mul_i32 s52, %[w], 20480\n\t"
  "v_mfma_f32_16x16x32_bf16 v[36:39], a[16:19], v[88:91], v[36:39]\n\t"
  "v_max_f32 v44, 0, v44\n\t"
  "v_max_f32 v45, 0, v45\n\t"
  "v_mfma_f32_16x16x32_bf16 v[40:43], a[20:23], v[88:91], v[40:43]\n\t"
  "s_waitcnt lgkmcnt(4)\n\t"
  "v_mul_f32 v46, v46, v128\n\t"
  "v_mfma_f32_16x16x32_bf16 v[20:23], a[24:27], v[68:71], v[20:23]\n\t"
  "ds_read_b64_tr_b16 v[84:85], v108 offset:16384\n\t"
  "s_add_u32 m0, s52, 0\n\t"
  "v_mfma_f32_16x16x32_bf16 v[24:27], a[28:31], v[68:71], v[24:27]\n\t"
  "ds_read_b64_tr_b16 v[86:87], v108 offset:18432\n\t"
  "v_mul_f32 v47, v47, v128\n\t"
  "v_mfma_f32_16x16x32_bf16 v[28:31], a[32:35], v[68:71], v[28:31]\n\t"
  "ds_read_b64_tr_b16 v[88:89], v109 offset:16384\n\t"
  "v_mul_f32 v44, v44, v46\n\t"
  "v_mfma_f32_16x16x32_bf16 v[32:35], a[24:27], v[72:75], v[32:35]\n\t"
  "ds_read_b64_tr_b16 v[90:91], v109 offset:18432\n\t"
  "v_mul_f32 v45, v45, v47\n\t"
  "v_mfma_f32_16x16x32_bf16 v[36:39], a[28:31], v[72:75], v[36:39]\n\t"
  "s_add_u32 s46, %[sW2], 26624\n\t"
  "v_cvt_pk_bf16_f32 v116, v44, v45\n\t"
  "v_mfma_f32_16x16x32_bf16 v[40:43], a[32:35], v[72:75], v[40:43]\n\t"
  "s_waitcnt lgkmcnt(4)\n\t"
  "v_cvt_pk_bf16_f32 v112, v44, v45\n\t"
  "v_mfma_f32_16x16x32_bf16 v[20:23], a[36:39], v[76:79], v[20:23]\n\t"
  "ds_read_b64_tr_b16 v[68:69], v108 offset:20480\n\t"
  "v_max_f32 v48, 0, v48\n\t"
  "v_mfma_f32_16x16x32_bf16 v[24:27], a[40:43], v[76:79], v[24:27]\n\t"
  "ds_read_b64_tr_b16 v[70:71], v108 offset:22528\n\t"
  "buffer_load_dwordx4 v1, %[srd], s46 offen offset:0 sc1 lds\n\t"
  "v_mfma_f32_16x16x32_bf16 v[28:31], a[44:47], v[76:79], v[28:31]\n\t"
  "ds_read_b64_tr_b16 v[72:73], v109 offset:20480\n\t"
  "v_max_f32 v49, 0, v49\n\t"
  "v_mfma_f32_16x16x32_bf16 v[32:35], a[36:39], v[80:83], v[32:35]\n\t"
  "ds_read_b64_tr_b16 v[74:75], v109 offset:22528\n\t"
  "v_mul_f32 v50, v50, v128\n\t"
  "v_mfma_f32_16x16x32_bf16 v[36:39], a[40:43], v[80:83], v[36:39]\n\t"
  "buffer_load_dwordx4 v1, %[srd], s46 offen offset:1024 sc1 lds\n\t"
  "v_mul_f32 v51, v51, v128\n\t"
  "v_mfma_f32_16x16x32_bf16 v[40:43], a[44:47], v[80:83], v[40:43]\n\t"
  "s_waitcnt lgkmcnt(4)\n\t"
  "v_mul_f32 v48, v48, v50\n\t"
  "v_mfma_f32_16x16x32_bf16 v[20:23], a[48:51], v[84:87], v[20:23]\n\t"
  "ds_read_b64_tr_b16 v[76:77], v108 offset:24576\n\t"
  "v_mul_f32 v49, v49, v51\n\t"
  "v_mfma_f32_16x16x32_bf16 v[24:27], a[52:55], v[84:87], v[24:27]\n\t"
  "ds_read_b64_tr_b16 v[78:79], v108 offset:26624\n\t"
  "v_cvt_pk_bf16_f32 v117, v48, v49\n\t"
  "v_mfma_f32_16x16x32_bf16 v[28:31], a[56:59], v[84:87], v[28:31]\n\t"
  "ds_read_b64_tr_b16 v[80:81], v109 offset:24576\n\t"
  "buffer_load_dwordx4 v1, %[srd], s46 offen offset:2048 sc1 lds\n\t"
  "v_mfma_f32_16x16x32_bf16 v[32:35], a[48:51], v[88:91], v[32:35]\n\t"
  "ds_read_b64_tr_b16 v[82:83], v109 offset:26624\n\t"
  "v_cvt_pk_bf16_f32 v113, v48, v49\n\t"
  "v_mfma_f32_16x16x32_bf16 v[36:39], a[52:55], v[88:91], v[36:39]\n\t"
  "v_max_f32 v52, 0, v52\n\t"
  "v_max_f32 v53, 0, v53\n\t"
  "v_mfma_f32_16x16x32_bf16 v[40:43], a[56:59], v[88:91], v[40:43]\n\t"
  "s_waitcnt lgkmcnt(4)\n\t"
  "buffer_load_dwordx4 v1, %[srd], s46 offen offset:3072 sc1 lds\n\t"
  "v_mfma_f32_16x16x32_bf16 v[20:23], a[60:63], v[68:71], v[20:23]\n\t"
  "ds_read_b64_tr_b16 v[84:85], v108 offset:28672\n\t"
  "v_mul_f32 v54, v54, v128\n\t"
  "v_mfma_f32_16x16x32_bf16 v[24:27], a[64:67], v[68:71], v[24:27]\n\t"
  "ds_read_b64_tr_b16 v[86:87], v108 offset:30720\n\t"
  "v_mul_f32 v55, v55, v128\n\t"
  "v_mfma_f32_16x16x32_bf16 v[28:31], a[68:71], v[68:71], v[28:31]\n\t"
  "ds_read_b64_tr_b16 v[88:89], v109 offset:28672\n\t"
  "v_mul_f32 v52, v52, v54\n\t"
  "v_mfma_f32_16x16x32_bf16 v[32:35], a[60:63], v[72:75], v[32:35]\n\t"
  "ds_read_b64_tr_b16 v[90:91], v109 offset:30720\n\t"
  "s_add_u32 m0, s52, 4096\n\t"
  "v_mfma_f32_16x16x32_bf16 v[36:39], a[64:67], v[72:75], v[36:39]\n\t"
  "v_permlane16_swap_b32 v116, v117\n\t"
  "v_mul_f32 v53, v53, v55\n\t"
  "v_mfma_f32_16x16x32_bf16 v[40:43], a[68:71], v[72:75], v[40:43]\n\t"
  "s_waitcnt lgkmcnt(4)\n\t"
  "v_cvt_pk_bf16_f32 v118, v52, v53\n\t"
  "v_mfma_f32_16x16x32_bf16 v[20:23], a[72:75], v[76:79], v[20:23]\n\t"
  "ds_read_b64_tr_b16 v[68:69], v108 offset:32768\n\t"
  "s_add_u32 s46, %[sW2], 30720\n\t"
  "v_mfma_f32_16x16x32_bf16 v[24:27], a[76:79], v[76:79], v[24:27]\n\t"
  "ds_read_b64_tr_b16 v[70:71], v108 offset:34816\n\t"
  "v_cvt_pk_bf16_f32 v114, v52, v53\n\t"
  "v_mfma_f32_16x16x32_bf16 v[28:31], a[80:83], v[76:79], v[28:31]\n\t"
  "ds_read_b64_tr_b16 v[72:73], v109 offset:32768\n\t"
  "s_mov_b32 s55, 98304\n\t"
  "v_mfma_f32_16x16x32_bf16 v[32:35], a[72:75], v[80:83], v[32:35]\n\t"
  "ds_read_b64_tr_b16 v[74:75], v109 offset:34816\n\t"
  "buffer_load_dwordx4 v1, %[srd], s46 offen offset:0 sc1 lds\n\t"
  "v_mfma_f32_16x16x32_bf16 v[36:39], a[76:79], v[80:83], v[36:39]\n\t"
  "v_permlane16_swap_b32 v118, v115\n\t"
  "v_mfma_f32_16x16x32_bf16 v[40:43], a[80:83], v[80:83], v[40:43]\n\t"
  "s_waitcnt lgkmcnt(4)\n\t"
  "v_permlane32_swap_b32 v117, v115\n\t"
  "v_mfma_f32_16x16x32_bf16 v[20:23], a[84:87], v[84:87], v[20:23]\n\t"
  "ds_read_b64_tr_b16 v[76:77], v108 offset:36864\n\t"
  "buffer_store_dwordx4 v[112:115], v8, %[srd], s55 offen sc1\n\t"
  "v_mfma_f32_16x16x32_bf16 v[24:27], a[88:91], v[84:87], v[24:27]\n\t"
  "ds_read_b64_tr_b16 v[78:79], v108 offset:38912\n\t"
  "buffer_load_dwordx4 v1, %[srd], s46 offen offset:1024 sc1 lds\n\t"
  "v_mfma_f32_16x16x32_bf16 v[28:31], a[92:95], v[84:87], v[28:31]\n\t"
  "ds_read_b64_tr_b16 v[80:81], v109 offset:36864\n\t"
  "v_max_f32 v56, 0, v56\n\t"
  "v_mfma_f32_16x16x32_bf16 v[32:35], a[84:87], v[88:91], v[32:35]\n\t"
  "ds_read_b64_tr_b16 v[82:83], v109 offset:38912\n\t"
  "v_max_f32 v57, 0, v57\n\t"
  "v_mfma_f32_16x16x32_bf16 v[36:39], a[88:91], v[88:91], v[36:39]\n\t"
  "buffer_load_dwordx4 v1, %[srd], s46 offen offset:2048 sc1 lds\n\t"
  "v_mul_f32 v58, v58, v129\n\t"
  "v_mfma_f32_16x16x32_bf16 v[40:43], a[92:95], v[88:91], v[40:43]\n\t"
  "s_waitcnt lgkmcnt(4)\n\t"
  "v_mul_f32 v59, v59, v129\n\t"
  "v_mfma_f32_16x16x32_bf16 v[20:23], a[96:99], v[68:71], v[20:23]\n\t"
  "ds_read_b64_tr_b16 v[84:85], v110 offset:0\n\t"
  "v_mul_f32 v56, v56, v58\n\t"
  "v_mfma_f32_16x16x32_bf16 v[24:27], a[100:103], v[68:71], v[24:27]\n\t"
  "ds_read_b64_tr_b16 v[86:87], v110 offset:2048\n\t"
  "buffer_load_dwordx4 v1, %[srd], s46 offen offset:3072 sc1 lds\n\t"
  "v_mfma_f32_16x16x32_bf16 v[28:31], a[104:107], v[68:71], v[28:31]\n\t"
  "ds_read_b64_tr_b16 v[88:89], v111 offset:0\n\t"
  "v_mul_f32 v57, v57, v59\n\t"
  "v_mfma_f32_16x16x32_bf16 v[32:35], a[96:99], v[72:75], v[32:35]\n\t"
  "ds_read_b64_tr_b16 v[90:91], v111 offset:2048\n\t"
  "v_cvt_pk_bf16_f32 v124, v56, v57\n\t"
  "v_mfma_f32_16x16x32_bf16 v[36:39], a[100:103], v[72:75], v[36:39]\n\t"
  "buffer_load_dwordx2 v[100:101], v136, %[srd], s54 offen offset:0 sc1\n\t"
  "buffer_load_dwordx2 v[102:103], v136, %[srd], s54 offen offset:1024 sc1\n\t"
  "v_mfma_f32_16x16x32_bf16 v[40:43], a[104:107], v[72:75], v[40:43]\n\t"
  "s_waitcnt lgkmcnt(4)\n\t"
  "buffer_load_dwordx2 v[104:105], v136, %[srd], s54 offen offset:2048 sc1\n\t"
  "v_mfma_f32_16x16x32_bf16 v[20:23], a[108:111], v[76:79], v[20:23]\n\t"
  "ds_read_b64_tr_b16 v[68:69], v110 offset:4096\n\t"
  "s_movk_i32 s56, 0\n\t"
  "v_mfma_f32_16x16x32_bf16 v[24:27], a[112:115], v[76:79], v[24:27]\n\t"
  "ds_read_b64_tr_b16 v[70:71], v110 offset:6144\n\t"
  "s_waitcnt vmcnt(12)\n\t"
  "v_mfma_f32_16x16x32_bf16 v[28:31], a[116:119], v[76:79], v[28:31]\n\t"
  "ds_read_b64_tr_b16 v[72:73], v111 offset:4096\n\t"
  "buffer_load_dwordx2 v[106:107], v136, %[srd], s54 offen offset:3072 sc1\n\t"
  "v_mfma_f32_16x16x32_bf16 v[32:35], a[108:111], v[80:83], v[32:35]\n\t"
  "ds_read_b64_tr_b16 v[74:75], v111 offset:6144\n\t"
  "buffer_store_dwordx4 v[132:135], v9, %[srd], s56 offen sc1\n\t"
  "v_mfma_f32_16x16x32_bf16 v[36:39], a[112:115], v[80:83], v[36:39]\n\t"
  "s_add_u32 m0, s52, 8192\n\t"
  "s_add_u32 s46, %[sW2], 34816\n\t"
  "v_mfma_f32_16x16x32_bf16 v[40:43], a[116:119], v[80:83], v[40:43]\n\t"
  "s_waitcnt lgkmcnt(4)\n\t"
  "v_cvt_pk_bf16_f32 v120, v56, v57\n\t"
  "v_mfma_f32_16x16x32_bf16 v[20:23], a[120:123], v[84:87], v[20:23]\n\t"
  "ds_read_b64_tr_b16 v[76:77], v110 offset:8192\n\t"
  "v_xor_b32 v108, 64, v5\n\t"
  "v_mfma_f32_16x16x32_bf16 v[24:27], a[124:127], v[84:87], v[24:27]\n\t"
  "ds_read_b64_tr_b16 v[78:79], v110 offset:10240\n\t"
  "v_add_u32 v108, 81920, v108\n\t"
  "v_mfma_f32_16x16x32_bf16 v[28:31], a[128:131], v[84:87], v[28:31]\n\t"
  "ds_read_b64_tr_b16 v[80:81], v111 offset:8192\n\t"
  "v_xor_b32 v109, 96, v5\n\t"
  "v_mfma_f32_16x16x32_bf16 v[32:35], a[120:123], v[88:91], v[32:35]\n\t"
  "ds_read_b64_tr_b16 v[82:83], v111 offset:10240\n\t"
  "v_add_u32 v109, 81920, v109\n\t"
  "v_mfma_f32_16x16x32_bf16 v[36:39], a[124:127], v[88:91], v[36:39]\n\t"
  "buffer_load_dwordx4 v1, %[srd], s46 offen offset:0 sc1 lds\n\t"
  "buffer_load_dwordx4 v1, %[srd], s46 offen offset:1024 sc1 lds\n\t"
  "v_mfma_f32_16x16x32_bf16 v[40:43], a[128:131], v[88:91], v[40:43]\n\t"
  "s_waitcnt lgkmcnt(4)\n\t"
  "v_max_f32 v60, 0, v60\n\t"
  "v_mfma_f32_16x16x32_bf16 v[20:23], a[132:135], v[68:71], v[20:23]\n\t"
  "ds_read_b64_tr_b16 v[84:85], v110 offset:12288\n\t"
  "buffer_load_dwordx4 v1, %[srd], s46 offen offset:2048 sc1 lds\n\t"
  "v_mfma_f32_16x16x32_bf16 v[24:27], a[136:139], v[68:71], v[24:27]\n\t"
  "ds_read_b64_tr_b16 v[86:87], v110 offset:14336\n\t"
  "v_max_f32 v61, 0, v61\n\t"
  "v_mfma_f32_16x16x32_bf16 v[28:31], a[140:143], v[68:71], v[28:31]\n\t"
  "ds_read_b64_tr_b16 v[88:89], v111 offset:12288\n\t"
  "v_mul_f32 v62, v62, v129\n\t"
  "v_mfma_f32_16x16x32_bf16 v[32:35], a[132:135], v[72:75], v[32:35]\n\t"
  "ds_read_b64_tr_b16 v[90:91], v111 offset:14336\n\t"
  "v_mul_f32 v63, v63, v129\n\t"
  "v_mfma_f32_16x16x32_bf16 v[36:39], a[136:139], v[72:75], v[36:39]\n\t"
  "buffer_load_dwordx4 v1, %[srd], s46 offen offset:3072 sc1 lds\n\t"
  "v_mul_f32 v60, v60, v62\n\t"
  "v_mfma_f32_16x16x32_bf16 v[40:43], a[140:143], v[72:75], v[40:43]\n\t"
  "s_waitcnt lgkmcnt(4)\n\t"
  "v_mul_f32 v61, v61, v63\n\t"
  "v_mfma_f32_16x16x32_bf16 v[20:23], a[144:147], v[76:79], v[20:23]\n\t"
  "ds_read_b64_tr_b16 v[68:69], v110 offset:16384\n\t"
  "v_cvt_pk_bf16_f32 v125, v60, v61\n\t"
  "v_mfma_f32_16x16x32_bf16 v[24:27], a[148:151], v[76:79], v[24:27]\n\t"
  "ds_read_b64_tr_b16 v[70:71], v110 offset:18432\n\t"
  "s_add_u32 m0, s52, 12288\n\t"
  "v_mfma_f32_16x16x32_bf16 v[28:31], a[152:155], v[76:79], v[28:31]\n\t"
  "ds_read_b64_tr_b16 v[72:73], v111 offset:16384\n\t"
  "v_cvt_pk_bf16_f32 v121, v60, v61\n\t"
  "v_mfma_f32_16x16x32_bf16 v[32:35], a[144:147], v[80:83], v[32:35]\n\t"
  "ds_read_b64_tr_b16 v[74:75], v111 offset:18432\n\t"
  "v_max_f32 v64, 0, v64\n\t"
  "v_mfma_f32_16x16x32_bf16 v[36:39], a[148:151], v[80:83], v[36:39]\n\t"
  "s_add_u32 s46, %[sW2], 38912\n\t"
  "v_max_f32 v65, 0, v65\n\t"
  "v_mfma_f32_16x16x32_bf16 v[40:43], a[152:155], v[80:83], v[40:43]\n\t"
  "s_waitcnt lgkmcnt(4)\n\t"
  "v_mul_f32 v66, v66, v129\n\t"
  "v_mfma_f32_16x16x32_bf16 v[20:23], a[156:159], v[84:87], v[20:23]\n\t"
  "ds_read_b64_tr_b16 v[76:77], v110 offset:20480\n\t"
  "v_mul_f32 v67, v67, v129\n\t"
  "v_mfma_f32_16x16x32_bf16 v[24:27], a[160:163], v[84:87], v[24:27]\n\t"
  "ds_read_b64_tr_b16 v[78:79], v110 offset:22528\n\t"
  "v_mul_f32 v64, v64, v66\n\t"
  "v_mfma_f32_16x16x32_bf16 v[28:31], a[164:167], v[84:87], v[28:31]\n\t"
  "ds_read_b64_tr_b16 v[80:81], v111 offset:20480\n\t"
  "buffer_load_dwordx4 v1, %[srd], s46 offen offset:0 sc1 lds\n\t"
  "v_mfma_f32_16x16x32_bf16 v[32:35], a[156:159], v[88:91], v[32:35]\n\t"
  "ds_read_b64_tr_b16 v[82:83], v111 offset:22528\n\t"
  "v_permlane16_swap_b32 v124, v125\n\t"
  "v_mfma_f32_16x16x32_bf16 v[36:39], a[160:163], v[88:91], v[36:39]\n\t"
  "v_mul_f32 v65, v65, v67\n\t"
  "v_cvt_pk_bf16_f32 v126, v64, v65\n\t"
  "v_mfma_f32_16x16x32_bf16 v[40:43], a[164:167], v[88:91], v[40:43]\n\t"
  "s_waitcnt lgkmcnt(4)\n\t"
  "buffer_load_dwordx4 v1, %[srd], s46 offen offset:1024 sc1 lds\n\t"
  "v_mfma_f32_16x16x32_bf16 v[20:23], a[168:171], v[68:71], v[20:23]\n\t"
  "ds_read_b64_tr_b16 v[84:85], v110 offset:24576\n\t"
  "v_cvt_pk_bf16_f32 v122, v64, v65\n\t"
  "v_mfma_f32_16x16x32_bf16 v[24:27], a[172:175], v[68:71], v[24:27]\n\t"
  "ds_read_b64_tr_b16 v[86:87], v110 offset:26624\n\t"
  "s_mov_b32 s55, 147456\n\t"
  "v_mfma_f32_16x16x32_bf16 v[28:31], a[176:179], v[68:71], v[28:31]\n\t"
  "ds_read_b64_tr_b16 v[88:89], v111 offset:24576\n\t"
  "v_permlane16_swap_b32 v126, v123\n\t"
  "v_mfma_f32_16x16x32_bf16 v[32:35], a[168:171], v[72:75], v[32:35]\n\t"
  "ds_read_b64_tr_b16 v[90:91], v111 offset:26624\n\t"
  "buffer_load_dwordx4 v1, %[srd], s46 offen offset:2048 sc1 lds\n\t"
  "v_mfma_f32_16x16x32_bf16 v[36:39], a[172:175], v[72:75], v[36:39]\n\t"
  "v_permlane32_swap_b32 v125, v123\n\t"
  "buffer_store_dwordx4 v[120:123], v8, %[srd], s55 offen sc1\n\t"
  "v_mfma_f32_16x16x32_bf16 v[40:43], a[176:179], v[72:75], v[40:43]\n\t"
  "s_waitcnt lgkmcnt(4)\n\t"
  "v_mfma_f32_16x16x32_bf16 v[20:23], a[180:183], v[76:79], v[20:23]\n\t"
  "ds_read_b64_tr_b16 v[68:69], v110 offset:28672\n\t"
  "buffer_load_dwordx4 v1, %[srd], s46 offen offset:3072 sc1 lds\n\t"
  "v_mfma_f32_16x16x32_bf16 v[24:27], a[184:187], v[76:79], v[24:27]\n\t"
  "ds_read_b64_tr_b16 v[70:71], v110 offset:30720\n\t"
  "v_mfma_f32_16x16x32_bf16 v[28:31], a[188:191], v[76:79], v[28:31]\n\t"
  "ds_read_b64_tr_b16 v[72:73], v111 offset:28672\n\t"
  "v_mfma_f32_16x16x32_bf16 v[32:35], a[180:183], v[80:83], v[32:35]\n\t"
  "ds_read_b64_tr_b16 v[74:75], v111 offset:30720\n\t"
  "s_add_u32 m0, s52, 16384\n\t"
  "v_mfma_f32_16x16x32_bf16 v[36:39], a[184:187], v[80:83], v[36:39]\n\t"
  "v_mfma_f32_16x16x32_bf16 v[40:43], a[188:191], v[80:83], v[40:43]\n\t"
  "s_waitcnt lgkmcnt(4)\n\t"
  "v_mfma_f32_16x16x32_bf16 v[20:23], a[192:195], v[84:87], v[20:23]\n\t"
  "ds_read_b64_tr_b16 v[76:77], v110 offset:32768\n\t"
  "v_mfma_f32_16x16x32_bf16 v[24:27], a[196:199], v[84:87], v[24:27]\n\t"
  "ds_read_b64_tr_b16 v[78:79], v110 offset:34816\n\t"
  "s_add_u32 s46, %[sW2], 43008\n\t"
  "v_mfma_f32_16x16x32_bf16 v[28:31], a[200:203], v[84:87], v[28:31]\n\t"
  "ds_read_b64_tr_b16 v[80:81], v111 offset:32768\n\t"
  "v_mfma_f32_16x16x32_bf16 v[32:35], a[192:195], v[88:91], v[32:35]\n\t"
  "ds_read_b64_tr_b16 v[82:83], v111 offset:34816\n\t"
  "v_mfma_f32_16x16x32_bf16 v[36:39], a[196:199], v[88:91], v[36:39]\n\t"
  "buffer_load_dwordx4 v1, %[srd], s46 offen offset:0 sc1 lds\n\t"
  "v_mfma_f32_16x16x32_bf16 v[40:43], a[200:203], v[88:91], v[40:43]\n\t"
  "s_waitcnt lgkmcnt(4)\n\t"
  "v_mfma_f32_16x16x32_bf16 v[20:23], a[204:207], v[68:71], v[20:23]\n\t"
  "ds_read_b64_tr_b16 v[84:85], v110 offset:36864\n\t"
  "v_mfma_f32_16x16x32_bf16 v[24:27], a[208:211], v[68:71], v[24:27]\n\t"
  "ds_read_b64_tr_b16 v[86:87], v110 offset:38912\n\t"
  "buffer_load_dwordx4 v1, %[srd], s46 offen offset:1024 sc1 lds\n\t"
  "v_mfma_f32_16x16x32_bf16 v[28:31], a[212:215], v[68:71], v[28:31]\n\t"
  "ds_read_b64_tr_b16 v[88:89], v111 offset:36864\n\t"
  "v_mfma_f32_16x16x32_bf16 v[32:35], a[204:207], v[72:75], v[32:35]\n\t"
  "ds_read_b64_tr_b16 v[90:91], v111 offset:38912\n\t"
  "v_mfma_f32_16x16x32_bf16 v[36:39], a[208:211], v[72:75], v[36:39]\n\t"
  "s_waitcnt vmcnt(14)\n\t"
  "v_min_u32 v131, v101, v103\n\t"
  "v_mfma_f32_16x16x32_bf16 v[40:43], a[212:215], v[72:75], v[40:43]\n\t"
  "s_waitcnt lgkmcnt(4)\n\t"
  "s_waitcnt vmcnt(12)\n\t"
  "v_min3_u32 v131, v131, v105, v107\n\t"
  "v_mfma_f32_16x16x32_bf16 v[20:23], a[216:219], v[76:79], v[20:23]\n\t"
  "ds_read_b64_tr_b16 v[68:69], v108 offset:0\n\t"
  "v_cmp_eq_u32 vcc, 0, v131\n\t"
  "v_mfma_f32_16x16x32_bf16 v[24:27], a[220:223], v[76:79], v[24:27]\n\t"
  "ds_read_b64_tr_b16 v[70:71], v108 offset:2048\n\t"
  "buffer_load_dwordx4 v1, %[srd], s46 offen offset:2048 sc1 lds\n\t"
  "v_mfma_f32_16x16x32_bf16 v[28:31], a[224:227], v[76:79], v[28:31]\n\t"
  "ds_read_b64_tr_b16 v[72:73], v109 offset:0\n\t"
  "s_cbranch_vccnz Lst1_slow%=\n\t"
  "Lst1_back%=:\n\t"
  "v_mfma_f32_16x16x32_bf16 v[32:35], a[216:219], v[80:83], v[32:35]\n\t"
  "ds_read_b64_tr_b16 v[74:75], v109 offset:2048\n\t"
  "buffer_load_dwordx4 v1, %[srd], s46 offen offset:3072 sc1 lds\n\t"
  "v_mfma_f32_16x16x32_bf16 v[36:39], a[220:223], v[80:83], v[36:39]\n\t"
  "v_mfma_f32_16x16x32_bf16 v[40:43], a[224:227], v[80:83], v[40:43]\n\t"
  "s_waitcnt lgkmcnt(4)\n\t"
  "v_mfma_f32_16x16x32_bf16 v[20:23], a[228:231], v[84:87], v[20:23]\n\t"
  "ds_read_b64_tr_b16 v[76:77], v108 offset:4096\n\t"
  "v_mfma_f32_16x16x32_bf16 v[24:27], a[232:235], v[84:87], v[24:27]\n\t"
  "ds_read_b64_tr_b16 v[78:79], v108 offset:6144\n\t"
  "v_mfma_f32_16x16x32_bf16 v[28:31], a[236:239], v[84:87], v[28:31]\n\t"
  "ds_read_b64_tr_b16 v[80:81], v109 offset:4096\n\t"
  "v_mfma_f32_16x16x32_bf16 v[32:35], a[228:231], v[88:91], v[32:35]\n\t"
  "ds_read_b64_tr_b16 v[82:83], v109 offset:6144\n\t"
  "v_mfma_f32_16x16x32_bf16 v[36:39], a[232:235], v[88:91], v[36:39]\n\t"
  "v_mfma_f32_16x16x32_bf16 v[40:43], a[236:239], v[88:91], v[40:43]\n\t"
  "v_mfma_f32_16x16x16_bf16 v[20:23], a[240:241], v[100:101], v[20:23]\n\t"
  "v_mfma_f32_16x16x16_bf16 v[24:27], a[242:243], v[100:101], v[24:27]\n\t"
  "v_mfma_f32_16x16x16_bf16 v[28:31], a[244:245], v[100:101], v[28:31]\n\t"
  "v_mfma_f32_16x16x16_bf16 v[32:35], a[240:241], v[102:103], v[32:35]\n\t"
  "v_mfma_f32_16x16x16_bf16 v[36:39], a[242:243], v[102:103], v[36:39]\n\t"
  "v_mfma_f32_16x16x16_bf16 v[40:43], a[244:245], v[102:103], v[40:43]\n\t"
  "s_waitcnt lgkmcnt(4)\n\t"
  "v_mfma_f32_16x16x32_bf16 v[44:47], a[0:3], v[68:71], 0\n\t"
  "ds_read_b64_tr_b16 v[84:85], v108 offset:8192\n\t"
  "v_xor_b32 v110, 64, v5\n\t"
  "v_mfma_f32_16x16x32_bf16 v[48:51], a[4:7], v[68:71], 0\n\t"
  "ds_read_b64_tr_b16 v[86:87], v108 offset:10240\n\t"
  "v_add_u32 v110, 122880, v110\n\t"
  "v_mfma_f32_16x16x32_bf16 v[52:55], a[8:11], v[68:71], 0\n\t"
  "ds_read_b64_tr_b16 v[88:89], v109 offset:8192\n\t"
  "v_xor_b32 v111, 96, v5\n\t"
  "v_mfma_f32_16x16x32_bf16 v[56:59], a[0:3], v[72:75], 0\n\t"
  "ds_read_b64_tr_b16 v[90:91], v109 offset:10240\n\t"
  "v_add_u32 v111, 122880, v111\n\t"
  "v_mfma_f32_16x16x32_bf16 v[60:63], a[4:7], v[72:75], 0\n\t"
  "v_and_b32 v128, 0xffff0000, v101\n\t"
  "v_and_b32 v129, 0xffff0000, v103\n\t"
  "v_mfma_f32_16x16x32_bf16 v[64:67], a[8:11], v[72:75], 0\n\t"
  "s_waitcnt lgkmcnt(4)\n\t"
  "v_rcp_f32 v128, v128\n\t"
  "v_mfma_f32_16x16x32_bf16 v[44:47], a[12:15], v[76:79], v[44:47]\n\t"
  "ds_read_b64_tr_b16 v[68:69], v108 offset:12288\n\t"
  "s_add_u32 s45, %[sW2], 47104\n\t"
  "v_mfma_f32_16x16x32_bf16 v[48:51], a[16:19], v[76:79], v[48:51]\n\t"
  "ds_read_b64_tr_b16 v[70:71], v108 offset:14336\n\t"
  "v_rcp_f32 v129, v129\n\t"
  "v_mfma_f32_16x16x32_bf16 v[52:55], a[20:23], v[76:79], v[52:55]\n\t"
  "ds_read_b64_tr_b16 v[72:73], v109 offset:12288\n\t"
  "v_mul_f32 v128, v128, v128\n\t"
  "v_mfma_f32_16x16x32_bf16 v[56:59], a[12:15], v[80:83], v[56:59]\n\t"
  "ds_read_b64_tr_b16 v[74:75], v109 offset:14336\n\t"
  "v_mul_f32 v129, v129, v129\n\t"
  "v_mfma_f32_16x16x32_bf16 v[60:63], a[16:19], v[80:83], v[60:63]\n\t"
  "buffer_load_dwordx4 a[0:3], v1, %[srd], s45 offen offset:0 sc1\n\t"
  "v_max_f32 v20, 0, v20\n\t"
  "v_mfma_f32_16x16x32_bf16 v[64:67], a[20:23], v[80:83], v[64:67]\n\t"
  "s_waitcnt lgkmcnt(4)\n\t"
  "v_max_f32 v21, 0, v21\n\t"
  "v_mfma_f32_16x16x32_bf16 v[44:47], a[24:27], v[84:87], v[44:47]\n\t"
  "ds_read_b64_tr_b16 v[76:77], v108 offset:16384\n\t"
  "v_mul_f32 v22, v22, v128\n\t"
  "v_mfma_f32_16x16x32_bf16 v[48:51], a[28:31], v[84:87], v[48:51]\n\t"
  "ds_read_b64_tr_b16 v[78:79], v108 offset:18432\n\t"
  "v_mul_f32 v23, v23, v128\n\t"
  "v_mfma_f32_16x16x32_bf16 v[52:55], a[32:35], v[84:87], v[52:55]\n\t"
  "ds_read_b64_tr_b16 v[80:81], v109 offset:16384\n\t"
  "buffer_load_dwordx4 a[4:7], v1, %[srd], s45 offen offset:1024 sc1\n\t"
  "v_mfma_f32_16x16x32_bf16 v[56:59], a[24:27], v[88:91], v[56:59]\n\t"
  "ds_read_b64_tr_b16 v[82:83], v109 offset:18432\n\t"
  "v_mul_f32 v20, v20, v22\n\t"
  "v_mfma_f32_16x16x32_bf16 v[60:63], a[28:31], v[88:91], v[60:63]\n\t"
  "v_mul_f32 v21, v21, v23\n\t"
  "v_cvt_pk_bf16_f32 v116, v20, v21\n\t"
  "v_mfma_f32_16x16x32_bf16 v[64:67], a[32:35], v[88:91], v[64:67]\n\t"
  "s_waitcnt lgkmcnt(4)\n\t"
  "v_cvt_pk_bf16_f32 v112, v20, v21\n\t"
  "v_mfma_f32_16x16x32_bf16 v[44:47], a[36:39], v[68:71], v[44:47]\n\t"
  "ds_read_b64_tr_b16 v[84:85], v108 offset:20480\n\t"
  "s_movk_i32 s56, 8192\n\t"
  "v_mfma_f32_16x16x32_bf16 v[48:51], a[40:43], v[68:71], v[48:51]\n\t"
  "ds_read_b64_tr_b16 v[86:87], v108 offset:22528\n\t"
  "s_waitcnt vmcnt(7)\n\t"
  "v_mfma_f32_16x16x32_bf16 v[52:55], a[44:47], v[68:71], v[52:55]\n\t"
  "ds_read_b64_tr_b16 v[88:89], v109 offset:20480\n\t"
  "buffer_store_dwordx4 v[132:135], v9, %[srd], s56 offen sc1\n\t"
  "v_mfma_f32_16x16x32_bf16 v[56:59], a[36:39], v[72:75], v[56:59]\n\t"
  "ds_read_b64_tr_b16 v[90:91], v109 offset:22528\n\t"
  "buffer_load_dwordx4 a[8:11], v1, %[srd], s45 offen offset:2048 sc1\n\t"
  "v_mfma_f32_16x16x32_bf16 v[60:63], a[40:43], v[72:75], v[60:63]\n\t"
  "buffer_load_dwordx4 a[12:15], v1, %[srd], s45 offen offset:3072 sc1\n\t"
  "v_max_f32 v24, 0, v24\n\t"
  "v_mfma_f32_16x16x32_bf16 v[64:67], a[44:47], v[72:75], v[64:67]\n\t"
  "s_waitcnt lgkmcnt(4)\n\t"
  "v_max_f32 v25, 0, v25\n\t"
  "v_mfma_f32_16x16x32_bf16 v[44:47], a[48:51], v[76:79], v[44:47]\n\t"
  "ds_read_b64_tr_b16 v[68:69], v108 offset:24576\n\t"
  "v_mul_f32 v26, v26, v128\n\t"
  "v_mfma_f32_16x16x32_bf16 v[48:51], a[52:55], v[76:79], v[48:51]\n\t"
  "ds_read_b64_tr_b16 v[70:71], v108 offset:26624\n\t"
  "v_mul_f32 v27, v27, v128\n\t"
  "v_mfma_f32_16x16x32_bf16 v[52:55], a[56:59], v[76:79], v[52:55]\n\t"
  "ds_read_b64_tr_b16 v[72:73], v109 offset:24576\n\t"
  "s_add_u32 s45, %[sW2], 51200\n\t"
  "v_mfma_f32_16x16x32_bf16 v[56:59], a[48:51], v[80:83], v[56:59]\n\t"
  "ds_read_b64_tr_b16 v[74:75], v109 offset:26624\n\t"
  "v_mul_f32 v24, v24, v26\n\t"
  "v_mfma_f32_16x16x32_bf16 v[60:63], a[52:55], v[80:83], v[60:63]\n\t"
  "v_mul_f32 v25, v25, v27\n\t"
  "v_cvt_pk_bf16_f32 v117, v24, v25\n\t"
  "v_mfma_f32_16x16x32_bf16 v[64:67], a[56:59], v[80:83], v[64:67]\n\t"
  "s_waitcnt lgkmcnt(4)\n\t"
  "v_cvt_pk_bf16_f32 v113, v24, v25\n\t"
  "v_mfma_f32_16x16x32_bf16 v[44:47], a[60:63], v[84:87], v[44:47]\n\t"
  "ds_read_b64_tr_b16 v[76:77], v108 offset:28672\n\t"
  "s_add_u32 s57, %[sFL], 0\n\t"
  "v_mfma_f32_16x16x32_bf16 v[48:51], a[64:67], v[84:87], v[48:51]\n\t"
  "ds_read_b64_tr_b16 v[78:79], v108 offset:30720\n\t"
  "buffer_load_dwordx2 v[138:139], v137, %[srd], s57 offen sc1\n\t"
  "v_mfma_f32_16x16x32_bf16 v[52:55], a[68:71], v[84:87], v[52:55]\n\t"
  "ds_read_b64_tr_b16 v[80:81], v109 offset:28672\n\t"
  "buffer_load_dwordx4 a[16:19], v1, %[srd], s45 offen offset:0 sc1\n\t"
  "v_mfma_f32_16x16x32_bf16 v[56:59], a[60:63], v[88:91], v[56:59]\n\t"
  "ds_read_b64_tr_b16 v[82:83], v109 offset:30720\n\t"
  "v_max_f32 v28, 0, v28\n\t"
  "v_mfma_f32_16x16x32_bf16 v[60:63], a[64:67], v[88:91], v[60:63]\n\t"
  "buffer_load_dwordx4 a[20:23], v1, %[srd], s45 offen offset:1024 sc1\n\t"
  "v_max_f32 v29, 0, v29\n\t"
  "v_mfma_f32_16x16x32_bf16 v[64:67], a[68:71], v[88:91], v[64:67]\n\t"
  "s_waitcnt lgkmcnt(4)\n\t"
  "v_mul_f32 v30, v30, v128\n\t"
  "v_mfma_f32_16x16x32_bf16 v[44:47], a[72:75], v[68:71], v[44:47]\n\t"
  "ds_read_b64_tr_b16 v[84:85], v108 offset:32768\n\t"
  "v_mul_f32 v31, v31, v128\n\t"
  "v_mfma_f32_16x16x32_bf16 v[48:51], a[76:79], v[68:71], v[48:51]\n\t"
  "ds_read_b64_tr_b16 v[86:87], v108 offset:34816\n\t"
  "v_mul_f32 v28, v28, v30\n\t"
  "v_mfma_f32_16x16x32_bf16 v[52:55], a[80:83], v[68:71], v[52:55]\n\t"
  "ds_read_b64_tr_b16 v[88:89], v109 offset:32768\n\t"
  "buffer_load_dwordx4 a[24:27], v1, %[srd], s45 offen offset:2048 sc1\n\t"
  "v_mfma_f32_16x16x32_bf16 v[56:59], a[72:75], v[72:75], v[56:59]\n\t"
  "ds_read_b64_tr_b16 v[90:91], v109 offset:34816\n\t"
  "v_permlane16_swap_b32 v116, v117\n\t"
  "v_mfma_f32_16x16x32_bf16 v[60:63], a[76:79], v[72:75], v[60:63]\n\t"
  "v_mul_f32 v29, v29, v31\n\t"
  "v_cvt_pk_bf16_f32 v118, v28, v29\n\t"
  "v_mfma_f32_16x16x32_bf16 v[64:67], a[80:83], v[72:75], v[64:67]\n\t"
  "s_waitcnt lgkmcnt(4)\n\t"
  "v_cvt_pk_bf16_f32 v114, v28, v29\n\t"
  "v_mfma_f32_16x16x32_bf16 v[44:47], a[84:87], v[76:79], v[44:47]\n\t"
  "ds_read_b64_tr_b16 v[68:69], v108 offset:36864\n\t"
  "s_mov_b32 s55, 3145728\n\t"
  "v_mfma_f32_16x16x32_bf16 v[48:51], a[88:91], v[76:79], v[48:51]\n\t"
  "ds_read_b64_tr_b16 v[70:71], v108 offset:38912\n\t"
  "buffer_load_dwordx4 a[28:31], v1, %[srd], s45 offen offset:3072 sc1\n\t"
  "v_mfma_f32_16x16x32_bf16 v[52:55], a[92:95], v[76:79], v[52:55]\n\t"
  "ds_read_b64_tr_b16 v[72:73], v109 offset:36864\n\t"
  "v_permlane16_swap_b32 v118, v115\n\t"
  "v_mfma_f32_16x16x32_bf16 v[56:59], a[84:87], v[80:83], v[56:59]\n\t"
  "ds_read_b64_tr_b16 v[74:75], v109 offset:38912\n\t"
  "v_permlane32_swap_b32 v117, v115\n\t"
  "v_mfma_f32_16x16x32_bf16 v[60:63], a[88:91], v[80:83], v[60:63]\n\t"
  "buffer_store_dwordx4 v[112:115], v8, %[srd], s55 offen sc1\n\t"
  "v_max_f32 v32, 0, v32\n\t"
  "v_mfma_f32_16x16x32_bf16 v[64:67], a[92:95], v[80:83], v[64:67]\n\t"
  "s_waitcnt lgkmcnt(4)\n\t"
  "s_add_u32 s45, %[sW2], 55296\n\t"
  "v_mfma_f32_16x16x32_bf16 v[44:47], a[96:99], v[84:87], v[44:47]\n\t"
  "ds_read_b64_tr_b16 v[76:77], v110 offset:0\n\t"
  "v_max_f32 v33, 0, v33\n\t"
  "v_mfma_f32_16x16x32_bf16 v[48:51], a[100:103], v[84:87], v[48:51]\n\t"
  "ds_read_b64_tr_b16 v[78:79], v110 offset:2048\n\t"
  "v_mul_f32 v34, v34, v129\n\t"
  "v_mfma_f32_16x16x32_bf16 v[52:55], a[104:107], v[84:87], v[52:55]\n\t"
  "ds_read_b64_tr_b16 v[80:81], v111 offset:0\n\t"
  "v_mul_f32 v35, v35, v129\n\t"
  "v_mfma_f32_16x16x32_bf16 v[56:59], a[96:99], v[88:91], v[56:59]\n\t"
  "ds_read_b64_tr_b16 v[82:83], v111 offset:2048\n\t"
  "buffer_load_dwordx4 a[32:35], v1, %[srd], s45 offen offset:0 sc1\n\t"
  "v_mfma_f32_16x16x32_bf16 v[60:63], a[100:103], v[88:91], v[60:63]\n\t"
  "v_mul_f32 v32, v32, v34\n\t"
  "v_mul_f32 v33, v33, v35\n\t"
  "v_mfma_f32_16x16x32_bf16 v[64:67], a[104:107], v[88:91], v[64:67]\n\t"
  "s_waitcnt lgkmcnt(4)\n\t"
  "v_cvt_pk_bf16_f32 v124, v32, v33\n\t"
  "v_mfma_f32_16x16x32_bf16 v[44:47], a[108:111], v[68:71], v[44:47]\n\t"
  "ds_read_b64_tr_b16 v[84:85], v110 offset:4096\n\t"
  "v_cvt_pk_bf16_f32 v120, v32, v33\n\t"
  "v_mfma_f32_16x16x32_bf16 v[48:51], a[112:115], v[68:71], v[48:51]\n\t"
  "ds_read_b64_tr_b16 v[86:87], v110 offset:6144\n\t"
  "buffer_load_dwordx4 a[36:39], v1, %[srd], s45 offen offset:1024 sc1\n\t"
  "v_mfma_f32_16x16x32_bf16 v[52:55], a[116:119], v[68:71], v[52:55]\n\t"
  "ds_read_b64_tr_b16 v[88:89], v111 offset:4096\n\t"
  "v_max_f32 v36, 0, v36\n\t"
  "v_mfma_f32_16x16x32_bf16 v[56:59], a[108:111], v[72:75], v[56:59]\n\t"
  "ds_read_b64_tr_b16 v[90:91], v111 offset:6144\n\t"
  "v_max_f32 v37, 0, v37\n\t"
  "v_mfma_f32_16x16x32_bf16 v[60:63], a[112:115], v[72:75], v[60:63]\n\t"
  "v_mul_f32 v38, v38, v129\n\t"
  "v_mul_f32 v39, v39, v129\n\t"
  "v_mfma_f32_16x16x32_bf16 v[64:67], a[116:119], v[72:75], v[64:67]\n\t"
  "s_waitcnt lgkmcnt(4)\n\t"
  "buffer_load_dwordx4 a[40:43], v1, %[srd], s45 offen offset:2048 sc1\n\t"
  "v_mfma_f32_16x16x32_bf16 v[44:47], a[120:123], v[76:79], v[44:47]\n\t"
  "ds_read_b64_tr_b16 v[68:69], v110 offset:8192\n\t"
  "v_mul_f32 v36, v36, v38\n\t"
  "v_mfma_f32_16x16x32_bf16 v[48:51], a[124:127], v[76:79], v[48:51]\n\t"
  "ds_read_b64_tr_b16 v[70:71], v110 offset:10240\n\t"
  "v_mul_f32 v37, v37, v39\n\t"
  "v_mfma_f32_16x16x32_bf16 v[52:55], a[128:131], v[76:79], v[52:55]\n\t"
  "ds_read_b64_tr_b16 v[72:73], v111 offset:8192\n\t"
  "v_cvt_pk_bf16_f32 v125, v36, v37\n\t"
  "v_mfma_f32_16x16x32_bf16 v[56:59], a[120:123], v[80:83], v[56:59]\n\t"
  "ds_read_b64_tr_b16 v[74:75], v111 offset:10240\n\t"
  "buffer_load_dwordx4 a[44:47], v1, %[srd], s45 offen offset:3072 sc1\n\t"
  "v_mfma_f32_16x16x32_bf16 v[60:63], a[124:127], v[80:83], v[60:63]\n\t"
  "v_cvt_pk_bf16_f32 v121, v36, v37\n\t"
  "v_max_f32 v40, 0, v40\n\t"
  "v_mfma_f32_16x16x32_bf16 v[64:67], a[128:131], v[80:83], v[64:67]\n\t"
  "s_waitcnt lgkmcnt(4)\n\t"
  "v_max_f32 v41, 0, v41\n\t"
  "v_mfma_f32_16x16x32_bf16 v[44:47], a[132:135], v[84:87], v[44:47]\n\t"
  "ds_read_b64_tr_b16 v[76:77], v110 offset:12288\n\t"
  "v_mul_f32 v42, v42, v129\n\t"
  "v_mfma_f32_16x16x32_bf16 v[48:51], a[136:139], v[84:87], v[48:51]\n\t"
  "ds_read_b64_tr_b16 v[78:79], v110 offset:14336\n\t"
  "s_add_u32 s45, %[sW2], 59392\n\t"
  "v_mfma_f32_16x16x32_bf16 v[52:55], a[140:143], v[84:87], v[52:55]\n\t"
  "ds_read_b64_tr_b16 v[80:81], v111 offset:12288\n\t"
  "v_mul_f32 v43, v43, v129\n\t"
  "v_mfma_f32_16x16x32_bf16 v[56:59], a[132:135], v[88:91], v[56:59]\n\t"
  "ds_read_b64_tr_b16 v[82:83], v111 offset:14336\n\t"
  "v_mul_f32 v40, v40, v42\n\t"
  "v_mfma_f32_16x16x32_bf16 v[60:63], a[136:139], v[88:91], v[60:63]\n\t"
  "v_permlane16_swap_b32 v124, v125\n\t"
  "v_mul_f32 v41, v41, v43\n\t"
  "v_mfma_f32_16x16x32_bf16 v[64:67], a[140:143], v[88:91], v[64:67]\n\t"
  "s_waitcnt lgkmcnt(4)\n\t"
  "buffer_load_dwordx4 a[48:51], v1, %[srd], s45 offen offset:0 sc1\n\t"
  "v_mfma_f32_16x16x32_bf16 v[44:47], a[144:147], v[68:71], v[44:47]\n\t"
  "ds_read_b64_tr_b16 v[84:85], v110 offset:16384\n\t"
  "v_cvt_pk_bf16_f32 v126, v40, v41\n\t"
  "v_mfma_f32_16x16x32_bf16 v[48:51], a[148:151], v[68:71], v[48:51]\n\t"
  "ds_read_b64_tr_b16 v[86:87], v110 offset:18432\n\t"
  "v_cvt_pk_bf16_f32 v122, v40, v41\n\t"
  "v_mfma_f32_16x16x32_bf16 v[52:55], a[152:155], v[68:71], v[52:55]\n\t"
  "ds_read_b64_tr_b16 v[88:89], v111 offset:16384\n\t"
  "s_mov_b32 s55, 3194880\n\t"
  "v_mfma_f32_16x16x32_bf16 v[56:59], a[144:147], v[72:75], v[56:59]\n\t"
  "ds_read_b64_tr_b16 v[90:91], v111 offset:18432\n\t"
  "buffer_load_dwordx4 a[52:55], v1, %[srd], s45 offen offset:1024 sc1\n\t"
  "v_mfma_f32_16x16x32_bf16 v[60:63], a[148:151], v[72:75], v[60:63]\n\t"
  "v_permlane16_swap_b32 v126, v123\n\t"
  "v_mfma_f32_16x16x32_bf16 v[64:67], a[152:155], v[72:75], v[64:67]\n\t"
  "s_waitcnt lgkmcnt(4)\n\t"
  "v_permlane32_swap_b32 v125, v123\n\t"
  "v_mfma_f32_16x16x32_bf16 v[44:47], a[156:159], v[76:79], v[44:47]\n\t"
  "ds_read_b64_tr_b16 v[68:69], v110 offset:20480\n\t"
  "buffer_store_dwordx4 v[120:123], v8, %[srd], s55 offen sc1\n\t"
  "v_mfma_f32_16x16x32_bf16 v[48:51], a[160:163], v[76:79], v[48:51]\n\t"
  "ds_read_b64_tr_b16 v[70:71], v110 offset:22528\n\t"
  "v_and_b32 v128, 0xffff0000, v105\n\t"
  "v_mfma_f32_16x16x32_bf16 v[52:55], a[164:167], v[76:79], v[52:55]\n\t"
  "ds_read_b64_tr_b16 v[72:73], v111 offset:20480\n\t"
  "v_and_b32 v129, 0xffff0000, v107\n\t"
  "v_mfma_f32_16x16x32_bf16 v[56:59], a[156:159], v[80:83], v[56:59]\n\t"
  "ds_read_b64_tr_b16 v[74:75], v111 offset:22528\n\t"
  "s_waitcnt vmcnt(12)\n\t"
  "v_cmp_ne_u64 vcc, v[132:133], v[138:139]\n\t"
  "v_mfma_f32_16x16x32_bf16 v[60:63], a[160:163], v[80:83], v[60:63]\n\t"
  "v_rcp_f32 v128, v128\n\t"
  "v_rcp_f32 v129, v129\n\t"
  "v_mfma_f32_16x16x32_bf16 v[64:67], a[164:167], v[80:83], v[64:67]\n\t"
  "s_waitcnt lgkmcnt(4)\n\t"
  "s_cbranch_vccnz Lfl0_slow%=\n\t"
  "Lfl0_back%=:\n\t"
  "v_mfma_f32_16x16x32_bf16 v[44:47], a[168:171], v[84:87], v[44:47]\n\t"
  "ds_read_b64_tr_b16 v[76:77], v110 offset:24576\n\t"
  "s_add_u32 s44, %[sH2ld], 0\n\t"
  "v_mfma_f32_16x16x32_bf16 v[48:51], a[172:175], v[84:87], v[48:51]\n\t"
  "ds_read_b64_tr_b16 v[78:79], v110 offset:26624\n\t"
  "v_mul_f32 v128, v128, v128\n\t"
  "v_mfma_f32_16x16x32_bf16 v[52:55], a[176:179], v[84:87], v[52:55]\n\t"
  "ds_read_b64_tr_b16 v[80:81], v111 offset:24576\n\t"
  "buffer_load_dwordx4 a[56:59], v1, %[srd], s44 offen offset:0 sc1\n\t"
  "v_mfma_f32_16x16x32_bf16 v[56:59], a[168:171], v[88:91], v[56:59]\n\t"
  "ds_read_b64_tr_b16 v[82:83], v111 offset:26624\n\t"
  "ds_read_b128 a[160:163], v141 offset:0\n\t"
  "v_mfma_f32_16x16x32_bf16 v[60:63], a[172:175], v[88:91], v[60:63]\n\t"
  "ds_read_b128 a[164:167], v141 offset:1024\n\t"
  "v_mul_f32 v129, v129, v129\n\t"
  "v_mfma_f32_16x16x32_bf16 v[64:67], a[176:179], v[88:91], v[64:67]\n\t"
  "s_waitcnt lgkmcnt(6)\n\t"
  "buffer_load_dwordx4 a[60:63], v1, %[srd], s44 offen offset:1024 sc1\n\t"
  "v_mfma_f32_16x16x32_bf16 v[44:47], a[180:183], v[68:71], v[44:47]\n\t"
  "ds_read_b64_tr_b16 v[84:85], v110 offset:28672\n\t"
  "ds_read_b128 a[168:171], v141 offset:2048\n\t"
  "v_mfma_f32_16x16x32_bf16 v[48:51], a[184:187], v[68:71], v[48:51]\n\t"
  "ds_read_b64_tr_b16 v[86:87], v110 offset:30720\n\t"
  "ds_read_b128 a[172:175], v141 offset:3072\n\t"
  "v_mfma_f32_16x16x32_bf16 v[52:55], a[188:191], v[68:71], v[52:55]\n\t"
  "ds_read_b64_tr_b16 v[88:89], v111 offset:28672\n\t"
  "buffer_load_dwordx4 a[64:67], v1, %[srd], s44 offen offset:2048 sc1\n\t"
  "v_mfma_f32_16x16x32_bf16 v[56:59], a[180:183], v[72:75], v[56:59]\n\t"
  "ds_read_b64_tr_b16 v[90:91], v111 offset:30720\n\t"
  "ds_read_b128 a[176:179], v141 offset:4096\n\t"
  "v_mfma_f32_16x16x32_bf16 v[60:63], a[184:187], v[72:75], v[60:63]\n\t"
  "v_mfma_f32_16x16x32_bf16 v[64:67], a[188:191], v[72:75], v[64:67]\n\t"
  "s_waitcnt lgkmcnt(9)\n\t"
  "buffer_load_dwordx4 a[68:71], v1, %[srd], s44 offen offset:3072 sc1\n\t"
  "v_mfma_f32_16x16x32_bf16 v[44:47], a[192:195], v[76:79], v[44:47]\n\t"
  "ds_read_b64_tr_b16 v[68:69], v110 offset:32768\n\t"
  "ds_read_b128 a[180:183], v141 offset:5120\n\t"
  "v_mfma_f32_16x16x32_bf16 v[48:51], a[196:199], v[76:79], v[48:51]\n\t"
  "ds_read_b64_tr_b16 v[70:71], v110 offset:34816\n\t"
  "ds_read_b128 a[184:187], v141 offset:6144\n\t"
  "v_mfma_f32_16x16x32_bf16 v[52:55], a[200:203], v[76:79], v[52:55]\n\t"
  "ds_read_b64_tr_b16 v[72:73], v111 offset:32768\n\t"
  "s_add_u32 s44, %[sH2ld], 4096\n\t"
  "v_mfma_f32_16x16x32_bf16 v[56:59], a[192:195], v[80:83], v[56:59]\n\t"
  "ds_read_b64_tr_b16 v[74:75], v111 offset:34816\n\t"
  "ds_read_b128 a[188:191], v141 offset:7168\n\t"
  "v_mfma_f32_16x16x32_bf16 v[60:63], a[196:199], v[80:83], v[60:63]\n\t"
  "buffer_load_dwordx4 a[72:75], v1, %[srd], s44 offen offset:0 sc1\n\t"
  "v_mfma_f32_16x16x32_bf16 v[64:67], a[200:203], v[80:83], v[64:67]\n\t"
  "s_waitcnt lgkmcnt(8)\n\t"
  "ds_read_b128 a[192:195], v141 offset:8192\n\t"
  "v_mfma_f32_16x16x32_bf16 v[44:47], a[204:207], v[84:87], v[44:47]\n\t"
  "ds_read_b64_tr_b16 v[76:77], v110 offset:36864\n\t"
  "ds_read_b128 a[196:199], v141 offset:9216\n\t"
  "v_mfma_f32_16x16x32_bf16 v[48:51], a[208:211], v[84:87], v[48:51]\n\t"
  "ds_read_b64_tr_b16 v[78:79], v110 offset:38912\n\t"
  "buffer_load_dwordx4 a[76:79], v1, %[srd], s44 offen offset:1024 sc1\n\t"
  "v_mfma_f32_16x16x32_bf16 v[52:55], a[212:215], v[84:87], v[52:55]\n\t"
  "ds_read_b64_tr_b16 v[80:81], v111 offset:36864\n\t"
  "ds_read_b128 a[200:203], v141 offset:10240\n\t"
  "v_mfma_f32_16x16x32_bf16 v[56:59], a[204:207], v[88:91], v[56:59]\n\t"
  "ds_read_b64_tr_b16 v[82:83], v111 offset:38912\n\t"
  "v_mfma_f32_16x16x32_bf16 v[60:63], a[208:211], v[88:91], v[60:63]\n\t"
  "buffer_load_dwordx4 a[80:83], v1, %[srd], s44 offen offset:2048 sc1\n\t"
  "v_mfma_f32_16x16x32_bf16 v[64:67], a[212:215], v[88:91], v[64:67]\n\t"
  "s_waitcnt lgkmcnt(8)\n\t"
  "ds_read_b128 a[204:207], v141 offset:11264\n\t"
  "v_mfma_f32_16x16x32_bf16 v[44:47], a[216:219], v[68:71], v[44:47]\n\t"
  "ds_read_b128 a[208:211], v141 offset:12288\n\t"
  "ds_read_b128 a[212:215], v141 offset:13312\n\t"
  "v_mfma_f32_16x16x32_bf16 v[48:51], a[220:223], v[68:71], v[48:51]\n\t"
  "buffer_load_dwordx4 a[84:87], v1, %[srd], s44 offen offset:3072 sc1\n\t"
  "v_mfma_f32_16x16x32_bf16 v[52:55], a[224:227], v[68:71], v[52:55]\n\t"
  "v_mfma_f32_16x16x32_bf16 v[56:59], a[216:219], v[72:75], v[56:59]\n\t"
  "v_mfma_f32_16x16x32_bf16 v[60:63], a[220:223], v[72:75], v[60:63]\n\t"
  "s_add_u32 s44, %[sH2ld], 8192\n\t"
  "v_mfma_f32_16x16x32_bf16 v[64:67], a[224:227], v[72:75], v[64:67]\n\t"
  "s_waitcnt lgkmcnt(3)\n\t"
  "ds_read_b128 a[216:219], v141 offset:14336\n\t"
  "v_mfma_f32_16x16x32_bf16 v[44:47], a[228:231], v[76:79], v[44:47]\n\t"
  "s_add_u32 s57, %[sFL], 8192\n\t"
  "buffer_load_dwordx2 v[138:139], v137, %[srd], s57 offen sc1\n\t"
  "v_mfma_f32_16x16x32_bf16 v[48:51], a[232:235], v[76:79], v[48:51]\n\t"
  "buffer_load_dwordx4 a[88:91], v1, %[srd], s44 offen offset:0 sc1\n\t"
  "ds_read_b128 a[220:223], v141 offset:15360\n\t"
  "v_mfma_f32_16x16x32_bf16 v[52:55], a[236:239], v[76:79], v[52:55]\n\t"
  "ds_read_b128 a[224:227], v141 offset:16384\n\t"
  "v_mfma_f32_16x16x32_bf16 v[56:59], a[228:231], v[80:83], v[56:59]\n\t"
  "buffer_load_dwordx4 a[92:95], v1, %[srd], s44 offen offset:1024 sc1\n\t"
  "v_mfma_f32_16x16x32_bf16 v[60:63], a[232:235], v[80:83], v[60:63]\n\t"
  "v_mfma_f32_16x16x32_bf16 v[64:67], a[236:239], v[80:83], v[64:67]\n\t"
  "ds_read_b128 a[228:231], v141 offset:17408\n\t"
  "ds_read_b128 a[232:235], v141 offset:18432\n\t"
  "v_mfma_f32_16x16x16_bf16 v[44:47], a[240:241], v[104:105], v[44:47]\n\t"
  "buffer_load_dwordx4 a[96:99], v1, %[srd], s44 offen offset:2048 sc1\n\t"
  "ds_read_b128 a[236:239], v141 offset:19456\n\t"
  "v_mfma_f32_16x16x16_bf16 v[48:51], a[242:243], v[104:105], v[48:51]\n\t"
  "v_mfma_f32_16x16x16_bf16 v[52:55], a[244:245], v[104:105], v[52:55]\n\t"
  "v_mfma_f32_16x16x16_bf16 v[56:59], a[240:241], v[106:107], v[56:59]\n\t"
  "buffer_load_dwordx4 a[100:103], v1, %[srd], s44 offen offset:3072 sc1\n\t"
  "v_mfma_f32_16x16x16_bf16 v[60:63], a[242:243], v[106:107], v[60:63]\n\t"
  "v_mfma_f32_16x16x16_bf16 v[64:67], a[244:245], v[106:107], v[64:67]\n\t"
  "s_movk_i32 s56, 16384\n\t"
  "s_waitcnt vmcnt(13)\n\t"
  "buffer_store_dwordx4 v[132:135], v9, %[srd], s56 offen sc1\n\t"
  "v_max_f32 v44, 0, v44\n\t"
  "v_max_f32 v45, 0, v45\n\t"
  "v_pk_mul_f32 v[46:47], v[46:47], v[128:129] op_sel:[0,0] op_sel_hi:[1,0]\n\t"
  "v_pk_mul_f32 v[44:45], v[44:45], v[46:47]\n\t"
  "v_cvt_pk_bf16_f32 v116, v44, v45\n\t"
  "v_cvt_pk_bf16_f32 v112, v44, v45\n\t"
  "v_max_f32 v48, 0, v48\n\t"
  "v_max_f32 v49, 0, v49\n\t"
  "v_pk_mul_f32 v[50:51], v[50:51], v[128:129] op_sel:[0,0] op_sel_hi:[1,0]\n\t"
  "v_pk_mul_f32 v[48:49], v[48:49], v[50:51]\n\t"
  "v_cvt_pk_bf16_f32 v117, v48, v49\n\t"
  "v_cvt_pk_bf16_f32 v113, v48, v49\n\t"
  "v_max_f32 v52, 0, v52\n\t"
  "v_max_f32 v53, 0, v53\n\t"
  "v_pk_mul_f32 v[54:55], v[54:55], v[128:129] op_sel:[0,0] op_sel_hi:[1,0]\n\t"
  "v_permlane16_swap_b32 v116, v117\n\t"
  "v_pk_mul_f32 v[52:53], v[52:53], v[54:55]\n\t"
  "v_cvt_pk_bf16_f32 v118, v52, v53\n\t"
  "v_cvt_pk_bf16_f32 v114, v52, v53\n\t"
  "s_mov_b32 s55, 3244032\n\t"
  "v_permlane16_swap_b32 v118, v115\n\t"
  "s_nop 1\n\t"
  "v_permlane32_swap_b32 v117, v115\n\t"
  "buffer_store_dwordx4 v[112:115], v8, %[srd], s55 offen sc1\n\t"
  "v_max_f32 v56, 0, v56\n\t"
  "v_max_f32 v57, 0, v57\n\t"
  "v_pk_mul_f32 v[58:59], v[58:59], v[128:129] op_sel:[0,1] op_sel_hi:[1,1]\n\t"
  "v_pk_mul_f32 v[56:57], v[56:57], v[58:59]\n\t"
  "v_cvt_pk_bf16_f32 v124, v56, v57\n\t"
  "v_cvt_pk_bf16_f32 v120, v56, v57\n\t"
  "v_max_f32 v60, 0, v60\n\t"
  "v_max_f32 v61, 0, v61\n\t"
  "v_pk_mul_f32 v[62:63], v[62:63], v[128:129] op_sel:[0,1] op_sel_hi:[1,1]\n\t"
  "v_pk_mul_f32 v[60:61], v[60:61], v[62:63]\n\t"
  "v_cvt_pk_bf16_f32 v125, v60, v61\n\t"
  "v_cvt_pk_bf16_f32 v121, v60, v61\n\t"
  "v_max_f32 v64, 0, v64\n\t"
  "v_max_f32 v65, 0, v65\n\t"
  "v_pk_mul_f32 v[66:67], v[66:67], v[128:129] op_sel:[0,1] op_sel_hi:[1,1]\n\t"
  "v_permlane16_swap_b32 v124, v125\n\t"
  "v_pk_mul_f32 v[64:65], v[64:65], v[66:67]\n\t"
  "v_cvt_pk_bf16_f32 v126, v64, v65\n\t"
  "v_cvt_pk_bf16_f32 v122, v64, v65\n\t"
  "s_mov_b32 s55, 3293184\n\t"
  "v_permlane16_swap_b32 v126, v123\n\t"
  "s_nop 1\n\t"
  "v_permlane32_swap_b32 v125, v123\n\t"
  "buffer_store_dwordx4 v[120:123], v8, %[srd], s55 offen sc1\n\t"
  "s_waitcnt lgkmcnt(0)\n\t"
  "s_barrier\n\t"
  "s_waitcnt vmcnt(15)\n\t"
  "v_mfma_f32_16x16x32_bf16 v[20:23], v[144:147], a[56:59], 0\n\t"
  "v_lshrrev_b32 v63, 3, v1\n\t"
  "v_and_b32 v63, 0x60, v63\n\t"
  "v_mfma_f32_16x16x32_bf16 v[24:27], v[148:151], a[56:59], 0\n\t"
  "v_lshrrev_b32 v62, 1, v63\n\t"
  "buffer_load_dwordx4 v[68:71], v63, %[srd], %[sRT] offen offset:0 sc1\n\t"
  "v_mfma_f32_16x16x32_bf16 v[28:31], v[152:155], a[56:59], 0\n\t"
  "buffer_load_dwordx4 v[72:75], v63, %[srd], %[sRT] offen offset:16 sc1\n\t"
  "buffer_load_dwordx4 v[76:79], v63, %[srd], %[sRT] offen offset:128 sc1\n\t"
  "v_mfma_f32_16x16x32_bf16 v[32:35], v[156:159], a[56:59], 0\n\t"
  "buffer_load_dwordx4 v[80:83], v63, %[srd], %[sRT] offen offset:144 sc1\n\t"
  "buffer_load_dwordx4 v[84:87], v62, %[srd], %[sRT] offen offset:256 sc1\n\t"
  "v_mfma_f32_16x16x32_bf16 v[36:39], v[160:163], a[56:59], 0\n\t"
  "buffer_load_dwordx4 v[64:67], v17, %[srd], %[sRT] offen offset:0 sc1\n\t"
  "buffer_load_dwordx4 v[108:111], v17, %[srd], %[sRT] offen offset:16 sc1\n\t"
  "s_waitcnt vmcnt(21)\n\t"
  "v_mfma_f32_16x16x32_bf16 v[20:23], v[164:167], a[60:63], v[20:23]\n\t"
  "s_mul_i32 s48, %[w], -15360\n\t"
  "s_add_u32 s48, s48, 81920\n\t"
  "v_mfma_f32_16x16x32_bf16 v[24:27], v[168:171], a[60:63], v[24:27]\n\t"
  "v_add_u32 v62, s48, v13\n\t"
  "v_add_u32 v60, 61440, v14\n\t"
  "v_mfma_f32_16x16x32_bf16 v[28:31], v[172:175], a[60:63], v[28:31]\n\t"
  "v_add_u32 v61, 61440, v15\n\t"
  "v_accvgpr_read_b32 v89, a246\n\t"
  "v_mfma_f32_16x16x32_bf16 v[32:35], v[176:179], a[60:63], v[32:35]\n\t"
  "s_waitcnt vmcnt(14)\n\t"
  "v_cmp_ne_u64 vcc, v[132:133], v[138:139]\n\t"
  "v_mfma_f32_16x16x32_bf16 v[36:39], v[180:183], a[60:63], v[36:39]\n\t"
  "v_lshlrev_b32 v88, 16, v89\n\t"
  "v_and_b32 v89, 0xffff0000, v89\n\t"
  "v_mfma_f32_16x16x32_bf16 v[20:23], v[184:187], a[64:67], v[20:23]\n\t"
  "s_cbranch_vccnz Lfl1_slow%=\n\t"
  "Lfl1_back%=:\n\t"
  "v_mfma_f32_16x16x32_bf16 v[24:27], v[188:191], a[64:67], v[24:27]\n\t"
  "v_accvgpr_read_b32 v91, a247\n\t"
  "v_lshlrev_b32 v90, 16, v91\n\t"
  "v_mfma_f32_16x16x32_bf16 v[28:31], v[192:195], a[64:67], v[28:31]\n\t"
  "s_add_u32 s45, %[sH2ld], 98304\n\t"
  "v_and_b32 v91, 0xffff0000, v91\n\t"
  "v_mfma_f32_16x16x32_bf16 v[32:35], v[196:199], a[64:67], v[32:35]\n\t"
  "v_accvgpr_read_b32 v93, a248\n\t"
  "v_lshlrev_b32 v92, 16, v93\n\t"
  "v_mfma_f32_16x16x32_bf16 v[36:39], v[200:203], a[64:67], v[36:39]\n\t"
  "v_and_b32 v93, 0xffff0000, v93\n\t"
  "v_accvgpr_read_b32 v95, a249\n\t"
  "v_mfma_f32_16x16x32_bf16 v[20:23], v[204:207], a[68:71], v[20:23]\n\t"
  "buffer_load_dwordx4 a[104:107], v1, %[srd], s45 offen offset:0 sc1\n\t"
  "v_lshlrev_b32 v94, 16, v95\n\t"
  "v_mfma_f32_16x16x32_bf16 v[24:27], v[208:211], a[68:71], v[24:27]\n\t"
  "v_and_b32 v95, 0xffff0000, v95\n\t"
  "v_accvgpr_read_b32 v97, a250\n\t"
  "v_mfma_f32_16x16x32_bf16 v[28:31], v[212:215], a[68:71], v[28:31]\n\t"
  "v_lshlrev_b32 v96, 16, v97\n\t"
  "v_and_b32 v97, 0xffff0000, v97\n\t"
  "v_mfma_f32_16x16x32_bf16 v[32:35], v[216:219], a[68:71], v[32:35]\n\t"
  "buffer_load_dwordx4 a[108:111], v1, %[srd], s45 offen offset:1024 sc1\n\t"
  "v_accvgpr_read_b32 v99, a251\n\t"
  "v_mfma_f32_16x16x32_bf16 v[36:39], v[220:223], a[68:71], v[36:39]\n\t"
  "v_lshlrev_b32 v98, 16, v99\n\t"
  "v_and_b32 v99, 0xffff0000, v99\n\t"
  "v_mfma_f32_16x16x32_bf16 v[20:23], v[224:227], a[72:75], v[20:23]\n\t"
  "v_accvgpr_read_b32 v101, a252\n\t"
  "v_lshlrev_b32 v100, 16, v101\n\t"
  "v_mfma_f32_16x16x32_bf16 v[24:27], v[228:231], a[72:75], v[24:27]\n\t"
  "buffer_load_dwordx4 a[112:115], v1, %[srd], s45 offen offset:2048 sc1\n\t"
  "v_and_b32 v101, 0xffff0000, v101\n\t"
  "v_mfma_f32_16x16x32_bf16 v[28:31], v[232:235], a[72:75], v[28:31]\n\t"
  "v_accvgpr_read_b32 v103, a253\n\t"
  "v_lshlrev_b32 v102, 16, v103\n\t"
  "v_mfma_f32_16x16x32_bf16 v[32:35], v[236:239], a[72:75], v[32:35]\n\t"
  "v_and_b32 v103, 0xffff0000, v103\n\t"
  "v_accvgpr_read_b32 v105, a254\n\t"
  "v_mfma_f32_16x16x32_bf16 v[36:39], v[240:243], a[72:75], v[36:39]\n\t"
  "buffer_load_dwordx4 a[116:119], v1, %[srd], s45 offen offset:3072 sc1\n\t"
  "s_add_u32 s57, %[sFL], 16384\n\t"
  "v_mfma_f32_16x16x32_bf16 v[20:23], v[244:247], a[76:79], v[20:23]\n\t"
  "buffer_load_dwordx2 v[138:139], v137, %[srd], s57 offen sc1\n\t"
  "v_lshlrev_b32 v104, 16, v105\n\t"
  "v_mfma_f32_16x16x32_bf16 v[24:27], a[160:163], a[76:79], v[24:27]\n\t"
  "s_movk_i32 s56, 24576\n\t"
  "s_waitcnt vmcnt(12)\n\t"
  "v_mfma_f32_16x16x32_bf16 v[28:31], a[164:167], a[76:79], v[28:31]\n\t"
  "buffer_store_dwordx4 v[132:135], v9, %[srd], s56 offen sc1\n\t"
  "v_and_b32 v105, 0xffff0000, v105\n\t"
  "v_mfma_f32_16x16x32_bf16 v[32:35], a[168:171], a[76:79], v[32:35]\n\t"
  "s_add_u32 s45, %[sH2ld], 102400\n\t"
  "v_accvgpr_read_b32 v107, a255\n\t"
  "v_mfma_f32_16x16x32_bf16 v[36:39], a[172:175], a[76:79], v[36:39]\n\t"
  "v_lshlrev_b32 v106, 16, v107\n\t"
  "v_and_b32 v107, 0xffff0000, v107\n\t"
  "v_mfma_f32_16x16x32_bf16 v[20:23], a[176:179], a[80:83], v[20:23]\n\t"
  "v_mfma_f32_16x16x32_bf16 v[24:27], a[180:183], a[80:83], v[24:27]\n\t"
  "buffer_load_dwordx4 a[120:123], v1, %[srd], s45 offen offset:0 sc1\n\t"
  "v_mfma_f32_16x16x32_bf16 v[28:31], a[184:187], a[80:83], v[28:31]\n\t"
  "v_mfma_f32_16x16x32_bf16 v[32:35], a[188:191], a[80:83], v[32:35]\n\t"
  "v_mfma_f32_16x16x32_bf16 v[36:39], a[192:195], a[80:83], v[36:39]\n\t"
  "buffer_load_dwordx4 a[124:127], v1, %[srd], s45 offen offset:1024 sc1\n\t"
  "v_mfma_f32_16x16x32_bf16 v[20:23], a[196:199], a[84:87], v[20:23]\n\t"
  "v_mfma_f32_16x16x32_bf16 v[24:27], a[200:203], a[84:87], v[24:27]\n\t"
  "v_mfma_f32_16x16x32_bf16 v[28:31], a[204:207], a[84:87], v[28:31]\n\t"
  "buffer_load_dwordx4 a[128:131], v1, %[srd], s45 offen offset:2048 sc1\n\t"
  "v_mfma_f32_16x16x32_bf16 v[32:35], a[208:211], a[84:87], v[32:35]\n\t"
  "v_mfma_f32_16x16x32_bf16 v[36:39], a[212:215], a[84:87], v[36:39]\n\t"
  "v_mfma_f32_16x16x32_bf16 v[20:23], a[216:219], a[88:91], v[20:23]\n\t"
  "buffer_load_dwordx4 a[132:135], v1, %[srd], s45 offen offset:3072 sc1\n\t"
  "v_mfma_f32_16x16x32_bf16 v[24:27], a[220:223], a[88:91], v[24:27]\n\t"
  "v_mfma_f32_16x16x32_bf16 v[28:31], a[224:227], a[88:91], v[28:31]\n\t"
  "v_mfma_f32_16x16x32_bf16 v[32:35], a[228:231], a[88:91], v[32:35]\n\t"
  "v_mfma_f32_16x16x32_bf16 v[36:39], a[232:235], a[88:91], v[36:39]\n\t"
  "s_add_u32 s45, %[sH2ld], 106496\n\t"
  "v_mfma_f32_16x16x32_bf16 v[20:23], a[236:239], a[92:95], v[20:23]\n\t"
  "s_waitcnt vmcnt(16)\n\t"
  "v_mul_f32 v88, v88, v68\n\t"
  "v_mfma_f32_16x16x32_bf16 v[24:27], a[0:3], a[92:95], v[24:27]\n\t"
  "v_mul_f32 v89, v89, v69\n\t"
  "v_mul_f32 v90, v90, v70\n\t"
  "v_mfma_f32_16x16x32_bf16 v[28:31], a[4:7], a[92:95], v[28:31]\n\t"
  "buffer_load_dwordx4 a[136:139], v1, %[srd], s45 offen offset:0 sc1\n\t"
  "v_mul_f32 v91, v91, v71\n\t"
  "v_mfma_f32_16x16x32_bf16 v[32:35], a[8:11], a[92:95], v[32:35]\n\t"
  "ds_write_b128 v62, v[88:91] offset:0\n\t"
  "s_waitcnt vmcnt(16)\n\t"
  "v_mul_f32 v92, v92, v72\n\t"
  "v_mfma_f32_16x16x32_bf16 v[36:39], a[12:15], a[92:95], v[36:39]\n\t"
  "v_mul_f32 v93, v93, v73\n\t"
  "v_mul_f32 v94, v94, v74\n\t"
  "v_mfma_f32_16x16x32_bf16 v[20:23], a[16:19], a[96:99], v[20:23]\n\t"
  "buffer_load_dwordx4 a[140:143], v1, %[srd], s45 offen offset:1024 sc1\n\t"
  "v_mul_f32 v95, v95, v75\n\t"
  "v_mfma_f32_16x16x32_bf16 v[24:27], a[20:23], a[96:99], v[24:27]\n\t"
  "ds_write_b128 v62, v[92:95] offset:1024\n\t"
  "s_waitcnt vmcnt(16)\n\t"
  "v_mul_f32 v96, v96, v76\n\t"
  "v_mfma_f32_16x16x32_bf16 v[28:31], a[24:27], a[96:99], v[28:31]\n\t"
  "v_mul_f32 v97, v97, v77\n\t"
  "v_mul_f32 v98, v98, v78\n\t"
  "v_mfma_f32_16x16x32_bf16 v[32:35], a[28:31], a[96:99], v[32:35]\n\t"
  "buffer_load_dwordx4 a[144:147], v1, %[srd], s45 offen offset:2048 sc1\n\t"
  "v_mul_f32 v99, v99, v79\n\t"
  "v_mfma_f32_16x16x32_bf16 v[36:39], a[32:35], a[96:99], v[36:39]\n\t"
  "ds_write_b128 v62, v[96:99] offset:2048\n\t"
  "s_waitcnt vmcnt(16)\n\t"
  "v_mul_f32 v100, v100, v80\n\t"
  "v_mfma_f32_16x16x32_bf16 v[20:23], a[36:39], a[100:103], v[20:23]\n\t"
  "v_mul_f32 v101, v101, v81\n\t"
  "v_mul_f32 v102, v102, v82\n\t"
  "v_mfma_f32_16x16x32_bf16 v[24:27], a[40:43], a[100:103], v[24:27]\n\t"
  "buffer_load_dwordx4 a[148:151], v1, %[srd], s45 offen offset:3072 sc1\n\t"
  "v_mul_f32 v103, v103, v83\n\t"
  "v_mfma_f32_16x16x32_bf16 v[28:31], a[44:47], a[100:103], v[28:31]\n\t"
  "ds_write_b128 v62, v[100:103] offset:3072\n\t"
  "s_waitcnt vmcnt(16)\n\t"
  "v_mul_f32 v104, v104, v84\n\t"
  "v_mfma_f32_16x16x32_bf16 v[32:35], a[48:51], a[100:103], v[32:35]\n\t"
  "v_mul_f32 v105, v105, v85\n\t"
  "v_mul_f32 v106, v106, v86\n\t"
  "v_mfma_f32_16x16x32_bf16 v[36:39], a[52:55], a[100:103], v[36:39]\n\t"
  "s_waitcnt vmcnt(12)\n\t"
  "v_mul_f32 v107, v107, v87\n\t"
  "v_mfma_f32_16x16x32_bf16 v[40:43], v[144:147], a[104:107], 0\n\t"
  "ds_write_b128 v62, v[104:107] offset:4096\n\t"
  "v_mfma_f32_16x16x32_bf16 v[44:47], v[148:151], a[104:107], 0\n\t"
  "v_mfma_f32_16x16x32_bf16 v[48:51], v[152:155], a[104:107], 0\n\t"
  "v_mfma_f32_16x16x32_bf16 v[52:55], v[156:159], a[104:107], 0\n\t"
  "ds_write_b128 v13, v[20:23] offset:0\n\t"
  "v_mfma_f32_16x16x32_bf16 v[56:59], v[160:163], a[104:107], 0\n\t"
  "v_mfma_f32_16x16x32_bf16 v[40:43], v[164:167], a[108:111], v[40:43]\n\t"
  "v_mfma_f32_16x16x32_bf16 v[44:47], v[168:171], a[108:111], v[44:47]\n\t"
  "ds_write_b128 v13, v[24:27] offset:1024\n\t"
  "v_mfma_f32_16x16x32_bf16 v[48:51], v[172:175], a[108:111], v[48:51]\n\t"
  "v_mfma_f32_16x16x32_bf16 v[52:55], v[176:179], a[108:111], v[52:55]\n\t"
  "v_mfma_f32_16x16x32_bf16 v[56:59], v[180:183], a[108:111], v[56:59]\n\t"
  "s_waitcnt vmcnt(10)\n\t"
  "ds_write_b128 v13, v[28:31] offset:2048\n\t"
  "v_mfma_f32_16x16x32_bf16 v[40:43], v[184:187], a[112:115], v[40:43]\n\t"
  "v_mfma_f32_16x16x32_bf16 v[44:47], v[188:191], a[112:115], v[44:47]\n\t"
  "v_mfma_f32_16x16x32_bf16 v[48:51], v[192:195], a[112:115], v[48:51]\n\t"
  "ds_write_b128 v13, v[32:35] offset:3072\n\t"
  "v_mfma_f32_16x16x32_bf16 v[52:55], v[196:199], a[112:115], v[52:55]\n\t"
  "v_mfma_f32_16x16x32_bf16 v[56:59], v[200:203], a[112:115], v[56:59]\n\t"
  "v_mfma_f32_16x16x32_bf16 v[40:43], v[204:207], a[116:119], v[40:43]\n\t"
  "s_waitcnt vmcnt(9)\n\t"
  "v_cmp_ne_u64 vcc, v[132:133], v[138:139]\n\t"
  "v_mfma_f32_16x16x32_bf16 v[44:47], v[208:211], a[116:119], v[44:47]\n\t"
  "ds_write_b128 v13, v[36:39] offset:4096\n\t"
  "v_mfma_f32_16x16x32_bf16 v[48:51], v[212:215], a[116:119], v[48:51]\n\t"
  "s_cbranch_vccnz Lfl2_slow%=\n\t"
  "Lfl2_back%=:\n\t"
  "v_mfma_f32_16x16x32_bf16 v[52:55], v[216:219], a[116:119], v[52:55]\n\t"
  "s_add_u32 s44, %[sH2ld], 3145728\n\t"
  "v_mfma_f32_16x16x32_bf16 v[56:59], v[220:223], a[116:119], v[56:59]\n\t"
  "s_waitcnt vmcnt(6)\n\t"
  "v_mfma_f32_16x16x32_bf16 v[40:43], v[224:227], a[120:123], v[40:43]\n\t"
  "v_mfma_f32_16x16x32_bf16 v[44:47], v[228:231], a[120:123], v[44:47]\n\t"
  "buffer_load_dwordx4 a[152:155], v1, %[srd], s44 offen offset:0 sc1\n\t"
  "v_mfma_f32_16x16x32_bf16 v[48:51], v[232:235], a[120:123], v[48:51]\n\t"
  "v_mfma_f32_16x16x32_bf16 v[52:55], v[236:239], a[120:123], v[52:55]\n\t"
  "v_mfma_f32_16x16x32_bf16 v[56:59], v[240:243], a[120:123], v[56:59]\n\t"
  "buffer_load_dwordx4 a[156:159], v1, %[srd], s44 offen offset:1024 sc1\n\t"
  "s_add_u32 s57, %[sFL], 24576\n\t"
  "v_mfma_f32_16x16x32_bf16 v[40:43], v[244:247], a[124:127], v[40:43]\n\t"
  "buffer_load_dwordx2 v[138:139], v137, %[srd], s57 offen sc1\n\t"
  "s_waitcnt lgkmcnt(0)\n\t"
  "s_barrier\n\t"
  "v_mfma_f32_16x16x32_bf16 v[44:47], a[160:163], a[124:127], v[44:47]\n\t"
  "ds_read_b128 v[68:71], v14 offset:0\n\t"
  "ds_read_b128 v[72:75], v15 offset:0\n\t"
  "v_mfma_f32_16x16x32_bf16 v[48:51], a[164:167], a[124:127], v[48:51]\n\t"
  "buffer_load_dwordx4 v[248:251], v1, %[srd], s44 offen offset:2048 sc1\n\t"
  "ds_read_b128 v[76:79], v14 offset:20480\n\t"
  "v_mfma_f32_16x16x32_bf16 v[52:55], a[168:171], a[124:127], v[52:55]\n\t"
  "ds_read_b128 v[80:83], v15 offset:20480\n\t"
  "ds_read_b128 v[84:87], v14 offset:40960\n\t"
  "v_mfma_f32_16x16x32_bf16 v[56:59], a[172:175], a[124:127], v[56:59]\n\t"
  "s_waitcnt vmcnt(8)\n\t"
  "ds_read_b128 v[88:91], v15 offset:40960\n\t"
  "v_mfma_f32_16x16x32_bf16 v[40:43], a[176:179], a[128:131], v[40:43]\n\t"
  "buffer_load_dwordx4 v[252:255], v1, %[srd], s44 offen offset:3072 sc1\n\t"
  "ds_read_b128 v[92:95], v60 offset:0\n\t"
  "v_mfma_f32_16x16x32_bf16 v[44:47], a[180:183], a[128:131], v[44:47]\n\t"
  "ds_read_b128 v[96:99], v61 offset:0\n\t"
  "ds_read_b128 v[100:103], v60 offset:20480\n\t"
  "v_mfma_f32_16x16x32_bf16 v[48:51], a[184:187], a[128:131], v[48:51]\n\t"
  "ds_read_b128 v[104:107], v61 offset:20480\n\t"
  "v_mfma_f32_16x16x32_bf16 v[52:55], a[188:191], a[128:131], v[52:55]\n\t"
  "v_mfma_f32_16x16x32_bf16 v[56:59], a[192:195], a[128:131], v[56:59]\n\t"
  "s_add_u32 s44, %[sH2ld], 3149824\n\t"
  "v_mfma_f32_16x16x32_bf16 v[40:43], a[196:199], a[132:135], v[40:43]\n\t"
  "v_mfma_f32_16x16x32_bf16 v[44:47], a[200:203], a[132:135], v[44:47]\n\t"
  "v_mfma_f32_16x16x32_bf16 v[48:51], a[204:207], a[132:135], v[48:51]\n\t"
  "buffer_load_dwordx4 v[112:115], v1, %[srd], s44 offen offset:0 sc1\n\t"
  "v_mfma_f32_16x16x32_bf16 v[52:55], a[208:211], a[132:135], v[52:55]\n\t"
  "v_mfma_f32_16x16x32_bf16 v[56:59], a[212:215], a[132:135], v[56:59]\n\t"
  "s_waitcnt vmcnt(8)\n\t"
  "v_mfma_f32_16x16x32_bf16 v[40:43], a[216:219], a[136:139], v[40:43]\n\t"
  "buffer_load_dwordx4 v[116:119], v1, %[srd], s44 offen offset:1024 sc1\n\t"
  "s_waitcnt lgkmcnt(0)\n\t"
  "v_mfma_f32_16x16x32_bf16 v[44:47], a[220:223], a[136:139], v[44:47]\n\t"
  "v_add_f32 v68, v68, v76\n\t"
  "v_add_f32 v69, v69, v77\n\t"
  "v_mfma_f32_16x16x32_bf16 v[48:51], a[224:227], a[136:139], v[48:51]\n\t"
  "v_add_f32 v70, v70, v78\n\t"
  "v_add_f32 v71, v71, v79\n\t"
  "v_mfma_f32_16x16x32_bf16 v[52:55], a[228:231], a[136:139], v[52:55]\n\t"
  "buffer_load_dwordx4 v[120:123], v1, %[srd], s44 offen offset:2048 sc1\n\t"
  "v_add_f32 v72, v72, v80\n\t"
  "v_mfma_f32_16x16x32_bf16 v[56:59], a[232:235], a[136:139], v[56:59]\n\t"
  "v_add_f32 v73, v73, v81\n\t"
  "v_add_f32 v74, v74, v82\n\t"
  "v_mfma_f32_16x16x32_bf16 v[40:43], a[236:239], a[140:143], v[40:43]\n\t"
  "v_add_f32 v75, v75, v83\n\t"
  "v_add_f32 v84, v84, v92\n\t"
  "v_mfma_f32_16x16x32_bf16 v[44:47], a[0:3], a[140:143], v[44:47]\n\t"
  "buffer_load_dwordx4 v[124:127], v1, %[srd], s44 offen offset:3072 sc1\n\t"
  "v_add_f32 v85, v85, v93\n\t"
  "v_mfma_f32_16x16x32_bf16 v[48:51], a[4:7], a[140:143], v[48:51]\n\t"
  "v_add_f32 v86, v86, v94\n\t"
  "v_add_f32 v87, v87, v95\n\t"
  "v_mfma_f32_16x16x32_bf16 v[52:55], a[8:11], a[140:143], v[52:55]\n\t"
  "v_add_f32 v88, v88, v96\n\t"
  "v_add_f32 v89, v89, v97\n\t"
  "v_mfma_f32_16x16x32_bf16 v[56:59], a[12:15], a[140:143], v[56:59]\n\t"
  "s_waitcnt vmcnt(9)\n\t"
  "v_add_f32 v90, v90, v98\n\t"
  "v_mfma_f32_16x16x32_bf16 v[40:43], a[16:19], a[144:147], v[40:43]\n\t"
  "s_add_u32 s44, %[sH2ld], 3153920\n\t"
  "v_add_f32 v91, v91, v99\n\t"
  "v_mfma_f32_16x16x32_bf16 v[44:47], a[20:23], a[144:147], v[44:47]\n\t"
  "v_add_f32 v68, v68, v84\n\t"
  "v_add_f32 v69, v69, v85\n\t"
  "v_mfma_f32_16x16x32_bf16 v[48:51], a[24:27], a[144:147], v[48:51]\n\t"
  "v_add_f32 v70, v70, v86\n\t"
  "v_add_f32 v71, v71, v87\n\t"
  "v_mfma_f32_16x16x32_bf16 v[52:55], a[28:31], a[144:147], v[52:55]\n\t"
  "buffer_load_dwordx4 v[128:131], v1, %[srd], s44 offen offset:0 sc1\n\t"
  "v_add_f32 v72, v72, v88\n\t"
  "v_mfma_f32_16x16x32_bf16 v[56:59], a[32:35], a[144:147], v[56:59]\n\t"
  "v_add_f32 v73, v73, v89\n\t"
  "v_add_f32 v74, v74, v90\n\t"
  "v_mfma_f32_16x16x32_bf16 v[40:43], a[36:39], a[148:151], v[40:43]\n\t"
  "v_add_f32 v75, v75, v91\n\t"
  "v_fma_f32 v68, v68, v64, v100\n\t"
  "v_mfma_f32_16x16x32_bf16 v[44:47], a[40:43], a[148:151], v[44:47]\n\t"
  "buffer_load_dwordx4 a[240:243], v1, %[srd], s44 offen offset:1024 sc1\n\t"
  "v_fma_f32 v69, v69, v65, v101\n\t"
  "v_mfma_f32_16x16x32_bf16 v[48:51], a[44:47], a[148:151], v[48:51]\n\t"
  "v_fma_f32 v70, v70, v66, v102\n\t"
  "v_fma_f32 v71, v71, v67, v103\n\t"
  "v_mfma_f32_16x16x32_bf16 v[52:55], a[48:51], a[148:151], v[52:55]\n\t"
  "v_fma_f32 v72, v72, v108, v104\n\t"
  "v_fma_f32 v73, v73, v109, v105\n\t"
  "v_mfma_f32_16x16x32_bf16 v[56:59], a[52:55], a[148:151], v[56:59]\n\t"
  "s_waitcnt vmcnt(9)\n\t"
  "buffer_load_dwordx4 a[56:59], v1, %[srd], s44 offen offset:2048 sc1\n\t"
  "v_mfma_f32_16x16x32_bf16 v[20:23], v[144:147], a[152:155], 0\n\t"
  "v_fma_f32 v74, v74, v110, v106\n\t"
  "v_fma_f32 v75, v75, v111, v107\n\t"
  "v_mfma_f32_16x16x32_bf16 v[24:27], v[148:151], a[152:155], 0\n\t"
  "v_cvt_pk_bf16_f32 v76, v68, v69\n\t"
  "v_cvt_pk_bf16_f32 v77, v70, v71\n\t"
  "v_mfma_f32_16x16x32_bf16 v[28:31], v[152:155], a[152:155], 0\n\t"
  "buffer_load_dwordx4 a[60:63], v1, %[srd], s44 offen offset:3072 sc1\n\t"
  "v_cvt_pk_bf16_f32 v78, v72, v73\n\t"
  "v_mfma_f32_16x16x32_bf16 v[32:35], v[156:159], a[152:155], 0\n\t"
  "v_cvt_pk_bf16_f32 v79, v74, v75\n\t"
  "s_mov_b32 s55, 0\n\t"
  "v_mfma_f32_16x16x32_bf16 v[36:39], v[160:163], a[152:155], 0\n\t"
  "buffer_store_dwordx4 v[76:79], v16, %[srdo], s55 offen\n\t"
  "ds_write_b128 v13, v[40:43] offset:5120\n\t"
  "v_mfma_f32_16x16x32_bf16 v[20:23], v[164:167], a[156:159], v[20:23]\n\t"
  "v_mfma_f32_16x16x32_bf16 v[24:27], v[168:171], a[156:159], v[24:27]\n\t"
  "v_mfma_f32_16x16x32_bf16 v[28:31], v[172:175], a[156:159], v[28:31]\n\t"
  "ds_write_b128 v13, v[44:47] offset:6144\n\t"
  "v_mfma_f32_16x16x32_bf16 v[32:35], v[176:179], a[156:159], v[32:35]\n\t"
  "v_mfma_f32_16x16x32_bf16 v[36:39], v[180:183], a[156:159], v[36:39]\n\t"
  "s_waitcnt vmcnt(9)\n\t"
  "v_mfma_f32_16x16x32_bf16 v[20:23], v[184:187], v[248:251], v[20:23]\n\t"
  "ds_write_b128 v13, v[48:51] offset:7168\n\t"
  "v_mfma_f32_16x16x32_bf16 v[24:27], v[188:191], v[248:251], v[24:27]\n\t"
  "v_mfma_f32_16x16x32_bf16 v[28:31], v[192:195], v[248:251], v[28:31]\n\t"
  "v_mfma_f32_16x16x32_bf16 v[32:35], v[196:199], v[248:251], v[32:35]\n\t"
  "ds_write_b128 v13, v[52:55] offset:8192\n\t"
  "v_mfma_f32_16x16x32_bf16 v[36:39], v[200:203], v[248:251], v[36:39]\n\t"
  "v_mfma_f32_16x16x32_bf16 v[20:23], v[204:207], v[252:255], v[20:23]\n\t"
  "v_cmp_ne_u64 vcc, v[132:133], v[138:139]\n\t"
  "v_mfma_f32_16x16x32_bf16 v[24:27], v[208:211], v[252:255], v[24:27]\n\t"
  "ds_write_b128 v13, v[56:59] offset:9216\n\t"
  "v_mfma_f32_16x16x32_bf16 v[28:31], v[212:215], v[252:255], v[28:31]\n\t"
  "s_cbranch_vccnz Lfl3_slow%=\n\t"
  "Lfl3_back%=:\n\t"
  "v_mfma_f32_16x16x32_bf16 v[32:35], v[216:219], v[252:255], v[32:35]\n\t"
  "s_add_u32 s45, %[sH2ld], 3244032\n\t"
  "v_mfma_f32_16x16x32_bf16 v[36:39], v[220:223], v[252:255], v[36:39]\n\t"
  "s_waitcnt vmcnt(7)\n\t"
  "v_mfma_f32_16x16x32_bf16 v[20:23], v[224:227], v[112:115], v[20:23]\n\t"
  "buffer_load_dwordx4 a[104:107], v1, %[srd], s45 offen offset:0 sc1\n\t"
  "v_mfma_f32_16x16x32_bf16 v[24:27], v[228:231], v[112:115], v[24:27]\n\t"
  "v_mfma_f32_16x16x32_bf16 v[28:31], v[232:235], v[112:115], v[28:31]\n\t"
  "buffer_load_dwordx4 a[108:111], v1, %[srd], s45 offen offset:1024 sc1\n\t"
  "v_mfma_f32_16x16x32_bf16 v[32:35], v[236:239], v[112:115], v[32:35]\n\t"
  "v_mfma_f32_16x16x32_bf16 v[36:39], v[240:243], v[112:115], v[36:39]\n\t"
  "s_waitcnt lgkmcnt(0)\n\t"
  "s_barrier\n\t"
  "v_mfma_f32_16x16x32_bf16 v[20:23], v[244:247], v[116:119], v[20:23]\n\t"
  "buffer_load_dwordx4 a[64:67], v1, %[srd], s45 offen offset:2048 sc1\n\t"
  "ds_read_b128 v[68:71], v14 offset:5120\n\t"
  "v_mfma_f32_16x16x32_bf16 v[24:27], a[160:163], v[116:119], v[24:27]\n\t"
  "ds_read_b128 v[72:75], v15 offset:5120\n\t"
  "ds_read_b128 v[76:79], v14 offset:25600\n\t"
  "v_mfma_f32_16x16x32_bf16 v[28:31], a[164:167], v[116:119], v[28:31]\n\t"
  "buffer_load_dwordx4 a[68:71], v1, %[srd], s45 offen offset:3072 sc1\n\t"
  "ds_read_b128 v[80:83], v15 offset:25600\n\t"
  "v_mfma_f32_16x16x32_bf16 v[32:35], a[168:171], v[116:119], v[32:35]\n\t"
  "ds_read_b128 v[84:87], v14 offset:46080\n\t"
  "ds_read_b128 v[88:91], v15 offset:46080\n\t"
  "v_mfma_f32_16x16x32_bf16 v[36:39], a[172:175], v[116:119], v[36:39]\n\t"
  "s_waitcnt vmcnt(9)\n\t"
  "ds_read_b128 v[92:95], v60 offset:5120\n\t"
  "v_mfma_f32_16x16x32_bf16 v[20:23], a[176:179], v[120:123], v[20:23]\n\t"
  "s_add_u32 s45, %[sH2ld], 3248128\n\t"
  "ds_read_b128 v[96:99], v61 offset:5120\n\t"
  "v_mfma_f32_16x16x32_bf16 v[24:27], a[180:183], v[120:123], v[24:27]\n\t"
  "ds_read_b128 v[100:103], v60 offset:25600\n\t"
  "ds_read_b128 v[104:107], v61 offset:25600\n\t"
  "v_mfma_f32_16x16x32_bf16 v[28:31], a[184:187], v[120:123], v[28:31]\n\t"
  "buffer_load_dwordx4 a[72:75], v1, %[srd], s45 offen offset:0 sc1\n\t"
  "v_mfma_f32_16x16x32_bf16 v[32:35], a[188:191], v[120:123], v[32:35]\n\t"
  "v_mfma_f32_16x16x32_bf16 v[36:39], a[192:195], v[120:123], v[36:39]\n\t"
  "buffer_load_dwordx4 a[76:79], v1, %[srd], s45 offen offset:1024 sc1\n\t"
  "v_mfma_f32_16x16x32_bf16 v[20:23], a[196:199], v[124:127], v[20:23]\n\t"
  "v_mfma_f32_16x16x32_bf16 v[24:27], a[200:203], v[124:127], v[24:27]\n\t"
  "v_mfma_f32_16x16x32_bf16 v[28:31], a[204:207], v[124:127], v[28:31]\n\t"
  "buffer_load_dwordx4 a[80:83], v1, %[srd], s45 offen offset:2048 sc1\n\t"
  "v_mfma_f32_16x16x32_bf16 v[32:35], a[208:211], v[124:127], v[32:35]\n\t"
  "v_mfma_f32_16x16x32_bf16 v[36:39], a[212:215], v[124:127], v[36:39]\n\t"
  "s_waitcnt vmcnt(10)\n\t"
  "buffer_load_dwordx4 a[84:87], v1, %[srd], s45 offen offset:3072 sc1\n\t"
  "v_mfma_f32_16x16x32_bf16 v[20:23], a[216:219], v[128:131], v[20:23]\n\t"
  "s_waitcnt lgkmcnt(0)\n\t"
  "v_add_f32 v68, v68, v76\n\t"
  "v_mfma_f32_16x16x32_bf16 v[24:27], a[220:223], v[128:131], v[24:27]\n\t"
  "v_add_f32 v69, v69, v77\n\t"
  "v_add_f32 v70, v70, v78\n\t"
  "v_mfma_f32_16x16x32_bf16 v[28:31], a[224:227], v[128:131], v[28:31]\n\t"
  "s_add_u32 s45, %[sH2ld], 3252224\n\t"
  "v_add_f32 v71, v71, v79\n\t"
  "v_mfma_f32_16x16x32_bf16 v[32:35], a[228:231], v[128:131], v[32:35]\n\t"
  "v_add_f32 v72, v72, v80\n\t"
  "v_add_f32 v73, v73, v81\n\t"
  "v_mfma_f32_16x16x32_bf16 v[36:39], a[232:235], v[128:131], v[36:39]\n\t"
  "buffer_load_dwordx4 a[88:91], v1, %[srd], s45 offen offset:0 sc1\n\t"
  "v_add_f32 v74, v74, v82\n\t"
  "v_mfma_f32_16x16x32_bf16 v[20:23], a[236:239], a[240:243], v[20:23]\n\t"
  "v_add_f32 v75, v75, v83\n\t"
  "v_add_f32 v84, v84, v92\n\t"
  "v_mfma_f32_16x16x32_bf16 v[24:27], a[0:3], a[240:243], v[24:27]\n\t"
  "buffer_load_dwordx4 a[92:95], v1, %[srd], s45 offen offset:1024 sc1\n\t"
  "v_add_f32 v85, v85, v93\n\t"
  "v_mfma_f32_16x16x32_bf16 v[28:31], a[4:7], a[240:243], v[28:31]\n\t"
  "v_add_f32 v86, v86, v94\n\t"
  "v_add_f32 v87, v87, v95\n\t"
  "v_mfma_f32_16x16x32_bf16 v[32:35], a[8:11], a[240:243], v[32:35]\n\t"
  "v_add_f32 v88, v88, v96\n\t"
  "v_add_f32 v89, v89, v97\n\t"
  "v_mfma_f32_16x16x32_bf16 v[36:39], a[12:15], a[240:243], v[36:39]\n\t"
  "s_waitcnt vmcnt(11)\n\t"
  "buffer_load_dwordx4 a[96:99], v1, %[srd], s45 offen offset:2048 sc1\n\t"
  "v_mfma_f32_16x16x32_bf16 v[20:23], a[16:19], a[56:59], v[20:23]\n\t"
  "v_add_f32 v90, v90, v98\n\t"
  "v_add_f32 v91, v91, v99\n\t"
  "v_mfma_f32_16x16x32_bf16 v[24:27], a[20:23], a[56:59], v[24:27]\n\t"
  "buffer_load_dwordx4 a[100:103], v1, %[srd], s45 offen offset:3072 sc1\n\t"
  "v_add_f32 v68, v68, v84\n\t"
  "v_mfma_f32_16x16x32_bf16 v[28:31], a[24:27], a[56:59], v[28:31]\n\t"
  "v_add_f32 v69, v69, v85\n\t"
  "v_add_f32 v70, v70, v86\n\t"
  "v_mfma_f32_16x16x32_bf16 v[32:35], a[28:31], a[56:59], v[32:35]\n\t"
  "v_add_f32 v71, v71, v87\n\t"
  "v_add_f32 v72, v72, v88\n\t"
  "v_mfma_f32_16x16x32_bf16 v[36:39], a[32:35], a[56:59], v[36:39]\n\t"
  "v_add_f32 v73, v73, v89\n\t"
  "v_add_f32 v74, v74, v90\n\t"
  "v_mfma_f32_16x16x32_bf16 v[20:23], a[36:39], a[60:63], v[20:23]\n\t"
  "v_add_f32 v75, v75, v91\n\t"
  "v_fma_f32 v68, v68, v64, v100\n\t"
  "v_mfma_f32_16x16x32_bf16 v[24:27], a[40:43], a[60:63], v[24:27]\n\t"
  "v_fma_f32 v69, v69, v65, v101\n\t"
  "v_fma_f32 v70, v70, v66, v102\n\t"
  "v_mfma_f32_16x16x32_bf16 v[28:31], a[44:47], a[60:63], v[28:31]\n\t"
  "v_fma_f32 v71, v71, v67, v103\n\t"
  "v_fma_f32 v72, v72, v108, v104\n\t"
  "v_mfma_f32_16x16x32_bf16 v[32:35], a[48:51], a[60:63], v[32:35]\n\t"
  "v_fma_f32 v73, v73, v109, v105\n\t"
  "v_fma_f32 v74, v74, v110, v106\n\t"
  "v_mfma_f32_16x16x32_bf16 v[36:39], a[52:55], a[60:63], v[36:39]\n\t"
  "s_waitcnt vmcnt(10)\n\t"
  "v_fma_f32 v75, v75, v111, v107\n\t"
  "v_mfma_f32_16x16x32_bf16 v[40:43], v[144:147], a[104:107], 0\n\t"
  "v_cvt_pk_bf16_f32 v76, v68, v69\n\t"
  "v_cvt_pk_bf16_f32 v77, v70, v71\n\t"
  "v_mfma_f32_16x16x32_bf16 v[44:47], v[148:151], a[104:107], 0\n\t"
  "v_cvt_pk_bf16_f32 v78, v72, v73\n\t"
  "v_cvt_pk_bf16_f32 v79, v74, v75\n\t"
  "v_mfma_f32_16x16x32_bf16 v[48:51], v[152:155], a[104:107], 0\n\t"
  "s_mov_b32 s55, 40960\n\t"
  "buffer_store_dwordx4 v[76:79], v16, %[srdo], s55 offen\n\t"
  "v_mfma_f32_16x16x32_bf16 v[52:55], v[156:159], a[104:107], 0\n\t"
  "ds_write_b128 v13, v[20:23] offset:10240\n\t"
  "v_mfma_f32_16x16x32_bf16 v[56:59], v[160:163], a[104:107], 0\n\t"
  "v_mfma_f32_16x16x32_bf16 v[40:43], v[164:167], a[108:111], v[40:43]\n\t"
  "v_mfma_f32_16x16x32_bf16 v[44:47], v[168:171], a[108:111], v[44:47]\n\t"
  "ds_write_b128 v13, v[24:27] offset:11264\n\t"
  "v_mfma_f32_16x16x32_bf16 v[48:51], v[172:175], a[108:111], v[48:51]\n\t"
  "v_mfma_f32_16x16x32_bf16 v[52:55], v[176:179], a[108:111], v[52:55]\n\t"
  "v_mfma_f32_16x16x32_bf16 v[56:59], v[180:183], a[108:111], v[56:59]\n\t"
  "s_waitcnt vmcnt(9)\n\t"
  "ds_write_b128 v13, v[28:31] offset:12288\n\t"
  "v_mfma_f32_16x16x32_bf16 v[40:43], v[184:187], a[64:67], v[40:43]\n\t"
  "v_mfma_f32_16x16x32_bf16 v[44:47], v[188:191], a[64:67], v[44:47]\n\t"
  "v_mfma_f32_16x16x32_bf16 v[48:51], v[192:195], a[64:67], v[48:51]\n\t"
  "ds_write_b128 v13, v[32:35] offset:13312\n\t"
  "v_mfma_f32_16x16x32_bf16 v[52:55], v[196:199], a[64:67], v[52:55]\n\t"
  "v_mfma_f32_16x16x32_bf16 v[56:59], v[200:203], a[64:67], v[56:59]\n\t"
  "v_mfma_f32_16x16x32_bf16 v[40:43], v[204:207], a[68:71], v[40:43]\n\t"
  "ds_write_b128 v13, v[36:39] offset:14336\n\t"
  "v_mfma_f32_16x16x32_bf16 v[44:47], v[208:211], a[68:71], v[44:47]\n\t"
  "v_mfma_f32_16x16x32_bf16 v[48:51], v[212:215], a[68:71], v[48:51]\n\t"
  "v_mfma_f32_16x16x32_bf16 v[52:55], v[216:219], a[68:71], v[52:55]\n\t"
  "v_mfma_f32_16x16x32_bf16 v[56:59], v[220:223], a[68:71], v[56:59]\n\t"
  "s_waitcnt vmcnt(7)\n\t"
  "v_mfma_f32_16x16x32_bf16 v[40:43], v[224:227], a[72:75], v[40:43]\n\t"
  "v_mfma_f32_16x16x32_bf16 v[44:47], v[228:231], a[72:75], v[44:47]\n\t"
  "v_mfma_f32_16x16x32_bf16 v[48:51], v[232:235], a[72:75], v[48:51]\n\t"
  "v_mfma_f32_16x16x32_bf16 v[52:55], v[236:239], a[72:75], v[52:55]\n\t"
  "s_waitcnt lgkmcnt(0)\n\t"
  "s_barrier\n\t"
  "v_mfma_f32_16x16x32_bf16 v[56:59], v[240:243], a[72:75], v[56:59]\n\t"
  "ds_read_b128 v[68:71], v14 offset:10240\n\t"
  "ds_read_b128 v[72:75], v15 offset:10240\n\t"
  "v_mfma_f32_16x16x32_bf16 v[40:43], v[244:247], a[76:79], v[40:43]\n\t"
  "ds_read_b128 v[76:79], v14 offset:30720\n\t"
  "ds_read_b128 v[80:83], v15 offset:30720\n\t"
  "v_mfma_f32_16x16x32_bf16 v[44:47], a[160:163], a[76:79], v[44:47]\n\t"
  "ds_read_b128 v[84:87], v14 offset:51200\n\t"
  "ds_read_b128 v[88:91], v15 offset:51200\n\t"
  "v_mfma_f32_16x16x32_bf16 v[48:51], a[164:167], a[76:79], v[48:51]\n\t"
  "ds_read_b128 v[92:95], v60 offset:10240\n\t"
  "ds_read_b128 v[96:99], v61 offset:10240\n\t"
  "v_mfma_f32_16x16x32_bf16 v[52:55], a[168:171], a[76:79], v[52:55]\n\t"
  "ds_read_b128 v[100:103], v60 offset:30720\n\t"
  "ds_read_b128 v[104:107], v61 offset:30720\n\t"
  "v_mfma_f32_16x16x32_bf16 v[56:59], a[172:175], a[76:79], v[56:59]\n\t"
  "s_waitcnt vmcnt(5)\n\t"
  "v_mfma_f32_16x16x32_bf16 v[40:43], a[176:179], a[80:83], v[40:43]\n\t"
  "v_mfma_f32_16x16x32_bf16 v[44:47], a[180:183], a[80:83], v[44:47]\n\t"
  "v_mfma_f32_16x16x32_bf16 v[48:51], a[184:187], a[80:83], v[48:51]\n\t"
  "v_mfma_f32_16x16x32_bf16 v[52:55], a[188:191], a[80:83], v[52:55]\n\t"
  "v_mfma_f32_16x16x32_bf16 v[56:59], a[192:195], a[80:83], v[56:59]\n\t"
  "v_mfma_f32_16x16x32_bf16 v[40:43], a[196:199], a[84:87], v[40:43]\n\t"
  "v_mfma_f32_16x16x32_bf16 v[44:47], a[200:203], a[84:87], v[44:47]\n\t"
  "s_waitcnt lgkmcnt(0)\n\t"
  "v_add_f32 v68, v68, v76\n\t"
  "v_mfma_f32_16x16x32_bf16 v[48:51], a[204:207], a[84:87], v[48:51]\n\t"
  "v_add_f32 v69, v69, v77\n\t"
  "v_add_f32 v70, v70, v78\n\t"
  "v_mfma_f32_16x16x32_bf16 v[52:55], a[208:211], a[84:87], v[52:55]\n\t"
  "v_add_f32 v71, v71, v79\n\t"
  "v_add_f32 v72, v72, v80\n\t"
  "v_mfma_f32_16x16x32_bf16 v[56:59], a[212:215], a[84:87], v[56:59]\n\t"
  "s_waitcnt vmcnt(3)\n\t"
  "v_add_f32 v73, v73, v81\n\t"
  "v_mfma_f32_16x16x32_bf16 v[40:43], a[216:219], a[88:91], v[40:43]\n\t"
  "v_add_f32 v74, v74, v82\n\t"
  "v_add_f32 v75, v75, v83\n\t"
  "v_mfma_f32_16x16x32_bf16 v[40:43], a[236:239], a[92:95], v[40:43]\n\t"
  "v_add_f32 v84, v84, v92\n\t"
  "v_add_f32 v85, v85, v93\n\t"
  "s_waitcnt vmcnt(2)\n\t"
  "v_mfma_f32_16x16x32_bf16 v[40:43], a[16:19], a[96:99], v[40:43]\n\t"
  "v_add_f32 v86, v86, v94\n\t"
  "v_add_f32 v87, v87, v95\n\t"
  "s_waitcnt vmcnt(1)\n\t"
  "v_mfma_f32_16x16x32_bf16 v[40:43], a[36:39], a[100:103], v[40:43]\n\t"
  "v_add_f32 v88, v88, v96\n\t"
  "v_add_f32 v89, v89, v97\n\t"
  "v_mfma_f32_16x16x32_bf16 v[44:47], a[220:223], a[88:91], v[44:47]\n\t"
  "v_add_f32 v90, v90, v98\n\t"
  "v_add_f32 v91, v91, v99\n\t"
  "v_mfma_f32_16x16x32_bf16 v[44:47], a[0:3], a[92:95], v[44:47]\n\t"
  "v_add_f32 v68, v68, v84\n\t"
  "v_add_f32 v69, v69, v85\n\t"
  "v_mfma_f32_16x16x32_bf16 v[44:47], a[20:23], a[96:99], v[44:47]\n\t"
  "v_add_f32 v70, v70, v86\n\t"
  "v_add_f32 v71, v71, v87\n\t"
  "v_mfma_f32_16x16x32_bf16 v[44:47], a[40:43], a[100:103], v[44:47]\n\t"
  "v_add_f32 v72, v72, v88\n\t"
  "v_add_f32 v73, v73, v89\n\t"
  "v_mfma_f32_16x16x32_bf16 v[48:51], a[224:227], a[88:91], v[48:51]\n\t"
  "v_add_f32 v74, v74, v90\n\t"
  "v_add_f32 v75, v75, v91\n\t"
  "v_mfma_f32_16x16x32_bf16 v[48:51], a[4:7], a[92:95], v[48:51]\n\t"
  "ds_write_b128 v13, v[40:43] offset:15360\n\t"
  "v_fma_f32 v68, v68, v64, v100\n\t"
  "v_mfma_f32_16x16x32_bf16 v[48:51], a[24:27], a[96:99], v[48:51]\n\t"
  "v_fma_f32 v69, v69, v65, v101\n\t"
  "v_fma_f32 v70, v70, v66, v102\n\t"
  "v_mfma_f32_16x16x32_bf16 v[48:51], a[44:47], a[100:103], v[48:51]\n\t"
  "v_fma_f32 v71, v71, v67, v103\n\t"
  "v_fma_f32 v72, v72, v108, v104\n\t"
  "v_mfma_f32_16x16x32_bf16 v[52:55], a[228:231], a[88:91], v[52:55]\n\t"
  "v_fma_f32 v73, v73, v109, v105\n\t"
  "v_fma_f32 v74, v74, v110, v106\n\t"
  "v_mfma_f32_16x16x32_bf16 v[52:55], a[8:11], a[92:95], v[52:55]\n\t"
  "ds_write_b128 v13, v[44:47] offset:16384\n\t"
  "v_fma_f32 v75, v75, v111, v107\n\t"
  "v_mfma_f32_16x16x32_bf16 v[52:55], a[28:31], a[96:99], v[52:55]\n\t"
  "v_cvt_pk_bf16_f32 v76, v68, v69\n\t"
  "v_cvt_pk_bf16_f32 v77, v70, v71\n\t"
  "v_mfma_f32_16x16x32_bf16 v[52:55], a[48:51], a[100:103], v[52:55]\n\t"
  "v_cvt_pk_bf16_f32 v78, v72, v73\n\t"
  "v_cvt_pk_bf16_f32 v79, v74, v75\n\t"
  "v_mfma_f32_16x16x32_bf16 v[56:59], a[232:235], a[88:91], v[56:59]\n\t"
  "s_mov_b32 s55, 1310720\n\t"
  "buffer_store_dwordx4 v[76:79], v16, %[srdo], s55 offen\n\t"
  "v_mfma_f32_16x16x32_bf16 v[56:59], a[12:15], a[92:95], v[56:59]\n\t"
  "ds_write_b128 v13, v[48:51] offset:17408\n\t"
  "v_mfma_f32_16x16x32_bf16 v[56:59], a[32:35], a[96:99], v[56:59]\n\t"
  "v_mfma_f32_16x16x32_bf16 v[56:59], a[52:55], a[100:103], v[56:59]\n\t"
  "ds_write_b128 v13, v[52:55] offset:18432\n\t"
  "s_nop 6\n\t"
  "ds_write_b128 v13, v[56:59] offset:19456\n\t"
  "buffer_store_dwordx2 v[132:133], v18, %[srd], 0 offen sc1\n\t"
  "s_waitcnt lgkmcnt(0)\n\t"
  "s_barrier\n\t"
  "ds_read_b128 v[68:71], v14 offset:15360\n\t"
  "ds_read_b128 v[72:75], v15 offset:15360\n\t"
  "ds_read_b128 v[76:79], v14 offset:35840\n\t"
  "ds_read_b128 v[80:83], v15 offset:35840\n\t"
  "ds_read_b128 v[84:87], v14 offset:56320\n\t"
  "ds_read_b128 v[88:91], v15 offset:56320\n\t"
  "ds_read_b128 v[92:95], v60 offset:15360\n\t"
  "ds_read_b128 v[96:99], v61 offset:15360\n\t"
  "ds_read_b128 v[100:103], v60 offset:35840\n\t"
  "ds_read_b128 v[104:107], v61 offset:35840\n\t"
  "s_waitcnt lgkmcnt(7)\n\t"
  "v_pk_add_f32 v[68:69], v[68:69], v[76:77]\n\t"
  "v_pk_add_f32 v[70:71], v[70:71], v[78:79]\n\t"
  "s_waitcnt lgkmcnt(6)\n\t"
  "v_pk_add_f32 v[72:73], v[72:73], v[80:81]\n\t"
  "v_pk_add_f32 v[74:75], v[74:75], v[82:83]\n\t"
  "s_waitcnt lgkmcnt(3)\n\t"
  "v_pk_add_f32 v[84:85], v[84:85], v[92:93]\n\t"
  "v_pk_add_f32 v[86:87], v[86:87], v[94:95]\n\t"
  "s_waitcnt lgkmcnt(2)\n\t"
  "v_pk_add_f32 v[88:89], v[88:89], v[96:97]\n\t"
  "v_pk_add_f32 v[90:91], v[90:91], v[98:99]\n\t"
  "v_pk_add_f32 v[68:69], v[68:69], v[84:85]\n\t"
  "v_pk_add_f32 v[70:71], v[70:71], v[86:87]\n\t"
  "v_pk_add_f32 v[72:73], v[72:73], v[88:89]\n\t"
  "v_pk_add_f32 v[74:75], v[74:75], v[90:91]\n\t"
  "s_waitcnt lgkmcnt(1)\n\t"
  "v_pk_fma_f32 v[68:69], v[68:69], v[64:65], v[100:101]\n\t"
  "v_pk_fma_f32 v[70:71], v[70:71], v[66:67], v[102:103]\n\t"
  "s_waitcnt lgkmcnt(0)\n\t"
  "v_pk_fma_f32 v[72:73], v[72:73], v[108:109], v[104:105]\n\t"
  "v_pk_fma_f32 v[74:75], v[74:75], v[110:111], v[106:107]\n\t"
  "v_cvt_pk_bf16_f32 v76, v68, v69\n\t"
  "v_cvt_pk_bf16_f32 v77, v70, v71\n\t"
  "v_cvt_pk_bf16_f32 v78, v72, v73\n\t"
  "v_cvt_pk_bf16_f32 v79, v74, v75\n\t"
  "s_mov_b32 s55, 1351680\n\t"
  "buffer_store_dwordx4 v[76:79], v16, %[srdo], s55 offen\n\t"
  "s_branch Lfend%=\n\t"
  "Lst0_slow%=:\n\t"
  "s_nop 7\n\t"
  "s_nop 3\n\t"
  "s_mov_b32 s49, 0x40000\n\t"
  "Lst0_loop%=:\n\t"
  "s_sleep 2\n\t"
  "buffer_load_dwordx2 v[92:93], v136, %[srd], %[sST] offen offset:0 sc1\n\t"
  "buffer_load_dwordx2 v[94:95], v136, %[srd], %[sST] offen offset:1024 sc1\n\t"
  "buffer_load_dwordx2 v[96:97], v136, %[srd], %[sST] offen offset:2048 sc1\n\t"
  "buffer_load_dwordx2 v[98:99], v136, %[srd], %[sST] offen offset:3072 sc1\n\t"
  "s_waitcnt vmcnt(0)\n\t"
  "v_min_u32 v131, v93, v95\n\t"
  "v_min3_u32 v131, v131, v97, v99\n\t"
  "v_cmp_eq_u32 vcc, 0, v131\n\t"
  "s_nop 3\n\t"
  "s_cmp_eq_u64 vcc, 0\n\t"
  "s_cbranch_scc1 Lst0_back%=\n\t"
  "s_sub_u32 s49, s49, 1\n\t"
  "s_cmp_lg_u32 s49, 0\n\t"
  "s_cbranch_scc1 Lst0_loop%=\n\t"
  "s_branch Lst0_back%=\n\t"
  "Lw2_slow%=:\n\t"
  "s_nop 7\n\t"
  "s_nop 3\n\t"
  "s_mov_b32 s49, 0x40000\n\t"
  "Lw2_loop%=:\n\t"
  "s_sleep 2\n\t"
  "buffer_load_dwordx2 v[138:139], v2, %[srd], 0 offen sc1\n\t"
  "s_waitcnt vmcnt(0)\n\t"
  "v_cmp_ne_u64 vcc, v[132:133], v[138:139]\n\t"
  "s_nop 3\n\t"
  "s_cmp_eq_u64 vcc, 0\n\t"
  "s_cbranch_scc1 Lw2_back%=\n\t"
  "s_sub_u32 s49, s49, 1\n\t"
  "s_cmp_lg_u32 s49, 0\n\t"
  "s_cbranch_scc1 Lw2_loop%=\n\t"
  "s_branch Lw2_back%=\n\t"
  "Lst1_slow%=:\n\t"
  "s_nop 7\n\t"
  "s_nop 3\n\t"
  "s_waitcnt vmcnt(0)\n\t"
  "s_movk_i32 s59, 8192\n\t"
  "buffer_store_dwordx4 v[132:135], v9, %[srd], s59 offen sc1\n\t"
  "s_mov_b32 s49, 0x40000\n\t"
  "Lst1_loop%=:\n\t"
  "s_sleep 2\n\t"
  "buffer_load_dwordx2 v[100:101], v136, %[srd], s54 offen offset:0 sc1\n\t"
  "buffer_load_dwordx2 v[102:103], v136, %[srd], s54 offen offset:1024 sc1\n\t"
  "buffer_load_dwordx2 v[104:105], v136, %[srd], s54 offen offset:2048 sc1\n\t"
  "buffer_load_dwordx2 v[106:107], v136, %[srd], s54 offen offset:3072 sc1\n\t"
  "s_waitcnt vmcnt(0)\n\t"
  "v_min_u32 v131, v101, v103\n\t"
  "v_min3_u32 v131, v131, v105, v107\n\t"
  "v_cmp_eq_u32 vcc, 0, v131\n\t"
  "s_nop 3\n\t"
  "s_cmp_eq_u64 vcc, 0\n\t"
  "s_cbranch_scc1 Lst1_back%=\n\t"
  "s_sub_u32 s49, s49, 1\n\t"
  "s_cmp_lg_u32 s49, 0\n\t"
  "s_cbranch_scc1 Lst1_loop%=\n\t"
  "s_branch Lst1_back%=\n\t"
  "Lfl0_slow%=:\n\t"
  "s_nop 7\n\t"
  "s_nop 3\n\t"
  "s_waitcnt vmcnt(0)\n\t"
  "s_movk_i32 s59, 16384\n\t"
  "buffer_store_dwordx4 v[132:135], v9, %[srd], s59 offen sc1\n\t"
  "s_mov_b32 s49, 0x40000\n\t"
  "Lfl0_loop%=:\n\t"
  "s_sleep 2\n\t"
  "s_add_u32 s57, %[sFL], 0\n\t"
  "buffer_load_dwordx2 v[138:139], v137, %[srd], s57 offen sc1\n\t"
  "s_waitcnt vmcnt(0)\n\t"
  "v_cmp_ne_u64 vcc, v[132:133], v[138:139]\n\t"
  "s_nop 3\n\t"
  "s_cmp_eq_u64 vcc, 0\n\t"
  "s_cbranch_scc1 Lfl0_back%=\n\t"
  "s_sub_u32 s49, s49, 1\n\t"
  "s_cmp_lg_u32 s49, 0\n\t"
  "s_cbranch_scc1 Lfl0_loop%=\n\t"
  "s_branch Lfl0_back%=\n\t"
  "Lfl1_slow%=:\n\t"
  "s_nop 7\n\t"
  "s_nop 3\n\t"
  "s_waitcnt vmcnt(0)\n\t"
  "s_movk_i32 s59, 24576\n\t"
  "buffer_store_dwordx4 v[132:135], v9, %[srd], s59 offen sc1\n\t"
  "s_mov_b32 s49, 0x40000\n\t"
  "Lfl1_loop%=:\n\t"
  "s_sleep 2\n\t"
  "s_add_u32 s57, %[sFL], 8192\n\t"
  "buffer_load_dwordx2 v[138:139], v137, %[srd], s57 offen sc1\n\t"
  "s_waitcnt vmcnt(0)\n\t"
  "v_cmp_ne_u64 vcc, v[132:133], v[138:139]\n\t"
  "s_nop 3\n\t"
  "s_cmp_eq_u64 vcc, 0\n\t"
  "s_cbranch_scc1 Lfl1_back%=\n\t"
  "s_sub_u32 s49, s49, 1\n\t"
  "s_cmp_lg_u32 s49, 0\n\t"
  "s_cbranch_scc1 Lfl1_loop%=\n\t"
  "s_branch Lfl1_back%=\n\t"
  "Lfl2_slow%=:\n\t"
  "s_nop 7\n\t"
  "s_nop 3\n\t"
  "s_mov_b32 s49, 0x40000\n\t"
  "Lfl2_loop%=:\n\t"
  "s_sleep 2\n\t"
  "s_add_u32 s57, %[sFL], 16384\n\t"
  "buffer_load_dwordx2 v[138:139], v137, %[srd], s57 offen sc1\n\t"
  "s_waitcnt vmcnt(0)\n\t"
  "v_cmp_ne_u64 vcc, v[132:133], v[138:139]\n\t"
  "s_nop 3\n\t"
  "s_cmp_eq_u64 vcc, 0\n\t"
  "s_cbranch_scc1 Lfl2_back%=\n\t"
  "s_sub_u32 s49, s49, 1\n\t"
  "s_cmp_lg_u32 s49, 0\n\t"
  "s_cbranch_scc1 Lfl2_loop%=\n\t"
  "s_branch Lfl2_back%=\n\t"
  "Lfl3_slow%=:\n\t"
  "s_nop 7\n\t"
  "s_nop 3\n\t"
  "s_mov_b32 s49, 0x40000\n\t"
  "Lfl3_loop%=:\n\t"
  "s_sleep 2\n\t"
  "s_add_u32 s57, %[sFL], 24576\n\t"
  "buffer_load_dwordx2 v[138:139], v137, %[srd], s57 offen sc1\n\t"
  "s_waitcnt vmcnt(0)\n\t"
  "v_cmp_ne_u64 vcc, v[132:133], v[138:139]\n\t"
  "s_nop 3\n\t"
  "s_cmp_eq_u64 vcc, 0\n\t"
  "s_cbranch_scc1 Lfl3_back%=\n\t"
  "s_sub_u32 s49, s49, 1\n\t"
  "s_cmp_lg_u32 s49, 0\n\t"
  "s_cbranch_scc1 Lfl3_loop%=\n\t"
  "s_branch Lfl3_back%=\n\t"
  "Lfend%=:\n\t"
  ".Lk_end:\n\t"
    :
    : [srd] "s"(srd), [srdo] "s"(srdo), [w] "s"(w),
      [tlo] "s"((u32)tag), [thi] "s"((u32)(tag >> 32)), [sW1] "s"(OFF_W1F + (j * 4 + w) * 61440u), [sW2] "s"(OFF_W2F + (ng * 4 + w) * 61440u),
      [sUB] "s"(OFF_UB + (j * 4 + w) * 1536u),
      [sH2ld] "s"(OFF_H2F + ((4 * p + sub) * 48 + 12 * w) * 1024u), [sST] "s"(OFF_ST2 + p * 4096u), [sFL] "s"(OFF_FL + p * 32768u + w * 2048u),
      [sRT] "s"(OFF_RT + ng * 320u), [wdone] "s"(wdone)
    : "memory", "vcc", "scc",
"a0", "a1", "a2", "a3", "a4", "a5", "a6", "a7", "a8", "a9", "a10", "a11", "a12", "a13", "a14", "a15", "a16", "a17", "a18", "a19", "a20", "a21", "a22", "a23", "a24", "a25", "a26", "a27", "a28", "a29", "a30", "a31", "a32", "a33", "a34", "a35", "a36", "a37", "a38", "a39", "a40", "a41", "a42", "a43", "a44", "a45", "a46", "a47", "a48", "a49", "a50", "a51", "a52", "a53", "a54", "a55", "a56", "a57", "a58", "a59", "a60", "a61", "a62", "a63", "a64", "a65", "a66", "a67", "a68", "a69", "a70", "a71", "a72", "a73", "a74", "a75", "a76", "a77", "a78", "a79", "a80", "a81", "a82", "a83", "a84", "a85", "a86", "a87", "a88", "a89", "a90", "a91", "a92", "a93", "a94", "a95", "a96", "a97", "a98", "a99", "a100", "a101", "a102", "a103", "a104", "a105", "a106", "a107", "a108", "a109", "a110", "a111", "a112", "a113", "a114", "a115", "a116", "a117", "a118", "a119", "a120", "a121", "a122", "a123", "a124", "a125", "a126", "a127", "a128", "a129", "a130", "a131", "a132", "a133", "a134", "a135", "a136", "a137", "a138", "a139", "a140", "a141", "a142", "a143", "a144", "a145", "a146", "a147", "a148", "a149", "a150", "a151", "a152", "a153", "a154", "a155", "a156", "a157", "a158", "a159", "a160", "a161", "a162", "a163", "a164", "a165", "a166", "a167", "a168", "a169", "a170", "a171", "a172", "a173", "a174", "a175", "a176", "a177", "a178", "a179", "a180", "a181", "a182", "a183", "a184", "a185", "a186", "a187", "a188", "a189", "a190", "a191", "a192", "a193", "a194", "a195", "a196", "a197", "a198", "a199", "a200", "a201", "a202", "a203", "a204", "a205", "a206", "a207", "a208", "a209", "a210", "a211", "a212", "a213", "a214", "a215", "a216", "a217", "a218", "a219", "a220", "a221", "a222", "a223", "a224", "a225", "a226", "a227", "a228", "a229", "a230", "a231", "a232", "a233", "a234", "a235", "a236", "a237", "a238", "a239", "a240", "a241", "a242", "a243", "a244", "a245", "a246", "a247", "a248", "a249", "a250", "a251", "a252", "a253", "a254", "a255", "s44", "s45", "s46", "s47", "s48", "s49", "s52", "s54", "s55", "s56", "s57", "s58", "s59", "s60", "s61", "v0", "v1", "v2", "v3", "v4", "v5", "v6", "v7", "v8", "v9", "v10", "v11", "v12", "v13", "v14", "v15", "v16", "v17", "v18", "v19", "v20", "v21", "v22", "v23", "v24", "v25", "v26", "v27", "v28", "v29", "v30", "v31", "v32", "v33", "v34", "v35", "v36", "v37", "v38", "v39", "v40", "v41", "v42", "v43", "v44", "v45", "v46", "v47", "v48", "v49", "v50", "v51", "v52", "v53", "v54", "v55", "v56", "v57", "v58", "v59", "v60", "v61", "v62", "v63", "v64", "v65", "v66", "v67", "v68", "v69", "v70", "v71", "v72", "v73", "v74", "v75", "v76", "v77", "v78", "v79", "v80", "v81", "v82", "v83", "v84", "v85", "v86", "v87", "v88", "v89", "v90", "v91", "v92", "v93", "v94", "v95", "v96", "v97", "v98", "v99", "v100", "v101", "v102", "v103", "v104", "v105", "v106", "v107", "v108", "v109", "v110", "v111", "v112", "v113", "v114", "v115", "v116", "v117", "v118", "v119", "v120", "v121", "v122", "v123", "v124", "v125", "v126", "v127", "v128", "v129", "v130", "v131", "v132", "v133", "v134", "v135", "v136", "v137", "v138", "v139", "v141", "v142", "v143", "v144", "v145", "v146", "v147", "v148", "v149", "v150", "v151", "v152", "v153", "v154", "v155", "v156", "v157", "v158", "v159", "v160", "v161", "v162", "v163", "v164", "v165", "v166", "v167", "v168", "v169", "v170", "v171", "v172", "v173", "v174", "v175", "v176", "v177", "v178", "v179", "v180", "v181", "v182", "v183", "v184", "v185", "v186", "v187", "v188", "v189", "v190", "v191", "v192", "v193", "v194", "v195", "v196", "v197", "v198", "v199", "v200", "v201", "v202", "v203", "v204", "v205", "v206", "v207", "v208", "v209", "v210", "v211", "v212", "v213", "v214", "v215", "v216", "v217", "v218", "v219", "v220", "v221", "v222", "v223", "v224", "v225", "v226", "v227", "v228", "v229", "v230", "v231", "v232", "v233", "v234", "v235", "v236", "v237", "v238", "v239", "v240", "v241", "v242", "v243", "v244", "v245", "v246", "v247", "v248", "v249", "v250", "v251", "v252", "v253", "v254", "v255"
  );
}

extern "C" void kernel_launch(void* const* d_in, const int* in_sizes, int n_in, void* d_out, int out_size,
                              void* d_ws, size_t ws_size, hipStream_t stream) {
  k_main<<<256, 256, 0, stream>>>((const u16*)d_in[0], (const u16*)d_in[1], (const u16*)d_in[2], (const u16*)d_in[3], (const u16*)d_in[4],
                                  (const u16*)d_in[5], (const u16*)d_in[7], (const u16*)d_in[8], (const u16*)d_in[9], (const u16*)d_in[10],
                                  (const u16*)d_in[11], (char*)d_ws, (u16*)d_out);
}
```

```cpp
#include <hip/hip_runtime.h>
#include <stdint.h>

typedef unsigned short u16;
typedef unsigned int u32;
typedef unsigned long long u64;
typedef __attribute__((ext_vector_type(8))) __bf16 bf16x8;
typedef __attribute__((ext_vector_type(2))) __bf16 bf16x2;
typedef __attribute__((ext_vector_type(4))) short s16x4;
typedef __attribute__((ext_vector_type(4))) float f32x4;
typedef __attribute__((ext_vector_type(2))) float f32x2;
typedef __attribute__((ext_vector_type(4))) u32 u32x4;
typedef __attribute__((ext_vector_type(2))) u32 u32x2;
#define LAS __attribute__((address_space(3)))
#define GAS __attribute__((address_space(1)))
#define DI __device__ __forceinline__

#define STAMP(id)
#define STAMP_DECL
#define STAMP_OUT(kid)
#define A1_R 4
#define A1_P 8
#define A1_S 2
#define A1_S0 0
#define A1_X 0
#define DBG_W 1
#define DBG_X 1
#define DBG_M 1
#define W_DELAY 0
#define ROLEMAP 0
#define SELF_BAR 0
#define CODEW 1
#define U_DELAY 16
#define PRE_ORDER 3
#define OPS_ORDER 0
#define WP2_EARLY 1
#define KV_EXACT 0
#define FX_R 8
#define CONV_FIRST 1
#define U_MAP 1
constexpr int S_ = 2048, D_ = 640, F_ = 1536;
constexpr unsigned OFF_EP = 0u;
constexpr unsigned OFF_FR = 2048u;
constexpr unsigned OFF_FA1 = 4096u;
constexpr unsigned OFF_FA2 = 24576u;
constexpr unsigned OFF_FA3 = 55296u;
constexpr unsigned OFF_FX = 71680u;
constexpr unsigned OFF_FW1 = 235520u;
constexpr unsigned OFF_FU = 243712u;
constexpr unsigned OFF_FW2 = 251904u;
constexpr unsigned OFF_RT = 256000u;
constexpr unsigned OFF_GB = 258560u;
constexpr unsigned OFF_RL = 261120u;
constexpr unsigned OFF_ST2 = 278528u;
constexpr unsigned OFF_FL = 409600u;
constexpr unsigned OFF_MODP = 933888u;
constexpr unsigned OFF_ST1 = 995328u;
constexpr unsigned OFF_UB = 1011712u;
constexpr unsigned OFF_KVT = 1110016u;
constexpr unsigned OFF_XT = 3731456u;
constexpr unsigned OFF_X2P = 6352896u;
constexpr unsigned OFF_H2F = 8974336u;
constexpr unsigned OFF_W1F = 15265792u;
constexpr unsigned OFF_W2F = 19197952u;
constexpr unsigned WS_END = 21164032u;
constexpr unsigned TAG_A = 2654435769u;
constexpr unsigned TAG_B = 2246822507u;
constexpr unsigned TAG_C = 2135587861u;
constexpr unsigned TAG_D = 3266489909u;
constexpr unsigned FC_PITCH = 4160u;
constexpr unsigned FC_CH = 16640u;
constexpr unsigned L_FC = 0u;
constexpr unsigned L_KV = 66560u;
constexpr unsigned L_X = 84992u;
constexpr unsigned L_ST = 101376u;
constexpr unsigned L_XR = 117760u;
constexpr unsigned L_CNT = 140288u;
constexpr unsigned L_STASH = 143360u;
constexpr unsigned L_UN = 71680u;
constexpr unsigned UN_SZ = 22528u;

constexpr unsigned OOB = 0x80000000u;
DI unsigned rfl(unsigned x) { return __builtin_amdgcn_readfirstlane(x); }
DI unsigned stkey(unsigned r) { const unsigned k = (r >> 1) & 7; return (((k ^ (k >> 1)) >> 1) & 1) | ((k & 1) << 1) | (k & 4); }
DI float bflo(u32 w) { return __uint_as_float(w << 16); }
DI float bfhi(u32 w) { return __uint_as_float(w & 0xffff0000u); }
DI u32 pack_bf16(float lo, float hi) { bf16x2 v; v[0] = (__bf16)lo; v[1] = (__bf16)hi; return __builtin_bit_cast(u32, v); }
DI float rbf(float x) { __bf16 b = (__bf16)x; return (float)b; }
DI float dot2(u32 a, u32 b, float c) { return __builtin_amdgcn_fdot2_f32_bf16(__builtin_bit_cast(bf16x2, a), __builtin_bit_cast(bf16x2, b), c, false); }
DI f32x4 mfma(u32x4 a, u32x4 b, f32x4 c) { return __builtin_amdgcn_mfma_f32_16x16x32_bf16(__builtin_bit_cast(bf16x8, a), __builtin_bit_cast(bf16x8, b), c, 0, 0, 0); }
DI void mfma_v(f32x4& c, u32x4 a, u32x4 b) { asm volatile("s_nop 1\n\tv_mfma_f32_16x16x32_bf16 %0, %1, %2, %0" : "+v"(c) : "v"(a), "v"(b)); }
DI u32x2 tr_read(const char* p) { return __builtin_bit_cast(u32x2, __builtin_amdgcn_ds_read_tr16_b64_v4i16((LAS s16x4*)p)); }
typedef __amdgpu_buffer_rsrc_t rsrc_t;
DI rsrc_t mkrsrc(const void* p, unsigned bytes) { return __builtin_amdgcn_make_buffer_rsrc((void*)p, 0, bytes, 0x00020000); }
#define BAR() asm volatile("s_waitcnt lgkmcnt(0)\n\ts_barrier" ::: "memory")
#define FENCE() do { asm volatile("" ::: "memory"); __builtin_amdgcn_sched_barrier(0); } while (0)
constexpr int SC1 = 16, NT = 2, W_IDLE_SLEEP = 8;

DI u64 load_tag(const char* ws) {
  u64 epoch;
  asm volatile("s_load_dwordx2 %0, %1, 0\n\ts_waitcnt lgkmcnt(0)" : "=&s"(epoch) : "s"(ws) : "memory");
  const u32 tl = ((u32)epoch ^ TAG_A) * TAG_B, th = ((u32)(epoch >> 32) ^ TAG_C) * TAG_D + tl;
  return ((u64)th << 32) | tl;
}
template <int N, int SL = 2>
DI void poll_flags(rsrc_t srd, unsigned voff, u64 tag) {
  const u32 lo = (u32)tag, hi = (u32)(tag >> 32);
#pragma unroll 1
  for (int spin = 0; spin < (1 << 18); ++spin) {
    u32 bad = 0;
#pragma unroll
    for (int k = 0; k < N; ++k) { const u32x2 f = __builtin_amdgcn_raw_buffer_load_b64(srd, voff + k * 512, 0, SC1); bad |= (f[0] ^ lo) | (f[1] ^ hi); }
    asm volatile("" ::: "memory");
    if (__all(bad == 0)) break;
    __builtin_amdgcn_s_sleep(SL);
  }
  FENCE();
}
DI void put_flag(rsrc_t srd, unsigned off, unsigned lane, u64 tag) {
  __builtin_amdgcn_raw_buffer_store_b128(u32x4{(u32)tag, (u32)(tag >> 32), (u32)tag, (u32)(tag >> 32)}, srd, lane < 8 ? off + lane * 16 : OOB, 0, SC1);
  FENCE();
}
DI float modval(rsrc_t srd, unsigned col) {
  float m[4];
#pragma unroll
  for (int k = 0; k < 4; ++k) m[k] = __uint_as_float(__builtin_amdgcn_raw_buffer_load_b32(srd, col * 4, OFF_MODP + k * 15360, SC1));
  return rbf((m[0] + m[1]) + (m[2] + m[3]));
}

constexpr int RP1 = 400, RP2 = 656, L_WS = 69632;

__global__ __launch_bounds__(256, 1) void k_main(const u16* __restrict__ x, const u16* __restrict__ kIn, const u16* __restrict__ vIn,
                                                 const u16* __restrict__ shk, const u16* __restrict__ ssmk, const u16* __restrict__ ssmD,
                                                 const u16* __restrict__ wg, const u16* __restrict__ wu, const u16* __restrict__ wd,
                                                 const u16* __restrict__ cIn, const u16* __restrict__ wmod, char* ws, u16* __restrict__ out) {
  __shared__ __attribute__((aligned(16))) char smem[163840];
  const unsigned b = blockIdx.x, tid = threadIdx.x, w = rfl(tid >> 6), lane = tid & 63;
  const unsigned xcd = b & 7, loc = b >> 3, pl = loc & 3, p = 4 * (xcd >> 1) + pl, j = 8 * (xcd & 1) + (loc >> 2), ng = j & 7, sub = j >> 3;
  if (loc >= 24) {
    u64 pc; unsigned len;
    asm volatile("s_getpc_b64 %0\n.Lk_here:\n\ts_mov_b32 %1, .Lk_end-.Lk_here" : "=s"(pc), "=s"(len));
    asm volatile("buffer_load_dword a255, %0, %1, 0 offen" :: "v"(lane < 16 ? (lane * 32 + (loc - 24) * 4 + w) * 128 : OOB), "s"(mkrsrc((const void*)pc, len)) : "a255", "memory");
  }
  asm volatile("" :: "s"(x), "s"(kIn), "s"(vIn), "s"(shk), "s"(ssmk), "s"(ssmD), "s"(wg), "s"(wu), "s"(wd), "s"(cIn), "s"(wmod), "s"(ws), "s"(out));
  STAMP_DECL; STAMP(0);
  const rsrc_t srd = mkrsrc(ws, WS_END);
  const rsrc_t srdo = mkrsrc(out, S_ * D_ * 2);
  const unsigned lb = (unsigned)(size_t)smem;
  const unsigned role = loc < 20 ? 0 : loc < 28 ? 1 : 2;
  const unsigned cb = xcd * 20 + loc, w1 = xcd * 8 + loc - 20, w2 = xcd * 4 + loc - 28;
  const unsigned tb = 4 * (cb & 31) + w, lkv = L_KV + 2048 + w * 9216;
  if (role == 0)
    asm volatile(
  "s_mov_b32 m0, %[lk]\n\t"
  "s_nop 3\n\t"
  "buffer_load_dwordx4 %[vo], %[rk], %[sh] offen nt lds\n\t"
  "s_add_u32 m0, %[lk], 1024\n\t"
  "s_add_u32 s44, %[so], 0\n\t"
  "buffer_load_dwordx4 %[vo], %[rk], s44 offen nt lds\n\t"
  "s_add_u32 m0, %[lk], 5120\n\t"
  "s_nop 0\n\t"
  "buffer_load_dwordx4 %[vo], %[rv], s44 offen nt lds\n\t"
  "s_add_u32 m0, %[lk], 2048\n\t"
  "s_add_u32 s44, %[so], 5120\n\t"
  "buffer_load_dwordx4 %[vo], %[rk], s44 offen nt lds\n\t"
  "s_add_u32 m0, %[lk], 6144\n\t"
  "s_nop 0\n\t"
  "buffer_load_dwordx4 %[vo], %[rv], s44 offen nt lds\n\t"
  "s_add_u32 m0, %[lk], 3072\n\t"
  "s_add_u32 s44, %[so], 10240\n\t"
  "buffer_load_dwordx4 %[vo], %[rk], s44 offen nt lds\n\t"
  "s_add_u32 m0, %[lk], 7168\n\t"
  "s_nop 0\n\t"
  "buffer_load_dwordx4 %[vo], %[rv], s44 offen nt lds\n\t"
  "s_add_u32 m0, %[lk], 4096\n\t"
  "s_add_u32 s44, %[so], 15360\n\t"
  "buffer_load_dwordx4 %[vo], %[rk], s44 offen nt lds\n\t"
  "s_add_u32 m0, %[lk], 8192\n\t"
  "s_nop 0\n\t"
  "buffer_load_dwordx4 %[vo], %[rv], s44 offen nt lds\n\t"
      :: [rk] "s"(mkrsrc(kIn, S_ * D_ * 2)), [rv] "s"(mkrsrc(vIn, S_ * D_ * 2)), [lk] "s"(lb + lkv), [so] "s"(tb * 20480u + (cb >> 5) * 256u), [sh] "s"(tb ? tb * 20480u + (cb >> 5) * 256u - 5120u : OOB),
         [vo] "v"((lane >> 4) * 1280 + (lane & 15) * 16) : "memory", "scc", "s44");
  FENCE();
  const u32x4 zero4 = {0, 0, 0, 0};
  unsigned wdone = 8, dval = 0;
#define U_KQ(u_) ((u_) & 3)
#define U_CG(u_) (((u_) >> 2) < 30 || ((u_) >> 2) >= 50 ? (u_) >> 2 : ((u_) >> 2) < 40 ? ((u_) >> 2) + 10 : ((u_) >> 2) - 10)
#define U_SRC(u_) (U_KQ(u_) * 160u * 7680u + U_CG(u_) * 128u)
#define U_FLAG(u_) (OFF_FA2 + (U_KQ(u_) * 60 + U_CG(u_)) * 128)
  const unsigned vm = (lane >> 3) * 7680 + (((lane & 7) ^ (((lane >> 4) & 3) << 1)) * 16);
#define GEMV_LOAD(u_, lu_) asm volatile( \
  "s_nop 4\n\t" \
  "s_add_u32 m0, %[lu], 20480\n\t" \
  "s_nop 0\n\t" \
  "buffer_load_dwordx4 %[vl16], %[rc], %[sC] offen lds\n\t" \
  "s_add_u32 m0, %[lu], 0\n\t" \
  "s_add_u32 s44, %[sMod], 0\n\t" \
  "buffer_load_dwordx4 %[vm], %[rm], s44 offen nt lds\n\t" \
  "s_add_u32 m0, %[lu], 1024\n\t" \
  "s_add_u32 s44, %[sMod], 61440\n\t" \
  "buffer_load_dwordx4 %[vm], %[rm], s44 offen nt lds\n\t" \
  "s_add_u32 m0, %[lu], 2048\n\t" \
  "s_add_u32 s44, %[sMod], 122880\n\t" \
  "buffer_load_dwordx4 %[vm], %[rm], s44 offen nt lds\n\t" \
  "s_add_u32 m0, %[lu], 3072\n\t" \
  "s_add_u32 s44, %[sMod], 184320\n\t" \
  "buffer_load_dwordx4 %[vm], %[rm], s44 offen nt lds\n\t" \
  "s_add_u32 m0, %[lu], 4096\n\t" \
  "s_add_u32 s44, %[sMod], 245760\n\t" \
  "buffer_load_dwordx4 %[vm], %[rm], s44 offen nt lds\n\t" \
  "s_add_u32 m0, %[lu], 5120\n\t" \
  "s_add_u32 s44, %[sMod], 307200\n\t" \
  "buffer_load_dwordx4 %[vm], %[rm], s44 offen nt lds\n\t" \
  "s_add_u32 m0, %[lu], 6144\n\t" \
  "s_add_u32 s44, %[sMod], 368640\n\t" \
  "buffer_load_dwordx4 %[vm], %[rm], s44 offen nt lds\n\t" \
  "s_add_u32 m0, %[lu], 7168\n\t" \
  "s_add_u32 s44, %[sMod], 430080\n\t" \
  "buffer_load_dwordx4 %[vm], %[rm], s44 offen nt lds\n\t" \
  "s_add_u32 m0, %[lu], 8192\n\t" \
  "s_add_u32 s44, %[sMod], 491520\n\t" \
  "buffer_load_dwordx4 %[vm], %[rm], s44 offen nt lds\n\t" \
  "s_add_u32 m0, %[lu], 9216\n\t" \
  "s_add_u32 s44, %[sMod], 552960\n\t" \
  "buffer_load_dwordx4 %[vm], %[rm], s44 offen nt lds\n\t" \
  "s_add_u32 m0, %[lu], 10240\n\t" \
  "s_add_u32 s44, %[sMod], 614400\n\t" \
  "buffer_load_dwordx4 %[vm], %[rm], s44 offen nt lds\n\t" \
  "s_add_u32 m0, %[lu], 11264\n\t" \
  "s_add_u32 s44, %[sMod], 675840\n\t" \
  "buffer_load_dwordx4 %[vm], %[rm], s44 offen nt lds\n\t" \
  "s_add_u32 m0, %[lu], 12288\n\t" \
  "s_add_u32 s44, %[sMod], 737280\n\t" \
  "buffer_load_dwordx4 %[vm], %[rm], s44 offen nt lds\n\t" \
  "s_add_u32 m0, %[lu], 13312\n\t" \
  "s_add_u32 s44, %[sMod], 798720\n\t" \
  "buffer_load_dwordx4 %[vm], %[rm], s44 offen nt lds\n\t" \
  "s_add_u32 m0, %[lu], 14336\n\t" \
  "s_add_u32 s44, %[sMod], 860160\n\t" \
  "buffer_load_dwordx4 %[vm], %[rm], s44 offen nt lds\n\t" \
  "s_add_u32 m0, %[lu], 15360\n\t" \
  "s_add_u32 s44, %[sMod], 921600\n\t" \
  "buffer_load_dwordx4 %[vm], %[rm], s44 offen nt lds\n\t" \
  "s_add_u32 m0, %[lu], 16384\n\t" \
  "s_add_u32 s44, %[sMod], 983040\n\t" \
  "buffer_load_dwordx4 %[vm], %[rm], s44 offen nt lds\n\t" \
  "s_add_u32 m0, %[lu], 17408\n\t" \
  "s_add_u32 s44, %[sMod], 1044480\n\t" \
  "buffer_load_dwordx4 %[vm], %[rm], s44 offen nt lds\n\t" \
  "s_add_u32 m0, %[lu], 18432\n\t" \
  "s_add_u32 s44, %[sMod], 1105920\n\t" \
  "buffer_load_dwordx4 %[vm], %[rm], s44 offen nt lds\n\t" \
  "s_add_u32 m0, %[lu], 19456\n\t" \
  "s_add_u32 s44, %[sMod], 1167360\n\t" \
  "buffer_load_dwordx4 %[vm], %[rm], s44 offen nt lds\n\t" \
        :: [rm] "s"(mkrsrc(wmod, D_ * 3840 * 2)), [rc] "s"(mkrsrc(cIn, D_ * 2)), [lu] "s"(lu_), [sC] "s"(U_KQ(u_) * 320u), [sMod] "s"(U_SRC(u_)), \
           [vl16] "v"(lane * 16), [vm] "v"(vm) : "memory", "scc", "s44")
#define GEMV_UNIT(u_, lu_, younger_) asm volatile( \
  "s_nop 4\n\t" \
  "v_lshlrev_b32 v176, 1, %[vl]\n\t" \
  "v_add_u32 v176, %[lu], v176\n\t" \
  "v_mov_b32 v96, 0\n\t" \
  "v_mov_b32 v97, 0\n\t" \
  "v_mov_b32 v98, 0\n\t" \
  "s_waitcnt vmcnt(%c[wa])\n\t" \
  "ds_read_u16_d16_hi v96, v176 offset:20480\n\t" \
  "ds_read_u16_d16_hi v97, v176 offset:20608\n\t" \
  "ds_read_u16_d16_hi v98, v176 offset:20736\n\t" \
  "s_waitcnt lgkmcnt(2)\n\t" \
  "v_mul_f32 v177, 0xbfb8aa3b, v96\n\t" \
  "s_waitcnt lgkmcnt(1)\n\t" \
  "v_mul_f32 v178, 0xbfb8aa3b, v97\n\t" \
  "s_waitcnt lgkmcnt(0)\n\t" \
  "v_mul_f32 v179, 0xbfb8aa3b, v98\n\t" \
  "v_exp_f32 v177, v177\n\t" \
  "v_exp_f32 v178, v178\n\t" \
  "v_exp_f32 v179, v179\n\t" \
  "v_add_f32 v177, 1.0, v177\n\t" \
  "v_add_f32 v178, 1.0, v178\n\t" \
  "v_add_f32 v179, 1.0, v179\n\t" \
  "v_rcp_f32 v177, v177\n\t" \
  "v_rcp_f32 v178, v178\n\t" \
  "v_rcp_f32 v179, v179\n\t" \
  "v_mul_f32 v177, v177, v96\n\t" \
  "v_mul_f32 v178, v178, v97\n\t" \
  "v_mul_f32 v179, v179, v98\n\t" \
  "v_cvt_pk_bf16_f32 v177, v177, v177\n\t" \
  "v_cvt_pk_bf16_f32 v178, v178, v178\n\t" \
  "v_cvt_pk_bf16_f32 v179, v179, v179\n\t" \
  "ds_write_b16 v176, v177 offset:21504\n\t" \
  "ds_write_b16 v176, v178 offset:21632\n\t" \
  "ds_write_b16 v176, v179 offset:21760\n\t" \
  "v_lshrrev_b32 v180, 4, %[vl]\n\t" \
  "v_lshlrev_b32 v180, 3, v180\n\t" \
  "v_add_u32 v180, %[lu], v180\n\t" \
  "ds_read_b64 v[100:101], v180 offset:21504\n\t" \
  "ds_read_b64 v[102:103], v180 offset:21536\n\t" \
  "ds_read_b64 v[104:105], v180 offset:21568\n\t" \
  "ds_read_b64 v[106:107], v180 offset:21600\n\t" \
  "ds_read_b64 v[108:109], v180 offset:21632\n\t" \
  "ds_read_b64 v[110:111], v180 offset:21664\n\t" \
  "ds_read_b64 v[112:113], v180 offset:21696\n\t" \
  "ds_read_b64 v[114:115], v180 offset:21728\n\t" \
  "ds_read_b64 v[116:117], v180 offset:21760\n\t" \
  "ds_read_b64 v[118:119], v180 offset:21792\n\t" \
  "v_xor_b32 v181, 32, %[vtm]\n\t" \
  "v_xor_b32 v182, 64, %[vtm]\n\t" \
  "v_xor_b32 v183, 96, %[vtm]\n\t" \
  "s_waitcnt vmcnt(%c[wb])\n\t" \
  "ds_read_b64_tr_b16 v[120:121], %[vtm] offset:0\n\t" \
  "ds_read_b64_tr_b16 v[122:123], %[vtm] offset:2048\n\t" \
  "ds_read_b64_tr_b16 v[124:125], v181 offset:0\n\t" \
  "ds_read_b64_tr_b16 v[126:127], v181 offset:2048\n\t" \
  "ds_read_b64_tr_b16 v[128:129], v182 offset:0\n\t" \
  "ds_read_b64_tr_b16 v[130:131], v182 offset:2048\n\t" \
  "ds_read_b64_tr_b16 v[132:133], v183 offset:0\n\t" \
  "ds_read_b64_tr_b16 v[134:135], v183 offset:2048\n\t" \
  "ds_read_b64_tr_b16 v[136:137], %[vtm] offset:4096\n\t" \
  "ds_read_b64_tr_b16 v[138:139], %[vtm] offset:6144\n\t" \
  "ds_read_b64_tr_b16 v[140:141], v181 offset:4096\n\t" \
  "ds_read_b64_tr_b16 v[142:143], v181 offset:6144\n\t" \
  "ds_read_b64_tr_b16 v[144:145], v182 offset:4096\n\t" \
  "ds_read_b64_tr_b16 v[146:147], v182 offset:6144\n\t" \
  "ds_read_b64_tr_b16 v[148:149], v183 offset:4096\n\t" \
  "ds_read_b64_tr_b16 v[150:151], v183 offset:6144\n\t" \
  "s_waitcnt lgkmcnt(14)\n\t" \
  "v_mfma_f32_16x16x32_bf16 v[152:155], v[100:103], v[120:123], 0\n\t" \
  "s_waitcnt lgkmcnt(12)\n\t" \
  "v_mfma_f32_16x16x32_bf16 v[156:159], v[100:103], v[124:127], 0\n\t" \
  "s_waitcnt lgkmcnt(10)\n\t" \
  "v_mfma_f32_16x16x32_bf16 v[160:163], v[100:103], v[128:131], 0\n\t" \
  "s_waitcnt lgkmcnt(8)\n\t" \
  "v_mfma_f32_16x16x32_bf16 v[164:167], v[100:103], v[132:135], 0\n\t" \
  "ds_read_b64_tr_b16 v[120:121], %[vtm] offset:8192\n\t" \
  "ds_read_b64_tr_b16 v[122:123], %[vtm] offset:10240\n\t" \
  "ds_read_b64_tr_b16 v[124:125], v181 offset:8192\n\t" \
  "ds_read_b64_tr_b16 v[126:127], v181 offset:10240\n\t" \
  "ds_read_b64_tr_b16 v[128:129], v182 offset:8192\n\t" \
  "ds_read_b64_tr_b16 v[130:131], v182 offset:10240\n\t" \
  "ds_read_b64_tr_b16 v[132:133], v183 offset:8192\n\t" \
  "ds_read_b64_tr_b16 v[134:135], v183 offset:10240\n\t" \
  "s_waitcnt lgkmcnt(14)\n\t" \
  "v_mfma_f32_16x16x32_bf16 v[152:155], v[104:107], v[136:139], v[152:155]\n\t" \
  "s_waitcnt lgkmcnt(12)\n\t" \
  "v_mfma_f32_16x16x32_bf16 v[156:159], v[104:107], v[140:143], v[156:159]\n\t" \
  "s_waitcnt lgkmcnt(10)\n\t" \
  "v_mfma_f32_16x16x32_bf16 v[160:163], v[104:107], v[144:147], v[160:163]\n\t" \
  "s_waitcnt lgkmcnt(8)\n\t" \
  "v_mfma_f32_16x16x32_bf16 v[164:167], v[104:107], v[148:151], v[164:167]\n\t" \
  "ds_read_b64_tr_b16 v[136:137], %[vtm] offset:12288\n\t" \
  "ds_read_b64_tr_b16 v[138:139], %[vtm] offset:14336\n\t" \
  "ds_read_b64_tr_b16 v[140:141], v181 offset:12288\n\t" \
  "ds_read_b64_tr_b16 v[142:143], v181 offset:14336\n\t" \
  "ds_read_b64_tr_b16 v[144:145], v182 offset:12288\n\t" \
  "ds_read_b64_tr_b16 v[146:147], v182 offset:14336\n\t" \
  "ds_read_b64_tr_b16 v[148:149], v183 offset:12288\n\t" \
  "ds_read_b64_tr_b16 v[150:151], v183 offset:14336\n\t" \
  "s_waitcnt lgkmcnt(14)\n\t" \
  "v_mfma_f32_16x16x32_bf16 v[152:155], v[108:111], v[120:123], v[152:155]\n\t" \
  "s_waitcnt lgkmcnt(12)\n\t" \
  "v_mfma_f32_16x16x32_bf16 v[156:159], v[108:111], v[124:127], v[156:159]\n\t" \
  "s_waitcnt lgkmcnt(10)\n\t" \
  "v_mfma_f32_16x16x32_bf16 v[160:163], v[108:111], v[128:131], v[160:163]\n\t" \
  "s_waitcnt lgkmcnt(8)\n\t" \
  "v_mfma_f32_16x16x32_bf16 v[164:167], v[108:111], v[132:135], v[164:167]\n\t" \
  "ds_read_b64_tr_b16 v[120:121], %[vtm] offset:16384\n\t" \
  "ds_read_b64_tr_b16 v[122:123], %[vtm] offset:18432\n\t" \
  "ds_read_b64_tr_b16 v[124:125], v181 offset:16384\n\t" \
  "ds_read_b64_tr_b16 v[126:127], v181 offset:18432\n\t" \
  "ds_read_b64_tr_b16 v[128:129], v182 offset:16384\n\t" \
  "ds_read_b64_tr_b16 v[130:131], v182 offset:18432\n\t" \
  "ds_read_b64_tr_b16 v[132:133], v183 offset:16384\n\t" \
  "ds_read_b64_tr_b16 v[134:135], v183 offset:18432\n\t" \
  "s_waitcnt lgkmcnt(14)\n\t" \
  "v_mfma_f32_16x16x32_bf16 v[152:155], v[112:115], v[136:139], v[152:155]\n\t" \
  "s_waitcnt lgkmcnt(12)\n\t" \
  "v_mfma_f32_16x16x32_bf16 v[156:159], v[112:115], v[140:143], v[156:159]\n\t" \
  "s_waitcnt lgkmcnt(10)\n\t" \
  "v_mfma_f32_16x16x32_bf16 v[160:163], v[112:115], v[144:147], v[160:163]\n\t" \
  "s_waitcnt lgkmcnt(8)\n\t" \
  "v_mfma_f32_16x16x32_bf16 v[164:167], v[112:115], v[148:151], v[164:167]\n\t" \
  "s_waitcnt lgkmcnt(6)\n\t" \
  "v_mfma_f32_16x16x32_bf16 v[152:155], v[116:119], v[120:123], v[152:155]\n\t" \
  "s_waitcnt lgkmcnt(4)\n\t" \
  "v_mfma_f32_16x16x32_bf16 v[156:159], v[116:119], v[124:127], v[156:159]\n\t" \
  "s_waitcnt lgkmcnt(2)\n\t" \
  "v_mfma_f32_16x16x32_bf16 v[160:163], v[116:119], v[128:131], v[160:163]\n\t" \
  "s_waitcnt lgkmcnt(0)\n\t" \
  "v_mfma_f32_16x16x32_bf16 v[164:167], v[116:119], v[132:135], v[164:167]\n\t" \
  "v_lshlrev_b32 v176, 2, %[vl]\n\t" \
  "s_nop 2\n\t" \
  "v_mov_b32_dpp v152, v156 quad_perm:[0,1,2,3] row_mask:0x2 bank_mask:0xf\n\t" \
  "s_nop 1\n\t" \
  "v_mov_b32_dpp v152, v160 quad_perm:[0,1,2,3] row_mask:0x4 bank_mask:0xf\n\t" \
  "s_nop 1\n\t" \
  "v_mov_b32_dpp v152, v164 quad_perm:[0,1,2,3] row_mask:0x8 bank_mask:0xf\n\t" \
  "buffer_store_dword v152, v176, %[srd], %[sMo] offen sc1\n\t" \
  "s_nop 1\n\t" \
        :: [srd] "s"(srd), [lu] "s"(lu_), [wa] "i"((younger_) + 20), [wb] "i"(younger_), [sMo] "s"(OFF_MODP + U_KQ(u_) * 15360u + U_CG(u_) * 256u), [vl] "v"(lane), \
           [vtm] "v"((lu_) + (lane >> 2) * 128 + ((lane >> 3) & 3) * 32 + (lane & 3) * 8) : "memory", "vcc", "scc", \
"v96", "v97", "v98", "v100", "v101", "v102", "v103", "v104", "v105", "v106", "v107", "v108", "v109", "v110", "v111", "v112", "v113", "v114", "v115", "v116", "v117", "v118", "v119", "v120", "v121", "v122", "v123", "v124", "v125", "v126", "v127", "v128", "v129", "v130", "v131", "v132", "v133", "v134", "v135", "v136", "v137", "v138", "v139", "v140", "v141", "v142", "v143", "v144", "v145", "v146", "v147", "v148", "v149", "v150", "v151", "v152", "v153", "v154", "v155", "v156", "v157", "v158", "v159", "v160", "v161", "v162", "v163", "v164", "v165", "v166", "v167", "v176", "v177", "v178", "v179", "v180", "v181", "v182", "v183" \
      )
  auto stash = [&]() {
    const unsigned vpub = role == 0 ? (tid < 8 * FX_R ? OFF_FX + ((tid >> 3) * 160 + cb) * 128 + (tid & 7) * 16 : OOB) : tid < 8 ? (role == 1 ? OFF_FU + w1 * 128 : OFF_FW2 + w2 * 128) + tid * 16 : OOB;
    const unsigned vpw = lane < 4 ? OFF_FW1 + (j * 4 + lane) * 128 : lane < 8 ? OFF_FW2 + ((4 * w + (lane & 3)) * 2 + (ng >> 2)) * 128
                                                                              : OFF_FU + ((lane < 12 ? j * 4 : 0) + (lane & 3)) * 128;
    const unsigned pb = 32 * (lane >> 3) + 8 * w + (lane & 7);
    const unsigned vpx = lane < 40 && !(role == 0 && pb == cb) ? OFF_FX + (xcd % FX_R * 160 + pb) * 128 : OFF_FW2 + min(lane - 40, 9u) * 128;
    const unsigned g = lane >> 4, q = (lane >> 2) & 3, pq = lane & 3;
    const unsigned row = 4 * g + q;
    const unsigned vtr = lb + row * 128 + ((row >> 1) & 3) * 32 + ((pq * 8) ^ ((row & 8) * 2));
    const unsigned vl16 = lane * 16;
    const unsigned vdma = OFF_X2P + p * 81920u + w * 4096u + lane * 16;
    const unsigned vh2st = lane < 48 ? OFF_H2F + (4 * p * 192 + 12 * j + 3 * w) * 256u + lane * 16 : OOB;
    const unsigned tk = 4 * j + w;
    const unsigned vstc = lb + lane * 128 + (((tk >> 4) ^ ((lane >> 1) & 3)) * 32) + (((tk & 15) * 2) ^ ((lane & 8) * 2));
    const unsigned vstw = lane == 63 ? tk * 64 : OOB;
    const unsigned vflw = lane < 8 ? OFF_FL + p * 32768u + (j * 4 + w) * 128u + lane * 16 : OOB;
    const unsigned vep = b == 0 && tid == 0 ? OFF_EP : OOB;
    const unsigned rs_ = 2 * (w & 1) + sub, rb = lb + (w >> 1) * 81920 + 80 * ng * 128;
    const unsigned r0 = 8 * g + q, r1 = 8 * g + 4 + q, r2 = 64 + 4 * g + q;
    const unsigned vres0 = rb + r0 * 128 + ((rs_ ^ ((r0 >> 1) & 3)) * 32) + ((pq * 8) ^ ((r0 & 8) * 2));
    const unsigned vres1 = rb + r1 * 128 + ((rs_ ^ ((r1 >> 1) & 3)) * 32) + ((pq * 8) ^ ((r1 & 8) * 2));
    const unsigned vres2 = rb + r2 * 128 + ((rs_ ^ ((r2 >> 1) & 3)) * 32) + ((pq * 8) ^ ((r2 & 8) * 2));
    const unsigned vwr = lb + w * 20480 + g * 256 + ((lane + 4 * g) & 15) * 16;
    const unsigned tq = lane < 32 ? q : (lane >> 1) & 3, tl = 4 * w + tq, g0 = lane < 32 ? pq : 2 * (lane & 1), g1 = lane < 32 ? pq : g0 + 1;
    const unsigned vrd0 = lb + (lane < 32 ? g * 2048 : 4096) + g0 * 256 + ((tl + 4 * g0) & 15) * 16;
    const unsigned vrd1 = lb + (lane < 32 ? g * 2048 + 1024 : 4096) + g1 * 256 + ((tl + 4 * g1) & 15) * 16;
    const unsigned vh1w = lane < 20 ? OFF_X2P + (16 + p) * 81920u + w * 4096u + (lane >> 2) * 16384 + ((lane & 3) * 8 + ng) * 128 : OOB;
    const unsigned vgb = OFF_GB - OFF_RT + (lane < 32 ? 32 * g + 8 * pq : 64 + 8 * (lane & 1)) * 4;
    const unsigned vout = lane < 40 ? ((64 * p + 16 * sub + tl) * D_ + 80 * ng + (lane < 32 ? 32 * g + 8 * pq : 64 + 8 * (lane & 1))) * 2 : OOB;
    *(u32x4*)(smem + L_STASH + 0 + tid * 16) = u32x4{vpub, vl16, vpw, vpx};
    *(u32x4*)(smem + L_STASH + 4096 + tid * 16) = u32x4{vdma, vtr, vstc, vstw};
    *(u32x4*)(smem + L_STASH + 8192 + tid * 16) = u32x4{vh2st, vflw, vres0, vres1};
    *(u32x4*)(smem + L_STASH + 12288 + tid * 16) = u32x4{vres2, vwr, vrd0, vrd1};
    *(u32x4*)(smem + L_STASH + 16384 + tid * 16) = u32x4{vout, vgb, vep, vh1w};
    FENCE();
  };
  unsigned c_vf2;
  unsigned c_vf1;
  unsigned c_vf3;
  unsigned c_vfw;
  unsigned c_va;
  unsigned c_vb0;
  unsigned c_vb1;
  unsigned c_vb2;
  unsigned c_vb3;
  unsigned c_vx;
  unsigned c_vs;
  unsigned c_vs2;
  unsigned c_vo;
  u32x4 c_vd;
  unsigned c_vl16;
  unsigned c_vkv;
  unsigned c_vmod;
  u64 tag;
  if (__builtin_expect(role == 0, CONV_FIRST)) {
    const rsrc_t rf = mkrsrc(ssmk, S_ * D_ * 2);
    const unsigned d0 = 128 * (cb >> 5) + 2 * lane;
    u32x4 fa[4];
    const u32x4 taps = *(const u32x4*)(shk + d0 * 4);
    dval = ssmD[8 * (cb >> 1) + 2 * w + (cb & 1)];
    FENCE();
    __builtin_amdgcn_raw_buffer_store_b128(u32x4{0, 0, 0, 0}, srd, tid < 64 && cb < 128 ? OFF_ST2 + cb * 1024 + tid * 16 : OOB, 0, SC1);
    FENCE();
#pragma unroll
    for (int n = 0; n < 4; ++n) fa[n] = __builtin_amdgcn_raw_buffer_load_b128(rf, lane * 16 + n * 1024, (8 * (cb >> 1) + 2 * w + (cb & 1)) * 4096u, NT);
    u32x4 xr[5];
    const bool isx = cb < 128;
    const rsrc_t rx = mkrsrc(isx ? x : wmod, isx ? S_ * D_ * 2 : D_ * 3840 * 2);
#pragma unroll
    for (int n = 0; n < 5; ++n)
      xr[n] = __builtin_amdgcn_raw_buffer_load_b128(rx, isx ? (lane >> 3) * 1280 + (lane & 7) * 16 : vm,
                                                    isx ? cb * 20480u + (w & 1) * 10240u + (w >> 1) * 128u + n * 256u : U_SRC(cb) + (4 * n + w) * 61440u, NT);
    const u32x4 cr = __builtin_amdgcn_raw_buffer_load_b128(mkrsrc(cIn, isx ? 0 : D_ * 2), lane * 16, U_KQ(cb) * 320u, 0);
    FENCE();
    STAMP(6);
    tag = load_tag(ws);
    if (w < 2) *(u32x4*)(smem + L_KV + tid * 16) = zero4;
    if (tid == 128) *(u32x4*)(smem + L_CNT) = zero4;
    {
      const unsigned d = 8 * (cb >> 1) + 2 * w + (cb & 1);
      const unsigned r = lane & 15, g = lane >> 4;
      const unsigned pp = 25 + r - 8 * g;
      const unsigned va = lb + L_FC + w * FC_CH + (pp & 3) * FC_PITCH + (pp & ~3u) * 2;
      unsigned vb[4];
  #pragma unroll
      for (int k = 0; k < 4; ++k) { const unsigned G = 14 - 4 * k + 2 * r + g; vb[k] = lb + L_KV + G * 64 + ((w ^ (G >> 2)) & 3) * 16; }
      const unsigned gx = (2 * r + (g >> 1)) * 64 + ((w ^ (r >> 1)) & 3) * 16;
      const unsigned vx = lb + L_X + gx + (g & 1) * 8, vs = lb + L_ST + r * 128 + ((2 * g) ^ stkey(r)) * 16;
      const unsigned to = 256 * (g & 1) + 16 * r + 8 * (g >> 1);
      const unsigned vo = (to >> 6) * 81920u + (((to & 63) * 2) ^ (((d >> 1) & 3) * 32 + (d & 8) * 2));
      const unsigned wh = (lane >> 2) & 3, mcol = (wh < 3 ? wh : 4) * 640 + d;
      const unsigned jd = 8 * g - 9 - r, dv = dval << (16 * (jd & 1));
      const u32x4 vd = {jd >> 1 == 0 ? dv : 0u, jd >> 1 == 1 ? dv : 0u, jd >> 1 == 2 ? dv : 0u, jd >> 1 == 3 ? dv : 0u};
      c_vf2 = lane < 32 ? OFF_FA3 + (32 * w + lane) * 128 : OFF_FA2 + ((lane & 3) * 60 + (mcol >> 6)) * 128;
      c_vf1 = OFF_FA1 + ((cb & ~31u) + 8 * w + (lane & 7)) * 128;
      c_vf3 = lane < 8 ? (cb < 128 ? OFF_FA3 + cb * 128 : U_FLAG(cb)) + lane * 16 : OOB;
      c_vfw = OFF_FW1 + (j * 4 + (lane & 3)) * 128;
      c_va = va;
      c_vb0 = vb[0];
      c_vb1 = vb[1];
      c_vb2 = vb[2];
      c_vb3 = vb[3];
      c_vx = vx;
      c_vs = vs;
      c_vs2 = vs ^ 16;
      c_vo = vo;
      c_vd = vd;
      c_vl16 = lane * 16;
      c_vkv = (lane >> 3) * 20480 + (lane & 7) * 16;
      c_vmod = OFF_MODP + ((lane & 3) * 3840 + mcol) * 4;
      asm volatile("" : "+v"(c_vf2), "+v"(c_vf1), "+v"(c_vf3), "+v"(c_vfw), "+v"(c_va), "+v"(c_vb0), "+v"(c_vb1), "+v"(c_vb2), "+v"(c_vb3), "+v"(c_vx), "+v"(c_vs), "+v"(c_vs2), "+v"(c_vo), "+v"(c_vd), "+v"(c_vl16), "+v"(c_vkv), "+v"(c_vmod));
    }
    FENCE();
    asm volatile("s_waitcnt vmcnt(11)" ::: "memory");
    STAMP(7);
    asm volatile(
  "s_nop 1\n\t"
  "ds_read2st64_b32 v[112:113], %[la] offset0:0 offset1:1\n\t"
  "ds_read2st64_b32 v[114:115], %[la] offset0:2 offset1:3\n\t"
  "ds_read2st64_b32 v[116:117], %[la] offset0:4 offset1:5\n\t"
  "ds_read2st64_b32 v[118:119], %[la] offset0:6 offset1:7\n\t"
  "ds_read2st64_b32 v[120:121], %[la] offset0:8 offset1:9\n\t"
  "ds_read2st64_b32 v[122:123], %[la] offset0:10 offset1:11\n\t"
  "ds_read2st64_b32 v[132:133], %[la] offset0:20 offset1:21\n\t"
  "ds_read2st64_b32 v[134:135], %[la] offset0:22 offset1:23\n\t"
  "ds_read2st64_b32 v[136:137], %[la] offset0:24 offset1:25\n\t"
  "ds_read2st64_b32 v[138:139], %[la] offset0:26 offset1:27\n\t"
  "ds_read2st64_b32 v[124:125], %[la] offset0:12 offset1:13\n\t"
  "ds_read2st64_b32 v[126:127], %[la] offset0:14 offset1:15\n\t"
  "ds_read2st64_b32 v[128:129], %[la] offset0:16 offset1:17\n\t"
  "ds_read2st64_b32 v[130:131], %[la] offset0:18 offset1:19\n\t"
  "ds_read2st64_b32 v[140:141], %[la] offset0:28 offset1:29\n\t"
  "ds_read2st64_b32 v[142:143], %[la] offset0:30 offset1:31\n\t"
  "ds_read2st64_b32 v[144:145], %[la] offset0:32 offset1:33\n\t"
  "ds_read2st64_b32 v[146:147], %[la] offset0:34 offset1:35\n\t"
  "v_lshlrev_b32 v186, 16, %[t0]\n\t"
  "v_lshlrev_b32 v187, 16, %[t2]\n\t"
  "v_and_b32 v188, 0xffff0000, %[t0]\n\t"
  "v_and_b32 v189, 0xffff0000, %[t2]\n\t"
  "v_lshlrev_b32 v190, 16, %[t1]\n\t"
  "v_lshlrev_b32 v191, 16, %[t3]\n\t"
  "v_and_b32 v192, 0xffff0000, %[t1]\n\t"
  "v_and_b32 v193, 0xffff0000, %[t3]\n\t"
  "v_mov_b32 v234, 0x3f803f80\n\t"
  "s_waitcnt lgkmcnt(15)\n\t"
  "v_lshlrev_b32 v148, 16, v113\n\t"
  "v_and_b32 v149, 0xffff0000, v113\n\t"
  "v_lshlrev_b32 v150, 16, v114\n\t"
  "v_and_b32 v151, 0xffff0000, v114\n\t"
  "v_lshlrev_b32 v152, 16, v115\n\t"
  "v_and_b32 v153, 0xffff0000, v115\n\t"
  "v_lshlrev_b32 v154, 16, v116\n\t"
  "v_and_b32 v155, 0xffff0000, v116\n\t"
  "v_lshlrev_b32 v156, 16, v117\n\t"
  "v_and_b32 v157, 0xffff0000, v117\n\t"
  "s_waitcnt lgkmcnt(14)\n\t"
  "v_lshlrev_b32 v158, 16, v118\n\t"
  "v_and_b32 v159, 0xffff0000, v118\n\t"
  "v_lshlrev_b32 v160, 16, v119\n\t"
  "v_and_b32 v161, 0xffff0000, v119\n\t"
  "s_waitcnt lgkmcnt(13)\n\t"
  "v_lshlrev_b32 v162, 16, v120\n\t"
  "v_and_b32 v163, 0xffff0000, v120\n\t"
  "v_lshlrev_b32 v164, 16, v121\n\t"
  "v_and_b32 v165, 0xffff0000, v121\n\t"
  "s_waitcnt lgkmcnt(12)\n\t"
  "v_lshlrev_b32 v166, 16, v122\n\t"
  "v_and_b32 v167, 0xffff0000, v122\n\t"
  "v_lshlrev_b32 v168, 16, v123\n\t"
  "v_and_b32 v169, 0xffff0000, v123\n\t"
  "v_pk_mul_f32 v[194:195], v[148:149], v[192:193]\n\t"
  "v_pk_mul_f32 v[196:197], v[150:151], v[192:193]\n\t"
  "v_pk_mul_f32 v[198:199], v[152:153], v[192:193]\n\t"
  "v_pk_mul_f32 v[200:201], v[154:155], v[192:193]\n\t"
  "v_pk_mul_f32 v[202:203], v[156:157], v[192:193]\n\t"
  "v_pk_mul_f32 v[204:205], v[158:159], v[192:193]\n\t"
  "v_pk_mul_f32 v[206:207], v[160:161], v[192:193]\n\t"
  "v_pk_mul_f32 v[208:209], v[162:163], v[192:193]\n\t"
  "v_pk_mul_f32 v[210:211], v[150:151], v[190:191]\n\t"
  "v_pk_mul_f32 v[212:213], v[152:153], v[190:191]\n\t"
  "v_pk_mul_f32 v[214:215], v[154:155], v[190:191]\n\t"
  "v_pk_mul_f32 v[216:217], v[156:157], v[190:191]\n\t"
  "v_pk_mul_f32 v[218:219], v[158:159], v[190:191]\n\t"
  "v_pk_mul_f32 v[220:221], v[160:161], v[190:191]\n\t"
  "v_pk_mul_f32 v[222:223], v[162:163], v[190:191]\n\t"
  "v_pk_mul_f32 v[224:225], v[164:165], v[190:191]\n\t"
  "v_cvt_pk_bf16_f32 v194, v194, v210\n\t"
  "v_cvt_pk_bf16_f32 v195, v195, v211\n\t"
  "v_cvt_pk_bf16_f32 v196, v196, v212\n\t"
  "v_cvt_pk_bf16_f32 v197, v197, v213\n\t"
  "v_cvt_pk_bf16_f32 v198, v198, v214\n\t"
  "v_cvt_pk_bf16_f32 v199, v199, v215\n\t"
  "v_cvt_pk_bf16_f32 v200, v200, v216\n\t"
  "v_cvt_pk_bf16_f32 v201, v201, v217\n\t"
  "v_cvt_pk_bf16_f32 v202, v202, v218\n\t"
  "v_cvt_pk_bf16_f32 v203, v203, v219\n\t"
  "v_cvt_pk_bf16_f32 v204, v204, v220\n\t"
  "v_cvt_pk_bf16_f32 v205, v205, v221\n\t"
  "v_cvt_pk_bf16_f32 v206, v206, v222\n\t"
  "v_cvt_pk_bf16_f32 v207, v207, v223\n\t"
  "v_cvt_pk_bf16_f32 v208, v208, v224\n\t"
  "v_cvt_pk_bf16_f32 v209, v209, v225\n\t"
  "v_dot2_f32_bf16 v194, v194, v234, 0\n\t"
  "v_dot2_f32_bf16 v195, v195, v234, 0\n\t"
  "v_dot2_f32_bf16 v196, v196, v234, 0\n\t"
  "v_dot2_f32_bf16 v197, v197, v234, 0\n\t"
  "v_dot2_f32_bf16 v198, v198, v234, 0\n\t"
  "v_dot2_f32_bf16 v199, v199, v234, 0\n\t"
  "v_dot2_f32_bf16 v200, v200, v234, 0\n\t"
  "v_dot2_f32_bf16 v201, v201, v234, 0\n\t"
  "v_dot2_f32_bf16 v202, v202, v234, 0\n\t"
  "v_dot2_f32_bf16 v203, v203, v234, 0\n\t"
  "v_dot2_f32_bf16 v204, v204, v234, 0\n\t"
  "v_dot2_f32_bf16 v205, v205, v234, 0\n\t"
  "v_dot2_f32_bf16 v206, v206, v234, 0\n\t"
  "v_dot2_f32_bf16 v207, v207, v234, 0\n\t"
  "v_dot2_f32_bf16 v208, v208, v234, 0\n\t"
  "v_dot2_f32_bf16 v209, v209, v234, 0\n\t"
  "v_pk_mul_f32 v[210:211], v[152:153], v[188:189]\n\t"
  "v_pk_mul_f32 v[212:213], v[154:155], v[188:189]\n\t"
  "v_pk_mul_f32 v[214:215], v[156:157], v[188:189]\n\t"
  "v_pk_mul_f32 v[216:217], v[158:159], v[188:189]\n\t"
  "v_pk_mul_f32 v[218:219], v[160:161], v[188:189]\n\t"
  "v_pk_mul_f32 v[220:221], v[162:163], v[188:189]\n\t"
  "v_pk_mul_f32 v[222:223], v[164:165], v[188:189]\n\t"
  "v_pk_mul_f32 v[224:225], v[166:167], v[188:189]\n\t"
  "v_cvt_pk_bf16_f32 v194, v194, v210\n\t"
  "v_cvt_pk_bf16_f32 v195, v195, v211\n\t"
  "v_cvt_pk_bf16_f32 v196, v196, v212\n\t"
  "v_cvt_pk_bf16_f32 v197, v197, v213\n\t"
  "v_cvt_pk_bf16_f32 v198, v198, v214\n\t"
  "v_cvt_pk_bf16_f32 v199, v199, v215\n\t"
  "v_cvt_pk_bf16_f32 v200, v200, v216\n\t"
  "v_cvt_pk_bf16_f32 v201, v201, v217\n\t"
  "v_cvt_pk_bf16_f32 v202, v202, v218\n\t"
  "v_cvt_pk_bf16_f32 v203, v203, v219\n\t"
  "v_cvt_pk_bf16_f32 v204, v204, v220\n\t"
  "v_cvt_pk_bf16_f32 v205, v205, v221\n\t"
  "v_cvt_pk_bf16_f32 v206, v206, v222\n\t"
  "v_cvt_pk_bf16_f32 v207, v207, v223\n\t"
  "v_cvt_pk_bf16_f32 v208, v208, v224\n\t"
  "v_cvt_pk_bf16_f32 v209, v209, v225\n\t"
  "v_dot2_f32_bf16 v194, v194, v234, 0\n\t"
  "v_dot2_f32_bf16 v195, v195, v234, 0\n\t"
  "v_dot2_f32_bf16 v196, v196, v234, 0\n\t"
  "v_dot2_f32_bf16 v197, v197, v234, 0\n\t"
  "v_dot2_f32_bf16 v198, v198, v234, 0\n\t"
  "v_dot2_f32_bf16 v199, v199, v234, 0\n\t"
  "v_dot2_f32_bf16 v200, v200, v234, 0\n\t"
  "v_dot2_f32_bf16 v201, v201, v234, 0\n\t"
  "v_dot2_f32_bf16 v202, v202, v234, 0\n\t"
  "v_dot2_f32_bf16 v203, v203, v234, 0\n\t"
  "v_dot2_f32_bf16 v204, v204, v234, 0\n\t"
  "v_dot2_f32_bf16 v205, v205, v234, 0\n\t"
  "v_dot2_f32_bf16 v206, v206, v234, 0\n\t"
  "v_dot2_f32_bf16 v207, v207, v234, 0\n\t"
  "v_dot2_f32_bf16 v208, v208, v234, 0\n\t"
  "v_dot2_f32_bf16 v209, v209, v234, 0\n\t"
  "v_pk_mul_f32 v[210:211], v[154:155], v[186:187]\n\t"
  "v_pk_mul_f32 v[212:213], v[156:157], v[186:187]\n\t"
  "v_pk_mul_f32 v[214:215], v[158:159], v[186:187]\n\t"
  "v_pk_mul_f32 v[216:217], v[160:161], v[186:187]\n\t"
  "v_pk_mul_f32 v[218:219], v[162:163], v[186:187]\n\t"
  "v_pk_mul_f32 v[220:221], v[164:165], v[186:187]\n\t"
  "v_pk_mul_f32 v[222:223], v[166:167], v[186:187]\n\t"
  "v_pk_mul_f32 v[224:225], v[168:169], v[186:187]\n\t"
  "v_cvt_pk_bf16_f32 v194, v194, v210\n\t"
  "v_cvt_pk_bf16_f32 v195, v195, v211\n\t"
  "v_cvt_pk_bf16_f32 v196, v196, v212\n\t"
  "v_cvt_pk_bf16_f32 v197, v197, v213\n\t"
  "v_cvt_pk_bf16_f32 v198, v198, v214\n\t"
  "v_cvt_pk_bf16_f32 v199, v199, v215\n\t"
  "v_cvt_pk_bf16_f32 v200, v200, v216\n\t"
  "v_cvt_pk_bf16_f32 v201, v201, v217\n\t"
  "v_cvt_pk_bf16_f32 v202, v202, v218\n\t"
  "v_cvt_pk_bf16_f32 v203, v203, v219\n\t"
  "v_cvt_pk_bf16_f32 v204, v204, v220\n\t"
  "v_cvt_pk_bf16_f32 v205, v205, v221\n\t"
  "v_cvt_pk_bf16_f32 v206, v206, v222\n\t"
  "v_cvt_pk_bf16_f32 v207, v207, v223\n\t"
  "v_cvt_pk_bf16_f32 v208, v208, v224\n\t"
  "v_cvt_pk_bf16_f32 v209, v209, v225\n\t"
  "v_dot2_f32_bf16 v194, v194, v234, 0\n\t"
  "v_dot2_f32_bf16 v195, v195, v234, 0\n\t"
  "v_dot2_f32_bf16 v196, v196, v234, 0\n\t"
  "v_dot2_f32_bf16 v197, v197, v234, 0\n\t"
  "v_dot2_f32_bf16 v198, v198, v234, 0\n\t"
  "v_dot2_f32_bf16 v199, v199, v234, 0\n\t"
  "v_dot2_f32_bf16 v200, v200, v234, 0\n\t"
  "v_dot2_f32_bf16 v201, v201, v234, 0\n\t"
  "v_dot2_f32_bf16 v202, v202, v234, 0\n\t"
  "v_dot2_f32_bf16 v203, v203, v234, 0\n\t"
  "v_dot2_f32_bf16 v204, v204, v234, 0\n\t"
  "v_dot2_f32_bf16 v205, v205, v234, 0\n\t"
  "v_dot2_f32_bf16 v206, v206, v234, 0\n\t"
  "v_dot2_f32_bf16 v207, v207, v234, 0\n\t"
  "v_dot2_f32_bf16 v208, v208, v234, 0\n\t"
  "v_dot2_f32_bf16 v209, v209, v234, 0\n\t"
  "s_waitcnt lgkmcnt(11)\n\t"
  "v_and_b32 v210, 0xffff, v132\n\t"
  "v_and_b32 v211, 0xffff0000, v132\n\t"
  "v_and_b32 v212, 0xffff, v133\n\t"
  "v_and_b32 v213, 0xffff0000, v133\n\t"
  "s_waitcnt lgkmcnt(10)\n\t"
  "v_and_b32 v214, 0xffff, v134\n\t"
  "v_and_b32 v215, 0xffff0000, v134\n\t"
  "v_and_b32 v216, 0xffff, v135\n\t"
  "v_and_b32 v217, 0xffff0000, v135\n\t"
  "s_waitcnt lgkmcnt(9)\n\t"
  "v_and_b32 v218, 0xffff, v136\n\t"
  "v_and_b32 v219, 0xffff0000, v136\n\t"
  "v_and_b32 v220, 0xffff, v137\n\t"
  "v_and_b32 v221, 0xffff0000, v137\n\t"
  "s_waitcnt lgkmcnt(8)\n\t"
  "v_and_b32 v222, 0xffff, v138\n\t"
  "v_and_b32 v223, 0xffff0000, v138\n\t"
  "v_and_b32 v224, 0xffff, v139\n\t"
  "v_and_b32 v225, 0xffff0000, v139\n\t"
  "v_cvt_pk_bf16_f32 v194, v194, v195\n\t"
  "v_cvt_pk_bf16_f32 v196, v196, v197\n\t"
  "v_cvt_pk_bf16_f32 v198, v198, v199\n\t"
  "v_cvt_pk_bf16_f32 v200, v200, v201\n\t"
  "v_cvt_pk_bf16_f32 v202, v202, v203\n\t"
  "v_cvt_pk_bf16_f32 v204, v204, v205\n\t"
  "v_cvt_pk_bf16_f32 v206, v206, v207\n\t"
  "v_cvt_pk_bf16_f32 v208, v208, v209\n\t"
  "v_dot2_f32_bf16 v210, v194, v210, 0\n\t"
  "v_dot2_f32_bf16 v211, v194, v211, 0\n\t"
  "v_dot2_f32_bf16 v212, v196, v212, 0\n\t"
  "v_dot2_f32_bf16 v213, v196, v213, 0\n\t"
  "v_dot2_f32_bf16 v214, v198, v214, 0\n\t"
  "v_dot2_f32_bf16 v215, v198, v215, 0\n\t"
  "v_dot2_f32_bf16 v216, v200, v216, 0\n\t"
  "v_dot2_f32_bf16 v217, v200, v217, 0\n\t"
  "v_dot2_f32_bf16 v218, v202, v218, 0\n\t"
  "v_dot2_f32_bf16 v219, v202, v219, 0\n\t"
  "v_dot2_f32_bf16 v220, v204, v220, 0\n\t"
  "v_dot2_f32_bf16 v221, v204, v221, 0\n\t"
  "v_dot2_f32_bf16 v222, v206, v222, 0\n\t"
  "v_dot2_f32_bf16 v223, v206, v223, 0\n\t"
  "v_dot2_f32_bf16 v224, v208, v224, 0\n\t"
  "v_dot2_f32_bf16 v225, v208, v225, 0\n\t"
  "v_cvt_pk_bf16_f32 v229, v212, v210\n\t"
  "v_cvt_pk_bf16_f32 v228, v216, v214\n\t"
  "v_cvt_pk_bf16_f32 v227, v220, v218\n\t"
  "v_cvt_pk_bf16_f32 v226, v224, v222\n\t"
  "buffer_store_dwordx4 v[226:229], %[vo], %[srd], 0 offen offset:0 sc1\n\t"
  "v_cvt_pk_bf16_f32 v233, v213, v211\n\t"
  "v_cvt_pk_bf16_f32 v232, v217, v215\n\t"
  "v_cvt_pk_bf16_f32 v231, v221, v219\n\t"
  "v_cvt_pk_bf16_f32 v230, v225, v223\n\t"
  "buffer_store_dwordx4 v[230:233], %[vo], %[srd], 0 offen offset:128 sc1\n\t"
  "s_waitcnt lgkmcnt(7)\n\t"
  "v_lshlrev_b32 v170, 16, v124\n\t"
  "v_and_b32 v171, 0xffff0000, v124\n\t"
  "v_lshlrev_b32 v172, 16, v125\n\t"
  "v_and_b32 v173, 0xffff0000, v125\n\t"
  "s_waitcnt lgkmcnt(6)\n\t"
  "v_lshlrev_b32 v174, 16, v126\n\t"
  "v_and_b32 v175, 0xffff0000, v126\n\t"
  "v_lshlrev_b32 v176, 16, v127\n\t"
  "v_and_b32 v177, 0xffff0000, v127\n\t"
  "s_waitcnt lgkmcnt(5)\n\t"
  "v_lshlrev_b32 v178, 16, v128\n\t"
  "v_and_b32 v179, 0xffff0000, v128\n\t"
  "v_lshlrev_b32 v180, 16, v129\n\t"
  "v_and_b32 v181, 0xffff0000, v129\n\t"
  "s_waitcnt lgkmcnt(4)\n\t"
  "v_lshlrev_b32 v182, 16, v130\n\t"
  "v_and_b32 v183, 0xffff0000, v130\n\t"
  "v_lshlrev_b32 v184, 16, v131\n\t"
  "v_and_b32 v185, 0xffff0000, v131\n\t"
  "v_pk_mul_f32 v[194:195], v[164:165], v[192:193]\n\t"
  "v_pk_mul_f32 v[196:197], v[166:167], v[192:193]\n\t"
  "v_pk_mul_f32 v[198:199], v[168:169], v[192:193]\n\t"
  "v_pk_mul_f32 v[200:201], v[170:171], v[192:193]\n\t"
  "v_pk_mul_f32 v[202:203], v[172:173], v[192:193]\n\t"
  "v_pk_mul_f32 v[204:205], v[174:175], v[192:193]\n\t"
  "v_pk_mul_f32 v[206:207], v[176:177], v[192:193]\n\t"
  "v_pk_mul_f32 v[208:209], v[178:179], v[192:193]\n\t"
  "v_pk_mul_f32 v[210:211], v[166:167], v[190:191]\n\t"
  "v_pk_mul_f32 v[212:213], v[168:169], v[190:191]\n\t"
  "v_pk_mul_f32 v[214:215], v[170:171], v[190:191]\n\t"
  "v_pk_mul_f32 v[216:217], v[172:173], v[190:191]\n\t"
  "v_pk_mul_f32 v[218:219], v[174:175], v[190:191]\n\t"
  "v_pk_mul_f32 v[220:221], v[176:177], v[190:191]\n\t"
  "v_pk_mul_f32 v[222:223], v[178:179], v[190:191]\n\t"
  "v_pk_mul_f32 v[224:225], v[180:181], v[190:191]\n\t"
  "v_cvt_pk_bf16_f32 v194, v194, v210\n\t"
  "v_cvt_pk_bf16_f32 v195, v195, v211\n\t"
  "v_cvt_pk_bf16_f32 v196, v196, v212\n\t"
  "v_cvt_pk_bf16_f32 v197, v197, v213\n\t"
  "v_cvt_pk_bf16_f32 v198, v198, v214\n\t"
  "v_cvt_pk_bf16_f32 v199, v199, v215\n\t"
  "v_cvt_pk_bf16_f32 v200, v200, v216\n\t"
  "v_cvt_pk_bf16_f32 v201, v201, v217\n\t"
  "v_cvt_pk_bf16_f32 v202, v202, v218\n\t"
  "v_cvt_pk_bf16_f32 v203, v203, v219\n\t"
  "v_cvt_pk_bf16_f32 v204, v204, v220\n\t"
  "v_cvt_pk_bf16_f32 v205, v205, v221\n\t"
  "v_cvt_pk_bf16_f32 v206, v206, v222\n\t"
  "v_cvt_pk_bf16_f32 v207, v207, v223\n\t"
  "v_cvt_pk_bf16_f32 v208, v208, v224\n\t"
  "v_cvt_pk_bf16_f32 v209, v209, v225\n\t"
  "v_dot2_f32_bf16 v194, v194, v234, 0\n\t"
  "v_dot2_f32_bf16 v195, v195, v234, 0\n\t"
  "v_dot2_f32_bf16 v196, v196, v234, 0\n\t"
  "v_dot2_f32_bf16 v197, v197, v234, 0\n\t"
  "v_dot2_f32_bf16 v198, v198, v234, 0\n\t"
  "v_dot2_f32_bf16 v199, v199, v234, 0\n\t"
  "v_dot2_f32_bf16 v200, v200, v234, 0\n\t"
  "v_dot2_f32_bf16 v201, v201, v234, 0\n\t"
  "v_dot2_f32_bf16 v202, v202, v234, 0\n\t"
  "v_dot2_f32_bf16 v203, v203, v234, 0\n\t"
  "v_dot2_f32_bf16 v204, v204, v234, 0\n\t"
  "v_dot2_f32_bf16 v205, v205, v234, 0\n\t"
  "v_dot2_f32_bf16 v206, v206, v234, 0\n\t"
  "v_dot2_f32_bf16 v207, v207, v234, 0\n\t"
  "v_dot2_f32_bf16 v208, v208, v234, 0\n\t"
  "v_dot2_f32_bf16 v209, v209, v234, 0\n\t"
  "v_pk_mul_f32 v[210:211], v[168:169], v[188:189]\n\t"
  "v_pk_mul_f32 v[212:213], v[170:171], v[188:189]\n\t"
  "v_pk_mul_f32 v[214:215], v[172:173], v[188:189]\n\t"
  "v_pk_mul_f32 v[216:217], v[174:175], v[188:189]\n\t"
  "v_pk_mul_f32 v[218:219], v[176:177], v[188:189]\n\t"
  "v_pk_mul_f32 v[220:221], v[178:179], v[188:189]\n\t"
  "v_pk_mul_f32 v[222:223], v[180:181], v[188:189]\n\t"
  "v_pk_mul_f32 v[224:225], v[182:183], v[188:189]\n\t"
  "v_cvt_pk_bf16_f32 v194, v194, v210\n\t"
  "v_cvt_pk_bf16_f32 v195, v195, v211\n\t"
  "v_cvt_pk_bf16_f32 v196, v196, v212\n\t"
  "v_cvt_pk_bf16_f32 v197, v197, v213\n\t"
  "v_cvt_pk_bf16_f32 v198, v198, v214\n\t"
  "v_cvt_pk_bf16_f32 v199, v199, v215\n\t"
  "v_cvt_pk_bf16_f32 v200, v200, v216\n\t"
  "v_cvt_pk_bf16_f32 v201, v201, v217\n\t"
  "v_cvt_pk_bf16_f32 v202, v202, v218\n\t"
  "v_cvt_pk_bf16_f32 v203, v203, v219\n\t"
  "v_cvt_pk_bf16_f32 v204, v204, v220\n\t"
  "v_cvt_pk_bf16_f32 v205, v205, v221\n\t"
  "v_cvt_pk_bf16_f32 v206, v206, v222\n\t"
  "v_cvt_pk_bf16_f32 v207, v207, v223\n\t"
  "v_cvt_pk_bf16_f32 v208, v208, v224\n\t"
  "v_cvt_pk_bf16_f32 v209, v209, v225\n\t"
  "v_dot2_f32_bf16 v194, v194, v234, 0\n\t"
  "v_dot2_f32_bf16 v195, v195, v234, 0\n\t"
  "v_dot2_f32_bf16 v196, v196, v234, 0\n\t"
  "v_dot2_f32_bf16 v197, v197, v234, 0\n\t"
  "v_dot2_f32_bf16 v198, v198, v234, 0\n\t"
  "v_dot2_f32_bf16 v199, v199, v234, 0\n\t"
  "v_dot2_f32_bf16 v200, v200, v234, 0\n\t"
  "v_dot2_f32_bf16 v201, v201, v234, 0\n\t"
  "v_dot2_f32_bf16 v202, v202, v234, 0\n\t"
  "v_dot2_f32_bf16 v203, v203, v234, 0\n\t"
  "v_dot2_f32_bf16 v204, v204, v234, 0\n\t"
  "v_dot2_f32_bf16 v205, v205, v234, 0\n\t"
  "v_dot2_f32_bf16 v206, v206, v234, 0\n\t"
  "v_dot2_f32_bf16 v207, v207, v234, 0\n\t"
  "v_dot2_f32_bf16 v208, v208, v234, 0\n\t"
  "v_dot2_f32_bf16 v209, v209, v234, 0\n\t"
  "v_pk_mul_f32 v[210:211], v[170:171], v[186:187]\n\t"
  "v_pk_mul_f32 v[212:213], v[172:173], v[186:187]\n\t"
  "v_pk_mul_f32 v[214:215], v[174:175], v[186:187]\n\t"
  "v_pk_mul_f32 v[216:217], v[176:177], v[186:187]\n\t"
  "v_pk_mul_f32 v[218:219], v[178:179], v[186:187]\n\t"
  "v_pk_mul_f32 v[220:221], v[180:181], v[186:187]\n\t"
  "v_pk_mul_f32 v[222:223], v[182:183], v[186:187]\n\t"
  "v_pk_mul_f32 v[224:225], v[184:185], v[186:187]\n\t"
  "v_cvt_pk_bf16_f32 v194, v194, v210\n\t"
  "v_cvt_pk_bf16_f32 v195, v195, v211\n\t"
  "v_cvt_pk_bf16_f32 v196, v196, v212\n\t"
  "v_cvt_pk_bf16_f32 v197, v197, v213\n\t"
  "v_cvt_pk_bf16_f32 v198, v198, v214\n\t"
  "v_cvt_pk_bf16_f32 v199, v199, v215\n\t"
  "v_cvt_pk_bf16_f32 v200, v200, v216\n\t"
  "v_cvt_pk_bf16_f32 v201, v201, v217\n\t"
  "v_cvt_pk_bf16_f32 v202, v202, v218\n\t"
  "v_cvt_pk_bf16_f32 v203, v203, v219\n\t"
  "v_cvt_pk_bf16_f32 v204, v204, v220\n\t"
  "v_cvt_pk_bf16_f32 v205, v205, v221\n\t"
  "v_cvt_pk_bf16_f32 v206, v206, v222\n\t"
  "v_cvt_pk_bf16_f32 v207, v207, v223\n\t"
  "v_cvt_pk_bf16_f32 v208, v208, v224\n\t"
  "v_cvt_pk_bf16_f32 v209, v209, v225\n\t"
  "v_dot2_f32_bf16 v194, v194, v234, 0\n\t"
  "v_dot2_f32_bf16 v195, v195, v234, 0\n\t"
  "v_dot2_f32_bf16 v196, v196, v234, 0\n\t"
  "v_dot2_f32_bf16 v197, v197, v234, 0\n\t"
  "v_dot2_f32_bf16 v198, v198, v234, 0\n\t"
  "v_dot2_f32_bf16 v199, v199, v234, 0\n\t"
  "v_dot2_f32_bf16 v200, v200, v234, 0\n\t"
  "v_dot2_f32_bf16 v201, v201, v234, 0\n\t"
  "v_dot2_f32_bf16 v202, v202, v234, 0\n\t"
  "v_dot2_f32_bf16 v203, v203, v234, 0\n\t"
  "v_dot2_f32_bf16 v204, v204, v234, 0\n\t"
  "v_dot2_f32_bf16 v205, v205, v234, 0\n\t"
  "v_dot2_f32_bf16 v206, v206, v234, 0\n\t"
  "v_dot2_f32_bf16 v207, v207, v234, 0\n\t"
  "v_dot2_f32_bf16 v208, v208, v234, 0\n\t"
  "v_dot2_f32_bf16 v209, v209, v234, 0\n\t"
  "s_waitcnt lgkmcnt(3)\n\t"
  "v_and_b32 v210, 0xffff, v140\n\t"
  "v_and_b32 v211, 0xffff0000, v140\n\t"
  "v_and_b32 v212, 0xffff, v141\n\t"
  "v_and_b32 v213, 0xffff0000, v141\n\t"
  "s_waitcnt lgkmcnt(2)\n\t"
  "v_and_b32 v214, 0xffff, v142\n\t"
  "v_and_b32 v215, 0xffff0000, v142\n\t"
  "v_and_b32 v216, 0xffff, v143\n\t"
  "v_and_b32 v217, 0xffff0000, v143\n\t"
  "s_waitcnt lgkmcnt(1)\n\t"
  "v_and_b32 v218, 0xffff, v144\n\t"
  "v_and_b32 v219, 0xffff0000, v144\n\t"
  "v_and_b32 v220, 0xffff, v145\n\t"
  "v_and_b32 v221, 0xffff0000, v145\n\t"
  "s_waitcnt lgkmcnt(0)\n\t"
  "v_and_b32 v222, 0xffff, v146\n\t"
  "v_and_b32 v223, 0xffff0000, v146\n\t"
  "v_and_b32 v224, 0xffff, v147\n\t"
  "v_and_b32 v225, 0xffff0000, v147\n\t"
  "v_cvt_pk_bf16_f32 v194, v194, v195\n\t"
  "v_cvt_pk_bf16_f32 v196, v196, v197\n\t"
  "v_cvt_pk_bf16_f32 v198, v198, v199\n\t"
  "v_cvt_pk_bf16_f32 v200, v200, v201\n\t"
  "v_cvt_pk_bf16_f32 v202, v202, v203\n\t"
  "v_cvt_pk_bf16_f32 v204, v204, v205\n\t"
  "v_cvt_pk_bf16_f32 v206, v206, v207\n\t"
  "v_cvt_pk_bf16_f32 v208, v208, v209\n\t"
  "v_dot2_f32_bf16 v210, v194, v210, 0\n\t"
  "v_dot2_f32_bf16 v211, v194, v211, 0\n\t"
  "v_dot2_f32_bf16 v212, v196, v212, 0\n\t"
  "v_dot2_f32_bf16 v213, v196, v213, 0\n\t"
  "v_dot2_f32_bf16 v214, v198, v214, 0\n\t"
  "v_dot2_f32_bf16 v215, v198, v215, 0\n\t"
  "v_dot2_f32_bf16 v216, v200, v216, 0\n\t"
  "v_dot2_f32_bf16 v217, v200, v217, 0\n\t"
  "v_dot2_f32_bf16 v218, v202, v218, 0\n\t"
  "v_dot2_f32_bf16 v219, v202, v219, 0\n\t"
  "v_dot2_f32_bf16 v220, v204, v220, 0\n\t"
  "v_dot2_f32_bf16 v221, v204, v221, 0\n\t"
  "v_dot2_f32_bf16 v222, v206, v222, 0\n\t"
  "v_dot2_f32_bf16 v223, v206, v223, 0\n\t"
  "v_dot2_f32_bf16 v224, v208, v224, 0\n\t"
  "v_dot2_f32_bf16 v225, v208, v225, 0\n\t"
  "v_cvt_pk_bf16_f32 v229, v212, v210\n\t"
  "v_cvt_pk_bf16_f32 v228, v216, v214\n\t"
  "v_cvt_pk_bf16_f32 v227, v220, v218\n\t"
  "v_cvt_pk_bf16_f32 v226, v224, v222\n\t"
  "buffer_store_dwordx4 v[226:229], %[vo], %[srd], 0 offen offset:64 sc1\n\t"
  "v_cvt_pk_bf16_f32 v233, v213, v211\n\t"
  "v_cvt_pk_bf16_f32 v232, v217, v215\n\t"
  "v_cvt_pk_bf16_f32 v231, v221, v219\n\t"
  "v_cvt_pk_bf16_f32 v230, v225, v223\n\t"
  "buffer_store_dwordx4 v[230:233], %[vo], %[srd], 0 offen offset:192 sc1\n\t"
  "s_nop 1\n\t"
      :: [srd] "s"(srd), [la] "v"(lb + lkv + lane * 4), [t0] "v"(taps[0]), [t1] "v"(taps[1]), [t2] "v"(taps[2]), [t3] "v"(taps[3]),
         [vo] "v"(OFF_KVT + (tb * 160 + 2 * (d0 >> 3)) * 128 + ((lane ^ (tb >> 1)) & 3) * 16)
      : "memory",
"v112", "v113", "v114", "v115", "v116", "v117", "v118", "v119", "v120", "v121", "v122", "v123", "v124", "v125", "v126", "v127", "v128", "v129", "v130", "v131", "v132", "v133", "v134", "v135", "v136", "v137", "v138", "v139", "v140", "v141", "v142", "v143", "v144", "v145", "v146", "v147", "v148", "v149", "v150", "v151", "v152", "v153", "v154", "v155", "v156", "v157", "v158", "v159", "v160", "v161", "v162", "v163", "v164", "v165", "v166", "v167", "v168", "v169", "v170", "v171", "v172", "v173", "v174", "v175", "v176", "v177", "v178", "v179", "v180", "v181", "v182", "v183", "v184", "v185", "v186", "v187", "v188", "v189", "v190", "v191", "v192", "v193", "v194", "v195", "v196", "v197", "v198", "v199", "v200", "v201", "v202", "v203", "v204", "v205", "v206", "v207", "v208", "v209", "v210", "v211", "v212", "v213", "v214", "v215", "v216", "v217", "v218", "v219", "v220", "v221", "v222", "v223", "v224", "v225", "v226", "v227", "v228", "v229", "v230", "v231", "v232", "v233", "v234"
    );
    STAMP(1);
    char* fcw = smem + L_FC + w * FC_CH;
    auto put = [&](unsigned u, u32x4 a, u32x2 h) {
      const u32 W[6] = {a[0], a[1], a[2], a[3], h[0], h[1]};
      u32 O[5];
#pragma unroll
      for (int i = 0; i < 5; ++i) O[i] = __builtin_amdgcn_alignbit(W[i + 1], W[i], 16);
      *(u32x4*)(fcw + 0 * FC_PITCH + u * 16) = a;
      *(u32x4*)(fcw + 1 * FC_PITCH + u * 16) = u32x4{O[0], O[1], O[2], O[3]};
      *(u32x4*)(fcw + 2 * FC_PITCH + u * 16) = u32x4{W[1], W[2], W[3], W[4]};
      *(u32x4*)(fcw + 3 * FC_PITCH + u * 16) = u32x4{O[1], O[2], O[3], O[4]};
    };
#pragma unroll
    for (int n = 0; n < 4; ++n) {
      u32x2 h;
#pragma unroll
      for (int e = 0; e < 2; ++e) h[e] = __builtin_amdgcn_update_dpp(n < 3 ? rfl(fa[n < 3 ? n + 1 : n][e]) : 0u, fa[n][e], 0x130, 0xf, 0xf, false);
      put(lane + 64 * n + 2, fa[n], h);
    }
    if (lane < 2) put(1 - lane, u32x4{0, 0, 0, 0}, lane == 0 ? u32x2{fa[0][0], fa[0][1]} : u32x2{0, 0});
    FENCE();
#pragma unroll
    for (int n = 0; n < 5; ++n) *(u32x4*)(smem + L_XR + n * 4096 + w * 1024 + lane * 16) = xr[n];
    if (w == 1) *(u32x4*)(smem + L_XR + 20480 + lane * 16) = cr;
    FENCE();
    asm volatile("s_waitcnt vmcnt(0) lgkmcnt(0)\n\ts_barrier" ::: "memory");
    __builtin_amdgcn_raw_buffer_store_b128(u32x4{(u32)tag, (u32)(tag >> 32), (u32)tag, (u32)(tag >> 32)}, srd, tid < 8 ? OFF_FA1 + cb * 128 + tid * 16 : OOB, 0, SC1);
    FENCE();
    STAMP(2);
    if (isx) {
      const unsigned g = lane >> 4, q = (lane >> 2) & 3, ch = 16 * (g >> 1) + (lane & 15);
      asm volatile(
  "s_nop 4\n\t"
  "s_add_u32 s44, %[w], 1\n\t"
  "s_and_b32 s44, s44, 2\n\t"
  "s_cbranch_scc0 Lxs%=\n\t"
  "ds_read_b64_tr_b16 v[96:97], %[vtx] offset:0\n\t"
  "ds_read_b64_tr_b16 v[98:99], %[vtx] offset:512\n\t"
  "ds_read_b64_tr_b16 v[100:101], %[vtx] offset:64\n\t"
  "ds_read_b64_tr_b16 v[102:103], %[vtx] offset:576\n\t"
  "ds_read_b64_tr_b16 v[104:105], %[vtx] offset:2048\n\t"
  "ds_read_b64_tr_b16 v[106:107], %[vtx] offset:2560\n\t"
  "ds_read_b64_tr_b16 v[108:109], %[vtx] offset:2112\n\t"
  "ds_read_b64_tr_b16 v[110:111], %[vtx] offset:2624\n\t"
  "ds_read_b64_tr_b16 v[112:113], %[vtx] offset:4096\n\t"
  "ds_read_b64_tr_b16 v[114:115], %[vtx] offset:4608\n\t"
  "ds_read_b64_tr_b16 v[116:117], %[vtx] offset:4160\n\t"
  "ds_read_b64_tr_b16 v[118:119], %[vtx] offset:4672\n\t"
  "ds_read_b64_tr_b16 v[120:121], %[vtx] offset:6144\n\t"
  "ds_read_b64_tr_b16 v[122:123], %[vtx] offset:6656\n\t"
  "ds_read_b64_tr_b16 v[124:125], %[vtx] offset:6208\n\t"
  "ds_read_b64_tr_b16 v[126:127], %[vtx] offset:6720\n\t"
  "s_add_u32 s44, %[sXo], 0\n\t"
  "s_waitcnt lgkmcnt(14)\n\t"
  "buffer_store_dwordx4 v[96:99], %[vxo], %[srd], s44 offen offset:0 sc1\n\t"
  "ds_read_b64_tr_b16 v[128:129], %[vtx] offset:8192\n\t"
  "ds_read_b64_tr_b16 v[130:131], %[vtx] offset:8704\n\t"
  "s_waitcnt lgkmcnt(14)\n\t"
  "buffer_store_dwordx4 v[100:103], %[vxo], %[srd], s44 offen offset:1024 sc1\n\t"
  "ds_read_b64_tr_b16 v[132:133], %[vtx] offset:8256\n\t"
  "ds_read_b64_tr_b16 v[134:135], %[vtx] offset:8768\n\t"
  "s_waitcnt lgkmcnt(14)\n\t"
  "buffer_store_dwordx4 v[104:107], %[vxo], %[srd], s44 offen offset:2048 sc1\n\t"
  "s_waitcnt lgkmcnt(12)\n\t"
  "buffer_store_dwordx4 v[108:111], %[vxo], %[srd], s44 offen offset:3072 sc1\n\t"
  "s_add_u32 s44, %[sXo], 4096\n\t"
  "s_waitcnt lgkmcnt(10)\n\t"
  "buffer_store_dwordx4 v[112:115], %[vxo], %[srd], s44 offen offset:0 sc1\n\t"
  "s_waitcnt lgkmcnt(8)\n\t"
  "buffer_store_dwordx4 v[116:119], %[vxo], %[srd], s44 offen offset:1024 sc1\n\t"
  "s_waitcnt lgkmcnt(6)\n\t"
  "buffer_store_dwordx4 v[120:123], %[vxo], %[srd], s44 offen offset:2048 sc1\n\t"
  "s_waitcnt lgkmcnt(4)\n\t"
  "buffer_store_dwordx4 v[124:127], %[vxo], %[srd], s44 offen offset:3072 sc1\n\t"
  "s_add_u32 s44, %[sXo], 8192\n\t"
  "s_waitcnt lgkmcnt(2)\n\t"
  "buffer_store_dwordx4 v[128:131], %[vxo], %[srd], s44 offen offset:0 sc1\n\t"
  "s_waitcnt lgkmcnt(0)\n\t"
  "buffer_store_dwordx4 v[132:135], %[vxo], %[srd], s44 offen offset:1024 sc1\n\t"
  "s_branch Lxe%=\n\t"
  "Lxs%=:\n\t"
  "ds_read_b128 v[96:99], %[vsx] offset:0\n\t"
  "ds_read_b128 v[100:103], %[vsx] offset:2048\n\t"
  "ds_read_b128 v[104:107], %[vsx] offset:4096\n\t"
  "ds_read_b128 v[108:111], %[vsx] offset:6144\n\t"
  "ds_read_b128 v[112:115], %[vsx] offset:8192\n\t"
  "ds_read_b128 v[116:119], %[vsx] offset:10240\n\t"
  "ds_read_b128 v[120:123], %[vsx] offset:12288\n\t"
  "ds_read_b128 v[124:127], %[vsx] offset:14336\n\t"
  "ds_read_b128 v[128:131], %[vsx] offset:16384\n\t"
  "ds_read_b128 v[132:135], %[vsx] offset:18432\n\t"
  "v_mov_b32 v184, 0\n\t"
  "v_mov_b32 v185, 0\n\t"
  "v_mov_b32 v186, 0x3f803f80\n\t"
  "s_waitcnt lgkmcnt(9)\n\t"
  "v_dot2c_f32_bf16 v184, v96, v186\n\t"
  "v_dot2c_f32_bf16 v185, v96, v96\n\t"
  "v_dot2c_f32_bf16 v184, v97, v186\n\t"
  "v_dot2c_f32_bf16 v185, v97, v97\n\t"
  "v_dot2c_f32_bf16 v184, v98, v186\n\t"
  "v_dot2c_f32_bf16 v185, v98, v98\n\t"
  "v_dot2c_f32_bf16 v184, v99, v186\n\t"
  "v_dot2c_f32_bf16 v185, v99, v99\n\t"
  "s_waitcnt lgkmcnt(8)\n\t"
  "v_dot2c_f32_bf16 v184, v100, v186\n\t"
  "v_dot2c_f32_bf16 v185, v100, v100\n\t"
  "v_dot2c_f32_bf16 v184, v101, v186\n\t"
  "v_dot2c_f32_bf16 v185, v101, v101\n\t"
  "v_dot2c_f32_bf16 v184, v102, v186\n\t"
  "v_dot2c_f32_bf16 v185, v102, v102\n\t"
  "v_dot2c_f32_bf16 v184, v103, v186\n\t"
  "v_dot2c_f32_bf16 v185, v103, v103\n\t"
  "s_waitcnt lgkmcnt(7)\n\t"
  "v_dot2c_f32_bf16 v184, v104, v186\n\t"
  "v_dot2c_f32_bf16 v185, v104, v104\n\t"
  "v_dot2c_f32_bf16 v184, v105, v186\n\t"
  "v_dot2c_f32_bf16 v185, v105, v105\n\t"
  "v_dot2c_f32_bf16 v184, v106, v186\n\t"
  "v_dot2c_f32_bf16 v185, v106, v106\n\t"
  "v_dot2c_f32_bf16 v184, v107, v186\n\t"
  "v_dot2c_f32_bf16 v185, v107, v107\n\t"
  "s_waitcnt lgkmcnt(6)\n\t"
  "v_dot2c_f32_bf16 v184, v108, v186\n\t"
  "v_dot2c_f32_bf16 v185, v108, v108\n\t"
  "v_dot2c_f32_bf16 v184, v109, v186\n\t"
  "v_dot2c_f32_bf16 v185, v109, v109\n\t"
  "v_dot2c_f32_bf16 v184, v110, v186\n\t"
  "v_dot2c_f32_bf16 v185, v110, v110\n\t"
  "v_dot2c_f32_bf16 v184, v111, v186\n\t"
  "v_dot2c_f32_bf16 v185, v111, v111\n\t"
  "s_waitcnt lgkmcnt(5)\n\t"
  "v_dot2c_f32_bf16 v184, v112, v186\n\t"
  "v_dot2c_f32_bf16 v185, v112, v112\n\t"
  "v_dot2c_f32_bf16 v184, v113, v186\n\t"
  "v_dot2c_f32_bf16 v185, v113, v113\n\t"
  "v_dot2c_f32_bf16 v184, v114, v186\n\t"
  "v_dot2c_f32_bf16 v185, v114, v114\n\t"
  "v_dot2c_f32_bf16 v184, v115, v186\n\t"
  "v_dot2c_f32_bf16 v185, v115, v115\n\t"
  "s_waitcnt lgkmcnt(4)\n\t"
  "v_dot2c_f32_bf16 v184, v116, v186\n\t"
  "v_dot2c_f32_bf16 v185, v116, v116\n\t"
  "v_dot2c_f32_bf16 v184, v117, v186\n\t"
  "v_dot2c_f32_bf16 v185, v117, v117\n\t"
  "v_dot2c_f32_bf16 v184, v118, v186\n\t"
  "v_dot2c_f32_bf16 v185, v118, v118\n\t"
  "v_dot2c_f32_bf16 v184, v119, v186\n\t"
  "v_dot2c_f32_bf16 v185, v119, v119\n\t"
  "s_waitcnt lgkmcnt(3)\n\t"
  "v_dot2c_f32_bf16 v184, v120, v186\n\t"
  "v_dot2c_f32_bf16 v185, v120, v120\n\t"
  "v_dot2c_f32_bf16 v184, v121, v186\n\t"
  "v_dot2c_f32_bf16 v185, v121, v121\n\t"
  "v_dot2c_f32_bf16 v184, v122, v186\n\t"
  "v_dot2c_f32_bf16 v185, v122, v122\n\t"
  "v_dot2c_f32_bf16 v184, v123, v186\n\t"
  "v_dot2c_f32_bf16 v185, v123, v123\n\t"
  "s_waitcnt lgkmcnt(2)\n\t"
  "v_dot2c_f32_bf16 v184, v124, v186\n\t"
  "v_dot2c_f32_bf16 v185, v124, v124\n\t"
  "v_dot2c_f32_bf16 v184, v125, v186\n\t"
  "v_dot2c_f32_bf16 v185, v125, v125\n\t"
  "v_dot2c_f32_bf16 v184, v126, v186\n\t"
  "v_dot2c_f32_bf16 v185, v126, v126\n\t"
  "v_dot2c_f32_bf16 v184, v127, v186\n\t"
  "v_dot2c_f32_bf16 v185, v127, v127\n\t"
  "s_waitcnt lgkmcnt(1)\n\t"
  "v_dot2c_f32_bf16 v184, v128, v186\n\t"
  "v_dot2c_f32_bf16 v185, v128, v128\n\t"
  "v_dot2c_f32_bf16 v184, v129, v186\n\t"
  "v_dot2c_f32_bf16 v185, v129, v129\n\t"
  "v_dot2c_f32_bf16 v184, v130, v186\n\t"
  "v_dot2c_f32_bf16 v185, v130, v130\n\t"
  "v_dot2c_f32_bf16 v184, v131, v186\n\t"
  "v_dot2c_f32_bf16 v185, v131, v131\n\t"
  "s_waitcnt lgkmcnt(0)\n\t"
  "v_dot2c_f32_bf16 v184, v132, v186\n\t"
  "v_dot2c_f32_bf16 v185, v132, v132\n\t"
  "v_dot2c_f32_bf16 v184, v133, v186\n\t"
  "v_dot2c_f32_bf16 v185, v133, v133\n\t"
  "v_dot2c_f32_bf16 v184, v134, v186\n\t"
  "v_dot2c_f32_bf16 v185, v134, v134\n\t"
  "v_dot2c_f32_bf16 v184, v135, v186\n\t"
  "v_dot2c_f32_bf16 v185, v135, v135\n\t"
  "s_nop 1\n\t"
  "v_add_f32_dpp v184, v184, v184 quad_perm:[1,0,3,2] row_mask:0xf bank_mask:0xf\n\t"
  "v_add_f32_dpp v185, v185, v185 quad_perm:[1,0,3,2] row_mask:0xf bank_mask:0xf\n\t"
  "s_nop 0\n\t"
  "v_add_f32_dpp v184, v184, v184 quad_perm:[2,3,0,1] row_mask:0xf bank_mask:0xf\n\t"
  "v_add_f32_dpp v185, v185, v185 quad_perm:[2,3,0,1] row_mask:0xf bank_mask:0xf\n\t"
  "s_nop 0\n\t"
  "v_add_f32_dpp v184, v184, v184 row_half_mirror row_mask:0xf bank_mask:0xf\n\t"
  "v_add_f32_dpp v185, v185, v185 row_half_mirror row_mask:0xf bank_mask:0xf\n\t"
  "v_mul_f32 v184, 0xbacccccd, v184\n\t"
  "v_mul_f32 v185, 0x3acccccd, v185\n\t"
  "v_fma_f32 v185, -v184, v184, v185\n\t"
  "v_max_f32 v185, 0, v185\n\t"
  "v_cvt_pk_bf16_f32 v185, 0, v185\n\t"
  "v_cvt_pk_bf16_f32 v184, 0, v184\n\t"
  "v_add_f32 v185, 0x3727c5ac, v185\n\t"
  "v_cvt_pk_bf16_f32 v185, 0, v185\n\t"
  "v_sqrt_f32 v185, v185\n\t"
  "s_nop 0\n\t"
  "v_cvt_pk_bf16_f32 v185, 0, v185\n\t"
  "v_rcp_f32 v188, v185\n\t"
  "s_nop 0\n\t"
  "v_mul_f32 v189, v184, v188\n\t"
  "s_nop 1\n\t"
  "v_mov_b32_dpp v188, v189 quad_perm:[0,1,2,3] row_mask:0xf bank_mask:0xa\n\t"
  "buffer_store_dword v188, %[vso], %[srd], %[sSo] offen sc1\n\t"
  "Lxe%=:\n\t"
  "s_nop 1\n\t"
        :: [srd] "s"(srd), [w] "s"(w), [sSo] "s"(OFF_ST1 + cb * 128u), [sXo] "s"(OFF_XT + cb * 20480u + (w - 1) * 10240u),
           [vtx] "v"(lb + L_XR + (w - 1) * 10240 + (8 * (g & 1) + q) * 128 + (g >> 1) * 32 + (lane & 3) * 8), [vsx] "v"(lb + L_XR + (w & 1) * 1024 + lane * 16),
           [vso] "v"((lane & 3) == 0 ? (((w & 1) * 4 + g) ^ stkey(cb)) * 16 + ((lane >> 3) & 1) * 4 + (lane & 4) * 2 : OOB), [vxo] "v"((2 * (ch >> 3) + (ch & 1)) * 128 + ((((ch ^ cb) >> 1) & 3) + 4 * (g & 1)) * 16)
        : "memory", "vcc", "scc",
"s44", "v96", "v97", "v98", "v99", "v100", "v101", "v102", "v103", "v104", "v105", "v106", "v107", "v108", "v109", "v110", "v111", "v112", "v113", "v114", "v115", "v116", "v117", "v118", "v119", "v120", "v121", "v122", "v123", "v124", "v125", "v126", "v127", "v128", "v129", "v130", "v131", "v132", "v133", "v134", "v135", "v184", "v185", "v186", "v188", "v189"
      );
    } else if (w == 1) GEMV_UNIT(cb, lb + L_XR, 8);
    stash();
    STAMP(5);
  } else {
    const unsigned lu = lb + L_UN + w * UN_SZ;
    const unsigned g = lane >> 4, q = (lane >> 2) & 3, p4 = lane & 3;
    if (W_DELAY) __builtin_amdgcn_s_sleep(W_DELAY);
    if (role == 2) {
      const unsigned ui = U_MAP ? 16 * (w2 >> 2) + 4 * w + (w2 & 3) : 4 * w2 + w;
      GEMV_LOAD(ui, lu);
      const rsrc_t rd = mkrsrc(wd, D_ * F_ * 2);
      const unsigned r8 = 24 * w + (lane >> 3), so = (96 * (w2 >> 1) * D_ + 320 * (w2 & 1)) * 2;
      u32x4 raw[15];
#pragma unroll
      for (int n = 0; n < 15; ++n) raw[n] = __builtin_amdgcn_raw_buffer_load_b128(rd, r8 * (D_ * 2) + (lane & 7) * 16 + (n % 5) * 128, so + (n / 5) * 8 * D_ * 2, NT);
      FENCE();
      tag = load_tag(ws);
      GEMV_UNIT(ui, lu, 15);
      STAMP(1);
#pragma unroll
      for (int n = 0; n < 15; ++n) *(u32x4*)(smem + r8 * RP2 + (lane & 7) * 16 + (n % 5) * 128 + (n / 5) * 8 * RP2) = raw[n];
      FENCE();
      asm volatile("s_waitcnt vmcnt(0)" ::: "memory");
      put_flag(srd, U_FLAG(ui), lane, tag);
      STAMP(2);
      BAR();
#pragma unroll
      for (int n = 0; n < 15; ++n) {
        const int i = n % 5;
        const char* a = smem + (32 * (n / 5) + 8 * g + q) * RP2 + (80 * w + (i < 4 ? 32 * (i >> 1) + 8 * p4 + 4 * (i & 1) : 64 + 4 * p4)) * 2;
        const u32x2 lo = tr_read(a), hi = tr_read(a + 4 * RP2);
        raw[n] = u32x4{lo[0], lo[1], hi[0], hi[1]};
      }
      FENCE();
#pragma unroll
      for (int n = 0; n < 15; ++n)
        __builtin_amdgcn_raw_buffer_store_b128(raw[n], srd, lane * 16 + (n & 3) * 1024, OFF_W2F + ((4 * (w2 & 1) + w) * 240 + 15 * (w2 >> 1) + (n & ~3)) * 1024, SC1);
      FENCE();
      STAMP(7);
      stash();
      if (w2 < 10 && w == 0) {
        poll_flags<1, 4>(srd, OFF_FA2 + ((lane & 3) * 60 + 40 + w2) * 128, tag);
        const float sc = rbf(1.f + modval(srd, 4 * 640 + lane + 64 * w2));
        const u32 rv = __float_as_uint(sc == 0.f ? 0x1p40f : 1.f / sc);
        __builtin_amdgcn_raw_buffer_store_b32(rv, srd, lane * 4, OFF_RT + w2 * 256, SC1);
        __builtin_amdgcn_raw_buffer_store_b32(rv, srd, lane * 48, OFF_RL + (w2 < 2 ? w2 : w2 + 2) * 4, SC1);
      }
    } else {
      const rsrc_t rg = mkrsrc(wg, D_ * F_ * 2), ru = mkrsrc(wu, D_ * F_ * 2);
      const unsigned rw = 5 * w + lane / 12, ck = lane % 12, ui = 160 + 64 * w + w1;
      const unsigned vo = lane < 60 ? rw * (F_ * 2) + ck * 16 : OOB, so = (160 * (w1 & 3) * F_ + 96 * (w1 >> 2)) * 2;
      u32x4 raw[16];
#pragma unroll
      for (int n = 0; n < 8; ++n) {
        raw[2 * n] = __builtin_amdgcn_raw_buffer_load_b128(rg, vo, so + n * 20 * F_ * 2, NT);
        raw[2 * n + 1] = __builtin_amdgcn_raw_buffer_load_b128(ru, vo, so + n * 20 * F_ * 2, NT);
      }
      FENCE();
      tag = load_tag(ws);
      char* o = smem + (lane < 60 ? rw * RP1 + ck * 32 : 384);
      u32x4 fr[15];
#pragma unroll
      for (int h = 0; h < 2; ++h) {
#pragma unroll
        for (int n = h ? 5 : 0; n < (h ? 8 : 5); ++n) {
          *(u32x4*)(o + n * 20 * RP1) = u32x4{raw[2 * n][0], raw[2 * n + 1][0], raw[2 * n][1], raw[2 * n + 1][1]};
          *(u32x4*)(o + n * 20 * RP1 + (lane < 60 ? 16 : 0)) = u32x4{raw[2 * n][2], raw[2 * n + 1][2], raw[2 * n][3], raw[2 * n + 1][3]};
        }
        BAR();
#pragma unroll
        for (int n = h ? 9 : 0; n < (h ? 15 : 9); ++n) {
          const int ks = n / 3, i = n % 3;
          const char* a = smem + (32 * ks + 4 * g + q) * RP1 + (12 * w + (p4 < 3 ? 4 * p4 + i : 4 * i + 3)) * 8;
          const u32x2 lo = tr_read(a), hi = tr_read(a + 16 * RP1);
          fr[n] = u32x4{lo[0], lo[1], hi[0], hi[1]};
        }
        FENCE();
#pragma unroll
        for (int n = h ? 9 : 0; n < (h ? 15 : 9); ++n)
          __builtin_amdgcn_raw_buffer_store_b128(fr[n], srd, lane * 16 + (n & 3) * 1024, OFF_W1F + (((w1 & ~3u) + w) * 60 + 15 * (w1 & 3) + (n & ~3)) * 1024, SC1);
        FENCE();
      }
      STAMP(6);
      if (ui < 240) { GEMV_LOAD(ui, lu); asm volatile("s_waitcnt vmcnt(21)" ::: "memory"); } else asm volatile("s_waitcnt vmcnt(0)" ::: "memory");
      asm volatile("s_barrier" ::: "memory");
      if (w == 3) put_flag(srd, OFF_FW1 + w1 * 128, lane, tag);
      STAMP(7);
      if (ui < 240) {
        GEMV_UNIT(ui, lu, 0);
        asm volatile("s_waitcnt vmcnt(0)" ::: "memory");
        put_flag(srd, U_FLAG(ui), lane, tag);
        STAMP(2);
      }
      poll_flags<1, 4>(srd, OFF_FW1 + (j * 4 + (lane & 3)) * 128, tag);
      asm volatile(
  "s_nop 4\n\t"
  "s_add_u32 s44, %[sW1], 0\n\t"
  "buffer_load_dwordx4 a[0:3], %[vl16], %[srd], s44 offen offset:0 sc1\n\t"
  "buffer_load_dwordx4 a[4:7], %[vl16], %[srd], s44 offen offset:1024 sc1\n\t"
  "buffer_load_dwordx4 a[8:11], %[vl16], %[srd], s44 offen offset:2048 sc1\n\t"
  "buffer_load_dwordx4 a[12:15], %[vl16], %[srd], s44 offen offset:3072 sc1\n\t"
  "s_add_u32 s44, %[sW1], 4096\n\t"
  "buffer_load_dwordx4 a[16:19], %[vl16], %[srd], s44 offen offset:0 sc1\n\t"
  "buffer_load_dwordx4 a[20:23], %[vl16], %[srd], s44 offen offset:1024 sc1\n\t"
  "buffer_load_dwordx4 a[24:27], %[vl16], %[srd], s44 offen offset:2048 sc1\n\t"
  "buffer_load_dwordx4 a[28:31], %[vl16], %[srd], s44 offen offset:3072 sc1\n\t"
  "s_add_u32 s44, %[sW1], 8192\n\t"
  "buffer_load_dwordx4 a[32:35], %[vl16], %[srd], s44 offen offset:0 sc1\n\t"
  "buffer_load_dwordx4 a[36:39], %[vl16], %[srd], s44 offen offset:1024 sc1\n\t"
  "buffer_load_dwordx4 a[40:43], %[vl16], %[srd], s44 offen offset:2048 sc1\n\t"
  "buffer_load_dwordx4 a[44:47], %[vl16], %[srd], s44 offen offset:3072 sc1\n\t"
  "s_add_u32 s44, %[sW1], 12288\n\t"
  "buffer_load_dwordx4 a[48:51], %[vl16], %[srd], s44 offen offset:0 sc1\n\t"
  "buffer_load_dwordx4 a[52:55], %[vl16], %[srd], s44 offen offset:1024 sc1\n\t"
  "buffer_load_dwordx4 a[56:59], %[vl16], %[srd], s44 offen offset:2048 sc1\n\t"
  "buffer_load_dwordx4 a[60:63], %[vl16], %[srd], s44 offen offset:3072 sc1\n\t"
  "s_add_u32 s44, %[sW1], 16384\n\t"
  "buffer_load_dwordx4 a[64:67], %[vl16], %[srd], s44 offen offset:0 sc1\n\t"
  "buffer_load_dwordx4 a[68:71], %[vl16], %[srd], s44 offen offset:1024 sc1\n\t"
  "buffer_load_dwordx4 a[72:75], %[vl16], %[srd], s44 offen offset:2048 sc1\n\t"
  "buffer_load_dwordx4 a[76:79], %[vl16], %[srd], s44 offen offset:3072 sc1\n\t"
  "s_add_u32 s44, %[sW1], 20480\n\t"
  "buffer_load_dwordx4 a[80:83], %[vl16], %[srd], s44 offen offset:0 sc1\n\t"
  "buffer_load_dwordx4 a[84:87], %[vl16], %[srd], s44 offen offset:1024 sc1\n\t"
  "buffer_load_dwordx4 a[88:91], %[vl16], %[srd], s44 offen offset:2048 sc1\n\t"
  "buffer_load_dwordx4 a[92:95], %[vl16], %[srd], s44 offen offset:3072 sc1\n\t"
  "s_add_u32 s44, %[sW1], 24576\n\t"
  "buffer_load_dwordx4 a[96:99], %[vl16], %[srd], s44 offen offset:0 sc1\n\t"
  "buffer_load_dwordx4 a[100:103], %[vl16], %[srd], s44 offen offset:1024 sc1\n\t"
  "buffer_load_dwordx4 a[104:107], %[vl16], %[srd], s44 offen offset:2048 sc1\n\t"
  "buffer_load_dwordx4 a[108:111], %[vl16], %[srd], s44 offen offset:3072 sc1\n\t"
  "s_add_u32 s44, %[sW1], 28672\n\t"
  "buffer_load_dwordx4 a[112:115], %[vl16], %[srd], s44 offen offset:0 sc1\n\t"
  "buffer_load_dwordx4 a[116:119], %[vl16], %[srd], s44 offen offset:1024 sc1\n\t"
  "buffer_load_dwordx4 a[120:123], %[vl16], %[srd], s44 offen offset:2048 sc1\n\t"
  "buffer_load_dwordx4 a[124:127], %[vl16], %[srd], s44 offen offset:3072 sc1\n\t"
  "s_add_u32 s44, %[sW1], 32768\n\t"
  "buffer_load_dwordx4 a[128:131], %[vl16], %[srd], s44 offen offset:0 sc1\n\t"
  "buffer_load_dwordx4 a[132:135], %[vl16], %[srd], s44 offen offset:1024 sc1\n\t"
  "buffer_load_dwordx4 a[136:139], %[vl16], %[srd], s44 offen offset:2048 sc1\n\t"
  "buffer_load_dwordx4 a[140:143], %[vl16], %[srd], s44 offen offset:3072 sc1\n\t"
  "s_add_u32 s44, %[sW1], 36864\n\t"
  "buffer_load_dwordx4 a[144:147], %[vl16], %[srd], s44 offen offset:0 sc1\n\t"
  "buffer_load_dwordx4 a[148:151], %[vl16], %[srd], s44 offen offset:1024 sc1\n\t"
  "buffer_load_dwordx4 a[152:155], %[vl16], %[srd], s44 offen offset:2048 sc1\n\t"
  "buffer_load_dwordx4 a[156:159], %[vl16], %[srd], s44 offen offset:3072 sc1\n\t"
  "s_add_u32 s44, %[sW1], 40960\n\t"
  "buffer_load_dwordx4 a[160:163], %[vl16], %[srd], s44 offen offset:0 sc1\n\t"
  "buffer_load_dwordx4 a[164:167], %[vl16], %[srd], s44 offen offset:1024 sc1\n\t"
  "buffer_load_dwordx4 a[168:171], %[vl16], %[srd], s44 offen offset:2048 sc1\n\t"
  "buffer_load_dwordx4 a[172:175], %[vl16], %[srd], s44 offen offset:3072 sc1\n\t"
  "s_add_u32 s44, %[sW1], 45056\n\t"
  "buffer_load_dwordx4 a[176:179], %[vl16], %[srd], s44 offen offset:0 sc1\n\t"
  "buffer_load_dwordx4 a[180:183], %[vl16], %[srd], s44 offen offset:1024 sc1\n\t"
  "buffer_load_dwordx4 a[184:187], %[vl16], %[srd], s44 offen offset:2048 sc1\n\t"
  "buffer_load_dwordx4 a[188:191], %[vl16], %[srd], s44 offen offset:3072 sc1\n\t"
  "s_add_u32 s44, %[sW1], 49152\n\t"
  "buffer_load_dwordx4 a[192:195], %[vl16], %[srd], s44 offen offset:0 sc1\n\t"
  "buffer_load_dwordx4 a[196:199], %[vl16], %[srd], s44 offen offset:1024 sc1\n\t"
  "buffer_load_dwordx4 a[200:203], %[vl16], %[srd], s44 offen offset:2048 sc1\n\t"
  "buffer_load_dwordx4 a[204:207], %[vl16], %[srd], s44 offen offset:3072 sc1\n\t"
  "s_add_u32 s44, %[sW1], 53248\n\t"
  "buffer_load_dwordx4 a[208:211], %[vl16], %[srd], s44 offen offset:0 sc1\n\t"
  "buffer_load_dwordx4 a[212:215], %[vl16], %[srd], s44 offen offset:1024 sc1\n\t"
  "buffer_load_dwordx4 a[216:219], %[vl16], %[srd], s44 offen offset:2048 sc1\n\t"
  "buffer_load_dwordx4 a[220:223], %[vl16], %[srd], s44 offen offset:3072 sc1\n\t"
  "s_add_u32 s44, %[sW1], 57344\n\t"
  "buffer_load_dwordx4 a[224:227], %[vl16], %[srd], s44 offen offset:0 sc1\n\t"
  "buffer_load_dwordx4 a[228:231], %[vl16], %[srd], s44 offen offset:1024 sc1\n\t"
  "buffer_load_dwordx4 a[232:235], %[vl16], %[srd], s44 offen offset:2048 sc1\n\t"
  "buffer_load_dwordx4 a[236:239], %[vl16], %[srd], s44 offen offset:3072 sc1\n\t"
        :: [srd] "s"(srd), [sW1] "s"(OFF_W1F + (j * 4 + w) * 61440u), [vl16] "v"(lane * 16) : "memory", "scc",
"a0", "a1", "a2", "a3", "a4", "a5", "a6", "a7", "a8", "a9", "a10", "a11", "a12", "a13", "a14", "a15", "a16", "a17", "a18", "a19", "a20", "a21", "a22", "a23", "a24", "a25", "a26", "a27", "a28", "a29", "a30", "a31", "a32", "a33", "a34", "a35", "a36", "a37", "a38", "a39", "a40", "a41", "a42", "a43", "a44", "a45", "a46", "a47", "a48", "a49", "a50", "a51", "a52", "a53", "a54", "a55", "a56", "a57", "a58", "a59", "a60", "a61", "a62", "a63", "a64", "a65", "a66", "a67", "a68", "a69", "a70", "a71", "a72", "a73", "a74", "a75", "a76", "a77", "a78", "a79", "a80", "a81", "a82", "a83", "a84", "a85", "a86", "a87", "a88", "a89", "a90", "a91", "a92", "a93", "a94", "a95", "a96", "a97", "a98", "a99", "a100", "a101", "a102", "a103", "a104", "a105", "a106", "a107", "a108", "a109", "a110", "a111", "a112", "a113", "a114", "a115", "a116", "a117", "a118", "a119", "a120", "a121", "a122", "a123", "a124", "a125", "a126", "a127", "a128", "a129", "a130", "a131", "a132", "a133", "a134", "a135", "a136", "a137", "a138", "a139", "a140", "a141", "a142", "a143", "a144", "a145", "a146", "a147", "a148", "a149", "a150", "a151", "a152", "a153", "a154", "a155", "a156", "a157", "a158", "a159", "a160", "a161", "a162", "a163", "a164", "a165", "a166", "a167", "a168", "a169", "a170", "a171", "a172", "a173", "a174", "a175", "a176", "a177", "a178", "a179", "a180", "a181", "a182", "a183", "a184", "a185", "a186", "a187", "a188", "a189", "a190", "a191", "a192", "a193", "a194", "a195", "a196", "a197", "a198", "a199", "a200", "a201", "a202", "a203", "a204", "a205", "a206", "a207", "a208", "a209", "a210", "a211", "a212", "a213", "a214", "a215", "a216", "a217", "a218", "a219", "a220", "a221", "a222", "a223", "a224", "a225", "a226", "a227", "a228", "a229", "a230", "a231", "a232", "a233", "a234", "a235", "a236", "a237", "a238", "a239", "s44"
      );
      wdone = 7;
      stash();
      if (w > 1 || (w == 1 && w1 < 4)) {
        const unsigned c0 = (6 - w) * 640 + 160 * (w1 & 3);
        poll_flags<1, 4>(srd, OFF_FA2 + ((lane & 3) * 60 + ((c0 + min(64 * ((lane >> 2) & 3), 159u)) >> 6)) * 128, tag);
        f32x4 m[4];
#pragma unroll
        for (int k = 0; k < 4; ++k) m[k] = __builtin_bit_cast(f32x4, __builtin_amdgcn_raw_buffer_load_b128(srd, lane < 40 ? lane * 16 : OOB, OFF_MODP + k * 15360 + c0 * 4, SC1));
        f32x4 v = (m[0] + m[1]) + (m[2] + m[3]);
#pragma unroll
        for (int e = 0; e < 4; ++e) v[e] = rbf(v[e]);
        if (w == 1) __builtin_amdgcn_raw_buffer_store_b128(__builtin_bit_cast(u32x4, v), srd, lane < 40 ? lane * 16 : OOB, OFF_GB + 640 * w1, SC1);
        else {
          const float of = w == 2 ? 1.f : 0.f;
          *(u32x2*)(smem + L_WS + (w - 2) * 1024 + lane * 8) = u32x2{pack_bf16(of + v[0], of + v[1]), pack_bf16(of + v[2], of + v[3])};
        }
      }
      STAMP(5);
      BAR();
      STAMP(3);
      f32x4 aU[3], aB[3];
#pragma unroll
      for (int i = 0; i < 3; ++i) { aU[i] = f32x4{0, 0, 0, 0}; aB[i] = f32x4{0, 0, 0, 0}; }
#pragma unroll
      for (int ks = 0; ks < 5; ++ks) {
        const char* t = smem + L_WS + (32 * ks + 4 * g) * 2;
        const u32x2 s0 = *(const u32x2*)t, s1 = *(const u32x2*)(t + 32), b0 = *(const u32x2*)(t + 1024), b1 = *(const u32x2*)(t + 1056);
#pragma unroll
        for (int i = 0; i < 3; ++i) {
          mfma_v(aU[i], u32x4{s0[0], s0[1], s1[0], s1[1]}, fr[3 * ks + i]);
          mfma_v(aB[i], u32x4{b0[0], b0[1], b1[0], b1[1]}, fr[3 * ks + i]);
        }
      }
      asm volatile("s_nop 7\n\ts_nop 1" : "+v"(aU[0]), "+v"(aU[1]), "+v"(aU[2]), "+v"(aB[0]), "+v"(aB[1]), "+v"(aB[2]));
      const float u = g == 0 ? aU[0][0] : g == 1 ? aU[1][0] : aU[2][0], bb = g == 0 ? aB[0][0] : g == 1 ? aB[1][0] : aB[2][0];
      const float uh = rbf(u), bh = rbf(bb);
      __builtin_amdgcn_raw_buffer_store_b64(u32x2{pack_bf16(uh, u - uh), pack_bf16(bh, bb - bh)}, srd,
                                            g < 3 ? OFF_UB + ((((w1 & ~3u) + w) * 3 + g) * 64 + (w1 & 3) * 16 + (lane & 15)) * 8 : OOB, 0, SC1);
    }
  }
  if (role == 0) {
    const unsigned d = 8 * (cb >> 1) + 2 * w + (cb & 1);
    asm volatile(
  "s_nop 4\n\t"
  "buffer_load_dwordx2 v[160:161], %[vf1], %[srd], 0 offen sc1\n\t"
  "s_lshl_b32 s45, %[w], 12\n\t"
  "v_mov_b32 v252, %[tlo]\n\t"
  "v_mov_b32 v253, %[thi]\n\t"
  "v_mov_b32 v254, %[tlo]\n\t"
  "v_mov_b32 v255, %[thi]\n\t"
  "v_mov_b32 v162, 140288\n\t"
  "v_mov_b32 v163, 1\n\t"
  "s_mov_b64 exec, 1\n\t"
  "s_waitcnt vmcnt(1)\n\t"
  "ds_add_rtn_u32 v164, v162, v163\n\t"
  "s_waitcnt lgkmcnt(0)\n\t"
  "s_mov_b64 exec, -1\n\t"
  "v_readfirstlane_b32 s44, v164\n\t"
  "s_nop 3\n\t"
  "s_cmp_lg_u32 s44, 3\n\t"
  "s_cbranch_scc1 Lnox%=\n\t"
  "buffer_store_dwordx4 v[252:255], %[vf3], %[srd], 0 offen sc1\n\t"
  "s_waitcnt vmcnt(1)\n\t"
  "s_branch Lx%=\n\t"
  "Lnox%=:\n\t"
  "s_waitcnt vmcnt(0)\n\t"
  "Lx%=:\n\t"
  "v_cmp_ne_u64 vcc, v[252:253], v[160:161]\n\t"
  "s_nop 1\n\t"
  "s_cbranch_vccnz La1_slow%=\n\t"
  "La1_back%=:\n\t"
  "s_add_u32 m0, s45, 68608\n\t"
  "s_add_u32 s44, %[sKV], 0\n\t"
  "buffer_load_dwordx4 %[vkv], %[srd], s44 offen sc1 lds\n\t"
  "s_add_u32 m0, s45, 69632\n\t"
  "s_add_u32 s44, %[sKV], 163840\n\t"
  "buffer_load_dwordx4 %[vkv], %[srd], s44 offen sc1 lds\n\t"
  "s_add_u32 m0, s45, 70656\n\t"
  "s_add_u32 s44, %[sKV], 327680\n\t"
  "buffer_load_dwordx4 %[vkv], %[srd], s44 offen sc1 lds\n\t"
  "s_add_u32 m0, s45, 71680\n\t"
  "s_add_u32 s44, %[sKV], 491520\n\t"
  "buffer_load_dwordx4 %[vkv], %[srd], s44 offen sc1 lds\n\t"
  "ds_read_b64 v[64:65], %[va] offset:0\n\t"
  "ds_read_b64 v[66:67], %[va] offset:8\n\t"
  "ds_read_b64 v[68:69], %[va] offset:512\n\t"
  "ds_read_b64 v[70:71], %[va] offset:520\n\t"
  "ds_read_b64 v[72:73], %[va] offset:1024\n\t"
  "ds_read_b64 v[74:75], %[va] offset:1032\n\t"
  "ds_read_b64 v[76:77], %[va] offset:1536\n\t"
  "ds_read_b64 v[78:79], %[va] offset:1544\n\t"
  "ds_read_b64 v[80:81], %[va] offset:2048\n\t"
  "ds_read_b64 v[82:83], %[va] offset:2056\n\t"
  "ds_read_b64 v[84:85], %[va] offset:2560\n\t"
  "ds_read_b64 v[86:87], %[va] offset:2568\n\t"
  "ds_read_b64 v[88:89], %[va] offset:3072\n\t"
  "ds_read_b64 v[90:91], %[va] offset:3080\n\t"
  "ds_read_b64 v[92:93], %[va] offset:3584\n\t"
  "ds_read_b64 v[94:95], %[va] offset:3592\n\t"
  "s_waitcnt vmcnt(0) lgkmcnt(0)\n\t"
  "s_barrier\n\t"
  "ds_read_b128 v[124:127], %[vb0] offset:15360\n\t"
  "ds_read_b128 v[120:123], %[vb0] offset:13312\n\t"
  "ds_read_b128 v[116:119], %[vb0] offset:11264\n\t"
  "ds_read_b128 v[112:115], %[vb0] offset:9216\n\t"
  "ds_read_b128 v[108:111], %[vb0] offset:7168\n\t"
  "ds_read_b128 v[104:107], %[vb0] offset:5120\n\t"
  "ds_read_b128 v[100:103], %[vb0] offset:3072\n\t"
  "ds_read_b128 v[96:99], %[vb0] offset:1024\n\t"
  "s_waitcnt lgkmcnt(7)\n\t"
  "v_mfma_f32_16x16x32_bf16 v[60:63], v[64:67], v[124:127], 0\n\t"
  "ds_read_b128 v[156:159], %[vb1] offset:15360\n\t"
  "s_waitcnt lgkmcnt(7)\n\t"
  "v_mfma_f32_16x16x32_bf16 v[56:59], v[64:67], v[120:123], 0\n\t"
  "s_waitcnt lgkmcnt(6)\n\t"
  "v_mfma_f32_16x16x32_bf16 v[52:55], v[64:67], v[116:119], 0\n\t"
  "s_waitcnt lgkmcnt(5)\n\t"
  "v_mfma_f32_16x16x32_bf16 v[48:51], v[64:67], v[112:115], 0\n\t"
  "s_waitcnt lgkmcnt(4)\n\t"
  "v_mfma_f32_16x16x32_bf16 v[44:47], v[64:67], v[108:111], 0\n\t"
  "ds_read_b128 v[152:155], %[vb1] offset:13312\n\t"
  "s_waitcnt lgkmcnt(4)\n\t"
  "v_mfma_f32_16x16x32_bf16 v[40:43], v[64:67], v[104:107], 0\n\t"
  "s_waitcnt lgkmcnt(3)\n\t"
  "v_mfma_f32_16x16x32_bf16 v[36:39], v[64:67], v[100:103], 0\n\t"
  "s_waitcnt lgkmcnt(2)\n\t"
  "v_mfma_f32_16x16x32_bf16 v[32:35], v[64:67], v[96:99], 0\n\t"
  "v_mfma_f32_16x16x32_bf16 v[60:63], %[vd], v[124:127], v[60:63]\n\t"
  "ds_read_b128 v[148:151], %[vb1] offset:11264\n\t"
  "v_mfma_f32_16x16x32_bf16 v[56:59], %[vd], v[120:123], v[56:59]\n\t"
  "v_mfma_f32_16x16x32_bf16 v[52:55], %[vd], v[116:119], v[52:55]\n\t"
  "v_mfma_f32_16x16x32_bf16 v[48:51], %[vd], v[112:115], v[48:51]\n\t"
  "v_mfma_f32_16x16x32_bf16 v[44:47], %[vd], v[108:111], v[44:47]\n\t"
  "ds_read_b128 v[144:147], %[vb1] offset:9216\n\t"
  "v_mfma_f32_16x16x32_bf16 v[40:43], %[vd], v[104:107], v[40:43]\n\t"
  "v_mfma_f32_16x16x32_bf16 v[36:39], %[vd], v[100:103], v[36:39]\n\t"
  "v_mfma_f32_16x16x32_bf16 v[32:35], %[vd], v[96:99], v[32:35]\n\t"
  "ds_read_b64 v[64:65], %[va] offset:64\n\t"
  "ds_read_b64 v[66:67], %[va] offset:72\n\t"
  "v_mfma_f32_16x16x32_bf16 v[60:63], v[68:71], v[120:123], v[60:63]\n\t"
  "ds_read_b128 v[140:143], %[vb1] offset:7168\n\t"
  "v_mfma_f32_16x16x32_bf16 v[56:59], v[68:71], v[116:119], v[56:59]\n\t"
  "v_mfma_f32_16x16x32_bf16 v[52:55], v[68:71], v[112:115], v[52:55]\n\t"
  "v_mfma_f32_16x16x32_bf16 v[48:51], v[68:71], v[108:111], v[48:51]\n\t"
  "v_mfma_f32_16x16x32_bf16 v[44:47], v[68:71], v[104:107], v[44:47]\n\t"
  "ds_read_b128 v[136:139], %[vb1] offset:5120\n\t"
  "v_mfma_f32_16x16x32_bf16 v[40:43], v[68:71], v[100:103], v[40:43]\n\t"
  "v_mfma_f32_16x16x32_bf16 v[36:39], v[68:71], v[96:99], v[36:39]\n\t"
  "ds_read_b64 v[68:69], %[va] offset:576\n\t"
  "ds_read_b64 v[70:71], %[va] offset:584\n\t"
  "v_mfma_f32_16x16x32_bf16 v[60:63], v[72:75], v[116:119], v[60:63]\n\t"
  "v_mfma_f32_16x16x32_bf16 v[56:59], v[72:75], v[112:115], v[56:59]\n\t"
  "ds_read_b128 v[132:135], %[vb1] offset:3072\n\t"
  "buffer_load_dwordx2 v[250:251], %[vfw], %[srd], 0 offen sc1\n\t"
  "v_mfma_f32_16x16x32_bf16 v[52:55], v[72:75], v[108:111], v[52:55]\n\t"
  "v_mfma_f32_16x16x32_bf16 v[48:51], v[72:75], v[104:107], v[48:51]\n\t"
  "v_mfma_f32_16x16x32_bf16 v[44:47], v[72:75], v[100:103], v[44:47]\n\t"
  "v_mfma_f32_16x16x32_bf16 v[40:43], v[72:75], v[96:99], v[40:43]\n\t"
  "ds_read_b64 v[72:73], %[va] offset:1088\n\t"
  "ds_read_b128 v[128:131], %[vb1] offset:1024\n\t"
  "v_mfma_f32_16x16x32_bf16 v[60:63], v[76:79], v[112:115], v[60:63]\n\t"
  "ds_read_b64 v[74:75], %[va] offset:1096\n\t"
  "v_mfma_f32_16x16x32_bf16 v[56:59], v[76:79], v[108:111], v[56:59]\n\t"
  "v_mfma_f32_16x16x32_bf16 v[52:55], v[76:79], v[104:107], v[52:55]\n\t"
  "v_mfma_f32_16x16x32_bf16 v[48:51], v[76:79], v[100:103], v[48:51]\n\t"
  "v_mfma_f32_16x16x32_bf16 v[44:47], v[76:79], v[96:99], v[44:47]\n\t"
  "ds_read_b64 v[76:77], %[va] offset:1600\n\t"
  "ds_read_b64 v[78:79], %[va] offset:1608\n\t"
  "v_mfma_f32_16x16x32_bf16 v[60:63], v[80:83], v[108:111], v[60:63]\n\t"
  "v_mfma_f32_16x16x32_bf16 v[56:59], v[80:83], v[104:107], v[56:59]\n\t"
  "v_mfma_f32_16x16x32_bf16 v[52:55], v[80:83], v[100:103], v[52:55]\n\t"
  "v_mfma_f32_16x16x32_bf16 v[48:51], v[80:83], v[96:99], v[48:51]\n\t"
  "ds_read_b64 v[80:81], %[va] offset:2112\n\t"
  "ds_read_b64 v[82:83], %[va] offset:2120\n\t"
  "v_mfma_f32_16x16x32_bf16 v[60:63], v[84:87], v[104:107], v[60:63]\n\t"
  "v_mfma_f32_16x16x32_bf16 v[56:59], v[84:87], v[100:103], v[56:59]\n\t"
  "v_mfma_f32_16x16x32_bf16 v[52:55], v[84:87], v[96:99], v[52:55]\n\t"
  "ds_read_b64 v[84:85], %[va] offset:2624\n\t"
  "ds_read_b64 v[86:87], %[va] offset:2632\n\t"
  "v_mfma_f32_16x16x32_bf16 v[60:63], v[88:91], v[100:103], v[60:63]\n\t"
  "v_mfma_f32_16x16x32_bf16 v[56:59], v[88:91], v[96:99], v[56:59]\n\t"
  "ds_read_b64 v[88:89], %[va] offset:3136\n\t"
  "ds_read_b64 v[90:91], %[va] offset:3144\n\t"
  "v_mfma_f32_16x16x32_bf16 v[60:63], v[92:95], v[96:99], v[60:63]\n\t"
  "s_waitcnt lgkmcnt(9)\n\t"
  "ds_read_b64 v[92:93], %[va] offset:3648\n\t"
  "v_mfma_f32_16x16x32_bf16 v[60:63], v[64:67], v[156:159], v[60:63]\n\t"
  "ds_read_b128 v[124:127], %[vb2] offset:15360\n\t"
  "ds_read_b64 v[94:95], %[va] offset:3656\n\t"
  "v_mfma_f32_16x16x32_bf16 v[56:59], v[64:67], v[152:155], v[56:59]\n\t"
  "v_mfma_f32_16x16x32_bf16 v[52:55], v[64:67], v[148:151], v[52:55]\n\t"
  "v_mfma_f32_16x16x32_bf16 v[48:51], v[64:67], v[144:147], v[48:51]\n\t"
  "ds_read_b128 v[120:123], %[vb2] offset:13312\n\t"
  "v_mfma_f32_16x16x32_bf16 v[44:47], v[64:67], v[140:143], v[44:47]\n\t"
  "v_mfma_f32_16x16x32_bf16 v[40:43], v[64:67], v[136:139], v[40:43]\n\t"
  "v_mfma_f32_16x16x32_bf16 v[36:39], v[64:67], v[132:135], v[36:39]\n\t"
  "ds_read_b128 v[116:119], %[vb2] offset:11264\n\t"
  "v_mfma_f32_16x16x32_bf16 v[32:35], v[64:67], v[128:131], v[32:35]\n\t"
  "ds_read_b64 v[64:65], %[va] offset:128\n\t"
  "ds_read_b64 v[66:67], %[va] offset:136\n\t"
  "v_mfma_f32_16x16x32_bf16 v[60:63], v[68:71], v[152:155], v[60:63]\n\t"
  "v_mfma_f32_16x16x32_bf16 v[56:59], v[68:71], v[148:151], v[56:59]\n\t"
  "ds_read_b128 v[112:115], %[vb2] offset:9216\n\t"
  "v_mfma_f32_16x16x32_bf16 v[52:55], v[68:71], v[144:147], v[52:55]\n\t"
  "v_mfma_f32_16x16x32_bf16 v[48:51], v[68:71], v[140:143], v[48:51]\n\t"
  "v_mfma_f32_16x16x32_bf16 v[44:47], v[68:71], v[136:139], v[44:47]\n\t"
  "ds_read_b128 v[108:111], %[vb2] offset:7168\n\t"
  "v_mfma_f32_16x16x32_bf16 v[40:43], v[68:71], v[132:135], v[40:43]\n\t"
  "v_mfma_f32_16x16x32_bf16 v[36:39], v[68:71], v[128:131], v[36:39]\n\t"
  "s_waitcnt lgkmcnt(11)\n\t"
  "ds_read_b64 v[68:69], %[va] offset:640\n\t"
  "v_mfma_f32_16x16x32_bf16 v[60:63], v[72:75], v[148:151], v[60:63]\n\t"
  "ds_read_b128 v[104:107], %[vb2] offset:5120\n\t"
  "ds_read_b64 v[70:71], %[va] offset:648\n\t"
  "v_mfma_f32_16x16x32_bf16 v[56:59], v[72:75], v[144:147], v[56:59]\n\t"
  "v_mfma_f32_16x16x32_bf16 v[52:55], v[72:75], v[140:143], v[52:55]\n\t"
  "v_mfma_f32_16x16x32_bf16 v[48:51], v[72:75], v[136:139], v[48:51]\n\t"
  "ds_read_b128 v[100:103], %[vb2] offset:3072\n\t"
  "v_mfma_f32_16x16x32_bf16 v[44:47], v[72:75], v[132:135], v[44:47]\n\t"
  "v_mfma_f32_16x16x32_bf16 v[40:43], v[72:75], v[128:131], v[40:43]\n\t"
  "ds_read_b64 v[72:73], %[va] offset:1152\n\t"
  "ds_read_b64 v[74:75], %[va] offset:1160\n\t"
  "v_mfma_f32_16x16x32_bf16 v[60:63], v[76:79], v[144:147], v[60:63]\n\t"
  "ds_read_b128 v[96:99], %[vb2] offset:1024\n\t"
  "s_waitcnt vmcnt(0)\n\t"
  "v_cmp_eq_u64 s[54:55], v[252:253], v[250:251]\n\t"
  "v_mfma_f32_16x16x32_bf16 v[56:59], v[76:79], v[140:143], v[56:59]\n\t"
  "v_mfma_f32_16x16x32_bf16 v[52:55], v[76:79], v[136:139], v[52:55]\n\t"
  "v_mfma_f32_16x16x32_bf16 v[48:51], v[76:79], v[132:135], v[48:51]\n\t"
  "s_nop 0\n\t"
  "s_cmp_eq_u64 s[54:55], -1\n\t"
  "s_cselect_b32 s53, 1, 0\n\t"
  "s_cselect_b32 s56, %[sW1], 0x70000000\n\t"
  "v_mfma_f32_16x16x32_bf16 v[44:47], v[76:79], v[128:131], v[44:47]\n\t"
  "ds_read_b64 v[76:77], %[va] offset:1664\n\t"
  "ds_read_b64 v[78:79], %[va] offset:1672\n\t"
  "v_mfma_f32_16x16x32_bf16 v[60:63], v[80:83], v[140:143], v[60:63]\n\t"
  "s_mov_b32 %[wdone], s53\n\t"
  "buffer_load_dwordx4 a[0:3], %[vl16], %[srd], s56 offen offset:0 sc1\n\t"
  "v_mfma_f32_16x16x32_bf16 v[56:59], v[80:83], v[136:139], v[56:59]\n\t"
  "v_mfma_f32_16x16x32_bf16 v[52:55], v[80:83], v[132:135], v[52:55]\n\t"
  "v_mfma_f32_16x16x32_bf16 v[48:51], v[80:83], v[128:131], v[48:51]\n\t"
  "ds_read_b64 v[80:81], %[va] offset:2176\n\t"
  "ds_read_b64 v[82:83], %[va] offset:2184\n\t"
  "v_mfma_f32_16x16x32_bf16 v[60:63], v[84:87], v[136:139], v[60:63]\n\t"
  "buffer_load_dwordx4 a[4:7], %[vl16], %[srd], s56 offen offset:1024 sc1\n\t"
  "v_mfma_f32_16x16x32_bf16 v[56:59], v[84:87], v[132:135], v[56:59]\n\t"
  "v_mfma_f32_16x16x32_bf16 v[52:55], v[84:87], v[128:131], v[52:55]\n\t"
  "s_waitcnt lgkmcnt(15)\n\t"
  "ds_read_b64 v[84:85], %[va] offset:2688\n\t"
  "v_mfma_f32_16x16x32_bf16 v[60:63], v[88:91], v[132:135], v[60:63]\n\t"
  "ds_read_b64 v[86:87], %[va] offset:2696\n\t"
  "buffer_load_dwordx4 a[8:11], %[vl16], %[srd], s56 offen offset:2048 sc1\n\t"
  "v_mfma_f32_16x16x32_bf16 v[56:59], v[88:91], v[128:131], v[56:59]\n\t"
  "ds_read_b64 v[88:89], %[va] offset:3200\n\t"
  "ds_read_b64 v[90:91], %[va] offset:3208\n\t"
  "v_mfma_f32_16x16x32_bf16 v[60:63], v[92:95], v[128:131], v[60:63]\n\t"
  "s_waitcnt lgkmcnt(8)\n\t"
  "ds_read_b64 v[92:93], %[va] offset:3712\n\t"
  "v_mfma_f32_16x16x32_bf16 v[60:63], v[64:67], v[124:127], v[60:63]\n\t"
  "ds_read_b128 v[156:159], %[vb3] offset:15360\n\t"
  "ds_read_b64 v[94:95], %[va] offset:3720\n\t"
  "v_mfma_f32_16x16x32_bf16 v[56:59], v[64:67], v[120:123], v[56:59]\n\t"
  "buffer_load_dwordx4 a[12:15], %[vl16], %[srd], s56 offen offset:3072 sc1\n\t"
  "v_mfma_f32_16x16x32_bf16 v[52:55], v[64:67], v[116:119], v[52:55]\n\t"
  "v_mfma_f32_16x16x32_bf16 v[48:51], v[64:67], v[112:115], v[48:51]\n\t"
  "ds_read_b128 v[152:155], %[vb3] offset:13312\n\t"
  "v_mfma_f32_16x16x32_bf16 v[44:47], v[64:67], v[108:111], v[44:47]\n\t"
  "s_add_u32 s56, s56, 4096\n\t"
  "v_mfma_f32_16x16x32_bf16 v[40:43], v[64:67], v[104:107], v[40:43]\n\t"
  "v_mfma_f32_16x16x32_bf16 v[36:39], v[64:67], v[100:103], v[36:39]\n\t"
  "ds_read_b128 v[148:151], %[vb3] offset:11264\n\t"
  "v_mfma_f32_16x16x32_bf16 v[32:35], v[64:67], v[96:99], v[32:35]\n\t"
  "ds_read_b64 v[64:65], %[va] offset:192\n\t"
  "ds_read_b64 v[66:67], %[va] offset:200\n\t"
  "v_mfma_f32_16x16x32_bf16 v[60:63], v[68:71], v[120:123], v[60:63]\n\t"
  "buffer_load_dwordx4 a[16:19], %[vl16], %[srd], s56 offen offset:0 sc1\n\t"
  "v_mfma_f32_16x16x32_bf16 v[56:59], v[68:71], v[116:119], v[56:59]\n\t"
  "ds_read_b128 v[144:147], %[vb3] offset:9216\n\t"
  "v_mfma_f32_16x16x32_bf16 v[52:55], v[68:71], v[112:115], v[52:55]\n\t"
  "v_mfma_f32_16x16x32_bf16 v[48:51], v[68:71], v[108:111], v[48:51]\n\t"
  "buffer_load_dwordx4 a[20:23], %[vl16], %[srd], s56 offen offset:1024 sc1\n\t"
  "v_mfma_f32_16x16x32_bf16 v[44:47], v[68:71], v[104:107], v[44:47]\n\t"
  "ds_read_b128 v[140:143], %[vb3] offset:7168\n\t"
  "v_mfma_f32_16x16x32_bf16 v[40:43], v[68:71], v[100:103], v[40:43]\n\t"
  "v_mfma_f32_16x16x32_bf16 v[36:39], v[68:71], v[96:99], v[36:39]\n\t"
  "s_waitcnt lgkmcnt(11)\n\t"
  "ds_read_b64 v[68:69], %[va] offset:704\n\t"
  "v_mfma_f32_16x16x32_bf16 v[60:63], v[72:75], v[116:119], v[60:63]\n\t"
  "ds_read_b128 v[136:139], %[vb3] offset:5120\n\t"
  "ds_read_b64 v[70:71], %[va] offset:712\n\t"
  "v_mfma_f32_16x16x32_bf16 v[56:59], v[72:75], v[112:115], v[56:59]\n\t"
  "buffer_load_dwordx4 a[24:27], %[vl16], %[srd], s56 offen offset:2048 sc1\n\t"
  "v_mfma_f32_16x16x32_bf16 v[52:55], v[72:75], v[108:111], v[52:55]\n\t"
  "v_mfma_f32_16x16x32_bf16 v[48:51], v[72:75], v[104:107], v[48:51]\n\t"
  "ds_read_b128 v[132:135], %[vb3] offset:3072\n\t"
  "buffer_load_dwordx4 a[28:31], %[vl16], %[srd], s56 offen offset:3072 sc1\n\t"
  "v_mfma_f32_16x16x32_bf16 v[44:47], v[72:75], v[100:103], v[44:47]\n\t"
  "v_mfma_f32_16x16x32_bf16 v[40:43], v[72:75], v[96:99], v[40:43]\n\t"
  "ds_read_b64 v[72:73], %[va] offset:1216\n\t"
  "ds_read_b64 v[74:75], %[va] offset:1224\n\t"
  "v_mfma_f32_16x16x32_bf16 v[60:63], v[76:79], v[112:115], v[60:63]\n\t"
  "ds_read_b128 v[128:131], %[vb3] offset:1024\n\t"
  "s_add_u32 s56, s56, 4096\n\t"
  "v_mfma_f32_16x16x32_bf16 v[56:59], v[76:79], v[108:111], v[56:59]\n\t"
  "v_mfma_f32_16x16x32_bf16 v[52:55], v[76:79], v[104:107], v[52:55]\n\t"
  "v_mfma_f32_16x16x32_bf16 v[48:51], v[76:79], v[100:103], v[48:51]\n\t"
  "v_mfma_f32_16x16x32_bf16 v[44:47], v[76:79], v[96:99], v[44:47]\n\t"
  "ds_read_b64 v[76:77], %[va] offset:1728\n\t"
  "ds_read_b64 v[78:79], %[va] offset:1736\n\t"
  "v_mfma_f32_16x16x32_bf16 v[60:63], v[80:83], v[108:111], v[60:63]\n\t"
  "buffer_load_dwordx4 a[32:35], %[vl16], %[srd], s56 offen offset:0 sc1\n\t"
  "v_mfma_f32_16x16x32_bf16 v[56:59], v[80:83], v[104:107], v[56:59]\n\t"
  "v_mfma_f32_16x16x32_bf16 v[52:55], v[80:83], v[100:103], v[52:55]\n\t"
  "buffer_load_dwordx4 a[36:39], %[vl16], %[srd], s56 offen offset:1024 sc1\n\t"
  "v_mfma_f32_16x16x32_bf16 v[48:51], v[80:83], v[96:99], v[48:51]\n\t"
  "ds_read_b64 v[80:81], %[va] offset:2240\n\t"
  "ds_read_b64 v[82:83], %[va] offset:2248\n\t"
  "v_mfma_f32_16x16x32_bf16 v[60:63], v[84:87], v[104:107], v[60:63]\n\t"
  "v_mfma_f32_16x16x32_bf16 v[56:59], v[84:87], v[100:103], v[56:59]\n\t"
  "v_mfma_f32_16x16x32_bf16 v[52:55], v[84:87], v[96:99], v[52:55]\n\t"
  "s_waitcnt lgkmcnt(15)\n\t"
  "ds_read_b64 v[84:85], %[va] offset:2752\n\t"
  "v_mfma_f32_16x16x32_bf16 v[60:63], v[88:91], v[100:103], v[60:63]\n\t"
  "ds_read_b64 v[86:87], %[va] offset:2760\n\t"
  "buffer_load_dwordx4 a[40:43], %[vl16], %[srd], s56 offen offset:2048 sc1\n\t"
  "v_mfma_f32_16x16x32_bf16 v[56:59], v[88:91], v[96:99], v[56:59]\n\t"
  "ds_read_b64 v[88:89], %[va] offset:3264\n\t"
  "ds_read_b64 v[90:91], %[va] offset:3272\n\t"
  "v_mfma_f32_16x16x32_bf16 v[60:63], v[92:95], v[96:99], v[60:63]\n\t"
  "s_waitcnt lgkmcnt(8)\n\t"
  "ds_read_b64 v[92:93], %[va] offset:3776\n\t"
  "v_mfma_f32_16x16x32_bf16 v[60:63], v[64:67], v[156:159], v[60:63]\n\t"
  "ds_read_b128 v[124:127], %[vb0] offset:14336\n\t"
  "ds_read_b64 v[94:95], %[va] offset:3784\n\t"
  "v_mfma_f32_16x16x32_bf16 v[56:59], v[64:67], v[152:155], v[56:59]\n\t"
  "buffer_load_dwordx4 a[44:47], %[vl16], %[srd], s56 offen offset:3072 sc1\n\t"
  "v_mfma_f32_16x16x32_bf16 v[52:55], v[64:67], v[148:151], v[52:55]\n\t"
  "v_mfma_f32_16x16x32_bf16 v[48:51], v[64:67], v[144:147], v[48:51]\n\t"
  "ds_read_b128 v[120:123], %[vb0] offset:12288\n\t"
  "s_add_u32 s56, s56, 4096\n\t"
  "v_mfma_f32_16x16x32_bf16 v[44:47], v[64:67], v[140:143], v[44:47]\n\t"
  "v_mfma_f32_16x16x32_bf16 v[40:43], v[64:67], v[136:139], v[40:43]\n\t"
  "v_mfma_f32_16x16x32_bf16 v[36:39], v[64:67], v[132:135], v[36:39]\n\t"
  "ds_read_b128 v[116:119], %[vb0] offset:10240\n\t"
  "buffer_load_dwordx4 a[48:51], %[vl16], %[srd], s56 offen offset:0 sc1\n\t"
  "v_mfma_f32_16x16x32_bf16 v[32:35], v[64:67], v[128:131], v[32:35]\n\t"
  "ds_read_b64 v[64:65], %[va] offset:256\n\t"
  "ds_read_b64 v[66:67], %[va] offset:264\n\t"
  "v_mfma_f32_16x16x32_bf16 v[60:63], v[68:71], v[152:155], v[60:63]\n\t"
  "v_mfma_f32_16x16x32_bf16 v[56:59], v[68:71], v[148:151], v[56:59]\n\t"
  "ds_read_b128 v[112:115], %[vb0] offset:8192\n\t"
  "v_mfma_f32_16x16x32_bf16 v[52:55], v[68:71], v[144:147], v[52:55]\n\t"
  "buffer_load_dwordx4 a[52:55], %[vl16], %[srd], s56 offen offset:1024 sc1\n\t"
  "v_mfma_f32_16x16x32_bf16 v[48:51], v[68:71], v[140:143], v[48:51]\n\t"
  "v_mfma_f32_16x16x32_bf16 v[44:47], v[68:71], v[136:139], v[44:47]\n\t"
  "ds_read_b128 v[108:111], %[vb0] offset:6144\n\t"
  "v_mfma_f32_16x16x32_bf16 v[40:43], v[68:71], v[132:135], v[40:43]\n\t"
  "v_mfma_f32_16x16x32_bf16 v[36:39], v[68:71], v[128:131], v[36:39]\n\t"
  "s_waitcnt lgkmcnt(11)\n\t"
  "ds_read_b64 v[68:69], %[va] offset:768\n\t"
  "v_mfma_f32_16x16x32_bf16 v[60:63], v[72:75], v[148:151], v[60:63]\n\t"
  "ds_read_b128 v[104:107], %[vb0] offset:4096\n\t"
  "ds_read_b64 v[70:71], %[va] offset:776\n\t"
  "v_mfma_f32_16x16x32_bf16 v[56:59], v[72:75], v[144:147], v[56:59]\n\t"
  "v_mfma_f32_16x16x32_bf16 v[52:55], v[72:75], v[140:143], v[52:55]\n\t"
  "v_mfma_f32_16x16x32_bf16 v[48:51], v[72:75], v[136:139], v[48:51]\n\t"
  "ds_read_b128 v[100:103], %[vb0] offset:2048\n\t"
  "v_mfma_f32_16x16x32_bf16 v[44:47], v[72:75], v[132:135], v[44:47]\n\t"
  "v_mfma_f32_16x16x32_bf16 v[40:43], v[72:75], v[128:131], v[40:43]\n\t"
  "ds_read_b64 v[72:73], %[va] offset:1280\n\t"
  "ds_read_b64 v[74:75], %[va] offset:1288\n\t"
  "v_mfma_f32_16x16x32_bf16 v[60:63], v[76:79], v[144:147], v[60:63]\n\t"
  "ds_read_b128 v[96:99], %[vb0] offset:0\n\t"
  "v_mfma_f32_16x16x32_bf16 v[56:59], v[76:79], v[140:143], v[56:59]\n\t"
  "v_mfma_f32_16x16x32_bf16 v[52:55], v[76:79], v[136:139], v[52:55]\n\t"
  "v_mfma_f32_16x16x32_bf16 v[48:51], v[76:79], v[132:135], v[48:51]\n\t"
  "buffer_load_dwordx2 v[162:163], %[vf2], %[srd], 0 offen sc1\n\t"
  "v_mfma_f32_16x16x32_bf16 v[44:47], v[76:79], v[128:131], v[44:47]\n\t"
  "ds_read_b64 v[76:77], %[va] offset:1792\n\t"
  "ds_read_b64 v[78:79], %[va] offset:1800\n\t"
  "v_mfma_f32_16x16x32_bf16 v[60:63], v[80:83], v[140:143], v[60:63]\n\t"
  "v_mfma_f32_16x16x32_bf16 v[56:59], v[80:83], v[136:139], v[56:59]\n\t"
  "v_mfma_f32_16x16x32_bf16 v[52:55], v[80:83], v[132:135], v[52:55]\n\t"
  "v_mfma_f32_16x16x32_bf16 v[48:51], v[80:83], v[128:131], v[48:51]\n\t"
  "ds_read_b64 v[80:81], %[va] offset:2304\n\t"
  "ds_read_b64 v[82:83], %[va] offset:2312\n\t"
  "v_mfma_f32_16x16x32_bf16 v[60:63], v[84:87], v[136:139], v[60:63]\n\t"
  "v_mfma_f32_16x16x32_bf16 v[56:59], v[84:87], v[132:135], v[56:59]\n\t"
  "v_mfma_f32_16x16x32_bf16 v[52:55], v[84:87], v[128:131], v[52:55]\n\t"
  "s_waitcnt lgkmcnt(15)\n\t"
  "ds_read_b64 v[84:85], %[va] offset:2816\n\t"
  "v_mfma_f32_16x16x32_bf16 v[60:63], v[88:91], v[132:135], v[60:63]\n\t"
  "ds_read_b64 v[86:87], %[va] offset:2824\n\t"
  "v_mfma_f32_16x16x32_bf16 v[56:59], v[88:91], v[128:131], v[56:59]\n\t"
  "ds_read_b64 v[88:89], %[va] offset:3328\n\t"
  "ds_read_b64 v[90:91], %[va] offset:3336\n\t"
  "v_mfma_f32_16x16x32_bf16 v[60:63], v[92:95], v[128:131], v[60:63]\n\t"
  "s_waitcnt lgkmcnt(8)\n\t"
  "ds_read_b64 v[92:93], %[va] offset:3840\n\t"
  "v_mfma_f32_16x16x32_bf16 v[60:63], v[64:67], v[124:127], v[60:63]\n\t"
  "ds_read_b128 v[156:159], %[vb1] offset:14336\n\t"
  "ds_read_b64 v[94:95], %[va] offset:3848\n\t"
  "v_mfma_f32_16x16x32_bf16 v[56:59], v[64:67], v[120:123], v[56:59]\n\t"
  "v_mfma_f32_16x16x32_bf16 v[52:55], v[64:67], v[116:119], v[52:55]\n\t"
  "v_mfma_f32_16x16x32_bf16 v[48:51], v[64:67], v[112:115], v[48:51]\n\t"
  "ds_read_b128 v[152:155], %[vb1] offset:12288\n\t"
  "v_mfma_f32_16x16x32_bf16 v[44:47], v[64:67], v[108:111], v[44:47]\n\t"
  "v_mfma_f32_16x16x32_bf16 v[40:43], v[64:67], v[104:107], v[40:43]\n\t"
  "v_mfma_f32_16x16x32_bf16 v[36:39], v[64:67], v[100:103], v[36:39]\n\t"
  "ds_read_b128 v[148:151], %[vb1] offset:10240\n\t"
  "v_mfma_f32_16x16x32_bf16 v[32:35], v[64:67], v[96:99], v[32:35]\n\t"
  "ds_read_b64 v[64:65], %[va] offset:320\n\t"
  "ds_read_b64 v[66:67], %[va] offset:328\n\t"
  "v_mfma_f32_16x16x32_bf16 v[60:63], v[68:71], v[120:123], v[60:63]\n\t"
  "buffer_load_dwordx2 v[250:251], %[vfw], %[srd], 0 offen sc1\n\t"
  "v_mfma_f32_16x16x32_bf16 v[56:59], v[68:71], v[116:119], v[56:59]\n\t"
  "ds_read_b128 v[144:147], %[vb1] offset:8192\n\t"
  "v_mfma_f32_16x16x32_bf16 v[52:55], v[68:71], v[112:115], v[52:55]\n\t"
  "v_mfma_f32_16x16x32_bf16 v[48:51], v[68:71], v[108:111], v[48:51]\n\t"
  "v_mfma_f32_16x16x32_bf16 v[44:47], v[68:71], v[104:107], v[44:47]\n\t"
  "ds_read_b128 v[140:143], %[vb1] offset:6144\n\t"
  "v_mfma_f32_16x16x32_bf16 v[40:43], v[68:71], v[100:103], v[40:43]\n\t"
  "v_mfma_f32_16x16x32_bf16 v[36:39], v[68:71], v[96:99], v[36:39]\n\t"
  "s_waitcnt lgkmcnt(11)\n\t"
  "ds_read_b64 v[68:69], %[va] offset:832\n\t"
  "v_mfma_f32_16x16x32_bf16 v[60:63], v[72:75], v[116:119], v[60:63]\n\t"
  "ds_read_b128 v[136:139], %[vb1] offset:4096\n\t"
  "ds_read_b64 v[70:71], %[va] offset:840\n\t"
  "v_mfma_f32_16x16x32_bf16 v[56:59], v[72:75], v[112:115], v[56:59]\n\t"
  "v_mfma_f32_16x16x32_bf16 v[52:55], v[72:75], v[108:111], v[52:55]\n\t"
  "v_mfma_f32_16x16x32_bf16 v[48:51], v[72:75], v[104:107], v[48:51]\n\t"
  "ds_read_b128 v[132:135], %[vb1] offset:2048\n\t"
  "v_mfma_f32_16x16x32_bf16 v[44:47], v[72:75], v[100:103], v[44:47]\n\t"
  "v_mfma_f32_16x16x32_bf16 v[40:43], v[72:75], v[96:99], v[40:43]\n\t"
  "ds_read_b64 v[72:73], %[va] offset:1344\n\t"
  "ds_read_b64 v[74:75], %[va] offset:1352\n\t"
  "v_mfma_f32_16x16x32_bf16 v[60:63], v[76:79], v[112:115], v[60:63]\n\t"
  "ds_read_b128 v[128:131], %[vb1] offset:0\n\t"
  "v_mfma_f32_16x16x32_bf16 v[56:59], v[76:79], v[108:111], v[56:59]\n\t"
  "v_mfma_f32_16x16x32_bf16 v[52:55], v[76:79], v[104:107], v[52:55]\n\t"
  "v_mfma_f32_16x16x32_bf16 v[48:51], v[76:79], v[100:103], v[48:51]\n\t"
  "v_mfma_f32_16x16x32_bf16 v[44:47], v[76:79], v[96:99], v[44:47]\n\t"
  "ds_read_b64 v[76:77], %[va] offset:1856\n\t"
  "ds_read_b64 v[78:79], %[va] offset:1864\n\t"
  "v_mfma_f32_16x16x32_bf16 v[60:63], v[80:83], v[108:111], v[60:63]\n\t"
  "v_mfma_f32_16x16x32_bf16 v[56:59], v[80:83], v[104:107], v[56:59]\n\t"
  "v_mfma_f32_16x16x32_bf16 v[52:55], v[80:83], v[100:103], v[52:55]\n\t"
  "s_waitcnt vmcnt(1)\n\t"
  "v_cmp_ne_u64 vcc, v[252:253], v[162:163]\n\t"
  "v_mfma_f32_16x16x32_bf16 v[48:51], v[80:83], v[96:99], v[48:51]\n\t"
  "ds_read_b64 v[80:81], %[va] offset:2368\n\t"
  "ds_read_b64 v[82:83], %[va] offset:2376\n\t"
  "v_mfma_f32_16x16x32_bf16 v[60:63], v[84:87], v[104:107], v[60:63]\n\t"
  "s_cbranch_vccnz La2_slow%=\n\t"
  "La2_back%=:\n\t"
  "v_mfma_f32_16x16x32_bf16 v[56:59], v[84:87], v[100:103], v[56:59]\n\t"
  "buffer_load_dword v244, %[vmod], %[srd], 0 offen sc1\n\t"
  "s_add_u32 m0, s45, 84992\n\t"
  "v_mfma_f32_16x16x32_bf16 v[52:55], v[84:87], v[96:99], v[52:55]\n\t"
  "s_waitcnt lgkmcnt(15)\n\t"
  "ds_read_b64 v[84:85], %[va] offset:2880\n\t"
  "v_mfma_f32_16x16x32_bf16 v[60:63], v[88:91], v[100:103], v[60:63]\n\t"
  "ds_read_b64 v[86:87], %[va] offset:2888\n\t"
  "s_add_u32 s44, %[sKV], 2621440\n\t"
  "v_mfma_f32_16x16x32_bf16 v[56:59], v[88:91], v[96:99], v[56:59]\n\t"
  "ds_read_b64 v[88:89], %[va] offset:3392\n\t"
  "ds_read_b64 v[90:91], %[va] offset:3400\n\t"
  "v_mfma_f32_16x16x32_bf16 v[60:63], v[92:95], v[96:99], v[60:63]\n\t"
  "s_waitcnt lgkmcnt(8)\n\t"
  "ds_read_b64 v[92:93], %[va] offset:3904\n\t"
  "v_mfma_f32_16x16x32_bf16 v[60:63], v[64:67], v[156:159], v[60:63]\n\t"
  "ds_read_b128 v[124:127], %[vb2] offset:14336\n\t"
  "ds_read_b64 v[94:95], %[va] offset:3912\n\t"
  "v_mfma_f32_16x16x32_bf16 v[56:59], v[64:67], v[152:155], v[56:59]\n\t"
  "buffer_load_dwordx4 %[vkv], %[srd], s44 offen sc1 lds\n\t"
  "s_add_u32 m0, s45, 86016\n\t"
  "v_mfma_f32_16x16x32_bf16 v[52:55], v[64:67], v[148:151], v[52:55]\n\t"
  "s_add_u32 s44, %[sKV], 2785280\n\t"
  "buffer_load_dwordx4 %[vkv], %[srd], s44 offen sc1 lds\n\t"
  "v_mfma_f32_16x16x32_bf16 v[48:51], v[64:67], v[144:147], v[48:51]\n\t"
  "ds_read_b128 v[120:123], %[vb2] offset:12288\n\t"
  "s_add_u32 m0, s45, 87040\n\t"
  "v_mfma_f32_16x16x32_bf16 v[44:47], v[64:67], v[140:143], v[44:47]\n\t"
  "s_add_u32 s44, %[sKV], 2949120\n\t"
  "buffer_load_dwordx4 %[vkv], %[srd], s44 offen sc1 lds\n\t"
  "v_mfma_f32_16x16x32_bf16 v[40:43], v[64:67], v[136:139], v[40:43]\n\t"
  "s_add_u32 m0, s45, 88064\n\t"
  "s_add_u32 s44, %[sKV], 3112960\n\t"
  "v_mfma_f32_16x16x32_bf16 v[36:39], v[64:67], v[132:135], v[36:39]\n\t"
  "ds_read_b128 v[116:119], %[vb2] offset:10240\n\t"
  "buffer_load_dwordx4 %[vkv], %[srd], s44 offen sc1 lds\n\t"
  "v_mfma_f32_16x16x32_bf16 v[32:35], v[64:67], v[128:131], v[32:35]\n\t"
  "ds_read_b64 v[64:65], %[va] offset:384\n\t"
  "ds_read_b64 v[66:67], %[va] offset:392\n\t"
  "v_mfma_f32_16x16x32_bf16 v[60:63], v[68:71], v[152:155], v[60:63]\n\t"
  "s_add_u32 m0, s45, 101376\n\t"
  "s_add_u32 s44, s45, 995328\n\t"
  "v_mfma_f32_16x16x32_bf16 v[56:59], v[68:71], v[148:151], v[56:59]\n\t"
  "ds_read_b128 v[112:115], %[vb2] offset:8192\n\t"
  "buffer_load_dwordx4 %[vl16], %[srd], s44 offen offset:0 sc1 lds\n\t"
  "v_mfma_f32_16x16x32_bf16 v[52:55], v[68:71], v[144:147], v[52:55]\n\t"
  "buffer_load_dwordx4 %[vl16], %[srd], s44 offen offset:1024 sc1 lds\n\t"
  "buffer_load_dwordx4 %[vl16], %[srd], s44 offen offset:2048 sc1 lds\n\t"
  "v_mfma_f32_16x16x32_bf16 v[48:51], v[68:71], v[140:143], v[48:51]\n\t"
  "buffer_load_dwordx4 %[vl16], %[srd], s44 offen offset:3072 sc1 lds\n\t"
  "v_mfma_f32_16x16x32_bf16 v[44:47], v[68:71], v[136:139], v[44:47]\n\t"
  "ds_read_b128 v[108:111], %[vb2] offset:6144\n\t"
  "s_waitcnt vmcnt(9)\n\t"
  "v_cmp_eq_u64 s[54:55], v[252:253], v[250:251]\n\t"
  "v_mfma_f32_16x16x32_bf16 v[40:43], v[68:71], v[132:135], v[40:43]\n\t"
  "v_mfma_f32_16x16x32_bf16 v[36:39], v[68:71], v[128:131], v[36:39]\n\t"
  "s_waitcnt lgkmcnt(11)\n\t"
  "ds_read_b64 v[68:69], %[va] offset:896\n\t"
  "v_mfma_f32_16x16x32_bf16 v[60:63], v[72:75], v[148:151], v[60:63]\n\t"
  "ds_read_b128 v[104:107], %[vb2] offset:4096\n\t"
  "ds_read_b64 v[70:71], %[va] offset:904\n\t"
  "v_mfma_f32_16x16x32_bf16 v[56:59], v[72:75], v[144:147], v[56:59]\n\t"
  "s_cmp_eq_u64 s[54:55], -1\n\t"
  "s_cselect_b32 s53, 2, 0\n\t"
  "s_cselect_b32 s57, %[sW1], 0x70000000\n\t"
  "v_mfma_f32_16x16x32_bf16 v[52:55], v[72:75], v[140:143], v[52:55]\n\t"
  "s_or_b32 %[wdone], %[wdone], s53\n\t"
  "v_mfma_f32_16x16x32_bf16 v[48:51], v[72:75], v[136:139], v[48:51]\n\t"
  "ds_read_b128 v[100:103], %[vb2] offset:2048\n\t"
  "s_add_u32 s57, s57, 14336\n\t"
  "v_mfma_f32_16x16x32_bf16 v[44:47], v[72:75], v[132:135], v[44:47]\n\t"
  "v_mfma_f32_16x16x32_bf16 v[40:43], v[72:75], v[128:131], v[40:43]\n\t"
  "ds_read_b64 v[72:73], %[va] offset:1408\n\t"
  "ds_read_b64 v[74:75], %[va] offset:1416\n\t"
  "v_mfma_f32_16x16x32_bf16 v[60:63], v[76:79], v[144:147], v[60:63]\n\t"
  "ds_read_b128 v[96:99], %[vb2] offset:0\n\t"
  "buffer_load_dwordx4 a[56:59], %[vl16], %[srd], s57 offen offset:0 sc1\n\t"
  "v_mfma_f32_16x16x32_bf16 v[56:59], v[76:79], v[140:143], v[56:59]\n\t"
  "v_mfma_f32_16x16x32_bf16 v[52:55], v[76:79], v[136:139], v[52:55]\n\t"
  "v_mfma_f32_16x16x32_bf16 v[48:51], v[76:79], v[132:135], v[48:51]\n\t"
  "buffer_load_dwordx4 a[60:63], %[vl16], %[srd], s57 offen offset:1024 sc1\n\t"
  "v_mfma_f32_16x16x32_bf16 v[44:47], v[76:79], v[128:131], v[44:47]\n\t"
  "ds_read_b64 v[76:77], %[va] offset:1920\n\t"
  "ds_read_b64 v[78:79], %[va] offset:1928\n\t"
  "v_mfma_f32_16x16x32_bf16 v[60:63], v[80:83], v[140:143], v[60:63]\n\t"
  "v_mfma_f32_16x16x32_bf16 v[56:59], v[80:83], v[136:139], v[56:59]\n\t"
  "v_mfma_f32_16x16x32_bf16 v[52:55], v[80:83], v[132:135], v[52:55]\n\t"
  "buffer_load_dwordx4 a[64:67], %[vl16], %[srd], s57 offen offset:2048 sc1\n\t"
  "v_mfma_f32_16x16x32_bf16 v[48:51], v[80:83], v[128:131], v[48:51]\n\t"
  "ds_read_b64 v[80:81], %[va] offset:2432\n\t"
  "ds_read_b64 v[82:83], %[va] offset:2440\n\t"
  "v_mfma_f32_16x16x32_bf16 v[60:63], v[84:87], v[136:139], v[60:63]\n\t"
  "v_mfma_f32_16x16x32_bf16 v[56:59], v[84:87], v[132:135], v[56:59]\n\t"
  "buffer_load_dwordx4 a[68:71], %[vl16], %[srd], s57 offen offset:3072 sc1\n\t"
  "v_mfma_f32_16x16x32_bf16 v[52:55], v[84:87], v[128:131], v[52:55]\n\t"
  "s_waitcnt lgkmcnt(15)\n\t"
  "ds_read_b64 v[84:85], %[va] offset:2944\n\t"
  "v_mfma_f32_16x16x32_bf16 v[60:63], v[88:91], v[132:135], v[60:63]\n\t"
  "ds_read_b64 v[86:87], %[va] offset:2952\n\t"
  "v_mfma_f32_16x16x32_bf16 v[56:59], v[88:91], v[128:131], v[56:59]\n\t"
  "ds_read_b64 v[88:89], %[va] offset:3456\n\t"
  "ds_read_b64 v[90:91], %[va] offset:3464\n\t"
  "v_mfma_f32_16x16x32_bf16 v[60:63], v[92:95], v[128:131], v[60:63]\n\t"
  "s_waitcnt lgkmcnt(8)\n\t"
  "ds_read_b64 v[92:93], %[va] offset:3968\n\t"
  "v_mfma_f32_16x16x32_bf16 v[60:63], v[64:67], v[124:127], v[60:63]\n\t"
  "ds_read_b128 v[156:159], %[vb3] offset:14336\n\t"
  "ds_read_b64 v[94:95], %[va] offset:3976\n\t"
  "v_mfma_f32_16x16x32_bf16 v[56:59], v[64:67], v[120:123], v[56:59]\n\t"
  "s_add_u32 s57, s57, 4096\n\t"
  "v_mfma_f32_16x16x32_bf16 v[52:55], v[64:67], v[116:119], v[52:55]\n\t"
  "buffer_load_dwordx4 a[72:75], %[vl16], %[srd], s57 offen offset:0 sc1\n\t"
  "v_mfma_f32_16x16x32_bf16 v[48:51], v[64:67], v[112:115], v[48:51]\n\t"
  "ds_read_b128 v[152:155], %[vb3] offset:12288\n\t"
  "v_mfma_f32_16x16x32_bf16 v[44:47], v[64:67], v[108:111], v[44:47]\n\t"
  "v_mfma_f32_16x16x32_bf16 v[40:43], v[64:67], v[104:107], v[40:43]\n\t"
  "buffer_load_dwordx4 a[76:79], %[vl16], %[srd], s57 offen offset:1024 sc1\n\t"
  "v_mfma_f32_16x16x32_bf16 v[36:39], v[64:67], v[100:103], v[36:39]\n\t"
  "ds_read_b128 v[148:151], %[vb3] offset:10240\n\t"
  "v_mfma_f32_16x16x32_bf16 v[32:35], v[64:67], v[96:99], v[32:35]\n\t"
  "ds_read_b64 v[64:65], %[va] offset:448\n\t"
  "ds_read_b64 v[66:67], %[va] offset:456\n\t"
  "v_mfma_f32_16x16x32_bf16 v[60:63], v[68:71], v[120:123], v[60:63]\n\t"
  "buffer_load_dwordx4 a[80:83], %[vl16], %[srd], s57 offen offset:2048 sc1\n\t"
  "v_mfma_f32_16x16x32_bf16 v[56:59], v[68:71], v[116:119], v[56:59]\n\t"
  "ds_read_b128 v[144:147], %[vb3] offset:8192\n\t"
  "v_mfma_f32_16x16x32_bf16 v[52:55], v[68:71], v[112:115], v[52:55]\n\t"
  "v_mfma_f32_16x16x32_bf16 v[48:51], v[68:71], v[108:111], v[48:51]\n\t"
  "v_mfma_f32_16x16x32_bf16 v[44:47], v[68:71], v[104:107], v[44:47]\n\t"
  "ds_read_b128 v[140:143], %[vb3] offset:6144\n\t"
  "buffer_load_dwordx4 a[84:87], %[vl16], %[srd], s57 offen offset:3072 sc1\n\t"
  "v_mfma_f32_16x16x32_bf16 v[40:43], v[68:71], v[100:103], v[40:43]\n\t"
  "v_mfma_f32_16x16x32_bf16 v[36:39], v[68:71], v[96:99], v[36:39]\n\t"
  "s_waitcnt lgkmcnt(11)\n\t"
  "ds_read_b64 v[68:69], %[va] offset:960\n\t"
  "v_mfma_f32_16x16x32_bf16 v[60:63], v[72:75], v[116:119], v[60:63]\n\t"
  "ds_read_b128 v[136:139], %[vb3] offset:4096\n\t"
  "ds_read_b64 v[70:71], %[va] offset:968\n\t"
  "v_mfma_f32_16x16x32_bf16 v[56:59], v[72:75], v[112:115], v[56:59]\n\t"
  "s_add_u32 s57, s57, 4096\n\t"
  "v_mfma_f32_16x16x32_bf16 v[52:55], v[72:75], v[108:111], v[52:55]\n\t"
  "v_mfma_f32_16x16x32_bf16 v[48:51], v[72:75], v[104:107], v[48:51]\n\t"
  "ds_read_b128 v[132:135], %[vb3] offset:2048\n\t"
  "buffer_load_dwordx4 a[88:91], %[vl16], %[srd], s57 offen offset:0 sc1\n\t"
  "v_mfma_f32_16x16x32_bf16 v[44:47], v[72:75], v[100:103], v[44:47]\n\t"
  "v_mfma_f32_16x16x32_bf16 v[40:43], v[72:75], v[96:99], v[40:43]\n\t"
  "ds_read_b64 v[72:73], %[va] offset:1472\n\t"
  "ds_read_b64 v[74:75], %[va] offset:1480\n\t"
  "v_mfma_f32_16x16x32_bf16 v[60:63], v[76:79], v[112:115], v[60:63]\n\t"
  "ds_read_b128 v[128:131], %[vb3] offset:0\n\t"
  "v_mfma_f32_16x16x32_bf16 v[56:59], v[76:79], v[108:111], v[56:59]\n\t"
  "buffer_load_dwordx4 a[92:95], %[vl16], %[srd], s57 offen offset:1024 sc1\n\t"
  "v_mfma_f32_16x16x32_bf16 v[52:55], v[76:79], v[104:107], v[52:55]\n\t"
  "v_mfma_f32_16x16x32_bf16 v[48:51], v[76:79], v[100:103], v[48:51]\n\t"
  "v_mfma_f32_16x16x32_bf16 v[44:47], v[76:79], v[96:99], v[44:47]\n\t"
  "ds_read_b64 v[76:77], %[va] offset:1984\n\t"
  "ds_read_b64 v[78:79], %[va] offset:1992\n\t"
  "v_mfma_f32_16x16x32_bf16 v[60:63], v[80:83], v[108:111], v[60:63]\n\t"
  "buffer_load_dwordx4 a[96:99], %[vl16], %[srd], s57 offen offset:2048 sc1\n\t"
  "v_mfma_f32_16x16x32_bf16 v[56:59], v[80:83], v[104:107], v[56:59]\n\t"
  "s_waitcnt vmcnt(19)\n\t"
  "v_add_f32_dpp v244, v244, v244 quad_perm:[1,0,3,2] row_mask:0xf bank_mask:0xf\n\t"
  "v_mfma_f32_16x16x32_bf16 v[52:55], v[80:83], v[100:103], v[52:55]\n\t"
  "v_mfma_f32_16x16x32_bf16 v[48:51], v[80:83], v[96:99], v[48:51]\n\t"
  "ds_read_b64 v[80:81], %[va] offset:2496\n\t"
  "ds_read_b64 v[82:83], %[va] offset:2504\n\t"
  "v_mfma_f32_16x16x32_bf16 v[60:63], v[84:87], v[104:107], v[60:63]\n\t"
  "buffer_load_dwordx4 a[100:103], %[vl16], %[srd], s57 offen offset:3072 sc1\n\t"
  "v_add_f32_dpp v244, v244, v244 quad_perm:[2,3,0,1] row_mask:0xf bank_mask:0xf\n\t"
  "v_mfma_f32_16x16x32_bf16 v[56:59], v[84:87], v[100:103], v[56:59]\n\t"
  "v_cvt_pk_bf16_f32 v244, 0, v244\n\t"
  "v_add_f32 v245, 1.0, v244\n\t"
  "v_mfma_f32_16x16x32_bf16 v[52:55], v[84:87], v[96:99], v[52:55]\n\t"
  "s_waitcnt lgkmcnt(15)\n\t"
  "ds_read_b64 v[84:85], %[va] offset:3008\n\t"
  "v_mfma_f32_16x16x32_bf16 v[60:63], v[88:91], v[100:103], v[60:63]\n\t"
  "ds_read_b64 v[86:87], %[va] offset:3016\n\t"
  "s_add_u32 s57, s57, 4096\n\t"
  "v_mfma_f32_16x16x32_bf16 v[56:59], v[88:91], v[96:99], v[56:59]\n\t"
  "ds_read_b64 v[88:89], %[va] offset:3520\n\t"
  "ds_read_b64 v[90:91], %[va] offset:3528\n\t"
  "v_mfma_f32_16x16x32_bf16 v[60:63], v[92:95], v[96:99], v[60:63]\n\t"
  "s_waitcnt lgkmcnt(8)\n\t"
  "ds_read_b64 v[92:93], %[va] offset:4032\n\t"
  "v_mfma_f32_16x16x32_bf16 v[60:63], v[64:67], v[156:159], v[60:63]\n\t"
  "ds_read_b64 v[94:95], %[va] offset:4040\n\t"
  "buffer_load_dwordx4 a[104:107], %[vl16], %[srd], s57 offen offset:0 sc1\n\t"
  "v_mfma_f32_16x16x32_bf16 v[56:59], v[64:67], v[152:155], v[56:59]\n\t"
  "v_cvt_pk_bf16_f32 v245, 0, v245\n\t"
  "v_readlane_b32 s46, v244, 0\n\t"
  "v_mfma_f32_16x16x32_bf16 v[52:55], v[64:67], v[148:151], v[52:55]\n\t"
  "v_readlane_b32 s48, v244, 8\n\t"
  "v_readlane_b32 s47, v245, 4\n\t"
  "v_mfma_f32_16x16x32_bf16 v[48:51], v[64:67], v[144:147], v[48:51]\n\t"
  "buffer_load_dwordx4 a[108:111], %[vl16], %[srd], s57 offen offset:1024 sc1\n\t"
  "v_readlane_b32 s51, v245, 12\n\t"
  "v_mfma_f32_16x16x32_bf16 v[44:47], v[64:67], v[140:143], v[44:47]\n\t"
  "v_mfma_f32_16x16x32_bf16 v[40:43], v[64:67], v[136:139], v[40:43]\n\t"
  "s_nop 1\n\t"
  "s_and_b32 s52, s51, 0x7fffffff\n\t"
  "s_cselect_b32 s51, s51, 0x2b800000\n\t"
  "v_mfma_f32_16x16x32_bf16 v[36:39], v[64:67], v[132:135], v[36:39]\n\t"
  "buffer_load_dwordx4 a[112:115], %[vl16], %[srd], s57 offen offset:2048 sc1\n\t"
  "v_mov_b32 v245, s48\n\t"
  "v_mfma_f32_16x16x32_bf16 v[32:35], v[64:67], v[128:131], v[32:35]\n\t"
  "v_mul_f32 v245, s51, v245\n\t"
  "v_mul_f32 v244, s47, v245\n\t"
  "v_mfma_f32_16x16x32_bf16 v[60:63], v[68:71], v[152:155], v[60:63]\n\t"
  "v_mul_f32 v245, s46, v245\n\t"
  "v_mfma_f32_16x16x32_bf16 v[56:59], v[68:71], v[148:151], v[56:59]\n\t"
  "buffer_load_dwordx4 a[116:119], %[vl16], %[srd], s57 offen offset:3072 sc1\n\t"
  "v_mfma_f32_16x16x32_bf16 v[52:55], v[68:71], v[144:147], v[52:55]\n\t"
  "s_waitcnt vmcnt(16)\n\t"
  "s_barrier\n\t"
  "v_mfma_f32_16x16x32_bf16 v[48:51], v[68:71], v[140:143], v[48:51]\n\t"
  "v_mov_b32 v246, s51\n\t"
  "ds_read_b64 v[224:225], %[vx] offset:0\n\t"
  "v_mfma_f32_16x16x32_bf16 v[44:47], v[68:71], v[136:139], v[44:47]\n\t"
  "ds_read_b128 v[160:163], %[vs] offset:0\n\t"
  "ds_read_b128 v[164:167], %[vs2] offset:0\n\t"
  "v_mfma_f32_16x16x32_bf16 v[40:43], v[68:71], v[132:135], v[40:43]\n\t"
  "s_add_u32 s57, s57, 4096\n\t"
  "ds_read_b64 v[226:227], %[vx] offset:2048\n\t"
  "v_mfma_f32_16x16x32_bf16 v[36:39], v[68:71], v[128:131], v[36:39]\n\t"
  "s_waitcnt lgkmcnt(8)\n\t"
  "ds_read_b128 v[168:171], %[vs] offset:2048\n\t"
  "v_mfma_f32_16x16x32_bf16 v[60:63], v[72:75], v[148:151], v[60:63]\n\t"
  "ds_read_b128 v[172:175], %[vs2] offset:2048\n\t"
  "ds_read_b64 v[228:229], %[vx] offset:4096\n\t"
  "v_mfma_f32_16x16x32_bf16 v[56:59], v[72:75], v[144:147], v[56:59]\n\t"
  "buffer_load_dwordx4 a[120:123], %[vl16], %[srd], s57 offen offset:0 sc1\n\t"
  "ds_read_b128 v[176:179], %[vs] offset:4096\n\t"
  "v_mfma_f32_16x16x32_bf16 v[52:55], v[72:75], v[140:143], v[52:55]\n\t"
  "ds_read_b128 v[180:183], %[vs2] offset:4096\n\t"
  "ds_read_b64 v[230:231], %[vx] offset:6144\n\t"
  "v_mfma_f32_16x16x32_bf16 v[48:51], v[72:75], v[136:139], v[48:51]\n\t"
  "ds_read_b128 v[184:187], %[vs] offset:6144\n\t"
  "ds_read_b128 v[188:191], %[vs2] offset:6144\n\t"
  "v_mfma_f32_16x16x32_bf16 v[44:47], v[72:75], v[132:135], v[44:47]\n\t"
  "buffer_load_dwordx4 a[124:127], %[vl16], %[srd], s57 offen offset:1024 sc1\n\t"
  "ds_read_b64 v[232:233], %[vx] offset:8192\n\t"
  "v_mfma_f32_16x16x32_bf16 v[40:43], v[72:75], v[128:131], v[40:43]\n\t"
  "ds_read_b128 v[192:195], %[vs] offset:8192\n\t"
  "ds_read_b128 v[196:199], %[vs2] offset:8192\n\t"
  "v_mfma_f32_16x16x32_bf16 v[60:63], v[76:79], v[144:147], v[60:63]\n\t"
  "ds_read_b64 v[234:235], %[vx] offset:10240\n\t"
  "ds_read_b128 v[200:203], %[vs] offset:10240\n\t"
  "v_mfma_f32_16x16x32_bf16 v[56:59], v[76:79], v[140:143], v[56:59]\n\t"
  "ds_read_b128 v[204:207], %[vs2] offset:10240\n\t"
  "ds_read_b64 v[236:237], %[vx] offset:12288\n\t"
  "v_mfma_f32_16x16x32_bf16 v[52:55], v[76:79], v[136:139], v[52:55]\n\t"
  "buffer_load_dwordx4 a[128:131], %[vl16], %[srd], s57 offen offset:2048 sc1\n\t"
  "ds_read_b128 v[208:211], %[vs] offset:12288\n\t"
  "v_mfma_f32_16x16x32_bf16 v[48:51], v[76:79], v[132:135], v[48:51]\n\t"
  "ds_read_b128 v[212:215], %[vs2] offset:12288\n\t"
  "ds_read_b64 v[238:239], %[vx] offset:14336\n\t"
  "v_mfma_f32_16x16x32_bf16 v[44:47], v[76:79], v[128:131], v[44:47]\n\t"
  "ds_read_b128 v[216:219], %[vs] offset:14336\n\t"
  "ds_read_b128 v[220:223], %[vs2] offset:14336\n\t"
  "v_mfma_f32_16x16x32_bf16 v[60:63], v[80:83], v[140:143], v[60:63]\n\t"
  "buffer_load_dwordx4 a[132:135], %[vl16], %[srd], s57 offen offset:3072 sc1\n\t"
  "v_mfma_f32_16x16x32_bf16 v[56:59], v[80:83], v[136:139], v[56:59]\n\t"
  "v_mfma_f32_16x16x32_bf16 v[52:55], v[80:83], v[132:135], v[52:55]\n\t"
  "v_mfma_f32_16x16x32_bf16 v[48:51], v[80:83], v[128:131], v[48:51]\n\t"
  "v_mfma_f32_16x16x32_bf16 v[60:63], v[84:87], v[136:139], v[60:63]\n\t"
  "v_mfma_f32_16x16x32_bf16 v[56:59], v[84:87], v[132:135], v[56:59]\n\t"
  "v_mfma_f32_16x16x32_bf16 v[52:55], v[84:87], v[128:131], v[52:55]\n\t"
  "s_waitcnt lgkmcnt(15)\n\t"
  "v_mfma_f32_16x16x32_bf16 v[60:63], v[88:91], v[132:135], v[60:63]\n\t"
  "v_mfma_f32_16x16x32_bf16 v[56:59], v[88:91], v[128:131], v[56:59]\n\t"
  "v_mfma_f32_16x16x32_bf16 v[60:63], v[92:95], v[128:131], v[60:63]\n\t"
  "v_lshlrev_b32 v96, 16, v224\n\t"
  "v_and_b32 v97, 0xffff0000, v224\n\t"
  "v_lshlrev_b32 v98, 16, v225\n\t"
  "v_and_b32 v99, 0xffff0000, v225\n\t"
  "v_lshlrev_b32 v100, 16, v226\n\t"
  "v_and_b32 v101, 0xffff0000, v226\n\t"
  "v_lshlrev_b32 v102, 16, v227\n\t"
  "v_and_b32 v103, 0xffff0000, v227\n\t"
  "v_pk_fma_f32 v[160:161], v[96:97], v[160:161], v[162:163]\n\t"
  "v_pk_fma_f32 v[164:165], v[98:99], v[164:165], v[166:167]\n\t"
  "v_pk_fma_f32 v[168:169], v[100:101], v[168:169], v[170:171]\n\t"
  "v_pk_fma_f32 v[172:173], v[102:103], v[172:173], v[174:175]\n\t"
  "v_pk_mul_f32 v[96:97], v[96:97], v[246:247] op_sel_hi:[1,0]\n\t"
  "v_pk_mul_f32 v[98:99], v[98:99], v[246:247] op_sel_hi:[1,0]\n\t"
  "v_pk_mul_f32 v[100:101], v[100:101], v[246:247] op_sel_hi:[1,0]\n\t"
  "v_pk_mul_f32 v[102:103], v[102:103], v[246:247] op_sel_hi:[1,0]\n\t"
  "v_pk_fma_f32 v[160:161], v[160:161], v[244:245], v[244:245] op_sel:[0,0,1] op_sel_hi:[1,0,1]\n\t"
  "v_pk_fma_f32 v[164:165], v[164:165], v[244:245], v[244:245] op_sel:[0,0,1] op_sel_hi:[1,0,1]\n\t"
  "v_pk_fma_f32 v[168:169], v[168:169], v[244:245], v[244:245] op_sel:[0,0,1] op_sel_hi:[1,0,1]\n\t"
  "v_pk_fma_f32 v[172:173], v[172:173], v[244:245], v[244:245] op_sel:[0,0,1] op_sel_hi:[1,0,1]\n\t"
  "v_pk_fma_f32 v[96:97], v[160:161], v[32:33], v[96:97]\n\t"
  "v_pk_fma_f32 v[98:99], v[164:165], v[34:35], v[98:99]\n\t"
  "v_pk_fma_f32 v[100:101], v[168:169], v[36:37], v[100:101]\n\t"
  "v_pk_fma_f32 v[102:103], v[172:173], v[38:39], v[102:103]\n\t"
  "v_cvt_pk_bf16_f32 v64, v96, v97\n\t"
  "v_cvt_pk_bf16_f32 v66, v100, v101\n\t"
  "v_cvt_pk_bf16_f32 v65, v98, v99\n\t"
  "v_cvt_pk_bf16_f32 v67, v102, v103\n\t"
  "s_add_u32 s44, %[sO], 0\n\t"
  "v_permlane16_swap_b32 v64, v66\n\t"
  "v_permlane16_swap_b32 v65, v67\n\t"
  "buffer_store_dwordx4 v[64:67], %[vo], %[srd], s44 offen sc1\n\t"
  "v_lshlrev_b32 v104, 16, v228\n\t"
  "v_and_b32 v105, 0xffff0000, v228\n\t"
  "v_lshlrev_b32 v106, 16, v229\n\t"
  "v_and_b32 v107, 0xffff0000, v229\n\t"
  "s_waitcnt lgkmcnt(14)\n\t"
  "v_lshlrev_b32 v108, 16, v230\n\t"
  "v_and_b32 v109, 0xffff0000, v230\n\t"
  "v_lshlrev_b32 v110, 16, v231\n\t"
  "v_and_b32 v111, 0xffff0000, v231\n\t"
  "v_pk_fma_f32 v[176:177], v[104:105], v[176:177], v[178:179]\n\t"
  "v_pk_fma_f32 v[180:181], v[106:107], v[180:181], v[182:183]\n\t"
  "s_waitcnt lgkmcnt(13)\n\t"
  "v_pk_fma_f32 v[184:185], v[108:109], v[184:185], v[186:187]\n\t"
  "s_waitcnt lgkmcnt(12)\n\t"
  "v_pk_fma_f32 v[188:189], v[110:111], v[188:189], v[190:191]\n\t"
  "v_pk_mul_f32 v[104:105], v[104:105], v[246:247] op_sel_hi:[1,0]\n\t"
  "v_pk_mul_f32 v[106:107], v[106:107], v[246:247] op_sel_hi:[1,0]\n\t"
  "v_pk_mul_f32 v[108:109], v[108:109], v[246:247] op_sel_hi:[1,0]\n\t"
  "v_pk_mul_f32 v[110:111], v[110:111], v[246:247] op_sel_hi:[1,0]\n\t"
  "v_pk_fma_f32 v[176:177], v[176:177], v[244:245], v[244:245] op_sel:[0,0,1] op_sel_hi:[1,0,1]\n\t"
  "v_pk_fma_f32 v[180:181], v[180:181], v[244:245], v[244:245] op_sel:[0,0,1] op_sel_hi:[1,0,1]\n\t"
  "v_pk_fma_f32 v[184:185], v[184:185], v[244:245], v[244:245] op_sel:[0,0,1] op_sel_hi:[1,0,1]\n\t"
  "v_pk_fma_f32 v[188:189], v[188:189], v[244:245], v[244:245] op_sel:[0,0,1] op_sel_hi:[1,0,1]\n\t"
  "v_pk_fma_f32 v[104:105], v[176:177], v[40:41], v[104:105]\n\t"
  "v_pk_fma_f32 v[106:107], v[180:181], v[42:43], v[106:107]\n\t"
  "v_pk_fma_f32 v[108:109], v[184:185], v[44:45], v[108:109]\n\t"
  "v_pk_fma_f32 v[110:111], v[188:189], v[46:47], v[110:111]\n\t"
  "v_cvt_pk_bf16_f32 v68, v104, v105\n\t"
  "v_cvt_pk_bf16_f32 v70, v108, v109\n\t"
  "v_cvt_pk_bf16_f32 v69, v106, v107\n\t"
  "v_cvt_pk_bf16_f32 v71, v110, v111\n\t"
  "s_add_u32 s44, %[sO], 655360\n\t"
  "v_permlane16_swap_b32 v68, v70\n\t"
  "v_permlane16_swap_b32 v69, v71\n\t"
  "buffer_store_dwordx4 v[68:71], %[vo], %[srd], s44 offen sc1\n\t"
  "s_waitcnt lgkmcnt(11)\n\t"
  "v_lshlrev_b32 v112, 16, v232\n\t"
  "v_and_b32 v113, 0xffff0000, v232\n\t"
  "v_lshlrev_b32 v114, 16, v233\n\t"
  "v_and_b32 v115, 0xffff0000, v233\n\t"
  "s_waitcnt lgkmcnt(8)\n\t"
  "v_lshlrev_b32 v116, 16, v234\n\t"
  "v_and_b32 v117, 0xffff0000, v234\n\t"
  "v_lshlrev_b32 v118, 16, v235\n\t"
  "v_and_b32 v119, 0xffff0000, v235\n\t"
  "v_pk_fma_f32 v[192:193], v[112:113], v[192:193], v[194:195]\n\t"
  "v_pk_fma_f32 v[196:197], v[114:115], v[196:197], v[198:199]\n\t"
  "s_waitcnt lgkmcnt(7)\n\t"
  "v_pk_fma_f32 v[200:201], v[116:117], v[200:201], v[202:203]\n\t"
  "s_waitcnt lgkmcnt(6)\n\t"
  "v_pk_fma_f32 v[204:205], v[118:119], v[204:205], v[206:207]\n\t"
  "v_pk_mul_f32 v[112:113], v[112:113], v[246:247] op_sel_hi:[1,0]\n\t"
  "v_pk_mul_f32 v[114:115], v[114:115], v[246:247] op_sel_hi:[1,0]\n\t"
  "v_pk_mul_f32 v[116:117], v[116:117], v[246:247] op_sel_hi:[1,0]\n\t"
  "v_pk_mul_f32 v[118:119], v[118:119], v[246:247] op_sel_hi:[1,0]\n\t"
  "v_pk_fma_f32 v[192:193], v[192:193], v[244:245], v[244:245] op_sel:[0,0,1] op_sel_hi:[1,0,1]\n\t"
  "v_pk_fma_f32 v[196:197], v[196:197], v[244:245], v[244:245] op_sel:[0,0,1] op_sel_hi:[1,0,1]\n\t"
  "v_pk_fma_f32 v[200:201], v[200:201], v[244:245], v[244:245] op_sel:[0,0,1] op_sel_hi:[1,0,1]\n\t"
  "v_pk_fma_f32 v[204:205], v[204:205], v[244:245], v[244:245] op_sel:[0,0,1] op_sel_hi:[1,0,1]\n\t"
  "v_pk_fma_f32 v[112:113], v[192:193], v[48:49], v[112:113]\n\t"
  "v_pk_fma_f32 v[114:115], v[196:197], v[50:51], v[114:115]\n\t"
  "v_pk_fma_f32 v[116:117], v[200:201], v[52:53], v[116:117]\n\t"
  "v_pk_fma_f32 v[118:119], v[204:205], v[54:55], v[118:119]\n\t"
  "v_cvt_pk_bf16_f32 v72, v112, v113\n\t"
  "v_cvt_pk_bf16_f32 v74, v116, v117\n\t"
  "v_cvt_pk_bf16_f32 v73, v114, v115\n\t"
  "v_cvt_pk_bf16_f32 v75, v118, v119\n\t"
  "s_add_u32 s44, %[sO], 1310720\n\t"
  "v_permlane16_swap_b32 v72, v74\n\t"
  "v_permlane16_swap_b32 v73, v75\n\t"
  "buffer_store_dwordx4 v[72:75], %[vo], %[srd], s44 offen sc1\n\t"
  "s_waitcnt lgkmcnt(5)\n\t"
  "v_lshlrev_b32 v120, 16, v236\n\t"
  "v_and_b32 v121, 0xffff0000, v236\n\t"
  "v_lshlrev_b32 v122, 16, v237\n\t"
  "v_and_b32 v123, 0xffff0000, v237\n\t"
  "s_waitcnt lgkmcnt(2)\n\t"
  "v_lshlrev_b32 v124, 16, v238\n\t"
  "v_and_b32 v125, 0xffff0000, v238\n\t"
  "v_lshlrev_b32 v126, 16, v239\n\t"
  "v_and_b32 v127, 0xffff0000, v239\n\t"
  "v_pk_fma_f32 v[208:209], v[120:121], v[208:209], v[210:211]\n\t"
  "v_pk_fma_f32 v[212:213], v[122:123], v[212:213], v[214:215]\n\t"
  "s_waitcnt lgkmcnt(1)\n\t"
  "v_pk_fma_f32 v[216:217], v[124:125], v[216:217], v[218:219]\n\t"
  "s_waitcnt lgkmcnt(0)\n\t"
  "v_pk_fma_f32 v[220:221], v[126:127], v[220:221], v[222:223]\n\t"
  "v_pk_mul_f32 v[120:121], v[120:121], v[246:247] op_sel_hi:[1,0]\n\t"
  "v_pk_mul_f32 v[122:123], v[122:123], v[246:247] op_sel_hi:[1,0]\n\t"
  "v_pk_mul_f32 v[124:125], v[124:125], v[246:247] op_sel_hi:[1,0]\n\t"
  "v_pk_mul_f32 v[126:127], v[126:127], v[246:247] op_sel_hi:[1,0]\n\t"
  "v_pk_fma_f32 v[208:209], v[208:209], v[244:245], v[244:245] op_sel:[0,0,1] op_sel_hi:[1,0,1]\n\t"
  "v_pk_fma_f32 v[212:213], v[212:213], v[244:245], v[244:245] op_sel:[0,0,1] op_sel_hi:[1,0,1]\n\t"
  "v_pk_fma_f32 v[216:217], v[216:217], v[244:245], v[244:245] op_sel:[0,0,1] op_sel_hi:[1,0,1]\n\t"
  "v_pk_fma_f32 v[220:221], v[220:221], v[244:245], v[244:245] op_sel:[0,0,1] op_sel_hi:[1,0,1]\n\t"
  "v_pk_fma_f32 v[120:121], v[208:209], v[56:57], v[120:121]\n\t"
  "v_pk_fma_f32 v[122:123], v[212:213], v[58:59], v[122:123]\n\t"
  "v_pk_fma_f32 v[124:125], v[216:217], v[60:61], v[124:125]\n\t"
  "v_pk_fma_f32 v[126:127], v[220:221], v[62:63], v[126:127]\n\t"
  "v_cvt_pk_bf16_f32 v76, v120, v121\n\t"
  "v_cvt_pk_bf16_f32 v78, v124, v125\n\t"
  "v_cvt_pk_bf16_f32 v77, v122, v123\n\t"
  "v_cvt_pk_bf16_f32 v79, v126, v127\n\t"
  "s_add_u32 s44, %[sO], 1966080\n\t"
  "v_permlane16_swap_b32 v76, v78\n\t"
  "v_permlane16_swap_b32 v77, v79\n\t"
  "buffer_store_dwordx4 v[76:79], %[vo], %[srd], s44 offen sc1\n\t"
  "s_nop 1\n\t"
  "s_branch Lcend%=\n\t"
  "La1_slow%=:\n\t"
  "s_nop 7\n\t"
  "s_nop 3\n\t"
  "s_mov_b32 s49, 0x40000\n\t"
  "La1_loop%=:\n\t"
  "s_sleep 3\n\t"
  "buffer_load_dwordx2 v[160:161], %[vf1], %[srd], 0 offen sc1\n\t"
  "s_waitcnt vmcnt(0)\n\t"
  "v_cmp_ne_u64 vcc, v[252:253], v[160:161]\n\t"
  "s_nop 3\n\t"
  "s_cmp_eq_u64 vcc, 0\n\t"
  "s_cbranch_scc1 La1_back%=\n\t"
  "s_sub_u32 s49, s49, 1\n\t"
  "s_cmp_lg_u32 s49, 0\n\t"
  "s_cbranch_scc1 La1_loop%=\n\t"
  "s_branch La1_back%=\n\t"
  "La2_slow%=:\n\t"
  "s_nop 7\n\t"
  "s_nop 3\n\t"
  "s_mov_b32 s49, 0x40000\n\t"
  "La2_loop%=:\n\t"
  "s_sleep 3\n\t"
  "buffer_load_dwordx2 v[162:163], %[vf2], %[srd], 0 offen sc1\n\t"
  "s_waitcnt vmcnt(0)\n\t"
  "v_cmp_ne_u64 vcc, v[252:253], v[162:163]\n\t"
  "s_nop 3\n\t"
  "s_cmp_eq_u64 vcc, 0\n\t"
  "s_cbranch_scc1 La2_back%=\n\t"
  "s_sub_u32 s49, s49, 1\n\t"
  "s_cmp_lg_u32 s49, 0\n\t"
  "s_cbranch_scc1 La2_loop%=\n\t"
  "s_branch La2_back%=\n\t"
  "Lcend%=:\n\t"
      : [wdone] "=&s"(wdone)
      : [srd] "s"(srd), [sO] "s"(OFF_X2P + d * 128u), [w] "s"(w), [sKV] "s"(OFF_KVT + cb * 128u + w * 655360u),
        [tlo] "s"((u32)tag), [thi] "s"((u32)(tag >> 32)), [sW1] "s"(OFF_W1F + (j * 4 + w) * 61440u),
        [vf2] "v"(c_vf2),
        [vf1] "v"(c_vf1), [vf3] "v"(c_vf3), [vfw] "v"(c_vfw),
        [va] "v"(c_va), [vb0] "v"(c_vb0), [vb1] "v"(c_vb1), [vb2] "v"(c_vb2), [vb3] "v"(c_vb3), [vx] "v"(c_vx), [vs] "v"(c_vs), [vs2] "v"(c_vs2), [vo] "v"(c_vo), [vd] "v"(c_vd),
        [vl16] "v"(c_vl16), [vkv] "v"(c_vkv),
        [vmod] "v"(c_vmod)
      : "memory", "vcc", "scc",
"a0", "a1", "a2", "a3", "a4", "a5", "a6", "a7", "a8", "a9", "a10", "a11", "a12", "a13", "a14", "a15", "a16", "a17", "a18", "a19", "a20", "a21", "a22", "a23", "a24", "a25", "a26", "a27", "a28", "a29", "a30", "a31", "a32", "a33", "a34", "a35", "a36", "a37", "a38", "a39", "a40", "a41", "a42", "a43", "a44", "a45", "a46", "a47", "a48", "a49", "a50", "a51", "a52", "a53", "a54", "a55", "a56", "a57", "a58", "a59", "a60", "a61", "a62", "a63", "a64", "a65", "a66", "a67", "a68", "a69", "a70", "a71", "a72", "a73", "a74", "a75", "a76", "a77", "a78", "a79", "a80", "a81", "a82", "a83", "a84", "a85", "a86", "a87", "a88", "a89", "a90", "a91", "a92", "a93", "a94", "a95", "a96", "a97", "a98", "a99", "a100", "a101", "a102", "a103", "a104", "a105", "a106", "a107", "a108", "a109", "a110", "a111", "a112", "a113", "a114", "a115", "a116", "a117", "a118", "a119", "a120", "a121", "a122", "a123", "a124", "a125", "a126", "a127", "a128", "a129", "a130", "a131", "a132", "a133", "a134", "a135", "s44", "s45", "s46", "s47", "s48", "s49", "s51", "s52", "s53", "s54", "s55", "s56", "s57", "v32", "v33", "v34", "v35", "v36", "v37", "v38", "v39", "v40", "v41", "v42", "v43", "v44", "v45", "v46", "v47", "v48", "v49", "v50", "v51", "v52", "v53", "v54", "v55", "v56", "v57", "v58", "v59", "v60", "v61", "v62", "v63", "v64", "v65", "v66", "v67", "v68", "v69", "v70", "v71", "v72", "v73", "v74", "v75", "v76", "v77", "v78", "v79", "v80", "v81", "v82", "v83", "v84", "v85", "v86", "v87", "v88", "v89", "v90", "v91", "v92", "v93", "v94", "v95", "v96", "v97", "v98", "v99", "v100", "v101", "v102", "v103", "v104", "v105", "v106", "v107", "v108", "v109", "v110", "v111", "v112", "v113", "v114", "v115", "v116", "v117", "v118", "v119", "v120", "v121", "v122", "v123", "v124", "v125", "v126", "v127", "v128", "v129", "v130", "v131", "v132", "v133", "v134", "v135", "v136", "v137", "v138", "v139", "v140", "v141", "v142", "v143", "v144", "v145", "v146", "v147", "v148", "v149", "v150", "v151", "v152", "v153", "v154", "v155", "v156", "v157", "v158", "v159", "v160", "v161", "v162", "v163", "v164", "v165", "v166", "v167", "v168", "v169", "v170", "v171", "v172", "v173", "v174", "v175", "v176", "v177", "v178", "v179", "v180", "v181", "v182", "v183", "v184", "v185", "v186", "v187", "v188", "v189", "v190", "v191", "v192", "v193", "v194", "v195", "v196", "v197", "v198", "v199", "v200", "v201", "v202", "v203", "v204", "v205", "v206", "v207", "v208", "v209", "v210", "v211", "v212", "v213", "v214", "v215", "v216", "v217", "v218", "v219", "v220", "v221", "v222", "v223", "v224", "v225", "v226", "v227", "v228", "v229", "v230", "v231", "v232", "v233", "v234", "v235", "v236", "v237", "v238", "v239", "v244", "v245", "v246", "v247", "v250", "v251", "v252", "v253", "v254", "v255"
    );
  }
  STAMP(4);
  STAMP_OUT(1);
  asm volatile(
  "v_mbcnt_lo_u32_b32 v20, -1, 0\n\t"
  "v_mbcnt_hi_u32_b32 v20, -1, v20\n\t"
  "s_lshl_b32 s52, %[w], 10\n\t"
  "s_add_u32 s52, s52, 143360\n\t"
  "v_lshl_add_u32 v20, v20, 4, s52\n\t"
  "ds_read_b128 v[0:3], v20 offset:0\n\t"
  "ds_read_b128 v[4:7], v20 offset:4096\n\t"
  "ds_read_b128 v[8:11], v20 offset:8192\n\t"
  "ds_read_b128 v[12:15], v20 offset:12288\n\t"
  "ds_read_b128 v[16:19], v20 offset:16384\n\t"
  "s_waitcnt lgkmcnt(0)\n\t"
  "v_mov_b32 v132, %[tlo]\n\t"
  "v_mov_b32 v133, %[thi]\n\t"
  "v_mov_b32 v134, %[tlo]\n\t"
  "v_mov_b32 v135, %[thi]\n\t"
  "s_lshl_b32 s52, %[w], 12\n\t"
  "s_mov_b64 s[60:61], 0\n\t"
  "v_bfrev_b32 v128, 1\n\t"
  "v_add_u32 v124, 40960, v6\n\t"
  "v_mov_b32 v112, 0\n\t"
  "v_mov_b32 v113, 0\n\t"
  "v_mov_b32 v114, 0\n\t"
  "v_mov_b32 v115, 0\n\t"
  "v_mov_b32 v116, 0\n\t"
  "v_mov_b32 v117, 0\n\t"
  "v_mov_b32 v118, 0\n\t"
  "v_mov_b32 v119, 0\n\t"
  "v_mov_b32 v120, 0\n\t"
  "v_mov_b32 v121, 0\n\t"
  "v_and_b32 v136, 0xf0, v1\n\t"
  "v_lshlrev_b32 v137, 3, v136\n\t"
  "v_lshlrev_b32 v136, 2, v136\n\t"
  "s_add_u32 s54, %[sST], 65536\n\t"
  "s_mul_i32 s48, %[w], 20480\n\t"
  "v_add_u32 v141, s48, v1\n\t"
  "v_xor_b32 v109, 32, v5\n\t"
  "v_mov_b32 v108, v5\n\t"
  "v_mul_u32_u24 v139, 3, v1\n\t"
  "s_mov_b32 s47, 261120\n\t"
  "v_not_b32 v126, v132\n\t"
  "v_mov_b32 v127, 0\n\t"
  "s_waitcnt vmcnt(0) lgkmcnt(0)\n\t"
  "s_barrier\n\t"
  "buffer_store_dwordx4 v[132:135], v0, %[srd], 0 offen sc1\n\t"
  "s_movk_i32 s59, 0\n\t"
  "buffer_store_dwordx4 v[112:115], v9, %[srd], s59 offen sc1\n\t"
  "s_movk_i32 s59, 8192\n\t"
  "buffer_store_dwordx4 v[112:115], v9, %[srd], s59 offen sc1\n\t"
  "s_movk_i32 s59, 16384\n\t"
  "buffer_store_dwordx4 v[112:115], v9, %[srd], s59 offen sc1\n\t"
  "s_movk_i32 s59, 24576\n\t"
  "buffer_store_dwordx4 v[112:115], v9, %[srd], s59 offen sc1\n\t"
  "s_bitcmp1_b32 %[wdone], 1\n\t"
  "s_cbranch_scc1 Lw2%=\n\t"
  "s_mov_b32 s49, 0x40000\n\t"
  "Lpw_loop%=:\n\t"
  "buffer_load_dwordx2 v[130:131], v2, %[srd], 0 offen sc1\n\t"
  "s_waitcnt vmcnt(0)\n\t"
  "v_cmp_eq_u64 vcc, v[132:133], v[130:131]\n\t"
  "s_nop 3\n\t"
  "s_orn2_b64 vcc, vcc, 15\n\t"
  "s_cmp_eq_u64 vcc, -1\n\t"
  "s_cbranch_scc1 Lpw_done%=\n\t"
  "s_sleep 3\n\t"
  "s_sub_u32 s49, s49, 1\n\t"
  "s_cmp_lg_u32 s49, 0\n\t"
  "s_cbranch_scc1 Lpw_loop%=\n\t"
  "Lpw_done%=:\n\t"
  "s_add_u32 s44, %[sW1], 12288\n\t"
  "buffer_load_dwordx4 a[56:59], v1, %[srd], s44 offen offset:2048 sc1\n\t"
  "s_add_u32 s44, %[sW1], 12288\n\t"
  "buffer_load_dwordx4 a[60:63], v1, %[srd], s44 offen offset:3072 sc1\n\t"
  "s_add_u32 s44, %[sW1], 16384\n\t"
  "buffer_load_dwordx4 a[64:67], v1, %[srd], s44 offen offset:0 sc1\n\t"
  "s_add_u32 s44, %[sW1], 16384\n\t"
  "buffer_load_dwordx4 a[68:71], v1, %[srd], s44 offen offset:1024 sc1\n\t"
  "s_add_u32 s44, %[sW1], 16384\n\t"
  "buffer_load_dwordx4 a[72:75], v1, %[srd], s44 offen offset:2048 sc1\n\t"
  "s_add_u32 s44, %[sW1], 16384\n\t"
  "buffer_load_dwordx4 a[76:79], v1, %[srd], s44 offen offset:3072 sc1\n\t"
  "s_add_u32 s44, %[sW1], 20480\n\t"
  "buffer_load_dwordx4 a[80:83], v1, %[srd], s44 offen offset:0 sc1\n\t"
  "s_add_u32 s44, %[sW1], 20480\n\t"
  "buffer_load_dwordx4 a[84:87], v1, %[srd], s44 offen offset:1024 sc1\n\t"
  "s_add_u32 s44, %[sW1], 20480\n\t"
  "buffer_load_dwordx4 a[88:91], v1, %[srd], s44 offen offset:2048 sc1\n\t"
  "s_add_u32 s44, %[sW1], 20480\n\t"
  "buffer_load_dwordx4 a[92:95], v1, %[srd], s44 offen offset:3072 sc1\n\t"
  "s_add_u32 s44, %[sW1], 24576\n\t"
  "buffer_load_dwordx4 a[96:99], v1, %[srd], s44 offen offset:0 sc1\n\t"
  "s_add_u32 s44, %[sW1], 24576\n\t"
  "buffer_load_dwordx4 a[100:103], v1, %[srd], s44 offen offset:1024 sc1\n\t"
  "s_add_u32 s44, %[sW1], 24576\n\t"
  "buffer_load_dwordx4 a[104:107], v1, %[srd], s44 offen offset:2048 sc1\n\t"
  "s_add_u32 s44, %[sW1], 24576\n\t"
  "buffer_load_dwordx4 a[108:111], v1, %[srd], s44 offen offset:3072 sc1\n\t"
  "s_add_u32 s44, %[sW1], 28672\n\t"
  "buffer_load_dwordx4 a[112:115], v1, %[srd], s44 offen offset:0 sc1\n\t"
  "s_add_u32 s44, %[sW1], 28672\n\t"
  "buffer_load_dwordx4 a[116:119], v1, %[srd], s44 offen offset:1024 sc1\n\t"
  "s_add_u32 s44, %[sW1], 28672\n\t"
  "buffer_load_dwordx4 a[120:123], v1, %[srd], s44 offen offset:2048 sc1\n\t"
  "s_add_u32 s44, %[sW1], 28672\n\t"
  "buffer_load_dwordx4 a[124:127], v1, %[srd], s44 offen offset:3072 sc1\n\t"
  "s_add_u32 s44, %[sW1], 32768\n\t"
  "buffer_load_dwordx4 a[128:131], v1, %[srd], s44 offen offset:0 sc1\n\t"
  "s_add_u32 s44, %[sW1], 32768\n\t"
  "buffer_load_dwordx4 a[132:135], v1, %[srd], s44 offen offset:1024 sc1\n\t"
  "Lw2%=:\n\t"
  "s_bitcmp1_b32 %[wdone], 0\n\t"
  "s_cbranch_scc1 Lw1%=\n\t"
  "s_add_u32 s44, %[sW1], 0\n\t"
  "buffer_load_dwordx4 a[0:3], v1, %[srd], s44 offen offset:0 sc1\n\t"
  "s_add_u32 s44, %[sW1], 0\n\t"
  "buffer_load_dwordx4 a[4:7], v1, %[srd], s44 offen offset:1024 sc1\n\t"
  "s_add_u32 s44, %[sW1], 0\n\t"
  "buffer_load_dwordx4 a[8:11], v1, %[srd], s44 offen offset:2048 sc1\n\t"
  "s_add_u32 s44, %[sW1], 0\n\t"
  "buffer_load_dwordx4 a[12:15], v1, %[srd], s44 offen offset:3072 sc1\n\t"
  "s_add_u32 s44, %[sW1], 4096\n\t"
  "buffer_load_dwordx4 a[16:19], v1, %[srd], s44 offen offset:0 sc1\n\t"
  "s_add_u32 s44, %[sW1], 4096\n\t"
  "buffer_load_dwordx4 a[20:23], v1, %[srd], s44 offen offset:1024 sc1\n\t"
  "s_add_u32 s44, %[sW1], 4096\n\t"
  "buffer_load_dwordx4 a[24:27], v1, %[srd], s44 offen offset:2048 sc1\n\t"
  "s_add_u32 s44, %[sW1], 4096\n\t"
  "buffer_load_dwordx4 a[28:31], v1, %[srd], s44 offen offset:3072 sc1\n\t"
  "s_add_u32 s44, %[sW1], 8192\n\t"
  "buffer_load_dwordx4 a[32:35], v1, %[srd], s44 offen offset:0 sc1\n\t"
  "s_add_u32 s44, %[sW1], 8192\n\t"
  "buffer_load_dwordx4 a[36:39], v1, %[srd], s44 offen offset:1024 sc1\n\t"
  "s_add_u32 s44, %[sW1], 8192\n\t"
  "buffer_load_dwordx4 a[40:43], v1, %[srd], s44 offen offset:2048 sc1\n\t"
  "s_add_u32 s44, %[sW1], 8192\n\t"
  "buffer_load_dwordx4 a[44:47], v1, %[srd], s44 offen offset:3072 sc1\n\t"
  "s_add_u32 s44, %[sW1], 12288\n\t"
  "buffer_load_dwordx4 a[48:51], v1, %[srd], s44 offen offset:0 sc1\n\t"
  "s_add_u32 s44, %[sW1], 12288\n\t"
  "buffer_load_dwordx4 a[52:55], v1, %[srd], s44 offen offset:1024 sc1\n\t"
  "Lw1%=:\n\t"
  "s_mov_b32 s58, 4\n\t"
  "s_bitcmp1_b32 %[wdone], 2\n\t"
  "s_cbranch_scc1 Lpx_chk%=\n\t"
  "s_mov_b32 s58, 0\n\t"
  "v_cmp_eq_u64 vcc, v[132:133], v[126:127]\n\t"
  "s_nop 3\n\t"
  "s_or_b64 s[60:61], s[60:61], vcc\n\t"
  "s_cmp_eq_u64 s[60:61], -1\n\t"
  "s_cbranch_scc1 Lpx_done%=\n\t"
  "v_cndmask_b32 v3, v3, v128, vcc\n\t"
  "buffer_load_dwordx2 v[126:127], v3, %[srd], 0 offen sc1\n\t"
  "s_add_u32 s44, %[sW1], 32768\n\t"
  "buffer_load_dwordx4 a[136:139], v1, %[srd], s44 offen offset:2048 sc1\n\t"
  "s_add_u32 s44, %[sW1], 32768\n\t"
  "buffer_load_dwordx4 a[140:143], v1, %[srd], s44 offen offset:3072 sc1\n\t"
  "s_add_u32 s44, %[sW1], 36864\n\t"
  "buffer_load_dwordx4 a[144:147], v1, %[srd], s44 offen offset:0 sc1\n\t"
  "s_add_u32 s44, %[sW1], 36864\n\t"
  "buffer_load_dwordx4 a[148:151], v1, %[srd], s44 offen offset:1024 sc1\n\t"
  "s_add_u32 s44, %[sW1], 36864\n\t"
  "buffer_load_dwordx4 a[152:155], v1, %[srd], s44 offen offset:2048 sc1\n\t"
  "s_add_u32 s44, %[sW1], 36864\n\t"
  "buffer_load_dwordx4 a[156:159], v1, %[srd], s44 offen offset:3072 sc1\n\t"
  "s_waitcnt vmcnt(6)\n\t"
  "s_mov_b32 s58, 1\n\t"
  "v_cmp_eq_u64 vcc, v[132:133], v[126:127]\n\t"
  "s_nop 3\n\t"
  "s_or_b64 s[60:61], s[60:61], vcc\n\t"
  "s_cmp_eq_u64 s[60:61], -1\n\t"
  "s_cbranch_scc1 Lpx_done%=\n\t"
  "v_cndmask_b32 v3, v3, v128, vcc\n\t"
  "buffer_load_dwordx2 v[126:127], v3, %[srd], 0 offen sc1\n\t"
  "s_add_u32 s44, %[sW1], 40960\n\t"
  "buffer_load_dwordx4 a[160:163], v1, %[srd], s44 offen offset:0 sc1\n\t"
  "s_add_u32 s44, %[sW1], 40960\n\t"
  "buffer_load_dwordx4 a[164:167], v1, %[srd], s44 offen offset:1024 sc1\n\t"
  "s_add_u32 s44, %[sW1], 40960\n\t"
  "buffer_load_dwordx4 a[168:171], v1, %[srd], s44 offen offset:2048 sc1\n\t"
  "s_add_u32 s44, %[sW1], 40960\n\t"
  "buffer_load_dwordx4 a[172:175], v1, %[srd], s44 offen offset:3072 sc1\n\t"
  "s_add_u32 s44, %[sW1], 45056\n\t"
  "buffer_load_dwordx4 a[176:179], v1, %[srd], s44 offen offset:0 sc1\n\t"
  "s_add_u32 s44, %[sW1], 45056\n\t"
  "buffer_load_dwordx4 a[180:183], v1, %[srd], s44 offen offset:1024 sc1\n\t"
  "s_add_u32 s44, %[sW1], 45056\n\t"
  "buffer_load_dwordx4 a[184:187], v1, %[srd], s44 offen offset:2048 sc1\n\t"
  "s_waitcnt vmcnt(7)\n\t"
  "s_mov_b32 s58, 2\n\t"
  "v_cmp_eq_u64 vcc, v[132:133], v[126:127]\n\t"
  "s_nop 3\n\t"
  "s_or_b64 s[60:61], s[60:61], vcc\n\t"
  "s_cmp_eq_u64 s[60:61], -1\n\t"
  "s_cbranch_scc1 Lpx_done%=\n\t"
  "v_cndmask_b32 v3, v3, v128, vcc\n\t"
  "buffer_load_dwordx2 v[126:127], v3, %[srd], 0 offen sc1\n\t"
  "s_add_u32 s44, %[sW1], 45056\n\t"
  "buffer_load_dwordx4 a[188:191], v1, %[srd], s44 offen offset:3072 sc1\n\t"
  "s_add_u32 s44, %[sW1], 49152\n\t"
  "buffer_load_dwordx4 a[192:195], v1, %[srd], s44 offen offset:0 sc1\n\t"
  "s_add_u32 s44, %[sW1], 49152\n\t"
  "buffer_load_dwordx4 a[196:199], v1, %[srd], s44 offen offset:1024 sc1\n\t"
  "s_add_u32 s44, %[sW1], 49152\n\t"
  "buffer_load_dwordx4 a[200:203], v1, %[srd], s44 offen offset:2048 sc1\n\t"
  "s_add_u32 s44, %[sW1], 49152\n\t"
  "buffer_load_dwordx4 a[204:207], v1, %[srd], s44 offen offset:3072 sc1\n\t"
  "s_add_u32 s44, %[sW1], 53248\n\t"
  "buffer_load_dwordx4 a[208:211], v1, %[srd], s44 offen offset:0 sc1\n\t"
  "s_add_u32 s44, %[sW1], 53248\n\t"
  "buffer_load_dwordx4 a[212:215], v1, %[srd], s44 offen offset:1024 sc1\n\t"
  "s_waitcnt vmcnt(7)\n\t"
  "s_mov_b32 s58, 3\n\t"
  "v_cmp_eq_u64 vcc, v[132:133], v[126:127]\n\t"
  "s_nop 3\n\t"
  "s_or_b64 s[60:61], s[60:61], vcc\n\t"
  "s_cmp_eq_u64 s[60:61], -1\n\t"
  "s_cbranch_scc1 Lpx_done%=\n\t"
  "v_cndmask_b32 v3, v3, v128, vcc\n\t"
  "buffer_load_dwordx2 v[126:127], v3, %[srd], 0 offen sc1\n\t"
  "s_add_u32 s44, %[sW1], 53248\n\t"
  "buffer_load_dwordx4 a[216:219], v1, %[srd], s44 offen offset:2048 sc1\n\t"
  "s_add_u32 s44, %[sW1], 53248\n\t"
  "buffer_load_dwordx4 a[220:223], v1, %[srd], s44 offen offset:3072 sc1\n\t"
  "s_add_u32 s44, %[sW1], 57344\n\t"
  "buffer_load_dwordx4 a[224:227], v1, %[srd], s44 offen offset:0 sc1\n\t"
  "s_add_u32 s44, %[sW1], 57344\n\t"
  "buffer_load_dwordx4 a[228:231], v1, %[srd], s44 offen offset:1024 sc1\n\t"
  "s_add_u32 s44, %[sW1], 57344\n\t"
  "buffer_load_dwordx4 a[232:235], v1, %[srd], s44 offen offset:2048 sc1\n\t"
  "s_add_u32 s44, %[sW1], 57344\n\t"
  "buffer_load_dwordx4 a[236:239], v1, %[srd], s44 offen offset:3072 sc1\n\t"
  "s_waitcnt vmcnt(6)\n\t"
  "s_mov_b32 s58, 4\n\t"
  "Lpx_chk%=:\n\t"
  "s_mov_b32 s49, 0x40000\n\t"
  "Lpx_loop%=:\n\t"
  "v_cmp_eq_u64 vcc, v[132:133], v[126:127]\n\t"
  "s_nop 3\n\t"
  "s_or_b64 s[60:61], s[60:61], vcc\n\t"
  "s_cmp_eq_u64 s[60:61], -1\n\t"
  "s_cbranch_scc1 Lpx_done%=\n\t"
  "v_cndmask_b32 v3, v3, v128, vcc\n\t"
  "s_sleep 2\n\t"
  "buffer_load_dwordx2 v[126:127], v3, %[srd], 0 offen sc1\n\t"
  "s_waitcnt vmcnt(0)\n\t"
  "s_sub_u32 s49, s49, 1\n\t"
  "s_cmp_lg_u32 s49, 0\n\t"
  "s_cbranch_scc1 Lpx_loop%=\n\t"
  "Lpx_done%=:\n\t"
  "s_add_u32 m0, s52, 0\n\t"
  "s_mov_b32 s46, 0\n\t"
  "buffer_load_dwordx4 v4, %[srd], s46 offen offset:0 sc1 lds\n\t"
  "buffer_load_dwordx4 v4, %[srd], s46 offen offset:1024 sc1 lds\n\t"
  "buffer_load_dwordx4 v4, %[srd], s46 offen offset:2048 sc1 lds\n\t"
  "buffer_load_dwordx4 v4, %[srd], s46 offen offset:3072 sc1 lds\n\t"
  "s_add_u32 m0, s52, 16384\n\t"
  "s_mov_b32 s46, 16384\n\t"
  "buffer_load_dwordx4 v4, %[srd], s46 offen offset:0 sc1 lds\n\t"
  "buffer_load_dwordx4 v4, %[srd], s46 offen offset:1024 sc1 lds\n\t"
  "buffer_load_dwordx4 v4, %[srd], s46 offen offset:2048 sc1 lds\n\t"
  "buffer_load_dwordx4 v4, %[srd], s46 offen offset:3072 sc1 lds\n\t"
  "s_add_u32 m0, s52, 32768\n\t"
  "s_mov_b32 s46, 32768\n\t"
  "buffer_load_dwordx4 v4, %[srd], s46 offen offset:0 sc1 lds\n\t"
  "buffer_load_dwordx4 v4, %[srd], s46 offen offset:1024 sc1 lds\n\t"
  "buffer_load_dwordx4 v4, %[srd], s46 offen offset:2048 sc1 lds\n\t"
  "buffer_load_dwordx4 v4, %[srd], s46 offen offset:3072 sc1 lds\n\t"
  "s_add_u32 m0, s52, 49152\n\t"
  "s_mov_b32 s46, 49152\n\t"
  "buffer_load_dwordx4 v4, %[srd], s46 offen offset:0 sc1 lds\n\t"
  "buffer_load_dwordx4 v4, %[srd], s46 offen offset:1024 sc1 lds\n\t"
  "buffer_load_dwordx4 v4, %[srd], s46 offen offset:2048 sc1 lds\n\t"
  "buffer_load_dwordx4 v4, %[srd], s46 offen offset:3072 sc1 lds\n\t"
  "s_add_u32 m0, s52, 65536\n\t"
  "s_mov_b32 s46, 65536\n\t"
  "buffer_load_dwordx4 v4, %[srd], s46 offen offset:0 sc1 lds\n\t"
  "buffer_load_dwordx4 v4, %[srd], s46 offen offset:1024 sc1 lds\n\t"
  "buffer_load_dwordx4 v4, %[srd], s46 offen offset:2048 sc1 lds\n\t"
  "buffer_load_dwordx4 v4, %[srd], s46 offen offset:3072 sc1 lds\n\t"
  "buffer_load_dwordx2 v[142:143], v139, %[srd], s47 offen sc1\n\t"
  "buffer_load_dwordx4 v[248:251], v139, %[srd], s47 offen offset:16 sc1\n\t"
  "buffer_load_dwordx4 v[252:255], v139, %[srd], s47 offen offset:32 sc1\n\t"
  "s_cmp_gt_u32 s58, 0\n\t"
  "s_cbranch_scc1 Lc0%=\n\t"
  "s_add_u32 s44, %[sW1], 32768\n\t"
  "buffer_load_dwordx4 a[136:139], v1, %[srd], s44 offen offset:2048 sc1\n\t"
  "s_add_u32 s44, %[sW1], 32768\n\t"
  "buffer_load_dwordx4 a[140:143], v1, %[srd], s44 offen offset:3072 sc1\n\t"
  "s_add_u32 s44, %[sW1], 36864\n\t"
  "buffer_load_dwordx4 a[144:147], v1, %[srd], s44 offen offset:0 sc1\n\t"
  "s_add_u32 s44, %[sW1], 36864\n\t"
  "buffer_load_dwordx4 a[148:151], v1, %[srd], s44 offen offset:1024 sc1\n\t"
  "s_add_u32 s44, %[sW1], 36864\n\t"
  "buffer_load_dwordx4 a[152:155], v1, %[srd], s44 offen offset:2048 sc1\n\t"
  "s_add_u32 s44, %[sW1], 36864\n\t"
  "buffer_load_dwordx4 a[156:159], v1, %[srd], s44 offen offset:3072 sc1\n\t"
  "Lc0%=:\n\t"
  "s_cmp_gt_u32 s58, 1\n\t"
  "s_cbranch_scc1 Lc1%=\n\t"
  "s_add_u32 s44, %[sW1], 40960\n\t"
  "buffer_load_dwordx4 a[160:163], v1, %[srd], s44 offen offset:0 sc1\n\t"
  "s_add_u32 s44, %[sW1], 40960\n\t"
  "buffer_load_dwordx4 a[164:167], v1, %[srd], s44 offen offset:1024 sc1\n\t"
  "s_add_u32 s44, %[sW1], 40960\n\t"
  "buffer_load_dwordx4 a[168:171], v1, %[srd], s44 offen offset:2048 sc1\n\t"
  "s_add_u32 s44, %[sW1], 40960\n\t"
  "buffer_load_dwordx4 a[172:175], v1, %[srd], s44 offen offset:3072 sc1\n\t"
  "s_add_u32 s44, %[sW1], 45056\n\t"
  "buffer_load_dwordx4 a[176:179], v1, %[srd], s44 offen offset:0 sc1\n\t"
  "s_add_u32 s44, %[sW1], 45056\n\t"
  "buffer_load_dwordx4 a[180:183], v1, %[srd], s44 offen offset:1024 sc1\n\t"
  "s_add_u32 s44, %[sW1], 45056\n\t"
  "buffer_load_dwordx4 a[184:187], v1, %[srd], s44 offen offset:2048 sc1\n\t"
  "Lc1%=:\n\t"
  "s_cmp_gt_u32 s58, 2\n\t"
  "s_cbranch_scc1 Lc2%=\n\t"
  "s_add_u32 s44, %[sW1], 45056\n\t"
  "buffer_load_dwordx4 a[188:191], v1, %[srd], s44 offen offset:3072 sc1\n\t"
  "s_add_u32 s44, %[sW1], 49152\n\t"
  "buffer_load_dwordx4 a[192:195], v1, %[srd], s44 offen offset:0 sc1\n\t"
  "s_add_u32 s44, %[sW1], 49152\n\t"
  "buffer_load_dwordx4 a[196:199], v1, %[srd], s44 offen offset:1024 sc1\n\t"
  "s_add_u32 s44, %[sW1], 49152\n\t"
  "buffer_load_dwordx4 a[200:203], v1, %[srd], s44 offen offset:2048 sc1\n\t"
  "s_add_u32 s44, %[sW1], 49152\n\t"
  "buffer_load_dwordx4 a[204:207], v1, %[srd], s44 offen offset:3072 sc1\n\t"
  "s_add_u32 s44, %[sW1], 53248\n\t"
  "buffer_load_dwordx4 a[208:211], v1, %[srd], s44 offen offset:0 sc1\n\t"
  "s_add_u32 s44, %[sW1], 53248\n\t"
  "buffer_load_dwordx4 a[212:215], v1, %[srd], s44 offen offset:1024 sc1\n\t"
  "Lc2%=:\n\t"
  "s_cmp_gt_u32 s58, 3\n\t"
  "s_cbranch_scc1 Lc3%=\n\t"
  "s_add_u32 s44, %[sW1], 53248\n\t"
  "buffer_load_dwordx4 a[216:219], v1, %[srd], s44 offen offset:2048 sc1\n\t"
  "s_add_u32 s44, %[sW1], 53248\n\t"
  "buffer_load_dwordx4 a[220:223], v1, %[srd], s44 offen offset:3072 sc1\n\t"
  "s_add_u32 s44, %[sW1], 57344\n\t"
  "buffer_load_dwordx4 a[224:227], v1, %[srd], s44 offen offset:0 sc1\n\t"
  "s_add_u32 s44, %[sW1], 57344\n\t"
  "buffer_load_dwordx4 a[228:231], v1, %[srd], s44 offen offset:1024 sc1\n\t"
  "s_add_u32 s44, %[sW1], 57344\n\t"
  "buffer_load_dwordx4 a[232:235], v1, %[srd], s44 offen offset:2048 sc1\n\t"
  "s_add_u32 s44, %[sW1], 57344\n\t"
  "buffer_load_dwordx4 a[236:239], v1, %[srd], s44 offen offset:3072 sc1\n\t"
  "Lc3%=:\n\t"
  "s_waitcnt vmcnt(26)\n\t"
  "s_cmp_eq_u32 s58, 0\n\t"
  "s_cbranch_scc1 Lcw%=\n\t"
  "s_waitcnt vmcnt(20)\n\t"
  "s_cmp_eq_u32 s58, 1\n\t"
  "s_cbranch_scc1 Lcw%=\n\t"
  "s_waitcnt vmcnt(13)\n\t"
  "s_cmp_eq_u32 s58, 2\n\t"
  "s_cbranch_scc1 Lcw%=\n\t"
  "s_waitcnt vmcnt(6)\n\t"
  "s_cmp_eq_u32 s58, 3\n\t"
  "s_cbranch_scc1 Lcw%=\n\t"
  "s_waitcnt vmcnt(0)\n\t"
  "Lcw%=:\n\t"
  "s_barrier\n\t"
  "ds_read_b64_tr_b16 v[68:69], v108 offset:0\n\t"
  "ds_read_b64_tr_b16 v[70:71], v108 offset:2048\n\t"
  "ds_read_b64_tr_b16 v[72:73], v109 offset:0\n\t"
  "ds_read_b64_tr_b16 v[74:75], v109 offset:2048\n\t"
  "ds_read_b64_tr_b16 v[76:77], v108 offset:4096\n\t"
  "ds_read_b64_tr_b16 v[78:79], v108 offset:6144\n\t"
  "ds_read_b64_tr_b16 v[80:81], v109 offset:4096\n\t"
  "ds_read_b64_tr_b16 v[82:83], v109 offset:6144\n\t"
  "ds_read_u16_d16_hi v112, v6 offset:0\n\t"
  "ds_read_u16_d16_hi v113, v6 offset:8192\n\t"
  "ds_read_u16_d16_hi v114, v6 offset:16384\n\t"
  "ds_read_u16_d16_hi v115, v6 offset:24576\n\t"
  "ds_read_u16_d16_hi v116, v6 offset:32768\n\t"
  "ds_read_u16_d16_hi v117, v124 offset:0\n\t"
  "ds_read_u16_d16_hi v118, v124 offset:8192\n\t"
  "ds_read_u16_d16_hi v119, v124 offset:16384\n\t"
  "ds_read_u16_d16_hi v120, v124 offset:24576\n\t"
  "ds_read_u16_d16_hi v121, v124 offset:32768\n\t"
  "s_waitcnt lgkmcnt(15)\n\t"
  "v_mfma_f32_16x16x32_bf16 v[20:23], a[0:3], v[68:71], 0\n\t"
  "ds_read_b64_tr_b16 v[84:85], v108 offset:8192\n\t"
  "v_add_u32 v110, 40960, v5\n\t"
  "v_mfma_f32_16x16x32_bf16 v[24:27], a[4:7], v[68:71], 0\n\t"
  "ds_read_b64_tr_b16 v[86:87], v108 offset:10240\n\t"
  "v_xor_b32 v111, 32, v5\n\t"
  "v_mfma_f32_16x16x32_bf16 v[28:31], a[8:11], v[68:71], 0\n\t"
  "ds_read_b64_tr_b16 v[88:89], v109 offset:8192\n\t"
  "v_add_u32 v111, 40960, v111\n\t"
  "s_waitcnt lgkmcnt(15)\n\t"
  "v_mfma_f32_16x16x32_bf16 v[32:35], a[0:3], v[72:75], 0\n\t"
  "ds_read_b64_tr_b16 v[90:91], v109 offset:10240\n\t"
  "s_add_u32 m0, s52, 81920\n\t"
  "v_mfma_f32_16x16x32_bf16 v[36:39], a[4:7], v[72:75], 0\n\t"
  "s_mov_b32 s46, 1310720\n\t"
  "buffer_load_dwordx4 v4, %[srd], s46 offen offset:0 sc1 lds\n\t"
  "v_mfma_f32_16x16x32_bf16 v[40:43], a[8:11], v[72:75], 0\n\t"
  "s_waitcnt lgkmcnt(14)\n\t"
  "v_mfma_f32_16x16x32_bf16 v[20:23], a[12:15], v[76:79], v[20:23]\n\t"
  "ds_read_b64_tr_b16 v[68:69], v108 offset:12288\n\t"
  "v_mfma_f32_16x16x32_bf16 v[24:27], a[16:19], v[76:79], v[24:27]\n\t"
  "ds_read_b64_tr_b16 v[70:71], v108 offset:14336\n\t"
  "buffer_load_dwordx4 v4, %[srd], s46 offen offset:1024 sc1 lds\n\t"
  "v_mfma_f32_16x16x32_bf16 v[28:31], a[20:23], v[76:79], v[28:31]\n\t"
  "ds_read_b64_tr_b16 v[72:73], v109 offset:12288\n\t"
  "v_mfma_f32_16x16x32_bf16 v[32:35], a[12:15], v[80:83], v[32:35]\n\t"
  "ds_read_b64_tr_b16 v[74:75], v109 offset:14336\n\t"
  "s_waitcnt lgkmcnt(15)\n\t"
  "v_mul_f32 v112, v112, v142\n\t"
  "v_mul_f32 v113, v113, v143\n\t"
  "v_mfma_f32_16x16x32_bf16 v[36:39], a[16:19], v[80:83], v[36:39]\n\t"
  "v_mul_f32 v114, v114, v248\n\t"
  "s_waitcnt lgkmcnt(14)\n\t"
  "v_mul_f32 v115, v115, v249\n\t"
  "s_waitcnt lgkmcnt(13)\n\t"
  "v_mul_f32 v116, v116, v250\n\t"
  "v_mfma_f32_16x16x32_bf16 v[40:43], a[20:23], v[80:83], v[40:43]\n\t"
  "s_waitcnt lgkmcnt(4)\n\t"
  "v_mul_f32 v117, v117, v251\n\t"
  "v_mul_f32 v118, v118, v252\n\t"
  "v_mul_f32 v119, v119, v253\n\t"
  "v_mfma_f32_16x16x32_bf16 v[20:23], a[24:27], v[84:87], v[20:23]\n\t"
  "ds_read_b64_tr_b16 v[76:77], v108 offset:16384\n\t"
  "v_mul_f32 v120, v120, v254\n\t"
  "v_mul_f32 v121, v121, v255\n\t"
  "v_add_f32 v122, v112, v113\n\t"
  "v_mfma_f32_16x16x32_bf16 v[24:27], a[28:31], v[84:87], v[24:27]\n\t"
  "ds_read_b64_tr_b16 v[78:79], v108 offset:18432\n\t"
  "v_mul_f32 v123, v112, v112\n\t"
  "v_fmac_f32 v123, v113, v113\n\t"
  "v_add_f32 v122, v122, v114\n\t"
  "v_mfma_f32_16x16x32_bf16 v[28:31], a[32:35], v[84:87], v[28:31]\n\t"
  "ds_read_b64_tr_b16 v[80:81], v109 offset:16384\n\t"
  "v_fmac_f32 v123, v114, v114\n\t"
  "v_add_f32 v122, v122, v115\n\t"
  "v_fmac_f32 v123, v115, v115\n\t"
  "v_mfma_f32_16x16x32_bf16 v[32:35], a[24:27], v[88:91], v[32:35]\n\t"
  "ds_read_b64_tr_b16 v[82:83], v109 offset:18432\n\t"
  "v_add_f32 v122, v122, v116\n\t"
  "v_fmac_f32 v123, v116, v116\n\t"
  "v_add_f32 v122, v122, v117\n\t"
  "v_mfma_f32_16x16x32_bf16 v[36:39], a[28:31], v[88:91], v[36:39]\n\t"
  "v_fmac_f32 v123, v117, v117\n\t"
  "v_add_f32 v122, v122, v118\n\t"
  "v_fmac_f32 v123, v118, v118\n\t"
  "v_add_f32 v122, v122, v119\n\t"
  "v_mfma_f32_16x16x32_bf16 v[40:43], a[32:35], v[88:91], v[40:43]\n\t"
  "s_waitcnt lgkmcnt(4)\n\t"
  "v_fmac_f32 v123, v119, v119\n\t"
  "v_add_f32 v122, v122, v120\n\t"
  "v_fmac_f32 v123, v120, v120\n\t"
  "v_mfma_f32_16x16x32_bf16 v[20:23], a[36:39], v[68:71], v[20:23]\n\t"
  "ds_read_b64_tr_b16 v[84:85], v108 offset:20480\n\t"
  "v_add_f32 v122, v122, v121\n\t"
  "v_fmac_f32 v123, v121, v121\n\t"
  "buffer_load_dwordx4 v4, %[srd], s46 offen offset:2048 sc1 lds\n\t"
  "v_mfma_f32_16x16x32_bf16 v[24:27], a[40:43], v[68:71], v[24:27]\n\t"
  "ds_read_b64_tr_b16 v[86:87], v108 offset:22528\n\t"
  "v_add_f32_dpp v122, v122, v122 quad_perm:[1,0,3,2] row_mask:0xf bank_mask:0xf\n\t"
  "v_add_f32_dpp v123, v123, v123 quad_perm:[1,0,3,2] row_mask:0xf bank_mask:0xf\n\t"
  "buffer_load_dwordx4 v4, %[srd], s46 offen offset:3072 sc1 lds\n\t"
  "v_mfma_f32_16x16x32_bf16 v[28:31], a[44:47], v[68:71], v[28:31]\n\t"
  "ds_read_b64_tr_b16 v[88:89], v109 offset:20480\n\t"
  "v_add_f32_dpp v122, v122, v122 quad_perm:[2,3,0,1] row_mask:0xf bank_mask:0xf\n\t"
  "v_add_f32_dpp v123, v123, v123 quad_perm:[2,3,0,1] row_mask:0xf bank_mask:0xf\n\t"
  "s_add_u32 m0, s52, 98304\n\t"
  "v_mfma_f32_16x16x32_bf16 v[32:35], a[36:39], v[72:75], v[32:35]\n\t"
  "ds_read_b64_tr_b16 v[90:91], v109 offset:22528\n\t"
  "v_add_f32_dpp v122, v122, v122 row_half_mirror row_mask:0xf bank_mask:0xf\n\t"
  "v_add_f32_dpp v123, v123, v123 row_half_mirror row_mask:0xf bank_mask:0xf\n\t"
  "s_mov_b32 s46, 1327104\n\t"
  "v_mfma_f32_16x16x32_bf16 v[36:39], a[40:43], v[72:75], v[36:39]\n\t"
  "v_add_f32_dpp v122, v122, v122 row_mirror row_mask:0xf bank_mask:0xf\n\t"
  "v_add_f32_dpp v123, v123, v123 row_mirror row_mask:0xf bank_mask:0xf\n\t"
  "buffer_load_dwordx4 v4, %[srd], s46 offen offset:0 sc1 lds\n\t"
  "v_add_f32_dpp v122, v122, v122 row_bcast:15 row_mask:0xa bank_mask:0xf\n\t"
  "v_mfma_f32_16x16x32_bf16 v[40:43], a[44:47], v[72:75], v[40:43]\n\t"
  "s_waitcnt lgkmcnt(4)\n\t"
  "v_add_f32_dpp v123, v123, v123 row_bcast:15 row_mask:0xa bank_mask:0xf\n\t"
  "v_add_f32_dpp v122, v122, v122 row_bcast:31 row_mask:0xc bank_mask:0xf\n\t"
  "buffer_load_dwordx4 v4, %[srd], s46 offen offset:1024 sc1 lds\n\t"
  "v_mfma_f32_16x16x32_bf16 v[20:23], a[48:51], v[76:79], v[20:23]\n\t"
  "ds_read_b64_tr_b16 v[68:69], v108 offset:24576\n\t"
  "v_add_f32_dpp v123, v123, v123 row_bcast:31 row_mask:0xc bank_mask:0xf\n\t"
  "v_mul_f32 v122, 0xbacccccd, v122\n\t"
  "v_mul_f32 v123, 0x3acccccd, v123\n\t"
  "v_mfma_f32_16x16x32_bf16 v[24:27], a[52:55], v[76:79], v[24:27]\n\t"
  "ds_read_b64_tr_b16 v[70:71], v108 offset:26624\n\t"
  "v_fma_f32 v123, -v122, v122, v123\n\t"
  "v_max_f32 v123, 0, v123\n\t"
  "v_cvt_pk_bf16_f32 v123, 0, v123\n\t"
  "v_mfma_f32_16x16x32_bf16 v[28:31], a[56:59], v[76:79], v[28:31]\n\t"
  "ds_read_b64_tr_b16 v[72:73], v109 offset:24576\n\t"
  "v_cvt_pk_bf16_f32 v126, v122, v122\n\t"
  "v_add_f32 v123, 0x3727c5ac, v123\n\t"
  "v_cvt_pk_bf16_f32 v123, 0, v123\n\t"
  "v_mfma_f32_16x16x32_bf16 v[32:35], a[48:51], v[80:83], v[32:35]\n\t"
  "ds_read_b64_tr_b16 v[74:75], v109 offset:26624\n\t"
  "v_sqrt_f32 v123, v123\n\t"
  "buffer_load_dwordx4 v4, %[srd], s46 offen offset:2048 sc1 lds\n\t"
  "v_cvt_pk_bf16_f32 v127, v123, v123\n\t"
  "v_mfma_f32_16x16x32_bf16 v[36:39], a[52:55], v[80:83], v[36:39]\n\t"
  "buffer_store_dwordx2 v[126:127], v7, %[srd], %[sST] offen sc1\n\t"
  "buffer_load_dwordx4 v4, %[srd], s46 offen offset:3072 sc1 lds\n\t"
  "s_add_u32 m0, s52, 114688\n\t"
  "v_add_u32 v124, 122880, v6\n\t"
  "v_mfma_f32_16x16x32_bf16 v[40:43], a[56:59], v[80:83], v[40:43]\n\t"
  "s_waitcnt lgkmcnt(4)\n\t"
  "v_add_u32 v125, 81920, v6\n\t"
  "v_mfma_f32_16x16x32_bf16 v[20:23], a[60:63], v[84:87], v[20:23]\n\t"
  "ds_read_b64_tr_b16 v[76:77], v108 offset:28672\n\t"
  "v_mov_b32 v112, 0\n\t"
  "v_mfma_f32_16x16x32_bf16 v[24:27], a[64:67], v[84:87], v[24:27]\n\t"
  "ds_read_b64_tr_b16 v[78:79], v108 offset:30720\n\t"
  "s_mov_b32 s46, 1343488\n\t"
  "v_mfma_f32_16x16x32_bf16 v[28:31], a[68:71], v[84:87], v[28:31]\n\t"
  "ds_read_b64_tr_b16 v[80:81], v109 offset:28672\n\t"
  "v_mov_b32 v113, 0\n\t"
  "v_mfma_f32_16x16x32_bf16 v[32:35], a[60:63], v[88:91], v[32:35]\n\t"
  "ds_read_b64_tr_b16 v[82:83], v109 offset:30720\n\t"
  "buffer_load_dwordx4 v4, %[srd], s46 offen offset:0 sc1 lds\n\t"
  "v_mfma_f32_16x16x32_bf16 v[36:39], a[64:67], v[88:91], v[36:39]\n\t"
  "buffer_load_dwordx2 v[138:139], v2, %[srd], 0 offen sc1\n\t"
  "v_mov_b32 v114, 0\n\t"
  "v_mfma_f32_16x16x32_bf16 v[40:43], a[68:71], v[88:91], v[40:43]\n\t"
  "s_waitcnt lgkmcnt(4)\n\t"
  "v_mov_b32 v115, 0\n\t"
  "v_mfma_f32_16x16x32_bf16 v[20:23], a[72:75], v[68:71], v[20:23]\n\t"
  "ds_read_b64_tr_b16 v[84:85], v108 offset:32768\n\t"
  "buffer_load_dwordx4 v4, %[srd], s46 offen offset:1024 sc1 lds\n\t"
  "v_mfma_f32_16x16x32_bf16 v[24:27], a[76:79], v[68:71], v[24:27]\n\t"
  "ds_read_b64_tr_b16 v[86:87], v108 offset:34816\n\t"
  "v_mov_b32 v116, 0\n\t"
  "v_mfma_f32_16x16x32_bf16 v[28:31], a[80:83], v[68:71], v[28:31]\n\t"
  "ds_read_b64_tr_b16 v[88:89], v109 offset:32768\n\t"
  "buffer_load_dwordx4 v4, %[srd], s46 offen offset:2048 sc1 lds\n\t"
  "v_mfma_f32_16x16x32_bf16 v[32:35], a[72:75], v[72:75], v[32:35]\n\t"
  "ds_read_b64_tr_b16 v[90:91], v109 offset:34816\n\t"
  "v_mov_b32 v117, 0\n\t"
  "v_mfma_f32_16x16x32_bf16 v[36:39], a[76:79], v[72:75], v[36:39]\n\t"
  "buffer_load_dwordx4 v4, %[srd], s46 offen offset:3072 sc1 lds\n\t"
  "v_mov_b32 v118, 0\n\t"
  "v_mfma_f32_16x16x32_bf16 v[40:43], a[80:83], v[72:75], v[40:43]\n\t"
  "s_waitcnt lgkmcnt(4)\n\t"
  "v_mov_b32 v119, 0\n\t"
  "v_mfma_f32_16x16x32_bf16 v[20:23], a[84:87], v[76:79], v[20:23]\n\t"
  "ds_read_b64_tr_b16 v[68:69], v108 offset:36864\n\t"
  "v_mov_b32 v120, 0\n\t"
  "v_mfma_f32_16x16x32_bf16 v[24:27], a[88:91], v[76:79], v[24:27]\n\t"
  "ds_read_b64_tr_b16 v[70:71], v108 offset:38912\n\t"
  "s_add_u32 m0, s52, 131072\n\t"
  "v_mfma_f32_16x16x32_bf16 v[28:31], a[92:95], v[76:79], v[28:31]\n\t"
  "ds_read_b64_tr_b16 v[72:73], v109 offset:36864\n\t"
  "v_mov_b32 v121, 0\n\t"
  "v_mfma_f32_16x16x32_bf16 v[32:35], a[84:87], v[80:83], v[32:35]\n\t"
  "ds_read_b64_tr_b16 v[74:75], v109 offset:38912\n\t"
  "s_mov_b32 s46, 1359872\n\t"
  "v_mfma_f32_16x16x32_bf16 v[36:39], a[88:91], v[80:83], v[36:39]\n\t"
  "v_mfma_f32_16x16x32_bf16 v[40:43], a[92:95], v[80:83], v[40:43]\n\t"
  "s_waitcnt lgkmcnt(4)\n\t"
  "v_mfma_f32_16x16x32_bf16 v[20:23], a[96:99], v[84:87], v[20:23]\n\t"
  "ds_read_b64_tr_b16 v[76:77], v110 offset:0\n\t"
  "buffer_load_dwordx4 v4, %[srd], s46 offen offset:0 sc1 lds\n\t"
  "v_mfma_f32_16x16x32_bf16 v[24:27], a[100:103], v[84:87], v[24:27]\n\t"
  "ds_read_b64_tr_b16 v[78:79], v110 offset:2048\n\t"
  "v_mfma_f32_16x16x32_bf16 v[28:31], a[104:107], v[84:87], v[28:31]\n\t"
  "ds_read_b64_tr_b16 v[80:81], v111 offset:0\n\t"
  "buffer_load_dwordx4 v4, %[srd], s46 offen offset:1024 sc1 lds\n\t"
  "v_mfma_f32_16x16x32_bf16 v[32:35], a[96:99], v[88:91], v[32:35]\n\t"
  "ds_read_b64_tr_b16 v[82:83], v111 offset:2048\n\t"
  "v_mfma_f32_16x16x32_bf16 v[36:39], a[100:103], v[88:91], v[36:39]\n\t"
  "buffer_load_dwordx4 v4, %[srd], s46 offen offset:2048 sc1 lds\n\t"
  "v_mfma_f32_16x16x32_bf16 v[40:43], a[104:107], v[88:91], v[40:43]\n\t"
  "s_waitcnt lgkmcnt(4)\n\t"
  "v_mfma_f32_16x16x32_bf16 v[20:23], a[108:111], v[68:71], v[20:23]\n\t"
  "ds_read_b64_tr_b16 v[84:85], v110 offset:4096\n\t"
  "v_mfma_f32_16x16x32_bf16 v[24:27], a[112:115], v[68:71], v[24:27]\n\t"
  "ds_read_b64_tr_b16 v[86:87], v110 offset:6144\n\t"
  "buffer_load_dwordx4 v4, %[srd], s46 offen offset:3072 sc1 lds\n\t"
  "v_mfma_f32_16x16x32_bf16 v[28:31], a[116:119], v[68:71], v[28:31]\n\t"
  "ds_read_b64_tr_b16 v[88:89], v111 offset:4096\n\t"
  "v_mfma_f32_16x16x32_bf16 v[32:35], a[108:111], v[72:75], v[32:35]\n\t"
  "ds_read_b64_tr_b16 v[90:91], v111 offset:6144\n\t"
  "s_add_u32 m0, s52, 147456\n\t"
  "v_mfma_f32_16x16x32_bf16 v[36:39], a[112:115], v[72:75], v[36:39]\n\t"
  "v_mfma_f32_16x16x32_bf16 v[40:43], a[116:119], v[72:75], v[40:43]\n\t"
  "s_waitcnt lgkmcnt(4)\n\t"
  "v_mfma_f32_16x16x32_bf16 v[20:23], a[120:123], v[76:79], v[20:23]\n\t"
  "ds_read_b64_tr_b16 v[68:69], v110 offset:8192\n\t"
  "v_xor_b32 v108, 64, v5\n\t"
  "v_mfma_f32_16x16x32_bf16 v[24:27], a[124:127], v[76:79], v[24:27]\n\t"
  "ds_read_b64_tr_b16 v[70:71], v110 offset:10240\n\t"
  "v_add_u32 v108, 0, v108\n\t"
  "v_mfma_f32_16x16x32_bf16 v[28:31], a[128:131], v[76:79], v[28:31]\n\t"
  "ds_read_b64_tr_b16 v[72:73], v111 offset:8192\n\t"
  "v_xor_b32 v109, 96, v5\n\t"
  "v_mfma_f32_16x16x32_bf16 v[32:35], a[120:123], v[80:83], v[32:35]\n\t"
  "ds_read_b64_tr_b16 v[74:75], v111 offset:10240\n\t"
  "v_add_u32 v109, 0, v109\n\t"
  "v_mfma_f32_16x16x32_bf16 v[36:39], a[124:127], v[80:83], v[36:39]\n\t"
  "s_mov_b32 s46, 1376256\n\t"
  "buffer_load_dwordx4 v4, %[srd], s46 offen offset:0 sc1 lds\n\t"
  "v_mfma_f32_16x16x32_bf16 v[40:43], a[128:131], v[80:83], v[40:43]\n\t"
  "s_waitcnt lgkmcnt(4)\n\t"
  "buffer_load_dwordx4 v4, %[srd], s46 offen offset:1024 sc1 lds\n\t"
  "v_mfma_f32_16x16x32_bf16 v[20:23], a[132:135], v[84:87], v[20:23]\n\t"
  "ds_read_b64_tr_b16 v[76:77], v110 offset:12288\n\t"
  "s_waitcnt vmcnt(45)\n\t"
  "v_mfma_f32_16x16x32_bf16 v[24:27], a[136:139], v[84:87], v[24:27]\n\t"
  "ds_read_b64_tr_b16 v[78:79], v110 offset:14336\n\t"
  "buffer_load_dwordx4 v4, %[srd], s46 offen offset:2048 sc1 lds\n\t"
  "s_waitcnt vmcnt(45)\n\t"
  "v_mfma_f32_16x16x32_bf16 v[28:31], a[140:143], v[84:87], v[28:31]\n\t"
  "ds_read_b64_tr_b16 v[80:81], v111 offset:12288\n\t"
  "v_mfma_f32_16x16x32_bf16 v[32:35], a[132:135], v[88:91], v[32:35]\n\t"
  "ds_read_b64_tr_b16 v[82:83], v111 offset:14336\n\t"
  "buffer_load_dwordx4 v4, %[srd], s46 offen offset:3072 sc1 lds\n\t"
  "v_mfma_f32_16x16x32_bf16 v[36:39], a[136:139], v[88:91], v[36:39]\n\t"
  "v_mfma_f32_16x16x32_bf16 v[40:43], a[140:143], v[88:91], v[40:43]\n\t"
  "s_waitcnt lgkmcnt(4)\n\t"
  "s_waitcnt vmcnt(45)\n\t"
  "v_mfma_f32_16x16x32_bf16 v[20:23], a[144:147], v[68:71], v[20:23]\n\t"
  "ds_read_b64_tr_b16 v[84:85], v110 offset:16384\n\t"
  "s_waitcnt vmcnt(44)\n\t"
  "v_mfma_f32_16x16x32_bf16 v[24:27], a[148:151], v[68:71], v[24:27]\n\t"
  "ds_read_b64_tr_b16 v[86:87], v110 offset:18432\n\t"
  "s_waitcnt vmcnt(43)\n\t"
  "v_mfma_f32_16x16x32_bf16 v[28:31], a[152:155], v[68:71], v[28:31]\n\t"
  "ds_read_b64_tr_b16 v[88:89], v111 offset:16384\n\t"
  "v_mfma_f32_16x16x32_bf16 v[32:35], a[144:147], v[72:75], v[32:35]\n\t"
  "ds_read_b64_tr_b16 v[90:91], v111 offset:18432\n\t"
  "v_mfma_f32_16x16x32_bf16 v[36:39], a[148:151], v[72:75], v[36:39]\n\t"
  "v_mfma_f32_16x16x32_bf16 v[40:43], a[152:155], v[72:75], v[40:43]\n\t"
  "s_waitcnt lgkmcnt(4)\n\t"
  "s_waitcnt vmcnt(42)\n\t"
  "v_mfma_f32_16x16x32_bf16 v[20:23], a[156:159], v[76:79], v[20:23]\n\t"
  "ds_read_b64_tr_b16 v[68:69], v110 offset:20480\n\t"
  "s_waitcnt vmcnt(41)\n\t"
  "v_mfma_f32_16x16x32_bf16 v[24:27], a[160:163], v[76:79], v[24:27]\n\t"
  "ds_read_b64_tr_b16 v[70:71], v110 offset:22528\n\t"
  "s_waitcnt vmcnt(40)\n\t"
  "v_mfma_f32_16x16x32_bf16 v[28:31], a[164:167], v[76:79], v[28:31]\n\t"
  "ds_read_b64_tr_b16 v[72:73], v111 offset:20480\n\t"
  "v_mfma_f32_16x16x32_bf16 v[32:35], a[156:159], v[80:83], v[32:35]\n\t"
  "ds_read_b64_tr_b16 v[74:75], v111 offset:22528\n\t"
  "v_mfma_f32_16x16x32_bf16 v[36:39], a[160:163], v[80:83], v[36:39]\n\t"
  "v_mfma_f32_16x16x32_bf16 v[40:43], a[164:167], v[80:83], v[40:43]\n\t"
  "s_waitcnt lgkmcnt(4)\n\t"
  "s_waitcnt vmcnt(39)\n\t"
  "v_mfma_f32_16x16x32_bf16 v[20:23], a[168:171], v[84:87], v[20:23]\n\t"
  "ds_read_b64_tr_b16 v[76:77], v110 offset:24576\n\t"
  "s_waitcnt vmcnt(11)\n\t"
  "v_cmp_ne_u64 vcc, v[132:133], v[138:139]\n\t"
  "v_mfma_f32_16x16x32_bf16 v[24:27], a[172:175], v[84:87], v[24:27]\n\t"
  "ds_read_b64_tr_b16 v[78:79], v110 offset:26624\n\t"
  "v_mfma_f32_16x16x32_bf16 v[28:31], a[176:179], v[84:87], v[28:31]\n\t"
  "ds_read_b64_tr_b16 v[80:81], v111 offset:24576\n\t"
  "s_cbranch_vccnz Lw2_slow%=\n\t"
  "Lw2_back%=:\n\t"
  "v_mfma_f32_16x16x32_bf16 v[32:35], a[168:171], v[88:91], v[32:35]\n\t"
  "ds_read_b64_tr_b16 v[82:83], v111 offset:26624\n\t"
  "v_mfma_f32_16x16x32_bf16 v[36:39], a[172:175], v[88:91], v[36:39]\n\t"
  "v_mfma_f32_16x16x32_bf16 v[40:43], a[176:179], v[88:91], v[40:43]\n\t"
  "s_waitcnt lgkmcnt(4)\n\t"
  "v_mfma_f32_16x16x32_bf16 v[20:23], a[180:183], v[68:71], v[20:23]\n\t"
  "ds_read_b64_tr_b16 v[84:85], v110 offset:28672\n\t"
  "v_lshrrev_b32 v138, 1, v1\n\t"
  "v_mfma_f32_16x16x32_bf16 v[24:27], a[184:187], v[68:71], v[24:27]\n\t"
  "ds_read_b64_tr_b16 v[86:87], v110 offset:30720\n\t"
  "buffer_load_dwordx2 a[240:241], v138, %[srd], %[sUB] offen offset:0 sc1\n\t"
  "v_mfma_f32_16x16x32_bf16 v[28:31], a[188:191], v[68:71], v[28:31]\n\t"
  "ds_read_b64_tr_b16 v[88:89], v111 offset:28672\n\t"
  "buffer_load_dwordx2 a[242:243], v138, %[srd], %[sUB] offen offset:512 sc1\n\t"
  "v_mfma_f32_16x16x32_bf16 v[32:35], a[180:183], v[72:75], v[32:35]\n\t"
  "ds_read_b64_tr_b16 v[90:91], v111 offset:30720\n\t"
  "buffer_load_dwordx2 a[244:245], v138, %[srd], %[sUB] offen offset:1024 sc1\n\t"
  "v_mfma_f32_16x16x32_bf16 v[36:39], a[184:187], v[72:75], v[36:39]\n\t"
  "s_add_u32 s45, %[sW2], 0\n\t"
  "buffer_load_dwordx4 v[144:147], v1, %[srd], s45 offen offset:0 sc1\n\t"
  "v_mfma_f32_16x16x32_bf16 v[40:43], a[188:191], v[72:75], v[40:43]\n\t"
  "s_waitcnt lgkmcnt(4)\n\t"
  "v_mfma_f32_16x16x32_bf16 v[20:23], a[192:195], v[76:79], v[20:23]\n\t"
  "ds_read_b64_tr_b16 v[68:69], v110 offset:32768\n\t"
  "v_mfma_f32_16x16x32_bf16 v[24:27], a[196:199], v[76:79], v[24:27]\n\t"
  "ds_read_b64_tr_b16 v[70:71], v110 offset:34816\n\t"
  "v_mfma_f32_16x16x32_bf16 v[28:31], a[200:203], v[76:79], v[28:31]\n\t"
  "ds_read_b64_tr_b16 v[72:73], v111 offset:32768\n\t"
  "v_mfma_f32_16x16x32_bf16 v[32:35], a[192:195], v[80:83], v[32:35]\n\t"
  "ds_read_b64_tr_b16 v[74:75], v111 offset:34816\n\t"
  "buffer_load_dwordx4 v[148:151], v1, %[srd], s45 offen offset:1024 sc1\n\t"
  "v_mfma_f32_16x16x32_bf16 v[36:39], a[196:199], v[80:83], v[36:39]\n\t"
  "v_mfma_f32_16x16x32_bf16 v[40:43], a[200:203], v[80:83], v[40:43]\n\t"
  "s_waitcnt lgkmcnt(4)\n\t"
  "v_mfma_f32_16x16x32_bf16 v[20:23], a[204:207], v[84:87], v[20:23]\n\t"
  "ds_read_b64_tr_b16 v[76:77], v110 offset:36864\n\t"
  "v_mfma_f32_16x16x32_bf16 v[24:27], a[208:211], v[84:87], v[24:27]\n\t"
  "ds_read_b64_tr_b16 v[78:79], v110 offset:38912\n\t"
  "v_mfma_f32_16x16x32_bf16 v[28:31], a[212:215], v[84:87], v[28:31]\n\t"
  "ds_read_b64_tr_b16 v[80:81], v111 offset:36864\n\t"
  "buffer_load_dwordx4 v[152:155], v1, %[srd], s45 offen offset:2048 sc1\n\t"
  "v_mfma_f32_16x16x32_bf16 v[32:35], a[204:207], v[88:91], v[32:35]\n\t"
  "ds_read_b64_tr_b16 v[82:83], v111 offset:38912\n\t"
  "v_mfma_f32_16x16x32_bf16 v[36:39], a[208:211], v[88:91], v[36:39]\n\t"
  "v_mfma_f32_16x16x32_bf16 v[40:43], a[212:215], v[88:91], v[40:43]\n\t"
  "s_waitcnt lgkmcnt(4)\n\t"
  "v_mfma_f32_16x16x32_bf16 v[20:23], a[216:219], v[68:71], v[20:23]\n\t"
  "ds_read_b64_tr_b16 v[84:85], v108 offset:0\n\t"
  "v_mfma_f32_16x16x32_bf16 v[24:27], a[220:223], v[68:71], v[24:27]\n\t"
  "ds_read_b64_tr_b16 v[86:87], v108 offset:2048\n\t"
  "buffer_load_dwordx4 v[156:159], v1, %[srd], s45 offen offset:3072 sc1\n\t"
  "v_mfma_f32_16x16x32_bf16 v[28:31], a[224:227], v[68:71], v[28:31]\n\t"
  "ds_read_b64_tr_b16 v[88:89], v109 offset:0\n\t"
  "v_mfma_f32_16x16x32_bf16 v[32:35], a[216:219], v[72:75], v[32:35]\n\t"
  "ds_read_b64_tr_b16 v[90:91], v109 offset:2048\n\t"
  "v_mfma_f32_16x16x32_bf16 v[36:39], a[220:223], v[72:75], v[36:39]\n\t"
  "v_mfma_f32_16x16x32_bf16 v[40:43], a[224:227], v[72:75], v[40:43]\n\t"
  "s_waitcnt lgkmcnt(4)\n\t"
  "v_mfma_f32_16x16x32_bf16 v[20:23], a[228:231], v[76:79], v[20:23]\n\t"
  "ds_read_b64_tr_b16 v[68:69], v108 offset:4096\n\t"
  "s_add_u32 s45, %[sW2], 4096\n\t"
  "v_mfma_f32_16x16x32_bf16 v[24:27], a[232:235], v[76:79], v[24:27]\n\t"
  "ds_read_b64_tr_b16 v[70:71], v108 offset:6144\n\t"
  "v_mfma_f32_16x16x32_bf16 v[28:31], a[236:239], v[76:79], v[28:31]\n\t"
  "ds_read_b64_tr_b16 v[72:73], v109 offset:4096\n\t"
  "v_mfma_f32_16x16x32_bf16 v[32:35], a[228:231], v[80:83], v[32:35]\n\t"
  "ds_read_b64_tr_b16 v[74:75], v109 offset:6144\n\t"
  "v_mfma_f32_16x16x32_bf16 v[36:39], a[232:235], v[80:83], v[36:39]\n\t"
  "v_mfma_f32_16x16x32_bf16 v[40:43], a[236:239], v[80:83], v[40:43]\n\t"
  "s_waitcnt lgkmcnt(4)\n\t"
  "buffer_load_dwordx4 v[160:163], v1, %[srd], s45 offen offset:0 sc1\n\t"
  "v_mfma_f32_16x16x32_bf16 v[44:47], a[0:3], v[84:87], 0\n\t"
  "ds_read_b64_tr_b16 v[76:77], v108 offset:8192\n\t"
  "v_xor_b32 v110, 64, v5\n\t"
  "v_mfma_f32_16x16x32_bf16 v[48:51], a[4:7], v[84:87], 0\n\t"
  "ds_read_b64_tr_b16 v[78:79], v108 offset:10240\n\t"
  "v_add_u32 v110, 40960, v110\n\t"
  "v_mfma_f32_16x16x32_bf16 v[52:55], a[8:11], v[84:87], 0\n\t"
  "ds_read_b64_tr_b16 v[80:81], v109 offset:8192\n\t"
  "v_xor_b32 v111, 96, v5\n\t"
  "v_mfma_f32_16x16x32_bf16 v[56:59], a[0:3], v[88:91], 0\n\t"
  "ds_read_b64_tr_b16 v[82:83], v109 offset:10240\n\t"
  "v_add_u32 v111, 40960, v111\n\t"
  "v_mfma_f32_16x16x32_bf16 v[60:63], a[4:7], v[88:91], 0\n\t"
  "buffer_load_dwordx4 v[164:167], v1, %[srd], s45 offen offset:1024 sc1\n\t"
  "v_mfma_f32_16x16x32_bf16 v[64:67], a[8:11], v[88:91], 0\n\t"
  "s_waitcnt lgkmcnt(4)\n\t"
  "v_mfma_f32_16x16x32_bf16 v[44:47], a[12:15], v[68:71], v[44:47]\n\t"
  "ds_read_b64_tr_b16 v[84:85], v108 offset:12288\n\t"
  "buffer_load_dwordx2 v[92:93], v136, %[srd], %[sST] offen offset:0 sc1\n\t"
  "v_mfma_f32_16x16x32_bf16 v[48:51], a[16:19], v[68:71], v[48:51]\n\t"
  "ds_read_b64_tr_b16 v[86:87], v108 offset:14336\n\t"
  "buffer_load_dwordx2 v[94:95], v136, %[srd], %[sST] offen offset:1024 sc1\n\t"
  "v_mfma_f32_16x16x32_bf16 v[52:55], a[20:23], v[68:71], v[52:55]\n\t"
  "ds_read_b64_tr_b16 v[88:89], v109 offset:12288\n\t"
  "buffer_load_dwordx2 v[96:97], v136, %[srd], %[sST] offen offset:2048 sc1\n\t"
  "v_mfma_f32_16x16x32_bf16 v[56:59], a[12:15], v[72:75], v[56:59]\n\t"
  "ds_read_b64_tr_b16 v[90:91], v109 offset:14336\n\t"
  "buffer_load_dwordx2 v[98:99], v136, %[srd], %[sST] offen offset:3072 sc1\n\t"
  "v_mfma_f32_16x16x32_bf16 v[60:63], a[16:19], v[72:75], v[60:63]\n\t"
  "buffer_load_dwordx4 v[168:171], v1, %[srd], s45 offen offset:2048 sc1\n\t"
  "v_mfma_f32_16x16x32_bf16 v[64:67], a[20:23], v[72:75], v[64:67]\n\t"
  "s_waitcnt lgkmcnt(4)\n\t"
  "v_mfma_f32_16x16x32_bf16 v[44:47], a[24:27], v[76:79], v[44:47]\n\t"
  "ds_read_b64_tr_b16 v[68:69], v108 offset:16384\n\t"
  "v_mfma_f32_16x16x32_bf16 v[48:51], a[28:31], v[76:79], v[48:51]\n\t"
  "ds_read_b64_tr_b16 v[70:71], v108 offset:18432\n\t"
  "v_mfma_f32_16x16x32_bf16 v[52:55], a[32:35], v[76:79], v[52:55]\n\t"
  "ds_read_b64_tr_b16 v[72:73], v109 offset:16384\n\t"
  "s_waitcnt vmcnt(14)\n\t"
  "s_barrier\n\t"
  "v_mfma_f32_16x16x32_bf16 v[56:59], a[24:27], v[80:83], v[56:59]\n\t"
  "ds_read_b64_tr_b16 v[74:75], v109 offset:18432\n\t"
  "buffer_load_dwordx4 v[172:175], v1, %[srd], s45 offen offset:3072 sc1\n\t"
  "v_mfma_f32_16x16x32_bf16 v[60:63], a[28:31], v[80:83], v[60:63]\n\t"
  "ds_read_u16_d16_hi v112, v125 offset:0\n\t"
  "ds_read_b64_tr_b16 a[246:247], v10 offset:0\n\t"
  "v_mfma_f32_16x16x32_bf16 v[64:67], a[32:35], v[80:83], v[64:67]\n\t"
  "s_waitcnt lgkmcnt(6)\n\t"
  "ds_read_u16_d16_hi v113, v125 offset:8192\n\t"
  "v_mfma_f32_16x16x32_bf16 v[44:47], a[36:39], v[84:87], v[44:47]\n\t"
  "ds_read_b64_tr_b16 v[76:77], v108 offset:20480\n\t"
  "ds_read_u16_d16_hi v114, v125 offset:16384\n\t"
  "v_mfma_f32_16x16x32_bf16 v[48:51], a[40:43], v[84:87], v[48:51]\n\t"
  "ds_read_b64_tr_b16 v[78:79], v108 offset:22528\n\t"
  "s_add_u32 s45, %[sW2], 8192\n\t"
  "v_mfma_f32_16x16x32_bf16 v[52:55], a[44:47], v[84:87], v[52:55]\n\t"
  "ds_read_b64_tr_b16 v[80:81], v109 offset:20480\n\t"
  "ds_read_u16_d16_hi v115, v125 offset:24576\n\t"
  "v_mfma_f32_16x16x32_bf16 v[56:59], a[36:39], v[88:91], v[56:59]\n\t"
  "ds_read_b64_tr_b16 v[82:83], v109 offset:22528\n\t"
  "ds_read_u16_d16_hi v116, v125 offset:32768\n\t"
  "v_mfma_f32_16x16x32_bf16 v[60:63], a[40:43], v[88:91], v[60:63]\n\t"
  "ds_read_u16_d16_hi v117, v124 offset:0\n\t"
  "ds_read_b64_tr_b16 a[248:249], v11 offset:0\n\t"
  "v_mfma_f32_16x16x32_bf16 v[64:67], a[44:47], v[88:91], v[64:67]\n\t"
  "s_waitcnt lgkmcnt(12)\n\t"
  "ds_read_u16_d16_hi v118, v124 offset:8192\n\t"
  "v_mfma_f32_16x16x32_bf16 v[44:47], a[48:51], v[68:71], v[44:47]\n\t"
  "ds_read_b64_tr_b16 v[84:85], v108 offset:24576\n\t"
  "buffer_load_dwordx4 v[176:179], v1, %[srd], s45 offen offset:0 sc1\n\t"
  "v_mfma_f32_16x16x32_bf16 v[48:51], a[52:55], v[68:71], v[48:51]\n\t"
  "ds_read_b64_tr_b16 v[86:87], v108 offset:26624\n\t"
  "ds_read_u16_d16_hi v119, v124 offset:16384\n\t"
  "v_mfma_f32_16x16x32_bf16 v[52:55], a[56:59], v[68:71], v[52:55]\n\t"
  "ds_read_b64_tr_b16 v[88:89], v109 offset:24576\n\t"
  "ds_read_u16_d16_hi v120, v124 offset:24576\n\t"
  "v_mfma_f32_16x16x32_bf16 v[56:59], a[48:51], v[72:75], v[56:59]\n\t"
  "ds_read_b64_tr_b16 v[90:91], v109 offset:26624\n\t"
  "ds_read_u16_d16_hi v121, v124 offset:32768\n\t"
  "v_mfma_f32_16x16x32_bf16 v[60:63], a[52:55], v[72:75], v[60:63]\n\t"
  "buffer_load_dwordx4 v[180:183], v1, %[srd], s45 offen offset:1024 sc1\n\t"
  "ds_read_b64_tr_b16 a[250:251], v10 offset:4096\n\t"
  "v_mfma_f32_16x16x32_bf16 v[64:67], a[56:59], v[72:75], v[64:67]\n\t"
  "s_waitcnt lgkmcnt(12)\n\t"
  "ds_read_b64_tr_b16 a[252:253], v11 offset:4096\n\t"
  "v_mfma_f32_16x16x32_bf16 v[44:47], a[60:63], v[76:79], v[44:47]\n\t"
  "ds_read_b64_tr_b16 v[68:69], v108 offset:28672\n\t"
  "ds_read_b64_tr_b16 a[254:255], v12 offset:0\n\t"
  "v_mfma_f32_16x16x32_bf16 v[48:51], a[64:67], v[76:79], v[48:51]\n\t"
  "ds_read_b64_tr_b16 v[70:71], v108 offset:30720\n\t"
  "v_mfma_f32_16x16x32_bf16 v[52:55], a[68:71], v[76:79], v[52:55]\n\t"
  "ds_read_b64_tr_b16 v[72:73], v109 offset:28672\n\t"
  "v_mfma_f32_16x16x32_bf16 v[56:59], a[60:63], v[80:83], v[56:59]\n\t"
  "ds_read_b64_tr_b16 v[74:75], v109 offset:30720\n\t"
  "buffer_load_dwordx4 v[184:187], v1, %[srd], s45 offen offset:2048 sc1\n\t"
  "v_mfma_f32_16x16x32_bf16 v[60:63], a[64:67], v[80:83], v[60:63]\n\t"
  "v_mfma_f32_16x16x32_bf16 v[64:67], a[68:71], v[80:83], v[64:67]\n\t"
  "s_waitcnt lgkmcnt(8)\n\t"
  "v_mfma_f32_16x16x32_bf16 v[44:47], a[72:75], v[84:87], v[44:47]\n\t"
  "ds_read_b64_tr_b16 v[76:77], v108 offset:32768\n\t"
  "v_mfma_f32_16x16x32_bf16 v[48:51], a[76:79], v[84:87], v[48:51]\n\t"
  "ds_read_b64_tr_b16 v[78:79], v108 offset:34816\n\t"
  "v_mul_f32 v112, v112, v142\n\t"
  "v_mfma_f32_16x16x32_bf16 v[52:55], a[80:83], v[84:87], v[52:55]\n\t"
  "ds_read_b64_tr_b16 v[80:81], v109 offset:32768\n\t"
  "buffer_load_dwordx4 v[188:191], v1, %[srd], s45 offen offset:3072 sc1\n\t"
  "v_mfma_f32_16x16x32_bf16 v[56:59], a[72:75], v[88:91], v[56:59]\n\t"
  "ds_read_b64_tr_b16 v[82:83], v109 offset:34816\n\t"
  "v_mul_f32 v113, v113, v143\n\t"
  "v_mfma_f32_16x16x32_bf16 v[60:63], a[76:79], v[88:91], v[60:63]\n\t"
  "v_mul_f32 v114, v114, v248\n\t"
  "v_mul_f32 v115, v115, v249\n\t"
  "v_mfma_f32_16x16x32_bf16 v[64:67], a[80:83], v[88:91], v[64:67]\n\t"
  "s_waitcnt lgkmcnt(4)\n\t"
  "v_mul_f32 v116, v116, v250\n\t"
  "v_mfma_f32_16x16x32_bf16 v[44:47], a[84:87], v[68:71], v[44:47]\n\t"
  "ds_read_b64_tr_b16 v[84:85], v108 offset:36864\n\t"
  "v_mul_f32 v117, v117, v251\n\t"
  "v_mfma_f32_16x16x32_bf16 v[48:51], a[88:91], v[68:71], v[48:51]\n\t"
  "ds_read_b64_tr_b16 v[86:87], v108 offset:38912\n\t"
  "s_add_u32 s45, %[sW2], 12288\n\t"
  "v_mfma_f32_16x16x32_bf16 v[52:55], a[92:95], v[68:71], v[52:55]\n\t"
  "ds_read_b64_tr_b16 v[88:89], v109 offset:36864\n\t"
  "v_mul_f32 v118, v118, v252\n\t"
  "v_mfma_f32_16x16x32_bf16 v[56:59], a[84:87], v[72:75], v[56:59]\n\t"
  "ds_read_b64_tr_b16 v[90:91], v109 offset:38912\n\t"
  "v_mul_f32 v119, v119, v253\n\t"
  "v_mfma_f32_16x16x32_bf16 v[60:63], a[88:91], v[72:75], v[60:63]\n\t"
  "v_mul_f32 v120, v120, v254\n\t"
  "v_mul_f32 v121, v121, v255\n\t"
  "v_mfma_f32_16x16x32_bf16 v[64:67], a[92:95], v[72:75], v[64:67]\n\t"
  "s_waitcnt lgkmcnt(4)\n\t"
  "v_add_f32 v122, v112, v113\n\t"
  "v_mfma_f32_16x16x32_bf16 v[44:47], a[96:99], v[76:79], v[44:47]\n\t"
  "ds_read_b64_tr_b16 v[68:69], v110 offset:0\n\t"
  "buffer_load_dwordx4 v[192:195], v1, %[srd], s45 offen offset:0 sc1\n\t"
  "v_mfma_f32_16x16x32_bf16 v[48:51], a[100:103], v[76:79], v[48:51]\n\t"
  "ds_read_b64_tr_b16 v[70:71], v110 offset:2048\n\t"
  "v_mul_f32 v123, v112, v112\n\t"
  "v_mfma_f32_16x16x32_bf16 v[52:55], a[104:107], v[76:79], v[52:55]\n\t"
  "ds_read_b64_tr_b16 v[72:73], v111 offset:0\n\t"
  "v_fmac_f32 v123, v113, v113\n\t"
  "v_mfma_f32_16x16x32_bf16 v[56:59], a[96:99], v[80:83], v[56:59]\n\t"
  "ds_read_b64_tr_b16 v[74:75], v111 offset:2048\n\t"
  "v_add_f32 v122, v122, v114\n\t"
  "v_mfma_f32_16x16x32_bf16 v[60:63], a[100:103], v[80:83], v[60:63]\n\t"
  "v_fmac_f32 v123, v114, v114\n\t"
  "v_add_f32 v122, v122, v115\n\t"
  "v_mfma_f32_16x16x32_bf16 v[64:67], a[104:107], v[80:83], v[64:67]\n\t"
  "s_waitcnt lgkmcnt(4)\n\t"
  "buffer_load_dwordx4 v[196:199], v1, %[srd], s45 offen offset:1024 sc1\n\t"
  "v_mfma_f32_16x16x32_bf16 v[44:47], a[108:111], v[84:87], v[44:47]\n\t"
  "ds_read_b64_tr_b16 v[76:77], v110 offset:4096\n\t"
  "v_fmac_f32 v123, v115, v115\n\t"
  "v_mfma_f32_16x16x32_bf16 v[48:51], a[112:115], v[84:87], v[48:51]\n\t"
  "ds_read_b64_tr_b16 v[78:79], v110 offset:6144\n\t"
  "v_add_f32 v122, v122, v116\n\t"
  "v_mfma_f32_16x16x32_bf16 v[52:55], a[116:119], v[84:87], v[52:55]\n\t"
  "ds_read_b64_tr_b16 v[80:81], v111 offset:4096\n\t"
  "v_fmac_f32 v123, v116, v116\n\t"
  "v_mfma_f32_16x16x32_bf16 v[56:59], a[108:111], v[88:91], v[56:59]\n\t"
  "ds_read_b64_tr_b16 v[82:83], v111 offset:6144\n\t"
  "v_add_f32 v122, v122, v117\n\t"
  "v_mfma_f32_16x16x32_bf16 v[60:63], a[112:115], v[88:91], v[60:63]\n\t"
  "buffer_load_dwordx4 v[200:203], v1, %[srd], s45 offen offset:2048 sc1\n\t"
  "v_fmac_f32 v123, v117, v117\n\t"
  "v_mfma_f32_16x16x32_bf16 v[64:67], a[116:119], v[88:91], v[64:67]\n\t"
  "s_waitcnt lgkmcnt(4)\n\t"
  "s_waitcnt vmcnt(11)\n\t"
  "v_min_u32 v131, v93, v95\n\t"
  "v_mfma_f32_16x16x32_bf16 v[44:47], a[120:123], v[68:71], v[44:47]\n\t"
  "ds_read_b64_tr_b16 v[84:85], v110 offset:8192\n\t"
  "s_waitcnt vmcnt(9)\n\t"
  "v_min3_u32 v131, v131, v97, v99\n\t"
  "v_mfma_f32_16x16x32_bf16 v[48:51], a[124:127], v[68:71], v[48:51]\n\t"
  "ds_read_b64_tr_b16 v[86:87], v110 offset:10240\n\t"
  "v_cmp_eq_u32 vcc, 0, v131\n\t"
  "v_mfma_f32_16x16x32_bf16 v[52:55], a[128:131], v[68:71], v[52:55]\n\t"
  "ds_read_b64_tr_b16 v[88:89], v111 offset:8192\n\t"
  "v_add_u32 v108, 81920, v5\n\t"
  "v_mfma_f32_16x16x32_bf16 v[56:59], a[120:123], v[72:75], v[56:59]\n\t"
  "ds_read_b64_tr_b16 v[90:91], v111 offset:10240\n\t"
  "s_cbranch_vccnz Lst0_slow%=\n\t"
  "Lst0_back%=:\n\t"
  "v_mfma_f32_16x16x32_bf16 v[60:63], a[124:127], v[72:75], v[60:63]\n\t"
  "v_xor_b32 v109, 32, v5\n\t"
  "v_add_u32 v109, 81920, v109\n\t"
  "v_mfma_f32_16x16x32_bf16 v[64:67], a[128:131], v[72:75], v[64:67]\n\t"
  "s_waitcnt lgkmcnt(4)\n\t"
  "buffer_load_dwordx4 v[204:207], v1, %[srd], s45 offen offset:3072 sc1\n\t"
  "v_mfma_f32_16x16x32_bf16 v[44:47], a[132:135], v[76:79], v[44:47]\n\t"
  "ds_read_b64_tr_b16 v[68:69], v110 offset:12288\n\t"
  "v_add_f32 v122, v122, v118\n\t"
  "v_mfma_f32_16x16x32_bf16 v[48:51], a[136:139], v[76:79], v[48:51]\n\t"
  "ds_read_b64_tr_b16 v[70:71], v110 offset:14336\n\t"
  "v_fmac_f32 v123, v118, v118\n\t"
  "v_mfma_f32_16x16x32_bf16 v[52:55], a[140:143], v[76:79], v[52:55]\n\t"
  "ds_read_b64_tr_b16 v[72:73], v111 offset:12288\n\t"
  "s_add_u32 s45, %[sW2], 16384\n\t"
  "v_mfma_f32_16x16x32_bf16 v[56:59], a[132:135], v[80:83], v[56:59]\n\t"
  "ds_read_b64_tr_b16 v[74:75], v111 offset:14336\n\t"
  "v_add_f32 v122, v122, v119\n\t"
  "v_mfma_f32_16x16x32_bf16 v[60:63], a[136:139], v[80:83], v[60:63]\n\t"
  "v_fmac_f32 v123, v119, v119\n\t"
  "v_add_f32 v122, v122, v120\n\t"
  "v_mfma_f32_16x16x32_bf16 v[64:67], a[140:143], v[80:83], v[64:67]\n\t"
  "v_fmac_f32 v123, v120, v120\n\t"
  "v_add_f32 v122, v122, v121\n\t"
  "v_mfma_f32_16x16x16_bf16 v[20:23], a[240:241], v[92:93], v[20:23]\n\t"
  "v_fmac_f32 v123, v121, v121\n\t"
  "v_add_f32_dpp v122, v122, v122 quad_perm:[1,0,3,2] row_mask:0xf bank_mask:0xf\n\t"
  "v_mfma_f32_16x16x16_bf16 v[24:27], a[242:243], v[92:93], v[24:27]\n\t"
  "buffer_load_dwordx4 v[208:211], v1, %[srd], s45 offen offset:0 sc1\n\t"
  "v_add_f32_dpp v123, v123, v123 quad_perm:[1,0,3,2] row_mask:0xf bank_mask:0xf\n\t"
  "v_mfma_f32_16x16x16_bf16 v[28:31], a[244:245], v[92:93], v[28:31]\n\t"
  "v_add_f32_dpp v122, v122, v122 quad_perm:[2,3,0,1] row_mask:0xf bank_mask:0xf\n\t"
  "v_add_f32_dpp v123, v123, v123 quad_perm:[2,3,0,1] row_mask:0xf bank_mask:0xf\n\t"
  "v_mfma_f32_16x16x16_bf16 v[32:35], a[240:241], v[94:95], v[32:35]\n\t"
  "v_add_f32_dpp v122, v122, v122 row_half_mirror row_mask:0xf bank_mask:0xf\n\t"
  "v_add_f32_dpp v123, v123, v123 row_half_mirror row_mask:0xf bank_mask:0xf\n\t"
  "v_mfma_f32_16x16x16_bf16 v[36:39], a[242:243], v[94:95], v[36:39]\n\t"
  "v_add_f32_dpp v122, v122, v122 row_mirror row_mask:0xf bank_mask:0xf\n\t"
  "v_add_f32_dpp v123, v123, v123 row_mirror row_mask:0xf bank_mask:0xf\n\t"
  "v_mfma_f32_16x16x16_bf16 v[40:43], a[244:245], v[94:95], v[40:43]\n\t"
  "s_waitcnt lgkmcnt(4)\n\t"
  "v_add_f32_dpp v122, v122, v122 row_bcast:15 row_mask:0xa bank_mask:0xf\n\t"
  "v_mfma_f32_16x16x32_bf16 v[44:47], a[144:147], v[84:87], v[44:47]\n\t"
  "ds_read_b64_tr_b16 v[76:77], v110 offset:16384\n\t"
  "buffer_load_dwordx4 v[212:215], v1, %[srd], s45 offen offset:1024 sc1\n\t"
  "v_mfma_f32_16x16x32_bf16 v[48:51], a[148:151], v[84:87], v[48:51]\n\t"
  "ds_read_b64_tr_b16 v[78:79], v110 offset:18432\n\t"
  "v_add_f32_dpp v123, v123, v123 row_bcast:15 row_mask:0xa bank_mask:0xf\n\t"
  "v_mfma_f32_16x16x32_bf16 v[52:55], a[152:155], v[84:87], v[52:55]\n\t"
  "ds_read_b64_tr_b16 v[80:81], v111 offset:16384\n\t"
  "v_add_f32_dpp v122, v122, v122 row_bcast:31 row_mask:0xc bank_mask:0xf\n\t"
  "v_mfma_f32_16x16x32_bf16 v[56:59], a[144:147], v[88:91], v[56:59]\n\t"
  "ds_read_b64_tr_b16 v[82:83], v111 offset:18432\n\t"
  "v_add_f32_dpp v123, v123, v123 row_bcast:31 row_mask:0xc bank_mask:0xf\n\t"
  "v_mfma_f32_16x16x32_bf16 v[60:63], a[148:151], v[88:91], v[60:63]\n\t"
  "buffer_load_dwordx4 v[216:219], v1, %[srd], s45 offen offset:2048 sc1\n\t"
  "v_mul_f32 v122, 0xbacccccd, v122\n\t"
  "v_mfma_f32_16x16x32_bf16 v[64:67], a[152:155], v[88:91], v[64:67]\n\t"
  "s_waitcnt lgkmcnt(4)\n\t"
  "v_mul_f32 v123, 0x3acccccd, v123\n\t"
  "v_fma_f32 v123, -v122, v122, v123\n\t"
  "v_max_f32 v123, 0, v123\n\t"
  "v_cvt_pk_bf16_f32 v123, 0, v123\n\t"
  "v_cvt_pk_bf16_f32 v126, v122, v122\n\t"
  "v_add_f32 v123, 0x3727c5ac, v123\n\t"
  "v_cvt_pk_bf16_f32 v123, 0, v123\n\t"
  "v_sqrt_f32 v123, v123\n\t"
  "s_nop 0\n\t"
  "v_cvt_pk_bf16_f32 v127, v123, v123\n\t"
  "buffer_store_dwordx2 v[126:127], v7, %[srd], s54 offen sc1\n\t"
  "v_mfma_f32_16x16x32_bf16 v[44:47], a[156:159], v[68:71], v[44:47]\n\t"
  "ds_read_b64_tr_b16 v[84:85], v110 offset:20480\n\t"
  "v_and_b32 v128, 0xffff0000, v93\n\t"
  "v_mfma_f32_16x16x32_bf16 v[48:51], a[160:163], v[68:71], v[48:51]\n\t"
  "ds_read_b64_tr_b16 v[86:87], v110 offset:22528\n\t"
  "v_and_b32 v129, 0xffff0000, v95\n\t"
  "v_mfma_f32_16x16x32_bf16 v[52:55], a[164:167], v[68:71], v[52:55]\n\t"
  "ds_read_b64_tr_b16 v[88:89], v111 offset:20480\n\t"
  "v_rcp_f32 v128, v128\n\t"
  "v_mfma_f32_16x16x32_bf16 v[56:59], a[156:159], v[72:75], v[56:59]\n\t"
  "ds_read_b64_tr_b16 v[90:91], v111 offset:22528\n\t"
  "buffer_load_dwordx4 v[220:223], v1, %[srd], s45 offen offset:3072 sc1\n\t"
  "v_mfma_f32_16x16x32_bf16 v[60:63], a[160:163], v[72:75], v[60:63]\n\t"
  "v_rcp_f32 v129, v129\n\t"
  "v_mul_f32 v128, v128, v128\n\t"
  "v_mfma_f32_16x16x32_bf16 v[64:67], a[164:167], v[72:75], v[64:67]\n\t"
  "s_waitcnt lgkmcnt(4)\n\t"
  "v_mul_f32 v129, v129, v129\n\t"
  "v_mfma_f32_16x16x32_bf16 v[44:47], a[168:171], v[76:79], v[44:47]\n\t"
  "ds_read_b64_tr_b16 v[68:69], v110 offset:24576\n\t"
  "v_max_f32 v20, 0, v20\n\t"
  "v_mfma_f32_16x16x32_bf16 v[48:51], a[172:175], v[76:79], v[48:51]\n\t"
  "ds_read_b64_tr_b16 v[70:71], v110 offset:26624\n\t"
  "v_max_f32 v21, 0, v21\n\t"
  "v_mfma_f32_16x16x32_bf16 v[52:55], a[176:179], v[76:79], v[52:55]\n\t"
  "ds_read_b64_tr_b16 v[72:73], v111 offset:24576\n\t"
  "s_add_u32 s45, %[sW2], 20480\n\t"
  "v_mfma_f32_16x16x32_bf16 v[56:59], a[168:171], v[80:83], v[56:59]\n\t"
  "ds_read_b64_tr_b16 v[74:75], v111 offset:26624\n\t"
  "v_mul_f32 v22, v22, v128\n\t"
  "v_mfma_f32_16x16x32_bf16 v[60:63], a[172:175], v[80:83], v[60:63]\n\t"
  "v_mul_f32 v23, v23, v128\n\t"
  "v_mul_f32 v20, v20, v22\n\t"
  "v_mfma_f32_16x16x32_bf16 v[64:67], a[176:179], v[80:83], v[64:67]\n\t"
  "s_waitcnt lgkmcnt(4)\n\t"
  "v_mul_f32 v21, v21, v23\n\t"
  "v_mfma_f32_16x16x32_bf16 v[44:47], a[180:183], v[84:87], v[44:47]\n\t"
  "ds_read_b64_tr_b16 v[76:77], v110 offset:28672\n\t"
  "v_cvt_pk_bf16_f32 v116, v20, v21\n\t"
  "v_mfma_f32_16x16x32_bf16 v[48:51], a[184:187], v[84:87], v[48:51]\n\t"
  "ds_read_b64_tr_b16 v[78:79], v110 offset:30720\n\t"
  "buffer_load_dwordx4 v[224:227], v1, %[srd], s45 offen offset:0 sc1\n\t"
  "v_mfma_f32_16x16x32_bf16 v[52:55], a[188:191], v[84:87], v[52:55]\n\t"
  "ds_read_b64_tr_b16 v[80:81], v111 offset:28672\n\t"
  "v_cvt_pk_bf16_f32 v112, v20, v21\n\t"
  "v_mfma_f32_16x16x32_bf16 v[56:59], a[180:183], v[88:91], v[56:59]\n\t"
  "ds_read_b64_tr_b16 v[82:83], v111 offset:30720\n\t"
  "v_max_f32 v24, 0, v24\n\t"
  "v_mfma_f32_16x16x32_bf16 v[60:63], a[184:187], v[88:91], v[60:63]\n\t"
  "v_max_f32 v25, 0, v25\n\t"
  "v_mul_f32 v26, v26, v128\n\t"
  "v_mfma_f32_16x16x32_bf16 v[64:67], a[188:191], v[88:91], v[64:67]\n\t"
  "s_waitcnt lgkmcnt(4)\n\t"
  "v_mul_f32 v27, v27, v128\n\t"
  "v_mfma_f32_16x16x32_bf16 v[44:47], a[192:195], v[68:71], v[44:47]\n\t"
  "ds_read_b64_tr_b16 v[84:85], v110 offset:32768\n\t"
  "buffer_load_dwordx4 v[228:231], v1, %[srd], s45 offen offset:1024 sc1\n\t"
  "v_mfma_f32_16x16x32_bf16 v[48:51], a[196:199], v[68:71], v[48:51]\n\t"
  "ds_read_b64_tr_b16 v[86:87], v110 offset:34816\n\t"
  "v_mul_f32 v24, v24, v26\n\t"
  "v_mfma_f32_16x16x32_bf16 v[52:55], a[200:203], v[68:71], v[52:55]\n\t"
  "ds_read_b64_tr_b16 v[88:89], v111 offset:32768\n\t"
  "v_mul_f32 v25, v25, v27\n\t"
  "v_mfma_f32_16x16x32_bf16 v[56:59], a[192:195], v[72:75], v[56:59]\n\t"
  "ds_read_b64_tr_b16 v[90:91], v111 offset:34816\n\t"
  "v_cvt_pk_bf16_f32 v117, v24, v25\n\t"
  "v_mfma_f32_16x16x32_bf16 v[60:63], a[196:199], v[72:75], v[60:63]\n\t"
  "v_cvt_pk_bf16_f32 v113, v24, v25\n\t"
  "v_max_f32 v28, 0, v28\n\t"
  "v_mfma_f32_16x16x32_bf16 v[64:67], a[200:203], v[72:75], v[64:67]\n\t"
  "s_waitcnt lgkmcnt(4)\n\t"
  "buffer_load_dwordx4 v[232:235], v1, %[srd], s45 offen offset:2048 sc1\n\t"
  "v_mfma_f32_16x16x32_bf16 v[44:47], a[204:207], v[76:79], v[44:47]\n\t"
  "ds_read_b64_tr_b16 v[68:69], v110 offset:36864\n\t"
  "v_max_f32 v29, 0, v29\n\t"
  "v_mfma_f32_16x16x32_bf16 v[48:51], a[208:211], v[76:79], v[48:51]\n\t"
  "ds_read_b64_tr_b16 v[70:71], v110 offset:38912\n\t"
  "v_mul_f32 v30, v30, v128\n\t"
  "v_mfma_f32_16x16x32_bf16 v[52:55], a[212:215], v[76:79], v[52:55]\n\t"
  "ds_read_b64_tr_b16 v[72:73], v111 offset:36864\n\t"
  "v_mul_f32 v31, v31, v128\n\t"
  "v_mfma_f32_16x16x32_bf16 v[56:59], a[204:207], v[80:83], v[56:59]\n\t"
  "ds_read_b64_tr_b16 v[74:75], v111 offset:38912\n\t"
  "v_mul_f32 v28, v28, v30\n\t"
  "v_mfma_f32_16x16x32_bf16 v[60:63], a[208:211], v[80:83], v[60:63]\n\t"
  "buffer_load_dwordx4 v[236:239], v1, %[srd], s45 offen offset:3072 sc1\n\t"
  "v_permlane16_swap_b32 v116, v117\n\t"
  "v_mfma_f32_16x16x32_bf16 v[64:67], a[212:215], v[80:83], v[64:67]\n\t"
  "s_waitcnt lgkmcnt(4)\n\t"
  "v_mul_f32 v29, v29, v31\n\t"
  "v_mfma_f32_16x16x32_bf16 v[44:47], a[216:219], v[84:87], v[44:47]\n\t"
  "ds_read_b64_tr_b16 v[76:77], v108 offset:0\n\t"
  "v_cvt_pk_bf16_f32 v118, v28, v29\n\t"
  "v_mfma_f32_16x16x32_bf16 v[48:51], a[220:223], v[84:87], v[48:51]\n\t"
  "ds_read_b64_tr_b16 v[78:79], v108 offset:2048\n\t"
  "v_cvt_pk_bf16_f32 v114, v28, v29\n\t"
  "v_mfma_f32_16x16x32_bf16 v[52:55], a[224:227], v[84:87], v[52:55]\n\t"
  "ds_read_b64_tr_b16 v[80:81], v109 offset:0\n\t"
  "s_mov_b32 s55, 0\n\t"
  "v_mfma_f32_16x16x32_bf16 v[56:59], a[216:219], v[88:91], v[56:59]\n\t"
  "ds_read_b64_tr_b16 v[82:83], v109 offset:2048\n\t"
  "s_add_u32 s45, %[sW2], 24576\n\t"
  "v_mfma_f32_16x16x32_bf16 v[60:63], a[220:223], v[88:91], v[60:63]\n\t"
  "v_permlane16_swap_b32 v118, v115\n\t"
  "v_mfma_f32_16x16x32_bf16 v[64:67], a[224:227], v[88:91], v[64:67]\n\t"
  "s_waitcnt lgkmcnt(4)\n\t"
  "v_permlane32_swap_b32 v117, v115\n\t"
  "v_mfma_f32_16x16x32_bf16 v[44:47], a[228:231], v[68:71], v[44:47]\n\t"
  "ds_read_b64_tr_b16 v[84:85], v108 offset:4096\n\t"
  "buffer_store_dwordx4 v[112:115], v8, %[srd], s55 offen sc1\n\t"
  "v_mfma_f32_16x16x32_bf16 v[48:51], a[232:235], v[68:71], v[48:51]\n\t"
  "ds_read_b64_tr_b16 v[86:87], v108 offset:6144\n\t"
  "v_max_f32 v32, 0, v32\n\t"
  "v_mfma_f32_16x16x32_bf16 v[52:55], a[236:239], v[68:71], v[52:55]\n\t"
  "ds_read_b64_tr_b16 v[88:89], v109 offset:4096\n\t"
  "buffer_load_dwordx4 v[240:243], v1, %[srd], s45 offen offset:0 sc1\n\t"
  "v_mfma_f32_16x16x32_bf16 v[56:59], a[228:231], v[72:75], v[56:59]\n\t"
  "ds_read_b64_tr_b16 v[90:91], v109 offset:6144\n\t"
  "v_max_f32 v33, 0, v33\n\t"
  "v_mfma_f32_16x16x32_bf16 v[60:63], a[232:235], v[72:75], v[60:63]\n\t"
  "v_mul_f32 v34, v34, v129\n\t"
  "v_mul_f32 v35, v35, v129\n\t"
  "v_mfma_f32_16x16x32_bf16 v[64:67], a[236:239], v[72:75], v[64:67]\n\t"
  "v_mul_f32 v32, v32, v34\n\t"
  "v_mul_f32 v33, v33, v35\n\t"
  "v_mfma_f32_16x16x16_bf16 v[44:47], a[240:241], v[96:97], v[44:47]\n\t"
  "v_cvt_pk_bf16_f32 v124, v32, v33\n\t"
  "v_cvt_pk_bf16_f32 v120, v32, v33\n\t"
  "v_mfma_f32_16x16x16_bf16 v[48:51], a[242:243], v[96:97], v[48:51]\n\t"
  "buffer_load_dwordx4 v[244:247], v1, %[srd], s45 offen offset:1024 sc1\n\t"
  "v_max_f32 v36, 0, v36\n\t"
  "v_mfma_f32_16x16x16_bf16 v[52:55], a[244:245], v[96:97], v[52:55]\n\t"
  "v_max_f32 v37, 0, v37\n\t"
  "v_mul_f32 v38, v38, v129\n\t"
  "v_mfma_f32_16x16x16_bf16 v[56:59], a[240:241], v[98:99], v[56:59]\n\t"
  "v_mul_f32 v39, v39, v129\n\t"
  "v_mul_f32 v36, v36, v38\n\t"
  "v_mfma_f32_16x16x16_bf16 v[60:63], a[242:243], v[98:99], v[60:63]\n\t"
  "v_mul_f32 v37, v37, v39\n\t"
  "v_cvt_pk_bf16_f32 v125, v36, v37\n\t"
  "v_cvt_pk_bf16_f32 v121, v36, v37\n\t"
  "v_max_f32 v40, 0, v40\n\t"
  "v_max_f32 v41, 0, v41\n\t"
  "v_mul_f32 v42, v42, v129\n\t"
  "v_mul_f32 v43, v43, v129\n\t"
  "v_mul_f32 v40, v40, v42\n\t"
  "v_permlane16_swap_b32 v124, v125\n\t"
  "v_mul_f32 v41, v41, v43\n\t"
  "v_cvt_pk_bf16_f32 v126, v40, v41\n\t"
  "v_cvt_pk_bf16_f32 v122, v40, v41\n\t"
  "s_mov_b32 s55, 49152\n\t"
  "v_permlane16_swap_b32 v126, v123\n\t"
  "s_nop 1\n\t"
  "v_permlane32_swap_b32 v125, v123\n\t"
  "buffer_store_dwordx4 v[120:123], v8, %[srd], s55 offen sc1\n\t"
  "v_mfma_f32_16x16x16_bf16 v[64:67], a[244:245], v[98:99], v[64:67]\n\t"
  "s_waitcnt lgkmcnt(4)\n\t"
  "v_mfma_f32_16x16x32_bf16 v[20:23], a[0:3], v[76:79], 0\n\t"
  "ds_read_b64_tr_b16 v[68:69], v108 offset:8192\n\t"
  "v_add_u32 v110, 122880, v5\n\t"
  "v_mfma_f32_16x16x32_bf16 v[24:27], a[4:7], v[76:79], 0\n\t"
  "ds_read_b64_tr_b16 v[70:71], v108 offset:10240\n\t"
  "v_xor_b32 v111, 32, v5\n\t"
  "v_mfma_f32_16x16x32_bf16 v[28:31], a[8:11], v[76:79], 0\n\t"
  "ds_read_b64_tr_b16 v[72:73], v109 offset:8192\n\t"
  "v_add_u32 v111, 122880, v111\n\t"
  "v_mfma_f32_16x16x32_bf16 v[32:35], a[0:3], v[80:83], 0\n\t"
  "ds_read_b64_tr_b16 v[74:75], v109 offset:10240\n\t"
  "v_and_b32 v128, 0xffff0000, v97\n\t"
  "v_mfma_f32_16x16x32_bf16 v[36:39], a[4:7], v[80:83], 0\n\t"
  "v_and_b32 v129, 0xffff0000, v99\n\t"
  "v_rcp_f32 v128, v128\n\t"
  "v_mfma_f32_16x16x32_bf16 v[40:43], a[8:11], v[80:83], 0\n\t"
  "s_waitcnt lgkmcnt(4)\n\t"
  "v_rcp_f32 v129, v129\n\t"
  "v_mfma_f32_16x16x32_bf16 v[20:23], a[12:15], v[84:87], v[20:23]\n\t"
  "ds_read_b64_tr_b16 v[76:77], v108 offset:12288\n\t"
  "s_barrier\n\t"
  "v_mfma_f32_16x16x32_bf16 v[24:27], a[16:19], v[84:87], v[24:27]\n\t"
  "ds_read_b64_tr_b16 v[78:79], v108 offset:14336\n\t"
  "v_mul_f32 v128, v128, v128\n\t"
  "v_mfma_f32_16x16x32_bf16 v[28:31], a[20:23], v[84:87], v[28:31]\n\t"
  "ds_read_b64_tr_b16 v[80:81], v109 offset:12288\n\t"
  "v_mul_f32 v129, v129, v129\n\t"
  "v_mfma_f32_16x16x32_bf16 v[32:35], a[12:15], v[88:91], v[32:35]\n\t"
  "ds_read_b64_tr_b16 v[82:83], v109 offset:14336\n\t"
  "s_mul_i32 s52, %[w], 20480\n\t"
  "v_mfma_f32_16x16x32_bf16 v[36:39], a[16:19], v[88:91], v[36:39]\n\t"
  "v_max_f32 v44, 0, v44\n\t"
  "v_max_f32 v45, 0, v45\n\t"
  "v_mfma_f32_16x16x32_bf16 v[40:43], a[20:23], v[88:91], v[40:43]\n\t"
  "s_waitcnt lgkmcnt(4)\n\t"
  "v_mul_f32 v46, v46, v128\n\t"
  "v_mfma_f32_16x16x32_bf16 v[20:23], a[24:27], v[68:71], v[20:23]\n\t"
  "ds_read_b64_tr_b16 v[84:85], v108 offset:16384\n\t"
  "s_add_u32 m0, s52, 0\n\t"
  "v_mfma_f32_16x16x32_bf16 v[24:27], a[28:31], v[68:71], v[24:27]\n\t"
  "ds_read_b64_tr_b16 v[86:87], v108 offset:18432\n\t"
  "v_mul_f32 v47, v47, v128\n\t"
  "v_mfma_f32_16x16x32_bf16 v[28:31], a[32:35], v[68:71], v[28:31]\n\t"
  "ds_read_b64_tr_b16 v[88:89], v109 offset:16384\n\t"
  "v_mul_f32 v44, v44, v46\n\t"
  "v_mfma_f32_16x16x32_bf16 v[32:35], a[24:27], v[72:75], v[32:35]\n\t"
  "ds_read_b64_tr_b16 v[90:91], v109 offset:18432\n\t"
  "v_mul_f32 v45, v45, v47\n\t"
  "v_mfma_f32_16x16x32_bf16 v[36:39], a[28:31], v[72:75], v[36:39]\n\t"
  "s_add_u32 s46, %[sW2], 26624\n\t"
  "v_cvt_pk_bf16_f32 v116, v44, v45\n\t"
  "v_mfma_f32_16x16x32_bf16 v[40:43], a[32:35], v[72:75], v[40:43]\n\t"
  "s_waitcnt lgkmcnt(4)\n\t"
  "v_cvt_pk_bf16_f32 v112, v44, v45\n\t"
  "v_mfma_f32_16x16x32_bf16 v[20:23], a[36:39], v[76:79], v[20:23]\n\t"
  "ds_read_b64_tr_b16 v[68:69], v108 offset:20480\n\t"
  "v_max_f32 v48, 0, v48\n\t"
  "v_mfma_f32_16x16x32_bf16 v[24:27], a[40:43], v[76:79], v[24:27]\n\t"
  "ds_read_b64_tr_b16 v[70:71], v108 offset:22528\n\t"
  "buffer_load_dwordx4 v1, %[srd], s46 offen offset:0 sc1 lds\n\t"
  "v_mfma_f32_16x16x32_bf16 v[28:31], a[44:47], v[76:79], v[28:31]\n\t"
  "ds_read_b64_tr_b16 v[72:73], v109 offset:20480\n\t"
  "v_max_f32 v49, 0, v49\n\t"
  "v_mfma_f32_16x16x32_bf16 v[32:35], a[36:39], v[80:83], v[32:35]\n\t"
  "ds_read_b64_tr_b16 v[74:75], v109 offset:22528\n\t"
  "v_mul_f32 v50, v50, v128\n\t"
  "v_mfma_f32_16x16x32_bf16 v[36:39], a[40:43], v[80:83], v[36:39]\n\t"
  "buffer_load_dwordx4 v1, %[srd], s46 offen offset:1024 sc1 lds\n\t"
  "v_mul_f32 v51, v51, v128\n\t"
  "v_mfma_f32_16x16x32_bf16 v[40:43], a[44:47], v[80:83], v[40:43]\n\t"
  "s_waitcnt lgkmcnt(4)\n\t"
  "v_mul_f32 v48, v48, v50\n\t"
  "v_mfma_f32_16x16x32_bf16 v[20:23], a[48:51], v[84:87], v[20:23]\n\t"
  "ds_read_b64_tr_b16 v[76:77], v108 offset:24576\n\t"
  "v_mul_f32 v49, v49, v51\n\t"
  "v_mfma_f32_16x16x32_bf16 v[24:27], a[52:55], v[84:87], v[24:27]\n\t"
  "ds_read_b64_tr_b16 v[78:79], v108 offset:26624\n\t"
  "v_cvt_pk_bf16_f32 v117, v48, v49\n\t"
  "v_mfma_f32_16x16x32_bf16 v[28:31], a[56:59], v[84:87], v[28:31]\n\t"
  "ds_read_b64_tr_b16 v[80:81], v109 offset:24576\n\t"
  "buffer_load_dwordx4 v1, %[srd], s46 offen offset:2048 sc1 lds\n\t"
  "v_mfma_f32_16x16x32_bf16 v[32:35], a[48:51], v[88:91], v[32:35]\n\t"
  "ds_read_b64_tr_b16 v[82:83], v109 offset:26624\n\t"
  "v_cvt_pk_bf16_f32 v113, v48, v49\n\t"
  "v_mfma_f32_16x16x32_bf16 v[36:39], a[52:55], v[88:91], v[36:39]\n\t"
  "v_max_f32 v52, 0, v52\n\t"
  "v_max_f32 v53, 0, v53\n\t"
  "v_mfma_f32_16x16x32_bf16 v[40:43], a[56:59], v[88:91], v[40:43]\n\t"
  "s_waitcnt lgkmcnt(4)\n\t"
  "buffer_load_dwordx4 v1, %[srd], s46 offen offset:3072 sc1 lds\n\t"
  "v_mfma_f32_16x16x32_bf16 v[20:23], a[60:63], v[68:71], v[20:23]\n\t"
  "ds_read_b64_tr_b16 v[84:85], v108 offset:28672\n\t"
  "v_mul_f32 v54, v54, v128\n\t"
  "v_mfma_f32_16x16x32_bf16 v[24:27], a[64:67], v[68:71], v[24:27]\n\t"
  "ds_read_b64_tr_b16 v[86:87], v108 offset:30720\n\t"
  "v_mul_f32 v55, v55, v128\n\t"
  "v_mfma_f32_16x16x32_bf16 v[28:31], a[68:71], v[68:71], v[28:31]\n\t"
  "ds_read_b64_tr_b16 v[88:89], v109 offset:28672\n\t"
  "v_mul_f32 v52, v52, v54\n\t"
  "v_mfma_f32_16x16x32_bf16 v[32:35], a[60:63], v[72:75], v[32:35]\n\t"
  "ds_read_b64_tr_b16 v[90:91], v109 offset:30720\n\t"
  "s_add_u32 m0, s52, 4096\n\t"
  "v_mfma_f32_16x16x32_bf16 v[36:39], a[64:67], v[72:75], v[36:39]\n\t"
  "v_permlane16_swap_b32 v116, v117\n\t"
  "v_mul_f32 v53, v53, v55\n\t"
  "v_mfma_f32_16x16x32_bf16 v[40:43], a[68:71], v[72:75], v[40:43]\n\t"
  "s_waitcnt lgkmcnt(4)\n\t"
  "v_cvt_pk_bf16_f32 v118, v52, v53\n\t"
  "v_mfma_f32_16x16x32_bf16 v[20:23], a[72:75], v[76:79], v[20:23]\n\t"
  "ds_read_b64_tr_b16 v[68:69], v108 offset:32768\n\t"
  "s_add_u32 s46, %[sW2], 30720\n\t"
  "v_mfma_f32_16x16x32_bf16 v[24:27], a[76:79], v[76:79], v[24:27]\n\t"
  "ds_read_b64_tr_b16 v[70:71], v108 offset:34816\n\t"
  "v_cvt_pk_bf16_f32 v114, v52, v53\n\t"
  "v_mfma_f32_16x16x32_bf16 v[28:31], a[80:83], v[76:79], v[28:31]\n\t"
  "ds_read_b64_tr_b16 v[72:73], v109 offset:32768\n\t"
  "s_mov_b32 s55, 98304\n\t"
  "v_mfma_f32_16x16x32_bf16 v[32:35], a[72:75], v[80:83], v[32:35]\n\t"
  "ds_read_b64_tr_b16 v[74:75], v109 offset:34816\n\t"
  "buffer_load_dwordx4 v1, %[srd], s46 offen offset:0 sc1 lds\n\t"
  "v_mfma_f32_16x16x32_bf16 v[36:39], a[76:79], v[80:83], v[36:39]\n\t"
  "v_permlane16_swap_b32 v118, v115\n\t"
  "v_mfma_f32_16x16x32_bf16 v[40:43], a[80:83], v[80:83], v[40:43]\n\t"
  "s_waitcnt lgkmcnt(4)\n\t"
  "v_permlane32_swap_b32 v117, v115\n\t"
  "v_mfma_f32_16x16x32_bf16 v[20:23], a[84:87], v[84:87], v[20:23]\n\t"
  "ds_read_b64_tr_b16 v[76:77], v108 offset:36864\n\t"
  "buffer_store_dwordx4 v[112:115], v8, %[srd], s55 offen sc1\n\t"
  "v_mfma_f32_16x16x32_bf16 v[24:27], a[88:91], v[84:87], v[24:27]\n\t"
  "ds_read_b64_tr_b16 v[78:79], v108 offset:38912\n\t"
  "buffer_load_dwordx4 v1, %[srd], s46 offen offset:1024 sc1 lds\n\t"
  "v_mfma_f32_16x16x32_bf16 v[28:31], a[92:95], v[84:87], v[28:31]\n\t"
  "ds_read_b64_tr_b16 v[80:81], v109 offset:36864\n\t"
  "v_max_f32 v56, 0, v56\n\t"
  "v_mfma_f32_16x16x32_bf16 v[32:35], a[84:87], v[88:91], v[32:35]\n\t"
  "ds_read_b64_tr_b16 v[82:83], v109 offset:38912\n\t"
  "v_max_f32 v57, 0, v57\n\t"
  "v_mfma_f32_16x16x32_bf16 v[36:39], a[88:91], v[88:91], v[36:39]\n\t"
  "buffer_load_dwordx4 v1, %[srd], s46 offen offset:2048 sc1 lds\n\t"
  "v_mul_f32 v58, v58, v129\n\t"
  "v_mfma_f32_16x16x32_bf16 v[40:43], a[92:95], v[88:91], v[40:43]\n\t"
  "s_waitcnt lgkmcnt(4)\n\t"
  "v_mul_f32 v59, v59, v129\n\t"
  "v_mfma_f32_16x16x32_bf16 v[20:23], a[96:99], v[68:71], v[20:23]\n\t"
  "ds_read_b64_tr_b16 v[84:85], v110 offset:0\n\t"
  "v_mul_f32 v56, v56, v58\n\t"
  "v_mfma_f32_16x16x32_bf16 v[24:27], a[100:103], v[68:71], v[24:27]\n\t"
  "ds_read_b64_tr_b16 v[86:87], v110 offset:2048\n\t"
  "buffer_load_dwordx4 v1, %[srd], s46 offen offset:3072 sc1 lds\n\t"
  "v_mfma_f32_16x16x32_bf16 v[28:31], a[104:107], v[68:71], v[28:31]\n\t"
  "ds_read_b64_tr_b16 v[88:89], v111 offset:0\n\t"
  "v_mul_f32 v57, v57, v59\n\t"
  "v_mfma_f32_16x16x32_bf16 v[32:35], a[96:99], v[72:75], v[32:35]\n\t"
  "ds_read_b64_tr_b16 v[90:91], v111 offset:2048\n\t"
  "v_cvt_pk_bf16_f32 v124, v56, v57\n\t"
  "v_mfma_f32_16x16x32_bf16 v[36:39], a[100:103], v[72:75], v[36:39]\n\t"
  "buffer_load_dwordx2 v[100:101], v136, %[srd], s54 offen offset:0 sc1\n\t"
  "buffer_load_dwordx2 v[102:103], v136, %[srd], s54 offen offset:1024 sc1\n\t"
  "v_mfma_f32_16x16x32_bf16 v[40:43], a[104:107], v[72:75], v[40:43]\n\t"
  "s_waitcnt lgkmcnt(4)\n\t"
  "buffer_load_dwordx2 v[104:105], v136, %[srd], s54 offen offset:2048 sc1\n\t"
  "v_mfma_f32_16x16x32_bf16 v[20:23], a[108:111], v[76:79], v[20:23]\n\t"
  "ds_read_b64_tr_b16 v[68:69], v110 offset:4096\n\t"
  "s_movk_i32 s56, 0\n\t"
  "v_mfma_f32_16x16x32_bf16 v[24:27], a[112:115], v[76:79], v[24:27]\n\t"
  "ds_read_b64_tr_b16 v[70:71], v110 offset:6144\n\t"
  "s_waitcnt vmcnt(12)\n\t"
  "v_mfma_f32_16x16x32_bf16 v[28:31], a[116:119], v[76:79], v[28:31]\n\t"
  "ds_read_b64_tr_b16 v[72:73], v111 offset:4096\n\t"
  "buffer_load_dwordx2 v[106:107], v136, %[srd], s54 offen offset:3072 sc1\n\t"
  "v_mfma_f32_16x16x32_bf16 v[32:35], a[108:111], v[80:83], v[32:35]\n\t"
  "ds_read_b64_tr_b16 v[74:75], v111 offset:6144\n\t"
  "buffer_store_dwordx4 v[132:135], v9, %[srd], s56 offen sc1\n\t"
  "v_mfma_f32_16x16x32_bf16 v[36:39], a[112:115], v[80:83], v[36:39]\n\t"
  "s_add_u32 m0, s52, 8192\n\t"
  "s_add_u32 s46, %[sW2], 34816\n\t"
  "v_mfma_f32_16x16x32_bf16 v[40:43], a[116:119], v[80:83], v[40:43]\n\t"
  "s_waitcnt lgkmcnt(4)\n\t"
  "v_cvt_pk_bf16_f32 v120, v56, v57\n\t"
  "v_mfma_f32_16x16x32_bf16 v[20:23], a[120:123], v[84:87], v[20:23]\n\t"
  "ds_read_b64_tr_b16 v[76:77], v110 offset:8192\n\t"
  "v_xor_b32 v108, 64, v5\n\t"
  "v_mfma_f32_16x16x32_bf16 v[24:27], a[124:127], v[84:87], v[24:27]\n\t"
  "ds_read_b64_tr_b16 v[78:79], v110 offset:10240\n\t"
  "v_add_u32 v108, 81920, v108\n\t"
  "v_mfma_f32_16x16x32_bf16 v[28:31], a[128:131], v[84:87], v[28:31]\n\t"
  "ds_read_b64_tr_b16 v[80:81], v111 offset:8192\n\t"
  "v_xor_b32 v109, 96, v5\n\t"
  "v_mfma_f32_16x16x32_bf16 v[32:35], a[120:123], v[88:91], v[32:35]\n\t"
  "ds_read_b64_tr_b16 v[82:83], v111 offset:10240\n\t"
  "v_add_u32 v109, 81920, v109\n\t"
  "v_mfma_f32_16x16x32_bf16 v[36:39], a[124:127], v[88:91], v[36:39]\n\t"
  "buffer_load_dwordx4 v1, %[srd], s46 offen offset:0 sc1 lds\n\t"
  "buffer_load_dwordx4 v1, %[srd], s46 offen offset:1024 sc1 lds\n\t"
  "v_mfma_f32_16x16x32_bf16 v[40:43], a[128:131], v[88:91], v[40:43]\n\t"
  "s_waitcnt lgkmcnt(4)\n\t"
  "v_max_f32 v60, 0, v60\n\t"
  "v_mfma_f32_16x16x32_bf16 v[20:23], a[132:135], v[68:71], v[20:23]\n\t"
  "ds_read_b64_tr_b16 v[84:85], v110 offset:12288\n\t"
  "buffer_load_dwordx4 v1, %[srd], s46 offen offset:2048 sc1 lds\n\t"
  "v_mfma_f32_16x16x32_bf16 v[24:27], a[136:139], v[68:71], v[24:27]\n\t"
  "ds_read_b64_tr_b16 v[86:87], v110 offset:14336\n\t"
  "v_max_f32 v61, 0, v61\n\t"
  "v_mfma_f32_16x16x32_bf16 v[28:31], a[140:143], v[68:71], v[28:31]\n\t"
  "ds_read_b64_tr_b16 v[88:89], v111 offset:12288\n\t"
  "v_mul_f32 v62, v62, v129\n\t"
  "v_mfma_f32_16x16x32_bf16 v[32:35], a[132:135], v[72:75], v[32:35]\n\t"
  "ds_read_b64_tr_b16 v[90:91], v111 offset:14336\n\t"
  "v_mul_f32 v63, v63, v129\n\t"
  "v_mfma_f32_16x16x32_bf16 v[36:39], a[136:139], v[72:75], v[36:39]\n\t"
  "buffer_load_dwordx4 v1, %[srd], s46 offen offset:3072 sc1 lds\n\t"
  "v_mul_f32 v60, v60, v62\n\t"
  "v_mfma_f32_16x16x32_bf16 v[40:43], a[140:143], v[72:75], v[40:43]\n\t"
  "s_waitcnt lgkmcnt(4)\n\t"
  "v_mul_f32 v61, v61, v63\n\t"
  "v_mfma_f32_16x16x32_bf16 v[20:23], a[144:147], v[76:79], v[20:23]\n\t"
  "ds_read_b64_tr_b16 v[68:69], v110 offset:16384\n\t"
  "v_cvt_pk_bf16_f32 v125, v60, v61\n\t"
  "v_mfma_f32_16x16x32_bf16 v[24:27], a[148:151], v[76:79], v[24:27]\n\t"
  "ds_read_b64_tr_b16 v[70:71], v110 offset:18432\n\t"
  "s_add_u32 m0, s52, 12288\n\t"
  "v_mfma_f32_16x16x32_bf16 v[28:31], a[152:155], v[76:79], v[28:31]\n\t"
  "ds_read_b64_tr_b16 v[72:73], v111 offset:16384\n\t"
  "v_cvt_pk_bf16_f32 v121, v60, v61\n\t"
  "v_mfma_f32_16x16x32_bf16 v[32:35], a[144:147], v[80:83], v[32:35]\n\t"
  "ds_read_b64_tr_b16 v[74:75], v111 offset:18432\n\t"
  "v_max_f32 v64, 0, v64\n\t"
  "v_mfma_f32_16x16x32_bf16 v[36:39], a[148:151], v[80:83], v[36:39]\n\t"
  "s_add_u32 s46, %[sW2], 38912\n\t"
  "v_max_f32 v65, 0, v65\n\t"
  "v_mfma_f32_16x16x32_bf16 v[40:43], a[152:155], v[80:83], v[40:43]\n\t"
  "s_waitcnt lgkmcnt(4)\n\t"
  "v_mul_f32 v66, v66, v129\n\t"
  "v_mfma_f32_16x16x32_bf16 v[20:23], a[156:159], v[84:87], v[20:23]\n\t"
  "ds_read_b64_tr_b16 v[76:77], v110 offset:20480\n\t"
  "v_mul_f32 v67, v67, v129\n\t"
  "v_mfma_f32_16x16x32_bf16 v[24:27], a[160:163], v[84:87], v[24:27]\n\t"
  "ds_read_b64_tr_b16 v[78:79], v110 offset:22528\n\t"
  "v_mul_f32 v64, v64, v66\n\t"
  "v_mfma_f32_16x16x32_bf16 v[28:31], a[164:167], v[84:87], v[28:31]\n\t"
  "ds_read_b64_tr_b16 v[80:81], v111 offset:20480\n\t"
  "buffer_load_dwordx4 v1, %[srd], s46 offen offset:0 sc1 lds\n\t"
  "v_mfma_f32_16x16x32_bf16 v[32:35], a[156:159], v[88:91], v[32:35]\n\t"
  "ds_read_b64_tr_b16 v[82:83], v111 offset:22528\n\t"
  "v_permlane16_swap_b32 v124, v125\n\t"
  "v_mfma_f32_16x16x32_bf16 v[36:39], a[160:163], v[88:91], v[36:39]\n\t"
  "v_mul_f32 v65, v65, v67\n\t"
  "v_cvt_pk_bf16_f32 v126, v64, v65\n\t"
  "v_mfma_f32_16x16x32_bf16 v[40:43], a[164:167], v[88:91], v[40:43]\n\t"
  "s_waitcnt lgkmcnt(4)\n\t"
  "buffer_load_dwordx4 v1, %[srd], s46 offen offset:1024 sc1 lds\n\t"
  "v_mfma_f32_16x16x32_bf16 v[20:23], a[168:171], v[68:71], v[20:23]\n\t"
  "ds_read_b64_tr_b16 v[84:85], v110 offset:24576\n\t"
  "v_cvt_pk_bf16_f32 v122, v64, v65\n\t"
  "v_mfma_f32_16x16x32_bf16 v[24:27], a[172:175], v[68:71], v[24:27]\n\t"
  "ds_read_b64_tr_b16 v[86:87], v110 offset:26624\n\t"
  "s_mov_b32 s55, 147456\n\t"
  "v_mfma_f32_16x16x32_bf16 v[28:31], a[176:179], v[68:71], v[28:31]\n\t"
  "ds_read_b64_tr_b16 v[88:89], v111 offset:24576\n\t"
  "v_permlane16_swap_b32 v126, v123\n\t"
  "v_mfma_f32_16x16x32_bf16 v[32:35], a[168:171], v[72:75], v[32:35]\n\t"
  "ds_read_b64_tr_b16 v[90:91], v111 offset:26624\n\t"
  "buffer_load_dwordx4 v1, %[srd], s46 offen offset:2048 sc1 lds\n\t"
  "v_mfma_f32_16x16x32_bf16 v[36:39], a[172:175], v[72:75], v[36:39]\n\t"
  "v_permlane32_swap_b32 v125, v123\n\t"
  "buffer_store_dwordx4 v[120:123], v8, %[srd], s55 offen sc1\n\t"
  "v_mfma_f32_16x16x32_bf16 v[40:43], a[176:179], v[72:75], v[40:43]\n\t"
  "s_waitcnt lgkmcnt(4)\n\t"
  "v_mfma_f32_16x16x32_bf16 v[20:23], a[180:183], v[76:79], v[20:23]\n\t"
  "ds_read_b64_tr_b16 v[68:69], v110 offset:28672\n\t"
  "buffer_load_dwordx4 v1, %[srd], s46 offen offset:3072 sc1 lds\n\t"
  "v_mfma_f32_16x16x32_bf16 v[24:27], a[184:187], v[76:79], v[24:27]\n\t"
  "ds_read_b64_tr_b16 v[70:71], v110 offset:30720\n\t"
  "v_mfma_f32_16x16x32_bf16 v[28:31], a[188:191], v[76:79], v[28:31]\n\t"
  "ds_read_b64_tr_b16 v[72:73], v111 offset:28672\n\t"
  "v_mfma_f32_16x16x32_bf16 v[32:35], a[180:183], v[80:83], v[32:35]\n\t"
  "ds_read_b64_tr_b16 v[74:75], v111 offset:30720\n\t"
  "s_add_u32 m0, s52, 16384\n\t"
  "v_mfma_f32_16x16x32_bf16 v[36:39], a[184:187], v[80:83], v[36:39]\n\t"
  "v_mfma_f32_16x16x32_bf16 v[40:43], a[188:191], v[80:83], v[40:43]\n\t"
  "s_waitcnt lgkmcnt(4)\n\t"
  "v_mfma_f32_16x16x32_bf16 v[20:23], a[192:195], v[84:87], v[20:23]\n\t"
  "ds_read_b64_tr_b16 v[76:77], v110 offset:32768\n\t"
  "v_mfma_f32_16x16x32_bf16 v[24:27], a[196:199], v[84:87], v[24:27]\n\t"
  "ds_read_b64_tr_b16 v[78:79], v110 offset:34816\n\t"
  "s_add_u32 s46, %[sW2], 43008\n\t"
  "v_mfma_f32_16x16x32_bf16 v[28:31], a[200:203], v[84:87], v[28:31]\n\t"
  "ds_read_b64_tr_b16 v[80:81], v111 offset:32768\n\t"
  "v_mfma_f32_16x16x32_bf16 v[32:35], a[192:195], v[88:91], v[32:35]\n\t"
  "ds_read_b64_tr_b16 v[82:83], v111 offset:34816\n\t"
  "v_mfma_f32_16x16x32_bf16 v[36:39], a[196:199], v[88:91], v[36:39]\n\t"
  "buffer_load_dwordx4 v1, %[srd], s46 offen offset:0 sc1 lds\n\t"
  "v_mfma_f32_16x16x32_bf16 v[40:43], a[200:203], v[88:91], v[40:43]\n\t"
  "s_waitcnt lgkmcnt(4)\n\t"
  "v_mfma_f32_16x16x32_bf16 v[20:23], a[204:207], v[68:71], v[20:23]\n\t"
  "ds_read_b64_tr_b16 v[84:85], v110 offset:36864\n\t"
  "v_mfma_f32_16x16x32_bf16 v[24:27], a[208:211], v[68:71], v[24:27]\n\t"
  "ds_read_b64_tr_b16 v[86:87], v110 offset:38912\n\t"
  "buffer_load_dwordx4 v1, %[srd], s46 offen offset:1024 sc1 lds\n\t"
  "v_mfma_f32_16x16x32_bf16 v[28:31], a[212:215], v[68:71], v[28:31]\n\t"
  "ds_read_b64_tr_b16 v[88:89], v111 offset:36864\n\t"
  "v_mfma_f32_16x16x32_bf16 v[32:35], a[204:207], v[72:75], v[32:35]\n\t"
  "ds_read_b64_tr_b16 v[90:91], v111 offset:38912\n\t"
  "v_mfma_f32_16x16x32_bf16 v[36:39], a[208:211], v[72:75], v[36:39]\n\t"
  "s_waitcnt vmcnt(14)\n\t"
  "v_min_u32 v131, v101, v103\n\t"
  "v_mfma_f32_16x16x32_bf16 v[40:43], a[212:215], v[72:75], v[40:43]\n\t"
  "s_waitcnt lgkmcnt(4)\n\t"
  "s_waitcnt vmcnt(12)\n\t"
  "v_min3_u32 v131, v131, v105, v107\n\t"
  "v_mfma_f32_16x16x32_bf16 v[20:23], a[216:219], v[76:79], v[20:23]\n\t"
  "ds_read_b64_tr_b16 v[68:69], v108 offset:0\n\t"
  "v_cmp_eq_u32 vcc, 0, v131\n\t"
  "v_mfma_f32_16x16x32_bf16 v[24:27], a[220:223], v[76:79], v[24:27]\n\t"
  "ds_read_b64_tr_b16 v[70:71], v108 offset:2048\n\t"
  "buffer_load_dwordx4 v1, %[srd], s46 offen offset:2048 sc1 lds\n\t"
  "v_mfma_f32_16x16x32_bf16 v[28:31], a[224:227], v[76:79], v[28:31]\n\t"
  "ds_read_b64_tr_b16 v[72:73], v109 offset:0\n\t"
  "s_cbranch_vccnz Lst1_slow%=\n\t"
  "Lst1_back%=:\n\t"
  "v_mfma_f32_16x16x32_bf16 v[32:35], a[216:219], v[80:83], v[32:35]\n\t"
  "ds_read_b64_tr_b16 v[74:75], v109 offset:2048\n\t"
  "buffer_load_dwordx4 v1, %[srd], s46 offen offset:3072 sc1 lds\n\t"
  "v_mfma_f32_16x16x32_bf16 v[36:39], a[220:223], v[80:83], v[36:39]\n\t"
  "v_mfma_f32_16x16x32_bf16 v[40:43], a[224:227], v[80:83], v[40:43]\n\t"
  "s_waitcnt lgkmcnt(4)\n\t"
  "v_mfma_f32_16x16x32_bf16 v[20:23], a[228:231], v[84:87], v[20:23]\n\t"
  "ds_read_b64_tr_b16 v[76:77], v108 offset:4096\n\t"
  "v_mfma_f32_16x16x32_bf16 v[24:27], a[232:235], v[84:87], v[24:27]\n\t"
  "ds_read_b64_tr_b16 v[78:79], v108 offset:6144\n\t"
  "v_mfma_f32_16x16x32_bf16 v[28:31], a[236:239], v[84:87], v[28:31]\n\t"
  "ds_read_b64_tr_b16 v[80:81], v109 offset:4096\n\t"
  "v_mfma_f32_16x16x32_bf16 v[32:35], a[228:231], v[88:91], v[32:35]\n\t"
  "ds_read_b64_tr_b16 v[82:83], v109 offset:6144\n\t"
  "v_mfma_f32_16x16x32_bf16 v[36:39], a[232:235], v[88:91], v[36:39]\n\t"
  "v_mfma_f32_16x16x32_bf16 v[40:43], a[236:239], v[88:91], v[40:43]\n\t"
  "v_mfma_f32_16x16x16_bf16 v[20:23], a[240:241], v[100:101], v[20:23]\n\t"
  "v_mfma_f32_16x16x16_bf16 v[24:27], a[242:243], v[100:101], v[24:27]\n\t"
  "v_mfma_f32_16x16x16_bf16 v[28:31], a[244:245], v[100:101], v[28:31]\n\t"
  "v_mfma_f32_16x16x16_bf16 v[32:35], a[240:241], v[102:103], v[32:35]\n\t"
  "v_mfma_f32_16x16x16_bf16 v[36:39], a[242:243], v[102:103], v[36:39]\n\t"
  "v_mfma_f32_16x16x16_bf16 v[40:43], a[244:245], v[102:103], v[40:43]\n\t"
  "s_waitcnt lgkmcnt(4)\n\t"
  "v_mfma_f32_16x16x32_bf16 v[44:47], a[0:3], v[68:71], 0\n\t"
  "ds_read_b64_tr_b16 v[84:85], v108 offset:8192\n\t"
  "v_xor_b32 v110, 64, v5\n\t"
  "v_mfma_f32_16x16x32_bf16 v[48:51], a[4:7], v[68:71], 0\n\t"
  "ds_read_b64_tr_b16 v[86:87], v108 offset:10240\n\t"
  "v_add_u32 v110, 122880, v110\n\t"
  "v_mfma_f32_16x16x32_bf16 v[52:55], a[8:11], v[68:71], 0\n\t"
  "ds_read_b64_tr_b16 v[88:89], v109 offset:8192\n\t"
  "v_xor_b32 v111, 96, v5\n\t"
  "v_mfma_f32_16x16x32_bf16 v[56:59], a[0:3], v[72:75], 0\n\t"
  "ds_read_b64_tr_b16 v[90:91], v109 offset:10240\n\t"
  "v_add_u32 v111, 122880, v111\n\t"
  "v_mfma_f32_16x16x32_bf16 v[60:63], a[4:7], v[72:75], 0\n\t"
  "v_and_b32 v128, 0xffff0000, v101\n\t"
  "v_and_b32 v129, 0xffff0000, v103\n\t"
  "v_mfma_f32_16x16x32_bf16 v[64:67], a[8:11], v[72:75], 0\n\t"
  "s_waitcnt lgkmcnt(4)\n\t"
  "v_rcp_f32 v128, v128\n\t"
  "v_mfma_f32_16x16x32_bf16 v[44:47], a[12:15], v[76:79], v[44:47]\n\t"
  "ds_read_b64_tr_b16 v[68:69], v108 offset:12288\n\t"
  "s_add_u32 s45, %[sW2], 47104\n\t"
  "v_mfma_f32_16x16x32_bf16 v[48:51], a[16:19], v[76:79], v[48:51]\n\t"
  "ds_read_b64_tr_b16 v[70:71], v108 offset:14336\n\t"
  "v_rcp_f32 v129, v129\n\t"
  "v_mfma_f32_16x16x32_bf16 v[52:55], a[20:23], v[76:79], v[52:55]\n\t"
  "ds_read_b64_tr_b16 v[72:73], v109 offset:12288\n\t"
  "v_mul_f32 v128, v128, v128\n\t"
  "v_mfma_f32_16x16x32_bf16 v[56:59], a[12:15], v[80:83], v[56:59]\n\t"
  "ds_read_b64_tr_b16 v[74:75], v109 offset:14336\n\t"
  "v_mul_f32 v129, v129, v129\n\t"
  "v_mfma_f32_16x16x32_bf16 v[60:63], a[16:19], v[80:83], v[60:63]\n\t"
  "buffer_load_dwordx4 a[0:3], v1, %[srd], s45 offen offset:0 sc1\n\t"
  "v_max_f32 v20, 0, v20\n\t"
  "v_mfma_f32_16x16x32_bf16 v[64:67], a[20:23], v[80:83], v[64:67]\n\t"
  "s_waitcnt lgkmcnt(4)\n\t"
  "v_max_f32 v21, 0, v21\n\t"
  "v_mfma_f32_16x16x32_bf16 v[44:47], a[24:27], v[84:87], v[44:47]\n\t"
  "ds_read_b64_tr_b16 v[76:77], v108 offset:16384\n\t"
  "v_mul_f32 v22, v22, v128\n\t"
  "v_mfma_f32_16x16x32_bf16 v[48:51], a[28:31], v[84:87], v[48:51]\n\t"
  "ds_read_b64_tr_b16 v[78:79], v108 offset:18432\n\t"
  "v_mul_f32 v23, v23, v128\n\t"
  "v_mfma_f32_16x16x32_bf16 v[52:55], a[32:35], v[84:87], v[52:55]\n\t"
  "ds_read_b64_tr_b16 v[80:81], v109 offset:16384\n\t"
  "buffer_load_dwordx4 a[4:7], v1, %[srd], s45 offen offset:1024 sc1\n\t"
  "v_mfma_f32_16x16x32_bf16 v[56:59], a[24:27], v[88:91], v[56:59]\n\t"
  "ds_read_b64_tr_b16 v[82:83], v109 offset:18432\n\t"
  "v_mul_f32 v20, v20, v22\n\t"
  "v_mfma_f32_16x16x32_bf16 v[60:63], a[28:31], v[88:91], v[60:63]\n\t"
  "v_mul_f32 v21, v21, v23\n\t"
  "v_cvt_pk_bf16_f32 v116, v20, v21\n\t"
  "v_mfma_f32_16x16x32_bf16 v[64:67], a[32:35], v[88:91], v[64:67]\n\t"
  "s_waitcnt lgkmcnt(4)\n\t"
  "v_cvt_pk_bf16_f32 v112, v20, v21\n\t"
  "v_mfma_f32_16x16x32_bf16 v[44:47], a[36:39], v[68:71], v[44:47]\n\t"
  "ds_read_b64_tr_b16 v[84:85], v108 offset:20480\n\t"
  "s_movk_i32 s56, 8192\n\t"
  "v_mfma_f32_16x16x32_bf16 v[48:51], a[40:43], v[68:71], v[48:51]\n\t"
  "ds_read_b64_tr_b16 v[86:87], v108 offset:22528\n\t"
  "s_waitcnt vmcnt(7)\n\t"
  "v_mfma_f32_16x16x32_bf16 v[52:55], a[44:47], v[68:71], v[52:55]\n\t"
  "ds_read_b64_tr_b16 v[88:89], v109 offset:20480\n\t"
  "buffer_store_dwordx4 v[132:135], v9, %[srd], s56 offen sc1\n\t"
  "v_mfma_f32_16x16x32_bf16 v[56:59], a[36:39], v[72:75], v[56:59]\n\t"
  "ds_read_b64_tr_b16 v[90:91], v109 offset:22528\n\t"
  "buffer_load_dwordx4 a[8:11], v1, %[srd], s45 offen offset:2048 sc1\n\t"
  "v_mfma_f32_16x16x32_bf16 v[60:63], a[40:43], v[72:75], v[60:63]\n\t"
  "buffer_load_dwordx4 a[12:15], v1, %[srd], s45 offen offset:3072 sc1\n\t"
  "v_max_f32 v24, 0, v24\n\t"
  "v_mfma_f32_16x16x32_bf16 v[64:67], a[44:47], v[72:75], v[64:67]\n\t"
  "s_waitcnt lgkmcnt(4)\n\t"
  "v_max_f32 v25, 0, v25\n\t"
  "v_mfma_f32_16x16x32_bf16 v[44:47], a[48:51], v[76:79], v[44:47]\n\t"
  "ds_read_b64_tr_b16 v[68:69], v108 offset:24576\n\t"
  "v_mul_f32 v26, v26, v128\n\t"
  "v_mfma_f32_16x16x32_bf16 v[48:51], a[52:55], v[76:79], v[48:51]\n\t"
  "ds_read_b64_tr_b16 v[70:71], v108 offset:26624\n\t"
  "v_mul_f32 v27, v27, v128\n\t"
  "v_mfma_f32_16x16x32_bf16 v[52:55], a[56:59], v[76:79], v[52:55]\n\t"
  "ds_read_b64_tr_b16 v[72:73], v109 offset:24576\n\t"
  "s_add_u32 s45, %[sW2], 51200\n\t"
  "v_mfma_f32_16x16x32_bf16 v[56:59], a[48:51], v[80:83], v[56:59]\n\t"
  "ds_read_b64_tr_b16 v[74:75], v109 offset:26624\n\t"
  "v_mul_f32 v24, v24, v26\n\t"
  "v_mfma_f32_16x16x32_bf16 v[60:63], a[52:55], v[80:83], v[60:63]\n\t"
  "v_mul_f32 v25, v25, v27\n\t"
  "v_cvt_pk_bf16_f32 v117, v24, v25\n\t"
  "v_mfma_f32_16x16x32_bf16 v[64:67], a[56:59], v[80:83], v[64:67]\n\t"
  "s_waitcnt lgkmcnt(4)\n\t"
  "v_cvt_pk_bf16_f32 v113, v24, v25\n\t"
  "v_mfma_f32_16x16x32_bf16 v[44:47], a[60:63], v[84:87], v[44:47]\n\t"
  "ds_read_b64_tr_b16 v[76:77], v108 offset:28672\n\t"
  "s_add_u32 s57, %[sFL], 0\n\t"
  "v_mfma_f32_16x16x32_bf16 v[48:51], a[64:67], v[84:87], v[48:51]\n\t"
  "ds_read_b64_tr_b16 v[78:79], v108 offset:30720\n\t"
  "buffer_load_dwordx2 v[138:139], v137, %[srd], s57 offen sc1\n\t"
  "v_mfma_f32_16x16x32_bf16 v[52:55], a[68:71], v[84:87], v[52:55]\n\t"
  "ds_read_b64_tr_b16 v[80:81], v109 offset:28672\n\t"
  "buffer_load_dwordx4 a[16:19], v1, %[srd], s45 offen offset:0 sc1\n\t"
  "v_mfma_f32_16x16x32_bf16 v[56:59], a[60:63], v[88:91], v[56:59]\n\t"
  "ds_read_b64_tr_b16 v[82:83], v109 offset:30720\n\t"
  "v_max_f32 v28, 0, v28\n\t"
  "v_mfma_f32_16x16x32_bf16 v[60:63], a[64:67], v[88:91], v[60:63]\n\t"
  "buffer_load_dwordx4 a[20:23], v1, %[srd], s45 offen offset:1024 sc1\n\t"
  "v_max_f32 v29, 0, v29\n\t"
  "v_mfma_f32_16x16x32_bf16 v[64:67], a[68:71], v[88:91], v[64:67]\n\t"
  "s_waitcnt lgkmcnt(4)\n\t"
  "v_mul_f32 v30, v30, v128\n\t"
  "v_mfma_f32_16x16x32_bf16 v[44:47], a[72:75], v[68:71], v[44:47]\n\t"
  "ds_read_b64_tr_b16 v[84:85], v108 offset:32768\n\t"
  "v_mul_f32 v31, v31, v128\n\t"
  "v_mfma_f32_16x16x32_bf16 v[48:51], a[76:79], v[68:71], v[48:51]\n\t"
  "ds_read_b64_tr_b16 v[86:87], v108 offset:34816\n\t"
  "v_mul_f32 v28, v28, v30\n\t"
  "v_mfma_f32_16x16x32_bf16 v[52:55], a[80:83], v[68:71], v[52:55]\n\t"
  "ds_read_b64_tr_b16 v[88:89], v109 offset:32768\n\t"
  "buffer_load_dwordx4 a[24:27], v1, %[srd], s45 offen offset:2048 sc1\n\t"
  "v_mfma_f32_16x16x32_bf16 v[56:59], a[72:75], v[72:75], v[56:59]\n\t"
  "ds_read_b64_tr_b16 v[90:91], v109 offset:34816\n\t"
  "v_permlane16_swap_b32 v116, v117\n\t"
  "v_mfma_f32_16x16x32_bf16 v[60:63], a[76:79], v[72:75], v[60:63]\n\t"
  "v_mul_f32 v29, v29, v31\n\t"
  "v_cvt_pk_bf16_f32 v118, v28, v29\n\t"
  "v_mfma_f32_16x16x32_bf16 v[64:67], a[80:83], v[72:75], v[64:67]\n\t"
  "s_waitcnt lgkmcnt(4)\n\t"
  "v_cvt_pk_bf16_f32 v114, v28, v29\n\t"
  "v_mfma_f32_16x16x32_bf16 v[44:47], a[84:87], v[76:79], v[44:47]\n\t"
  "ds_read_b64_tr_b16 v[68:69], v108 offset:36864\n\t"
  "s_mov_b32 s55, 3145728\n\t"
  "v_mfma_f32_16x16x32_bf16 v[48:51], a[88:91], v[76:79], v[48:51]\n\t"
  "ds_read_b64_tr_b16 v[70:71], v108 offset:38912\n\t"
  "buffer_load_dwordx4 a[28:31], v1, %[srd], s45 offen offset:3072 sc1\n\t"
  "v_mfma_f32_16x16x32_bf16 v[52:55], a[92:95], v[76:79], v[52:55]\n\t"
  "ds_read_b64_tr_b16 v[72:73], v109 offset:36864\n\t"
  "v_permlane16_swap_b32 v118, v115\n\t"
  "v_mfma_f32_16x16x32_bf16 v[56:59], a[84:87], v[80:83], v[56:59]\n\t"
  "ds_read_b64_tr_b16 v[74:75], v109 offset:38912\n\t"
  "v_permlane32_swap_b32 v117, v115\n\t"
  "v_mfma_f32_16x16x32_bf16 v[60:63], a[88:91], v[80:83], v[60:63]\n\t"
  "buffer_store_dwordx4 v[112:115], v8, %[srd], s55 offen sc1\n\t"
  "v_max_f32 v32, 0, v32\n\t"
  "v_mfma_f32_16x16x32_bf16 v[64:67], a[92:95], v[80:83], v[64:67]\n\t"
  "s_waitcnt lgkmcnt(4)\n\t"
  "s_add_u32 s45, %[sW2], 55296\n\t"
  "v_mfma_f32_16x16x32_bf16 v[44:47], a[96:99], v[84:87], v[44:47]\n\t"
  "ds_read_b64_tr_b16 v[76:77], v110 offset:0\n\t"
  "v_max_f32 v33, 0, v33\n\t"
  "v_mfma_f32_16x16x32_bf16 v[48:51], a[100:103], v[84:87], v[48:51]\n\t"
  "ds_read_b64_tr_b16 v[78:79], v110 offset:2048\n\t"
  "v_mul_f32 v34, v34, v129\n\t"
  "v_mfma_f32_16x16x32_bf16 v[52:55], a[104:107], v[84:87], v[52:55]\n\t"
  "ds_read_b64_tr_b16 v[80:81], v111 offset:0\n\t"
  "v_mul_f32 v35, v35, v129\n\t"
  "v_mfma_f32_16x16x32_bf16 v[56:59], a[96:99], v[88:91], v[56:59]\n\t"
  "ds_read_b64_tr_b16 v[82:83], v111 offset:2048\n\t"
  "buffer_load_dwordx4 a[32:35], v1, %[srd], s45 offen offset:0 sc1\n\t"
  "v_mfma_f32_16x16x32_bf16 v[60:63], a[100:103], v[88:91], v[60:63]\n\t"
  "v_mul_f32 v32, v32, v34\n\t"
  "v_mul_f32 v33, v33, v35\n\t"
  "v_mfma_f32_16x16x32_bf16 v[64:67], a[104:107], v[88:91], v[64:67]\n\t"
  "s_waitcnt lgkmcnt(4)\n\t"
  "v_cvt_pk_bf16_f32 v124, v32, v33\n\t"
  "v_mfma_f32_16x16x32_bf16 v[44:47], a[108:111], v[68:71], v[44:47]\n\t"
  "ds_read_b64_tr_b16 v[84:85], v110 offset:4096\n\t"
  "v_cvt_pk_bf16_f32 v120, v32, v33\n\t"
  "v_mfma_f32_16x16x32_bf16 v[48:51], a[112:115], v[68:71], v[48:51]\n\t"
  "ds_read_b64_tr_b16 v[86:87], v110 offset:6144\n\t"
  "buffer_load_dwordx4 a[36:39], v1, %[srd], s45 offen offset:1024 sc1\n\t"
  "v_mfma_f32_16x16x32_bf16 v[52:55], a[116:119], v[68:71], v[52:55]\n\t"
  "ds_read_b64_tr_b16 v[88:89], v111 offset:4096\n\t"
  "v_max_f32 v36, 0, v36\n\t"
  "v_mfma_f32_16x16x32_bf16 v[56:59], a[108:111], v[72:75], v[56:59]\n\t"
  "ds_read_b64_tr_b16 v[90:91], v111 offset:6144\n\t"
  "v_max_f32 v37, 0, v37\n\t"
  "v_mfma_f32_16x16x32_bf16 v[60:63], a[112:115], v[72:75], v[60:63]\n\t"
  "v_mul_f32 v38, v38, v129\n\t"
  "v_mul_f32 v39, v39, v129\n\t"
  "v_mfma_f32_16x16x32_bf16 v[64:67], a[116:119], v[72:75], v[64:67]\n\t"
  "s_waitcnt lgkmcnt(4)\n\t"
  "buffer_load_dwordx4 a[40:43], v1, %[srd], s45 offen offset:2048 sc1\n\t"
  "v_mfma_f32_16x16x32_bf16 v[44:47], a[120:123], v[76:79], v[44:47]\n\t"
  "ds_read_b64_tr_b16 v[68:69], v110 offset:8192\n\t"
  "v_mul_f32 v36, v36, v38\n\t"
  "v_mfma_f32_16x16x32_bf16 v[48:51], a[124:127], v[76:79], v[48:51]\n\t"
  "ds_read_b64_tr_b16 v[70:71], v110 offset:10240\n\t"
  "v_mul_f32 v37, v37, v39\n\t"
  "v_mfma_f32_16x16x32_bf16 v[52:55], a[128:131], v[76:79], v[52:55]\n\t"
  "ds_read_b64_tr_b16 v[72:73], v111 offset:8192\n\t"
  "v_cvt_pk_bf16_f32 v125, v36, v37\n\t"
  "v_mfma_f32_16x16x32_bf16 v[56:59], a[120:123], v[80:83], v[56:59]\n\t"
  "ds_read_b64_tr_b16 v[74:75], v111 offset:10240\n\t"
  "buffer_load_dwordx4 a[44:47], v1, %[srd], s45 offen offset:3072 sc1\n\t"
  "v_mfma_f32_16x16x32_bf16 v[60:63], a[124:127], v[80:83], v[60:63]\n\t"
  "v_cvt_pk_bf16_f32 v121, v36, v37\n\t"
  "v_max_f32 v40, 0, v40\n\t"
  "v_mfma_f32_16x16x32_bf16 v[64:67], a[128:131], v[80:83], v[64:67]\n\t"
  "s_waitcnt lgkmcnt(4)\n\t"
  "v_max_f32 v41, 0, v41\n\t"
  "v_mfma_f32_16x16x32_bf16 v[44:47], a[132:135], v[84:87], v[44:47]\n\t"
  "ds_read_b64_tr_b16 v[76:77], v110 offset:12288\n\t"
  "v_mul_f32 v42, v42, v129\n\t"
  "v_mfma_f32_16x16x32_bf16 v[48:51], a[136:139], v[84:87], v[48:51]\n\t"
  "ds_read_b64_tr_b16 v[78:79], v110 offset:14336\n\t"
  "s_add_u32 s45, %[sW2], 59392\n\t"
  "v_mfma_f32_16x16x32_bf16 v[52:55], a[140:143], v[84:87], v[52:55]\n\t"
  "ds_read_b64_tr_b16 v[80:81], v111 offset:12288\n\t"
  "v_mul_f32 v43, v43, v129\n\t"
  "v_mfma_f32_16x16x32_bf16 v[56:59], a[132:135], v[88:91], v[56:59]\n\t"
  "ds_read_b64_tr_b16 v[82:83], v111 offset:14336\n\t"
  "v_mul_f32 v40, v40, v42\n\t"
  "v_mfma_f32_16x16x32_bf16 v[60:63], a[136:139], v[88:91], v[60:63]\n\t"
  "v_permlane16_swap_b32 v124, v125\n\t"
  "v_mul_f32 v41, v41, v43\n\t"
  "v_mfma_f32_16x16x32_bf16 v[64:67], a[140:143], v[88:91], v[64:67]\n\t"
  "s_waitcnt lgkmcnt(4)\n\t"
  "buffer_load_dwordx4 a[48:51], v1, %[srd], s45 offen offset:0 sc1\n\t"
  "v_mfma_f32_16x16x32_bf16 v[44:47], a[144:147], v[68:71], v[44:47]\n\t"
  "ds_read_b64_tr_b16 v[84:85], v110 offset:16384\n\t"
  "v_cvt_pk_bf16_f32 v126, v40, v41\n\t"
  "v_mfma_f32_16x16x32_bf16 v[48:51], a[148:151], v[68:71], v[48:51]\n\t"
  "ds_read_b64_tr_b16 v[86:87], v110 offset:18432\n\t"
  "v_cvt_pk_bf16_f32 v122, v40, v41\n\t"
  "v_mfma_f32_16x16x32_bf16 v[52:55], a[152:155], v[68:71], v[52:55]\n\t"
  "ds_read_b64_tr_b16 v[88:89], v111 offset:16384\n\t"
  "s_mov_b32 s55, 3194880\n\t"
  "v_mfma_f32_16x16x32_bf16 v[56:59], a[144:147], v[72:75], v[56:59]\n\t"
  "ds_read_b64_tr_b16 v[90:91], v111 offset:18432\n\t"
  "buffer_load_dwordx4 a[52:55], v1, %[srd], s45 offen offset:1024 sc1\n\t"
  "v_mfma_f32_16x16x32_bf16 v[60:63], a[148:151], v[72:75], v[60:63]\n\t"
  "v_permlane16_swap_b32 v126, v123\n\t"
  "v_mfma_f32_16x16x32_bf16 v[64:67], a[152:155], v[72:75], v[64:67]\n\t"
  "s_waitcnt lgkmcnt(4)\n\t"
  "v_permlane32_swap_b32 v125, v123\n\t"
  "v_mfma_f32_16x16x32_bf16 v[44:47], a[156:159], v[76:79], v[44:47]\n\t"
  "ds_read_b64_tr_b16 v[68:69], v110 offset:20480\n\t"
  "buffer_store_dwordx4 v[120:123], v8, %[srd], s55 offen sc1\n\t"
  "v_mfma_f32_16x16x32_bf16 v[48:51], a[160:163], v[76:79], v[48:51]\n\t"
  "ds_read_b64_tr_b16 v[70:71], v110 offset:22528\n\t"
  "v_and_b32 v128, 0xffff0000, v105\n\t"
  "v_mfma_f32_16x16x32_bf16 v[52:55], a[164:167], v[76:79], v[52:55]\n\t"
  "ds_read_b64_tr_b16 v[72:73], v111 offset:20480\n\t"
  "v_and_b32 v129, 0xffff0000, v107\n\t"
  "v_mfma_f32_16x16x32_bf16 v[56:59], a[156:159], v[80:83], v[56:59]\n\t"
  "ds_read_b64_tr_b16 v[74:75], v111 offset:22528\n\t"
  "s_waitcnt vmcnt(12)\n\t"
  "v_cmp_ne_u64 vcc, v[132:133], v[138:139]\n\t"
  "v_mfma_f32_16x16x32_bf16 v[60:63], a[160:163], v[80:83], v[60:63]\n\t"
  "v_rcp_f32 v128, v128\n\t"
  "v_rcp_f32 v129, v129\n\t"
  "v_mfma_f32_16x16x32_bf16 v[64:67], a[164:167], v[80:83], v[64:67]\n\t"
  "s_waitcnt lgkmcnt(4)\n\t"
  "s_cbranch_vccnz Lfl0_slow%=\n\t"
  "Lfl0_back%=:\n\t"
  "v_mfma_f32_16x16x32_bf16 v[44:47], a[168:171], v[84:87], v[44:47]\n\t"
  "ds_read_b64_tr_b16 v[76:77], v110 offset:24576\n\t"
  "s_add_u32 s44, %[sH2ld], 0\n\t"
  "v_mfma_f32_16x16x32_bf16 v[48:51], a[172:175], v[84:87], v[48:51]\n\t"
  "ds_read_b64_tr_b16 v[78:79], v110 offset:26624\n\t"
  "v_mul_f32 v128, v128, v128\n\t"
  "v_mfma_f32_16x16x32_bf16 v[52:55], a[176:179], v[84:87], v[52:55]\n\t"
  "ds_read_b64_tr_b16 v[80:81], v111 offset:24576\n\t"
  "buffer_load_dwordx4 a[56:59], v1, %[srd], s44 offen offset:0 sc1\n\t"
  "v_mfma_f32_16x16x32_bf16 v[56:59], a[168:171], v[88:91], v[56:59]\n\t"
  "ds_read_b64_tr_b16 v[82:83], v111 offset:26624\n\t"
  "ds_read_b128 a[160:163], v141 offset:0\n\t"
  "v_mfma_f32_16x16x32_bf16 v[60:63], a[172:175], v[88:91], v[60:63]\n\t"
  "ds_read_b128 a[164:167], v141 offset:1024\n\t"
  "v_mul_f32 v129, v129, v129\n\t"
  "v_mfma_f32_16x16x32_bf16 v[64:67], a[176:179], v[88:91], v[64:67]\n\t"
  "s_waitcnt lgkmcnt(6)\n\t"
  "buffer_load_dwordx4 a[60:63], v1, %[srd], s44 offen offset:1024 sc1\n\t"
  "v_mfma_f32_16x16x32_bf16 v[44:47], a[180:183], v[68:71], v[44:47]\n\t"
  "ds_read_b64_tr_b16 v[84:85], v110 offset:28672\n\t"
  "ds_read_b128 a[168:171], v141 offset:2048\n\t"
  "v_mfma_f32_16x16x32_bf16 v[48:51], a[184:187], v[68:71], v[48:51]\n\t"
  "ds_read_b64_tr_b16 v[86:87], v110 offset:30720\n\t"
  "ds_read_b128 a[172:175], v141 offset:3072\n\t"
  "v_mfma_f32_16x16x32_bf16 v[52:55], a[188:191], v[68:71], v[52:55]\n\t"
  "ds_read_b64_tr_b16 v[88:89], v111 offset:28672\n\t"
  "buffer_load_dwordx4 a[64:67], v1, %[srd], s44 offen offset:2048 sc1\n\t"
  "v_mfma_f32_16x16x32_bf16 v[56:59], a[180:183], v[72:75], v[56:59]\n\t"
  "ds_read_b64_tr_b16 v[90:91], v111 offset:30720\n\t"
  "ds_read_b128 a[176:179], v141 offset:4096\n\t"
  "v_mfma_f32_16x16x32_bf16 v[60:63], a[184:187], v[72:75], v[60:63]\n\t"
  "v_mfma_f32_16x16x32_bf16 v[64:67], a[188:191], v[72:75], v[64:67]\n\t"
  "s_waitcnt lgkmcnt(9)\n\t"
  "buffer_load_dwordx4 a[68:71], v1, %[srd], s44 offen offset:3072 sc1\n\t"
  "v_mfma_f32_16x16x32_bf16 v[44:47], a[192:195], v[76:79], v[44:47]\n\t"
  "ds_read_b64_tr_b16 v[68:69], v110 offset:32768\n\t"
  "ds_read_b128 a[180:183], v141 offset:5120\n\t"
  "v_mfma_f32_16x16x32_bf16 v[48:51], a[196:199], v[76:79], v[48:51]\n\t"
  "ds_read_b64_tr_b16 v[70:71], v110 offset:34816\n\t"
  "ds_read_b128 a[184:187], v141 offset:6144\n\t"
  "v_mfma_f32_16x16x32_bf16 v[52:55], a[200:203], v[76:79], v[52:55]\n\t"
  "ds_read_b64_tr_b16 v[72:73], v111 offset:32768\n\t"
  "s_add_u32 s44, %[sH2ld], 4096\n\t"
  "v_mfma_f32_16x16x32_bf16 v[56:59], a[192:195], v[80:83], v[56:59]\n\t"
  "ds_read_b64_tr_b16 v[74:75], v111 offset:34816\n\t"
  "ds_read_b128 a[188:191], v141 offset:7168\n\t"
  "v_mfma_f32_16x16x32_bf16 v[60:63], a[196:199], v[80:83], v[60:63]\n\t"
  "buffer_load_dwordx4 a[72:75], v1, %[srd], s44 offen offset:0 sc1\n\t"
  "v_mfma_f32_16x16x32_bf16 v[64:67], a[200:203], v[80:83], v[64:67]\n\t"
  "s_waitcnt lgkmcnt(8)\n\t"
  "ds_read_b128 a[192:195], v141 offset:8192\n\t"
  "v_mfma_f32_16x16x32_bf16 v[44:47], a[204:207], v[84:87], v[44:47]\n\t"
  "ds_read_b64_tr_b16 v[76:77], v110 offset:36864\n\t"
  "ds_read_b128 a[196:199], v141 offset:9216\n\t"
  "v_mfma_f32_16x16x32_bf16 v[48:51], a[208:211], v[84:87], v[48:51]\n\t"
  "ds_read_b64_tr_b16 v[78:79], v110 offset:38912\n\t"
  "buffer_load_dwordx4 a[76:79], v1, %[srd], s44 offen offset:1024 sc1\n\t"
  "v_mfma_f32_16x16x32_bf16 v[52:55], a[212:215], v[84:87], v[52:55]\n\t"
  "ds_read_b64_tr_b16 v[80:81], v111 offset:36864\n\t"
  "ds_read_b128 a[200:203], v141 offset:10240\n\t"
  "v_mfma_f32_16x16x32_bf16 v[56:59], a[204:207], v[88:91], v[56:59]\n\t"
  "ds_read_b64_tr_b16 v[82:83], v111 offset:38912\n\t"
  "v_mfma_f32_16x16x32_bf16 v[60:63], a[208:211], v[88:91], v[60:63]\n\t"
  "buffer_load_dwordx4 a[80:83], v1, %[srd], s44 offen offset:2048 sc1\n\t"
  "v_mfma_f32_16x16x32_bf16 v[64:67], a[212:215], v[88:91], v[64:67]\n\t"
  "s_waitcnt lgkmcnt(8)\n\t"
  "ds_read_b128 a[204:207], v141 offset:11264\n\t"
  "v_mfma_f32_16x16x32_bf16 v[44:47], a[216:219], v[68:71], v[44:47]\n\t"
  "ds_read_b128 a[208:211], v141 offset:12288\n\t"
  "ds_read_b128 a[212:215], v141 offset:13312\n\t"
  "v_mfma_f32_16x16x32_bf16 v[48:51], a[220:223], v[68:71], v[48:51]\n\t"
  "buffer_load_dwordx4 a[84:87], v1, %[srd], s44 offen offset:3072 sc1\n\t"
  "v_mfma_f32_16x16x32_bf16 v[52:55], a[224:227], v[68:71], v[52:55]\n\t"
  "v_mfma_f32_16x16x32_bf16 v[56:59], a[216:219], v[72:75], v[56:59]\n\t"
  "v_mfma_f32_16x16x32_bf16 v[60:63], a[220:223], v[72:75], v[60:63]\n\t"
  "s_add_u32 s44, %[sH2ld], 8192\n\t"
  "v_mfma_f32_16x16x32_bf16 v[64:67], a[224:227], v[72:75], v[64:67]\n\t"
  "s_waitcnt lgkmcnt(3)\n\t"
  "ds_read_b128 a[216:219], v141 offset:14336\n\t"
  "v_mfma_f32_16x16x32_bf16 v[44:47], a[228:231], v[76:79], v[44:47]\n\t"
  "s_add_u32 s57, %[sFL], 8192\n\t"
  "buffer_load_dwordx2 v[138:139], v137, %[srd], s57 offen sc1\n\t"
  "v_mfma_f32_16x16x32_bf16 v[48:51], a[232:235], v[76:79], v[48:51]\n\t"
  "buffer_load_dwordx4 a[88:91], v1, %[srd], s44 offen offset:0 sc1\n\t"
  "ds_read_b128 a[220:223], v141 offset:15360\n\t"
  "v_mfma_f32_16x16x32_bf16 v[52:55], a[236:239], v[76:79], v[52:55]\n\t"
  "ds_read_b128 a[224:227], v141 offset:16384\n\t"
  "v_mfma_f32_16x16x32_bf16 v[56:59], a[228:231], v[80:83], v[56:59]\n\t"
  "buffer_load_dwordx4 a[92:95], v1, %[srd], s44 offen offset:1024 sc1\n\t"
  "v_mfma_f32_16x16x32_bf16 v[60:63], a[232:235], v[80:83], v[60:63]\n\t"
  "v_mfma_f32_16x16x32_bf16 v[64:67], a[236:239], v[80:83], v[64:67]\n\t"
  "ds_read_b128 a[228:231], v141 offset:17408\n\t"
  "ds_read_b128 a[232:235], v141 offset:18432\n\t"
  "v_mfma_f32_16x16x16_bf16 v[44:47], a[240:241], v[104:105], v[44:47]\n\t"
  "buffer_load_dwordx4 a[96:99], v1, %[srd], s44 offen offset:2048 sc1\n\t"
  "ds_read_b128 a[236:239], v141 offset:19456\n\t"
  "v_mfma_f32_16x16x16_bf16 v[48:51], a[242:243], v[104:105], v[48:51]\n\t"
  "v_mfma_f32_16x16x16_bf16 v[52:55], a[244:245], v[104:105], v[52:55]\n\t"
  "v_mfma_f32_16x16x16_bf16 v[56:59], a[240:241], v[106:107], v[56:59]\n\t"
  "buffer_load_dwordx4 a[100:103], v1, %[srd], s44 offen offset:3072 sc1\n\t"
  "v_mfma_f32_16x16x16_bf16 v[60:63], a[242:243], v[106:107], v[60:63]\n\t"
  "v_mfma_f32_16x16x16_bf16 v[64:67], a[244:245], v[106:107], v[64:67]\n\t"
  "s_movk_i32 s56, 16384\n\t"
  "s_waitcnt vmcnt(13)\n\t"
  "buffer_store_dwordx4 v[132:135], v9, %[srd], s56 offen sc1\n\t"
  "v_max_f32 v44, 0, v44\n\t"
  "v_max_f32 v45, 0, v45\n\t"
  "v_pk_mul_f32 v[46:47], v[46:47], v[128:129] op_sel:[0,0] op_sel_hi:[1,0]\n\t"
  "v_pk_mul_f32 v[44:45], v[44:45], v[46:47]\n\t"
  "v_cvt_pk_bf16_f32 v116, v44, v45\n\t"
  "v_cvt_pk_bf16_f32 v112, v44, v45\n\t"
  "v_max_f32 v48, 0, v48\n\t"
  "v_max_f32 v49, 0, v49\n\t"
  "v_pk_mul_f32 v[50:51], v[50:51], v[128:129] op_sel:[0,0] op_sel_hi:[1,0]\n\t"
  "v_pk_mul_f32 v[48:49], v[48:49], v[50:51]\n\t"
  "v_cvt_pk_bf16_f32 v117, v48, v49\n\t"
  "v_cvt_pk_bf16_f32 v113, v48, v49\n\t"
  "v_max_f32 v52, 0, v52\n\t"
  "v_max_f32 v53, 0, v53\n\t"
  "v_pk_mul_f32 v[54:55], v[54:55], v[128:129] op_sel:[0,0] op_sel_hi:[1,0]\n\t"
  "v_permlane16_swap_b32 v116, v117\n\t"
  "v_pk_mul_f32 v[52:53], v[52:53], v[54:55]\n\t"
  "v_cvt_pk_bf16_f32 v118, v52, v53\n\t"
  "v_cvt_pk_bf16_f32 v114, v52, v53\n\t"
  "s_mov_b32 s55, 3244032\n\t"
  "v_permlane16_swap_b32 v118, v115\n\t"
  "s_nop 1\n\t"
  "v_permlane32_swap_b32 v117, v115\n\t"
  "buffer_store_dwordx4 v[112:115], v8, %[srd], s55 offen sc1\n\t"
  "v_max_f32 v56, 0, v56\n\t"
  "v_max_f32 v57, 0, v57\n\t"
  "v_pk_mul_f32 v[58:59], v[58:59], v[128:129] op_sel:[0,1] op_sel_hi:[1,1]\n\t"
  "v_pk_mul_f32 v[56:57], v[56:57], v[58:59]\n\t"
  "v_cvt_pk_bf16_f32 v124, v56, v57\n\t"
  "v_cvt_pk_bf16_f32 v120, v56, v57\n\t"
  "v_max_f32 v60, 0, v60\n\t"
  "v_max_f32 v61, 0, v61\n\t"
  "v_pk_mul_f32 v[62:63], v[62:63], v[128:129] op_sel:[0,1] op_sel_hi:[1,1]\n\t"
  "v_pk_mul_f32 v[60:61], v[60:61], v[62:63]\n\t"
  "v_cvt_pk_bf16_f32 v125, v60, v61\n\t"
  "v_cvt_pk_bf16_f32 v121, v60, v61\n\t"
  "v_max_f32 v64, 0, v64\n\t"
  "v_max_f32 v65, 0, v65\n\t"
  "v_pk_mul_f32 v[66:67], v[66:67], v[128:129] op_sel:[0,1] op_sel_hi:[1,1]\n\t"
  "v_permlane16_swap_b32 v124, v125\n\t"
  "v_pk_mul_f32 v[64:65], v[64:65], v[66:67]\n\t"
  "v_cvt_pk_bf16_f32 v126, v64, v65\n\t"
  "v_cvt_pk_bf16_f32 v122, v64, v65\n\t"
  "s_mov_b32 s55, 3293184\n\t"
  "v_permlane16_swap_b32 v126, v123\n\t"
  "s_nop 1\n\t"
  "v_permlane32_swap_b32 v125, v123\n\t"
  "buffer_store_dwordx4 v[120:123], v8, %[srd], s55 offen sc1\n\t"
  "s_waitcnt lgkmcnt(0)\n\t"
  "s_barrier\n\t"
  "s_waitcnt vmcnt(15)\n\t"
  "v_mfma_f32_16x16x32_bf16 v[20:23], v[144:147], a[56:59], 0\n\t"
  "v_lshrrev_b32 v63, 3, v1\n\t"
  "v_and_b32 v63, 0x60, v63\n\t"
  "v_mfma_f32_16x16x32_bf16 v[24:27], v[148:151], a[56:59], 0\n\t"
  "v_lshrrev_b32 v62, 1, v63\n\t"
  "buffer_load_dwordx4 v[68:71], v63, %[srd], %[sRT] offen offset:0 sc1\n\t"
  "v_mfma_f32_16x16x32_bf16 v[28:31], v[152:155], a[56:59], 0\n\t"
  "buffer_load_dwordx4 v[72:75], v63, %[srd], %[sRT] offen offset:16 sc1\n\t"
  "buffer_load_dwordx4 v[76:79], v63, %[srd], %[sRT] offen offset:128 sc1\n\t"
  "v_mfma_f32_16x16x32_bf16 v[32:35], v[156:159], a[56:59], 0\n\t"
  "buffer_load_dwordx4 v[80:83], v63, %[srd], %[sRT] offen offset:144 sc1\n\t"
  "buffer_load_dwordx4 v[84:87], v62, %[srd], %[sRT] offen offset:256 sc1\n\t"
  "v_mfma_f32_16x16x32_bf16 v[36:39], v[160:163], a[56:59], 0\n\t"
  "buffer_load_dwordx4 v[64:67], v17, %[srd], %[sRT] offen offset:0 sc1\n\t"
  "buffer_load_dwordx4 v[108:111], v17, %[srd], %[sRT] offen offset:16 sc1\n\t"
  "s_waitcnt vmcnt(21)\n\t"
  "v_mfma_f32_16x16x32_bf16 v[20:23], v[164:167], a[60:63], v[20:23]\n\t"
  "s_mul_i32 s48, %[w], -15360\n\t"
  "s_add_u32 s48, s48, 81920\n\t"
  "v_mfma_f32_16x16x32_bf16 v[24:27], v[168:171], a[60:63], v[24:27]\n\t"
  "v_add_u32 v62, s48, v13\n\t"
  "v_add_u32 v60, 61440, v14\n\t"
  "v_mfma_f32_16x16x32_bf16 v[28:31], v[172:175], a[60:63], v[28:31]\n\t"
  "v_add_u32 v61, 61440, v15\n\t"
  "v_accvgpr_read_b32 v89, a246\n\t"
  "v_mfma_f32_16x16x32_bf16 v[32:35], v[176:179], a[60:63], v[32:35]\n\t"
  "s_waitcnt vmcnt(14)\n\t"
  "v_cmp_ne_u64 vcc, v[132:133], v[138:139]\n\t"
  "v_mfma_f32_16x16x32_bf16 v[36:39], v[180:183], a[60:63], v[36:39]\n\t"
  "v_lshlrev_b32 v88, 16, v89\n\t"
  "v_and_b32 v89, 0xffff0000, v89\n\t"
  "v_mfma_f32_16x16x32_bf16 v[20:23], v[184:187], a[64:67], v[20:23]\n\t"
  "s_cbranch_vccnz Lfl1_slow%=\n\t"
  "Lfl1_back%=:\n\t"
  "v_mfma_f32_16x16x32_bf16 v[24:27], v[188:191], a[64:67], v[24:27]\n\t"
  "v_accvgpr_read_b32 v91, a247\n\t"
  "v_lshlrev_b32 v90, 16, v91\n\t"
  "v_mfma_f32_16x16x32_bf16 v[28:31], v[192:195], a[64:67], v[28:31]\n\t"
  "s_add_u32 s45, %[sH2ld], 98304\n\t"
  "v_and_b32 v91, 0xffff0000, v91\n\t"
  "v_mfma_f32_16x16x32_bf16 v[32:35], v[196:199], a[64:67], v[32:35]\n\t"
  "v_accvgpr_read_b32 v93, a248\n\t"
  "v_lshlrev_b32 v92, 16, v93\n\t"
  "v_mfma_f32_16x16x32_bf16 v[36:39], v[200:203], a[64:67], v[36:39]\n\t"
  "v_and_b32 v93, 0xffff0000, v93\n\t"
  "v_accvgpr_read_b32 v95, a249\n\t"
  "v_mfma_f32_16x16x32_bf16 v[20:23], v[204:207], a[68:71], v[20:23]\n\t"
  "buffer_load_dwordx4 a[104:107], v1, %[srd], s45 offen offset:0 sc1\n\t"
  "v_lshlrev_b32 v94, 16, v95\n\t"
  "v_mfma_f32_16x16x32_bf16 v[24:27], v[208:211], a[68:71], v[24:27]\n\t"
  "v_and_b32 v95, 0xffff0000, v95\n\t"
  "v_accvgpr_read_b32 v97, a250\n\t"
  "v_mfma_f32_16x16x32_bf16 v[28:31], v[212:215], a[68:71], v[28:31]\n\t"
  "v_lshlrev_b32 v96, 16, v97\n\t"
  "v_and_b32 v97, 0xffff0000, v97\n\t"
  "v_mfma_f32_16x16x32_bf16 v[32:35], v[216:219], a[68:71], v[32:35]\n\t"
  "buffer_load_dwordx4 a[108:111], v1, %[srd], s45 offen offset:1024 sc1\n\t"
  "v_accvgpr_read_b32 v99, a251\n\t"
  "v_mfma_f32_16x16x32_bf16 v[36:39], v[220:223], a[68:71], v[36:39]\n\t"
  "v_lshlrev_b32 v98, 16, v99\n\t"
  "v_and_b32 v99, 0xffff0000, v99\n\t"
  "v_mfma_f32_16x16x32_bf16 v[20:23], v[224:227], a[72:75], v[20:23]\n\t"
  "v_accvgpr_read_b32 v101, a252\n\t"
  "v_lshlrev_b32 v100, 16, v101\n\t"
  "v_mfma_f32_16x16x32_bf16 v[24:27], v[228:231], a[72:75], v[24:27]\n\t"
  "buffer_load_dwordx4 a[112:115], v1, %[srd], s45 offen offset:2048 sc1\n\t"
  "v_and_b32 v101, 0xffff0000, v101\n\t"
  "v_mfma_f32_16x16x32_bf16 v[28:31], v[232:235], a[72:75], v[28:31]\n\t"
  "v_accvgpr_read_b32 v103, a253\n\t"
  "v_lshlrev_b32 v102, 16, v103\n\t"
  "v_mfma_f32_16x16x32_bf16 v[32:35], v[236:239], a[72:75], v[32:35]\n\t"
  "v_and_b32 v103, 0xffff0000, v103\n\t"
  "v_accvgpr_read_b32 v105, a254\n\t"
  "v_mfma_f32_16x16x32_bf16 v[36:39], v[240:243], a[72:75], v[36:39]\n\t"
  "buffer_load_dwordx4 a[116:119], v1, %[srd], s45 offen offset:3072 sc1\n\t"
  "s_add_u32 s57, %[sFL], 16384\n\t"
  "v_mfma_f32_16x16x32_bf16 v[20:23], v[244:247], a[76:79], v[20:23]\n\t"
  "buffer_load_dwordx2 v[138:139], v137, %[srd], s57 offen sc1\n\t"
  "v_lshlrev_b32 v104, 16, v105\n\t"
  "v_mfma_f32_16x16x32_bf16 v[24:27], a[160:163], a[76:79], v[24:27]\n\t"
  "s_movk_i32 s56, 24576\n\t"
  "s_waitcnt vmcnt(12)\n\t"
  "v_mfma_f32_16x16x32_bf16 v[28:31], a[164:167], a[76:79], v[28:31]\n\t"
  "buffer_store_dwordx4 v[132:135], v9, %[srd], s56 offen sc1\n\t"
  "v_and_b32 v105, 0xffff0000, v105\n\t"
  "v_mfma_f32_16x16x32_bf16 v[32:35], a[168:171], a[76:79], v[32:35]\n\t"
  "s_add_u32 s45, %[sH2ld], 102400\n\t"
  "v_accvgpr_read_b32 v107, a255\n\t"
  "v_mfma_f32_16x16x32_bf16 v[36:39], a[172:175], a[76:79], v[36:39]\n\t"
  "v_lshlrev_b32 v106, 16, v107\n\t"
  "v_and_b32 v107, 0xffff0000, v107\n\t"
  "v_mfma_f32_16x16x32_bf16 v[20:23], a[176:179], a[80:83], v[20:23]\n\t"
  "v_mfma_f32_16x16x32_bf16 v[24:27], a[180:183], a[80:83], v[24:27]\n\t"
  "buffer_load_dwordx4 a[120:123], v1, %[srd], s45 offen offset:0 sc1\n\t"
  "v_mfma_f32_16x16x32_bf16 v[28:31], a[184:187], a[80:83], v[28:31]\n\t"
  "v_mfma_f32_16x16x32_bf16 v[32:35], a[188:191], a[80:83], v[32:35]\n\t"
  "v_mfma_f32_16x16x32_bf16 v[36:39], a[192:195], a[80:83], v[36:39]\n\t"
  "buffer_load_dwordx4 a[124:127], v1, %[srd], s45 offen offset:1024 sc1\n\t"
  "v_mfma_f32_16x16x32_bf16 v[20:23], a[196:199], a[84:87], v[20:23]\n\t"
  "v_mfma_f32_16x16x32_bf16 v[24:27], a[200:203], a[84:87], v[24:27]\n\t"
  "v_mfma_f32_16x16x32_bf16 v[28:31], a[204:207], a[84:87], v[28:31]\n\t"
  "buffer_load_dwordx4 a[128:131], v1, %[srd], s45 offen offset:2048 sc1\n\t"
  "v_mfma_f32_16x16x32_bf16 v[32:35], a[208:211], a[84:87], v[32:35]\n\t"
  "v_mfma_f32_16x16x32_bf16 v[36:39], a[212:215], a[84:87], v[36:39]\n\t"
  "v_mfma_f32_16x16x32_bf16 v[20:23], a[216:219], a[88:91], v[20:23]\n\t"
  "buffer_load_dwordx4 a[132:135], v1, %[srd], s45 offen offset:3072 sc1\n\t"
  "v_mfma_f32_16x16x32_bf16 v[24:27], a[220:223], a[88:91], v[24:27]\n\t"
  "v_mfma_f32_16x16x32_bf16 v[28:31], a[224:227], a[88:91], v[28:31]\n\t"
  "v_mfma_f32_16x16x32_bf16 v[32:35], a[228:231], a[88:91], v[32:35]\n\t"
  "v_mfma_f32_16x16x32_bf16 v[36:39], a[232:235], a[88:91], v[36:39]\n\t"
  "s_add_u32 s45, %[sH2ld], 106496\n\t"
  "v_mfma_f32_16x16x32_bf16 v[20:23], a[236:239], a[92:95], v[20:23]\n\t"
  "s_waitcnt vmcnt(16)\n\t"
  "v_mul_f32 v88, v88, v68\n\t"
  "v_mfma_f32_16x16x32_bf16 v[24:27], a[0:3], a[92:95], v[24:27]\n\t"
  "v_mul_f32 v89, v89, v69\n\t"
  "v_mul_f32 v90, v90, v70\n\t"
  "v_mfma_f32_16x16x32_bf16 v[28:31], a[4:7], a[92:95], v[28:31]\n\t"
  "buffer_load_dwordx4 a[136:139], v1, %[srd], s45 offen offset:0 sc1\n\t"
  "v_mul_f32 v91, v91, v71\n\t"
  "v_mfma_f32_16x16x32_bf16 v[32:35], a[8:11], a[92:95], v[32:35]\n\t"
  "ds_write_b128 v62, v[88:91] offset:0\n\t"
  "s_waitcnt vmcnt(16)\n\t"
  "v_mul_f32 v92, v92, v72\n\t"
  "v_mfma_f32_16x16x32_bf16 v[36:39], a[12:15], a[92:95], v[36:39]\n\t"
  "v_mul_f32 v93, v93, v73\n\t"
  "v_mul_f32 v94, v94, v74\n\t"
  "v_mfma_f32_16x16x32_bf16 v[20:23], a[16:19], a[96:99], v[20:23]\n\t"
  "buffer_load_dwordx4 a[140:143], v1, %[srd], s45 offen offset:1024 sc1\n\t"
  "v_mul_f32 v95, v95, v75\n\t"
  "v_mfma_f32_16x16x32_bf16 v[24:27], a[20:23], a[96:99], v[24:27]\n\t"
  "ds_write_b128 v62, v[92:95] offset:1024\n\t"
  "s_waitcnt vmcnt(16)\n\t"
  "v_mul_f32 v96, v96, v76\n\t"
  "v_mfma_f32_16x16x32_bf16 v[28:31], a[24:27], a[96:99], v[28:31]\n\t"
  "v_mul_f32 v97, v97, v77\n\t"
  "v_mul_f32 v98, v98, v78\n\t"
  "v_mfma_f32_16x16x32_bf16 v[32:35], a[28:31], a[96:99], v[32:35]\n\t"
  "buffer_load_dwordx4 a[144:147], v1, %[srd], s45 offen offset:2048 sc1\n\t"
  "v_mul_f32 v99, v99, v79\n\t"
  "v_mfma_f32_16x16x32_bf16 v[36:39], a[32:35], a[96:99], v[36:39]\n\t"
  "ds_write_b128 v62, v[96:99] offset:2048\n\t"
  "s_waitcnt vmcnt(16)\n\t"
  "v_mul_f32 v100, v100, v80\n\t"
  "v_mfma_f32_16x16x32_bf16 v[20:23], a[36:39], a[100:103], v[20:23]\n\t"
  "v_mul_f32 v101, v101, v81\n\t"
  "v_mul_f32 v102, v102, v82\n\t"
  "v_mfma_f32_16x16x32_bf16 v[24:27], a[40:43], a[100:103], v[24:27]\n\t"
  "buffer_load_dwordx4 a[148:151], v1, %[srd], s45 offen offset:3072 sc1\n\t"
  "v_mul_f32 v103, v103, v83\n\t"
  "v_mfma_f32_16x16x32_bf16 v[28:31], a[44:47], a[100:103], v[28:31]\n\t"
  "ds_write_b128 v62, v[100:103] offset:3072\n\t"
  "s_waitcnt vmcnt(16)\n\t"
  "v_mul_f32 v104, v104, v84\n\t"
  "v_mfma_f32_16x16x32_bf16 v[32:35], a[48:51], a[100:103], v[32:35]\n\t"
  "v_mul_f32 v105, v105, v85\n\t"
  "v_mul_f32 v106, v106, v86\n\t"
  "v_mfma_f32_16x16x32_bf16 v[36:39], a[52:55], a[100:103], v[36:39]\n\t"
  "s_waitcnt vmcnt(12)\n\t"
  "v_mul_f32 v107, v107, v87\n\t"
  "v_mfma_f32_16x16x32_bf16 v[40:43], v[144:147], a[104:107], 0\n\t"
  "ds_write_b128 v62, v[104:107] offset:4096\n\t"
  "v_mfma_f32_16x16x32_bf16 v[44:47], v[148:151], a[104:107], 0\n\t"
  "v_mfma_f32_16x16x32_bf16 v[48:51], v[152:155], a[104:107], 0\n\t"
  "v_mfma_f32_16x16x32_bf16 v[52:55], v[156:159], a[104:107], 0\n\t"
  "ds_write_b128 v13, v[20:23] offset:0\n\t"
  "v_mfma_f32_16x16x32_bf16 v[56:59], v[160:163], a[104:107], 0\n\t"
  "v_mfma_f32_16x16x32_bf16 v[40:43], v[164:167], a[108:111], v[40:43]\n\t"
  "v_mfma_f32_16x16x32_bf16 v[44:47], v[168:171], a[108:111], v[44:47]\n\t"
  "ds_write_b128 v13, v[24:27] offset:1024\n\t"
  "v_mfma_f32_16x16x32_bf16 v[48:51], v[172:175], a[108:111], v[48:51]\n\t"
  "v_mfma_f32_16x16x32_bf16 v[52:55], v[176:179], a[108:111], v[52:55]\n\t"
  "v_mfma_f32_16x16x32_bf16 v[56:59], v[180:183], a[108:111], v[56:59]\n\t"
  "s_waitcnt vmcnt(10)\n\t"
  "ds_write_b128 v13, v[28:31] offset:2048\n\t"
  "v_mfma_f32_16x16x32_bf16 v[40:43], v[184:187], a[112:115], v[40:43]\n\t"
  "v_mfma_f32_16x16x32_bf16 v[44:47], v[188:191], a[112:115], v[44:47]\n\t"
  "v_mfma_f32_16x16x32_bf16 v[48:51], v[192:195], a[112:115], v[48:51]\n\t"
  "ds_write_b128 v13, v[32:35] offset:3072\n\t"
  "v_mfma_f32_16x16x32_bf16 v[52:55], v[196:199], a[112:115], v[52:55]\n\t"
  "v_mfma_f32_16x16x32_bf16 v[56:59], v[200:203], a[112:115], v[56:59]\n\t"
  "v_mfma_f32_16x16x32_bf16 v[40:43], v[204:207], a[116:119], v[40:43]\n\t"
  "s_waitcnt vmcnt(9)\n\t"
  "v_cmp_ne_u64 vcc, v[132:133], v[138:139]\n\t"
  "v_mfma_f32_16x16x32_bf16 v[44:47], v[208:211], a[116:119], v[44:47]\n\t"
  "ds_write_b128 v13, v[36:39] offset:4096\n\t"
  "v_mfma_f32_16x16x32_bf16 v[48:51], v[212:215], a[116:119], v[48:51]\n\t"
  "s_cbranch_vccnz Lfl2_slow%=\n\t"
  "Lfl2_back%=:\n\t"
  "v_mfma_f32_16x16x32_bf16 v[52:55], v[216:219], a[116:119], v[52:55]\n\t"
  "s_add_u32 s44, %[sH2ld], 3145728\n\t"
  "v_mfma_f32_16x16x32_bf16 v[56:59], v[220:223], a[116:119], v[56:59]\n\t"
  "s_waitcnt vmcnt(6)\n\t"
  "v_mfma_f32_16x16x32_bf16 v[40:43], v[224:227], a[120:123], v[40:43]\n\t"
  "v_mfma_f32_16x16x32_bf16 v[44:47], v[228:231], a[120:123], v[44:47]\n\t"
  "buffer_load_dwordx4 a[152:155], v1, %[srd], s44 offen offset:0 sc1\n\t"
  "v_mfma_f32_16x16x32_bf16 v[48:51], v[232:235], a[120:123], v[48:51]\n\t"
  "v_mfma_f32_16x16x32_bf16 v[52:55], v[236:239], a[120:123], v[52:55]\n\t"
  "v_mfma_f32_16x16x32_bf16 v[56:59], v[240:243], a[120:123], v[56:59]\n\t"
  "buffer_load_dwordx4 a[156:159], v1, %[srd], s44 offen offset:1024 sc1\n\t"
  "s_add_u32 s57, %[sFL], 24576\n\t"
  "v_mfma_f32_16x16x32_bf16 v[40:43], v[244:247], a[124:127], v[40:43]\n\t"
  "buffer_load_dwordx2 v[138:139], v137, %[srd], s57 offen sc1\n\t"
  "s_waitcnt lgkmcnt(0)\n\t"
  "s_barrier\n\t"
  "v_mfma_f32_16x16x32_bf16 v[44:47], a[160:163], a[124:127], v[44:47]\n\t"
  "ds_read_b128 v[68:71], v14 offset:0\n\t"
  "ds_read_b128 v[72:75], v15 offset:0\n\t"
  "v_mfma_f32_16x16x32_bf16 v[48:51], a[164:167], a[124:127], v[48:51]\n\t"
  "buffer_load_dwordx4 v[248:251], v1, %[srd], s44 offen offset:2048 sc1\n\t"
  "ds_read_b128 v[76:79], v14 offset:20480\n\t"
  "v_mfma_f32_16x16x32_bf16 v[52:55], a[168:171], a[124:127], v[52:55]\n\t"
  "ds_read_b128 v[80:83], v15 offset:20480\n\t"
  "ds_read_b128 v[84:87], v14 offset:40960\n\t"
  "v_mfma_f32_16x16x32_bf16 v[56:59], a[172:175], a[124:127], v[56:59]\n\t"
  "s_waitcnt vmcnt(8)\n\t"
  "ds_read_b128 v[88:91], v15 offset:40960\n\t"
  "v_mfma_f32_16x16x32_bf16 v[40:43], a[176:179], a[128:131], v[40:43]\n\t"
  "buffer_load_dwordx4 v[252:255], v1, %[srd], s44 offen offset:3072 sc1\n\t"
  "ds_read_b128 v[92:95], v60 offset:0\n\t"
  "v_mfma_f32_16x16x32_bf16 v[44:47], a[180:183], a[128:131], v[44:47]\n\t"
  "ds_read_b128 v[96:99], v61 offset:0\n\t"
  "ds_read_b128 v[100:103], v60 offset:20480\n\t"
  "v_mfma_f32_16x16x32_bf16 v[48:51], a[184:187], a[128:131], v[48:51]\n\t"
  "ds_read_b128 v[104:107], v61 offset:20480\n\t"
  "v_mfma_f32_16x16x32_bf16 v[52:55], a[188:191], a[128:131], v[52:55]\n\t"
  "v_mfma_f32_16x16x32_bf16 v[56:59], a[192:195], a[128:131], v[56:59]\n\t"
  "s_add_u32 s44, %[sH2ld], 3149824\n\t"
  "v_mfma_f32_16x16x32_bf16 v[40:43], a[196:199], a[132:135], v[40:43]\n\t"
  "v_mfma_f32_16x16x32_bf16 v[44:47], a[200:203], a[132:135], v[44:47]\n\t"
  "v_mfma_f32_16x16x32_bf16 v[48:51], a[204:207], a[132:135], v[48:51]\n\t"
  "buffer_load_dwordx4 v[112:115], v1, %[srd], s44 offen offset:0 sc1\n\t"
  "v_mfma_f32_16x16x32_bf16 v[52:55], a[208:211], a[132:135], v[52:55]\n\t"
  "v_mfma_f32_16x16x32_bf16 v[56:59], a[212:215], a[132:135], v[56:59]\n\t"
  "s_waitcnt vmcnt(8)\n\t"
  "v_mfma_f32_16x16x32_bf16 v[40:43], a[216:219], a[136:139], v[40:43]\n\t"
  "buffer_load_dwordx4 v[116:119], v1, %[srd], s44 offen offset:1024 sc1\n\t"
  "s_waitcnt lgkmcnt(0)\n\t"
  "v_mfma_f32_16x16x32_bf16 v[44:47], a[220:223], a[136:139], v[44:47]\n\t"
  "v_add_f32 v68, v68, v76\n\t"
  "v_add_f32 v69, v69, v77\n\t"
  "v_mfma_f32_16x16x32_bf16 v[48:51], a[224:227], a[136:139], v[48:51]\n\t"
  "v_add_f32 v70, v70, v78\n\t"
  "v_add_f32 v71, v71, v79\n\t"
  "v_mfma_f32_16x16x32_bf16 v[52:55], a[228:231], a[136:139], v[52:55]\n\t"
  "buffer_load_dwordx4 v[120:123], v1, %[srd], s44 offen offset:2048 sc1\n\t"
  "v_add_f32 v72, v72, v80\n\t"
  "v_mfma_f32_16x16x32_bf16 v[56:59], a[232:235], a[136:139], v[56:59]\n\t"
  "v_add_f32 v73, v73, v81\n\t"
  "v_add_f32 v74, v74, v82\n\t"
  "v_mfma_f32_16x16x32_bf16 v[40:43], a[236:239], a[140:143], v[40:43]\n\t"
  "v_add_f32 v75, v75, v83\n\t"
  "v_add_f32 v84, v84, v92\n\t"
  "v_mfma_f32_16x16x32_bf16 v[44:47], a[0:3], a[140:143], v[44:47]\n\t"
  "buffer_load_dwordx4 v[124:127], v1, %[srd], s44 offen offset:3072 sc1\n\t"
  "v_add_f32 v85, v85, v93\n\t"
  "v_mfma_f32_16x16x32_bf16 v[48:51], a[4:7], a[140:143], v[48:51]\n\t"
  "v_add_f32 v86, v86, v94\n\t"
  "v_add_f32 v87, v87, v95\n\t"
  "v_mfma_f32_16x16x32_bf16 v[52:55], a[8:11], a[140:143], v[52:55]\n\t"
  "v_add_f32 v88, v88, v96\n\t"
  "v_add_f32 v89, v89, v97\n\t"
  "v_mfma_f32_16x16x32_bf16 v[56:59], a[12:15], a[140:143], v[56:59]\n\t"
  "s_waitcnt vmcnt(9)\n\t"
  "v_add_f32 v90, v90, v98\n\t"
  "v_mfma_f32_16x16x32_bf16 v[40:43], a[16:19], a[144:147], v[40:43]\n\t"
  "s_add_u32 s44, %[sH2ld], 3153920\n\t"
  "v_add_f32 v91, v91, v99\n\t"
  "v_mfma_f32_16x16x32_bf16 v[44:47], a[20:23], a[144:147], v[44:47]\n\t"
  "v_add_f32 v68, v68, v84\n\t"
  "v_add_f32 v69, v69, v85\n\t"
  "v_mfma_f32_16x16x32_bf16 v[48:51], a[24:27], a[144:147], v[48:51]\n\t"
  "v_add_f32 v70, v70, v86\n\t"
  "v_add_f32 v71, v71, v87\n\t"
  "v_mfma_f32_16x16x32_bf16 v[52:55], a[28:31], a[144:147], v[52:55]\n\t"
  "buffer_load_dwordx4 v[128:131], v1, %[srd], s44 offen offset:0 sc1\n\t"
  "v_add_f32 v72, v72, v88\n\t"
  "v_mfma_f32_16x16x32_bf16 v[56:59], a[32:35], a[144:147], v[56:59]\n\t"
  "v_add_f32 v73, v73, v89\n\t"
  "v_add_f32 v74, v74, v90\n\t"
  "v_mfma_f32_16x16x32_bf16 v[40:43], a[36:39], a[148:151], v[40:43]\n\t"
  "v_add_f32 v75, v75, v91\n\t"
  "v_fma_f32 v68, v68, v64, v100\n\t"
  "v_mfma_f32_16x16x32_bf16 v[44:47], a[40:43], a[148:151], v[44:47]\n\t"
  "buffer_load_dwordx4 a[240:243], v1, %[srd], s44 offen offset:1024 sc1\n\t"
  "v_fma_f32 v69, v69, v65, v101\n\t"
  "v_mfma_f32_16x16x32_bf16 v[48:51], a[44:47], a[148:151], v[48:51]\n\t"
  "v_fma_f32 v70, v70, v66, v102\n\t"
  "v_fma_f32 v71, v71, v67, v103\n\t"
  "v_mfma_f32_16x16x32_bf16 v[52:55], a[48:51], a[148:151], v[52:55]\n\t"
  "v_fma_f32 v72, v72, v108, v104\n\t"
  "v_fma_f32 v73, v73, v109, v105\n\t"
  "v_mfma_f32_16x16x32_bf16 v[56:59], a[52:55], a[148:151], v[56:59]\n\t"
  "s_waitcnt vmcnt(9)\n\t"
  "buffer_load_dwordx4 a[56:59], v1, %[srd], s44 offen offset:2048 sc1\n\t"
  "v_mfma_f32_16x16x32_bf16 v[20:23], v[144:147], a[152:155], 0\n\t"
  "v_fma_f32 v74, v74, v110, v106\n\t"
  "v_fma_f32 v75, v75, v111, v107\n\t"
  "v_mfma_f32_16x16x32_bf16 v[24:27], v[148:151], a[152:155], 0\n\t"
  "v_cvt_pk_bf16_f32 v76, v68, v69\n\t"
  "v_cvt_pk_bf16_f32 v77, v70, v71\n\t"
  "v_mfma_f32_16x16x32_bf16 v[28:31], v[152:155], a[152:155], 0\n\t"
  "buffer_load_dwordx4 a[60:63], v1, %[srd], s44 offen offset:3072 sc1\n\t"
  "v_cvt_pk_bf16_f32 v78, v72, v73\n\t"
  "v_mfma_f32_16x16x32_bf16 v[32:35], v[156:159], a[152:155], 0\n\t"
  "v_cvt_pk_bf16_f32 v79, v74, v75\n\t"
  "s_mov_b32 s55, 0\n\t"
  "v_mfma_f32_16x16x32_bf16 v[36:39], v[160:163], a[152:155], 0\n\t"
  "buffer_store_dwordx4 v[76:79], v16, %[srdo], s55 offen\n\t"
  "ds_write_b128 v13, v[40:43] offset:5120\n\t"
  "v_mfma_f32_16x16x32_bf16 v[20:23], v[164:167], a[156:159], v[20:23]\n\t"
  "v_mfma_f32_16x16x32_bf16 v[24:27], v[168:171], a[156:159], v[24:27]\n\t"
  "v_mfma_f32_16x16x32_bf16 v[28:31], v[172:175], a[156:159], v[28:31]\n\t"
  "ds_write_b128 v13, v[44:47] offset:6144\n\t"
  "v_mfma_f32_16x16x32_bf16 v[32:35], v[176:179], a[156:159], v[32:35]\n\t"
  "v_mfma_f32_16x16x32_bf16 v[36:39], v[180:183], a[156:159], v[36:39]\n\t"
  "s_waitcnt vmcnt(9)\n\t"
  "v_mfma_f32_16x16x32_bf16 v[20:23], v[184:187], v[248:251], v[20:23]\n\t"
  "ds_write_b128 v13, v[48:51] offset:7168\n\t"
  "v_mfma_f32_16x16x32_bf16 v[24:27], v[188:191], v[248:251], v[24:27]\n\t"
  "v_mfma_f32_16x16x32_bf16 v[28:31], v[192:195], v[248:251], v[28:31]\n\t"
  "v_mfma_f32_16x16x32_bf16 v[32:35], v[196:199], v[248:251], v[32:35]\n\t"
  "ds_write_b128 v13, v[52:55] offset:8192\n\t"
  "v_mfma_f32_16x16x32_bf16 v[36:39], v[200:203], v[248:251], v[36:39]\n\t"
  "v_mfma_f32_16x16x32_bf16 v[20:23], v[204:207], v[252:255], v[20:23]\n\t"
  "v_cmp_ne_u64 vcc, v[132:133], v[138:139]\n\t"
  "v_mfma_f32_16x16x32_bf16 v[24:27], v[208:211], v[252:255], v[24:27]\n\t"
  "ds_write_b128 v13, v[56:59] offset:9216\n\t"
  "v_mfma_f32_16x16x32_bf16 v[28:31], v[212:215], v[252:255], v[28:31]\n\t"
  "s_cbranch_vccnz Lfl3_slow%=\n\t"
  "Lfl3_back%=:\n\t"
  "v_mfma_f32_16x16x32_bf16 v[32:35], v[216:219], v[252:255], v[32:35]\n\t"
  "s_add_u32 s45, %[sH2ld], 3244032\n\t"
  "v_mfma_f32_16x16x32_bf16 v[36:39], v[220:223], v[252:255], v[36:39]\n\t"
  "s_waitcnt vmcnt(7)\n\t"
  "v_mfma_f32_16x16x32_bf16 v[20:23], v[224:227], v[112:115], v[20:23]\n\t"
  "buffer_load_dwordx4 a[104:107], v1, %[srd], s45 offen offset:0 sc1\n\t"
  "v_mfma_f32_16x16x32_bf16 v[24:27], v[228:231], v[112:115], v[24:27]\n\t"
  "v_mfma_f32_16x16x32_bf16 v[28:31], v[232:235], v[112:115], v[28:31]\n\t"
  "buffer_load_dwordx4 a[108:111], v1, %[srd], s45 offen offset:1024 sc1\n\t"
  "v_mfma_f32_16x16x32_bf16 v[32:35], v[236:239], v[112:115], v[32:35]\n\t"
  "v_mfma_f32_16x16x32_bf16 v[36:39], v[240:243], v[112:115], v[36:39]\n\t"
  "s_waitcnt lgkmcnt(0)\n\t"
  "s_barrier\n\t"
  "v_mfma_f32_16x16x32_bf16 v[20:23], v[244:247], v[116:119], v[20:23]\n\t"
  "buffer_load_dwordx4 a[64:67], v1, %[srd], s45 offen offset:2048 sc1\n\t"
  "ds_read_b128 v[68:71], v14 offset:5120\n\t"
  "v_mfma_f32_16x16x32_bf16 v[24:27], a[160:163], v[116:119], v[24:27]\n\t"
  "ds_read_b128 v[72:75], v15 offset:5120\n\t"
  "ds_read_b128 v[76:79], v14 offset:25600\n\t"
  "v_mfma_f32_16x16x32_bf16 v[28:31], a[164:167], v[116:119], v[28:31]\n\t"
  "buffer_load_dwordx4 a[68:71], v1, %[srd], s45 offen offset:3072 sc1\n\t"
  "ds_read_b128 v[80:83], v15 offset:25600\n\t"
  "v_mfma_f32_16x16x32_bf16 v[32:35], a[168:171], v[116:119], v[32:35]\n\t"
  "ds_read_b128 v[84:87], v14 offset:46080\n\t"
  "ds_read_b128 v[88:91], v15 offset:46080\n\t"
  "v_mfma_f32_16x16x32_bf16 v[36:39], a[172:175], v[116:119], v[36:39]\n\t"
  "s_waitcnt vmcnt(9)\n\t"
  "ds_read_b128 v[92:95], v60 offset:5120\n\t"
  "v_mfma_f32_16x16x32_bf16 v[20:23], a[176:179], v[120:123], v[20:23]\n\t"
  "s_add_u32 s45, %[sH2ld], 3248128\n\t"
  "ds_read_b128 v[96:99], v61 offset:5120\n\t"
  "v_mfma_f32_16x16x32_bf16 v[24:27], a[180:183], v[120:123], v[24:27]\n\t"
  "ds_read_b128 v[100:103], v60 offset:25600\n\t"
  "ds_read_b128 v[104:107], v61 offset:25600\n\t"
  "v_mfma_f32_16x16x32_bf16 v[28:31], a[184:187], v[120:123], v[28:31]\n\t"
  "buffer_load_dwordx4 a[72:75], v1, %[srd], s45 offen offset:0 sc1\n\t"
  "v_mfma_f32_16x16x32_bf16 v[32:35], a[188:191], v[120:123], v[32:35]\n\t"
  "v_mfma_f32_16x16x32_bf16 v[36:39], a[192:195], v[120:123], v[36:39]\n\t"
  "buffer_load_dwordx4 a[76:79], v1, %[srd], s45 offen offset:1024 sc1\n\t"
  "v_mfma_f32_16x16x32_bf16 v[20:23], a[196:199], v[124:127], v[20:23]\n\t"
  "v_mfma_f32_16x16x32_bf16 v[24:27], a[200:203], v[124:127], v[24:27]\n\t"
  "v_mfma_f32_16x16x32_bf16 v[28:31], a[204:207], v[124:127], v[28:31]\n\t"
  "buffer_load_dwordx4 a[80:83], v1, %[srd], s45 offen offset:2048 sc1\n\t"
  "v_mfma_f32_16x16x32_bf16 v[32:35], a[208:211], v[124:127], v[32:35]\n\t"
  "v_mfma_f32_16x16x32_bf16 v[36:39], a[212:215], v[124:127], v[36:39]\n\t"
  "s_waitcnt vmcnt(10)\n\t"
  "buffer_load_dwordx4 a[84:87], v1, %[srd], s45 offen offset:3072 sc1\n\t"
  "v_mfma_f32_16x16x32_bf16 v[20:23], a[216:219], v[128:131], v[20:23]\n\t"
  "s_waitcnt lgkmcnt(0)\n\t"
  "v_add_f32 v68, v68, v76\n\t"
  "v_mfma_f32_16x16x32_bf16 v[24:27], a[220:223], v[128:131], v[24:27]\n\t"
  "v_add_f32 v69, v69, v77\n\t"
  "v_add_f32 v70, v70, v78\n\t"
  "v_mfma_f32_16x16x32_bf16 v[28:31], a[224:227], v[128:131], v[28:31]\n\t"
  "s_add_u32 s45, %[sH2ld], 3252224\n\t"
  "v_add_f32 v71, v71, v79\n\t"
  "v_mfma_f32_16x16x32_bf16 v[32:35], a[228:231], v[128:131], v[32:35]\n\t"
  "v_add_f32 v72, v72, v80\n\t"
  "v_add_f32 v73, v73, v81\n\t"
  "v_mfma_f32_16x16x32_bf16 v[36:39], a[232:235], v[128:131], v[36:39]\n\t"
  "buffer_load_dwordx4 a[88:91], v1, %[srd], s45 offen offset:0 sc1\n\t"
  "v_add_f32 v74, v74, v82\n\t"
  "v_mfma_f32_16x16x32_bf16 v[20:23], a[236:239], a[240:243], v[20:23]\n\t"
  "v_add_f32 v75, v75, v83\n\t"
  "v_add_f32 v84, v84, v92\n\t"
  "v_mfma_f32_16x16x32_bf16 v[24:27], a[0:3], a[240:243], v[24:27]\n\t"
  "buffer_load_dwordx4 a[92:95], v1, %[srd], s45 offen offset:1024 sc1\n\t"
  "v_add_f32 v85, v85, v93\n\t"
  "v_mfma_f32_16x16x32_bf16 v[28:31], a[4:7], a[240:243], v[28:31]\n\t"
  "v_add_f32 v86, v86, v94\n\t"
  "v_add_f32 v87, v87, v95\n\t"
  "v_mfma_f32_16x16x32_bf16 v[32:35], a[8:11], a[240:243], v[32:35]\n\t"
  "v_add_f32 v88, v88, v96\n\t"
  "v_add_f32 v89, v89, v97\n\t"
  "v_mfma_f32_16x16x32_bf16 v[36:39], a[12:15], a[240:243], v[36:39]\n\t"
  "s_waitcnt vmcnt(11)\n\t"
  "buffer_load_dwordx4 a[96:99], v1, %[srd], s45 offen offset:2048 sc1\n\t"
  "v_mfma_f32_16x16x32_bf16 v[20:23], a[16:19], a[56:59], v[20:23]\n\t"
  "v_add_f32 v90, v90, v98\n\t"
  "v_add_f32 v91, v91, v99\n\t"
  "v_mfma_f32_16x16x32_bf16 v[24:27], a[20:23], a[56:59], v[24:27]\n\t"
  "buffer_load_dwordx4 a[100:103], v1, %[srd], s45 offen offset:3072 sc1\n\t"
  "v_add_f32 v68, v68, v84\n\t"
  "v_mfma_f32_16x16x32_bf16 v[28:31], a[24:27], a[56:59], v[28:31]\n\t"
  "v_add_f32 v69, v69, v85\n\t"
  "v_add_f32 v70, v70, v86\n\t"
  "v_mfma_f32_16x16x32_bf16 v[32:35], a[28:31], a[56:59], v[32:35]\n\t"
  "v_add_f32 v71, v71, v87\n\t"
  "v_add_f32 v72, v72, v88\n\t"
  "v_mfma_f32_16x16x32_bf16 v[36:39], a[32:35], a[56:59], v[36:39]\n\t"
  "v_add_f32 v73, v73, v89\n\t"
  "v_add_f32 v74, v74, v90\n\t"
  "v_mfma_f32_16x16x32_bf16 v[20:23], a[36:39], a[60:63], v[20:23]\n\t"
  "v_add_f32 v75, v75, v91\n\t"
  "v_fma_f32 v68, v68, v64, v100\n\t"
  "v_mfma_f32_16x16x32_bf16 v[24:27], a[40:43], a[60:63], v[24:27]\n\t"
  "v_fma_f32 v69, v69, v65, v101\n\t"
  "v_fma_f32 v70, v70, v66, v102\n\t"
  "v_mfma_f32_16x16x32_bf16 v[28:31], a[44:47], a[60:63], v[28:31]\n\t"
  "v_fma_f32 v71, v71, v67, v103\n\t"
  "v_fma_f32 v72, v72, v108, v104\n\t"
  "v_mfma_f32_16x16x32_bf16 v[32:35], a[48:51], a[60:63], v[32:35]\n\t"
  "v_fma_f32 v73, v73, v109, v105\n\t"
  "v_fma_f32 v74, v74, v110, v106\n\t"
  "v_mfma_f32_16x16x32_bf16 v[36:39], a[52:55], a[60:63], v[36:39]\n\t"
  "s_waitcnt vmcnt(10)\n\t"
  "v_fma_f32 v75, v75, v111, v107\n\t"
  "v_mfma_f32_16x16x32_bf16 v[40:43], v[144:147], a[104:107], 0\n\t"
  "v_cvt_pk_bf16_f32 v76, v68, v69\n\t"
  "v_cvt_pk_bf16_f32 v77, v70, v71\n\t"
  "v_mfma_f32_16x16x32_bf16 v[44:47], v[148:151], a[104:107], 0\n\t"
  "v_cvt_pk_bf16_f32 v78, v72, v73\n\t"
  "v_cvt_pk_bf16_f32 v79, v74, v75\n\t"
  "v_mfma_f32_16x16x32_bf16 v[48:51], v[152:155], a[104:107], 0\n\t"
  "s_mov_b32 s55, 40960\n\t"
  "buffer_store_dwordx4 v[76:79], v16, %[srdo], s55 offen\n\t"
  "v_mfma_f32_16x16x32_bf16 v[52:55], v[156:159], a[104:107], 0\n\t"
  "ds_write_b128 v13, v[20:23] offset:10240\n\t"
  "v_mfma_f32_16x16x32_bf16 v[56:59], v[160:163], a[104:107], 0\n\t"
  "v_mfma_f32_16x16x32_bf16 v[40:43], v[164:167], a[108:111], v[40:43]\n\t"
  "v_mfma_f32_16x16x32_bf16 v[44:47], v[168:171], a[108:111], v[44:47]\n\t"
  "ds_write_b128 v13, v[24:27] offset:11264\n\t"
  "v_mfma_f32_16x16x32_bf16 v[48:51], v[172:175], a[108:111], v[48:51]\n\t"
  "v_mfma_f32_16x16x32_bf16 v[52:55], v[176:179], a[108:111], v[52:55]\n\t"
  "v_mfma_f32_16x16x32_bf16 v[56:59], v[180:183], a[108:111], v[56:59]\n\t"
  "s_waitcnt vmcnt(9)\n\t"
  "ds_write_b128 v13, v[28:31] offset:12288\n\t"
  "v_mfma_f32_16x16x32_bf16 v[40:43], v[184:187], a[64:67], v[40:43]\n\t"
  "v_mfma_f32_16x16x32_bf16 v[44:47], v[188:191], a[64:67], v[44:47]\n\t"
  "v_mfma_f32_16x16x32_bf16 v[48:51], v[192:195], a[64:67], v[48:51]\n\t"
  "ds_write_b128 v13, v[32:35] offset:13312\n\t"
  "v_mfma_f32_16x16x32_bf16 v[52:55], v[196:199], a[64:67], v[52:55]\n\t"
  "v_mfma_f32_16x16x32_bf16 v[56:59], v[200:203], a[64:67], v[56:59]\n\t"
  "v_mfma_f32_16x16x32_bf16 v[40:43], v[204:207], a[68:71], v[40:43]\n\t"
  "ds_write_b128 v13, v[36:39] offset:14336\n\t"
  "v_mfma_f32_16x16x32_bf16 v[44:47], v[208:211], a[68:71], v[44:47]\n\t"
  "v_mfma_f32_16x16x32_bf16 v[48:51], v[212:215], a[68:71], v[48:51]\n\t"
  "v_mfma_f32_16x16x32_bf16 v[52:55], v[216:219], a[68:71], v[52:55]\n\t"
  "v_mfma_f32_16x16x32_bf16 v[56:59], v[220:223], a[68:71], v[56:59]\n\t"
  "s_waitcnt vmcnt(7)\n\t"
  "v_mfma_f32_16x16x32_bf16 v[40:43], v[224:227], a[72:75], v[40:43]\n\t"
  "v_mfma_f32_16x16x32_bf16 v[44:47], v[228:231], a[72:75], v[44:47]\n\t"
  "v_mfma_f32_16x16x32_bf16 v[48:51], v[232:235], a[72:75], v[48:51]\n\t"
  "v_mfma_f32_16x16x32_bf16 v[52:55], v[236:239], a[72:75], v[52:55]\n\t"
  "s_waitcnt lgkmcnt(0)\n\t"
  "s_barrier\n\t"
  "v_mfma_f32_16x16x32_bf16 v[56:59], v[240:243], a[72:75], v[56:59]\n\t"
  "ds_read_b128 v[68:71], v14 offset:10240\n\t"
  "ds_read_b128 v[72:75], v15 offset:10240\n\t"
  "v_mfma_f32_16x16x32_bf16 v[40:43], v[244:247], a[76:79], v[40:43]\n\t"
  "ds_read_b128 v[76:79], v14 offset:30720\n\t"
  "ds_read_b128 v[80:83], v15 offset:30720\n\t"
  "v_mfma_f32_16x16x32_bf16 v[44:47], a[160:163], a[76:79], v[44:47]\n\t"
  "ds_read_b128 v[84:87], v14 offset:51200\n\t"
  "ds_read_b128 v[88:91], v15 offset:51200\n\t"
  "v_mfma_f32_16x16x32_bf16 v[48:51], a[164:167], a[76:79], v[48:51]\n\t"
  "ds_read_b128 v[92:95], v60 offset:10240\n\t"
  "ds_read_b128 v[96:99], v61 offset:10240\n\t"
  "v_mfma_f32_16x16x32_bf16 v[52:55], a[168:171], a[76:79], v[52:55]\n\t"
  "ds_read_b128 v[100:103], v60 offset:30720\n\t"
  "ds_read_b128 v[104:107], v61 offset:30720\n\t"
  "v_mfma_f32_16x16x32_bf16 v[56:59], a[172:175], a[76:79], v[56:59]\n\t"
  "s_waitcnt vmcnt(5)\n\t"
  "v_mfma_f32_16x16x32_bf16 v[40:43], a[176:179], a[80:83], v[40:43]\n\t"
  "v_mfma_f32_16x16x32_bf16 v[44:47], a[180:183], a[80:83], v[44:47]\n\t"
  "v_mfma_f32_16x16x32_bf16 v[48:51], a[184:187], a[80:83], v[48:51]\n\t"
  "v_mfma_f32_16x16x32_bf16 v[52:55], a[188:191], a[80:83], v[52:55]\n\t"
  "v_mfma_f32_16x16x32_bf16 v[56:59], a[192:195], a[80:83], v[56:59]\n\t"
  "v_mfma_f32_16x16x32_bf16 v[40:43], a[196:199], a[84:87], v[40:43]\n\t"
  "v_mfma_f32_16x16x32_bf16 v[44:47], a[200:203], a[84:87], v[44:47]\n\t"
  "s_waitcnt lgkmcnt(0)\n\t"
  "v_add_f32 v68, v68, v76\n\t"
  "v_mfma_f32_16x16x32_bf16 v[48:51], a[204:207], a[84:87], v[48:51]\n\t"
  "v_add_f32 v69, v69, v77\n\t"
  "v_add_f32 v70, v70, v78\n\t"
  "v_mfma_f32_16x16x32_bf16 v[52:55], a[208:211], a[84:87], v[52:55]\n\t"
  "v_add_f32 v71, v71, v79\n\t"
  "v_add_f32 v72, v72, v80\n\t"
  "v_mfma_f32_16x16x32_bf16 v[56:59], a[212:215], a[84:87], v[56:59]\n\t"
  "s_waitcnt vmcnt(3)\n\t"
  "v_add_f32 v73, v73, v81\n\t"
  "v_mfma_f32_16x16x32_bf16 v[40:43], a[216:219], a[88:91], v[40:43]\n\t"
  "v_add_f32 v74, v74, v82\n\t"
  "v_add_f32 v75, v75, v83\n\t"
  "v_mfma_f32_16x16x32_bf16 v[40:43], a[236:239], a[92:95], v[40:43]\n\t"
  "v_add_f32 v84, v84, v92\n\t"
  "v_add_f32 v85, v85, v93\n\t"
  "s_waitcnt vmcnt(2)\n\t"
  "v_mfma_f32_16x16x32_bf16 v[40:43], a[16:19], a[96:99], v[40:43]\n\t"
  "v_add_f32 v86, v86, v94\n\t"
  "v_add_f32 v87, v87, v95\n\t"
  "s_waitcnt vmcnt(1)\n\t"
  "v_mfma_f32_16x16x32_bf16 v[40:43], a[36:39], a[100:103], v[40:43]\n\t"
  "v_add_f32 v88, v88, v96\n\t"
  "v_add_f32 v89, v89, v97\n\t"
  "v_mfma_f32_16x16x32_bf16 v[44:47], a[220:223], a[88:91], v[44:47]\n\t"
  "v_add_f32 v90, v90, v98\n\t"
  "v_add_f32 v91, v91, v99\n\t"
  "v_mfma_f32_16x16x32_bf16 v[44:47], a[0:3], a[92:95], v[44:47]\n\t"
  "v_add_f32 v68, v68, v84\n\t"
  "v_add_f32 v69, v69, v85\n\t"
  "v_mfma_f32_16x16x32_bf16 v[44:47], a[20:23], a[96:99], v[44:47]\n\t"
  "v_add_f32 v70, v70, v86\n\t"
  "v_add_f32 v71, v71, v87\n\t"
  "v_mfma_f32_16x16x32_bf16 v[44:47], a[40:43], a[100:103], v[44:47]\n\t"
  "v_add_f32 v72, v72, v88\n\t"
  "v_add_f32 v73, v73, v89\n\t"
  "v_mfma_f32_16x16x32_bf16 v[48:51], a[224:227], a[88:91], v[48:51]\n\t"
  "v_add_f32 v74, v74, v90\n\t"
  "v_add_f32 v75, v75, v91\n\t"
  "v_mfma_f32_16x16x32_bf16 v[48:51], a[4:7], a[92:95], v[48:51]\n\t"
  "ds_write_b128 v13, v[40:43] offset:15360\n\t"
  "v_fma_f32 v68, v68, v64, v100\n\t"
  "v_mfma_f32_16x16x32_bf16 v[48:51], a[24:27], a[96:99], v[48:51]\n\t"
  "v_fma_f32 v69, v69, v65, v101\n\t"
  "v_fma_f32 v70, v70, v66, v102\n\t"
  "v_mfma_f32_16x16x32_bf16 v[48:51], a[44:47], a[100:103], v[48:51]\n\t"
  "v_fma_f32 v71, v71, v67, v103\n\t"
  "v_fma_f32 v72, v72, v108, v104\n\t"
  "v_mfma_f32_16x16x32_bf16 v[52:55], a[228:231], a[88:91], v[52:55]\n\t"
  "v_fma_f32 v73, v73, v109, v105\n\t"
  "v_fma_f32 v74, v74, v110, v106\n\t"
  "v_mfma_f32_16x16x32_bf16 v[52:55], a[8:11], a[92:95], v[52:55]\n\t"
  "ds_write_b128 v13, v[44:47] offset:16384\n\t"
  "v_fma_f32 v75, v75, v111, v107\n\t"
  "v_mfma_f32_16x16x32_bf16 v[52:55], a[28:31], a[96:99], v[52:55]\n\t"
  "v_cvt_pk_bf16_f32 v76, v68, v69\n\t"
  "v_cvt_pk_bf16_f32 v77, v70, v71\n\t"
  "v_mfma_f32_16x16x32_bf16 v[52:55], a[48:51], a[100:103], v[52:55]\n\t"
  "v_cvt_pk_bf16_f32 v78, v72, v73\n\t"
  "v_cvt_pk_bf16_f32 v79, v74, v75\n\t"
  "v_mfma_f32_16x16x32_bf16 v[56:59], a[232:235], a[88:91], v[56:59]\n\t"
  "s_mov_b32 s55, 1310720\n\t"
  "buffer_store_dwordx4 v[76:79], v16, %[srdo], s55 offen\n\t"
  "v_mfma_f32_16x16x32_bf16 v[56:59], a[12:15], a[92:95], v[56:59]\n\t"
  "ds_write_b128 v13, v[48:51] offset:17408\n\t"
  "v_mfma_f32_16x16x32_bf16 v[56:59], a[32:35], a[96:99], v[56:59]\n\t"
  "v_mfma_f32_16x16x32_bf16 v[56:59], a[52:55], a[100:103], v[56:59]\n\t"
  "ds_write_b128 v13, v[52:55] offset:18432\n\t"
  "s_nop 6\n\t"
  "ds_write_b128 v13, v[56:59] offset:19456\n\t"
  "buffer_store_dwordx2 v[132:133], v18, %[srd], 0 offen sc1\n\t"
  "s_waitcnt lgkmcnt(0)\n\t"
  "s_barrier\n\t"
  "ds_read_b128 v[68:71], v14 offset:15360\n\t"
  "ds_read_b128 v[72:75], v15 offset:15360\n\t"
  "ds_read_b128 v[76:79], v14 offset:35840\n\t"
  "ds_read_b128 v[80:83], v15 offset:35840\n\t"
  "ds_read_b128 v[84:87], v14 offset:56320\n\t"
  "ds_read_b128 v[88:91], v15 offset:56320\n\t"
  "ds_read_b128 v[92:95], v60 offset:15360\n\t"
  "ds_read_b128 v[96:99], v61 offset:15360\n\t"
  "ds_read_b128 v[100:103], v60 offset:35840\n\t"
  "ds_read_b128 v[104:107], v61 offset:35840\n\t"
  "s_waitcnt lgkmcnt(7)\n\t"
  "v_pk_add_f32 v[68:69], v[68:69], v[76:77]\n\t"
  "v_pk_add_f32 v[70:71], v[70:71], v[78:79]\n\t"
  "s_waitcnt lgkmcnt(6)\n\t"
  "v_pk_add_f32 v[72:73], v[72:73], v[80:81]\n\t"
  "v_pk_add_f32 v[74:75], v[74:75], v[82:83]\n\t"
  "s_waitcnt lgkmcnt(3)\n\t"
  "v_pk_add_f32 v[84:85], v[84:85], v[92:93]\n\t"
  "v_pk_add_f32 v[86:87], v[86:87], v[94:95]\n\t"
  "s_waitcnt lgkmcnt(2)\n\t"
  "v_pk_add_f32 v[88:89], v[88:89], v[96:97]\n\t"
  "v_pk_add_f32 v[90:91], v[90:91], v[98:99]\n\t"
  "v_pk_add_f32 v[68:69], v[68:69], v[84:85]\n\t"
  "v_pk_add_f32 v[70:71], v[70:71], v[86:87]\n\t"
  "v_pk_add_f32 v[72:73], v[72:73], v[88:89]\n\t"
  "v_pk_add_f32 v[74:75], v[74:75], v[90:91]\n\t"
  "s_waitcnt lgkmcnt(1)\n\t"
  "v_pk_fma_f32 v[68:69], v[68:69], v[64:65], v[100:101]\n\t"
  "v_pk_fma_f32 v[70:71], v[70:71], v[66:67], v[102:103]\n\t"
  "s_waitcnt lgkmcnt(0)\n\t"
  "v_pk_fma_f32 v[72:73], v[72:73], v[108:109], v[104:105]\n\t"
  "v_pk_fma_f32 v[74:75], v[74:75], v[110:111], v[106:107]\n\t"
  "v_cvt_pk_bf16_f32 v76, v68, v69\n\t"
  "v_cvt_pk_bf16_f32 v77, v70, v71\n\t"
  "v_cvt_pk_bf16_f32 v78, v72, v73\n\t"
  "v_cvt_pk_bf16_f32 v79, v74, v75\n\t"
  "s_mov_b32 s55, 1351680\n\t"
  "buffer_store_dwordx4 v[76:79], v16, %[srdo], s55 offen\n\t"
  "s_branch Lfend%=\n\t"
  "Lst0_slow%=:\n\t"
  "s_nop 7\n\t"
  "s_nop 3\n\t"
  "s_mov_b32 s49, 0x40000\n\t"
  "Lst0_loop%=:\n\t"
  "s_sleep 2\n\t"
  "buffer_load_dwordx2 v[92:93], v136, %[srd], %[sST] offen offset:0 sc1\n\t"
  "buffer_load_dwordx2 v[94:95], v136, %[srd], %[sST] offen offset:1024 sc1\n\t"
  "buffer_load_dwordx2 v[96:97], v136, %[srd], %[sST] offen offset:2048 sc1\n\t"
  "buffer_load_dwordx2 v[98:99], v136, %[srd], %[sST] offen offset:3072 sc1\n\t"
  "s_waitcnt vmcnt(0)\n\t"
  "v_min_u32 v131, v93, v95\n\t"
  "v_min3_u32 v131, v131, v97, v99\n\t"
  "v_cmp_eq_u32 vcc, 0, v131\n\t"
  "s_nop 3\n\t"
  "s_cmp_eq_u64 vcc, 0\n\t"
  "s_cbranch_scc1 Lst0_back%=\n\t"
  "s_sub_u32 s49, s49, 1\n\t"
  "s_cmp_lg_u32 s49, 0\n\t"
  "s_cbranch_scc1 Lst0_loop%=\n\t"
  "s_branch Lst0_back%=\n\t"
  "Lw2_slow%=:\n\t"
  "s_nop 7\n\t"
  "s_nop 3\n\t"
  "s_mov_b32 s49, 0x40000\n\t"
  "Lw2_loop%=:\n\t"
  "s_sleep 2\n\t"
  "buffer_load_dwordx2 v[138:139], v2, %[srd], 0 offen sc1\n\t"
  "s_waitcnt vmcnt(0)\n\t"
  "v_cmp_ne_u64 vcc, v[132:133], v[138:139]\n\t"
  "s_nop 3\n\t"
  "s_cmp_eq_u64 vcc, 0\n\t"
  "s_cbranch_scc1 Lw2_back%=\n\t"
  "s_sub_u32 s49, s49, 1\n\t"
  "s_cmp_lg_u32 s49, 0\n\t"
  "s_cbranch_scc1 Lw2_loop%=\n\t"
  "s_branch Lw2_back%=\n\t"
  "Lst1_slow%=:\n\t"
  "s_nop 7\n\t"
  "s_nop 3\n\t"
  "s_waitcnt vmcnt(0)\n\t"
  "s_movk_i32 s59, 8192\n\t"
  "buffer_store_dwordx4 v[132:135], v9, %[srd], s59 offen sc1\n\t"
  "s_mov_b32 s49, 0x40000\n\t"
  "Lst1_loop%=:\n\t"
  "s_sleep 2\n\t"
  "buffer_load_dwordx2 v[100:101], v136, %[srd], s54 offen offset:0 sc1\n\t"
  "buffer_load_dwordx2 v[102:103], v136, %[srd], s54 offen offset:1024 sc1\n\t"
  "buffer_load_dwordx2 v[104:105], v136, %[srd], s54 offen offset:2048 sc1\n\t"
  "buffer_load_dwordx2 v[106:107], v136, %[srd], s54 offen offset:3072 sc1\n\t"
  "s_waitcnt vmcnt(0)\n\t"
  "v_min_u32 v131, v101, v103\n\t"
  "v_min3_u32 v131, v131, v105, v107\n\t"
  "v_cmp_eq_u32 vcc, 0, v131\n\t"
  "s_nop 3\n\t"
  "s_cmp_eq_u64 vcc, 0\n\t"
  "s_cbranch_scc1 Lst1_back%=\n\t"
  "s_sub_u32 s49, s49, 1\n\t"
  "s_cmp_lg_u32 s49, 0\n\t"
  "s_cbranch_scc1 Lst1_loop%=\n\t"
  "s_branch Lst1_back%=\n\t"
  "Lfl0_slow%=:\n\t"
  "s_nop 7\n\t"
  "s_nop 3\n\t"
  "s_waitcnt vmcnt(0)\n\t"
  "s_movk_i32 s59, 16384\n\t"
  "buffer_store_dwordx4 v[132:135], v9, %[srd], s59 offen sc1\n\t"
  "s_mov_b32 s49, 0x40000\n\t"
  "Lfl0_loop%=:\n\t"
  "s_sleep 2\n\t"
  "s_add_u32 s57, %[sFL], 0\n\t"
  "buffer_load_dwordx2 v[138:139], v137, %[srd], s57 offen sc1\n\t"
  "s_waitcnt vmcnt(0)\n\t"
  "v_cmp_ne_u64 vcc, v[132:133], v[138:139]\n\t"
  "s_nop 3\n\t"
  "s_cmp_eq_u64 vcc, 0\n\t"
  "s_cbranch_scc1 Lfl0_back%=\n\t"
  "s_sub_u32 s49, s49, 1\n\t"
  "s_cmp_lg_u32 s49, 0\n\t"
  "s_cbranch_scc1 Lfl0_loop%=\n\t"
  "s_branch Lfl0_back%=\n\t"
  "Lfl1_slow%=:\n\t"
  "s_nop 7\n\t"
  "s_nop 3\n\t"
  "s_waitcnt vmcnt(0)\n\t"
  "s_movk_i32 s59, 24576\n\t"
  "buffer_store_dwordx4 v[132:135], v9, %[srd], s59 offen sc1\n\t"
  "s_mov_b32 s49, 0x40000\n\t"
  "Lfl1_loop%=:\n\t"
  "s_sleep 2\n\t"
  "s_add_u32 s57, %[sFL], 8192\n\t"
  "buffer_load_dwordx2 v[138:139], v137, %[srd], s57 offen sc1\n\t"
  "s_waitcnt vmcnt(0)\n\t"
  "v_cmp_ne_u64 vcc, v[132:133], v[138:139]\n\t"
  "s_nop 3\n\t"
  "s_cmp_eq_u64 vcc, 0\n\t"
  "s_cbranch_scc1 Lfl1_back%=\n\t"
  "s_sub_u32 s49, s49, 1\n\t"
  "s_cmp_lg_u32 s49, 0\n\t"
  "s_cbranch_scc1 Lfl1_loop%=\n\t"
  "s_branch Lfl1_back%=\n\t"
  "Lfl2_slow%=:\n\t"
  "s_nop 7\n\t"
  "s_nop 3\n\t"
  "s_mov_b32 s49, 0x40000\n\t"
  "Lfl2_loop%=:\n\t"
  "s_sleep 2\n\t"
  "s_add_u32 s57, %[sFL], 16384\n\t"
  "buffer_load_dwordx2 v[138:139], v137, %[srd], s57 offen sc1\n\t"
  "s_waitcnt vmcnt(0)\n\t"
  "v_cmp_ne_u64 vcc, v[132:133], v[138:139]\n\t"
  "s_nop 3\n\t"
  "s_cmp_eq_u64 vcc, 0\n\t"
  "s_cbranch_scc1 Lfl2_back%=\n\t"
  "s_sub_u32 s49, s49, 1\n\t"
  "s_cmp_lg_u32 s49, 0\n\t"
  "s_cbranch_scc1 Lfl2_loop%=\n\t"
  "s_branch Lfl2_back%=\n\t"
  "Lfl3_slow%=:\n\t"
  "s_nop 7\n\t"
  "s_nop 3\n\t"
  "s_mov_b32 s49, 0x40000\n\t"
  "Lfl3_loop%=:\n\t"
  "s_sleep 2\n\t"
  "s_add_u32 s57, %[sFL], 24576\n\t"
  "buffer_load_dwordx2 v[138:139], v137, %[srd], s57 offen sc1\n\t"
  "s_waitcnt vmcnt(0)\n\t"
  "v_cmp_ne_u64 vcc, v[132:133], v[138:139]\n\t"
  "s_nop 3\n\t"
  "s_cmp_eq_u64 vcc, 0\n\t"
  "s_cbranch_scc1 Lfl3_back%=\n\t"
  "s_sub_u32 s49, s49, 1\n\t"
  "s_cmp_lg_u32 s49, 0\n\t"
  "s_cbranch_scc1 Lfl3_loop%=\n\t"
  "s_branch Lfl3_back%=\n\t"
  "Lfend%=:\n\t"
  ".Lk_end:\n\t"
    :
    : [srd] "s"(srd), [srdo] "s"(srdo), [w] "s"(w),
      [tlo] "s"((u32)tag), [thi] "s"((u32)(tag >> 32)), [sW1] "s"(OFF_W1F + (j * 4 + w) * 61440u), [sW2] "s"(OFF_W2F + (ng * 4 + w) * 61440u),
      [sUB] "s"(OFF_UB + (j * 4 + w) * 1536u),
      [sH2ld] "s"(OFF_H2F + ((4 * p + sub) * 48 + 12 * w) * 1024u), [sST] "s"(OFF_ST2 + p * 4096u), [sFL] "s"(OFF_FL + p * 32768u + w * 2048u),
      [sRT] "s"(OFF_RT + ng * 320u), [wdone] "s"(wdone)
    : "memory", "vcc", "scc",
"a0", "a1", "a2", "a3", "a4", "a5", "a6", "a7", "a8", "a9", "a10", "a11", "a12", "a13", "a14", "a15", "a16", "a17", "a18", "a19", "a20", "a21", "a22", "a23", "a24", "a25", "a26", "a27", "a28", "a29", "a30", "a31", "a32", "a33", "a34", "a35", "a36", "a37", "a38", "a39", "a40", "a41", "a42", "a43", "a44", "a45", "a46", "a47", "a48", "a49", "a50", "a51", "a52", "a53", "a54", "a55", "a56", "a57", "a58", "a59", "a60", "a61", "a62", "a63", "a64", "a65", "a66", "a67", "a68", "a69", "a70", "a71", "a72", "a73", "a74", "a75", "a76", "a77", "a78", "a79", "a80", "a81", "a82", "a83", "a84", "a85", "a86", "a87", "a88", "a89", "a90", "a91", "a92", "a93", "a94", "a95", "a96", "a97", "a98", "a99", "a100", "a101", "a102", "a103", "a104", "a105", "a106", "a107", "a108", "a109", "a110", "a111", "a112", "a113", "a114", "a115", "a116", "a117", "a118", "a119", "a120", "a121", "a122", "a123", "a124", "a125", "a126", "a127", "a128", "a129", "a130", "a131", "a132", "a133", "a134", "a135", "a136", "a137", "a138", "a139", "a140", "a141", "a142", "a143", "a144", "a145", "a146", "a147", "a148", "a149", "a150", "a151", "a152", "a153", "a154", "a155", "a156", "a157", "a158", "a159", "a160", "a161", "a162", "a163", "a164", "a165", "a166", "a167", "a168", "a169", "a170", "a171", "a172", "a173", "a174", "a175", "a176", "a177", "a178", "a179", "a180", "a181", "a182", "a183", "a184", "a185", "a186", "a187", "a188", "a189", "a190", "a191", "a192", "a193", "a194", "a195", "a196", "a197", "a198", "a199", "a200", "a201", "a202", "a203", "a204", "a205", "a206", "a207", "a208", "a209", "a210", "a211", "a212", "a213", "a214", "a215", "a216", "a217", "a218", "a219", "a220", "a221", "a222", "a223", "a224", "a225", "a226", "a227", "a228", "a229", "a230", "a231", "a232", "a233", "a234", "a235", "a236", "a237", "a238", "a239", "a240", "a241", "a242", "a243", "a244", "a245", "a246", "a247", "a248", "a249", "a250", "a251", "a252", "a253", "a254", "a255", "s44", "s45", "s46", "s47", "s48", "s49", "s52", "s54", "s55", "s56", "s57", "s58", "s59", "s60", "s61", "v0", "v1", "v2", "v3", "v4", "v5", "v6", "v7", "v8", "v9", "v10", "v11", "v12", "v13", "v14", "v15", "v16", "v17", "v18", "v19", "v20", "v21", "v22", "v23", "v24", "v25", "v26", "v27", "v28", "v29", "v30", "v31", "v32", "v33", "v34", "v35", "v36", "v37", "v38", "v39", "v40", "v41", "v42", "v43", "v44", "v45", "v46", "v47", "v48", "v49", "v50", "v51", "v52", "v53", "v54", "v55", "v56", "v57", "v58", "v59", "v60", "v61", "v62", "v63", "v64", "v65", "v66", "v67", "v68", "v69", "v70", "v71", "v72", "v73", "v74", "v75", "v76", "v77", "v78", "v79", "v80", "v81", "v82", "v83", "v84", "v85", "v86", "v87", "v88", "v89", "v90", "v91", "v92", "v93", "v94", "v95", "v96", "v97", "v98", "v99", "v100", "v101", "v102", "v103", "v104", "v105", "v106", "v107", "v108", "v109", "v110", "v111", "v112", "v113", "v114", "v115", "v116", "v117", "v118", "v119", "v120", "v121", "v122", "v123", "v124", "v125", "v126", "v127", "v128", "v129", "v130", "v131", "v132", "v133", "v134", "v135", "v136", "v137", "v138", "v139", "v141", "v142", "v143", "v144", "v145", "v146", "v147", "v148", "v149", "v150", "v151", "v152", "v153", "v154", "v155", "v156", "v157", "v158", "v159", "v160", "v161", "v162", "v163", "v164", "v165", "v166", "v167", "v168", "v169", "v170", "v171", "v172", "v173", "v174", "v175", "v176", "v177", "v178", "v179", "v180", "v181", "v182", "v183", "v184", "v185", "v186", "v187", "v188", "v189", "v190", "v191", "v192", "v193", "v194", "v195", "v196", "v197", "v198", "v199", "v200", "v201", "v202", "v203", "v204", "v205", "v206", "v207", "v208", "v209", "v210", "v211", "v212", "v213", "v214", "v215", "v216", "v217", "v218", "v219", "v220", "v221", "v222", "v223", "v224", "v225", "v226", "v227", "v228", "v229", "v230", "v231", "v232", "v233", "v234", "v235", "v236", "v237", "v238", "v239", "v240", "v241", "v242", "v243", "v244", "v245", "v246", "v247", "v248", "v249", "v250", "v251", "v252", "v253", "v254", "v255"
  );
}

extern "C" void kernel_launch(void* const* d_in, const int* in_sizes, int n_in, void* d_out, int out_size,
                              void* d_ws, size_t ws_size, hipStream_t stream) {
  k_main<<<256, 256, 0, stream>>>((const u16*)d_in[0], (const u16*)d_in[1], (const u16*)d_in[2], (const u16*)d_in[3], (const u16*)d_in[4],
                                  (const u16*)d_in[5], (const u16*)d_in[7], (const u16*)d_in[8], (const u16*)d_in[9], (const u16*)d_in[10],
                                  (const u16*)d_in[11], (char*)d_ws, (u16*)d_out);
}
```

```cpp
#define TWO 0
#include <hip/hip_runtime.h>
#include <stdint.h>

typedef unsigned short u16;
typedef unsigned int u32;
typedef unsigned long long u64;
typedef __attribute__((ext_vector_type(8))) __bf16 bf16x8;
typedef __attribute__((ext_vector_type(2))) __bf16 bf16x2;
typedef __attribute__((ext_vector_type(4))) short s16x4;
typedef __attribute__((ext_vector_type(4))) float f32x4;
typedef __attribute__((ext_vector_type(2))) float f32x2;
typedef __attribute__((ext_vector_type(4))) u32 u32x4;
typedef __attribute__((ext_vector_type(2))) u32 u32x2;
#define LAS __attribute__((address_space(3)))
#define GAS __attribute__((address_space(1)))
#define DI __device__ __forceinline__

#define STAMP(id)
#define STAMP_DECL
#define STAMP_OUT(kid)
#define A1_R 4
#define A1_P 8
#define A1_S 2
#define A1_S0 0
#define A1_X 0
#define DBG_W 1
#define DBG_X 1
#define DBG_M 1
#define W_DELAY 0
#define ROLEMAP 0
#define SELF_BAR 0
#define CODEW 1
#define U_DELAY 16
#define PRE_ORDER 3
#define OPS_ORDER 0
#define WP2_EARLY 1
#define KV_EXACT 0
#define FX_R 8
#define CONV_FIRST 1
#define U_MAP 1
#define WP2_SELF 1
constexpr int S_ = 2048, D_ = 640, F_ = 1536;
constexpr unsigned OFF_EP = 0u;
constexpr unsigned OFF_FR = 2048u;
constexpr unsigned OFF_FA1 = 4096u;
constexpr unsigned OFF_FA2 = 24576u;
constexpr unsigned OFF_FA3 = 55296u;
constexpr unsigned OFF_FX = 71680u;
constexpr unsigned OFF_FX1 = 235520u;
constexpr unsigned OFF_FW1 = 399360u;
constexpr unsigned OFF_FU = 407552u;
constexpr unsigned OFF_FW2 = 415744u;
constexpr unsigned OFF_RT = 419840u;
constexpr unsigned OFF_GB = 422400u;
constexpr unsigned OFF_RL = 424960u;
constexpr unsigned OFF_ST2 = 442368u;
constexpr unsigned OFF_FL = 573440u;
constexpr unsigned OFF_MODP = 1097728u;
constexpr unsigned OFF_ST1 = 1159168u;
constexpr unsigned OFF_UB = 1175552u;
constexpr unsigned OFF_KVT = 1273856u;
constexpr unsigned OFF_XT = 3895296u;
constexpr unsigned OFF_X2P = 6516736u;
constexpr unsigned OFF_H2F = 9138176u;
constexpr unsigned OFF_W1F = 15429632u;
constexpr unsigned OFF_W2F = 19361792u;
constexpr unsigned WS_END = 21327872u;
constexpr unsigned TAG_A = 2654435769u;
constexpr unsigned TAG_B = 2246822507u;
constexpr unsigned TAG_C = 2135587861u;
constexpr unsigned TAG_D = 3266489909u;
constexpr unsigned FC_PITCH = 4160u;
constexpr unsigned FC_CH = 16640u;
constexpr unsigned L_FC = 0u;
constexpr unsigned L_KV = 66560u;
constexpr unsigned L_X = 84992u;
constexpr unsigned L_ST = 101376u;
constexpr unsigned L_XR = 117760u;
constexpr unsigned L_CNT = 140288u;
constexpr unsigned L_STASH = 143360u;
constexpr unsigned L_UN = 71680u;
constexpr unsigned UN_SZ = 22528u;

constexpr unsigned OOB = 0x80000000u;
DI unsigned rfl(unsigned x) { return __builtin_amdgcn_readfirstlane(x); }
DI unsigned stkey(unsigned r) { const unsigned k = (r >> 1) & 7; return (((k ^ (k >> 1)) >> 1) & 1) | ((k & 1) << 1) | (k & 4); }
DI float bflo(u32 w) { return __uint_as_float(w << 16); }
DI float bfhi(u32 w) { return __uint_as_float(w & 0xffff0000u); }
DI u32 pack_bf16(float lo, float hi) { bf16x2 v; v[0] = (__bf16)lo; v[1] = (__bf16)hi; return __builtin_bit_cast(u32, v); }
DI float rbf(float x) { __bf16 b = (__bf16)x; return (float)b; }
DI float dot2(u32 a, u32 b, float c) { return __builtin_amdgcn_fdot2_f32_bf16(__builtin_bit_cast(bf16x2, a), __builtin_bit_cast(bf16x2, b), c, false); }
DI f32x4 mfma(u32x4 a, u32x4 b, f32x4 c) { return __builtin_amdgcn_mfma_f32_16x16x32_bf16(__builtin_bit_cast(bf16x8, a), __builtin_bit_cast(bf16x8, b), c, 0, 0, 0); }
DI void mfma_v(f32x4& c, u32x4 a, u32x4 b) { asm volatile("s_nop 1\n\tv_mfma_f32_16x16x32_bf16 %0, %1, %2, %0" : "+v"(c) : "v"(a), "v"(b)); }
DI u32x2 tr_read(const char* p) { return __builtin_bit_cast(u32x2, __builtin_amdgcn_ds_read_tr16_b64_v4i16((LAS s16x4*)p)); }
typedef __amdgpu_buffer_rsrc_t rsrc_t;
DI rsrc_t mkrsrc(const void* p, unsigned bytes) { return __builtin_amdgcn_make_buffer_rsrc((void*)p, 0, bytes, 0x00020000); }
#define BAR() asm volatile("s_waitcnt lgkmcnt(0)\n\ts_barrier" ::: "memory")
#define FENCE() do { asm volatile("" ::: "memory"); __builtin_amdgcn_sched_barrier(0); } while (0)
constexpr int SC1 = 16, NT = 2, W_IDLE_SLEEP = 8;

DI u64 load_tag(const char* ws) {
  u64 epoch;
  asm volatile("s_load_dwordx2 %0, %1, 0\n\ts_waitcnt lgkmcnt(0)" : "=&s"(epoch) : "s"(ws) : "memory");
  const u32 tl = ((u32)epoch ^ TAG_A) * TAG_B, th = ((u32)(epoch >> 32) ^ TAG_C) * TAG_D + tl;
  return ((u64)th << 32) | tl;
}
template <int N, int SL = 2>
DI void poll_flags(rsrc_t srd, unsigned voff, u64 tag) {
  const u32 lo = (u32)tag, hi = (u32)(tag >> 32);
#pragma unroll 1
  for (int spin = 0; spin < (1 << 18); ++spin) {
    u32 bad = 0;
#pragma unroll
    for (int k = 0; k < N; ++k) { const u32x2 f = __builtin_amdgcn_raw_buffer_load_b64(srd, voff + k * 512, 0, SC1); bad |= (f[0] ^ lo) | (f[1] ^ hi); }
    asm volatile("" ::: "memory");
    if (__all(bad == 0)) break;
    __builtin_amdgcn_s_sleep(SL);
  }
  FENCE();
}
DI void put_flag(rsrc_t srd, unsigned off, unsigned lane, u64 tag) {
  __builtin_amdgcn_raw_buffer_store_b128(u32x4{(u32)tag, (u32)(tag >> 32), (u32)tag, (u32)(tag >> 32)}, srd, lane < 8 ? off + lane * 16 : OOB, 0, SC1);
  FENCE();
}
DI float modval(rsrc_t srd, unsigned col) {
  float m[4];
#pragma unroll
  for (int k = 0; k < 4; ++k) m[k] = __uint_as_float(__builtin_amdgcn_raw_buffer_load_b32(srd, col * 4, OFF_MODP + k * 15360, SC1));
  return rbf((m[0] + m[1]) + (m[2] + m[3]));
}

constexpr int RP1 = 400, RP2 = 656, L_WS = 69632;

__global__ __launch_bounds__(256, 1) void k_main(const u16* __restrict__ x, const u16* __restrict__ kIn, const u16* __restrict__ vIn,
                                                 const u16* __restrict__ shk, const u16* __restrict__ ssmk, const u16* __restrict__ ssmD,
                                                 const u16* __restrict__ wg, const u16* __restrict__ wu, const u16* __restrict__ wd,
                                                 const u16* __restrict__ cIn, const u16* __restrict__ wmod, char* ws, u16* __restrict__ out) {
  __shared__ __attribute__((aligned(16))) char smem[163840];
  const unsigned b = blockIdx.x, tid = threadIdx.x, w = rfl(tid >> 6), lane = tid & 63;
  const unsigned xcd = b & 7, loc = b >> 3, pl = loc & 3, p = 4 * (xcd >> 1) + pl, j = 8 * (xcd & 1) + (loc >> 2), ng = j & 7, sub = j >> 3;
  if (loc >= 24) {
    u64 pc; unsigned len;
    asm volatile("s_getpc_b64 %0\n.Lk_here:\n\ts_mov_b32 %1, .Lk_end-.Lk_here" : "=s"(pc), "=s"(len));
    asm volatile("buffer_load_dword a255, %0, %1, 0 offen" :: "v"(lane < 16 ? (lane * 32 + (loc - 24) * 4 + w) * 128 : OOB), "s"(mkrsrc((const void*)pc, len)) : "a255", "memory");
  }
  asm volatile("" :: "s"(x), "s"(kIn), "s"(vIn), "s"(shk), "s"(ssmk), "s"(ssmD), "s"(wg), "s"(wu), "s"(wd), "s"(cIn), "s"(wmod), "s"(ws), "s"(out));
  STAMP_DECL; STAMP(0);
  const rsrc_t srd = mkrsrc(ws, WS_END);
  const rsrc_t srdo = mkrsrc(out, S_ * D_ * 2);
  const unsigned lb = (unsigned)(size_t)smem;
  const unsigned role = loc < 20 ? 0 : loc < 28 ? 1 : 2;
  const unsigned cb = xcd * 20 + loc, w1 = xcd * 8 + loc - 20, w2 = xcd * 4 + loc - 28;
  const unsigned tb = 4 * (cb & 31) + w, lkv = L_KV + 2048 + w * 9216;
  if (role == 0)
    asm volatile(
  "s_mov_b32 m0, %[lk]\n\t"
  "s_nop 3\n\t"
  "buffer_load_dwordx4 %[vo], %[rk], %[sh] offen nt lds\n\t"
  "s_add_u32 m0, %[lk], 1024\n\t"
  "s_add_u32 s44, %[so], 0\n\t"
  "buffer_load_dwordx4 %[vo], %[rk], s44 offen nt lds\n\t"
  "s_add_u32 m0, %[lk], 5120\n\t"
  "s_nop 0\n\t"
  "buffer_load_dwordx4 %[vo], %[rv], s44 offen nt lds\n\t"
  "s_add_u32 m0, %[lk], 2048\n\t"
  "s_add_u32 s44, %[so], 5120\n\t"
  "buffer_load_dwordx4 %[vo], %[rk], s44 offen nt lds\n\t"
  "s_add_u32 m0, %[lk], 6144\n\t"
  "s_nop 0\n\t"
  "buffer_load_dwordx4 %[vo], %[rv], s44 offen nt lds\n\t"
  "s_add_u32 m0, %[lk], 3072\n\t"
  "s_add_u32 s44, %[so], 10240\n\t"
  "buffer_load_dwordx4 %[vo], %[rk], s44 offen nt lds\n\t"
  "s_add_u32 m0, %[lk], 7168\n\t"
  "s_nop 0\n\t"
  "buffer_load_dwordx4 %[vo], %[rv], s44 offen nt lds\n\t"
  "s_add_u32 m0, %[lk], 4096\n\t"
  "s_add_u32 s44, %[so], 15360\n\t"
  "buffer_load_dwordx4 %[vo], %[rk], s44 offen nt lds\n\t"
  "s_add_u32 m0, %[lk], 8192\n\t"
  "s_nop 0\n\t"
  "buffer_load_dwordx4 %[vo], %[rv], s44 offen nt lds\n\t"
      :: [rk] "s"(mkrsrc(kIn, S_ * D_ * 2)), [rv] "s"(mkrsrc(vIn, S_ * D_ * 2)), [lk] "s"(lb + lkv), [so] "s"(tb * 20480u + (cb >> 5) * 256u), [sh] "s"(tb ? tb * 20480u + (cb >> 5) * 256u - 5120u : OOB),
         [vo] "v"((lane >> 4) * 1280 + (lane & 15) * 16) : "memory", "scc", "s44");
  FENCE();
  const u32x4 zero4 = {0, 0, 0, 0};
  unsigned wdone = 0, dval = 0;
#define U_KQ(u_) ((u_) & 3)
#define U_CG(u_) (((u_) >> 2) < 30 || ((u_) >> 2) >= 50 ? (u_) >> 2 : ((u_) >> 2) < 40 ? ((u_) >> 2) + 10 : ((u_) >> 2) - 10)
#define U_SRC(u_) (U_KQ(u_) * 160u * 7680u + U_CG(u_) * 128u)
#define U_FLAG(u_) (OFF_FA2 + (U_KQ(u_) * 60 + U_CG(u_)) * 128)
  const unsigned vm = (lane >> 3) * 7680 + (((lane & 7) ^ (((lane >> 4) & 3) << 1)) * 16);
#define GEMV_LOAD(u_, lu_) asm volatile( \
  "s_nop 4\n\t" \
  "s_add_u32 m0, %[lu], 20480\n\t" \
  "s_nop 0\n\t" \
  "buffer_load_dwordx4 %[vl16], %[rc], %[sC] offen lds\n\t" \
  "s_add_u32 m0, %[lu], 0\n\t" \
  "s_add_u32 s44, %[sMod], 0\n\t" \
  "buffer_load_dwordx4 %[vm], %[rm], s44 offen nt lds\n\t" \
  "s_add_u32 m0, %[lu], 1024\n\t" \
  "s_add_u32 s44, %[sMod], 61440\n\t" \
  "buffer_load_dwordx4 %[vm], %[rm], s44 offen nt lds\n\t" \
  "s_add_u32 m0, %[lu], 2048\n\t" \
  "s_add_u32 s44, %[sMod], 122880\n\t" \
  "buffer_load_dwordx4 %[vm], %[rm], s44 offen nt lds\n\t" \
  "s_add_u32 m0, %[lu], 3072\n\t" \
  "s_add_u32 s44, %[sMod], 184320\n\t" \
  "buffer_load_dwordx4 %[vm], %[rm], s44 offen nt lds\n\t" \
  "s_add_u32 m0, %[lu], 4096\n\t" \
  "s_add_u32 s44, %[sMod], 245760\n\t" \
  "buffer_load_dwordx4 %[vm], %[rm], s44 offen nt lds\n\t" \
  "s_add_u32 m0, %[lu], 5120\n\t" \
  "s_add_u32 s44, %[sMod], 307200\n\t" \
  "buffer_load_dwordx4 %[vm], %[rm], s44 offen nt lds\n\t" \
  "s_add_u32 m0, %[lu], 6144\n\t" \
  "s_add_u32 s44, %[sMod], 368640\n\t" \
  "buffer_load_dwordx4 %[vm], %[rm], s44 offen nt lds\n\t" \
  "s_add_u32 m0, %[lu], 7168\n\t" \
  "s_add_u32 s44, %[sMod], 430080\n\t" \
  "buffer_load_dwordx4 %[vm], %[rm], s44 offen nt lds\n\t" \
  "s_add_u32 m0, %[lu], 8192\n\t" \
  "s_add_u32 s44, %[sMod], 491520\n\t" \
  "buffer_load_dwordx4 %[vm], %[rm], s44 offen nt lds\n\t" \
  "s_add_u32 m0, %[lu], 9216\n\t" \
  "s_add_u32 s44, %[sMod], 552960\n\t" \
  "buffer_load_dwordx4 %[vm], %[rm], s44 offen nt lds\n\t" \
  "s_add_u32 m0, %[lu], 10240\n\t" \
  "s_add_u32 s44, %[sMod], 614400\n\t" \
  "buffer_load_dwordx4 %[vm], %[rm], s44 offen nt lds\n\t" \
  "s_add_u32 m0, %[lu], 11264\n\t" \
  "s_add_u32 s44, %[sMod], 675840\n\t" \
  "buffer_load_dwordx4 %[vm], %[rm], s44 offen nt lds\n\t" \
  "s_add_u32 m0, %[lu], 12288\n\t" \
  "s_add_u32 s44, %[sMod], 737280\n\t" \
  "buffer_load_dwordx4 %[vm], %[rm], s44 offen nt lds\n\t" \
  "s_add_u32 m0, %[lu], 13312\n\t" \
  "s_add_u32 s44, %[sMod], 798720\n\t" \
  "buffer_load_dwordx4 %[vm], %[rm], s44 offen nt lds\n\t" \
  "s_add_u32 m0, %[lu], 14336\n\t" \
  "s_add_u32 s44, %[sMod], 860160\n\t" \
  "buffer_load_dwordx4 %[vm], %[rm], s44 offen nt lds\n\t" \
  "s_add_u32 m0, %[lu], 15360\n\t" \
  "s_add_u32 s44, %[sMod], 921600\n\t" \
  "buffer_load_dwordx4 %[vm], %[rm], s44 offen nt lds\n\t" \
  "s_add_u32 m0, %[lu], 16384\n\t" \
  "s_add_u32 s44, %[sMod], 983040\n\t" \
  "buffer_load_dwordx4 %[vm], %[rm], s44 offen nt lds\n\t" \
  "s_add_u32 m0, %[lu], 17408\n\t" \
  "s_add_u32 s44, %[sMod], 1044480\n\t" \
  "buffer_load_dwordx4 %[vm], %[rm], s44 offen nt lds\n\t" \
  "s_add_u32 m0, %[lu], 18432\n\t" \
  "s_add_u32 s44, %[sMod], 1105920\n\t" \
  "buffer_load_dwordx4 %[vm], %[rm], s44 offen nt lds\n\t" \
  "s_add_u32 m0, %[lu], 19456\n\t" \
  "s_add_u32 s44, %[sMod], 1167360\n\t" \
  "buffer_load_dwordx4 %[vm], %[rm], s44 offen nt lds\n\t" \
        :: [rm] "s"(mkrsrc(wmod, D_ * 3840 * 2)), [rc] "s"(mkrsrc(cIn, D_ * 2)), [lu] "s"(lu_), [sC] "s"(U_KQ(u_) * 320u), [sMod] "s"(U_SRC(u_)), \
           [vl16] "v"(lane * 16), [vm] "v"(vm) : "memory", "scc", "s44")
#define W1_LOAD() asm volatile( \
  "s_nop 4\n\t" \
  "s_add_u32 s44, %[sW1], 0\n\t" \
  "buffer_load_dwordx4 a[0:3], %[vl16], %[srd], s44 offen offset:0 sc1\n\t" \
  "buffer_load_dwordx4 a[4:7], %[vl16], %[srd], s44 offen offset:1024 sc1\n\t" \
  "buffer_load_dwordx4 a[8:11], %[vl16], %[srd], s44 offen offset:2048 sc1\n\t" \
  "buffer_load_dwordx4 a[12:15], %[vl16], %[srd], s44 offen offset:3072 sc1\n\t" \
  "s_add_u32 s44, %[sW1], 4096\n\t" \
  "buffer_load_dwordx4 a[16:19], %[vl16], %[srd], s44 offen offset:0 sc1\n\t" \
  "buffer_load_dwordx4 a[20:23], %[vl16], %[srd], s44 offen offset:1024 sc1\n\t" \
  "buffer_load_dwordx4 a[24:27], %[vl16], %[srd], s44 offen offset:2048 sc1\n\t" \
  "buffer_load_dwordx4 a[28:31], %[vl16], %[srd], s44 offen offset:3072 sc1\n\t" \
  "s_add_u32 s44, %[sW1], 8192\n\t" \
  "buffer_load_dwordx4 a[32:35], %[vl16], %[srd], s44 offen offset:0 sc1\n\t" \
  "buffer_load_dwordx4 a[36:39], %[vl16], %[srd], s44 offen offset:1024 sc1\n\t" \
  "buffer_load_dwordx4 a[40:43], %[vl16], %[srd], s44 offen offset:2048 sc1\n\t" \
  "buffer_load_dwordx4 a[44:47], %[vl16], %[srd], s44 offen offset:3072 sc1\n\t" \
  "s_add_u32 s44, %[sW1], 12288\n\t" \
  "buffer_load_dwordx4 a[48:51], %[vl16], %[srd], s44 offen offset:0 sc1\n\t" \
  "buffer_load_dwordx4 a[52:55], %[vl16], %[srd], s44 offen offset:1024 sc1\n\t" \
  "buffer_load_dwordx4 a[56:59], %[vl16], %[srd], s44 offen offset:2048 sc1\n\t" \
  "buffer_load_dwordx4 a[60:63], %[vl16], %[srd], s44 offen offset:3072 sc1\n\t" \
  "s_add_u32 s44, %[sW1], 16384\n\t" \
  "buffer_load_dwordx4 a[64:67], %[vl16], %[srd], s44 offen offset:0 sc1\n\t" \
  "buffer_load_dwordx4 a[68:71], %[vl16], %[srd], s44 offen offset:1024 sc1\n\t" \
  "buffer_load_dwordx4 a[72:75], %[vl16], %[srd], s44 offen offset:2048 sc1\n\t" \
  "buffer_load_dwordx4 a[76:79], %[vl16], %[srd], s44 offen offset:3072 sc1\n\t" \
  "s_add_u32 s44, %[sW1], 20480\n\t" \
  "buffer_load_dwordx4 a[80:83], %[vl16], %[srd], s44 offen offset:0 sc1\n\t" \
  "buffer_load_dwordx4 a[84:87], %[vl16], %[srd], s44 offen offset:1024 sc1\n\t" \
  "buffer_load_dwordx4 a[88:91], %[vl16], %[srd], s44 offen offset:2048 sc1\n\t" \
  "buffer_load_dwordx4 a[92:95], %[vl16], %[srd], s44 offen offset:3072 sc1\n\t" \
  "s_add_u32 s44, %[sW1], 24576\n\t" \
  "buffer_load_dwordx4 a[96:99], %[vl16], %[srd], s44 offen offset:0 sc1\n\t" \
  "buffer_load_dwordx4 a[100:103], %[vl16], %[srd], s44 offen offset:1024 sc1\n\t" \
  "buffer_load_dwordx4 a[104:107], %[vl16], %[srd], s44 offen offset:2048 sc1\n\t" \
  "buffer_load_dwordx4 a[108:111], %[vl16], %[srd], s44 offen offset:3072 sc1\n\t" \
  "s_add_u32 s44, %[sW1], 28672\n\t" \
  "buffer_load_dwordx4 a[112:115], %[vl16], %[srd], s44 offen offset:0 sc1\n\t" \
  "buffer_load_dwordx4 a[116:119], %[vl16], %[srd], s44 offen offset:1024 sc1\n\t" \
  "buffer_load_dwordx4 a[120:123], %[vl16], %[srd], s44 offen offset:2048 sc1\n\t" \
  "buffer_load_dwordx4 a[124:127], %[vl16], %[srd], s44 offen offset:3072 sc1\n\t" \
  "s_add_u32 s44, %[sW1], 32768\n\t" \
  "buffer_load_dwordx4 a[128:131], %[vl16], %[srd], s44 offen offset:0 sc1\n\t" \
  "buffer_load_dwordx4 a[132:135], %[vl16], %[srd], s44 offen offset:1024 sc1\n\t" \
  "buffer_load_dwordx4 a[136:139], %[vl16], %[srd], s44 offen offset:2048 sc1\n\t" \
  "buffer_load_dwordx4 a[140:143], %[vl16], %[srd], s44 offen offset:3072 sc1\n\t" \
  "s_add_u32 s44, %[sW1], 36864\n\t" \
  "buffer_load_dwordx4 a[144:147], %[vl16], %[srd], s44 offen offset:0 sc1\n\t" \
  "buffer_load_dwordx4 a[148:151], %[vl16], %[srd], s44 offen offset:1024 sc1\n\t" \
  "buffer_load_dwordx4 a[152:155], %[vl16], %[srd], s44 offen offset:2048 sc1\n\t" \
  "buffer_load_dwordx4 a[156:159], %[vl16], %[srd], s44 offen offset:3072 sc1\n\t" \
  "s_add_u32 s44, %[sW1], 40960\n\t" \
  "buffer_load_dwordx4 a[160:163], %[vl16], %[srd], s44 offen offset:0 sc1\n\t" \
  "buffer_load_dwordx4 a[164:167], %[vl16], %[srd], s44 offen offset:1024 sc1\n\t" \
  "buffer_load_dwordx4 a[168:171], %[vl16], %[srd], s44 offen offset:2048 sc1\n\t" \
  "buffer_load_dwordx4 a[172:175], %[vl16], %[srd], s44 offen offset:3072 sc1\n\t" \
  "s_add_u32 s44, %[sW1], 45056\n\t" \
  "buffer_load_dwordx4 a[176:179], %[vl16], %[srd], s44 offen offset:0 sc1\n\t" \
  "buffer_load_dwordx4 a[180:183], %[vl16], %[srd], s44 offen offset:1024 sc1\n\t" \
  "buffer_load_dwordx4 a[184:187], %[vl16], %[srd], s44 offen offset:2048 sc1\n\t" \
  "buffer_load_dwordx4 a[188:191], %[vl16], %[srd], s44 offen offset:3072 sc1\n\t" \
  "s_add_u32 s44, %[sW1], 49152\n\t" \
  "buffer_load_dwordx4 a[192:195], %[vl16], %[srd], s44 offen offset:0 sc1\n\t" \
  "buffer_load_dwordx4 a[196:199], %[vl16], %[srd], s44 offen offset:1024 sc1\n\t" \
  "buffer_load_dwordx4 a[200:203], %[vl16], %[srd], s44 offen offset:2048 sc1\n\t" \
  "buffer_load_dwordx4 a[204:207], %[vl16], %[srd], s44 offen offset:3072 sc1\n\t" \
  "s_add_u32 s44, %[sW1], 53248\n\t" \
  "buffer_load_dwordx4 a[208:211], %[vl16], %[srd], s44 offen offset:0 sc1\n\t" \
  "buffer_load_dwordx4 a[212:215], %[vl16], %[srd], s44 offen offset:1024 sc1\n\t" \
  "buffer_load_dwordx4 a[216:219], %[vl16], %[srd], s44 offen offset:2048 sc1\n\t" \
  "buffer_load_dwordx4 a[220:223], %[vl16], %[srd], s44 offen offset:3072 sc1\n\t" \
  "s_add_u32 s44, %[sW1], 57344\n\t" \
  "buffer_load_dwordx4 a[224:227], %[vl16], %[srd], s44 offen offset:0 sc1\n\t" \
  "buffer_load_dwordx4 a[228:231], %[vl16], %[srd], s44 offen offset:1024 sc1\n\t" \
  "buffer_load_dwordx4 a[232:235], %[vl16], %[srd], s44 offen offset:2048 sc1\n\t" \
  "buffer_load_dwordx4 a[236:239], %[vl16], %[srd], s44 offen offset:3072 sc1\n\t" \
        :: [srd] "s"(srd), [sW1] "s"(OFF_W1F + (j * 4 + w) * 61440u), [vl16] "v"(lane * 16) : "memory", "scc", \
"a0", "a1", "a2", "a3", "a4", "a5", "a6", "a7", "a8", "a9", "a10", "a11", "a12", "a13", "a14", "a15", "a16", "a17", "a18", "a19", "a20", "a21", "a22", "a23", "a24", "a25", "a26", "a27", "a28", "a29", "a30", "a31", "a32", "a33", "a34", "a35", "a36", "a37", "a38", "a39", "a40", "a41", "a42", "a43", "a44", "a45", "a46", "a47", "a48", "a49", "a50", "a51", "a52", "a53", "a54", "a55", "a56", "a57", "a58", "a59", "a60", "a61", "a62", "a63", "a64", "a65", "a66", "a67", "a68", "a69", "a70", "a71", "a72", "a73", "a74", "a75", "a76", "a77", "a78", "a79", "a80", "a81", "a82", "a83", "a84", "a85", "a86", "a87", "a88", "a89", "a90", "a91", "a92", "a93", "a94", "a95", "a96", "a97", "a98", "a99", "a100", "a101", "a102", "a103", "a104", "a105", "a106", "a107", "a108", "a109", "a110", "a111", "a112", "a113", "a114", "a115", "a116", "a117", "a118", "a119", "a120", "a121", "a122", "a123", "a124", "a125", "a126", "a127", "a128", "a129", "a130", "a131", "a132", "a133", "a134", "a135", "a136", "a137", "a138", "a139", "a140", "a141", "a142", "a143", "a144", "a145", "a146", "a147", "a148", "a149", "a150", "a151", "a152", "a153", "a154", "a155", "a156", "a157", "a158", "a159", "a160", "a161", "a162", "a163", "a164", "a165", "a166", "a167", "a168", "a169", "a170", "a171", "a172", "a173", "a174", "a175", "a176", "a177", "a178", "a179", "a180", "a181", "a182", "a183", "a184", "a185", "a186", "a187", "a188", "a189", "a190", "a191", "a192", "a193", "a194", "a195", "a196", "a197", "a198", "a199", "a200", "a201", "a202", "a203", "a204", "a205", "a206", "a207", "a208", "a209", "a210", "a211", "a212", "a213", "a214", "a215", "a216", "a217", "a218", "a219", "a220", "a221", "a222", "a223", "a224", "a225", "a226", "a227", "a228", "a229", "a230", "a231", "a232", "a233", "a234", "a235", "a236", "a237", "a238", "a239", "s44" \
      )
#define GEMV_UNIT(u_, lu_, younger_) asm volatile( \
  "s_nop 4\n\t" \
  "v_lshlrev_b32 v176, 1, %[vl]\n\t" \
  "v_add_u32 v176, %[lu], v176\n\t" \
  "v_mov_b32 v96, 0\n\t" \
  "v_mov_b32 v97, 0\n\t" \
  "v_mov_b32 v98, 0\n\t" \
  "s_waitcnt vmcnt(%c[wa])\n\t" \
  "ds_read_u16_d16_hi v96, v176 offset:20480\n\t" \
  "ds_read_u16_d16_hi v97, v176 offset:20608\n\t" \
  "ds_read_u16_d16_hi v98, v176 offset:20736\n\t" \
  "s_waitcnt lgkmcnt(2)\n\t" \
  "v_mul_f32 v177, 0xbfb8aa3b, v96\n\t" \
  "s_waitcnt lgkmcnt(1)\n\t" \
  "v_mul_f32 v178, 0xbfb8aa3b, v97\n\t" \
  "s_waitcnt lgkmcnt(0)\n\t" \
  "v_mul_f32 v179, 0xbfb8aa3b, v98\n\t" \
  "v_exp_f32 v177, v177\n\t" \
  "v_exp_f32 v178, v178\n\t" \
  "v_exp_f32 v179, v179\n\t" \
  "v_add_f32 v177, 1.0, v177\n\t" \
  "v_add_f32 v178, 1.0, v178\n\t" \
  "v_add_f32 v179, 1.0, v179\n\t" \
  "v_rcp_f32 v177, v177\n\t" \
  "v_rcp_f32 v178, v178\n\t" \
  "v_rcp_f32 v179, v179\n\t" \
  "v_mul_f32 v177, v177, v96\n\t" \
  "v_mul_f32 v178, v178, v97\n\t" \
  "v_mul_f32 v179, v179, v98\n\t" \
  "v_cvt_pk_bf16_f32 v177, v177, v177\n\t" \
  "v_cvt_pk_bf16_f32 v178, v178, v178\n\t" \
  "v_cvt_pk_bf16_f32 v179, v179, v179\n\t" \
  "ds_write_b16 v176, v177 offset:21504\n\t" \
  "ds_write_b16 v176, v178 offset:21632\n\t" \
  "ds_write_b16 v176, v179 offset:21760\n\t" \
  "v_lshrrev_b32 v180, 4, %[vl]\n\t" \
  "v_lshlrev_b32 v180, 3, v180\n\t" \
  "v_add_u32 v180, %[lu], v180\n\t" \
  "ds_read_b64 v[100:101], v180 offset:21504\n\t" \
  "ds_read_b64 v[102:103], v180 offset:21536\n\t" \
  "ds_read_b64 v[104:105], v180 offset:21568\n\t" \
  "ds_read_b64 v[106:107], v180 offset:21600\n\t" \
  "ds_read_b64 v[108:109], v180 offset:21632\n\t" \
  "ds_read_b64 v[110:111], v180 offset:21664\n\t" \
  "ds_read_b64 v[112:113], v180 offset:21696\n\t" \
  "ds_read_b64 v[114:115], v180 offset:21728\n\t" \
  "ds_read_b64 v[116:117], v180 offset:21760\n\t" \
  "ds_read_b64 v[118:119], v180 offset:21792\n\t" \
  "v_xor_b32 v181, 32, %[vtm]\n\t" \
  "v_xor_b32 v182, 64, %[vtm]\n\t" \
  "v_xor_b32 v183, 96, %[vtm]\n\t" \
  "s_waitcnt vmcnt(%c[wb])\n\t" \
  "ds_read_b64_tr_b16 v[120:121], %[vtm] offset:0\n\t" \
  "ds_read_b64_tr_b16 v[122:123], %[vtm] offset:2048\n\t" \
  "ds_read_b64_tr_b16 v[124:125], v181 offset:0\n\t" \
  "ds_read_b64_tr_b16 v[126:127], v181 offset:2048\n\t" \
  "ds_read_b64_tr_b16 v[128:129], v182 offset:0\n\t" \
  "ds_read_b64_tr_b16 v[130:131], v182 offset:2048\n\t" \
  "ds_read_b64_tr_b16 v[132:133], v183 offset:0\n\t" \
  "ds_read_b64_tr_b16 v[134:135], v183 offset:2048\n\t" \
  "ds_read_b64_tr_b16 v[136:137], %[vtm] offset:4096\n\t" \
  "ds_read_b64_tr_b16 v[138:139], %[vtm] offset:6144\n\t" \
  "ds_read_b64_tr_b16 v[140:141], v181 offset:4096\n\t" \
  "ds_read_b64_tr_b16 v[142:143], v181 offset:6144\n\t" \
  "ds_read_b64_tr_b16 v[144:145], v182 offset:4096\n\t" \
  "ds_read_b64_tr_b16 v[146:147], v182 offset:6144\n\t" \
  "ds_read_b64_tr_b16 v[148:149], v183 offset:4096\n\t" \
  "ds_read_b64_tr_b16 v[150:151], v183 offset:6144\n\t" \
  "s_waitcnt lgkmcnt(14)\n\t" \
  "v_mfma_f32_16x16x32_bf16 v[152:155], v[100:103], v[120:123], 0\n\t" \
  "s_waitcnt lgkmcnt(12)\n\t" \
  "v_mfma_f32_16x16x32_bf16 v[156:159], v[100:103], v[124:127], 0\n\t" \
  "s_waitcnt lgkmcnt(10)\n\t" \
  "v_mfma_f32_16x16x32_bf16 v[160:163], v[100:103], v[128:131], 0\n\t" \
  "s_waitcnt lgkmcnt(8)\n\t" \
  "v_mfma_f32_16x16x32_bf16 v[164:167], v[100:103], v[132:135], 0\n\t" \
  "ds_read_b64_tr_b16 v[120:121], %[vtm] offset:8192\n\t" \
  "ds_read_b64_tr_b16 v[122:123], %[vtm] offset:10240\n\t" \
  "ds_read_b64_tr_b16 v[124:125], v181 offset:8192\n\t" \
  "ds_read_b64_tr_b16 v[126:127], v181 offset:10240\n\t" \
  "ds_read_b64_tr_b16 v[128:129], v182 offset:8192\n\t" \
  "ds_read_b64_tr_b16 v[130:131], v182 offset:10240\n\t" \
  "ds_read_b64_tr_b16 v[132:133], v183 offset:8192\n\t" \
  "ds_read_b64_tr_b16 v[134:135], v183 offset:10240\n\t" \
  "s_waitcnt lgkmcnt(14)\n\t" \
  "v_mfma_f32_16x16x32_bf16 v[152:155], v[104:107], v[136:139], v[152:155]\n\t" \
  "s_waitcnt lgkmcnt(12)\n\t" \
  "v_mfma_f32_16x16x32_bf16 v[156:159], v[104:107], v[140:143], v[156:159]\n\t" \
  "s_waitcnt lgkmcnt(10)\n\t" \
  "v_mfma_f32_16x16x32_bf16 v[160:163], v[104:107], v[144:147], v[160:163]\n\t" \
  "s_waitcnt lgkmcnt(8)\n\t" \
  "v_mfma_f32_16x16x32_bf16 v[164:167], v[104:107], v[148:151], v[164:167]\n\t" \
  "ds_read_b64_tr_b16 v[136:137], %[vtm] offset:12288\n\t" \
  "ds_read_b64_tr_b16 v[138:139], %[vtm] offset:14336\n\t" \
  "ds_read_b64_tr_b16 v[140:141], v181 offset:12288\n\t" \
  "ds_read_b64_tr_b16 v[142:143], v181 offset:14336\n\t" \
  "ds_read_b64_tr_b16 v[144:145], v182 offset:12288\n\t" \
  "ds_read_b64_tr_b16 v[146:147], v182 offset:14336\n\t" \
  "ds_read_b64_tr_b16 v[148:149], v183 offset:12288\n\t" \
  "ds_read_b64_tr_b16 v[150:151], v183 offset:14336\n\t" \
  "s_waitcnt lgkmcnt(14)\n\t" \
  "v_mfma_f32_16x16x32_bf16 v[152:155], v[108:111], v[120:123], v[152:155]\n\t" \
  "s_waitcnt lgkmcnt(12)\n\t" \
  "v_mfma_f32_16x16x32_bf16 v[156:159], v[108:111], v[124:127], v[156:159]\n\t" \
  "s_waitcnt lgkmcnt(10)\n\t" \
  "v_mfma_f32_16x16x32_bf16 v[160:163], v[108:111], v[128:131], v[160:163]\n\t" \
  "s_waitcnt lgkmcnt(8)\n\t" \
  "v_mfma_f32_16x16x32_bf16 v[164:167], v[108:111], v[132:135], v[164:167]\n\t" \
  "ds_read_b64_tr_b16 v[120:121], %[vtm] offset:16384\n\t" \
  "ds_read_b64_tr_b16 v[122:123], %[vtm] offset:18432\n\t" \
  "ds_read_b64_tr_b16 v[124:125], v181 offset:16384\n\t" \
  "ds_read_b64_tr_b16 v[126:127], v181 offset:18432\n\t" \
  "ds_read_b64_tr_b16 v[128:129], v182 offset:16384\n\t" \
  "ds_read_b64_tr_b16 v[130:131], v182 offset:18432\n\t" \
  "ds_read_b64_tr_b16 v[132:133], v183 offset:16384\n\t" \
  "ds_read_b64_tr_b16 v[134:135], v183 offset:18432\n\t" \
  "s_waitcnt lgkmcnt(14)\n\t" \
  "v_mfma_f32_16x16x32_bf16 v[152:155], v[112:115], v[136:139], v[152:155]\n\t" \
  "s_waitcnt lgkmcnt(12)\n\t" \
  "v_mfma_f32_16x16x32_bf16 v[156:159], v[112:115], v[140:143], v[156:159]\n\t" \
  "s_waitcnt lgkmcnt(10)\n\t" \
  "v_mfma_f32_16x16x32_bf16 v[160:163], v[112:115], v[144:147], v[160:163]\n\t" \
  "s_waitcnt lgkmcnt(8)\n\t" \
  "v_mfma_f32_16x16x32_bf16 v[164:167], v[112:115], v[148:151], v[164:167]\n\t" \
  "s_waitcnt lgkmcnt(6)\n\t" \
  "v_mfma_f32_16x16x32_bf16 v[152:155], v[116:119], v[120:123], v[152:155]\n\t" \
  "s_waitcnt lgkmcnt(4)\n\t" \
  "v_mfma_f32_16x16x32_bf16 v[156:159], v[116:119], v[124:127], v[156:159]\n\t" \
  "s_waitcnt lgkmcnt(2)\n\t" \
  "v_mfma_f32_16x16x32_bf16 v[160:163], v[116:119], v[128:131], v[160:163]\n\t" \
  "s_waitcnt lgkmcnt(0)\n\t" \
  "v_mfma_f32_16x16x32_bf16 v[164:167], v[116:119], v[132:135], v[164:167]\n\t" \
  "v_lshlrev_b32 v176, 2, %[vl]\n\t" \
  "s_nop 2\n\t" \
  "v_mov_b32_dpp v152, v156 quad_perm:[0,1,2,3] row_mask:0x2 bank_mask:0xf\n\t" \
  "s_nop 1\n\t" \
  "v_mov_b32_dpp v152, v160 quad_perm:[0,1,2,3] row_mask:0x4 bank_mask:0xf\n\t" \
  "s_nop 1\n\t" \
  "v_mov_b32_dpp v152, v164 quad_perm:[0,1,2,3] row_mask:0x8 bank_mask:0xf\n\t" \
  "buffer_store_dword v152, v176, %[srd], %[sMo] offen sc1\n\t" \
  "s_nop 1\n\t" \
        :: [srd] "s"(srd), [lu] "s"(lu_), [wa] "i"((younger_) + 20), [wb] "i"(younger_), [sMo] "s"(OFF_MODP + U_KQ(u_) * 15360u + U_CG(u_) * 256u), [vl] "v"(lane), \
           [vtm] "v"((lu_) + (lane >> 2) * 128 + ((lane >> 3) & 3) * 32 + (lane & 3) * 8) : "memory", "vcc", "scc", \
"v96", "v97", "v98", "v100", "v101", "v102", "v103", "v104", "v105", "v106", "v107", "v108", "v109", "v110", "v111", "v112", "v113", "v114", "v115", "v116", "v117", "v118", "v119", "v120", "v121", "v122", "v123", "v124", "v125", "v126", "v127", "v128", "v129", "v130", "v131", "v132", "v133", "v134", "v135", "v136", "v137", "v138", "v139", "v140", "v141", "v142", "v143", "v144", "v145", "v146", "v147", "v148", "v149", "v150", "v151", "v152", "v153", "v154", "v155", "v156", "v157", "v158", "v159", "v160", "v161", "v162", "v163", "v164", "v165", "v166", "v167", "v176", "v177", "v178", "v179", "v180", "v181", "v182", "v183" \
      )
  auto stash = [&]() {
    const unsigned vpub = role == 0 ? (tid < 8 * FX_R ? (TWO ? OFF_FX1 : OFF_FX) + ((tid >> 3) * 160 + cb) * 128 + (tid & 7) * 16 : OOB) : tid < 8 ? (role == 1 ? OFF_FU + w1 * 128 : OFF_FW2 + w2 * 128) + tid * 16 : OOB;
    const unsigned vpw = lane < 4 ? OFF_FW1 + (j * 4 + lane) * 128 : lane < 8 ? OFF_FW2 + ((4 * w + (lane & 3)) * 2 + (ng >> 2)) * 128
                                                                              : OFF_FU + ((lane < 12 ? j * 4 : 0) + (lane & 3)) * 128;
    const unsigned pb = 32 * (lane >> 3) + 8 * w + (lane & 7);
    const unsigned vpx = lane < 40 && !(role == 0 && pb == cb) ? OFF_FX + (xcd % FX_R * 160 + pb) * 128 : OFF_FR + min(lane - 40, 9u) * 128;
    const unsigned g = lane >> 4, q = (lane >> 2) & 3, pq = lane & 3;
    const unsigned row = 4 * g + q;
    const unsigned vtr = lb + row * 128 + ((row >> 1) & 3) * 32 + ((pq * 8) ^ ((row & 8) * 2));
    const unsigned vl16 = lane * 16;
    const unsigned vdma = OFF_X2P + p * 81920u + w * 4096u + lane * 16;
    const unsigned vh2st = lane < 48 ? OFF_H2F + (4 * p * 192 + 12 * j + 3 * w) * 256u + lane * 16 : OOB;
    const unsigned tk = 4 * j + w;
    const unsigned vstc = lb + lane * 128 + (((tk >> 4) ^ ((lane >> 1) & 3)) * 32) + (((tk & 15) * 2) ^ ((lane & 8) * 2));
    const unsigned vstw = lane == 63 ? tk * 64 : OOB;
    const unsigned vflw = lane < 8 ? OFF_FL + p * 32768u + (j * 4 + w) * 128u + lane * 16 : OOB;
    const unsigned vep = b == 0 && tid == 0 ? OFF_EP : OOB;
    const unsigned rs_ = 2 * (w & 1) + sub, rb = lb + (w >> 1) * 81920 + 80 * ng * 128;
    const unsigned r0 = 8 * g + q, r1 = 8 * g + 4 + q, r2 = 64 + 4 * g + q;
    const unsigned vres0 = rb + r0 * 128 + ((rs_ ^ ((r0 >> 1) & 3)) * 32) + ((pq * 8) ^ ((r0 & 8) * 2));
    const unsigned vres1 = rb + r1 * 128 + ((rs_ ^ ((r1 >> 1) & 3)) * 32) + ((pq * 8) ^ ((r1 & 8) * 2));
    const unsigned vres2 = rb + r2 * 128 + ((rs_ ^ ((r2 >> 1) & 3)) * 32) + ((pq * 8) ^ ((r2 & 8) * 2));
    const unsigned vwr = lb + w * 20480 + g * 256 + ((lane + 4 * g) & 15) * 16;
    const unsigned tq = lane < 32 ? q : (lane >> 1) & 3, tl = 4 * w + tq, g0 = lane < 32 ? pq : 2 * (lane & 1), g1 = lane < 32 ? pq : g0 + 1;
    const unsigned vrd0 = lb + (lane < 32 ? g * 2048 : 4096) + g0 * 256 + ((tl + 4 * g0) & 15) * 16;
    const unsigned vrd1 = lb + (lane < 32 ? g * 2048 + 1024 : 4096) + g1 * 256 + ((tl + 4 * g1) & 15) * 16;
    const unsigned vh1w = lane < 40 ? OFF_FX1 + (xcd % FX_R * 160 + pb) * 128 : OFF_FU + ((lane < 44 ? j * 4 : 0) + (lane & 3)) * 128;
    const unsigned vgb = OFF_GB - OFF_RT + (lane < 32 ? 32 * g + 8 * pq : 64 + 8 * (lane & 1)) * 4;
    const unsigned vout = lane < 40 ? ((64 * p + 16 * sub + tl) * D_ + 80 * ng + (lane < 32 ? 32 * g + 8 * pq : 64 + 8 * (lane & 1))) * 2 : OOB;
    *(u32x4*)(smem + L_STASH + 0 + tid * 16) = u32x4{vpub, vl16, vpw, vpx};
    *(u32x4*)(smem + L_STASH + 4096 + tid * 16) = u32x4{vdma, vtr, vstc, vstw};
    *(u32x4*)(smem + L_STASH + 8192 + tid * 16) = u32x4{vh2st, vflw, vres0, vres1};
    *(u32x4*)(smem + L_STASH + 12288 + tid * 16) = u32x4{vres2, vwr, vrd0, vrd1};
    *(u32x4*)(smem + L_STASH + 16384 + tid * 16) = u32x4{vout, vgb, vep, vh1w};
    FENCE();
  };
  unsigned c_vf2;
  unsigned c_vf1, c_vp0;
  u64 pmask = 0;
  unsigned c_vf3;
  unsigned c_vfw;
  unsigned c_va;
  unsigned c_vb0;
  unsigned c_vb1;
  unsigned c_vb2;
  unsigned c_vb3;
  unsigned c_vx;
  unsigned c_vs;
  unsigned c_vs2;
  unsigned c_vo;
  u32x4 c_vd;
  unsigned c_vl16;
  unsigned c_vkv;
  unsigned c_vmod;
  u64 tag;
  if (__builtin_expect(role == 0, CONV_FIRST)) {
    const rsrc_t rf = mkrsrc(ssmk, S_ * D_ * 2);
    const unsigned d0 = 128 * (cb >> 5) + 2 * lane;
    u32x4 fa[4];
    const u32x4 taps = *(const u32x4*)(shk + d0 * 4);
    dval = ssmD[8 * (cb >> 1) + 2 * w + (cb & 1)];
    FENCE();
    __builtin_amdgcn_raw_buffer_store_b128(u32x4{0, 0, 0, 0}, srd, tid < 64 && cb < 128 ? OFF_ST2 + cb * 1024 + tid * 16 : OOB, 0, SC1);
    FENCE();
#pragma unroll
    for (int n = 0; n < 4; ++n) fa[n] = __builtin_amdgcn_raw_buffer_load_b128(rf, lane * 16 + n * 1024, (8 * (cb >> 1) + 2 * w + (cb & 1)) * 4096u, NT);
    u32x4 xr[5];
    const bool isx = cb < 128;
    const rsrc_t rx = mkrsrc(isx ? x : wmod, isx ? S_ * D_ * 2 : D_ * 3840 * 2);
#pragma unroll
    for (int n = 0; n < 5; ++n)
      xr[n] = __builtin_amdgcn_raw_buffer_load_b128(rx, isx ? (lane >> 3) * 1280 + (lane & 7) * 16 : vm,
                                                    isx ? cb * 20480u + (w & 1) * 10240u + (w >> 1) * 128u + n * 256u : U_SRC(cb) + (4 * n + w) * 61440u, NT);
    const u32x4 cr = __builtin_amdgcn_raw_buffer_load_b128(mkrsrc(cIn, isx ? 0 : D_ * 2), lane * 16, U_KQ(cb) * 320u, 0);
    FENCE();
    STAMP(6);
    tag = load_tag(ws);
    if (w < 2) *(u32x4*)(smem + L_KV + tid * 16) = zero4;
    if (tid == 128) *(u32x4*)(smem + L_CNT) = zero4;
    {
      const unsigned d = 8 * (cb >> 1) + 2 * w + (cb & 1);
      const unsigned r = lane & 15, g = lane >> 4;
      const unsigned pp = 25 + r - 8 * g;
      const unsigned va = lb + L_FC + w * FC_CH + (pp & 3) * FC_PITCH + (pp & ~3u) * 2;
      unsigned vb[4];
  #pragma unroll
      for (int k = 0; k < 4; ++k) { const unsigned G = 14 - 4 * k + 2 * r + g; vb[k] = lb + L_KV + G * 64 + ((w ^ (G >> 2)) & 3) * 16; }
      const unsigned gx = (2 * r + (g >> 1)) * 64 + ((w ^ (r >> 1)) & 3) * 16;
      const unsigned vx = lb + L_X + gx + (g & 1) * 8, vs = lb + L_ST + r * 128 + ((2 * g) ^ stkey(r)) * 16;
      const unsigned to = 256 * (g & 1) + 16 * r + 8 * (g >> 1);
      const unsigned vo = (to >> 6) * 81920u + (((to & 63) * 2) ^ (((d >> 1) & 3) * 32 + (d & 8) * 2));
      const unsigned wh = (lane >> 2) & 3, mcol = (wh < 3 ? wh : 4) * 640 + d;
      const unsigned jd = 8 * g - 9 - r, dv = dval << (16 * (jd & 1));
      const u32x4 vd = {jd >> 1 == 0 ? dv : 0u, jd >> 1 == 1 ? dv : 0u, jd >> 1 == 2 ? dv : 0u, jd >> 1 == 3 ? dv : 0u};
      c_vf2 = lane < 32 ? OFF_FA3 + (32 * w + lane) * 128 : OFF_FA2 + ((lane & 3) * 60 + (mcol >> 6)) * 128;
      c_vp0 = tid < 8 * FX_R ? OFF_FX + ((tid >> 3) * 160 + cb) * 128 + (tid & 7) * 16 : OOB;
      c_vf1 = OFF_FA1 + ((cb & ~31u) + 8 * w + (lane & 7)) * 128;
      c_vf3 = lane < 8 ? (cb < 128 ? OFF_FA3 + cb * 128 : U_FLAG(cb)) + lane * 16 : OOB;
      c_vfw = OFF_FW1 + (j * 4 + (lane & 3)) * 128;
      c_va = va;
      c_vb0 = vb[0];
      c_vb1 = vb[1];
      c_vb2 = vb[2];
      c_vb3 = vb[3];
      c_vx = vx;
      c_vs = vs;
      c_vs2 = vs ^ 16;
      c_vo = vo;
      c_vd = vd;
      c_vl16 = lane * 16;
      c_vkv = (lane >> 3) * 20480 + (lane & 7) * 16;
      c_vmod = OFF_MODP + ((lane & 3) * 3840 + mcol) * 4;
      asm volatile("" : "+v"(c_vf2), "+v"(c_vf1), "+v"(c_vp0), "+v"(c_vf3), "+v"(c_vfw), "+v"(c_va), "+v"(c_vb0), "+v"(c_vb1), "+v"(c_vb2), "+v"(c_vb3), "+v"(c_vx), "+v"(c_vs), "+v"(c_vs2), "+v"(c_vo), "+v"(c_vd), "+v"(c_vl16), "+v"(c_vkv), "+v"(c_vmod));
    }
    FENCE();
    asm volatile("s_waitcnt vmcnt(11)" ::: "memory");
    STAMP(7);
    asm volatile(
  "s_nop 1\n\t"
  "ds_read2st64_b32 v[112:113], %[la] offset0:0 offset1:1\n\t"
  "ds_read2st64_b32 v[114:115], %[la] offset0:2 offset1:3\n\t"
  "ds_read2st64_b32 v[116:117], %[la] offset0:4 offset1:5\n\t"
  "ds_read2st64_b32 v[118:119], %[la] offset0:6 offset1:7\n\t"
  "ds_read2st64_b32 v[120:121], %[la] offset0:8 offset1:9\n\t"
  "ds_read2st64_b32 v[122:123], %[la] offset0:10 offset1:11\n\t"
  "ds_read2st64_b32 v[132:133], %[la] offset0:20 offset1:21\n\t"
  "ds_read2st64_b32 v[134:135], %[la] offset0:22 offset1:23\n\t"
  "ds_read2st64_b32 v[136:137], %[la] offset0:24 offset1:25\n\t"
  "ds_read2st64_b32 v[138:139], %[la] offset0:26 offset1:27\n\t"
  "ds_read2st64_b32 v[124:125], %[la] offset0:12 offset1:13\n\t"
  "ds_read2st64_b32 v[126:127], %[la] offset0:14 offset1:15\n\t"
  "ds_read2st64_b32 v[128:129], %[la] offset0:16 offset1:17\n\t"
  "ds_read2st64_b32 v[130:131], %[la] offset0:18 offset1:19\n\t"
  "ds_read2st64_b32 v[140:141], %[la] offset0:28 offset1:29\n\t"
  "ds_read2st64_b32 v[142:143], %[la] offset0:30 offset1:31\n\t"
  "ds_read2st64_b32 v[144:145], %[la] offset0:32 offset1:33\n\t"
  "ds_read2st64_b32 v[146:147], %[la] offset0:34 offset1:35\n\t"
  "v_lshlrev_b32 v186, 16, %[t0]\n\t"
  "v_lshlrev_b32 v187, 16, %[t2]\n\t"
  "v_and_b32 v188, 0xffff0000, %[t0]\n\t"
  "v_and_b32 v189, 0xffff0000, %[t2]\n\t"
  "v_lshlrev_b32 v190, 16, %[t1]\n\t"
  "v_lshlrev_b32 v191, 16, %[t3]\n\t"
  "v_and_b32 v192, 0xffff0000, %[t1]\n\t"
  "v_and_b32 v193, 0xffff0000, %[t3]\n\t"
  "v_mov_b32 v234, 0x3f803f80\n\t"
  "s_waitcnt lgkmcnt(15)\n\t"
  "v_lshlrev_b32 v148, 16, v113\n\t"
  "v_and_b32 v149, 0xffff0000, v113\n\t"
  "v_lshlrev_b32 v150, 16, v114\n\t"
  "v_and_b32 v151, 0xffff0000, v114\n\t"
  "v_lshlrev_b32 v152, 16, v115\n\t"
  "v_and_b32 v153, 0xffff0000, v115\n\t"
  "v_lshlrev_b32 v154, 16, v116\n\t"
  "v_and_b32 v155, 0xffff0000, v116\n\t"
  "v_lshlrev_b32 v156, 16, v117\n\t"
  "v_and_b32 v157, 0xffff0000, v117\n\t"
  "s_waitcnt lgkmcnt(14)\n\t"
  "v_lshlrev_b32 v158, 16, v118\n\t"
  "v_and_b32 v159, 0xffff0000, v118\n\t"
  "v_lshlrev_b32 v160, 16, v119\n\t"
  "v_and_b32 v161, 0xffff0000, v119\n\t"
  "s_waitcnt lgkmcnt(13)\n\t"
  "v_lshlrev_b32 v162, 16, v120\n\t"
  "v_and_b32 v163, 0xffff0000, v120\n\t"
  "v_lshlrev_b32 v164, 16, v121\n\t"
  "v_and_b32 v165, 0xffff0000, v121\n\t"
  "s_waitcnt lgkmcnt(12)\n\t"
  "v_lshlrev_b32 v166, 16, v122\n\t"
  "v_and_b32 v167, 0xffff0000, v122\n\t"
  "v_lshlrev_b32 v168, 16, v123\n\t"
  "v_and_b32 v169, 0xffff0000, v123\n\t"
  "v_pk_mul_f32 v[194:195], v[148:149], v[192:193]\n\t"
  "v_pk_mul_f32 v[196:197], v[150:151], v[192:193]\n\t"
  "v_pk_mul_f32 v[198:199], v[152:153], v[192:193]\n\t"
  "v_pk_mul_f32 v[200:201], v[154:155], v[192:193]\n\t"
  "v_pk_mul_f32 v[202:203], v[156:157], v[192:193]\n\t"
  "v_pk_mul_f32 v[204:205], v[158:159], v[192:193]\n\t"
  "v_pk_mul_f32 v[206:207], v[160:161], v[192:193]\n\t"
  "v_pk_mul_f32 v[208:209], v[162:163], v[192:193]\n\t"
  "v_pk_mul_f32 v[210:211], v[150:151], v[190:191]\n\t"
  "v_pk_mul_f32 v[212:213], v[152:153], v[190:191]\n\t"
  "v_pk_mul_f32 v[214:215], v[154:155], v[190:191]\n\t"
  "v_pk_mul_f32 v[216:217], v[156:157], v[190:191]\n\t"
  "v_pk_mul_f32 v[218:219], v[158:159], v[190:191]\n\t"
  "v_pk_mul_f32 v[220:221], v[160:161], v[190:191]\n\t"
  "v_pk_mul_f32 v[222:223], v[162:163], v[190:191]\n\t"
  "v_pk_mul_f32 v[224:225], v[164:165], v[190:191]\n\t"
  "v_cvt_pk_bf16_f32 v194, v194, v210\n\t"
  "v_cvt_pk_bf16_f32 v195, v195, v211\n\t"
  "v_cvt_pk_bf16_f32 v196, v196, v212\n\t"
  "v_cvt_pk_bf16_f32 v197, v197, v213\n\t"
  "v_cvt_pk_bf16_f32 v198, v198, v214\n\t"
  "v_cvt_pk_bf16_f32 v199, v199, v215\n\t"
  "v_cvt_pk_bf16_f32 v200, v200, v216\n\t"
  "v_cvt_pk_bf16_f32 v201, v201, v217\n\t"
  "v_cvt_pk_bf16_f32 v202, v202, v218\n\t"
  "v_cvt_pk_bf16_f32 v203, v203, v219\n\t"
  "v_cvt_pk_bf16_f32 v204, v204, v220\n\t"
  "v_cvt_pk_bf16_f32 v205, v205, v221\n\t"
  "v_cvt_pk_bf16_f32 v206, v206, v222\n\t"
  "v_cvt_pk_bf16_f32 v207, v207, v223\n\t"
  "v_cvt_pk_bf16_f32 v208, v208, v224\n\t"
  "v_cvt_pk_bf16_f32 v209, v209, v225\n\t"
  "v_dot2_f32_bf16 v194, v194, v234, 0\n\t"
  "v_dot2_f32_bf16 v195, v195, v234, 0\n\t"
  "v_dot2_f32_bf16 v196, v196, v234, 0\n\t"
  "v_dot2_f32_bf16 v197, v197, v234, 0\n\t"
  "v_dot2_f32_bf16 v198, v198, v234, 0\n\t"
  "v_dot2_f32_bf16 v199, v199, v234, 0\n\t"
  "v_dot2_f32_bf16 v200, v200, v234, 0\n\t"
  "v_dot2_f32_bf16 v201, v201, v234, 0\n\t"
  "v_dot2_f32_bf16 v202, v202, v234, 0\n\t"
  "v_dot2_f32_bf16 v203, v203, v234, 0\n\t"
  "v_dot2_f32_bf16 v204, v204, v234, 0\n\t"
  "v_dot2_f32_bf16 v205, v205, v234, 0\n\t"
  "v_dot2_f32_bf16 v206, v206, v234, 0\n\t"
  "v_dot2_f32_bf16 v207, v207, v234, 0\n\t"
  "v_dot2_f32_bf16 v208, v208, v234, 0\n\t"
  "v_dot2_f32_bf16 v209, v209, v234, 0\n\t"
  "v_pk_mul_f32 v[210:211], v[152:153], v[188:189]\n\t"
  "v_pk_mul_f32 v[212:213], v[154:155], v[188:189]\n\t"
  "v_pk_mul_f32 v[214:215], v[156:157], v[188:189]\n\t"
  "v_pk_mul_f32 v[216:217], v[158:159], v[188:189]\n\t"
  "v_pk_mul_f32 v[218:219], v[160:161], v[188:189]\n\t"
  "v_pk_mul_f32 v[220:221], v[162:163], v[188:189]\n\t"
  "v_pk_mul_f32 v[222:223], v[164:165], v[188:189]\n\t"
  "v_pk_mul_f32 v[224:225], v[166:167], v[188:189]\n\t"
  "v_cvt_pk_bf16_f32 v194, v194, v210\n\t"
  "v_cvt_pk_bf16_f32 v195, v195, v211\n\t"
  "v_cvt_pk_bf16_f32 v196, v196, v212\n\t"
  "v_cvt_pk_bf16_f32 v197, v197, v213\n\t"
  "v_cvt_pk_bf16_f32 v198, v198, v214\n\t"
  "v_cvt_pk_bf16_f32 v199, v199, v215\n\t"
  "v_cvt_pk_bf16_f32 v200, v200, v216\n\t"
  "v_cvt_pk_bf16_f32 v201, v201, v217\n\t"
  "v_cvt_pk_bf16_f32 v202, v202, v218\n\t"
  "v_cvt_pk_bf16_f32 v203, v203, v219\n\t"
  "v_cvt_pk_bf16_f32 v204, v204, v220\n\t"
  "v_cvt_pk_bf16_f32 v205, v205, v221\n\t"
  "v_cvt_pk_bf16_f32 v206, v206, v222\n\t"
  "v_cvt_pk_bf16_f32 v207, v207, v223\n\t"
  "v_cvt_pk_bf16_f32 v208, v208, v224\n\t"
  "v_cvt_pk_bf16_f32 v209, v209, v225\n\t"
  "v_dot2_f32_bf16 v194, v194, v234, 0\n\t"
  "v_dot2_f32_bf16 v195, v195, v234, 0\n\t"
  "v_dot2_f32_bf16 v196, v196, v234, 0\n\t"
  "v_dot2_f32_bf16 v197, v197, v234, 0\n\t"
  "v_dot2_f32_bf16 v198, v198, v234, 0\n\t"
  "v_dot2_f32_bf16 v199, v199, v234, 0\n\t"
  "v_dot2_f32_bf16 v200, v200, v234, 0\n\t"
  "v_dot2_f32_bf16 v201, v201, v234, 0\n\t"
  "v_dot2_f32_bf16 v202, v202, v234, 0\n\t"
  "v_dot2_f32_bf16 v203, v203, v234, 0\n\t"
  "v_dot2_f32_bf16 v204, v204, v234, 0\n\t"
  "v_dot2_f32_bf16 v205, v205, v234, 0\n\t"
  "v_dot2_f32_bf16 v206, v206, v234, 0\n\t"
  "v_dot2_f32_bf16 v207, v207, v234, 0\n\t"
  "v_dot2_f32_bf16 v208, v208, v234, 0\n\t"
  "v_dot2_f32_bf16 v209, v209, v234, 0\n\t"
  "v_pk_mul_f32 v[210:211], v[154:155], v[186:187]\n\t"
  "v_pk_mul_f32 v[212:213], v[156:157], v[186:187]\n\t"
  "v_pk_mul_f32 v[214:215], v[158:159], v[186:187]\n\t"
  "v_pk_mul_f32 v[216:217], v[160:161], v[186:187]\n\t"
  "v_pk_mul_f32 v[218:219], v[162:163], v[186:187]\n\t"
  "v_pk_mul_f32 v[220:221], v[164:165], v[186:187]\n\t"
  "v_pk_mul_f32 v[222:223], v[166:167], v[186:187]\n\t"
  "v_pk_mul_f32 v[224:225], v[168:169], v[186:187]\n\t"
  "v_cvt_pk_bf16_f32 v194, v194, v210\n\t"
  "v_cvt_pk_bf16_f32 v195, v195, v211\n\t"
  "v_cvt_pk_bf16_f32 v196, v196, v212\n\t"
  "v_cvt_pk_bf16_f32 v197, v197, v213\n\t"
  "v_cvt_pk_bf16_f32 v198, v198, v214\n\t"
  "v_cvt_pk_bf16_f32 v199, v199, v215\n\t"
  "v_cvt_pk_bf16_f32 v200, v200, v216\n\t"
  "v_cvt_pk_bf16_f32 v201, v201, v217\n\t"
  "v_cvt_pk_bf16_f32 v202, v202, v218\n\t"
  "v_cvt_pk_bf16_f32 v203, v203, v219\n\t"
  "v_cvt_pk_bf16_f32 v204, v204, v220\n\t"
  "v_cvt_pk_bf16_f32 v205, v205, v221\n\t"
  "v_cvt_pk_bf16_f32 v206, v206, v222\n\t"
  "v_cvt_pk_bf16_f32 v207, v207, v223\n\t"
  "v_cvt_pk_bf16_f32 v208, v208, v224\n\t"
  "v_cvt_pk_bf16_f32 v209, v209, v225\n\t"
  "v_dot2_f32_bf16 v194, v194, v234, 0\n\t"
  "v_dot2_f32_bf16 v195, v195, v234, 0\n\t"
  "v_dot2_f32_bf16 v196, v196, v234, 0\n\t"
  "v_dot2_f32_bf16 v197, v197, v234, 0\n\t"
  "v_dot2_f32_bf16 v198, v198, v234, 0\n\t"
  "v_dot2_f32_bf16 v199, v199, v234, 0\n\t"
  "v_dot2_f32_bf16 v200, v200, v234, 0\n\t"
  "v_dot2_f32_bf16 v201, v201, v234, 0\n\t"
  "v_dot2_f32_bf16 v202, v202, v234, 0\n\t"
  "v_dot2_f32_bf16 v203, v203, v234, 0\n\t"
  "v_dot2_f32_bf16 v204, v204, v234, 0\n\t"
  "v_dot2_f32_bf16 v205, v205, v234, 0\n\t"
  "v_dot2_f32_bf16 v206, v206, v234, 0\n\t"
  "v_dot2_f32_bf16 v207, v207, v234, 0\n\t"
  "v_dot2_f32_bf16 v208, v208, v234, 0\n\t"
  "v_dot2_f32_bf16 v209, v209, v234, 0\n\t"
  "s_waitcnt lgkmcnt(11)\n\t"
  "v_and_b32 v210, 0xffff, v132\n\t"
  "v_and_b32 v211, 0xffff0000, v132\n\t"
  "v_and_b32 v212, 0xffff, v133\n\t"
  "v_and_b32 v213, 0xffff0000, v133\n\t"
  "s_waitcnt lgkmcnt(10)\n\t"
  "v_and_b32 v214, 0xffff, v134\n\t"
  "v_and_b32 v215, 0xffff0000, v134\n\t"
  "v_and_b32 v216, 0xffff, v135\n\t"
  "v_and_b32 v217, 0xffff0000, v135\n\t"
  "s_waitcnt lgkmcnt(9)\n\t"
  "v_and_b32 v218, 0xffff, v136\n\t"
  "v_and_b32 v219, 0xffff0000, v136\n\t"
  "v_and_b32 v220, 0xffff, v137\n\t"
  "v_and_b32 v221, 0xffff0000, v137\n\t"
  "s_waitcnt lgkmcnt(8)\n\t"
  "v_and_b32 v222, 0xffff, v138\n\t"
  "v_and_b32 v223, 0xffff0000, v138\n\t"
  "v_and_b32 v224, 0xffff, v139\n\t"
  "v_and_b32 v225, 0xffff0000, v139\n\t"
  "v_cvt_pk_bf16_f32 v194, v194, v195\n\t"
  "v_cvt_pk_bf16_f32 v196, v196, v197\n\t"
  "v_cvt_pk_bf16_f32 v198, v198, v199\n\t"
  "v_cvt_pk_bf16_f32 v200, v200, v201\n\t"
  "v_cvt_pk_bf16_f32 v202, v202, v203\n\t"
  "v_cvt_pk_bf16_f32 v204, v204, v205\n\t"
  "v_cvt_pk_bf16_f32 v206, v206, v207\n\t"
  "v_cvt_pk_bf16_f32 v208, v208, v209\n\t"
  "v_dot2_f32_bf16 v210, v194, v210, 0\n\t"
  "v_dot2_f32_bf16 v211, v194, v211, 0\n\t"
  "v_dot2_f32_bf16 v212, v196, v212, 0\n\t"
  "v_dot2_f32_bf16 v213, v196, v213, 0\n\t"
  "v_dot2_f32_bf16 v214, v198, v214, 0\n\t"
  "v_dot2_f32_bf16 v215, v198, v215, 0\n\t"
  "v_dot2_f32_bf16 v216, v200, v216, 0\n\t"
  "v_dot2_f32_bf16 v217, v200, v217, 0\n\t"
  "v_dot2_f32_bf16 v218, v202, v218, 0\n\t"
  "v_dot2_f32_bf16 v219, v202, v219, 0\n\t"
  "v_dot2_f32_bf16 v220, v204, v220, 0\n\t"
  "v_dot2_f32_bf16 v221, v204, v221, 0\n\t"
  "v_dot2_f32_bf16 v222, v206, v222, 0\n\t"
  "v_dot2_f32_bf16 v223, v206, v223, 0\n\t"
  "v_dot2_f32_bf16 v224, v208, v224, 0\n\t"
  "v_dot2_f32_bf16 v225, v208, v225, 0\n\t"
  "v_cvt_pk_bf16_f32 v229, v212, v210\n\t"
  "v_cvt_pk_bf16_f32 v228, v216, v214\n\t"
  "v_cvt_pk_bf16_f32 v227, v220, v218\n\t"
  "v_cvt_pk_bf16_f32 v226, v224, v222\n\t"
  "buffer_store_dwordx4 v[226:229], %[vo], %[srd], 0 offen offset:0 sc1\n\t"
  "v_cvt_pk_bf16_f32 v233, v213, v211\n\t"
  "v_cvt_pk_bf16_f32 v232, v217, v215\n\t"
  "v_cvt_pk_bf16_f32 v231, v221, v219\n\t"
  "v_cvt_pk_bf16_f32 v230, v225, v223\n\t"
  "buffer_store_dwordx4 v[230:233], %[vo], %[srd], 0 offen offset:128 sc1\n\t"
  "s_waitcnt lgkmcnt(7)\n\t"
  "v_lshlrev_b32 v170, 16, v124\n\t"
  "v_and_b32 v171, 0xffff0000, v124\n\t"
  "v_lshlrev_b32 v172, 16, v125\n\t"
  "v_and_b32 v173, 0xffff0000, v125\n\t"
  "s_waitcnt lgkmcnt(6)\n\t"
  "v_lshlrev_b32 v174, 16, v126\n\t"
  "v_and_b32 v175, 0xffff0000, v126\n\t"
  "v_lshlrev_b32 v176, 16, v127\n\t"
  "v_and_b32 v177, 0xffff0000, v127\n\t"
  "s_waitcnt lgkmcnt(5)\n\t"
  "v_lshlrev_b32 v178, 16, v128\n\t"
  "v_and_b32 v179, 0xffff0000, v128\n\t"
  "v_lshlrev_b32 v180, 16, v129\n\t"
  "v_and_b32 v181, 0xffff0000, v129\n\t"
  "s_waitcnt lgkmcnt(4)\n\t"
  "v_lshlrev_b32 v182, 16, v130\n\t"
  "v_and_b32 v183, 0xffff0000, v130\n\t"
  "v_lshlrev_b32 v184, 16, v131\n\t"
  "v_and_b32 v185, 0xffff0000, v131\n\t"
  "v_pk_mul_f32 v[194:195], v[164:165], v[192:193]\n\t"
  "v_pk_mul_f32 v[196:197], v[166:167], v[192:193]\n\t"
  "v_pk_mul_f32 v[198:199], v[168:169], v[192:193]\n\t"
  "v_pk_mul_f32 v[200:201], v[170:171], v[192:193]\n\t"
  "v_pk_mul_f32 v[202:203], v[172:173], v[192:193]\n\t"
  "v_pk_mul_f32 v[204:205], v[174:175], v[192:193]\n\t"
  "v_pk_mul_f32 v[206:207], v[176:177], v[192:193]\n\t"
  "v_pk_mul_f32 v[208:209], v[178:179], v[192:193]\n\t"
  "v_pk_mul_f32 v[210:211], v[166:167], v[190:191]\n\t"
  "v_pk_mul_f32 v[212:213], v[168:169], v[190:191]\n\t"
  "v_pk_mul_f32 v[214:215], v[170:171], v[190:191]\n\t"
  "v_pk_mul_f32 v[216:217], v[172:173], v[190:191]\n\t"
  "v_pk_mul_f32 v[218:219], v[174:175], v[190:191]\n\t"
  "v_pk_mul_f32 v[220:221], v[176:177], v[190:191]\n\t"
  "v_pk_mul_f32 v[222:223], v[178:179], v[190:191]\n\t"
  "v_pk_mul_f32 v[224:225], v[180:181], v[190:191]\n\t"
  "v_cvt_pk_bf16_f32 v194, v194, v210\n\t"
  "v_cvt_pk_bf16_f32 v195, v195, v211\n\t"
  "v_cvt_pk_bf16_f32 v196, v196, v212\n\t"
  "v_cvt_pk_bf16_f32 v197, v197, v213\n\t"
  "v_cvt_pk_bf16_f32 v198, v198, v214\n\t"
  "v_cvt_pk_bf16_f32 v199, v199, v215\n\t"
  "v_cvt_pk_bf16_f32 v200, v200, v216\n\t"
  "v_cvt_pk_bf16_f32 v201, v201, v217\n\t"
  "v_cvt_pk_bf16_f32 v202, v202, v218\n\t"
  "v_cvt_pk_bf16_f32 v203, v203, v219\n\t"
  "v_cvt_pk_bf16_f32 v204, v204, v220\n\t"
  "v_cvt_pk_bf16_f32 v205, v205, v221\n\t"
  "v_cvt_pk_bf16_f32 v206, v206, v222\n\t"
  "v_cvt_pk_bf16_f32 v207, v207, v223\n\t"
  "v_cvt_pk_bf16_f32 v208, v208, v224\n\t"
  "v_cvt_pk_bf16_f32 v209, v209, v225\n\t"
  "v_dot2_f32_bf16 v194, v194, v234, 0\n\t"
  "v_dot2_f32_bf16 v195, v195, v234, 0\n\t"
  "v_dot2_f32_bf16 v196, v196, v234, 0\n\t"
  "v_dot2_f32_bf16 v197, v197, v234, 0\n\t"
  "v_dot2_f32_bf16 v198, v198, v234, 0\n\t"
  "v_dot2_f32_bf16 v199, v199, v234, 0\n\t"
  "v_dot2_f32_bf16 v200, v200, v234, 0\n\t"
  "v_dot2_f32_bf16 v201, v201, v234, 0\n\t"
  "v_dot2_f32_bf16 v202, v202, v234, 0\n\t"
  "v_dot2_f32_bf16 v203, v203, v234, 0\n\t"
  "v_dot2_f32_bf16 v204, v204, v234, 0\n\t"
  "v_dot2_f32_bf16 v205, v205, v234, 0\n\t"
  "v_dot2_f32_bf16 v206, v206, v234, 0\n\t"
  "v_dot2_f32_bf16 v207, v207, v234, 0\n\t"
  "v_dot2_f32_bf16 v208, v208, v234, 0\n\t"
  "v_dot2_f32_bf16 v209, v209, v234, 0\n\t"
  "v_pk_mul_f32 v[210:211], v[168:169], v[188:189]\n\t"
  "v_pk_mul_f32 v[212:213], v[170:171], v[188:189]\n\t"
  "v_pk_mul_f32 v[214:215], v[172:173], v[188:189]\n\t"
  "v_pk_mul_f32 v[216:217], v[174:175], v[188:189]\n\t"
  "v_pk_mul_f32 v[218:219], v[176:177], v[188:189]\n\t"
  "v_pk_mul_f32 v[220:221], v[178:179], v[188:189]\n\t"
  "v_pk_mul_f32 v[222:223], v[180:181], v[188:189]\n\t"
  "v_pk_mul_f32 v[224:225], v[182:183], v[188:189]\n\t"
  "v_cvt_pk_bf16_f32 v194, v194, v210\n\t"
  "v_cvt_pk_bf16_f32 v195, v195, v211\n\t"
  "v_cvt_pk_bf16_f32 v196, v196, v212\n\t"
  "v_cvt_pk_bf16_f32 v197, v197, v213\n\t"
  "v_cvt_pk_bf16_f32 v198, v198, v214\n\t"
  "v_cvt_pk_bf16_f32 v199, v199, v215\n\t"
  "v_cvt_pk_bf16_f32 v200, v200, v216\n\t"
  "v_cvt_pk_bf16_f32 v201, v201, v217\n\t"
  "v_cvt_pk_bf16_f32 v202, v202, v218\n\t"
  "v_cvt_pk_bf16_f32 v203, v203, v219\n\t"
  "v_cvt_pk_bf16_f32 v204, v204, v220\n\t"
  "v_cvt_pk_bf16_f32 v205, v205, v221\n\t"
  "v_cvt_pk_bf16_f32 v206, v206, v222\n\t"
  "v_cvt_pk_bf16_f32 v207, v207, v223\n\t"
  "v_cvt_pk_bf16_f32 v208, v208, v224\n\t"
  "v_cvt_pk_bf16_f32 v209, v209, v225\n\t"
  "v_dot2_f32_bf16 v194, v194, v234, 0\n\t"
  "v_dot2_f32_bf16 v195, v195, v234, 0\n\t"
  "v_dot2_f32_bf16 v196, v196, v234, 0\n\t"
  "v_dot2_f32_bf16 v197, v197, v234, 0\n\t"
  "v_dot2_f32_bf16 v198, v198, v234, 0\n\t"
  "v_dot2_f32_bf16 v199, v199, v234, 0\n\t"
  "v_dot2_f32_bf16 v200, v200, v234, 0\n\t"
  "v_dot2_f32_bf16 v201, v201, v234, 0\n\t"
  "v_dot2_f32_bf16 v202, v202, v234, 0\n\t"
  "v_dot2_f32_bf16 v203, v203, v234, 0\n\t"
  "v_dot2_f32_bf16 v204, v204, v234, 0\n\t"
  "v_dot2_f32_bf16 v205, v205, v234, 0\n\t"
  "v_dot2_f32_bf16 v206, v206, v234, 0\n\t"
  "v_dot2_f32_bf16 v207, v207, v234, 0\n\t"
  "v_dot2_f32_bf16 v208, v208, v234, 0\n\t"
  "v_dot2_f32_bf16 v209, v209, v234, 0\n\t"
  "v_pk_mul_f32 v[210:211], v[170:171], v[186:187]\n\t"
  "v_pk_mul_f32 v[212:213], v[172:173], v[186:187]\n\t"
  "v_pk_mul_f32 v[214:215], v[174:175], v[186:187]\n\t"
  "v_pk_mul_f32 v[216:217], v[176:177], v[186:187]\n\t"
  "v_pk_mul_f32 v[218:219], v[178:179], v[186:187]\n\t"
  "v_pk_mul_f32 v[220:221], v[180:181], v[186:187]\n\t"
  "v_pk_mul_f32 v[222:223], v[182:183], v[186:187]\n\t"
  "v_pk_mul_f32 v[224:225], v[184:185], v[186:187]\n\t"
  "v_cvt_pk_bf16_f32 v194, v194, v210\n\t"
  "v_cvt_pk_bf16_f32 v195, v195, v211\n\t"
  "v_cvt_pk_bf16_f32 v196, v196, v212\n\t"
  "v_cvt_pk_bf16_f32 v197, v197, v213\n\t"
  "v_cvt_pk_bf16_f32 v198, v198, v214\n\t"
  "v_cvt_pk_bf16_f32 v199, v199, v215\n\t"
  "v_cvt_pk_bf16_f32 v200, v200, v216\n\t"
  "v_cvt_pk_bf16_f32 v201, v201, v217\n\t"
  "v_cvt_pk_bf16_f32 v202, v202, v218\n\t"
  "v_cvt_pk_bf16_f32 v203, v203, v219\n\t"
  "v_cvt_pk_bf16_f32 v204, v204, v220\n\t"
  "v_cvt_pk_bf16_f32 v205, v205, v221\n\t"
  "v_cvt_pk_bf16_f32 v206, v206, v222\n\t"
  "v_cvt_pk_bf16_f32 v207, v207, v223\n\t"
  "v_cvt_pk_bf16_f32 v208, v208, v224\n\t"
  "v_cvt_pk_bf16_f32 v209, v209, v225\n\t"
  "v_dot2_f32_bf16 v194, v194, v234, 0\n\t"
  "v_dot2_f32_bf16 v195, v195, v234, 0\n\t"
  "v_dot2_f32_bf16 v196, v196, v234, 0\n\t"
  "v_dot2_f32_bf16 v197, v197, v234, 0\n\t"
  "v_dot2_f32_bf16 v198, v198, v234, 0\n\t"
  "v_dot2_f32_bf16 v199, v199, v234, 0\n\t"
  "v_dot2_f32_bf16 v200, v200, v234, 0\n\t"
  "v_dot2_f32_bf16 v201, v201, v234, 0\n\t"
  "v_dot2_f32_bf16 v202, v202, v234, 0\n\t"
  "v_dot2_f32_bf16 v203, v203, v234, 0\n\t"
  "v_dot2_f32_bf16 v204, v204, v234, 0\n\t"
  "v_dot2_f32_bf16 v205, v205, v234, 0\n\t"
  "v_dot2_f32_bf16 v206, v206, v234, 0\n\t"
  "v_dot2_f32_bf16 v207, v207, v234, 0\n\t"
  "v_dot2_f32_bf16 v208, v208, v234, 0\n\t"
  "v_dot2_f32_bf16 v209, v209, v234, 0\n\t"
  "s_waitcnt lgkmcnt(3)\n\t"
  "v_and_b32 v210, 0xffff, v140\n\t"
  "v_and_b32 v211, 0xffff0000, v140\n\t"
  "v_and_b32 v212, 0xffff, v141\n\t"
  "v_and_b32 v213, 0xffff0000, v141\n\t"
  "s_waitcnt lgkmcnt(2)\n\t"
  "v_and_b32 v214, 0xffff, v142\n\t"
  "v_and_b32 v215, 0xffff0000, v142\n\t"
  "v_and_b32 v216, 0xffff, v143\n\t"
  "v_and_b32 v217, 0xffff0000, v143\n\t"
  "s_waitcnt lgkmcnt(1)\n\t"
  "v_and_b32 v218, 0xffff, v144\n\t"
  "v_and_b32 v219, 0xffff0000, v144\n\t"
  "v_and_b32 v220, 0xffff, v145\n\t"
  "v_and_b32 v221, 0xffff0000, v145\n\t"
  "s_waitcnt lgkmcnt(0)\n\t"
  "v_and_b32 v222, 0xffff, v146\n\t"
  "v_and_b32 v223, 0xffff0000, v146\n\t"
  "v_and_b32 v224, 0xffff, v147\n\t"
  "v_and_b32 v225, 0xffff0000, v147\n\t"
  "v_cvt_pk_bf16_f32 v194, v194, v195\n\t"
  "v_cvt_pk_bf16_f32 v196, v196, v197\n\t"
  "v_cvt_pk_bf16_f32 v198, v198, v199\n\t"
  "v_cvt_pk_bf16_f32 v200, v200, v201\n\t"
  "v_cvt_pk_bf16_f32 v202, v202, v203\n\t"
  "v_cvt_pk_bf16_f32 v204, v204, v205\n\t"
  "v_cvt_pk_bf16_f32 v206, v206, v207\n\t"
  "v_cvt_pk_bf16_f32 v208, v208, v209\n\t"
  "v_dot2_f32_bf16 v210, v194, v210, 0\n\t"
  "v_dot2_f32_bf16 v211, v194, v211, 0\n\t"
  "v_dot2_f32_bf16 v212, v196, v212, 0\n\t"
  "v_dot2_f32_bf16 v213, v196, v213, 0\n\t"
  "v_dot2_f32_bf16 v214, v198, v214, 0\n\t"
  "v_dot2_f32_bf16 v215, v198, v215, 0\n\t"
  "v_dot2_f32_bf16 v216, v200, v216, 0\n\t"
  "v_dot2_f32_bf16 v217, v200, v217, 0\n\t"
  "v_dot2_f32_bf16 v218, v202, v218, 0\n\t"
  "v_dot2_f32_bf16 v219, v202, v219, 0\n\t"
  "v_dot2_f32_bf16 v220, v204, v220, 0\n\t"
  "v_dot2_f32_bf16 v221, v204, v221, 0\n\t"
  "v_dot2_f32_bf16 v222, v206, v222, 0\n\t"
  "v_dot2_f32_bf16 v223, v206, v223, 0\n\t"
  "v_dot2_f32_bf16 v224, v208, v224, 0\n\t"
  "v_dot2_f32_bf16 v225, v208, v225, 0\n\t"
  "v_cvt_pk_bf16_f32 v229, v212, v210\n\t"
  "v_cvt_pk_bf16_f32 v228, v216, v214\n\t"
  "v_cvt_pk_bf16_f32 v227, v220, v218\n\t"
  "v_cvt_pk_bf16_f32 v226, v224, v222\n\t"
  "buffer_store_dwordx4 v[226:229], %[vo], %[srd], 0 offen offset:64 sc1\n\t"
  "v_cvt_pk_bf16_f32 v233, v213, v211\n\t"
  "v_cvt_pk_bf16_f32 v232, v217, v215\n\t"
  "v_cvt_pk_bf16_f32 v231, v221, v219\n\t"
  "v_cvt_pk_bf16_f32 v230, v225, v223\n\t"
  "buffer_store_dwordx4 v[230:233], %[vo], %[srd], 0 offen offset:192 sc1\n\t"
  "s_nop 1\n\t"
      :: [srd] "s"(srd), [la] "v"(lb + lkv + lane * 4), [t0] "v"(taps[0]), [t1] "v"(taps[1]), [t2] "v"(taps[2]), [t3] "v"(taps[3]),
         [vo] "v"(OFF_KVT + (tb * 160 + 2 * (d0 >> 3)) * 128 + ((lane ^ (tb >> 1)) & 3) * 16)
      : "memory",
"v112", "v113", "v114", "v115", "v116", "v117", "v118", "v119", "v120", "v121", "v122", "v123", "v124", "v125", "v126", "v127", "v128", "v129", "v130", "v131", "v132", "v133", "v134", "v135", "v136", "v137", "v138", "v139", "v140", "v141", "v142", "v143", "v144", "v145", "v146", "v147", "v148", "v149", "v150", "v151", "v152", "v153", "v154", "v155", "v156", "v157", "v158", "v159", "v160", "v161", "v162", "v163", "v164", "v165", "v166", "v167", "v168", "v169", "v170", "v171", "v172", "v173", "v174", "v175", "v176", "v177", "v178", "v179", "v180", "v181", "v182", "v183", "v184", "v185", "v186", "v187", "v188", "v189", "v190", "v191", "v192", "v193", "v194", "v195", "v196", "v197", "v198", "v199", "v200", "v201", "v202", "v203", "v204", "v205", "v206", "v207", "v208", "v209", "v210", "v211", "v212", "v213", "v214", "v215", "v216", "v217", "v218", "v219", "v220", "v221", "v222", "v223", "v224", "v225", "v226", "v227", "v228", "v229", "v230", "v231", "v232", "v233", "v234"
    );
    STAMP(1);
    char* fcw = smem + L_FC + w * FC_CH;
    auto put = [&](unsigned u, u32x4 a, u32x2 h) {
      const u32 W[6] = {a[0], a[1], a[2], a[3], h[0], h[1]};
      u32 O[5];
#pragma unroll
      for (int i = 0; i < 5; ++i) O[i] = __builtin_amdgcn_alignbit(W[i + 1], W[i], 16);
      *(u32x4*)(fcw + 0 * FC_PITCH + u * 16) = a;
      *(u32x4*)(fcw + 1 * FC_PITCH + u * 16) = u32x4{O[0], O[1], O[2], O[3]};
      *(u32x4*)(fcw + 2 * FC_PITCH + u * 16) = u32x4{W[1], W[2], W[3], W[4]};
      *(u32x4*)(fcw + 3 * FC_PITCH + u * 16) = u32x4{O[1], O[2], O[3], O[4]};
    };
#pragma unroll
    for (int n = 0; n < 4; ++n) {
      u32x2 h;
#pragma unroll
      for (int e = 0; e < 2; ++e) h[e] = __builtin_amdgcn_update_dpp(n < 3 ? rfl(fa[n < 3 ? n + 1 : n][e]) : 0u, fa[n][e], 0x130, 0xf, 0xf, false);
      put(lane + 64 * n + 2, fa[n], h);
    }
    if (lane < 2) put(1 - lane, u32x4{0, 0, 0, 0}, lane == 0 ? u32x2{fa[0][0], fa[0][1]} : u32x2{0, 0});
    FENCE();
#pragma unroll
    for (int n = 0; n < 5; ++n) *(u32x4*)(smem + L_XR + n * 4096 + w * 1024 + lane * 16) = xr[n];
    if (w == 1) *(u32x4*)(smem + L_XR + 20480 + lane * 16) = cr;
    FENCE();
    asm volatile("s_waitcnt vmcnt(0) lgkmcnt(0)\n\ts_barrier" ::: "memory");
    __builtin_amdgcn_raw_buffer_store_b128(u32x4{(u32)tag, (u32)(tag >> 32), (u32)tag, (u32)(tag >> 32)}, srd, tid < 8 ? OFF_FA1 + cb * 128 + tid * 16 : OOB, 0, SC1);
    FENCE();
    STAMP(2);
    if (isx) {
      const unsigned g = lane >> 4, q = (lane >> 2) & 3, ch = 16 * (g >> 1) + (lane & 15);
      asm volatile(
  "s_nop 4\n\t"
  "s_add_u32 s44, %[w], 1\n\t"
  "s_and_b32 s44, s44, 2\n\t"
  "s_cbranch_scc0 Lxs%=\n\t"
  "ds_read_b64_tr_b16 v[96:97], %[vtx] offset:0\n\t"
  "ds_read_b64_tr_b16 v[98:99], %[vtx] offset:512\n\t"
  "ds_read_b64_tr_b16 v[100:101], %[vtx] offset:64\n\t"
  "ds_read_b64_tr_b16 v[102:103], %[vtx] offset:576\n\t"
  "ds_read_b64_tr_b16 v[104:105], %[vtx] offset:2048\n\t"
  "ds_read_b64_tr_b16 v[106:107], %[vtx] offset:2560\n\t"
  "ds_read_b64_tr_b16 v[108:109], %[vtx] offset:2112\n\t"
  "ds_read_b64_tr_b16 v[110:111], %[vtx] offset:2624\n\t"
  "ds_read_b64_tr_b16 v[112:113], %[vtx] offset:4096\n\t"
  "ds_read_b64_tr_b16 v[114:115], %[vtx] offset:4608\n\t"
  "ds_read_b64_tr_b16 v[116:117], %[vtx] offset:4160\n\t"
  "ds_read_b64_tr_b16 v[118:119], %[vtx] offset:4672\n\t"
  "ds_read_b64_tr_b16 v[120:121], %[vtx] offset:6144\n\t"
  "ds_read_b64_tr_b16 v[122:123], %[vtx] offset:6656\n\t"
  "ds_read_b64_tr_b16 v[124:125], %[vtx] offset:6208\n\t"
  "ds_read_b64_tr_b16 v[126:127], %[vtx] offset:6720\n\t"
  "s_add_u32 s44, %[sXo], 0\n\t"
  "s_waitcnt lgkmcnt(14)\n\t"
  "buffer_store_dwordx4 v[96:99], %[vxo], %[srd], s44 offen offset:0 sc1\n\t"
  "ds_read_b64_tr_b16 v[128:129], %[vtx] offset:8192\n\t"
  "ds_read_b64_tr_b16 v[130:131], %[vtx] offset:8704\n\t"
  "s_waitcnt lgkmcnt(14)\n\t"
  "buffer_store_dwordx4 v[100:103], %[vxo], %[srd], s44 offen offset:1024 sc1\n\t"
  "ds_read_b64_tr_b16 v[132:133], %[vtx] offset:8256\n\t"
  "ds_read_b64_tr_b16 v[134:135], %[vtx] offset:8768\n\t"
  "s_waitcnt lgkmcnt(14)\n\t"
  "buffer_store_dwordx4 v[104:107], %[vxo], %[srd], s44 offen offset:2048 sc1\n\t"
  "s_waitcnt lgkmcnt(12)\n\t"
  "buffer_store_dwordx4 v[108:111], %[vxo], %[srd], s44 offen offset:3072 sc1\n\t"
  "s_add_u32 s44, %[sXo], 4096\n\t"
  "s_waitcnt lgkmcnt(10)\n\t"
  "buffer_store_dwordx4 v[112:115], %[vxo], %[srd], s44 offen offset:0 sc1\n\t"
  "s_waitcnt lgkmcnt(8)\n\t"
  "buffer_store_dwordx4 v[116:119], %[vxo], %[srd], s44 offen offset:1024 sc1\n\t"
  "s_waitcnt lgkmcnt(6)\n\t"
  "buffer_store_dwordx4 v[120:123], %[vxo], %[srd], s44 offen offset:2048 sc1\n\t"
  "s_waitcnt lgkmcnt(4)\n\t"
  "buffer_store_dwordx4 v[124:127], %[vxo], %[srd], s44 offen offset:3072 sc1\n\t"
  "s_add_u32 s44, %[sXo], 8192\n\t"
  "s_waitcnt lgkmcnt(2)\n\t"
  "buffer_store_dwordx4 v[128:131], %[vxo], %[srd], s44 offen offset:0 sc1\n\t"
  "s_waitcnt lgkmcnt(0)\n\t"
  "buffer_store_dwordx4 v[132:135], %[vxo], %[srd], s44 offen offset:1024 sc1\n\t"
  "s_branch Lxe%=\n\t"
  "Lxs%=:\n\t"
  "ds_read_b128 v[96:99], %[vsx] offset:0\n\t"
  "ds_read_b128 v[100:103], %[vsx] offset:2048\n\t"
  "ds_read_b128 v[104:107], %[vsx] offset:4096\n\t"
  "ds_read_b128 v[108:111], %[vsx] offset:6144\n\t"
  "ds_read_b128 v[112:115], %[vsx] offset:8192\n\t"
  "ds_read_b128 v[116:119], %[vsx] offset:10240\n\t"
  "ds_read_b128 v[120:123], %[vsx] offset:12288\n\t"
  "ds_read_b128 v[124:127], %[vsx] offset:14336\n\t"
  "ds_read_b128 v[128:131], %[vsx] offset:16384\n\t"
  "ds_read_b128 v[132:135], %[vsx] offset:18432\n\t"
  "v_mov_b32 v184, 0\n\t"
  "v_mov_b32 v185, 0\n\t"
  "v_mov_b32 v186, 0x3f803f80\n\t"
  "s_waitcnt lgkmcnt(9)\n\t"
  "v_dot2c_f32_bf16 v184, v96, v186\n\t"
  "v_dot2c_f32_bf16 v185, v96, v96\n\t"
  "v_dot2c_f32_bf16 v184, v97, v186\n\t"
  "v_dot2c_f32_bf16 v185, v97, v97\n\t"
  "v_dot2c_f32_bf16 v184, v98, v186\n\t"
  "v_dot2c_f32_bf16 v185, v98, v98\n\t"
  "v_dot2c_f32_bf16 v184, v99, v186\n\t"
  "v_dot2c_f32_bf16 v185, v99, v99\n\t"
  "s_waitcnt lgkmcnt(8)\n\t"
  "v_dot2c_f32_bf16 v184, v100, v186\n\t"
  "v_dot2c_f32_bf16 v185, v100, v100\n\t"
  "v_dot2c_f32_bf16 v184, v101, v186\n\t"
  "v_dot2c_f32_bf16 v185, v101, v101\n\t"
  "v_dot2c_f32_bf16 v184, v102, v186\n\t"
  "v_dot2c_f32_bf16 v185, v102, v102\n\t"
  "v_dot2c_f32_bf16 v184, v103, v186\n\t"
  "v_dot2c_f32_bf16 v185, v103, v103\n\t"
  "s_waitcnt lgkmcnt(7)\n\t"
  "v_dot2c_f32_bf16 v184, v104, v186\n\t"
  "v_dot2c_f32_bf16 v185, v104, v104\n\t"
  "v_dot2c_f32_bf16 v184, v105, v186\n\t"
  "v_dot2c_f32_bf16 v185, v105, v105\n\t"
  "v_dot2c_f32_bf16 v184, v106, v186\n\t"
  "v_dot2c_f32_bf16 v185, v106, v106\n\t"
  "v_dot2c_f32_bf16 v184, v107, v186\n\t"
  "v_dot2c_f32_bf16 v185, v107, v107\n\t"
  "s_waitcnt lgkmcnt(6)\n\t"
  "v_dot2c_f32_bf16 v184, v108, v186\n\t"
  "v_dot2c_f32_bf16 v185, v108, v108\n\t"
  "v_dot2c_f32_bf16 v184, v109, v186\n\t"
  "v_dot2c_f32_bf16 v185, v109, v109\n\t"
  "v_dot2c_f32_bf16 v184, v110, v186\n\t"
  "v_dot2c_f32_bf16 v185, v110, v110\n\t"
  "v_dot2c_f32_bf16 v184, v111, v186\n\t"
  "v_dot2c_f32_bf16 v185, v111, v111\n\t"
  "s_waitcnt lgkmcnt(5)\n\t"
  "v_dot2c_f32_bf16 v184, v112, v186\n\t"
  "v_dot2c_f32_bf16 v185, v112, v112\n\t"
  "v_dot2c_f32_bf16 v184, v113, v186\n\t"
  "v_dot2c_f32_bf16 v185, v113, v113\n\t"
  "v_dot2c_f32_bf16 v184, v114, v186\n\t"
  "v_dot2c_f32_bf16 v185, v114, v114\n\t"
  "v_dot2c_f32_bf16 v184, v115, v186\n\t"
  "v_dot2c_f32_bf16 v185, v115, v115\n\t"
  "s_waitcnt lgkmcnt(4)\n\t"
  "v_dot2c_f32_bf16 v184, v116, v186\n\t"
  "v_dot2c_f32_bf16 v185, v116, v116\n\t"
  "v_dot2c_f32_bf16 v184, v117, v186\n\t"
  "v_dot2c_f32_bf16 v185, v117, v117\n\t"
  "v_dot2c_f32_bf16 v184, v118, v186\n\t"
  "v_dot2c_f32_bf16 v185, v118, v118\n\t"
  "v_dot2c_f32_bf16 v184, v119, v186\n\t"
  "v_dot2c_f32_bf16 v185, v119, v119\n\t"
  "s_waitcnt lgkmcnt(3)\n\t"
  "v_dot2c_f32_bf16 v184, v120, v186\n\t"
  "v_dot2c_f32_bf16 v185, v120, v120\n\t"
  "v_dot2c_f32_bf16 v184, v121, v186\n\t"
  "v_dot2c_f32_bf16 v185, v121, v121\n\t"
  "v_dot2c_f32_bf16 v184, v122, v186\n\t"
  "v_dot2c_f32_bf16 v185, v122, v122\n\t"
  "v_dot2c_f32_bf16 v184, v123, v186\n\t"
  "v_dot2c_f32_bf16 v185, v123, v123\n\t"
  "s_waitcnt lgkmcnt(2)\n\t"
  "v_dot2c_f32_bf16 v184, v124, v186\n\t"
  "v_dot2c_f32_bf16 v185, v124, v124\n\t"
  "v_dot2c_f32_bf16 v184, v125, v186\n\t"
  "v_dot2c_f32_bf16 v185, v125, v125\n\t"
  "v_dot2c_f32_bf16 v184, v126, v186\n\t"
  "v_dot2c_f32_bf16 v185, v126, v126\n\t"
  "v_dot2c_f32_bf16 v184, v127, v186\n\t"
  "v_dot2c_f32_bf16 v185, v127, v127\n\t"
  "s_waitcnt lgkmcnt(1)\n\t"
  "v_dot2c_f32_bf16 v184, v128, v186\n\t"
  "v_dot2c_f32_bf16 v185, v128, v128\n\t"
  "v_dot2c_f32_bf16 v184, v129, v186\n\t"
  "v_dot2c_f32_bf16 v185, v129, v129\n\t"
  "v_dot2c_f32_bf16 v184, v130, v186\n\t"
  "v_dot2c_f32_bf16 v185, v130, v130\n\t"
  "v_dot2c_f32_bf16 v184, v131, v186\n\t"
  "v_dot2c_f32_bf16 v185, v131, v131\n\t"
  "s_waitcnt lgkmcnt(0)\n\t"
  "v_dot2c_f32_bf16 v184, v132, v186\n\t"
  "v_dot2c_f32_bf16 v185, v132, v132\n\t"
  "v_dot2c_f32_bf16 v184, v133, v186\n\t"
  "v_dot2c_f32_bf16 v185, v133, v133\n\t"
  "v_dot2c_f32_bf16 v184, v134, v186\n\t"
  "v_dot2c_f32_bf16 v185, v134, v134\n\t"
  "v_dot2c_f32_bf16 v184, v135, v186\n\t"
  "v_dot2c_f32_bf16 v185, v135, v135\n\t"
  "s_nop 1\n\t"
  "v_add_f32_dpp v184, v184, v184 quad_perm:[1,0,3,2] row_mask:0xf bank_mask:0xf\n\t"
  "v_add_f32_dpp v185, v185, v185 quad_perm:[1,0,3,2] row_mask:0xf bank_mask:0xf\n\t"
  "s_nop 0\n\t"
  "v_add_f32_dpp v184, v184, v184 quad_perm:[2,3,0,1] row_mask:0xf bank_mask:0xf\n\t"
  "v_add_f32_dpp v185, v185, v185 quad_perm:[2,3,0,1] row_mask:0xf bank_mask:0xf\n\t"
  "s_nop 0\n\t"
  "v_add_f32_dpp v184, v184, v184 row_half_mirror row_mask:0xf bank_mask:0xf\n\t"
  "v_add_f32_dpp v185, v185, v185 row_half_mirror row_mask:0xf bank_mask:0xf\n\t"
  "v_mul_f32 v184, 0xbacccccd, v184\n\t"
  "v_mul_f32 v185, 0x3acccccd, v185\n\t"
  "v_fma_f32 v185, -v184, v184, v185\n\t"
  "v_max_f32 v185, 0, v185\n\t"
  "v_cvt_pk_bf16_f32 v185, 0, v185\n\t"
  "v_cvt_pk_bf16_f32 v184, 0, v184\n\t"
  "v_add_f32 v185, 0x3727c5ac, v185\n\t"
  "v_cvt_pk_bf16_f32 v185, 0, v185\n\t"
  "v_sqrt_f32 v185, v185\n\t"
  "s_nop 0\n\t"
  "v_cvt_pk_bf16_f32 v185, 0, v185\n\t"
  "v_rcp_f32 v188, v185\n\t"
  "s_nop 0\n\t"
  "v_mul_f32 v189, v184, v188\n\t"
  "s_nop 1\n\t"
  "v_mov_b32_dpp v188, v189 quad_perm:[0,1,2,3] row_mask:0xf bank_mask:0xa\n\t"
  "buffer_store_dword v188, %[vso], %[srd], %[sSo] offen sc1\n\t"
  "Lxe%=:\n\t"
  "s_nop 1\n\t"
        :: [srd] "s"(srd), [w] "s"(w), [sSo] "s"(OFF_ST1 + cb * 128u), [sXo] "s"(OFF_XT + cb * 20480u + (w - 1) * 10240u),
           [vtx] "v"(lb + L_XR + (w - 1) * 10240 + (8 * (g & 1) + q) * 128 + (g >> 1) * 32 + (lane & 3) * 8), [vsx] "v"(lb + L_XR + (w & 1) * 1024 + lane * 16),
           [vso] "v"((lane & 3) == 0 ? (((w & 1) * 4 + g) ^ stkey(cb)) * 16 + ((lane >> 3) & 1) * 4 + (lane & 4) * 2 : OOB), [vxo] "v"((2 * (ch >> 3) + (ch & 1)) * 128 + ((((ch ^ cb) >> 1) & 3) + 4 * (g & 1)) * 16)
        : "memory", "vcc", "scc",
"s44", "v96", "v97", "v98", "v99", "v100", "v101", "v102", "v103", "v104", "v105", "v106", "v107", "v108", "v109", "v110", "v111", "v112", "v113", "v114", "v115", "v116", "v117", "v118", "v119", "v120", "v121", "v122", "v123", "v124", "v125", "v126", "v127", "v128", "v129", "v130", "v131", "v132", "v133", "v134", "v135", "v184", "v185", "v186", "v188", "v189"
      );
    } else if (w == 1) GEMV_UNIT(cb, lb + L_XR, 8);
    stash();
    STAMP(5);
  } else {
    const unsigned lu = lb + L_UN + w * UN_SZ;
    const unsigned g = lane >> 4, q = (lane >> 2) & 3, p4 = lane & 3;
    if (W_DELAY) __builtin_amdgcn_s_sleep(W_DELAY);
    if (role == 2) {
      const unsigned ui = U_MAP ? 16 * (w2 >> 2) + 4 * w + (w2 & 3) : 4 * w2 + w;
      GEMV_LOAD(ui, lu);
      const rsrc_t rd = mkrsrc(wd, D_ * F_ * 2);
      const unsigned r8 = 24 * w + (lane >> 3), so = (96 * (w2 >> 1) * D_ + 320 * (w2 & 1)) * 2;
      u32x4 raw[15];
#pragma unroll
      for (int n = 0; n < 15; ++n) raw[n] = __builtin_amdgcn_raw_buffer_load_b128(rd, r8 * (D_ * 2) + (lane & 7) * 16 + (n % 5) * 128, so + (n / 5) * 8 * D_ * 2, NT);
      FENCE();
      tag = load_tag(ws);
      GEMV_UNIT(ui, lu, 15);
      STAMP(1);
#pragma unroll
      for (int n = 0; n < 15; ++n) *(u32x4*)(smem + r8 * RP2 + (lane & 7) * 16 + (n % 5) * 128 + (n / 5) * 8 * RP2) = raw[n];
      FENCE();
      asm volatile("s_waitcnt vmcnt(0)" ::: "memory");
      put_flag(srd, U_FLAG(ui), lane, tag);
      STAMP(2);
      const bool rt = w2 < 10 && w == 0;
      poll_flags<1, 4>(srd, rt && lane >= 4 ? OFF_FA2 + ((lane & 3) * 60 + 40 + w2) * 128 : OFF_FW1 + (j * 4 + (lane & 3)) * 128, tag);
      if (rt) {
        const float sc = rbf(1.f + modval(srd, 4 * 640 + lane + 64 * w2));
        __builtin_amdgcn_raw_buffer_store_b32(__float_as_uint(sc == 0.f ? 0x1p40f : 1.f / sc), srd, lane * 4, OFF_RT + w2 * 256, SC1);
        asm volatile("s_waitcnt vmcnt(0)" ::: "memory");
        put_flag(srd, OFF_FR + w2 * 128, lane, tag);
      }
      W1_LOAD();
      wdone = 255;
      BAR();
#pragma unroll
      for (int n = 0; n < 15; ++n) {
        const int i = n % 5;
        const char* a = smem + (32 * (n / 5) + 8 * g + q) * RP2 + (80 * w + (i < 4 ? 32 * (i >> 1) + 8 * p4 + 4 * (i & 1) : 64 + 4 * p4)) * 2;
        const u32x2 lo = tr_read(a), hi = tr_read(a + 4 * RP2);
        raw[n] = u32x4{lo[0], lo[1], hi[0], hi[1]};
      }
      FENCE();
#pragma unroll
      for (int n = 0; n < 15; ++n)
        __builtin_amdgcn_raw_buffer_store_b128(raw[n], srd, lane * 16 + (n & 3) * 1024, OFF_W2F + ((4 * (w2 & 1) + w) * 240 + 15 * (w2 >> 1) + (n & ~3)) * 1024, SC1);
      FENCE();
      STAMP(7);
      stash();
    } else {
      const rsrc_t rg = mkrsrc(wg, D_ * F_ * 2), ru = mkrsrc(wu, D_ * F_ * 2);
      const unsigned rw = 5 * w + lane / 12, ck = lane % 12, ui = 160 + 64 * w + w1;
      const unsigned vo = lane < 60 ? rw * (F_ * 2) + ck * 16 : OOB, so = (160 * (w1 & 3) * F_ + 96 * (w1 >> 2)) * 2;
      u32x4 raw[16];
#pragma unroll
      for (int n = 0; n < 8; ++n) {
        raw[2 * n] = __builtin_amdgcn_raw_buffer_load_b128(rg, vo, so + n * 20 * F_ * 2, NT);
        raw[2 * n + 1] = __builtin_amdgcn_raw_buffer_load_b128(ru, vo, so + n * 20 * F_ * 2, NT);
      }
      FENCE();
      tag = load_tag(ws);
      char* o = smem + (lane < 60 ? rw * RP1 + ck * 32 : 384);
      u32x4 fr[15];
#pragma unroll
      for (int h = 0; h < 2; ++h) {
#pragma unroll
        for (int n = h ? 5 : 0; n < (h ? 8 : 5); ++n) {
          *(u32x4*)(o + n * 20 * RP1) = u32x4{raw[2 * n][0], raw[2 * n + 1][0], raw[2 * n][1], raw[2 * n + 1][1]};
          *(u32x4*)(o + n * 20 * RP1 + (lane < 60 ? 16 : 0)) = u32x4{raw[2 * n][2], raw[2 * n + 1][2], raw[2 * n][3], raw[2 * n + 1][3]};
        }
        BAR();
#pragma unroll
        for (int n = h ? 9 : 0; n < (h ? 15 : 9); ++n) {
          const int ks = n / 3, i = n % 3;
          const char* a = smem + (32 * ks + 4 * g + q) * RP1 + (12 * w + (p4 < 3 ? 4 * p4 + i : 4 * i + 3)) * 8;
          const u32x2 lo = tr_read(a), hi = tr_read(a + 16 * RP1);
          fr[n] = u32x4{lo[0], lo[1], hi[0], hi[1]};
        }
        FENCE();
#pragma unroll
        for (int n = h ? 9 : 0; n < (h ? 15 : 9); ++n)
          __builtin_amdgcn_raw_buffer_store_b128(fr[n], srd, lane * 16 + (n & 3) * 1024, OFF_W1F + (((w1 & ~3u) + w) * 60 + 15 * (w1 & 3) + (n & ~3)) * 1024, SC1);
        FENCE();
      }
      STAMP(6);
      if (ui < 240) { GEMV_LOAD(ui, lu); asm volatile("s_waitcnt vmcnt(21)" ::: "memory"); } else asm volatile("s_waitcnt vmcnt(0)" ::: "memory");
      asm volatile("s_barrier" ::: "memory");
      if (w == 3) put_flag(srd, OFF_FW1 + w1 * 128, lane, tag);
      STAMP(7);
      if (ui < 240) {
        GEMV_UNIT(ui, lu, 0);
        asm volatile("s_waitcnt vmcnt(0)" ::: "memory");
        put_flag(srd, U_FLAG(ui), lane, tag);
        STAMP(2);
      }
      poll_flags<1, 4>(srd, OFF_FW1 + (j * 4 + (lane & 3)) * 128, tag);
      W1_LOAD();
      wdone = 255;
      stash();
      if (w > 1 || (w == 1 && w1 < 4)) {
        const unsigned c0 = (6 - w) * 640 + 160 * (w1 & 3);
        poll_flags<1, 4>(srd, OFF_FA2 + ((lane & 3) * 60 + ((c0 + min(64 * ((lane >> 2) & 3), 159u)) >> 6)) * 128, tag);
        f32x4 m[4];
#pragma unroll
        for (int k = 0; k < 4; ++k) m[k] = __builtin_bit_cast(f32x4, __builtin_amdgcn_raw_buffer_load_b128(srd, lane < 40 ? lane * 16 : OOB, OFF_MODP + k * 15360 + c0 * 4, SC1));
        f32x4 v = (m[0] + m[1]) + (m[2] + m[3]);
#pragma unroll
        for (int e = 0; e < 4; ++e) v[e] = rbf(v[e]);
        if (w == 1) __builtin_amdgcn_raw_buffer_store_b128(__builtin_bit_cast(u32x4, v), srd, lane < 40 ? lane * 16 : OOB, OFF_GB + 640 * w1, SC1);
        else {
          const float of = w == 2 ? 1.f : 0.f;
          *(u32x2*)(smem + L_WS + (w - 2) * 1024 + lane * 8) = u32x2{pack_bf16(of + v[0], of + v[1]), pack_bf16(of + v[2], of + v[3])};
        }
      }
      STAMP(5);
      BAR();
      STAMP(3);
      f32x4 aU[3], aB[3];
#pragma unroll
      for (int i = 0; i < 3; ++i) { aU[i] = f32x4{0, 0, 0, 0}; aB[i] = f32x4{0, 0, 0, 0}; }
#pragma unroll
      for (int ks = 0; ks < 5; ++ks) {
        const char* t = smem + L_WS + (32 * ks + 4 * g) * 2;
        const u32x2 s0 = *(const u32x2*)t, s1 = *(const u32x2*)(t + 32), b0 = *(const u32x2*)(t + 1024), b1 = *(const u32x2*)(t + 1056);
#pragma unroll
        for (int i = 0; i < 3; ++i) {
          mfma_v(aU[i], u32x4{s0[0], s0[1], s1[0], s1[1]}, fr[3 * ks + i]);
          mfma_v(aB[i], u32x4{b0[0], b0[1], b1[0], b1[1]}, fr[3 * ks + i]);
        }
      }
      asm volatile("s_nop 7\n\ts_nop 1" : "+v"(aU[0]), "+v"(aU[1]), "+v"(aU[2]), "+v"(aB[0]), "+v"(aB[1]), "+v"(aB[2]));
      const float u = g == 0 ? aU[0][0] : g == 1 ? aU[1][0] : aU[2][0], bb = g == 0 ? aB[0][0] : g == 1 ? aB[1][0] : aB[2][0];
      const float uh = rbf(u), bh = rbf(bb);
      __builtin_amdgcn_raw_buffer_store_b64(u32x2{pack_bf16(uh, u - uh), pack_bf16(bh, bb - bh)}, srd,
                                            g < 3 ? OFF_UB + ((((w1 & ~3u) + w) * 3 + g) * 64 + (w1 & 3) * 16 + (lane & 15)) * 8 : OOB, 0, SC1);
    }
  }
  if (role == 0) {
    const unsigned d = 8 * (cb >> 1) + 2 * w + (cb & 1);
    asm volatile(
  "s_nop 4\n\t"
  "buffer_load_dwordx2 v[160:161], %[vf1], %[srd], 0 offen sc1\n\t"
  "s_lshl_b32 s45, %[w], 12\n\t"
  "s_mov_b64 s[80:81], %[ws]\n\t"
  "v_mov_b32 v252, %[tlo]\n\t"
  "v_mov_b32 v253, %[thi]\n\t"
  "v_mov_b32 v254, %[tlo]\n\t"
  "v_mov_b32 v255, %[thi]\n\t"
  "v_mov_b32 v162, 140288\n\t"
  "v_mov_b32 v163, 1\n\t"
  "s_mov_b64 exec, 1\n\t"
  "s_waitcnt vmcnt(1)\n\t"
  "ds_add_rtn_u32 v164, v162, v163\n\t"
  "s_waitcnt lgkmcnt(0)\n\t"
  "s_mov_b64 exec, -1\n\t"
  "v_readfirstlane_b32 s44, v164\n\t"
  "s_nop 3\n\t"
  "s_cmp_lg_u32 s44, 3\n\t"
  "s_cbranch_scc1 Lnox%=\n\t"
  "buffer_store_dwordx4 v[252:255], %[vf3], %[srd], 0 offen sc1\n\t"
  "s_waitcnt vmcnt(1)\n\t"
  "s_branch Lx%=\n\t"
  "Lnox%=:\n\t"
  "s_waitcnt vmcnt(0)\n\t"
  "Lx%=:\n\t"
  "v_cmp_ne_u64 vcc, v[252:253], v[160:161]\n\t"
  "s_nop 1\n\t"
  "s_cbranch_vccnz La1_slow%=\n\t"
  "La1_back%=:\n\t"
  "s_add_u32 m0, s45, 68608\n\t"
  "s_add_u32 s44, %[sKV], 0\n\t"
  "buffer_load_dwordx4 %[vkv], %[srd], s44 offen sc1 lds\n\t"
  "s_add_u32 m0, s45, 69632\n\t"
  "s_add_u32 s44, %[sKV], 163840\n\t"
  "buffer_load_dwordx4 %[vkv], %[srd], s44 offen sc1 lds\n\t"
  "s_add_u32 m0, s45, 70656\n\t"
  "s_add_u32 s44, %[sKV], 327680\n\t"
  "buffer_load_dwordx4 %[vkv], %[srd], s44 offen sc1 lds\n\t"
  "s_add_u32 m0, s45, 71680\n\t"
  "s_add_u32 s44, %[sKV], 491520\n\t"
  "buffer_load_dwordx4 %[vkv], %[srd], s44 offen sc1 lds\n\t"
  "buffer_load_dwordx2 v[250:251], %[vfw], %[srd], 0 offen sc1\n\t"
  "ds_read_b64 v[64:65], %[va] offset:0\n\t"
  "ds_read_b64 v[66:67], %[va] offset:8\n\t"
  "ds_read_b64 v[68:69], %[va] offset:512\n\t"
  "ds_read_b64 v[70:71], %[va] offset:520\n\t"
  "ds_read_b64 v[72:73], %[va] offset:1024\n\t"
  "ds_read_b64 v[74:75], %[va] offset:1032\n\t"
  "ds_read_b64 v[76:77], %[va] offset:1536\n\t"
  "ds_read_b64 v[78:79], %[va] offset:1544\n\t"
  "ds_read_b64 v[80:81], %[va] offset:2048\n\t"
  "ds_read_b64 v[82:83], %[va] offset:2056\n\t"
  "ds_read_b64 v[84:85], %[va] offset:2560\n\t"
  "ds_read_b64 v[86:87], %[va] offset:2568\n\t"
  "ds_read_b64 v[88:89], %[va] offset:3072\n\t"
  "ds_read_b64 v[90:91], %[va] offset:3080\n\t"
  "ds_read_b64 v[92:93], %[va] offset:3584\n\t"
  "ds_read_b64 v[94:95], %[va] offset:3592\n\t"
  "s_waitcnt vmcnt(0) lgkmcnt(0)\n\t"
  "s_barrier\n\t"
  "ds_read_b128 v[124:127], %[vb0] offset:15360\n\t"
  "ds_read_b128 v[120:123], %[vb0] offset:13312\n\t"
  "ds_read_b128 v[116:119], %[vb0] offset:11264\n\t"
  "ds_read_b128 v[112:115], %[vb0] offset:9216\n\t"
  "ds_read_b128 v[108:111], %[vb0] offset:7168\n\t"
  "ds_read_b128 v[104:107], %[vb0] offset:5120\n\t"
  "ds_read_b128 v[100:103], %[vb0] offset:3072\n\t"
  "ds_read_b128 v[96:99], %[vb0] offset:1024\n\t"
  "v_cmp_eq_u64 s[54:55], v[252:253], v[250:251]\n\t"
  "s_nop 3\n\t"
  "s_cmp_eq_u64 s[54:55], -1\n\t"
  "s_cselect_b32 s53, 1, 0\n\t"
  "s_cselect_b32 s56, %[sW1], 1879048192\n\t"
  "s_mov_b32 %[wdone], s53\n\t"
  "s_waitcnt lgkmcnt(7)\n\t"
  "v_mfma_f32_16x16x32_bf16 v[60:63], v[64:67], v[124:127], 0\n\t"
  "ds_read_b128 v[156:159], %[vb1] offset:15360\n\t"
  "buffer_load_dwordx4 a[0:3], %[vl16], %[srd], s56 offen offset:0 sc1\n\t"
  "s_waitcnt lgkmcnt(7)\n\t"
  "v_mfma_f32_16x16x32_bf16 v[56:59], v[64:67], v[120:123], 0\n\t"
  "s_waitcnt lgkmcnt(6)\n\t"
  "v_mfma_f32_16x16x32_bf16 v[52:55], v[64:67], v[116:119], 0\n\t"
  "s_waitcnt lgkmcnt(5)\n\t"
  "v_mfma_f32_16x16x32_bf16 v[48:51], v[64:67], v[112:115], 0\n\t"
  "buffer_load_dwordx4 a[4:7], %[vl16], %[srd], s56 offen offset:1024 sc1\n\t"
  "s_waitcnt lgkmcnt(4)\n\t"
  "v_mfma_f32_16x16x32_bf16 v[44:47], v[64:67], v[108:111], 0\n\t"
  "ds_read_b128 v[152:155], %[vb1] offset:13312\n\t"
  "s_waitcnt lgkmcnt(4)\n\t"
  "v_mfma_f32_16x16x32_bf16 v[40:43], v[64:67], v[104:107], 0\n\t"
  "s_waitcnt lgkmcnt(3)\n\t"
  "v_mfma_f32_16x16x32_bf16 v[36:39], v[64:67], v[100:103], 0\n\t"
  "buffer_load_dwordx4 a[8:11], %[vl16], %[srd], s56 offen offset:2048 sc1\n\t"
  "s_waitcnt lgkmcnt(2)\n\t"
  "v_mfma_f32_16x16x32_bf16 v[32:35], v[64:67], v[96:99], 0\n\t"
  "v_mfma_f32_16x16x32_bf16 v[60:63], %[vd], v[124:127], v[60:63]\n\t"
  "ds_read_b128 v[148:151], %[vb1] offset:11264\n\t"
  "v_mfma_f32_16x16x32_bf16 v[56:59], %[vd], v[120:123], v[56:59]\n\t"
  "v_mfma_f32_16x16x32_bf16 v[52:55], %[vd], v[116:119], v[52:55]\n\t"
  "buffer_load_dwordx4 a[12:15], %[vl16], %[srd], s56 offen offset:3072 sc1\n\t"
  "v_mfma_f32_16x16x32_bf16 v[48:51], %[vd], v[112:115], v[48:51]\n\t"
  "v_mfma_f32_16x16x32_bf16 v[44:47], %[vd], v[108:111], v[44:47]\n\t"
  "ds_read_b128 v[144:147], %[vb1] offset:9216\n\t"
  "v_mfma_f32_16x16x32_bf16 v[40:43], %[vd], v[104:107], v[40:43]\n\t"
  "s_add_u32 s56, s56, 4096\n\t"
  "v_mfma_f32_16x16x32_bf16 v[36:39], %[vd], v[100:103], v[36:39]\n\t"
  "v_mfma_f32_16x16x32_bf16 v[32:35], %[vd], v[96:99], v[32:35]\n\t"
  "ds_read_b64 v[64:65], %[va] offset:64\n\t"
  "ds_read_b64 v[66:67], %[va] offset:72\n\t"
  "v_mfma_f32_16x16x32_bf16 v[60:63], v[68:71], v[120:123], v[60:63]\n\t"
  "ds_read_b128 v[140:143], %[vb1] offset:7168\n\t"
  "buffer_load_dwordx2 v[250:251], %[vfw], %[srd], 0 offen sc1\n\t"
  "v_mfma_f32_16x16x32_bf16 v[56:59], v[68:71], v[116:119], v[56:59]\n\t"
  "buffer_load_dwordx4 a[16:19], %[vl16], %[srd], s56 offen offset:0 sc1\n\t"
  "v_mfma_f32_16x16x32_bf16 v[52:55], v[68:71], v[112:115], v[52:55]\n\t"
  "v_mfma_f32_16x16x32_bf16 v[48:51], v[68:71], v[108:111], v[48:51]\n\t"
  "v_mfma_f32_16x16x32_bf16 v[44:47], v[68:71], v[104:107], v[44:47]\n\t"
  "ds_read_b128 v[136:139], %[vb1] offset:5120\n\t"
  "buffer_load_dwordx4 a[20:23], %[vl16], %[srd], s56 offen offset:1024 sc1\n\t"
  "v_mfma_f32_16x16x32_bf16 v[40:43], v[68:71], v[100:103], v[40:43]\n\t"
  "v_mfma_f32_16x16x32_bf16 v[36:39], v[68:71], v[96:99], v[36:39]\n\t"
  "ds_read_b64 v[68:69], %[va] offset:576\n\t"
  "ds_read_b64 v[70:71], %[va] offset:584\n\t"
  "v_mfma_f32_16x16x32_bf16 v[60:63], v[72:75], v[116:119], v[60:63]\n\t"
  "v_mfma_f32_16x16x32_bf16 v[56:59], v[72:75], v[112:115], v[56:59]\n\t"
  "ds_read_b128 v[132:135], %[vb1] offset:3072\n\t"
  "buffer_load_dwordx4 a[24:27], %[vl16], %[srd], s56 offen offset:2048 sc1\n\t"
  "v_mfma_f32_16x16x32_bf16 v[52:55], v[72:75], v[108:111], v[52:55]\n\t"
  "v_mfma_f32_16x16x32_bf16 v[48:51], v[72:75], v[104:107], v[48:51]\n\t"
  "v_mfma_f32_16x16x32_bf16 v[44:47], v[72:75], v[100:103], v[44:47]\n\t"
  "buffer_load_dwordx4 a[28:31], %[vl16], %[srd], s56 offen offset:3072 sc1\n\t"
  "v_mfma_f32_16x16x32_bf16 v[40:43], v[72:75], v[96:99], v[40:43]\n\t"
  "ds_read_b64 v[72:73], %[va] offset:1088\n\t"
  "ds_read_b128 v[128:131], %[vb1] offset:1024\n\t"
  "v_mfma_f32_16x16x32_bf16 v[60:63], v[76:79], v[112:115], v[60:63]\n\t"
  "ds_read_b64 v[74:75], %[va] offset:1096\n\t"
  "v_mfma_f32_16x16x32_bf16 v[56:59], v[76:79], v[108:111], v[56:59]\n\t"
  "v_mfma_f32_16x16x32_bf16 v[52:55], v[76:79], v[104:107], v[52:55]\n\t"
  "s_add_u32 s56, s56, 4096\n\t"
  "v_mfma_f32_16x16x32_bf16 v[48:51], v[76:79], v[100:103], v[48:51]\n\t"
  "v_mfma_f32_16x16x32_bf16 v[44:47], v[76:79], v[96:99], v[44:47]\n\t"
  "ds_read_b64 v[76:77], %[va] offset:1600\n\t"
  "ds_read_b64 v[78:79], %[va] offset:1608\n\t"
  "v_mfma_f32_16x16x32_bf16 v[60:63], v[80:83], v[108:111], v[60:63]\n\t"
  "buffer_load_dwordx4 a[32:35], %[vl16], %[srd], s56 offen offset:0 sc1\n\t"
  "v_mfma_f32_16x16x32_bf16 v[56:59], v[80:83], v[104:107], v[56:59]\n\t"
  "v_mfma_f32_16x16x32_bf16 v[52:55], v[80:83], v[100:103], v[52:55]\n\t"
  "v_mfma_f32_16x16x32_bf16 v[48:51], v[80:83], v[96:99], v[48:51]\n\t"
  "ds_read_b64 v[80:81], %[va] offset:2112\n\t"
  "ds_read_b64 v[82:83], %[va] offset:2120\n\t"
  "v_mfma_f32_16x16x32_bf16 v[60:63], v[84:87], v[104:107], v[60:63]\n\t"
  "buffer_load_dwordx4 a[36:39], %[vl16], %[srd], s56 offen offset:1024 sc1\n\t"
  "v_mfma_f32_16x16x32_bf16 v[56:59], v[84:87], v[100:103], v[56:59]\n\t"
  "v_mfma_f32_16x16x32_bf16 v[52:55], v[84:87], v[96:99], v[52:55]\n\t"
  "ds_read_b64 v[84:85], %[va] offset:2624\n\t"
  "ds_read_b64 v[86:87], %[va] offset:2632\n\t"
  "v_mfma_f32_16x16x32_bf16 v[60:63], v[88:91], v[100:103], v[60:63]\n\t"
  "buffer_load_dwordx4 a[40:43], %[vl16], %[srd], s56 offen offset:2048 sc1\n\t"
  "v_mfma_f32_16x16x32_bf16 v[56:59], v[88:91], v[96:99], v[56:59]\n\t"
  "ds_read_b64 v[88:89], %[va] offset:3136\n\t"
  "ds_read_b64 v[90:91], %[va] offset:3144\n\t"
  "v_mfma_f32_16x16x32_bf16 v[60:63], v[92:95], v[96:99], v[60:63]\n\t"
  "s_waitcnt lgkmcnt(9)\n\t"
  "ds_read_b64 v[92:93], %[va] offset:3648\n\t"
  "v_mfma_f32_16x16x32_bf16 v[60:63], v[64:67], v[156:159], v[60:63]\n\t"
  "ds_read_b128 v[124:127], %[vb2] offset:15360\n\t"
  "ds_read_b64 v[94:95], %[va] offset:3656\n\t"
  "v_mfma_f32_16x16x32_bf16 v[56:59], v[64:67], v[152:155], v[56:59]\n\t"
  "buffer_load_dwordx4 a[44:47], %[vl16], %[srd], s56 offen offset:3072 sc1\n\t"
  "v_mfma_f32_16x16x32_bf16 v[52:55], v[64:67], v[148:151], v[52:55]\n\t"
  "v_mfma_f32_16x16x32_bf16 v[48:51], v[64:67], v[144:147], v[48:51]\n\t"
  "ds_read_b128 v[120:123], %[vb2] offset:13312\n\t"
  "v_mfma_f32_16x16x32_bf16 v[44:47], v[64:67], v[140:143], v[44:47]\n\t"
  "s_add_u32 s56, s56, 4096\n\t"
  "v_mfma_f32_16x16x32_bf16 v[40:43], v[64:67], v[136:139], v[40:43]\n\t"
  "v_mfma_f32_16x16x32_bf16 v[36:39], v[64:67], v[132:135], v[36:39]\n\t"
  "ds_read_b128 v[116:119], %[vb2] offset:11264\n\t"
  "v_mfma_f32_16x16x32_bf16 v[32:35], v[64:67], v[128:131], v[32:35]\n\t"
  "ds_read_b64 v[64:65], %[va] offset:128\n\t"
  "ds_read_b64 v[66:67], %[va] offset:136\n\t"
  "v_mfma_f32_16x16x32_bf16 v[60:63], v[68:71], v[152:155], v[60:63]\n\t"
  "buffer_load_dwordx4 a[48:51], %[vl16], %[srd], s56 offen offset:0 sc1\n\t"
  "v_mfma_f32_16x16x32_bf16 v[56:59], v[68:71], v[148:151], v[56:59]\n\t"
  "ds_read_b128 v[112:115], %[vb2] offset:9216\n\t"
  "v_mfma_f32_16x16x32_bf16 v[52:55], v[68:71], v[144:147], v[52:55]\n\t"
  "v_mfma_f32_16x16x32_bf16 v[48:51], v[68:71], v[140:143], v[48:51]\n\t"
  "buffer_load_dwordx4 a[52:55], %[vl16], %[srd], s56 offen offset:1024 sc1\n\t"
  "v_mfma_f32_16x16x32_bf16 v[44:47], v[68:71], v[136:139], v[44:47]\n\t"
  "ds_read_b128 v[108:111], %[vb2] offset:7168\n\t"
  "s_waitcnt vmcnt(10)\n\t"
  "v_cmp_eq_u64 s[54:55], v[252:253], v[250:251]\n\t"
  "v_mfma_f32_16x16x32_bf16 v[40:43], v[68:71], v[132:135], v[40:43]\n\t"
  "v_mfma_f32_16x16x32_bf16 v[36:39], v[68:71], v[128:131], v[36:39]\n\t"
  "s_waitcnt lgkmcnt(11)\n\t"
  "ds_read_b64 v[68:69], %[va] offset:640\n\t"
  "v_mfma_f32_16x16x32_bf16 v[60:63], v[72:75], v[148:151], v[60:63]\n\t"
  "ds_read_b128 v[104:107], %[vb2] offset:5120\n\t"
  "ds_read_b64 v[70:71], %[va] offset:648\n\t"
  "v_mfma_f32_16x16x32_bf16 v[56:59], v[72:75], v[144:147], v[56:59]\n\t"
  "s_cmp_eq_u64 s[54:55], -1\n\t"
  "s_cselect_b32 s53, 2, 0\n\t"
  "s_cselect_b32 s57, %[sW1], 1879048192\n\t"
  "v_mfma_f32_16x16x32_bf16 v[52:55], v[72:75], v[140:143], v[52:55]\n\t"
  "s_or_b32 %[wdone], %[wdone], s53\n\t"
  "buffer_load_dwordx4 a[56:59], %[vl16], %[srd], s56 offen offset:2048 sc1\n\t"
  "v_mfma_f32_16x16x32_bf16 v[48:51], v[72:75], v[136:139], v[48:51]\n\t"
  "ds_read_b128 v[100:103], %[vb2] offset:3072\n\t"
  "s_add_u32 s57, s57, 15360\n\t"
  "v_mfma_f32_16x16x32_bf16 v[44:47], v[72:75], v[132:135], v[44:47]\n\t"
  "v_mfma_f32_16x16x32_bf16 v[40:43], v[72:75], v[128:131], v[40:43]\n\t"
  "ds_read_b64 v[72:73], %[va] offset:1152\n\t"
  "ds_read_b64 v[74:75], %[va] offset:1160\n\t"
  "v_mfma_f32_16x16x32_bf16 v[60:63], v[76:79], v[144:147], v[60:63]\n\t"
  "ds_read_b128 v[96:99], %[vb2] offset:1024\n\t"
  "buffer_load_dwordx4 a[60:63], %[vl16], %[srd], s57 offen offset:0 sc1\n\t"
  "v_mfma_f32_16x16x32_bf16 v[56:59], v[76:79], v[140:143], v[56:59]\n\t"
  "v_mfma_f32_16x16x32_bf16 v[52:55], v[76:79], v[136:139], v[52:55]\n\t"
  "v_mfma_f32_16x16x32_bf16 v[48:51], v[76:79], v[132:135], v[48:51]\n\t"
  "v_mfma_f32_16x16x32_bf16 v[44:47], v[76:79], v[128:131], v[44:47]\n\t"
  "ds_read_b64 v[76:77], %[va] offset:1664\n\t"
  "ds_read_b64 v[78:79], %[va] offset:1672\n\t"
  "v_mfma_f32_16x16x32_bf16 v[60:63], v[80:83], v[140:143], v[60:63]\n\t"
  "buffer_load_dwordx4 a[64:67], %[vl16], %[srd], s57 offen offset:1024 sc1\n\t"
  "v_mfma_f32_16x16x32_bf16 v[56:59], v[80:83], v[136:139], v[56:59]\n\t"
  "v_mfma_f32_16x16x32_bf16 v[52:55], v[80:83], v[132:135], v[52:55]\n\t"
  "buffer_load_dwordx4 a[68:71], %[vl16], %[srd], s57 offen offset:2048 sc1\n\t"
  "v_mfma_f32_16x16x32_bf16 v[48:51], v[80:83], v[128:131], v[48:51]\n\t"
  "ds_read_b64 v[80:81], %[va] offset:2176\n\t"
  "ds_read_b64 v[82:83], %[va] offset:2184\n\t"
  "v_mfma_f32_16x16x32_bf16 v[60:63], v[84:87], v[136:139], v[60:63]\n\t"
  "v_mfma_f32_16x16x32_bf16 v[56:59], v[84:87], v[132:135], v[56:59]\n\t"
  "v_mfma_f32_16x16x32_bf16 v[52:55], v[84:87], v[128:131], v[52:55]\n\t"
  "s_waitcnt lgkmcnt(15)\n\t"
  "ds_read_b64 v[84:85], %[va] offset:2688\n\t"
  "v_mfma_f32_16x16x32_bf16 v[60:63], v[88:91], v[132:135], v[60:63]\n\t"
  "ds_read_b64 v[86:87], %[va] offset:2696\n\t"
  "buffer_load_dwordx4 a[72:75], %[vl16], %[srd], s57 offen offset:3072 sc1\n\t"
  "v_mfma_f32_16x16x32_bf16 v[56:59], v[88:91], v[128:131], v[56:59]\n\t"
  "ds_read_b64 v[88:89], %[va] offset:3200\n\t"
  "ds_read_b64 v[90:91], %[va] offset:3208\n\t"
  "v_mfma_f32_16x16x32_bf16 v[60:63], v[92:95], v[128:131], v[60:63]\n\t"
  "s_waitcnt lgkmcnt(8)\n\t"
  "ds_read_b64 v[92:93], %[va] offset:3712\n\t"
  "v_mfma_f32_16x16x32_bf16 v[60:63], v[64:67], v[124:127], v[60:63]\n\t"
  "ds_read_b128 v[156:159], %[vb3] offset:15360\n\t"
  "ds_read_b64 v[94:95], %[va] offset:3720\n\t"
  "v_mfma_f32_16x16x32_bf16 v[56:59], v[64:67], v[120:123], v[56:59]\n\t"
  "s_add_u32 s57, s57, 4096\n\t"
  "v_mfma_f32_16x16x32_bf16 v[52:55], v[64:67], v[116:119], v[52:55]\n\t"
  "v_mfma_f32_16x16x32_bf16 v[48:51], v[64:67], v[112:115], v[48:51]\n\t"
  "ds_read_b128 v[152:155], %[vb3] offset:13312\n\t"
  "buffer_load_dwordx4 a[76:79], %[vl16], %[srd], s57 offen offset:0 sc1\n\t"
  "v_mfma_f32_16x16x32_bf16 v[44:47], v[64:67], v[108:111], v[44:47]\n\t"
  "v_mfma_f32_16x16x32_bf16 v[40:43], v[64:67], v[104:107], v[40:43]\n\t"
  "v_mfma_f32_16x16x32_bf16 v[36:39], v[64:67], v[100:103], v[36:39]\n\t"
  "ds_read_b128 v[148:151], %[vb3] offset:11264\n\t"
  "v_mfma_f32_16x16x32_bf16 v[32:35], v[64:67], v[96:99], v[32:35]\n\t"
  "ds_read_b64 v[64:65], %[va] offset:192\n\t"
  "ds_read_b64 v[66:67], %[va] offset:200\n\t"
  "v_mfma_f32_16x16x32_bf16 v[60:63], v[68:71], v[120:123], v[60:63]\n\t"
  "buffer_load_dwordx4 a[80:83], %[vl16], %[srd], s57 offen offset:1024 sc1\n\t"
  "v_mfma_f32_16x16x32_bf16 v[56:59], v[68:71], v[116:119], v[56:59]\n\t"
  "ds_read_b128 v[144:147], %[vb3] offset:9216\n\t"
  "v_mfma_f32_16x16x32_bf16 v[52:55], v[68:71], v[112:115], v[52:55]\n\t"
  "buffer_load_dwordx4 a[84:87], %[vl16], %[srd], s57 offen offset:2048 sc1\n\t"
  "v_mfma_f32_16x16x32_bf16 v[48:51], v[68:71], v[108:111], v[48:51]\n\t"
  "v_mfma_f32_16x16x32_bf16 v[44:47], v[68:71], v[104:107], v[44:47]\n\t"
  "ds_read_b128 v[140:143], %[vb3] offset:7168\n\t"
  "v_mfma_f32_16x16x32_bf16 v[40:43], v[68:71], v[100:103], v[40:43]\n\t"
  "v_mfma_f32_16x16x32_bf16 v[36:39], v[68:71], v[96:99], v[36:39]\n\t"
  "s_waitcnt lgkmcnt(11)\n\t"
  "ds_read_b64 v[68:69], %[va] offset:704\n\t"
  "v_mfma_f32_16x16x32_bf16 v[60:63], v[72:75], v[116:119], v[60:63]\n\t"
  "ds_read_b128 v[136:139], %[vb3] offset:5120\n\t"
  "ds_read_b64 v[70:71], %[va] offset:712\n\t"
  "v_mfma_f32_16x16x32_bf16 v[56:59], v[72:75], v[112:115], v[56:59]\n\t"
  "buffer_load_dwordx4 a[88:91], %[vl16], %[srd], s57 offen offset:3072 sc1\n\t"
  "v_mfma_f32_16x16x32_bf16 v[52:55], v[72:75], v[108:111], v[52:55]\n\t"
  "v_mfma_f32_16x16x32_bf16 v[48:51], v[72:75], v[104:107], v[48:51]\n\t"
  "ds_read_b128 v[132:135], %[vb3] offset:3072\n\t"
  "s_add_u32 s57, s57, 4096\n\t"
  "v_mfma_f32_16x16x32_bf16 v[44:47], v[72:75], v[100:103], v[44:47]\n\t"
  "v_mfma_f32_16x16x32_bf16 v[40:43], v[72:75], v[96:99], v[40:43]\n\t"
  "ds_read_b64 v[72:73], %[va] offset:1216\n\t"
  "ds_read_b64 v[74:75], %[va] offset:1224\n\t"
  "v_mfma_f32_16x16x32_bf16 v[60:63], v[76:79], v[112:115], v[60:63]\n\t"
  "ds_read_b128 v[128:131], %[vb3] offset:1024\n\t"
  "buffer_load_dwordx4 a[92:95], %[vl16], %[srd], s57 offen offset:0 sc1\n\t"
  "v_mfma_f32_16x16x32_bf16 v[56:59], v[76:79], v[108:111], v[56:59]\n\t"
  "v_mfma_f32_16x16x32_bf16 v[52:55], v[76:79], v[104:107], v[52:55]\n\t"
  "v_mfma_f32_16x16x32_bf16 v[48:51], v[76:79], v[100:103], v[48:51]\n\t"
  "v_mfma_f32_16x16x32_bf16 v[44:47], v[76:79], v[96:99], v[44:47]\n\t"
  "ds_read_b64 v[76:77], %[va] offset:1728\n\t"
  "ds_read_b64 v[78:79], %[va] offset:1736\n\t"
  "v_mfma_f32_16x16x32_bf16 v[60:63], v[80:83], v[108:111], v[60:63]\n\t"
  "buffer_load_dwordx4 a[96:99], %[vl16], %[srd], s57 offen offset:1024 sc1\n\t"
  "v_mfma_f32_16x16x32_bf16 v[56:59], v[80:83], v[104:107], v[56:59]\n\t"
  "v_mfma_f32_16x16x32_bf16 v[52:55], v[80:83], v[100:103], v[52:55]\n\t"
  "buffer_load_dwordx4 a[100:103], %[vl16], %[srd], s57 offen offset:2048 sc1\n\t"
  "v_mfma_f32_16x16x32_bf16 v[48:51], v[80:83], v[96:99], v[48:51]\n\t"
  "ds_read_b64 v[80:81], %[va] offset:2240\n\t"
  "ds_read_b64 v[82:83], %[va] offset:2248\n\t"
  "v_mfma_f32_16x16x32_bf16 v[60:63], v[84:87], v[104:107], v[60:63]\n\t"
  "v_mfma_f32_16x16x32_bf16 v[56:59], v[84:87], v[100:103], v[56:59]\n\t"
  "v_mfma_f32_16x16x32_bf16 v[52:55], v[84:87], v[96:99], v[52:55]\n\t"
  "s_waitcnt lgkmcnt(15)\n\t"
  "ds_read_b64 v[84:85], %[va] offset:2752\n\t"
  "v_mfma_f32_16x16x32_bf16 v[60:63], v[88:91], v[100:103], v[60:63]\n\t"
  "ds_read_b64 v[86:87], %[va] offset:2760\n\t"
  "buffer_load_dwordx4 a[104:107], %[vl16], %[srd], s57 offen offset:3072 sc1\n\t"
  "v_mfma_f32_16x16x32_bf16 v[56:59], v[88:91], v[96:99], v[56:59]\n\t"
  "ds_read_b64 v[88:89], %[va] offset:3264\n\t"
  "ds_read_b64 v[90:91], %[va] offset:3272\n\t"
  "v_mfma_f32_16x16x32_bf16 v[60:63], v[92:95], v[96:99], v[60:63]\n\t"
  "s_waitcnt lgkmcnt(8)\n\t"
  "ds_read_b64 v[92:93], %[va] offset:3776\n\t"
  "v_mfma_f32_16x16x32_bf16 v[60:63], v[64:67], v[156:159], v[60:63]\n\t"
  "ds_read_b128 v[124:127], %[vb0] offset:14336\n\t"
  "ds_read_b64 v[94:95], %[va] offset:3784\n\t"
  "v_mfma_f32_16x16x32_bf16 v[56:59], v[64:67], v[152:155], v[56:59]\n\t"
  "s_add_u32 s57, s57, 4096\n\t"
  "v_mfma_f32_16x16x32_bf16 v[52:55], v[64:67], v[148:151], v[52:55]\n\t"
  "v_mfma_f32_16x16x32_bf16 v[48:51], v[64:67], v[144:147], v[48:51]\n\t"
  "ds_read_b128 v[120:123], %[vb0] offset:12288\n\t"
  "buffer_load_dwordx4 a[108:111], %[vl16], %[srd], s57 offen offset:0 sc1\n\t"
  "v_mfma_f32_16x16x32_bf16 v[44:47], v[64:67], v[140:143], v[44:47]\n\t"
  "v_mfma_f32_16x16x32_bf16 v[40:43], v[64:67], v[136:139], v[40:43]\n\t"
  "v_mfma_f32_16x16x32_bf16 v[36:39], v[64:67], v[132:135], v[36:39]\n\t"
  "ds_read_b128 v[116:119], %[vb0] offset:10240\n\t"
  "v_mfma_f32_16x16x32_bf16 v[32:35], v[64:67], v[128:131], v[32:35]\n\t"
  "ds_read_b64 v[64:65], %[va] offset:256\n\t"
  "ds_read_b64 v[66:67], %[va] offset:264\n\t"
  "v_mfma_f32_16x16x32_bf16 v[60:63], v[68:71], v[152:155], v[60:63]\n\t"
  "buffer_load_dwordx4 a[112:115], %[vl16], %[srd], s57 offen offset:1024 sc1\n\t"
  "v_mfma_f32_16x16x32_bf16 v[56:59], v[68:71], v[148:151], v[56:59]\n\t"
  "ds_read_b128 v[112:115], %[vb0] offset:8192\n\t"
  "v_mfma_f32_16x16x32_bf16 v[52:55], v[68:71], v[144:147], v[52:55]\n\t"
  "buffer_load_dwordx4 a[116:119], %[vl16], %[srd], s57 offen offset:2048 sc1\n\t"
  "v_mfma_f32_16x16x32_bf16 v[48:51], v[68:71], v[140:143], v[48:51]\n\t"
  "v_mfma_f32_16x16x32_bf16 v[44:47], v[68:71], v[136:139], v[44:47]\n\t"
  "ds_read_b128 v[108:111], %[vb0] offset:6144\n\t"
  "v_mfma_f32_16x16x32_bf16 v[40:43], v[68:71], v[132:135], v[40:43]\n\t"
  "v_mfma_f32_16x16x32_bf16 v[36:39], v[68:71], v[128:131], v[36:39]\n\t"
  "s_waitcnt lgkmcnt(11)\n\t"
  "ds_read_b64 v[68:69], %[va] offset:768\n\t"
  "v_mfma_f32_16x16x32_bf16 v[60:63], v[72:75], v[148:151], v[60:63]\n\t"
  "ds_read_b128 v[104:107], %[vb0] offset:4096\n\t"
  "ds_read_b64 v[70:71], %[va] offset:776\n\t"
  "v_mfma_f32_16x16x32_bf16 v[56:59], v[72:75], v[144:147], v[56:59]\n\t"
  "buffer_load_dwordx4 a[120:123], %[vl16], %[srd], s57 offen offset:3072 sc1\n\t"
  "v_mfma_f32_16x16x32_bf16 v[52:55], v[72:75], v[140:143], v[52:55]\n\t"
  "v_mfma_f32_16x16x32_bf16 v[48:51], v[72:75], v[136:139], v[48:51]\n\t"
  "ds_read_b128 v[100:103], %[vb0] offset:2048\n\t"
  "v_mfma_f32_16x16x32_bf16 v[44:47], v[72:75], v[132:135], v[44:47]\n\t"
  "v_mfma_f32_16x16x32_bf16 v[40:43], v[72:75], v[128:131], v[40:43]\n\t"
  "ds_read_b64 v[72:73], %[va] offset:1280\n\t"
  "ds_read_b64 v[74:75], %[va] offset:1288\n\t"
  "v_mfma_f32_16x16x32_bf16 v[60:63], v[76:79], v[144:147], v[60:63]\n\t"
  "ds_read_b128 v[96:99], %[vb0] offset:0\n\t"
  "v_mfma_f32_16x16x32_bf16 v[56:59], v[76:79], v[140:143], v[56:59]\n\t"
  "v_mfma_f32_16x16x32_bf16 v[52:55], v[76:79], v[136:139], v[52:55]\n\t"
  "v_mfma_f32_16x16x32_bf16 v[48:51], v[76:79], v[132:135], v[48:51]\n\t"
  "buffer_load_dwordx2 v[162:163], %[vf2], %[srd], 0 offen sc1\n\t"
  "v_mfma_f32_16x16x32_bf16 v[44:47], v[76:79], v[128:131], v[44:47]\n\t"
  "ds_read_b64 v[76:77], %[va] offset:1792\n\t"
  "ds_read_b64 v[78:79], %[va] offset:1800\n\t"
  "v_mfma_f32_16x16x32_bf16 v[60:63], v[80:83], v[140:143], v[60:63]\n\t"
  "v_mfma_f32_16x16x32_bf16 v[56:59], v[80:83], v[136:139], v[56:59]\n\t"
  "v_mfma_f32_16x16x32_bf16 v[52:55], v[80:83], v[132:135], v[52:55]\n\t"
  "v_mfma_f32_16x16x32_bf16 v[48:51], v[80:83], v[128:131], v[48:51]\n\t"
  "ds_read_b64 v[80:81], %[va] offset:2304\n\t"
  "ds_read_b64 v[82:83], %[va] offset:2312\n\t"
  "v_mfma_f32_16x16x32_bf16 v[60:63], v[84:87], v[136:139], v[60:63]\n\t"
  "v_mfma_f32_16x16x32_bf16 v[56:59], v[84:87], v[132:135], v[56:59]\n\t"
  "v_mfma_f32_16x16x32_bf16 v[52:55], v[84:87], v[128:131], v[52:55]\n\t"
  "s_waitcnt lgkmcnt(15)\n\t"
  "ds_read_b64 v[84:85], %[va] offset:2816\n\t"
  "v_mfma_f32_16x16x32_bf16 v[60:63], v[88:91], v[132:135], v[60:63]\n\t"
  "ds_read_b64 v[86:87], %[va] offset:2824\n\t"
  "v_mfma_f32_16x16x32_bf16 v[56:59], v[88:91], v[128:131], v[56:59]\n\t"
  "ds_read_b64 v[88:89], %[va] offset:3328\n\t"
  "ds_read_b64 v[90:91], %[va] offset:3336\n\t"
  "v_mfma_f32_16x16x32_bf16 v[60:63], v[92:95], v[128:131], v[60:63]\n\t"
  "s_waitcnt lgkmcnt(8)\n\t"
  "ds_read_b64 v[92:93], %[va] offset:3840\n\t"
  "v_mfma_f32_16x16x32_bf16 v[60:63], v[64:67], v[124:127], v[60:63]\n\t"
  "ds_read_b128 v[156:159], %[vb1] offset:14336\n\t"
  "ds_read_b64 v[94:95], %[va] offset:3848\n\t"
  "v_mfma_f32_16x16x32_bf16 v[56:59], v[64:67], v[120:123], v[56:59]\n\t"
  "v_mfma_f32_16x16x32_bf16 v[52:55], v[64:67], v[116:119], v[52:55]\n\t"
  "v_mfma_f32_16x16x32_bf16 v[48:51], v[64:67], v[112:115], v[48:51]\n\t"
  "ds_read_b128 v[152:155], %[vb1] offset:12288\n\t"
  "v_mfma_f32_16x16x32_bf16 v[44:47], v[64:67], v[108:111], v[44:47]\n\t"
  "v_mfma_f32_16x16x32_bf16 v[40:43], v[64:67], v[104:107], v[40:43]\n\t"
  "v_mfma_f32_16x16x32_bf16 v[36:39], v[64:67], v[100:103], v[36:39]\n\t"
  "ds_read_b128 v[148:151], %[vb1] offset:10240\n\t"
  "buffer_load_dwordx2 v[250:251], %[vfw], %[srd], 0 offen sc1\n\t"
  "v_mfma_f32_16x16x32_bf16 v[32:35], v[64:67], v[96:99], v[32:35]\n\t"
  "ds_read_b64 v[64:65], %[va] offset:320\n\t"
  "ds_read_b64 v[66:67], %[va] offset:328\n\t"
  "v_mfma_f32_16x16x32_bf16 v[60:63], v[68:71], v[120:123], v[60:63]\n\t"
  "v_mfma_f32_16x16x32_bf16 v[56:59], v[68:71], v[116:119], v[56:59]\n\t"
  "ds_read_b128 v[144:147], %[vb1] offset:8192\n\t"
  "v_mfma_f32_16x16x32_bf16 v[52:55], v[68:71], v[112:115], v[52:55]\n\t"
  "v_mfma_f32_16x16x32_bf16 v[48:51], v[68:71], v[108:111], v[48:51]\n\t"
  "v_mfma_f32_16x16x32_bf16 v[44:47], v[68:71], v[104:107], v[44:47]\n\t"
  "ds_read_b128 v[140:143], %[vb1] offset:6144\n\t"
  "v_mfma_f32_16x16x32_bf16 v[40:43], v[68:71], v[100:103], v[40:43]\n\t"
  "v_mfma_f32_16x16x32_bf16 v[36:39], v[68:71], v[96:99], v[36:39]\n\t"
  "s_waitcnt lgkmcnt(11)\n\t"
  "ds_read_b64 v[68:69], %[va] offset:832\n\t"
  "v_mfma_f32_16x16x32_bf16 v[60:63], v[72:75], v[116:119], v[60:63]\n\t"
  "ds_read_b128 v[136:139], %[vb1] offset:4096\n\t"
  "ds_read_b64 v[70:71], %[va] offset:840\n\t"
  "v_mfma_f32_16x16x32_bf16 v[56:59], v[72:75], v[112:115], v[56:59]\n\t"
  "v_mfma_f32_16x16x32_bf16 v[52:55], v[72:75], v[108:111], v[52:55]\n\t"
  "v_mfma_f32_16x16x32_bf16 v[48:51], v[72:75], v[104:107], v[48:51]\n\t"
  "ds_read_b128 v[132:135], %[vb1] offset:2048\n\t"
  "v_mfma_f32_16x16x32_bf16 v[44:47], v[72:75], v[100:103], v[44:47]\n\t"
  "v_mfma_f32_16x16x32_bf16 v[40:43], v[72:75], v[96:99], v[40:43]\n\t"
  "ds_read_b64 v[72:73], %[va] offset:1344\n\t"
  "ds_read_b64 v[74:75], %[va] offset:1352\n\t"
  "v_mfma_f32_16x16x32_bf16 v[60:63], v[76:79], v[112:115], v[60:63]\n\t"
  "ds_read_b128 v[128:131], %[vb1] offset:0\n\t"
  "v_mfma_f32_16x16x32_bf16 v[56:59], v[76:79], v[108:111], v[56:59]\n\t"
  "v_mfma_f32_16x16x32_bf16 v[52:55], v[76:79], v[104:107], v[52:55]\n\t"
  "v_mfma_f32_16x16x32_bf16 v[48:51], v[76:79], v[100:103], v[48:51]\n\t"
  "v_mfma_f32_16x16x32_bf16 v[44:47], v[76:79], v[96:99], v[44:47]\n\t"
  "ds_read_b64 v[76:77], %[va] offset:1856\n\t"
  "ds_read_b64 v[78:79], %[va] offset:1864\n\t"
  "v_mfma_f32_16x16x32_bf16 v[60:63], v[80:83], v[108:111], v[60:63]\n\t"
  "v_mfma_f32_16x16x32_bf16 v[56:59], v[80:83], v[104:107], v[56:59]\n\t"
  "v_mfma_f32_16x16x32_bf16 v[52:55], v[80:83], v[100:103], v[52:55]\n\t"
  "s_waitcnt vmcnt(1)\n\t"
  "v_cmp_ne_u64 vcc, v[252:253], v[162:163]\n\t"
  "v_mfma_f32_16x16x32_bf16 v[48:51], v[80:83], v[96:99], v[48:51]\n\t"
  "ds_read_b64 v[80:81], %[va] offset:2368\n\t"
  "ds_read_b64 v[82:83], %[va] offset:2376\n\t"
  "v_mfma_f32_16x16x32_bf16 v[60:63], v[84:87], v[104:107], v[60:63]\n\t"
  "s_cbranch_vccnz La2_slow%=\n\t"
  "La2_back%=:\n\t"
  "v_mfma_f32_16x16x32_bf16 v[56:59], v[84:87], v[100:103], v[56:59]\n\t"
  "buffer_load_dword v244, %[vmod], %[srd], 0 offen sc1\n\t"
  "s_add_u32 m0, s45, 84992\n\t"
  "v_mfma_f32_16x16x32_bf16 v[52:55], v[84:87], v[96:99], v[52:55]\n\t"
  "s_waitcnt lgkmcnt(15)\n\t"
  "ds_read_b64 v[84:85], %[va] offset:2880\n\t"
  "v_mfma_f32_16x16x32_bf16 v[60:63], v[88:91], v[100:103], v[60:63]\n\t"
  "ds_read_b64 v[86:87], %[va] offset:2888\n\t"
  "s_add_u32 s44, %[sKV], 2621440\n\t"
  "v_mfma_f32_16x16x32_bf16 v[56:59], v[88:91], v[96:99], v[56:59]\n\t"
  "ds_read_b64 v[88:89], %[va] offset:3392\n\t"
  "ds_read_b64 v[90:91], %[va] offset:3400\n\t"
  "v_mfma_f32_16x16x32_bf16 v[60:63], v[92:95], v[96:99], v[60:63]\n\t"
  "s_waitcnt lgkmcnt(8)\n\t"
  "ds_read_b64 v[92:93], %[va] offset:3904\n\t"
  "v_mfma_f32_16x16x32_bf16 v[60:63], v[64:67], v[156:159], v[60:63]\n\t"
  "ds_read_b128 v[124:127], %[vb2] offset:14336\n\t"
  "ds_read_b64 v[94:95], %[va] offset:3912\n\t"
  "v_mfma_f32_16x16x32_bf16 v[56:59], v[64:67], v[152:155], v[56:59]\n\t"
  "buffer_load_dwordx4 %[vkv], %[srd], s44 offen sc1 lds\n\t"
  "s_add_u32 m0, s45, 86016\n\t"
  "v_mfma_f32_16x16x32_bf16 v[52:55], v[64:67], v[148:151], v[52:55]\n\t"
  "s_add_u32 s44, %[sKV], 2785280\n\t"
  "buffer_load_dwordx4 %[vkv], %[srd], s44 offen sc1 lds\n\t"
  "v_mfma_f32_16x16x32_bf16 v[48:51], v[64:67], v[144:147], v[48:51]\n\t"
  "ds_read_b128 v[120:123], %[vb2] offset:12288\n\t"
  "s_add_u32 m0, s45, 87040\n\t"
  "v_mfma_f32_16x16x32_bf16 v[44:47], v[64:67], v[140:143], v[44:47]\n\t"
  "s_add_u32 s44, %[sKV], 2949120\n\t"
  "buffer_load_dwordx4 %[vkv], %[srd], s44 offen sc1 lds\n\t"
  "v_mfma_f32_16x16x32_bf16 v[40:43], v[64:67], v[136:139], v[40:43]\n\t"
  "s_add_u32 m0, s45, 88064\n\t"
  "s_add_u32 s44, %[sKV], 3112960\n\t"
  "v_mfma_f32_16x16x32_bf16 v[36:39], v[64:67], v[132:135], v[36:39]\n\t"
  "ds_read_b128 v[116:119], %[vb2] offset:10240\n\t"
  "buffer_load_dwordx4 %[vkv], %[srd], s44 offen sc1 lds\n\t"
  "v_mfma_f32_16x16x32_bf16 v[32:35], v[64:67], v[128:131], v[32:35]\n\t"
  "ds_read_b64 v[64:65], %[va] offset:384\n\t"
  "ds_read_b64 v[66:67], %[va] offset:392\n\t"
  "v_mfma_f32_16x16x32_bf16 v[60:63], v[68:71], v[152:155], v[60:63]\n\t"
  "s_add_u32 m0, s45, 101376\n\t"
  "s_add_u32 s44, s45, 1159168\n\t"
  "v_mfma_f32_16x16x32_bf16 v[56:59], v[68:71], v[148:151], v[56:59]\n\t"
  "ds_read_b128 v[112:115], %[vb2] offset:8192\n\t"
  "buffer_load_dwordx4 %[vl16], %[srd], s44 offen offset:0 sc1 lds\n\t"
  "v_mfma_f32_16x16x32_bf16 v[52:55], v[68:71], v[144:147], v[52:55]\n\t"
  "s_waitcnt vmcnt(6)\n\t"
  "v_cmp_eq_u64 s[54:55], v[252:253], v[250:251]\n\t"
  "v_mfma_f32_16x16x32_bf16 v[48:51], v[68:71], v[140:143], v[48:51]\n\t"
  "buffer_load_dwordx4 %[vl16], %[srd], s44 offen offset:1024 sc1 lds\n\t"
  "buffer_load_dwordx4 %[vl16], %[srd], s44 offen offset:2048 sc1 lds\n\t"
  "v_mfma_f32_16x16x32_bf16 v[44:47], v[68:71], v[136:139], v[44:47]\n\t"
  "ds_read_b128 v[108:111], %[vb2] offset:6144\n\t"
  "buffer_load_dwordx4 %[vl16], %[srd], s44 offen offset:3072 sc1 lds\n\t"
  "v_mfma_f32_16x16x32_bf16 v[40:43], v[68:71], v[132:135], v[40:43]\n\t"
  "s_cmp_eq_u64 s[54:55], -1\n\t"
  "s_cselect_b32 s53, 4, 0\n\t"
  "s_cselect_b32 s58, %[sW1], 1879048192\n\t"
  "v_mfma_f32_16x16x32_bf16 v[36:39], v[68:71], v[128:131], v[36:39]\n\t"
  "s_waitcnt lgkmcnt(11)\n\t"
  "ds_read_b64 v[68:69], %[va] offset:896\n\t"
  "v_mfma_f32_16x16x32_bf16 v[60:63], v[72:75], v[148:151], v[60:63]\n\t"
  "ds_read_b128 v[104:107], %[vb2] offset:4096\n\t"
  "ds_read_b64 v[70:71], %[va] offset:904\n\t"
  "v_mfma_f32_16x16x32_bf16 v[56:59], v[72:75], v[144:147], v[56:59]\n\t"
  "s_or_b32 %[wdone], %[wdone], s53\n\t"
  "s_add_u32 s58, s58, 31744\n\t"
  "v_mfma_f32_16x16x32_bf16 v[52:55], v[72:75], v[140:143], v[52:55]\n\t"
  "v_mfma_f32_16x16x32_bf16 v[48:51], v[72:75], v[136:139], v[48:51]\n\t"
  "ds_read_b128 v[100:103], %[vb2] offset:2048\n\t"
  "v_mfma_f32_16x16x32_bf16 v[44:47], v[72:75], v[132:135], v[44:47]\n\t"
  "buffer_load_dwordx4 a[124:127], %[vl16], %[srd], s58 offen offset:0 sc1\n\t"
  "v_mfma_f32_16x16x32_bf16 v[40:43], v[72:75], v[128:131], v[40:43]\n\t"
  "ds_read_b64 v[72:73], %[va] offset:1408\n\t"
  "ds_read_b64 v[74:75], %[va] offset:1416\n\t"
  "v_mfma_f32_16x16x32_bf16 v[60:63], v[76:79], v[144:147], v[60:63]\n\t"
  "ds_read_b128 v[96:99], %[vb2] offset:0\n\t"
  "v_mfma_f32_16x16x32_bf16 v[56:59], v[76:79], v[140:143], v[56:59]\n\t"
  "buffer_load_dwordx4 a[128:131], %[vl16], %[srd], s58 offen offset:1024 sc1\n\t"
  "v_mfma_f32_16x16x32_bf16 v[52:55], v[76:79], v[136:139], v[52:55]\n\t"
  "v_mfma_f32_16x16x32_bf16 v[48:51], v[76:79], v[132:135], v[48:51]\n\t"
  "v_mfma_f32_16x16x32_bf16 v[44:47], v[76:79], v[128:131], v[44:47]\n\t"
  "ds_read_b64 v[76:77], %[va] offset:1920\n\t"
  "ds_read_b64 v[78:79], %[va] offset:1928\n\t"
  "v_mfma_f32_16x16x32_bf16 v[60:63], v[80:83], v[140:143], v[60:63]\n\t"
  "buffer_load_dwordx4 a[132:135], %[vl16], %[srd], s58 offen offset:2048 sc1\n\t"
  "v_mfma_f32_16x16x32_bf16 v[56:59], v[80:83], v[136:139], v[56:59]\n\t"
  "v_mfma_f32_16x16x32_bf16 v[52:55], v[80:83], v[132:135], v[52:55]\n\t"
  "v_mfma_f32_16x16x32_bf16 v[48:51], v[80:83], v[128:131], v[48:51]\n\t"
  "ds_read_b64 v[80:81], %[va] offset:2432\n\t"
  "ds_read_b64 v[82:83], %[va] offset:2440\n\t"
  "v_mfma_f32_16x16x32_bf16 v[60:63], v[84:87], v[136:139], v[60:63]\n\t"
  "buffer_load_dwordx4 a[136:139], %[vl16], %[srd], s58 offen offset:3072 sc1\n\t"
  "v_mfma_f32_16x16x32_bf16 v[56:59], v[84:87], v[132:135], v[56:59]\n\t"
  "v_mfma_f32_16x16x32_bf16 v[52:55], v[84:87], v[128:131], v[52:55]\n\t"
  "s_waitcnt lgkmcnt(15)\n\t"
  "ds_read_b64 v[84:85], %[va] offset:2944\n\t"
  "v_mfma_f32_16x16x32_bf16 v[60:63], v[88:91], v[132:135], v[60:63]\n\t"
  "ds_read_b64 v[86:87], %[va] offset:2952\n\t"
  "s_add_u32 s58, s58, 4096\n\t"
  "v_mfma_f32_16x16x32_bf16 v[56:59], v[88:91], v[128:131], v[56:59]\n\t"
  "ds_read_b64 v[88:89], %[va] offset:3456\n\t"
  "ds_read_b64 v[90:91], %[va] offset:3464\n\t"
  "v_mfma_f32_16x16x32_bf16 v[60:63], v[92:95], v[128:131], v[60:63]\n\t"
  "s_waitcnt lgkmcnt(8)\n\t"
  "ds_read_b64 v[92:93], %[va] offset:3968\n\t"
  "v_mfma_f32_16x16x32_bf16 v[60:63], v[64:67], v[124:127], v[60:63]\n\t"
  "ds_read_b128 v[156:159], %[vb3] offset:14336\n\t"
  "ds_read_b64 v[94:95], %[va] offset:3976\n\t"
  "v_mfma_f32_16x16x32_bf16 v[56:59], v[64:67], v[120:123], v[56:59]\n\t"
  "buffer_load_dwordx4 a[140:143], %[vl16], %[srd], s58 offen offset:0 sc1\n\t"
  "v_mfma_f32_16x16x32_bf16 v[52:55], v[64:67], v[116:119], v[52:55]\n\t"
  "buffer_load_dwordx4 a[144:147], %[vl16], %[srd], s58 offen offset:1024 sc1\n\t"
  "v_mfma_f32_16x16x32_bf16 v[48:51], v[64:67], v[112:115], v[48:51]\n\t"
  "ds_read_b128 v[152:155], %[vb3] offset:12288\n\t"
  "v_mfma_f32_16x16x32_bf16 v[44:47], v[64:67], v[108:111], v[44:47]\n\t"
  "v_mfma_f32_16x16x32_bf16 v[40:43], v[64:67], v[104:107], v[40:43]\n\t"
  "v_mfma_f32_16x16x32_bf16 v[36:39], v[64:67], v[100:103], v[36:39]\n\t"
  "ds_read_b128 v[148:151], %[vb3] offset:10240\n\t"
  "buffer_load_dwordx4 a[148:151], %[vl16], %[srd], s58 offen offset:2048 sc1\n\t"
  "v_mfma_f32_16x16x32_bf16 v[32:35], v[64:67], v[96:99], v[32:35]\n\t"
  "ds_read_b64 v[64:65], %[va] offset:448\n\t"
  "ds_read_b64 v[66:67], %[va] offset:456\n\t"
  "v_mfma_f32_16x16x32_bf16 v[60:63], v[68:71], v[120:123], v[60:63]\n\t"
  "v_mfma_f32_16x16x32_bf16 v[56:59], v[68:71], v[116:119], v[56:59]\n\t"
  "ds_read_b128 v[144:147], %[vb3] offset:8192\n\t"
  "buffer_load_dwordx4 a[152:155], %[vl16], %[srd], s58 offen offset:3072 sc1\n\t"
  "v_mfma_f32_16x16x32_bf16 v[52:55], v[68:71], v[112:115], v[52:55]\n\t"
  "v_mfma_f32_16x16x32_bf16 v[48:51], v[68:71], v[108:111], v[48:51]\n\t"
  "v_mfma_f32_16x16x32_bf16 v[44:47], v[68:71], v[104:107], v[44:47]\n\t"
  "ds_read_b128 v[140:143], %[vb3] offset:6144\n\t"
  "s_add_u32 s58, s58, 4096\n\t"
  "v_mfma_f32_16x16x32_bf16 v[40:43], v[68:71], v[100:103], v[40:43]\n\t"
  "v_mfma_f32_16x16x32_bf16 v[36:39], v[68:71], v[96:99], v[36:39]\n\t"
  "s_waitcnt lgkmcnt(11)\n\t"
  "ds_read_b64 v[68:69], %[va] offset:960\n\t"
  "v_mfma_f32_16x16x32_bf16 v[60:63], v[72:75], v[116:119], v[60:63]\n\t"
  "ds_read_b128 v[136:139], %[vb3] offset:4096\n\t"
  "ds_read_b64 v[70:71], %[va] offset:968\n\t"
  "v_mfma_f32_16x16x32_bf16 v[56:59], v[72:75], v[112:115], v[56:59]\n\t"
  "buffer_load_dwordx4 a[156:159], %[vl16], %[srd], s58 offen offset:0 sc1\n\t"
  "v_mfma_f32_16x16x32_bf16 v[52:55], v[72:75], v[108:111], v[52:55]\n\t"
  "v_mfma_f32_16x16x32_bf16 v[48:51], v[72:75], v[104:107], v[48:51]\n\t"
  "ds_read_b128 v[132:135], %[vb3] offset:2048\n\t"
  "v_mfma_f32_16x16x32_bf16 v[44:47], v[72:75], v[100:103], v[44:47]\n\t"
  "buffer_load_dwordx4 a[160:163], %[vl16], %[srd], s58 offen offset:1024 sc1\n\t"
  "v_mfma_f32_16x16x32_bf16 v[40:43], v[72:75], v[96:99], v[40:43]\n\t"
  "ds_read_b64 v[72:73], %[va] offset:1472\n\t"
  "ds_read_b64 v[74:75], %[va] offset:1480\n\t"
  "v_mfma_f32_16x16x32_bf16 v[60:63], v[76:79], v[112:115], v[60:63]\n\t"
  "ds_read_b128 v[128:131], %[vb3] offset:0\n\t"
  "v_mfma_f32_16x16x32_bf16 v[56:59], v[76:79], v[108:111], v[56:59]\n\t"
  "buffer_load_dwordx4 a[164:167], %[vl16], %[srd], s58 offen offset:2048 sc1\n\t"
  "s_waitcnt vmcnt(19)\n\t"
  "v_add_f32_dpp v244, v244, v244 quad_perm:[1,0,3,2] row_mask:0xf bank_mask:0xf\n\t"
  "v_mfma_f32_16x16x32_bf16 v[52:55], v[76:79], v[104:107], v[52:55]\n\t"
  "v_mfma_f32_16x16x32_bf16 v[48:51], v[76:79], v[100:103], v[48:51]\n\t"
  "v_add_f32_dpp v244, v244, v244 quad_perm:[2,3,0,1] row_mask:0xf bank_mask:0xf\n\t"
  "v_cvt_pk_bf16_f32 v244, 0, v244\n\t"
  "v_mfma_f32_16x16x32_bf16 v[44:47], v[76:79], v[96:99], v[44:47]\n\t"
  "ds_read_b64 v[76:77], %[va] offset:1984\n\t"
  "ds_read_b64 v[78:79], %[va] offset:1992\n\t"
  "v_mfma_f32_16x16x32_bf16 v[60:63], v[80:83], v[108:111], v[60:63]\n\t"
  "buffer_load_dwordx4 a[168:171], %[vl16], %[srd], s58 offen offset:3072 sc1\n\t"
  "v_add_f32 v245, 1.0, v244\n\t"
  "v_mfma_f32_16x16x32_bf16 v[56:59], v[80:83], v[104:107], v[56:59]\n\t"
  "v_cvt_pk_bf16_f32 v245, 0, v245\n\t"
  "v_readlane_b32 s46, v244, 0\n\t"
  "v_mfma_f32_16x16x32_bf16 v[52:55], v[80:83], v[100:103], v[52:55]\n\t"
  "s_add_u32 s58, s58, 4096\n\t"
  "v_readlane_b32 s48, v244, 8\n\t"
  "v_mfma_f32_16x16x32_bf16 v[48:51], v[80:83], v[96:99], v[48:51]\n\t"
  "ds_read_b64 v[80:81], %[va] offset:2496\n\t"
  "ds_read_b64 v[82:83], %[va] offset:2504\n\t"
  "v_mfma_f32_16x16x32_bf16 v[60:63], v[84:87], v[104:107], v[60:63]\n\t"
  "v_readlane_b32 s47, v245, 4\n\t"
  "v_readlane_b32 s51, v245, 12\n\t"
  "v_mfma_f32_16x16x32_bf16 v[56:59], v[84:87], v[100:103], v[56:59]\n\t"
  "v_mfma_f32_16x16x32_bf16 v[52:55], v[84:87], v[96:99], v[52:55]\n\t"
  "s_waitcnt lgkmcnt(15)\n\t"
  "ds_read_b64 v[84:85], %[va] offset:3008\n\t"
  "v_mfma_f32_16x16x32_bf16 v[60:63], v[88:91], v[100:103], v[60:63]\n\t"
  "ds_read_b64 v[86:87], %[va] offset:3016\n\t"
  "buffer_load_dwordx4 a[172:175], %[vl16], %[srd], s58 offen offset:0 sc1\n\t"
  "v_mfma_f32_16x16x32_bf16 v[56:59], v[88:91], v[96:99], v[56:59]\n\t"
  "ds_read_b64 v[88:89], %[va] offset:3520\n\t"
  "ds_read_b64 v[90:91], %[va] offset:3528\n\t"
  "v_mfma_f32_16x16x32_bf16 v[60:63], v[92:95], v[96:99], v[60:63]\n\t"
  "s_waitcnt lgkmcnt(8)\n\t"
  "ds_read_b64 v[92:93], %[va] offset:4032\n\t"
  "v_mfma_f32_16x16x32_bf16 v[60:63], v[64:67], v[156:159], v[60:63]\n\t"
  "ds_read_b64 v[94:95], %[va] offset:4040\n\t"
  "buffer_load_dwordx4 a[176:179], %[vl16], %[srd], s58 offen offset:1024 sc1\n\t"
  "v_mfma_f32_16x16x32_bf16 v[56:59], v[64:67], v[152:155], v[56:59]\n\t"
  "s_and_b32 s52, s51, 0x7fffffff\n\t"
  "s_cselect_b32 s51, s51, 0x2b800000\n\t"
  "v_mfma_f32_16x16x32_bf16 v[52:55], v[64:67], v[148:151], v[52:55]\n\t"
  "buffer_load_dwordx4 a[180:183], %[vl16], %[srd], s58 offen offset:2048 sc1\n\t"
  "v_mov_b32 v245, s48\n\t"
  "v_mfma_f32_16x16x32_bf16 v[48:51], v[64:67], v[144:147], v[48:51]\n\t"
  "v_mul_f32 v245, s51, v245\n\t"
  "v_mul_f32 v244, s47, v245\n\t"
  "v_mfma_f32_16x16x32_bf16 v[44:47], v[64:67], v[140:143], v[44:47]\n\t"
  "v_mul_f32 v245, s46, v245\n\t"
  "v_mfma_f32_16x16x32_bf16 v[40:43], v[64:67], v[136:139], v[40:43]\n\t"
  "buffer_load_dwordx4 a[184:187], %[vl16], %[srd], s58 offen offset:3072 sc1\n\t"
  "v_mfma_f32_16x16x32_bf16 v[36:39], v[64:67], v[132:135], v[36:39]\n\t"
  "s_waitcnt vmcnt(16)\n\t"
  "s_barrier\n\t"
  "v_mfma_f32_16x16x32_bf16 v[32:35], v[64:67], v[128:131], v[32:35]\n\t"
  "v_mov_b32 v246, s51\n\t"
  "ds_read_b64 v[224:225], %[vx] offset:0\n\t"
  "v_mfma_f32_16x16x32_bf16 v[60:63], v[68:71], v[152:155], v[60:63]\n\t"
  "ds_read_b128 v[160:163], %[vs] offset:0\n\t"
  "ds_read_b128 v[164:167], %[vs2] offset:0\n\t"
  "v_mfma_f32_16x16x32_bf16 v[56:59], v[68:71], v[148:151], v[56:59]\n\t"
  "s_add_u32 s58, s58, 4096\n\t"
  "ds_read_b64 v[226:227], %[vx] offset:2048\n\t"
  "v_mfma_f32_16x16x32_bf16 v[52:55], v[68:71], v[144:147], v[52:55]\n\t"
  "ds_read_b128 v[168:171], %[vs] offset:2048\n\t"
  "ds_read_b128 v[172:175], %[vs2] offset:2048\n\t"
  "v_mfma_f32_16x16x32_bf16 v[48:51], v[68:71], v[140:143], v[48:51]\n\t"
  "ds_read_b64 v[228:229], %[vx] offset:4096\n\t"
  "ds_read_b128 v[176:179], %[vs] offset:4096\n\t"
  "v_mfma_f32_16x16x32_bf16 v[44:47], v[68:71], v[136:139], v[44:47]\n\t"
  "buffer_load_dwordx4 a[188:191], %[vl16], %[srd], s58 offen offset:0 sc1\n\t"
  "ds_read_b128 v[180:183], %[vs2] offset:4096\n\t"
  "v_mfma_f32_16x16x32_bf16 v[40:43], v[68:71], v[132:135], v[40:43]\n\t"
  "ds_read_b64 v[230:231], %[vx] offset:6144\n\t"
  "ds_read_b128 v[184:187], %[vs] offset:6144\n\t"
  "v_mfma_f32_16x16x32_bf16 v[36:39], v[68:71], v[128:131], v[36:39]\n\t"
  "s_waitcnt lgkmcnt(15)\n\t"
  "ds_read_b128 v[188:191], %[vs2] offset:6144\n\t"
  "v_mfma_f32_16x16x32_bf16 v[60:63], v[72:75], v[148:151], v[60:63]\n\t"
  "buffer_load_dwordx4 a[192:195], %[vl16], %[srd], s58 offen offset:1024 sc1\n\t"
  "ds_read_b64 v[232:233], %[vx] offset:8192\n\t"
  "v_mfma_f32_16x16x32_bf16 v[56:59], v[72:75], v[144:147], v[56:59]\n\t"
  "ds_read_b128 v[192:195], %[vs] offset:8192\n\t"
  "ds_read_b128 v[196:199], %[vs2] offset:8192\n\t"
  "v_mfma_f32_16x16x32_bf16 v[52:55], v[72:75], v[140:143], v[52:55]\n\t"
  "ds_read_b64 v[234:235], %[vx] offset:10240\n\t"
  "ds_read_b128 v[200:203], %[vs] offset:10240\n\t"
  "v_mfma_f32_16x16x32_bf16 v[48:51], v[72:75], v[136:139], v[48:51]\n\t"
  "buffer_load_dwordx4 a[196:199], %[vl16], %[srd], s58 offen offset:2048 sc1\n\t"
  "ds_read_b128 v[204:207], %[vs2] offset:10240\n\t"
  "v_mfma_f32_16x16x32_bf16 v[44:47], v[72:75], v[132:135], v[44:47]\n\t"
  "ds_read_b64 v[236:237], %[vx] offset:12288\n\t"
  "ds_read_b128 v[208:211], %[vs] offset:12288\n\t"
  "v_mfma_f32_16x16x32_bf16 v[40:43], v[72:75], v[128:131], v[40:43]\n\t"
  "ds_read_b128 v[212:215], %[vs2] offset:12288\n\t"
  "ds_read_b64 v[238:239], %[vx] offset:14336\n\t"
  "v_mfma_f32_16x16x32_bf16 v[60:63], v[76:79], v[144:147], v[60:63]\n\t"
  "ds_read_b128 v[216:219], %[vs] offset:14336\n\t"
  "ds_read_b128 v[220:223], %[vs2] offset:14336\n\t"
  "v_mfma_f32_16x16x32_bf16 v[56:59], v[76:79], v[140:143], v[56:59]\n\t"
  "buffer_load_dwordx4 a[200:203], %[vl16], %[srd], s58 offen offset:3072 sc1\n\t"
  "v_mfma_f32_16x16x32_bf16 v[52:55], v[76:79], v[136:139], v[52:55]\n\t"
  "v_mfma_f32_16x16x32_bf16 v[48:51], v[76:79], v[132:135], v[48:51]\n\t"
  "v_mfma_f32_16x16x32_bf16 v[44:47], v[76:79], v[128:131], v[44:47]\n\t"
  "s_add_u32 s58, s58, 4096\n\t"
  "v_mfma_f32_16x16x32_bf16 v[60:63], v[80:83], v[140:143], v[60:63]\n\t"
  "v_mfma_f32_16x16x32_bf16 v[56:59], v[80:83], v[136:139], v[56:59]\n\t"
  "v_mfma_f32_16x16x32_bf16 v[52:55], v[80:83], v[132:135], v[52:55]\n\t"
  "buffer_load_dwordx4 a[204:207], %[vl16], %[srd], s58 offen offset:0 sc1\n\t"
  "v_mfma_f32_16x16x32_bf16 v[48:51], v[80:83], v[128:131], v[48:51]\n\t"
  "v_mfma_f32_16x16x32_bf16 v[60:63], v[84:87], v[136:139], v[60:63]\n\t"
  "v_mfma_f32_16x16x32_bf16 v[56:59], v[84:87], v[132:135], v[56:59]\n\t"
  "v_mfma_f32_16x16x32_bf16 v[52:55], v[84:87], v[128:131], v[52:55]\n\t"
  "s_waitcnt lgkmcnt(15)\n\t"
  "v_mfma_f32_16x16x32_bf16 v[60:63], v[88:91], v[132:135], v[60:63]\n\t"
  "v_mfma_f32_16x16x32_bf16 v[56:59], v[88:91], v[128:131], v[56:59]\n\t"
  "v_mfma_f32_16x16x32_bf16 v[60:63], v[92:95], v[128:131], v[60:63]\n\t"
  "v_lshlrev_b32 v96, 16, v224\n\t"
  "v_and_b32 v97, 0xffff0000, v224\n\t"
  "v_lshlrev_b32 v98, 16, v225\n\t"
  "v_and_b32 v99, 0xffff0000, v225\n\t"
  "v_lshlrev_b32 v100, 16, v226\n\t"
  "v_and_b32 v101, 0xffff0000, v226\n\t"
  "v_lshlrev_b32 v102, 16, v227\n\t"
  "v_and_b32 v103, 0xffff0000, v227\n\t"
  "v_pk_fma_f32 v[160:161], v[96:97], v[160:161], v[162:163]\n\t"
  "v_pk_fma_f32 v[164:165], v[98:99], v[164:165], v[166:167]\n\t"
  "v_pk_fma_f32 v[168:169], v[100:101], v[168:169], v[170:171]\n\t"
  "v_pk_fma_f32 v[172:173], v[102:103], v[172:173], v[174:175]\n\t"
  "v_pk_mul_f32 v[96:97], v[96:97], v[246:247] op_sel_hi:[1,0]\n\t"
  "v_pk_mul_f32 v[98:99], v[98:99], v[246:247] op_sel_hi:[1,0]\n\t"
  "v_pk_mul_f32 v[100:101], v[100:101], v[246:247] op_sel_hi:[1,0]\n\t"
  "v_pk_mul_f32 v[102:103], v[102:103], v[246:247] op_sel_hi:[1,0]\n\t"
  "v_pk_fma_f32 v[160:161], v[160:161], v[244:245], v[244:245] op_sel:[0,0,1] op_sel_hi:[1,0,1]\n\t"
  "v_pk_fma_f32 v[164:165], v[164:165], v[244:245], v[244:245] op_sel:[0,0,1] op_sel_hi:[1,0,1]\n\t"
  "v_pk_fma_f32 v[168:169], v[168:169], v[244:245], v[244:245] op_sel:[0,0,1] op_sel_hi:[1,0,1]\n\t"
  "v_pk_fma_f32 v[172:173], v[172:173], v[244:245], v[244:245] op_sel:[0,0,1] op_sel_hi:[1,0,1]\n\t"
  "v_pk_fma_f32 v[96:97], v[160:161], v[32:33], v[96:97]\n\t"
  "v_pk_fma_f32 v[98:99], v[164:165], v[34:35], v[98:99]\n\t"
  "v_pk_fma_f32 v[100:101], v[168:169], v[36:37], v[100:101]\n\t"
  "v_pk_fma_f32 v[102:103], v[172:173], v[38:39], v[102:103]\n\t"
  "v_cvt_pk_bf16_f32 v64, v96, v97\n\t"
  "v_cvt_pk_bf16_f32 v66, v100, v101\n\t"
  "v_cvt_pk_bf16_f32 v65, v98, v99\n\t"
  "v_cvt_pk_bf16_f32 v67, v102, v103\n\t"
  "s_add_u32 s44, %[sO], 0\n\t"
  "v_permlane16_swap_b32 v64, v66\n\t"
  "v_permlane16_swap_b32 v65, v67\n\t"
  "buffer_store_dwordx4 v[64:67], %[vo], %[srd], s44 offen sc1\n\t"
  "v_lshlrev_b32 v104, 16, v228\n\t"
  "v_and_b32 v105, 0xffff0000, v228\n\t"
  "v_lshlrev_b32 v106, 16, v229\n\t"
  "v_and_b32 v107, 0xffff0000, v229\n\t"
  "s_waitcnt lgkmcnt(14)\n\t"
  "v_lshlrev_b32 v108, 16, v230\n\t"
  "v_and_b32 v109, 0xffff0000, v230\n\t"
  "v_lshlrev_b32 v110, 16, v231\n\t"
  "v_and_b32 v111, 0xffff0000, v231\n\t"
  "v_pk_fma_f32 v[176:177], v[104:105], v[176:177], v[178:179]\n\t"
  "v_pk_fma_f32 v[180:181], v[106:107], v[180:181], v[182:183]\n\t"
  "s_waitcnt lgkmcnt(13)\n\t"
  "v_pk_fma_f32 v[184:185], v[108:109], v[184:185], v[186:187]\n\t"
  "s_waitcnt lgkmcnt(12)\n\t"
  "v_pk_fma_f32 v[188:189], v[110:111], v[188:189], v[190:191]\n\t"
  "v_pk_mul_f32 v[104:105], v[104:105], v[246:247] op_sel_hi:[1,0]\n\t"
  "v_pk_mul_f32 v[106:107], v[106:107], v[246:247] op_sel_hi:[1,0]\n\t"
  "v_pk_mul_f32 v[108:109], v[108:109], v[246:247] op_sel_hi:[1,0]\n\t"
  "v_pk_mul_f32 v[110:111], v[110:111], v[246:247] op_sel_hi:[1,0]\n\t"
  "v_pk_fma_f32 v[176:177], v[176:177], v[244:245], v[244:245] op_sel:[0,0,1] op_sel_hi:[1,0,1]\n\t"
  "v_pk_fma_f32 v[180:181], v[180:181], v[244:245], v[244:245] op_sel:[0,0,1] op_sel_hi:[1,0,1]\n\t"
  "v_pk_fma_f32 v[184:185], v[184:185], v[244:245], v[244:245] op_sel:[0,0,1] op_sel_hi:[1,0,1]\n\t"
  "v_pk_fma_f32 v[188:189], v[188:189], v[244:245], v[244:245] op_sel:[0,0,1] op_sel_hi:[1,0,1]\n\t"
  "v_pk_fma_f32 v[104:105], v[176:177], v[40:41], v[104:105]\n\t"
  "v_pk_fma_f32 v[106:107], v[180:181], v[42:43], v[106:107]\n\t"
  "v_pk_fma_f32 v[108:109], v[184:185], v[44:45], v[108:109]\n\t"
  "v_pk_fma_f32 v[110:111], v[188:189], v[46:47], v[110:111]\n\t"
  "v_cvt_pk_bf16_f32 v68, v104, v105\n\t"
  "v_cvt_pk_bf16_f32 v70, v108, v109\n\t"
  "v_cvt_pk_bf16_f32 v69, v106, v107\n\t"
  "v_cvt_pk_bf16_f32 v71, v110, v111\n\t"
  "s_add_u32 s44, %[sO], 655360\n\t"
  "v_permlane16_swap_b32 v68, v70\n\t"
  "v_permlane16_swap_b32 v69, v71\n\t"
  "buffer_store_dwordx4 v[68:71], %[vo], %[srd], s44 offen sc1\n\t"
  "s_waitcnt lgkmcnt(11)\n\t"
  "v_lshlrev_b32 v112, 16, v232\n\t"
  "v_and_b32 v113, 0xffff0000, v232\n\t"
  "v_lshlrev_b32 v114, 16, v233\n\t"
  "v_and_b32 v115, 0xffff0000, v233\n\t"
  "s_waitcnt lgkmcnt(8)\n\t"
  "v_lshlrev_b32 v116, 16, v234\n\t"
  "v_and_b32 v117, 0xffff0000, v234\n\t"
  "v_lshlrev_b32 v118, 16, v235\n\t"
  "v_and_b32 v119, 0xffff0000, v235\n\t"
  "v_pk_fma_f32 v[192:193], v[112:113], v[192:193], v[194:195]\n\t"
  "v_pk_fma_f32 v[196:197], v[114:115], v[196:197], v[198:199]\n\t"
  "s_waitcnt lgkmcnt(7)\n\t"
  "v_pk_fma_f32 v[200:201], v[116:117], v[200:201], v[202:203]\n\t"
  "s_waitcnt lgkmcnt(6)\n\t"
  "v_pk_fma_f32 v[204:205], v[118:119], v[204:205], v[206:207]\n\t"
  "v_pk_mul_f32 v[112:113], v[112:113], v[246:247] op_sel_hi:[1,0]\n\t"
  "v_pk_mul_f32 v[114:115], v[114:115], v[246:247] op_sel_hi:[1,0]\n\t"
  "v_pk_mul_f32 v[116:117], v[116:117], v[246:247] op_sel_hi:[1,0]\n\t"
  "v_pk_mul_f32 v[118:119], v[118:119], v[246:247] op_sel_hi:[1,0]\n\t"
  "v_pk_fma_f32 v[192:193], v[192:193], v[244:245], v[244:245] op_sel:[0,0,1] op_sel_hi:[1,0,1]\n\t"
  "v_pk_fma_f32 v[196:197], v[196:197], v[244:245], v[244:245] op_sel:[0,0,1] op_sel_hi:[1,0,1]\n\t"
  "v_pk_fma_f32 v[200:201], v[200:201], v[244:245], v[244:245] op_sel:[0,0,1] op_sel_hi:[1,0,1]\n\t"
  "v_pk_fma_f32 v[204:205], v[204:205], v[244:245], v[244:245] op_sel:[0,0,1] op_sel_hi:[1,0,1]\n\t"
  "v_pk_fma_f32 v[112:113], v[192:193], v[48:49], v[112:113]\n\t"
  "v_pk_fma_f32 v[114:115], v[196:197], v[50:51], v[114:115]\n\t"
  "v_pk_fma_f32 v[116:117], v[200:201], v[52:53], v[116:117]\n\t"
  "v_pk_fma_f32 v[118:119], v[204:205], v[54:55], v[118:119]\n\t"
  "v_cvt_pk_bf16_f32 v72, v112, v113\n\t"
  "v_cvt_pk_bf16_f32 v74, v116, v117\n\t"
  "v_cvt_pk_bf16_f32 v73, v114, v115\n\t"
  "v_cvt_pk_bf16_f32 v75, v118, v119\n\t"
  "s_add_u32 s44, %[sO], 1310720\n\t"
  "v_permlane16_swap_b32 v72, v74\n\t"
  "v_permlane16_swap_b32 v73, v75\n\t"
  "buffer_store_dwordx4 v[72:75], %[vo], %[srd], s44 offen sc1\n\t"
  "s_waitcnt lgkmcnt(5)\n\t"
  "v_lshlrev_b32 v120, 16, v236\n\t"
  "v_and_b32 v121, 0xffff0000, v236\n\t"
  "v_lshlrev_b32 v122, 16, v237\n\t"
  "v_and_b32 v123, 0xffff0000, v237\n\t"
  "s_waitcnt lgkmcnt(2)\n\t"
  "v_lshlrev_b32 v124, 16, v238\n\t"
  "v_and_b32 v125, 0xffff0000, v238\n\t"
  "v_lshlrev_b32 v126, 16, v239\n\t"
  "v_and_b32 v127, 0xffff0000, v239\n\t"
  "v_pk_fma_f32 v[208:209], v[120:121], v[208:209], v[210:211]\n\t"
  "v_pk_fma_f32 v[212:213], v[122:123], v[212:213], v[214:215]\n\t"
  "s_waitcnt lgkmcnt(1)\n\t"
  "v_pk_fma_f32 v[216:217], v[124:125], v[216:217], v[218:219]\n\t"
  "s_waitcnt lgkmcnt(0)\n\t"
  "v_pk_fma_f32 v[220:221], v[126:127], v[220:221], v[222:223]\n\t"
  "v_pk_mul_f32 v[120:121], v[120:121], v[246:247] op_sel_hi:[1,0]\n\t"
  "v_pk_mul_f32 v[122:123], v[122:123], v[246:247] op_sel_hi:[1,0]\n\t"
  "v_pk_mul_f32 v[124:125], v[124:125], v[246:247] op_sel_hi:[1,0]\n\t"
  "v_pk_mul_f32 v[126:127], v[126:127], v[246:247] op_sel_hi:[1,0]\n\t"
  "v_pk_fma_f32 v[208:209], v[208:209], v[244:245], v[244:245] op_sel:[0,0,1] op_sel_hi:[1,0,1]\n\t"
  "v_pk_fma_f32 v[212:213], v[212:213], v[244:245], v[244:245] op_sel:[0,0,1] op_sel_hi:[1,0,1]\n\t"
  "v_pk_fma_f32 v[216:217], v[216:217], v[244:245], v[244:245] op_sel:[0,0,1] op_sel_hi:[1,0,1]\n\t"
  "v_pk_fma_f32 v[220:221], v[220:221], v[244:245], v[244:245] op_sel:[0,0,1] op_sel_hi:[1,0,1]\n\t"
  "v_pk_fma_f32 v[120:121], v[208:209], v[56:57], v[120:121]\n\t"
  "v_pk_fma_f32 v[122:123], v[212:213], v[58:59], v[122:123]\n\t"
  "v_pk_fma_f32 v[124:125], v[216:217], v[60:61], v[124:125]\n\t"
  "v_pk_fma_f32 v[126:127], v[220:221], v[62:63], v[126:127]\n\t"
  "v_cvt_pk_bf16_f32 v76, v120, v121\n\t"
  "v_cvt_pk_bf16_f32 v78, v124, v125\n\t"
  "v_cvt_pk_bf16_f32 v77, v122, v123\n\t"
  "v_cvt_pk_bf16_f32 v79, v126, v127\n\t"
  "s_add_u32 s44, %[sO], 1966080\n\t"
  "v_permlane16_swap_b32 v76, v78\n\t"
  "v_permlane16_swap_b32 v77, v79\n\t"
  "buffer_store_dwordx4 v[76:79], %[vo], %[srd], s44 offen sc1\n\t"
  "s_nop 1\n\t"
  "s_branch Lcend%=\n\t"
  "La1_slow%=:\n\t"
  "s_nop 7\n\t"
  "s_nop 3\n\t"
  "s_mov_b32 s49, 0x40000\n\t"
  "La1_loop%=:\n\t"
  "s_sleep 3\n\t"
  "buffer_load_dwordx2 v[160:161], %[vf1], %[srd], 0 offen sc1\n\t"
  "s_waitcnt vmcnt(0)\n\t"
  "v_cmp_ne_u64 vcc, v[252:253], v[160:161]\n\t"
  "s_nop 3\n\t"
  "s_cmp_eq_u64 vcc, 0\n\t"
  "s_cbranch_scc1 La1_back%=\n\t"
  "s_sub_u32 s49, s49, 1\n\t"
  "s_cmp_lg_u32 s49, 0\n\t"
  "s_cbranch_scc1 La1_loop%=\n\t"
  "s_branch La1_back%=\n\t"
  "La2_slow%=:\n\t"
  "s_nop 7\n\t"
  "s_nop 3\n\t"
  "s_mov_b32 s49, 0x40000\n\t"
  "La2_loop%=:\n\t"
  "s_sleep 3\n\t"
  "buffer_load_dwordx2 v[162:163], %[vf2], %[srd], 0 offen sc1\n\t"
  "s_waitcnt vmcnt(0)\n\t"
  "v_cmp_ne_u64 vcc, v[252:253], v[162:163]\n\t"
  "s_nop 3\n\t"
  "s_cmp_eq_u64 vcc, 0\n\t"
  "s_cbranch_scc1 La2_back%=\n\t"
  "s_sub_u32 s49, s49, 1\n\t"
  "s_cmp_lg_u32 s49, 0\n\t"
  "s_cbranch_scc1 La2_loop%=\n\t"
  "s_branch La2_back%=\n\t"
  "Lcend%=:\n\t"
      : [wdone] "=&s"(wdone), [pm] "=&s"(pmask)
      : [srd] "s"(srd), [ws] "s"(ws), [sO] "s"(OFF_X2P + d * 128u), [w] "s"(w), [sKV] "s"(OFF_KVT + cb * 128u + w * 655360u),
        [tlo] "s"((u32)tag), [thi] "s"((u32)(tag >> 32)), [sW1] "s"(OFF_W1F + (j * 4 + w) * 61440u),
        [vf2] "v"(c_vf2),
        [vf1] "v"(c_vf1), [vp0] "v"(c_vp0), [vsa] "v"(lb + L_STASH + tid * 16 + 12), [vf3] "v"(c_vf3), [vfw] "v"(c_vfw),
        [va] "v"(c_va), [vb0] "v"(c_vb0), [vb1] "v"(c_vb1), [vb2] "v"(c_vb2), [vb3] "v"(c_vb3), [vx] "v"(c_vx), [vs] "v"(c_vs), [vs2] "v"(c_vs2), [vo] "v"(c_vo), [vd] "v"(c_vd),
        [vl16] "v"(c_vl16), [vkv] "v"(c_vkv),
        [vmod] "v"(c_vmod)
      : "memory", "vcc", "scc",
"a0", "a1", "a2", "a3", "a4", "a5", "a6", "a7", "a8", "a9", "a10", "a11", "a12", "a13", "a14", "a15", "a16", "a17", "a18", "a19", "a20", "a21", "a22", "a23", "a24", "a25", "a26", "a27", "a28", "a29", "a30", "a31", "a32", "a33", "a34", "a35", "a36", "a37", "a38", "a39", "a40", "a41", "a42", "a43", "a44", "a45", "a46", "a47", "a48", "a49", "a50", "a51", "a52", "a53", "a54", "a55", "a56", "a57", "a58", "a59", "a60", "a61", "a62", "a63", "a64", "a65", "a66", "a67", "a68", "a69", "a70", "a71", "a72", "a73", "a74", "a75", "a76", "a77", "a78", "a79", "a80", "a81", "a82", "a83", "a84", "a85", "a86", "a87", "a88", "a89", "a90", "a91", "a92", "a93", "a94", "a95", "a96", "a97", "a98", "a99", "a100", "a101", "a102", "a103", "a104", "a105", "a106", "a107", "a108", "a109", "a110", "a111", "a112", "a113", "a114", "a115", "a116", "a117", "a118", "a119", "a120", "a121", "a122", "a123", "a124", "a125", "a126", "a127", "a128", "a129", "a130", "a131", "a132", "a133", "a134", "a135", "a136", "a137", "a138", "a139", "a140", "a141", "a142", "a143", "a144", "a145", "a146", "a147", "a148", "a149", "a150", "a151", "a152", "a153", "a154", "a155", "a156", "a157", "a158", "a159", "a160", "a161", "a162", "a163", "a164", "a165", "a166", "a167", "a168", "a169", "a170", "a171", "a172", "a173", "a174", "a175", "a176", "a177", "a178", "a179", "a180", "a181", "a182", "a183", "a184", "a185", "a186", "a187", "a188", "a189", "a190", "a191", "a192", "a193", "a194", "a195", "a196", "a197", "a198", "a199", "a200", "a201", "a202", "a203", "a204", "a205", "a206", "a207", "s44", "s45", "s46", "s47", "s48", "s49", "s51", "s52", "s53", "s54", "s55", "s56", "s57", "s58", "s80", "s81", "v32", "v33", "v34", "v35", "v36", "v37", "v38", "v39", "v40", "v41", "v42", "v43", "v44", "v45", "v46", "v47", "v48", "v49", "v50", "v51", "v52", "v53", "v54", "v55", "v56", "v57", "v58", "v59", "v60", "v61", "v62", "v63", "v64", "v65", "v66", "v67", "v68", "v69", "v70", "v71", "v72", "v73", "v74", "v75", "v76", "v77", "v78", "v79", "v80", "v81", "v82", "v83", "v84", "v85", "v86", "v87", "v88", "v89", "v90", "v91", "v92", "v93", "v94", "v95", "v96", "v97", "v98", "v99", "v100", "v101", "v102", "v103", "v104", "v105", "v106", "v107", "v108", "v109", "v110", "v111", "v112", "v113", "v114", "v115", "v116", "v117", "v118", "v119", "v120", "v121", "v122", "v123", "v124", "v125", "v126", "v127", "v128", "v129", "v130", "v131", "v132", "v133", "v134", "v135", "v136", "v137", "v138", "v139", "v140", "v141", "v142", "v143", "v144", "v145", "v146", "v147", "v148", "v149", "v150", "v151", "v152", "v153", "v154", "v155", "v156", "v157", "v158", "v159", "v160", "v161", "v162", "v163", "v164", "v165", "v166", "v167", "v168", "v169", "v170", "v171", "v172", "v173", "v174", "v175", "v176", "v177", "v178", "v179", "v180", "v181", "v182", "v183", "v184", "v185", "v186", "v187", "v188", "v189", "v190", "v191", "v192", "v193", "v194", "v195", "v196", "v197", "v198", "v199", "v200", "v201", "v202", "v203", "v204", "v205", "v206", "v207", "v208", "v209", "v210", "v211", "v212", "v213", "v214", "v215", "v216", "v217", "v218", "v219", "v220", "v221", "v222", "v223", "v224", "v225", "v226", "v227", "v228", "v229", "v230", "v231", "v232", "v233", "v234", "v235", "v236", "v237", "v238", "v239", "v244", "v245", "v246", "v247", "v250", "v251", "v252", "v253", "v254", "v255"
    );
  }
  STAMP(4);
  STAMP_OUT(1);
  asm volatile(
  "s_mov_b64 s[68:69], %[ws]\n\t"
  "v_mbcnt_lo_u32_b32 v20, -1, 0\n\t"
  "v_mbcnt_hi_u32_b32 v20, -1, v20\n\t"
  "s_lshl_b32 s52, %[w], 10\n\t"
  "s_add_u32 s52, s52, 143360\n\t"
  "v_lshl_add_u32 v20, v20, 4, s52\n\t"
  "ds_read_b128 v[0:3], v20 offset:0\n\t"
  "ds_read_b128 v[4:7], v20 offset:4096\n\t"
  "ds_read_b128 v[8:11], v20 offset:8192\n\t"
  "ds_read_b128 v[12:15], v20 offset:12288\n\t"
  "ds_read_b128 v[16:19], v20 offset:16384\n\t"
  "s_waitcnt lgkmcnt(0)\n\t"
  "v_mov_b32 v132, %[tlo]\n\t"
  "v_mov_b32 v133, %[thi]\n\t"
  "v_mov_b32 v134, %[tlo]\n\t"
  "v_mov_b32 v135, %[thi]\n\t"
  "s_lshl_b32 s52, %[w], 12\n\t"
  "s_mov_b64 s[60:61], 0\n\t"
  "s_mov_b32 s62, 0\n\t"
  "v_bfrev_b32 v128, 1\n\t"
  "v_add_u32 v124, 40960, v6\n\t"
  "v_mov_b32 v112, 0\n\t"
  "v_mov_b32 v113, 0\n\t"
  "v_mov_b32 v114, 0\n\t"
  "v_mov_b32 v115, 0\n\t"
  "v_mov_b32 v116, 0\n\t"
  "v_mov_b32 v117, 0\n\t"
  "v_mov_b32 v118, 0\n\t"
  "v_mov_b32 v119, 0\n\t"
  "v_mov_b32 v120, 0\n\t"
  "v_mov_b32 v121, 0\n\t"
  "v_and_b32 v136, 0xf0, v1\n\t"
  "v_lshlrev_b32 v137, 3, v136\n\t"
  "v_lshlrev_b32 v136, 2, v136\n\t"
  "s_add_u32 s54, %[sST], 65536\n\t"
  "s_mul_i32 s48, %[w], 20480\n\t"
  "v_add_u32 v141, s48, v1\n\t"
  "v_xor_b32 v109, 32, v5\n\t"
  "v_mov_b32 v108, v5\n\t"
  "v_lshrrev_b32 v139, 2, v1\n\t"
  "s_mov_b32 s47, 419840\n\t"
  "v_not_b32 v126, v132\n\t"
  "v_mov_b32 v127, 0\n\t"
  "s_waitcnt vmcnt(0) lgkmcnt(0)\n\t"
  "s_barrier\n\t"
  "buffer_store_dwordx4 v[132:135], v0, %[srd], 0 offen sc1\n\t"
  "s_movk_i32 s59, 0\n\t"
  "buffer_store_dwordx4 v[112:115], v9, %[srd], s59 offen sc1\n\t"
  "s_movk_i32 s59, 8192\n\t"
  "buffer_store_dwordx4 v[112:115], v9, %[srd], s59 offen sc1\n\t"
  "s_movk_i32 s59, 16384\n\t"
  "buffer_store_dwordx4 v[112:115], v9, %[srd], s59 offen sc1\n\t"
  "s_movk_i32 s59, 24576\n\t"
  "buffer_store_dwordx4 v[112:115], v9, %[srd], s59 offen sc1\n\t"
  "s_bitcmp1_b32 %[wdone], 2\n\t"
  "s_cbranch_scc1 Lw2%=\n\t"
  "s_bitcmp1_b32 %[wdone], 6\n\t"
  "s_cbranch_scc1 Lpw_done%=\n\t"
  "s_mov_b32 s49, 0x40000\n\t"
  "Lpw_loop%=:\n\t"
  "buffer_load_dwordx2 v[130:131], v2, %[srd], 0 offen sc1\n\t"
  "s_waitcnt vmcnt(0)\n\t"
  "v_cmp_eq_u64 vcc, v[132:133], v[130:131]\n\t"
  "s_nop 3\n\t"
  "s_orn2_b64 vcc, vcc, 15\n\t"
  "s_cmp_eq_u64 vcc, -1\n\t"
  "s_cbranch_scc1 Lpw_done%=\n\t"
  "s_sleep 3\n\t"
  "s_sub_u32 s49, s49, 1\n\t"
  "s_cmp_lg_u32 s49, 0\n\t"
  "s_cbranch_scc1 Lpw_loop%=\n\t"
  "Lpw_done%=:\n\t"
  "s_add_u32 s44, %[sW1], 28672\n\t"
  "buffer_load_dwordx4 a[124:127], v1, %[srd], s44 offen offset:3072 sc1\n\t"
  "s_add_u32 s44, %[sW1], 32768\n\t"
  "buffer_load_dwordx4 a[128:131], v1, %[srd], s44 offen offset:0 sc1\n\t"
  "s_add_u32 s44, %[sW1], 32768\n\t"
  "buffer_load_dwordx4 a[132:135], v1, %[srd], s44 offen offset:1024 sc1\n\t"
  "s_add_u32 s44, %[sW1], 32768\n\t"
  "buffer_load_dwordx4 a[136:139], v1, %[srd], s44 offen offset:2048 sc1\n\t"
  "s_add_u32 s44, %[sW1], 32768\n\t"
  "buffer_load_dwordx4 a[140:143], v1, %[srd], s44 offen offset:3072 sc1\n\t"
  "s_add_u32 s44, %[sW1], 36864\n\t"
  "buffer_load_dwordx4 a[144:147], v1, %[srd], s44 offen offset:0 sc1\n\t"
  "s_add_u32 s44, %[sW1], 36864\n\t"
  "buffer_load_dwordx4 a[148:151], v1, %[srd], s44 offen offset:1024 sc1\n\t"
  "s_add_u32 s44, %[sW1], 36864\n\t"
  "buffer_load_dwordx4 a[152:155], v1, %[srd], s44 offen offset:2048 sc1\n\t"
  "s_add_u32 s44, %[sW1], 36864\n\t"
  "buffer_load_dwordx4 a[156:159], v1, %[srd], s44 offen offset:3072 sc1\n\t"
  "s_add_u32 s44, %[sW1], 40960\n\t"
  "buffer_load_dwordx4 a[160:163], v1, %[srd], s44 offen offset:0 sc1\n\t"
  "s_add_u32 s44, %[sW1], 40960\n\t"
  "buffer_load_dwordx4 a[164:167], v1, %[srd], s44 offen offset:1024 sc1\n\t"
  "s_add_u32 s44, %[sW1], 40960\n\t"
  "buffer_load_dwordx4 a[168:171], v1, %[srd], s44 offen offset:2048 sc1\n\t"
  "s_add_u32 s44, %[sW1], 40960\n\t"
  "buffer_load_dwordx4 a[172:175], v1, %[srd], s44 offen offset:3072 sc1\n\t"
  "s_add_u32 s44, %[sW1], 45056\n\t"
  "buffer_load_dwordx4 a[176:179], v1, %[srd], s44 offen offset:0 sc1\n\t"
  "s_add_u32 s44, %[sW1], 45056\n\t"
  "buffer_load_dwordx4 a[180:183], v1, %[srd], s44 offen offset:1024 sc1\n\t"
  "s_add_u32 s44, %[sW1], 45056\n\t"
  "buffer_load_dwordx4 a[184:187], v1, %[srd], s44 offen offset:2048 sc1\n\t"
  "s_add_u32 s44, %[sW1], 45056\n\t"
  "buffer_load_dwordx4 a[188:191], v1, %[srd], s44 offen offset:3072 sc1\n\t"
  "s_add_u32 s44, %[sW1], 49152\n\t"
  "buffer_load_dwordx4 a[192:195], v1, %[srd], s44 offen offset:0 sc1\n\t"
  "s_add_u32 s44, %[sW1], 49152\n\t"
  "buffer_load_dwordx4 a[196:199], v1, %[srd], s44 offen offset:1024 sc1\n\t"
  "s_add_u32 s44, %[sW1], 49152\n\t"
  "buffer_load_dwordx4 a[200:203], v1, %[srd], s44 offen offset:2048 sc1\n\t"
  "s_add_u32 s44, %[sW1], 49152\n\t"
  "buffer_load_dwordx4 a[204:207], v1, %[srd], s44 offen offset:3072 sc1\n\t"
  "Lw2%=:\n\t"
  "s_bitcmp1_b32 %[wdone], 1\n\t"
  "s_cbranch_scc1 Lw1%=\n\t"
  "s_add_u32 s44, %[sW1], 12288\n\t"
  "buffer_load_dwordx4 a[60:63], v1, %[srd], s44 offen offset:3072 sc1\n\t"
  "s_add_u32 s44, %[sW1], 16384\n\t"
  "buffer_load_dwordx4 a[64:67], v1, %[srd], s44 offen offset:0 sc1\n\t"
  "s_add_u32 s44, %[sW1], 16384\n\t"
  "buffer_load_dwordx4 a[68:71], v1, %[srd], s44 offen offset:1024 sc1\n\t"
  "s_add_u32 s44, %[sW1], 16384\n\t"
  "buffer_load_dwordx4 a[72:75], v1, %[srd], s44 offen offset:2048 sc1\n\t"
  "s_add_u32 s44, %[sW1], 16384\n\t"
  "buffer_load_dwordx4 a[76:79], v1, %[srd], s44 offen offset:3072 sc1\n\t"
  "s_add_u32 s44, %[sW1], 20480\n\t"
  "buffer_load_dwordx4 a[80:83], v1, %[srd], s44 offen offset:0 sc1\n\t"
  "s_add_u32 s44, %[sW1], 20480\n\t"
  "buffer_load_dwordx4 a[84:87], v1, %[srd], s44 offen offset:1024 sc1\n\t"
  "s_add_u32 s44, %[sW1], 20480\n\t"
  "buffer_load_dwordx4 a[88:91], v1, %[srd], s44 offen offset:2048 sc1\n\t"
  "s_add_u32 s44, %[sW1], 20480\n\t"
  "buffer_load_dwordx4 a[92:95], v1, %[srd], s44 offen offset:3072 sc1\n\t"
  "s_add_u32 s44, %[sW1], 24576\n\t"
  "buffer_load_dwordx4 a[96:99], v1, %[srd], s44 offen offset:0 sc1\n\t"
  "s_add_u32 s44, %[sW1], 24576\n\t"
  "buffer_load_dwordx4 a[100:103], v1, %[srd], s44 offen offset:1024 sc1\n\t"
  "s_add_u32 s44, %[sW1], 24576\n\t"
  "buffer_load_dwordx4 a[104:107], v1, %[srd], s44 offen offset:2048 sc1\n\t"
  "s_add_u32 s44, %[sW1], 24576\n\t"
  "buffer_load_dwordx4 a[108:111], v1, %[srd], s44 offen offset:3072 sc1\n\t"
  "s_add_u32 s44, %[sW1], 28672\n\t"
  "buffer_load_dwordx4 a[112:115], v1, %[srd], s44 offen offset:0 sc1\n\t"
  "s_add_u32 s44, %[sW1], 28672\n\t"
  "buffer_load_dwordx4 a[116:119], v1, %[srd], s44 offen offset:1024 sc1\n\t"
  "s_add_u32 s44, %[sW1], 28672\n\t"
  "buffer_load_dwordx4 a[120:123], v1, %[srd], s44 offen offset:2048 sc1\n\t"
  "Lw1%=:\n\t"
  "s_bitcmp1_b32 %[wdone], 0\n\t"
  "s_cbranch_scc1 Lw0%=\n\t"
  "s_add_u32 s44, %[sW1], 0\n\t"
  "buffer_load_dwordx4 a[0:3], v1, %[srd], s44 offen offset:0 sc1\n\t"
  "s_add_u32 s44, %[sW1], 0\n\t"
  "buffer_load_dwordx4 a[4:7], v1, %[srd], s44 offen offset:1024 sc1\n\t"
  "s_add_u32 s44, %[sW1], 0\n\t"
  "buffer_load_dwordx4 a[8:11], v1, %[srd], s44 offen offset:2048 sc1\n\t"
  "s_add_u32 s44, %[sW1], 0\n\t"
  "buffer_load_dwordx4 a[12:15], v1, %[srd], s44 offen offset:3072 sc1\n\t"
  "s_add_u32 s44, %[sW1], 4096\n\t"
  "buffer_load_dwordx4 a[16:19], v1, %[srd], s44 offen offset:0 sc1\n\t"
  "s_add_u32 s44, %[sW1], 4096\n\t"
  "buffer_load_dwordx4 a[20:23], v1, %[srd], s44 offen offset:1024 sc1\n\t"
  "s_add_u32 s44, %[sW1], 4096\n\t"
  "buffer_load_dwordx4 a[24:27], v1, %[srd], s44 offen offset:2048 sc1\n\t"
  "s_add_u32 s44, %[sW1], 4096\n\t"
  "buffer_load_dwordx4 a[28:31], v1, %[srd], s44 offen offset:3072 sc1\n\t"
  "s_add_u32 s44, %[sW1], 8192\n\t"
  "buffer_load_dwordx4 a[32:35], v1, %[srd], s44 offen offset:0 sc1\n\t"
  "s_add_u32 s44, %[sW1], 8192\n\t"
  "buffer_load_dwordx4 a[36:39], v1, %[srd], s44 offen offset:1024 sc1\n\t"
  "s_add_u32 s44, %[sW1], 8192\n\t"
  "buffer_load_dwordx4 a[40:43], v1, %[srd], s44 offen offset:2048 sc1\n\t"
  "s_add_u32 s44, %[sW1], 8192\n\t"
  "buffer_load_dwordx4 a[44:47], v1, %[srd], s44 offen offset:3072 sc1\n\t"
  "s_add_u32 s44, %[sW1], 12288\n\t"
  "buffer_load_dwordx4 a[48:51], v1, %[srd], s44 offen offset:0 sc1\n\t"
  "s_add_u32 s44, %[sW1], 12288\n\t"
  "buffer_load_dwordx4 a[52:55], v1, %[srd], s44 offen offset:1024 sc1\n\t"
  "s_add_u32 s44, %[sW1], 12288\n\t"
  "buffer_load_dwordx4 a[56:59], v1, %[srd], s44 offen offset:2048 sc1\n\t"
  "Lw0%=:\n\t"
  "s_mov_b32 s58, 2\n\t"
  "s_bitcmp1_b32 %[wdone], 7\n\t"
  "s_cbranch_scc1 Lpx_chk%=\n\t"
  "s_mov_b32 s58, 0\n\t"
  "v_cmp_eq_u64 vcc, v[132:133], v[126:127]\n\t"
  "s_nop 3\n\t"
  "s_or_b64 s[60:61], s[60:61], vcc\n\t"
  "s_cmp_eq_u64 s[60:61], -1\n\t"
  "s_cbranch_scc1 Lpx_done%=\n\t"
  "v_cndmask_b32 v3, v3, v128, vcc\n\t"
  "buffer_load_dwordx2 v[126:127], v3, %[srd], 0 offen sc1\n\t"
  "s_add_u32 s44, %[sW1], 53248\n\t"
  "buffer_load_dwordx4 a[208:211], v1, %[srd], s44 offen offset:0 sc1\n\t"
  "s_add_u32 s44, %[sW1], 53248\n\t"
  "buffer_load_dwordx4 a[212:215], v1, %[srd], s44 offen offset:1024 sc1\n\t"
  "s_add_u32 s44, %[sW1], 53248\n\t"
  "buffer_load_dwordx4 a[216:219], v1, %[srd], s44 offen offset:2048 sc1\n\t"
  "s_add_u32 s44, %[sW1], 53248\n\t"
  "buffer_load_dwordx4 a[220:223], v1, %[srd], s44 offen offset:3072 sc1\n\t"
  "s_add_u32 s44, %[sW1], 57344\n\t"
  "buffer_load_dwordx4 a[224:227], v1, %[srd], s44 offen offset:0 sc1\n\t"
  "s_add_u32 s44, %[sW1], 57344\n\t"
  "buffer_load_dwordx4 a[228:231], v1, %[srd], s44 offen offset:1024 sc1\n\t"
  "s_waitcnt vmcnt(6)\n\t"
  "s_mov_b32 s58, 1\n\t"
  "v_cmp_eq_u64 vcc, v[132:133], v[126:127]\n\t"
  "s_nop 3\n\t"
  "s_or_b64 s[60:61], s[60:61], vcc\n\t"
  "s_cmp_eq_u64 s[60:61], -1\n\t"
  "s_cbranch_scc1 Lpx_done%=\n\t"
  "v_cndmask_b32 v3, v3, v128, vcc\n\t"
  "buffer_load_dwordx2 v[126:127], v3, %[srd], 0 offen sc1\n\t"
  "s_add_u32 s44, %[sW1], 57344\n\t"
  "buffer_load_dwordx4 a[232:235], v1, %[srd], s44 offen offset:2048 sc1\n\t"
  "s_add_u32 s44, %[sW1], 57344\n\t"
  "buffer_load_dwordx4 a[236:239], v1, %[srd], s44 offen offset:3072 sc1\n\t"
  "s_waitcnt vmcnt(2)\n\t"
  "s_mov_b32 s58, 2\n\t"
  "Lpx_chk%=:\n\t"
  "s_mov_b32 s49, 0x40000\n\t"
  "Lpx_loop%=:\n\t"
  "v_cmp_eq_u64 vcc, v[132:133], v[126:127]\n\t"
  "s_nop 3\n\t"
  "s_or_b64 s[60:61], s[60:61], vcc\n\t"
  "s_cmp_eq_u64 s[60:61], -1\n\t"
  "s_cbranch_scc1 Lpx_done%=\n\t"
  "v_cndmask_b32 v3, v3, v128, vcc\n\t"
  "s_bitcmp1_b32 s62, 0\n\t"
  "s_cbranch_scc1 Le1%=\n\t"
  "s_bfe_u32 s63, s60, 0x80000\n\t"
  "s_cmpk_lg_u32 s63, 0xff\n\t"
  "s_cbranch_scc1 Le1%=\n\t"
  "s_bitset1_b32 s62, 0\n\t"
  "s_add_u32 m0, s52, 0\n\t"
  "s_mov_b32 s46, 0\n\t"
  "buffer_load_dwordx4 v4, %[srd], s46 offen offset:0 sc1 lds\n\t"
  "buffer_load_dwordx4 v4, %[srd], s46 offen offset:1024 sc1 lds\n\t"
  "buffer_load_dwordx4 v4, %[srd], s46 offen offset:2048 sc1 lds\n\t"
  "buffer_load_dwordx4 v4, %[srd], s46 offen offset:3072 sc1 lds\n\t"
  "Le1%=:\n\t"
  "s_bitcmp1_b32 s62, 1\n\t"
  "s_cbranch_scc1 Le2%=\n\t"
  "s_bfe_u32 s63, s60, 0x80008\n\t"
  "s_cmpk_lg_u32 s63, 0xff\n\t"
  "s_cbranch_scc1 Le2%=\n\t"
  "s_bitset1_b32 s62, 1\n\t"
  "s_add_u32 m0, s52, 16384\n\t"
  "s_mov_b32 s46, 16384\n\t"
  "buffer_load_dwordx4 v4, %[srd], s46 offen offset:0 sc1 lds\n\t"
  "buffer_load_dwordx4 v4, %[srd], s46 offen offset:1024 sc1 lds\n\t"
  "buffer_load_dwordx4 v4, %[srd], s46 offen offset:2048 sc1 lds\n\t"
  "buffer_load_dwordx4 v4, %[srd], s46 offen offset:3072 sc1 lds\n\t"
  "Le2%=:\n\t"
  "s_bitcmp1_b32 s62, 2\n\t"
  "s_cbranch_scc1 Le3%=\n\t"
  "s_bfe_u32 s63, s60, 0x80010\n\t"
  "s_cmpk_lg_u32 s63, 0xff\n\t"
  "s_cbranch_scc1 Le3%=\n\t"
  "s_bitset1_b32 s62, 2\n\t"
  "s_add_u32 m0, s52, 32768\n\t"
  "s_mov_b32 s46, 32768\n\t"
  "buffer_load_dwordx4 v4, %[srd], s46 offen offset:0 sc1 lds\n\t"
  "buffer_load_dwordx4 v4, %[srd], s46 offen offset:1024 sc1 lds\n\t"
  "buffer_load_dwordx4 v4, %[srd], s46 offen offset:2048 sc1 lds\n\t"
  "buffer_load_dwordx4 v4, %[srd], s46 offen offset:3072 sc1 lds\n\t"
  "Le3%=:\n\t"
  "s_bitcmp1_b32 s62, 3\n\t"
  "s_cbranch_scc1 Le4%=\n\t"
  "s_bfe_u32 s63, s60, 0x80018\n\t"
  "s_cmpk_lg_u32 s63, 0xff\n\t"
  "s_cbranch_scc1 Le4%=\n\t"
  "s_bitset1_b32 s62, 3\n\t"
  "s_add_u32 m0, s52, 49152\n\t"
  "s_mov_b32 s46, 49152\n\t"
  "buffer_load_dwordx4 v4, %[srd], s46 offen offset:0 sc1 lds\n\t"
  "buffer_load_dwordx4 v4, %[srd], s46 offen offset:1024 sc1 lds\n\t"
  "buffer_load_dwordx4 v4, %[srd], s46 offen offset:2048 sc1 lds\n\t"
  "buffer_load_dwordx4 v4, %[srd], s46 offen offset:3072 sc1 lds\n\t"
  "Le4%=:\n\t"
  "s_bitcmp1_b32 s62, 4\n\t"
  "s_cbranch_scc1 Le5%=\n\t"
  "s_bfe_u32 s63, s61, 0x80000\n\t"
  "s_cmpk_lg_u32 s63, 0xff\n\t"
  "s_cbranch_scc1 Le5%=\n\t"
  "s_bitset1_b32 s62, 4\n\t"
  "s_add_u32 m0, s52, 65536\n\t"
  "s_mov_b32 s46, 65536\n\t"
  "buffer_load_dwordx4 v4, %[srd], s46 offen offset:0 sc1 lds\n\t"
  "buffer_load_dwordx4 v4, %[srd], s46 offen offset:1024 sc1 lds\n\t"
  "buffer_load_dwordx4 v4, %[srd], s46 offen offset:2048 sc1 lds\n\t"
  "buffer_load_dwordx4 v4, %[srd], s46 offen offset:3072 sc1 lds\n\t"
  "Le5%=:\n\t"
  "s_bitcmp1_b32 s62, 5\n\t"
  "s_cbranch_scc1 Le6%=\n\t"
  "s_ashr_i32 s63, s61, 8\n\t"
  "s_cmp_lg_u32 s63, -1\n\t"
  "s_cbranch_scc1 Le6%=\n\t"
  "s_bitset1_b32 s62, 5\n\t"
  "buffer_load_dword v142, v139, %[srd], s47 offen offset:0 sc1\n\t"
  "buffer_load_dword v143, v139, %[srd], s47 offen offset:256 sc1\n\t"
  "buffer_load_dword v248, v139, %[srd], s47 offen offset:512 sc1\n\t"
  "buffer_load_dword v249, v139, %[srd], s47 offen offset:768 sc1\n\t"
  "buffer_load_dword v250, v139, %[srd], s47 offen offset:1024 sc1\n\t"
  "buffer_load_dword v251, v139, %[srd], s47 offen offset:1280 sc1\n\t"
  "buffer_load_dword v252, v139, %[srd], s47 offen offset:1536 sc1\n\t"
  "buffer_load_dword v253, v139, %[srd], s47 offen offset:1792 sc1\n\t"
  "buffer_load_dword v254, v139, %[srd], s47 offen offset:2048 sc1\n\t"
  "buffer_load_dword v255, v139, %[srd], s47 offen offset:2304 sc1\n\t"
  "Le6%=:\n\t"
  "buffer_load_dwordx2 v[126:127], v3, %[srd], 0 offen sc1\n\t"
  "s_sleep 2\n\t"
  "s_waitcnt vmcnt(0)\n\t"
  "s_sub_u32 s49, s49, 1\n\t"
  "s_cmp_lg_u32 s49, 0\n\t"
  "s_cbranch_scc1 Lpx_loop%=\n\t"
  "Lpx_done%=:\n\t"
  "s_bitcmp1_b32 s62, 0\n\t"
  "s_cbranch_scc1 Lf0%=\n\t"
  "s_add_u32 m0, s52, 0\n\t"
  "s_mov_b32 s46, 0\n\t"
  "buffer_load_dwordx4 v4, %[srd], s46 offen offset:0 sc1 lds\n\t"
  "buffer_load_dwordx4 v4, %[srd], s46 offen offset:1024 sc1 lds\n\t"
  "buffer_load_dwordx4 v4, %[srd], s46 offen offset:2048 sc1 lds\n\t"
  "buffer_load_dwordx4 v4, %[srd], s46 offen offset:3072 sc1 lds\n\t"
  "Lf0%=:\n\t"
  "s_bitcmp1_b32 s62, 1\n\t"
  "s_cbranch_scc1 Lf1%=\n\t"
  "s_add_u32 m0, s52, 16384\n\t"
  "s_mov_b32 s46, 16384\n\t"
  "buffer_load_dwordx4 v4, %[srd], s46 offen offset:0 sc1 lds\n\t"
  "buffer_load_dwordx4 v4, %[srd], s46 offen offset:1024 sc1 lds\n\t"
  "buffer_load_dwordx4 v4, %[srd], s46 offen offset:2048 sc1 lds\n\t"
  "buffer_load_dwordx4 v4, %[srd], s46 offen offset:3072 sc1 lds\n\t"
  "Lf1%=:\n\t"
  "s_bitcmp1_b32 s62, 2\n\t"
  "s_cbranch_scc1 Lf2%=\n\t"
  "s_add_u32 m0, s52, 32768\n\t"
  "s_mov_b32 s46, 32768\n\t"
  "buffer_load_dwordx4 v4, %[srd], s46 offen offset:0 sc1 lds\n\t"
  "buffer_load_dwordx4 v4, %[srd], s46 offen offset:1024 sc1 lds\n\t"
  "buffer_load_dwordx4 v4, %[srd], s46 offen offset:2048 sc1 lds\n\t"
  "buffer_load_dwordx4 v4, %[srd], s46 offen offset:3072 sc1 lds\n\t"
  "Lf2%=:\n\t"
  "s_bitcmp1_b32 s62, 3\n\t"
  "s_cbranch_scc1 Lf3%=\n\t"
  "s_add_u32 m0, s52, 49152\n\t"
  "s_mov_b32 s46, 49152\n\t"
  "buffer_load_dwordx4 v4, %[srd], s46 offen offset:0 sc1 lds\n\t"
  "buffer_load_dwordx4 v4, %[srd], s46 offen offset:1024 sc1 lds\n\t"
  "buffer_load_dwordx4 v4, %[srd], s46 offen offset:2048 sc1 lds\n\t"
  "buffer_load_dwordx4 v4, %[srd], s46 offen offset:3072 sc1 lds\n\t"
  "Lf3%=:\n\t"
  "s_bitcmp1_b32 s62, 4\n\t"
  "s_cbranch_scc1 Lf4%=\n\t"
  "s_add_u32 m0, s52, 65536\n\t"
  "s_mov_b32 s46, 65536\n\t"
  "buffer_load_dwordx4 v4, %[srd], s46 offen offset:0 sc1 lds\n\t"
  "buffer_load_dwordx4 v4, %[srd], s46 offen offset:1024 sc1 lds\n\t"
  "buffer_load_dwordx4 v4, %[srd], s46 offen offset:2048 sc1 lds\n\t"
  "buffer_load_dwordx4 v4, %[srd], s46 offen offset:3072 sc1 lds\n\t"
  "Lf4%=:\n\t"
  "s_bitcmp1_b32 s62, 5\n\t"
  "s_cbranch_scc1 Lf5%=\n\t"
  "buffer_load_dword v142, v139, %[srd], s47 offen offset:0 sc1\n\t"
  "buffer_load_dword v143, v139, %[srd], s47 offen offset:256 sc1\n\t"
  "buffer_load_dword v248, v139, %[srd], s47 offen offset:512 sc1\n\t"
  "buffer_load_dword v249, v139, %[srd], s47 offen offset:768 sc1\n\t"
  "buffer_load_dword v250, v139, %[srd], s47 offen offset:1024 sc1\n\t"
  "buffer_load_dword v251, v139, %[srd], s47 offen offset:1280 sc1\n\t"
  "buffer_load_dword v252, v139, %[srd], s47 offen offset:1536 sc1\n\t"
  "buffer_load_dword v253, v139, %[srd], s47 offen offset:1792 sc1\n\t"
  "buffer_load_dword v254, v139, %[srd], s47 offen offset:2048 sc1\n\t"
  "buffer_load_dword v255, v139, %[srd], s47 offen offset:2304 sc1\n\t"
  "Lf5%=:\n\t"
  "s_waitcnt vmcnt(0)\n\t"
  "s_cmp_gt_u32 s58, 0\n\t"
  "s_cbranch_scc1 Lc0%=\n\t"
  "s_add_u32 s44, %[sW1], 53248\n\t"
  "buffer_load_dwordx4 a[208:211], v1, %[srd], s44 offen offset:0 sc1\n\t"
  "s_add_u32 s44, %[sW1], 53248\n\t"
  "buffer_load_dwordx4 a[212:215], v1, %[srd], s44 offen offset:1024 sc1\n\t"
  "s_add_u32 s44, %[sW1], 53248\n\t"
  "buffer_load_dwordx4 a[216:219], v1, %[srd], s44 offen offset:2048 sc1\n\t"
  "s_add_u32 s44, %[sW1], 53248\n\t"
  "buffer_load_dwordx4 a[220:223], v1, %[srd], s44 offen offset:3072 sc1\n\t"
  "s_add_u32 s44, %[sW1], 57344\n\t"
  "buffer_load_dwordx4 a[224:227], v1, %[srd], s44 offen offset:0 sc1\n\t"
  "s_add_u32 s44, %[sW1], 57344\n\t"
  "buffer_load_dwordx4 a[228:231], v1, %[srd], s44 offen offset:1024 sc1\n\t"
  "Lc0%=:\n\t"
  "s_cmp_gt_u32 s58, 1\n\t"
  "s_cbranch_scc1 Lc1%=\n\t"
  "s_add_u32 s44, %[sW1], 57344\n\t"
  "buffer_load_dwordx4 a[232:235], v1, %[srd], s44 offen offset:2048 sc1\n\t"
  "s_add_u32 s44, %[sW1], 57344\n\t"
  "buffer_load_dwordx4 a[236:239], v1, %[srd], s44 offen offset:3072 sc1\n\t"
  "Lc1%=:\n\t"
  "s_barrier\n\t"
  "ds_read_b64_tr_b16 v[68:69], v108 offset:0\n\t"
  "ds_read_b64_tr_b16 v[70:71], v108 offset:2048\n\t"
  "ds_read_b64_tr_b16 v[72:73], v109 offset:0\n\t"
  "ds_read_b64_tr_b16 v[74:75], v109 offset:2048\n\t"
  "ds_read_b64_tr_b16 v[76:77], v108 offset:4096\n\t"
  "ds_read_b64_tr_b16 v[78:79], v108 offset:6144\n\t"
  "ds_read_b64_tr_b16 v[80:81], v109 offset:4096\n\t"
  "ds_read_b64_tr_b16 v[82:83], v109 offset:6144\n\t"
  "ds_read_u16_d16_hi v112, v6 offset:0\n\t"
  "ds_read_u16_d16_hi v113, v6 offset:8192\n\t"
  "ds_read_u16_d16_hi v114, v6 offset:16384\n\t"
  "ds_read_u16_d16_hi v115, v6 offset:24576\n\t"
  "ds_read_u16_d16_hi v116, v6 offset:32768\n\t"
  "ds_read_u16_d16_hi v117, v124 offset:0\n\t"
  "ds_read_u16_d16_hi v118, v124 offset:8192\n\t"
  "ds_read_u16_d16_hi v119, v124 offset:16384\n\t"
  "ds_read_u16_d16_hi v120, v124 offset:24576\n\t"
  "ds_read_u16_d16_hi v121, v124 offset:32768\n\t"
  "s_waitcnt lgkmcnt(15)\n\t"
  "v_mfma_f32_16x16x32_bf16 v[20:23], a[0:3], v[68:71], 0\n\t"
  "ds_read_b64_tr_b16 v[84:85], v108 offset:8192\n\t"
  "v_add_u32 v110, 40960, v5\n\t"
  "v_mfma_f32_16x16x32_bf16 v[24:27], a[4:7], v[68:71], 0\n\t"
  "ds_read_b64_tr_b16 v[86:87], v108 offset:10240\n\t"
  "v_xor_b32 v111, 32, v5\n\t"
  "v_mfma_f32_16x16x32_bf16 v[28:31], a[8:11], v[68:71], 0\n\t"
  "ds_read_b64_tr_b16 v[88:89], v109 offset:8192\n\t"
  "v_add_u32 v111, 40960, v111\n\t"
  "s_waitcnt lgkmcnt(15)\n\t"
  "v_mfma_f32_16x16x32_bf16 v[32:35], a[0:3], v[72:75], 0\n\t"
  "ds_read_b64_tr_b16 v[90:91], v109 offset:10240\n\t"
  "s_add_u32 m0, s52, 81920\n\t"
  "v_mfma_f32_16x16x32_bf16 v[36:39], a[4:7], v[72:75], 0\n\t"
  "s_mov_b32 s46, 1310720\n\t"
  "buffer_load_dwordx4 v4, %[srd], s46 offen offset:0 sc1 lds\n\t"
  "v_mfma_f32_16x16x32_bf16 v[40:43], a[8:11], v[72:75], 0\n\t"
  "s_waitcnt lgkmcnt(14)\n\t"
  "v_mfma_f32_16x16x32_bf16 v[20:23], a[12:15], v[76:79], v[20:23]\n\t"
  "ds_read_b64_tr_b16 v[68:69], v108 offset:12288\n\t"
  "v_mfma_f32_16x16x32_bf16 v[24:27], a[16:19], v[76:79], v[24:27]\n\t"
  "ds_read_b64_tr_b16 v[70:71], v108 offset:14336\n\t"
  "buffer_load_dwordx4 v4, %[srd], s46 offen offset:1024 sc1 lds\n\t"
  "v_mfma_f32_16x16x32_bf16 v[28:31], a[20:23], v[76:79], v[28:31]\n\t"
  "ds_read_b64_tr_b16 v[72:73], v109 offset:12288\n\t"
  "v_mfma_f32_16x16x32_bf16 v[32:35], a[12:15], v[80:83], v[32:35]\n\t"
  "ds_read_b64_tr_b16 v[74:75], v109 offset:14336\n\t"
  "s_waitcnt lgkmcnt(15)\n\t"
  "v_mul_f32 v112, v112, v142\n\t"
  "v_mul_f32 v113, v113, v143\n\t"
  "v_mfma_f32_16x16x32_bf16 v[36:39], a[16:19], v[80:83], v[36:39]\n\t"
  "v_mul_f32 v114, v114, v248\n\t"
  "s_waitcnt lgkmcnt(14)\n\t"
  "v_mul_f32 v115, v115, v249\n\t"
  "s_waitcnt lgkmcnt(13)\n\t"
  "v_mul_f32 v116, v116, v250\n\t"
  "v_mfma_f32_16x16x32_bf16 v[40:43], a[20:23], v[80:83], v[40:43]\n\t"
  "s_waitcnt lgkmcnt(4)\n\t"
  "v_mul_f32 v117, v117, v251\n\t"
  "v_mul_f32 v118, v118, v252\n\t"
  "v_mul_f32 v119, v119, v253\n\t"
  "v_mfma_f32_16x16x32_bf16 v[20:23], a[24:27], v[84:87], v[20:23]\n\t"
  "ds_read_b64_tr_b16 v[76:77], v108 offset:16384\n\t"
  "v_mul_f32 v120, v120, v254\n\t"
  "v_mul_f32 v121, v121, v255\n\t"
  "v_add_f32 v122, v112, v113\n\t"
  "v_mfma_f32_16x16x32_bf16 v[24:27], a[28:31], v[84:87], v[24:27]\n\t"
  "ds_read_b64_tr_b16 v[78:79], v108 offset:18432\n\t"
  "v_mul_f32 v123, v112, v112\n\t"
  "v_fmac_f32 v123, v113, v113\n\t"
  "v_add_f32 v122, v122, v114\n\t"
  "v_mfma_f32_16x16x32_bf16 v[28:31], a[32:35], v[84:87], v[28:31]\n\t"
  "ds_read_b64_tr_b16 v[80:81], v109 offset:16384\n\t"
  "v_fmac_f32 v123, v114, v114\n\t"
  "v_add_f32 v122, v122, v115\n\t"
  "v_fmac_f32 v123, v115, v115\n\t"
  "v_mfma_f32_16x16x32_bf16 v[32:35], a[24:27], v[88:91], v[32:35]\n\t"
  "ds_read_b64_tr_b16 v[82:83], v109 offset:18432\n\t"
  "v_add_f32 v122, v122, v116\n\t"
  "v_fmac_f32 v123, v116, v116\n\t"
  "v_add_f32 v122, v122, v117\n\t"
  "v_mfma_f32_16x16x32_bf16 v[36:39], a[28:31], v[88:91], v[36:39]\n\t"
  "v_fmac_f32 v123, v117, v117\n\t"
  "v_add_f32 v122, v122, v118\n\t"
  "v_fmac_f32 v123, v118, v118\n\t"
  "v_add_f32 v122, v122, v119\n\t"
  "v_mfma_f32_16x16x32_bf16 v[40:43], a[32:35], v[88:91], v[40:43]\n\t"
  "s_waitcnt lgkmcnt(4)\n\t"
  "v_fmac_f32 v123, v119, v119\n\t"
  "v_add_f32 v122, v122, v120\n\t"
  "v_fmac_f32 v123, v120, v120\n\t"
  "v_mfma_f32_16x16x32_bf16 v[20:23], a[36:39], v[68:71], v[20:23]\n\t"
  "ds_read_b64_tr_b16 v[84:85], v108 offset:20480\n\t"
  "v_add_f32 v122, v122, v121\n\t"
  "v_fmac_f32 v123, v121, v121\n\t"
  "buffer_load_dwordx4 v4, %[srd], s46 offen offset:2048 sc1 lds\n\t"
  "v_mfma_f32_16x16x32_bf16 v[24:27], a[40:43], v[68:71], v[24:27]\n\t"
  "ds_read_b64_tr_b16 v[86:87], v108 offset:22528\n\t"
  "v_add_f32_dpp v122, v122, v122 quad_perm:[1,0,3,2] row_mask:0xf bank_mask:0xf\n\t"
  "v_add_f32_dpp v123, v123, v123 quad_perm:[1,0,3,2] row_mask:0xf bank_mask:0xf\n\t"
  "buffer_load_dwordx4 v4, %[srd], s46 offen offset:3072 sc1 lds\n\t"
  "v_mfma_f32_16x16x32_bf16 v[28:31], a[44:47], v[68:71], v[28:31]\n\t"
  "ds_read_b64_tr_b16 v[88:89], v109 offset:20480\n\t"
  "v_add_f32_dpp v122, v122, v122 quad_perm:[2,3,0,1] row_mask:0xf bank_mask:0xf\n\t"
  "v_add_f32_dpp v123, v123, v123 quad_perm:[2,3,0,1] row_mask:0xf bank_mask:0xf\n\t"
  "s_add_u32 m0, s52, 98304\n\t"
  "v_mfma_f32_16x16x32_bf16 v[32:35], a[36:39], v[72:75], v[32:35]\n\t"
  "ds_read_b64_tr_b16 v[90:91], v109 offset:22528\n\t"
  "v_add_f32_dpp v122, v122, v122 row_half_mirror row_mask:0xf bank_mask:0xf\n\t"
  "v_add_f32_dpp v123, v123, v123 row_half_mirror row_mask:0xf bank_mask:0xf\n\t"
  "s_mov_b32 s46, 1327104\n\t"
  "v_mfma_f32_16x16x32_bf16 v[36:39], a[40:43], v[72:75], v[36:39]\n\t"
  "v_add_f32_dpp v122, v122, v122 row_mirror row_mask:0xf bank_mask:0xf\n\t"
  "v_add_f32_dpp v123, v123, v123 row_mirror row_mask:0xf bank_mask:0xf\n\t"
  "buffer_load_dwordx4 v4, %[srd], s46 offen offset:0 sc1 lds\n\t"
  "v_add_f32_dpp v122, v122, v122 row_bcast:15 row_mask:0xa bank_mask:0xf\n\t"
  "v_mfma_f32_16x16x32_bf16 v[40:43], a[44:47], v[72:75], v[40:43]\n\t"
  "s_waitcnt lgkmcnt(4)\n\t"
  "v_add_f32_dpp v123, v123, v123 row_bcast:15 row_mask:0xa bank_mask:0xf\n\t"
  "v_add_f32_dpp v122, v122, v122 row_bcast:31 row_mask:0xc bank_mask:0xf\n\t"
  "buffer_load_dwordx4 v4, %[srd], s46 offen offset:1024 sc1 lds\n\t"
  "v_mfma_f32_16x16x32_bf16 v[20:23], a[48:51], v[76:79], v[20:23]\n\t"
  "ds_read_b64_tr_b16 v[68:69], v108 offset:24576\n\t"
  "v_add_f32_dpp v123, v123, v123 row_bcast:31 row_mask:0xc bank_mask:0xf\n\t"
  "v_mul_f32 v122, 0xbacccccd, v122\n\t"
  "v_mul_f32 v123, 0x3acccccd, v123\n\t"
  "v_mfma_f32_16x16x32_bf16 v[24:27], a[52:55], v[76:79], v[24:27]\n\t"
  "ds_read_b64_tr_b16 v[70:71], v108 offset:26624\n\t"
  "v_fma_f32 v123, -v122, v122, v123\n\t"
  "v_max_f32 v123, 0, v123\n\t"
  "v_cvt_pk_bf16_f32 v123, 0, v123\n\t"
  "v_mfma_f32_16x16x32_bf16 v[28:31], a[56:59], v[76:79], v[28:31]\n\t"
  "ds_read_b64_tr_b16 v[72:73], v109 offset:24576\n\t"
  "v_cvt_pk_bf16_f32 v126, v122, v122\n\t"
  "v_add_f32 v123, 0x3727c5ac, v123\n\t"
  "v_cvt_pk_bf16_f32 v123, 0, v123\n\t"
  "v_mfma_f32_16x16x32_bf16 v[32:35], a[48:51], v[80:83], v[32:35]\n\t"
  "ds_read_b64_tr_b16 v[74:75], v109 offset:26624\n\t"
  "v_sqrt_f32 v123, v123\n\t"
  "buffer_load_dwordx4 v4, %[srd], s46 offen offset:2048 sc1 lds\n\t"
  "v_cvt_pk_bf16_f32 v127, v123, v123\n\t"
  "v_mfma_f32_16x16x32_bf16 v[36:39], a[52:55], v[80:83], v[36:39]\n\t"
  "buffer_store_dwordx2 v[126:127], v7, %[srd], %[sST] offen sc1\n\t"
  "buffer_load_dwordx4 v4, %[srd], s46 offen offset:3072 sc1 lds\n\t"
  "s_add_u32 m0, s52, 114688\n\t"
  "v_add_u32 v124, 122880, v6\n\t"
  "v_mfma_f32_16x16x32_bf16 v[40:43], a[56:59], v[80:83], v[40:43]\n\t"
  "s_waitcnt lgkmcnt(4)\n\t"
  "v_add_u32 v125, 81920, v6\n\t"
  "v_mfma_f32_16x16x32_bf16 v[20:23], a[60:63], v[84:87], v[20:23]\n\t"
  "ds_read_b64_tr_b16 v[76:77], v108 offset:28672\n\t"
  "v_mov_b32 v112, 0\n\t"
  "v_mfma_f32_16x16x32_bf16 v[24:27], a[64:67], v[84:87], v[24:27]\n\t"
  "ds_read_b64_tr_b16 v[78:79], v108 offset:30720\n\t"
  "s_mov_b32 s46, 1343488\n\t"
  "v_mfma_f32_16x16x32_bf16 v[28:31], a[68:71], v[84:87], v[28:31]\n\t"
  "ds_read_b64_tr_b16 v[80:81], v109 offset:28672\n\t"
  "v_mov_b32 v113, 0\n\t"
  "v_mfma_f32_16x16x32_bf16 v[32:35], a[60:63], v[88:91], v[32:35]\n\t"
  "ds_read_b64_tr_b16 v[82:83], v109 offset:30720\n\t"
  "buffer_load_dwordx4 v4, %[srd], s46 offen offset:0 sc1 lds\n\t"
  "v_mfma_f32_16x16x32_bf16 v[36:39], a[64:67], v[88:91], v[36:39]\n\t"
  "v_mov_b32 v114, 0\n\t"
  "v_mov_b32 v115, 0\n\t"
  "v_mfma_f32_16x16x32_bf16 v[40:43], a[68:71], v[88:91], v[40:43]\n\t"
  "s_waitcnt lgkmcnt(4)\n\t"
  "v_mov_b32 v116, 0\n\t"
  "v_mfma_f32_16x16x32_bf16 v[20:23], a[72:75], v[68:71], v[20:23]\n\t"
  "ds_read_b64_tr_b16 v[84:85], v108 offset:32768\n\t"
  "buffer_load_dwordx4 v4, %[srd], s46 offen offset:1024 sc1 lds\n\t"
  "v_mfma_f32_16x16x32_bf16 v[24:27], a[76:79], v[68:71], v[24:27]\n\t"
  "ds_read_b64_tr_b16 v[86:87], v108 offset:34816\n\t"
  "v_mov_b32 v117, 0\n\t"
  "v_mfma_f32_16x16x32_bf16 v[28:31], a[80:83], v[68:71], v[28:31]\n\t"
  "ds_read_b64_tr_b16 v[88:89], v109 offset:32768\n\t"
  "buffer_load_dwordx4 v4, %[srd], s46 offen offset:2048 sc1 lds\n\t"
  "v_mfma_f32_16x16x32_bf16 v[32:35], a[72:75], v[72:75], v[32:35]\n\t"
  "ds_read_b64_tr_b16 v[90:91], v109 offset:34816\n\t"
  "v_mov_b32 v118, 0\n\t"
  "v_mfma_f32_16x16x32_bf16 v[36:39], a[76:79], v[72:75], v[36:39]\n\t"
  "buffer_load_dwordx4 v4, %[srd], s46 offen offset:3072 sc1 lds\n\t"
  "v_mov_b32 v119, 0\n\t"
  "v_mfma_f32_16x16x32_bf16 v[40:43], a[80:83], v[72:75], v[40:43]\n\t"
  "s_waitcnt lgkmcnt(4)\n\t"
  "v_mov_b32 v120, 0\n\t"
  "v_mfma_f32_16x16x32_bf16 v[20:23], a[84:87], v[76:79], v[20:23]\n\t"
  "ds_read_b64_tr_b16 v[68:69], v108 offset:36864\n\t"
  "v_mov_b32 v121, 0\n\t"
  "v_mfma_f32_16x16x32_bf16 v[24:27], a[88:91], v[76:79], v[24:27]\n\t"
  "ds_read_b64_tr_b16 v[70:71], v108 offset:38912\n\t"
  "s_add_u32 m0, s52, 131072\n\t"
  "v_mfma_f32_16x16x32_bf16 v[28:31], a[92:95], v[76:79], v[28:31]\n\t"
  "ds_read_b64_tr_b16 v[72:73], v109 offset:36864\n\t"
  "v_mfma_f32_16x16x32_bf16 v[32:35], a[84:87], v[80:83], v[32:35]\n\t"
  "ds_read_b64_tr_b16 v[74:75], v109 offset:38912\n\t"
  "s_mov_b32 s46, 1359872\n\t"
  "v_mfma_f32_16x16x32_bf16 v[36:39], a[88:91], v[80:83], v[36:39]\n\t"
  "buffer_load_dwordx2 v[138:139], v2, %[srd], 0 offen sc1\n\t"
  "v_mfma_f32_16x16x32_bf16 v[40:43], a[92:95], v[80:83], v[40:43]\n\t"
  "s_waitcnt lgkmcnt(4)\n\t"
  "v_mfma_f32_16x16x32_bf16 v[20:23], a[96:99], v[84:87], v[20:23]\n\t"
  "ds_read_b64_tr_b16 v[76:77], v110 offset:0\n\t"
  "buffer_load_dwordx4 v4, %[srd], s46 offen offset:0 sc1 lds\n\t"
  "v_mfma_f32_16x16x32_bf16 v[24:27], a[100:103], v[84:87], v[24:27]\n\t"
  "ds_read_b64_tr_b16 v[78:79], v110 offset:2048\n\t"
  "v_mfma_f32_16x16x32_bf16 v[28:31], a[104:107], v[84:87], v[28:31]\n\t"
  "ds_read_b64_tr_b16 v[80:81], v111 offset:0\n\t"
  "buffer_load_dwordx4 v4, %[srd], s46 offen offset:1024 sc1 lds\n\t"
  "v_mfma_f32_16x16x32_bf16 v[32:35], a[96:99], v[88:91], v[32:35]\n\t"
  "ds_read_b64_tr_b16 v[82:83], v111 offset:2048\n\t"
  "v_mfma_f32_16x16x32_bf16 v[36:39], a[100:103], v[88:91], v[36:39]\n\t"
  "buffer_load_dwordx4 v4, %[srd], s46 offen offset:2048 sc1 lds\n\t"
  "v_mfma_f32_16x16x32_bf16 v[40:43], a[104:107], v[88:91], v[40:43]\n\t"
  "s_waitcnt lgkmcnt(4)\n\t"
  "v_mfma_f32_16x16x32_bf16 v[20:23], a[108:111], v[68:71], v[20:23]\n\t"
  "ds_read_b64_tr_b16 v[84:85], v110 offset:4096\n\t"
  "v_mfma_f32_16x16x32_bf16 v[24:27], a[112:115], v[68:71], v[24:27]\n\t"
  "ds_read_b64_tr_b16 v[86:87], v110 offset:6144\n\t"
  "buffer_load_dwordx4 v4, %[srd], s46 offen offset:3072 sc1 lds\n\t"
  "v_mfma_f32_16x16x32_bf16 v[28:31], a[116:119], v[68:71], v[28:31]\n\t"
  "ds_read_b64_tr_b16 v[88:89], v111 offset:4096\n\t"
  "v_mfma_f32_16x16x32_bf16 v[32:35], a[108:111], v[72:75], v[32:35]\n\t"
  "ds_read_b64_tr_b16 v[90:91], v111 offset:6144\n\t"
  "s_add_u32 m0, s52, 147456\n\t"
  "v_mfma_f32_16x16x32_bf16 v[36:39], a[112:115], v[72:75], v[36:39]\n\t"
  "v_mfma_f32_16x16x32_bf16 v[40:43], a[116:119], v[72:75], v[40:43]\n\t"
  "s_waitcnt lgkmcnt(4)\n\t"
  "v_mfma_f32_16x16x32_bf16 v[20:23], a[120:123], v[76:79], v[20:23]\n\t"
  "ds_read_b64_tr_b16 v[68:69], v110 offset:8192\n\t"
  "v_xor_b32 v108, 64, v5\n\t"
  "v_mfma_f32_16x16x32_bf16 v[24:27], a[124:127], v[76:79], v[24:27]\n\t"
  "ds_read_b64_tr_b16 v[70:71], v110 offset:10240\n\t"
  "v_add_u32 v108, 0, v108\n\t"
  "v_mfma_f32_16x16x32_bf16 v[28:31], a[128:131], v[76:79], v[28:31]\n\t"
  "ds_read_b64_tr_b16 v[72:73], v111 offset:8192\n\t"
  "v_xor_b32 v109, 96, v5\n\t"
  "v_mfma_f32_16x16x32_bf16 v[32:35], a[120:123], v[80:83], v[32:35]\n\t"
  "ds_read_b64_tr_b16 v[74:75], v111 offset:10240\n\t"
  "v_add_u32 v109, 0, v109\n\t"
  "v_mfma_f32_16x16x32_bf16 v[36:39], a[124:127], v[80:83], v[36:39]\n\t"
  "s_mov_b32 s46, 1376256\n\t"
  "buffer_load_dwordx4 v4, %[srd], s46 offen offset:0 sc1 lds\n\t"
  "v_mfma_f32_16x16x32_bf16 v[40:43], a[128:131], v[80:83], v[40:43]\n\t"
  "s_waitcnt lgkmcnt(4)\n\t"
  "buffer_load_dwordx4 v4, %[srd], s46 offen offset:1024 sc1 lds\n\t"
  "v_mfma_f32_16x16x32_bf16 v[20:23], a[132:135], v[84:87], v[20:23]\n\t"
  "ds_read_b64_tr_b16 v[76:77], v110 offset:12288\n\t"
  "v_mfma_f32_16x16x32_bf16 v[24:27], a[136:139], v[84:87], v[24:27]\n\t"
  "ds_read_b64_tr_b16 v[78:79], v110 offset:14336\n\t"
  "buffer_load_dwordx4 v4, %[srd], s46 offen offset:2048 sc1 lds\n\t"
  "v_mfma_f32_16x16x32_bf16 v[28:31], a[140:143], v[84:87], v[28:31]\n\t"
  "ds_read_b64_tr_b16 v[80:81], v111 offset:12288\n\t"
  "v_mfma_f32_16x16x32_bf16 v[32:35], a[132:135], v[88:91], v[32:35]\n\t"
  "ds_read_b64_tr_b16 v[82:83], v111 offset:14336\n\t"
  "buffer_load_dwordx4 v4, %[srd], s46 offen offset:3072 sc1 lds\n\t"
  "v_mfma_f32_16x16x32_bf16 v[36:39], a[136:139], v[88:91], v[36:39]\n\t"
  "v_mfma_f32_16x16x32_bf16 v[40:43], a[140:143], v[88:91], v[40:43]\n\t"
  "s_waitcnt lgkmcnt(4)\n\t"
  "v_mfma_f32_16x16x32_bf16 v[20:23], a[144:147], v[68:71], v[20:23]\n\t"
  "ds_read_b64_tr_b16 v[84:85], v110 offset:16384\n\t"
  "v_mfma_f32_16x16x32_bf16 v[24:27], a[148:151], v[68:71], v[24:27]\n\t"
  "ds_read_b64_tr_b16 v[86:87], v110 offset:18432\n\t"
  "v_mfma_f32_16x16x32_bf16 v[28:31], a[152:155], v[68:71], v[28:31]\n\t"
  "ds_read_b64_tr_b16 v[88:89], v111 offset:16384\n\t"
  "v_mfma_f32_16x16x32_bf16 v[32:35], a[144:147], v[72:75], v[32:35]\n\t"
  "ds_read_b64_tr_b16 v[90:91], v111 offset:18432\n\t"
  "v_mfma_f32_16x16x32_bf16 v[36:39], a[148:151], v[72:75], v[36:39]\n\t"
  "v_mfma_f32_16x16x32_bf16 v[40:43], a[152:155], v[72:75], v[40:43]\n\t"
  "s_waitcnt lgkmcnt(4)\n\t"
  "v_mfma_f32_16x16x32_bf16 v[20:23], a[156:159], v[76:79], v[20:23]\n\t"
  "ds_read_b64_tr_b16 v[68:69], v110 offset:20480\n\t"
  "v_mfma_f32_16x16x32_bf16 v[24:27], a[160:163], v[76:79], v[24:27]\n\t"
  "ds_read_b64_tr_b16 v[70:71], v110 offset:22528\n\t"
  "v_mfma_f32_16x16x32_bf16 v[28:31], a[164:167], v[76:79], v[28:31]\n\t"
  "ds_read_b64_tr_b16 v[72:73], v111 offset:20480\n\t"
  "v_mfma_f32_16x16x32_bf16 v[32:35], a[156:159], v[80:83], v[32:35]\n\t"
  "ds_read_b64_tr_b16 v[74:75], v111 offset:22528\n\t"
  "v_mfma_f32_16x16x32_bf16 v[36:39], a[160:163], v[80:83], v[36:39]\n\t"
  "v_mfma_f32_16x16x32_bf16 v[40:43], a[164:167], v[80:83], v[40:43]\n\t"
  "s_waitcnt lgkmcnt(4)\n\t"
  "v_mfma_f32_16x16x32_bf16 v[20:23], a[168:171], v[84:87], v[20:23]\n\t"
  "ds_read_b64_tr_b16 v[76:77], v110 offset:24576\n\t"
  "v_mfma_f32_16x16x32_bf16 v[24:27], a[172:175], v[84:87], v[24:27]\n\t"
  "ds_read_b64_tr_b16 v[78:79], v110 offset:26624\n\t"
  "v_mfma_f32_16x16x32_bf16 v[28:31], a[176:179], v[84:87], v[28:31]\n\t"
  "ds_read_b64_tr_b16 v[80:81], v111 offset:24576\n\t"
  "v_mfma_f32_16x16x32_bf16 v[32:35], a[168:171], v[88:91], v[32:35]\n\t"
  "ds_read_b64_tr_b16 v[82:83], v111 offset:26624\n\t"
  "v_mfma_f32_16x16x32_bf16 v[36:39], a[172:175], v[88:91], v[36:39]\n\t"
  "v_mfma_f32_16x16x32_bf16 v[40:43], a[176:179], v[88:91], v[40:43]\n\t"
  "s_waitcnt lgkmcnt(4)\n\t"
  "v_mfma_f32_16x16x32_bf16 v[20:23], a[180:183], v[68:71], v[20:23]\n\t"
  "ds_read_b64_tr_b16 v[84:85], v110 offset:28672\n\t"
  "v_mfma_f32_16x16x32_bf16 v[24:27], a[184:187], v[68:71], v[24:27]\n\t"
  "ds_read_b64_tr_b16 v[86:87], v110 offset:30720\n\t"
  "v_mfma_f32_16x16x32_bf16 v[28:31], a[188:191], v[68:71], v[28:31]\n\t"
  "ds_read_b64_tr_b16 v[88:89], v111 offset:28672\n\t"
  "v_mfma_f32_16x16x32_bf16 v[32:35], a[180:183], v[72:75], v[32:35]\n\t"
  "ds_read_b64_tr_b16 v[90:91], v111 offset:30720\n\t"
  "v_mfma_f32_16x16x32_bf16 v[36:39], a[184:187], v[72:75], v[36:39]\n\t"
  "v_mfma_f32_16x16x32_bf16 v[40:43], a[188:191], v[72:75], v[40:43]\n\t"
  "s_waitcnt lgkmcnt(4)\n\t"
  "v_mfma_f32_16x16x32_bf16 v[20:23], a[192:195], v[76:79], v[20:23]\n\t"
  "ds_read_b64_tr_b16 v[68:69], v110 offset:32768\n\t"
  "s_waitcnt vmcnt(8)\n\t"
  "v_cmp_ne_u64 vcc, v[132:133], v[138:139]\n\t"
  "v_mfma_f32_16x16x32_bf16 v[24:27], a[196:199], v[76:79], v[24:27]\n\t"
  "ds_read_b64_tr_b16 v[70:71], v110 offset:34816\n\t"
  "v_mfma_f32_16x16x32_bf16 v[28:31], a[200:203], v[76:79], v[28:31]\n\t"
  "ds_read_b64_tr_b16 v[72:73], v111 offset:32768\n\t"
  "s_cbranch_vccnz Lw2_slow%=\n\t"
  "Lw2_back%=:\n\t"
  "v_mfma_f32_16x16x32_bf16 v[32:35], a[192:195], v[80:83], v[32:35]\n\t"
  "ds_read_b64_tr_b16 v[74:75], v111 offset:34816\n\t"
  "v_mfma_f32_16x16x32_bf16 v[36:39], a[196:199], v[80:83], v[36:39]\n\t"
  "v_mfma_f32_16x16x32_bf16 v[40:43], a[200:203], v[80:83], v[40:43]\n\t"
  "s_waitcnt lgkmcnt(4)\n\t"
  "v_mfma_f32_16x16x32_bf16 v[20:23], a[204:207], v[84:87], v[20:23]\n\t"
  "ds_read_b64_tr_b16 v[76:77], v110 offset:36864\n\t"
  "v_lshrrev_b32 v138, 1, v1\n\t"
  "v_mfma_f32_16x16x32_bf16 v[24:27], a[208:211], v[84:87], v[24:27]\n\t"
  "ds_read_b64_tr_b16 v[78:79], v110 offset:38912\n\t"
  "buffer_load_dwordx2 a[240:241], v138, %[srd], %[sUB] offen offset:0 sc1\n\t"
  "v_mfma_f32_16x16x32_bf16 v[28:31], a[212:215], v[84:87], v[28:31]\n\t"
  "ds_read_b64_tr_b16 v[80:81], v111 offset:36864\n\t"
  "buffer_load_dwordx2 a[242:243], v138, %[srd], %[sUB] offen offset:512 sc1\n\t"
  "v_mfma_f32_16x16x32_bf16 v[32:35], a[204:207], v[88:91], v[32:35]\n\t"
  "ds_read_b64_tr_b16 v[82:83], v111 offset:38912\n\t"
  "buffer_load_dwordx2 a[244:245], v138, %[srd], %[sUB] offen offset:1024 sc1\n\t"
  "v_mfma_f32_16x16x32_bf16 v[36:39], a[208:211], v[88:91], v[36:39]\n\t"
  "s_add_u32 s45, %[sW2], 0\n\t"
  "buffer_load_dwordx4 v[144:147], v1, %[srd], s45 offen offset:0 sc1\n\t"
  "v_mfma_f32_16x16x32_bf16 v[40:43], a[212:215], v[88:91], v[40:43]\n\t"
  "s_waitcnt lgkmcnt(4)\n\t"
  "v_mfma_f32_16x16x32_bf16 v[20:23], a[216:219], v[68:71], v[20:23]\n\t"
  "ds_read_b64_tr_b16 v[84:85], v108 offset:0\n\t"
  "v_mfma_f32_16x16x32_bf16 v[24:27], a[220:223], v[68:71], v[24:27]\n\t"
  "ds_read_b64_tr_b16 v[86:87], v108 offset:2048\n\t"
  "v_mfma_f32_16x16x32_bf16 v[28:31], a[224:227], v[68:71], v[28:31]\n\t"
  "ds_read_b64_tr_b16 v[88:89], v109 offset:0\n\t"
  "v_mfma_f32_16x16x32_bf16 v[32:35], a[216:219], v[72:75], v[32:35]\n\t"
  "ds_read_b64_tr_b16 v[90:91], v109 offset:2048\n\t"
  "buffer_load_dwordx4 v[148:151], v1, %[srd], s45 offen offset:1024 sc1\n\t"
  "v_mfma_f32_16x16x32_bf16 v[36:39], a[220:223], v[72:75], v[36:39]\n\t"
  "v_mfma_f32_16x16x32_bf16 v[40:43], a[224:227], v[72:75], v[40:43]\n\t"
  "s_waitcnt lgkmcnt(4)\n\t"
  "v_mfma_f32_16x16x32_bf16 v[20:23], a[228:231], v[76:79], v[20:23]\n\t"
  "ds_read_b64_tr_b16 v[68:69], v108 offset:4096\n\t"
  "v_mfma_f32_16x16x32_bf16 v[24:27], a[232:235], v[76:79], v[24:27]\n\t"
  "ds_read_b64_tr_b16 v[70:71], v108 offset:6144\n\t"
  "buffer_load_dwordx4 v[152:155], v1, %[srd], s45 offen offset:2048 sc1\n\t"
  "v_mfma_f32_16x16x32_bf16 v[28:31], a[236:239], v[76:79], v[28:31]\n\t"
  "ds_read_b64_tr_b16 v[72:73], v109 offset:4096\n\t"
  "v_mfma_f32_16x16x32_bf16 v[32:35], a[228:231], v[80:83], v[32:35]\n\t"
  "ds_read_b64_tr_b16 v[74:75], v109 offset:6144\n\t"
  "v_mfma_f32_16x16x32_bf16 v[36:39], a[232:235], v[80:83], v[36:39]\n\t"
  "v_mfma_f32_16x16x32_bf16 v[40:43], a[236:239], v[80:83], v[40:43]\n\t"
  "s_waitcnt lgkmcnt(4)\n\t"
  "v_mfma_f32_16x16x32_bf16 v[44:47], a[0:3], v[84:87], 0\n\t"
  "ds_read_b64_tr_b16 v[76:77], v108 offset:8192\n\t"
  "v_xor_b32 v110, 64, v5\n\t"
  "v_mfma_f32_16x16x32_bf16 v[48:51], a[4:7], v[84:87], 0\n\t"
  "ds_read_b64_tr_b16 v[78:79], v108 offset:10240\n\t"
  "v_add_u32 v110, 40960, v110\n\t"
  "v_mfma_f32_16x16x32_bf16 v[52:55], a[8:11], v[84:87], 0\n\t"
  "ds_read_b64_tr_b16 v[80:81], v109 offset:8192\n\t"
  "v_xor_b32 v111, 96, v5\n\t"
  "v_mfma_f32_16x16x32_bf16 v[56:59], a[0:3], v[88:91], 0\n\t"
  "ds_read_b64_tr_b16 v[82:83], v109 offset:10240\n\t"
  "v_add_u32 v111, 40960, v111\n\t"
  "v_mfma_f32_16x16x32_bf16 v[60:63], a[4:7], v[88:91], 0\n\t"
  "buffer_load_dwordx4 v[156:159], v1, %[srd], s45 offen offset:3072 sc1\n\t"
  "s_add_u32 s45, %[sW2], 4096\n\t"
  "v_mfma_f32_16x16x32_bf16 v[64:67], a[8:11], v[88:91], 0\n\t"
  "s_waitcnt lgkmcnt(4)\n\t"
  "v_mfma_f32_16x16x32_bf16 v[44:47], a[12:15], v[68:71], v[44:47]\n\t"
  "ds_read_b64_tr_b16 v[84:85], v108 offset:12288\n\t"
  "buffer_load_dwordx2 v[92:93], v136, %[srd], %[sST] offen offset:0 sc1\n\t"
  "v_mfma_f32_16x16x32_bf16 v[48:51], a[16:19], v[68:71], v[48:51]\n\t"
  "ds_read_b64_tr_b16 v[86:87], v108 offset:14336\n\t"
  "buffer_load_dwordx2 v[94:95], v136, %[srd], %[sST] offen offset:1024 sc1\n\t"
  "v_mfma_f32_16x16x32_bf16 v[52:55], a[20:23], v[68:71], v[52:55]\n\t"
  "ds_read_b64_tr_b16 v[88:89], v109 offset:12288\n\t"
  "buffer_load_dwordx2 v[96:97], v136, %[srd], %[sST] offen offset:2048 sc1\n\t"
  "v_mfma_f32_16x16x32_bf16 v[56:59], a[12:15], v[72:75], v[56:59]\n\t"
  "ds_read_b64_tr_b16 v[90:91], v109 offset:14336\n\t"
  "buffer_load_dwordx2 v[98:99], v136, %[srd], %[sST] offen offset:3072 sc1\n\t"
  "v_mfma_f32_16x16x32_bf16 v[60:63], a[16:19], v[72:75], v[60:63]\n\t"
  "buffer_load_dwordx4 v[160:163], v1, %[srd], s45 offen offset:0 sc1\n\t"
  "v_mfma_f32_16x16x32_bf16 v[64:67], a[20:23], v[72:75], v[64:67]\n\t"
  "s_waitcnt lgkmcnt(4)\n\t"
  "v_mfma_f32_16x16x32_bf16 v[44:47], a[24:27], v[76:79], v[44:47]\n\t"
  "ds_read_b64_tr_b16 v[68:69], v108 offset:16384\n\t"
  "v_mfma_f32_16x16x32_bf16 v[48:51], a[28:31], v[76:79], v[48:51]\n\t"
  "ds_read_b64_tr_b16 v[70:71], v108 offset:18432\n\t"
  "buffer_load_dwordx4 v[164:167], v1, %[srd], s45 offen offset:1024 sc1\n\t"
  "v_mfma_f32_16x16x32_bf16 v[52:55], a[32:35], v[76:79], v[52:55]\n\t"
  "ds_read_b64_tr_b16 v[72:73], v109 offset:16384\n\t"
  "s_waitcnt vmcnt(13)\n\t"
  "s_barrier\n\t"
  "v_mfma_f32_16x16x32_bf16 v[56:59], a[24:27], v[80:83], v[56:59]\n\t"
  "ds_read_b64_tr_b16 v[74:75], v109 offset:18432\n\t"
  "ds_read_u16_d16_hi v112, v125 offset:0\n\t"
  "v_mfma_f32_16x16x32_bf16 v[60:63], a[28:31], v[80:83], v[60:63]\n\t"
  "ds_read_u16_d16_hi v113, v125 offset:8192\n\t"
  "ds_read_b64_tr_b16 a[246:247], v10 offset:0\n\t"
  "v_mfma_f32_16x16x32_bf16 v[64:67], a[32:35], v[80:83], v[64:67]\n\t"
  "s_waitcnt lgkmcnt(7)\n\t"
  "ds_read_u16_d16_hi v114, v125 offset:16384\n\t"
  "v_mfma_f32_16x16x32_bf16 v[44:47], a[36:39], v[84:87], v[44:47]\n\t"
  "ds_read_b64_tr_b16 v[76:77], v108 offset:20480\n\t"
  "buffer_load_dwordx4 v[168:171], v1, %[srd], s45 offen offset:2048 sc1\n\t"
  "v_mfma_f32_16x16x32_bf16 v[48:51], a[40:43], v[84:87], v[48:51]\n\t"
  "ds_read_b64_tr_b16 v[78:79], v108 offset:22528\n\t"
  "ds_read_u16_d16_hi v115, v125 offset:24576\n\t"
  "v_mfma_f32_16x16x32_bf16 v[52:55], a[44:47], v[84:87], v[52:55]\n\t"
  "ds_read_b64_tr_b16 v[80:81], v109 offset:20480\n\t"
  "ds_read_u16_d16_hi v116, v125 offset:32768\n\t"
  "v_mfma_f32_16x16x32_bf16 v[56:59], a[36:39], v[88:91], v[56:59]\n\t"
  "ds_read_b64_tr_b16 v[82:83], v109 offset:22528\n\t"
  "ds_read_u16_d16_hi v117, v124 offset:0\n\t"
  "v_mfma_f32_16x16x32_bf16 v[60:63], a[40:43], v[88:91], v[60:63]\n\t"
  "buffer_load_dwordx4 v[172:175], v1, %[srd], s45 offen offset:3072 sc1\n\t"
  "ds_read_u16_d16_hi v118, v124 offset:8192\n\t"
  "v_mfma_f32_16x16x32_bf16 v[64:67], a[44:47], v[88:91], v[64:67]\n\t"
  "s_waitcnt lgkmcnt(12)\n\t"
  "ds_read_u16_d16_hi v119, v124 offset:16384\n\t"
  "v_mfma_f32_16x16x32_bf16 v[44:47], a[48:51], v[68:71], v[44:47]\n\t"
  "ds_read_b64_tr_b16 v[84:85], v108 offset:24576\n\t"
  "ds_read_u16_d16_hi v120, v124 offset:24576\n\t"
  "v_mfma_f32_16x16x32_bf16 v[48:51], a[52:55], v[68:71], v[48:51]\n\t"
  "ds_read_b64_tr_b16 v[86:87], v108 offset:26624\n\t"
  "ds_read_u16_d16_hi v121, v124 offset:32768\n\t"
  "v_mfma_f32_16x16x32_bf16 v[52:55], a[56:59], v[68:71], v[52:55]\n\t"
  "ds_read_b64_tr_b16 v[88:89], v109 offset:24576\n\t"
  "ds_read_b64_tr_b16 a[248:249], v11 offset:0\n\t"
  "v_mfma_f32_16x16x32_bf16 v[56:59], a[48:51], v[72:75], v[56:59]\n\t"
  "ds_read_b64_tr_b16 v[90:91], v109 offset:26624\n\t"
  "s_add_u32 s45, %[sW2], 8192\n\t"
  "v_mfma_f32_16x16x32_bf16 v[60:63], a[52:55], v[72:75], v[60:63]\n\t"
  "ds_read_b64_tr_b16 a[250:251], v10 offset:4096\n\t"
  "ds_read_b64_tr_b16 a[252:253], v11 offset:4096\n\t"
  "v_mfma_f32_16x16x32_bf16 v[64:67], a[56:59], v[72:75], v[64:67]\n\t"
  "s_waitcnt lgkmcnt(12)\n\t"
  "ds_read_b64_tr_b16 a[254:255], v12 offset:0\n\t"
  "v_mfma_f32_16x16x32_bf16 v[44:47], a[60:63], v[76:79], v[44:47]\n\t"
  "ds_read_b64_tr_b16 v[68:69], v108 offset:28672\n\t"
  "v_mfma_f32_16x16x32_bf16 v[48:51], a[64:67], v[76:79], v[48:51]\n\t"
  "ds_read_b64_tr_b16 v[70:71], v108 offset:30720\n\t"
  "v_mfma_f32_16x16x32_bf16 v[52:55], a[68:71], v[76:79], v[52:55]\n\t"
  "ds_read_b64_tr_b16 v[72:73], v109 offset:28672\n\t"
  "buffer_load_dwordx4 v[176:179], v1, %[srd], s45 offen offset:0 sc1\n\t"
  "v_mfma_f32_16x16x32_bf16 v[56:59], a[60:63], v[80:83], v[56:59]\n\t"
  "ds_read_b64_tr_b16 v[74:75], v109 offset:30720\n\t"
  "v_mfma_f32_16x16x32_bf16 v[60:63], a[64:67], v[80:83], v[60:63]\n\t"
  "v_mfma_f32_16x16x32_bf16 v[64:67], a[68:71], v[80:83], v[64:67]\n\t"
  "s_waitcnt lgkmcnt(7)\n\t"
  "v_mul_f32 v112, v112, v142\n\t"
  "v_mfma_f32_16x16x32_bf16 v[44:47], a[72:75], v[84:87], v[44:47]\n\t"
  "ds_read_b64_tr_b16 v[76:77], v108 offset:32768\n\t"
  "buffer_load_dwordx4 v[180:183], v1, %[srd], s45 offen offset:1024 sc1\n\t"
  "v_mfma_f32_16x16x32_bf16 v[48:51], a[76:79], v[84:87], v[48:51]\n\t"
  "ds_read_b64_tr_b16 v[78:79], v108 offset:34816\n\t"
  "v_mul_f32 v113, v113, v143\n\t"
  "v_mfma_f32_16x16x32_bf16 v[52:55], a[80:83], v[84:87], v[52:55]\n\t"
  "ds_read_b64_tr_b16 v[80:81], v109 offset:32768\n\t"
  "v_mul_f32 v114, v114, v248\n\t"
  "v_mfma_f32_16x16x32_bf16 v[56:59], a[72:75], v[88:91], v[56:59]\n\t"
  "ds_read_b64_tr_b16 v[82:83], v109 offset:34816\n\t"
  "v_mul_f32 v115, v115, v249\n\t"
  "v_mfma_f32_16x16x32_bf16 v[60:63], a[76:79], v[88:91], v[60:63]\n\t"
  "v_mul_f32 v116, v116, v250\n\t"
  "v_mul_f32 v117, v117, v251\n\t"
  "v_mfma_f32_16x16x32_bf16 v[64:67], a[80:83], v[88:91], v[64:67]\n\t"
  "s_waitcnt lgkmcnt(4)\n\t"
  "buffer_load_dwordx4 v[184:187], v1, %[srd], s45 offen offset:2048 sc1\n\t"
  "v_mfma_f32_16x16x32_bf16 v[44:47], a[84:87], v[68:71], v[44:47]\n\t"
  "ds_read_b64_tr_b16 v[84:85], v108 offset:36864\n\t"
  "v_mul_f32 v118, v118, v252\n\t"
  "v_mfma_f32_16x16x32_bf16 v[48:51], a[88:91], v[68:71], v[48:51]\n\t"
  "ds_read_b64_tr_b16 v[86:87], v108 offset:38912\n\t"
  "v_mul_f32 v119, v119, v253\n\t"
  "v_mfma_f32_16x16x32_bf16 v[52:55], a[92:95], v[68:71], v[52:55]\n\t"
  "ds_read_b64_tr_b16 v[88:89], v109 offset:36864\n\t"
  "v_mul_f32 v120, v120, v254\n\t"
  "v_mfma_f32_16x16x32_bf16 v[56:59], a[84:87], v[72:75], v[56:59]\n\t"
  "ds_read_b64_tr_b16 v[90:91], v109 offset:38912\n\t"
  "buffer_load_dwordx4 v[188:191], v1, %[srd], s45 offen offset:3072 sc1\n\t"
  "v_mfma_f32_16x16x32_bf16 v[60:63], a[88:91], v[72:75], v[60:63]\n\t"
  "v_mul_f32 v121, v121, v255\n\t"
  "v_add_f32 v122, v112, v113\n\t"
  "v_mfma_f32_16x16x32_bf16 v[64:67], a[92:95], v[72:75], v[64:67]\n\t"
  "s_waitcnt lgkmcnt(4)\n\t"
  "v_mul_f32 v123, v112, v112\n\t"
  "v_mfma_f32_16x16x32_bf16 v[44:47], a[96:99], v[76:79], v[44:47]\n\t"
  "ds_read_b64_tr_b16 v[68:69], v110 offset:0\n\t"
  "v_fmac_f32 v123, v113, v113\n\t"
  "v_mfma_f32_16x16x32_bf16 v[48:51], a[100:103], v[76:79], v[48:51]\n\t"
  "ds_read_b64_tr_b16 v[70:71], v110 offset:2048\n\t"
  "v_add_f32 v122, v122, v114\n\t"
  "v_mfma_f32_16x16x32_bf16 v[52:55], a[104:107], v[76:79], v[52:55]\n\t"
  "ds_read_b64_tr_b16 v[72:73], v111 offset:0\n\t"
  "s_add_u32 s45, %[sW2], 12288\n\t"
  "v_mfma_f32_16x16x32_bf16 v[56:59], a[96:99], v[80:83], v[56:59]\n\t"
  "ds_read_b64_tr_b16 v[74:75], v111 offset:2048\n\t"
  "v_fmac_f32 v123, v114, v114\n\t"
  "v_mfma_f32_16x16x32_bf16 v[60:63], a[100:103], v[80:83], v[60:63]\n\t"
  "v_add_f32 v122, v122, v115\n\t"
  "v_fmac_f32 v123, v115, v115\n\t"
  "v_mfma_f32_16x16x32_bf16 v[64:67], a[104:107], v[80:83], v[64:67]\n\t"
  "s_waitcnt lgkmcnt(4)\n\t"
  "v_add_f32 v122, v122, v116\n\t"
  "v_mfma_f32_16x16x32_bf16 v[44:47], a[108:111], v[84:87], v[44:47]\n\t"
  "ds_read_b64_tr_b16 v[76:77], v110 offset:4096\n\t"
  "buffer_load_dwordx4 v[192:195], v1, %[srd], s45 offen offset:0 sc1\n\t"
  "v_mfma_f32_16x16x32_bf16 v[48:51], a[112:115], v[84:87], v[48:51]\n\t"
  "ds_read_b64_tr_b16 v[78:79], v110 offset:6144\n\t"
  "v_fmac_f32 v123, v116, v116\n\t"
  "v_mfma_f32_16x16x32_bf16 v[52:55], a[116:119], v[84:87], v[52:55]\n\t"
  "ds_read_b64_tr_b16 v[80:81], v111 offset:4096\n\t"
  "v_add_f32 v122, v122, v117\n\t"
  "v_mfma_f32_16x16x32_bf16 v[56:59], a[108:111], v[88:91], v[56:59]\n\t"
  "ds_read_b64_tr_b16 v[82:83], v111 offset:6144\n\t"
  "v_fmac_f32 v123, v117, v117\n\t"
  "v_mfma_f32_16x16x32_bf16 v[60:63], a[112:115], v[88:91], v[60:63]\n\t"
  "v_add_f32 v122, v122, v118\n\t"
  "v_fmac_f32 v123, v118, v118\n\t"
  "v_mfma_f32_16x16x32_bf16 v[64:67], a[116:119], v[88:91], v[64:67]\n\t"
  "s_waitcnt lgkmcnt(4)\n\t"
  "s_waitcnt vmcnt(11)\n\t"
  "v_min_u32 v131, v93, v95\n\t"
  "v_mfma_f32_16x16x32_bf16 v[44:47], a[120:123], v[68:71], v[44:47]\n\t"
  "ds_read_b64_tr_b16 v[84:85], v110 offset:8192\n\t"
  "s_waitcnt vmcnt(9)\n\t"
  "v_min3_u32 v131, v131, v97, v99\n\t"
  "v_mfma_f32_16x16x32_bf16 v[48:51], a[124:127], v[68:71], v[48:51]\n\t"
  "ds_read_b64_tr_b16 v[86:87], v110 offset:10240\n\t"
  "v_cmp_eq_u32 vcc, 0, v131\n\t"
  "v_mfma_f32_16x16x32_bf16 v[52:55], a[128:131], v[68:71], v[52:55]\n\t"
  "ds_read_b64_tr_b16 v[88:89], v111 offset:8192\n\t"
  "v_add_u32 v108, 81920, v5\n\t"
  "v_mfma_f32_16x16x32_bf16 v[56:59], a[120:123], v[72:75], v[56:59]\n\t"
  "ds_read_b64_tr_b16 v[90:91], v111 offset:10240\n\t"
  "s_cbranch_vccnz Lst0_slow%=\n\t"
  "Lst0_back%=:\n\t"
  "v_mfma_f32_16x16x32_bf16 v[60:63], a[124:127], v[72:75], v[60:63]\n\t"
  "v_xor_b32 v109, 32, v5\n\t"
  "v_add_u32 v109, 81920, v109\n\t"
  "v_mfma_f32_16x16x32_bf16 v[64:67], a[128:131], v[72:75], v[64:67]\n\t"
  "s_waitcnt lgkmcnt(4)\n\t"
  "buffer_load_dwordx4 v[196:199], v1, %[srd], s45 offen offset:1024 sc1\n\t"
  "v_mfma_f32_16x16x32_bf16 v[44:47], a[132:135], v[76:79], v[44:47]\n\t"
  "ds_read_b64_tr_b16 v[68:69], v110 offset:12288\n\t"
  "buffer_load_dwordx4 v[200:203], v1, %[srd], s45 offen offset:2048 sc1\n\t"
  "v_mfma_f32_16x16x32_bf16 v[48:51], a[136:139], v[76:79], v[48:51]\n\t"
  "ds_read_b64_tr_b16 v[70:71], v110 offset:14336\n\t"
  "v_add_f32 v122, v122, v119\n\t"
  "v_mfma_f32_16x16x32_bf16 v[52:55], a[140:143], v[76:79], v[52:55]\n\t"
  "ds_read_b64_tr_b16 v[72:73], v111 offset:12288\n\t"
  "buffer_load_dwordx4 v[204:207], v1, %[srd], s45 offen offset:3072 sc1\n\t"
  "v_mfma_f32_16x16x32_bf16 v[56:59], a[132:135], v[80:83], v[56:59]\n\t"
  "ds_read_b64_tr_b16 v[74:75], v111 offset:14336\n\t"
  "v_fmac_f32 v123, v119, v119\n\t"
  "v_mfma_f32_16x16x32_bf16 v[60:63], a[136:139], v[80:83], v[60:63]\n\t"
  "v_add_f32 v122, v122, v120\n\t"
  "v_fmac_f32 v123, v120, v120\n\t"
  "v_mfma_f32_16x16x32_bf16 v[64:67], a[140:143], v[80:83], v[64:67]\n\t"
  "v_add_f32 v122, v122, v121\n\t"
  "v_fmac_f32 v123, v121, v121\n\t"
  "v_mfma_f32_16x16x16_bf16 v[20:23], a[240:241], v[92:93], v[20:23]\n\t"
  "s_add_u32 s45, %[sW2], 16384\n\t"
  "v_add_f32_dpp v122, v122, v122 quad_perm:[1,0,3,2] row_mask:0xf bank_mask:0xf\n\t"
  "v_mfma_f32_16x16x16_bf16 v[24:27], a[242:243], v[92:93], v[24:27]\n\t"
  "v_add_f32_dpp v123, v123, v123 quad_perm:[1,0,3,2] row_mask:0xf bank_mask:0xf\n\t"
  "v_add_f32_dpp v122, v122, v122 quad_perm:[2,3,0,1] row_mask:0xf bank_mask:0xf\n\t"
  "v_mfma_f32_16x16x16_bf16 v[28:31], a[244:245], v[92:93], v[28:31]\n\t"
  "v_add_f32_dpp v123, v123, v123 quad_perm:[2,3,0,1] row_mask:0xf bank_mask:0xf\n\t"
  "v_add_f32_dpp v122, v122, v122 row_half_mirror row_mask:0xf bank_mask:0xf\n\t"
  "v_mfma_f32_16x16x16_bf16 v[32:35], a[240:241], v[94:95], v[32:35]\n\t"
  "v_add_f32_dpp v123, v123, v123 row_half_mirror row_mask:0xf bank_mask:0xf\n\t"
  "v_add_f32_dpp v122, v122, v122 row_mirror row_mask:0xf bank_mask:0xf\n\t"
  "v_mfma_f32_16x16x16_bf16 v[36:39], a[242:243], v[94:95], v[36:39]\n\t"
  "v_add_f32_dpp v123, v123, v123 row_mirror row_mask:0xf bank_mask:0xf\n\t"
  "v_add_f32_dpp v122, v122, v122 row_bcast:15 row_mask:0xa bank_mask:0xf\n\t"
  "v_mfma_f32_16x16x16_bf16 v[40:43], a[244:245], v[94:95], v[40:43]\n\t"
  "s_waitcnt lgkmcnt(4)\n\t"
  "buffer_load_dwordx4 v[208:211], v1, %[srd], s45 offen offset:0 sc1\n\t"
  "v_mfma_f32_16x16x32_bf16 v[44:47], a[144:147], v[84:87], v[44:47]\n\t"
  "ds_read_b64_tr_b16 v[76:77], v110 offset:16384\n\t"
  "v_add_f32_dpp v123, v123, v123 row_bcast:15 row_mask:0xa bank_mask:0xf\n\t"
  "v_mfma_f32_16x16x32_bf16 v[48:51], a[148:151], v[84:87], v[48:51]\n\t"
  "ds_read_b64_tr_b16 v[78:79], v110 offset:18432\n\t"
  "v_add_f32_dpp v122, v122, v122 row_bcast:31 row_mask:0xc bank_mask:0xf\n\t"
  "v_mfma_f32_16x16x32_bf16 v[52:55], a[152:155], v[84:87], v[52:55]\n\t"
  "ds_read_b64_tr_b16 v[80:81], v111 offset:16384\n\t"
  "v_add_f32_dpp v123, v123, v123 row_bcast:31 row_mask:0xc bank_mask:0xf\n\t"
  "v_mfma_f32_16x16x32_bf16 v[56:59], a[144:147], v[88:91], v[56:59]\n\t"
  "ds_read_b64_tr_b16 v[82:83], v111 offset:18432\n\t"
  "v_mul_f32 v122, 0xbacccccd, v122\n\t"
  "v_mfma_f32_16x16x32_bf16 v[60:63], a[148:151], v[88:91], v[60:63]\n\t"
  "buffer_load_dwordx4 v[212:215], v1, %[srd], s45 offen offset:1024 sc1\n\t"
  "v_mul_f32 v123, 0x3acccccd, v123\n\t"
  "v_mfma_f32_16x16x32_bf16 v[64:67], a[152:155], v[88:91], v[64:67]\n\t"
  "s_waitcnt lgkmcnt(4)\n\t"
  "v_fma_f32 v123, -v122, v122, v123\n\t"
  "v_max_f32 v123, 0, v123\n\t"
  "v_cvt_pk_bf16_f32 v123, 0, v123\n\t"
  "v_cvt_pk_bf16_f32 v126, v122, v122\n\t"
  "v_add_f32 v123, 0x3727c5ac, v123\n\t"
  "v_cvt_pk_bf16_f32 v123, 0, v123\n\t"
  "v_sqrt_f32 v123, v123\n\t"
  "s_nop 0\n\t"
  "v_cvt_pk_bf16_f32 v127, v123, v123\n\t"
  "buffer_store_dwordx2 v[126:127], v7, %[srd], s54 offen sc1\n\t"
  "v_mfma_f32_16x16x32_bf16 v[44:47], a[156:159], v[68:71], v[44:47]\n\t"
  "ds_read_b64_tr_b16 v[84:85], v110 offset:20480\n\t"
  "v_and_b32 v128, 0xffff0000, v93\n\t"
  "v_mfma_f32_16x16x32_bf16 v[48:51], a[160:163], v[68:71], v[48:51]\n\t"
  "ds_read_b64_tr_b16 v[86:87], v110 offset:22528\n\t"
  "v_and_b32 v129, 0xffff0000, v95\n\t"
  "v_mfma_f32_16x16x32_bf16 v[52:55], a[164:167], v[68:71], v[52:55]\n\t"
  "ds_read_b64_tr_b16 v[88:89], v111 offset:20480\n\t"
  "buffer_load_dwordx4 v[216:219], v1, %[srd], s45 offen offset:2048 sc1\n\t"
  "v_mfma_f32_16x16x32_bf16 v[56:59], a[156:159], v[72:75], v[56:59]\n\t"
  "ds_read_b64_tr_b16 v[90:91], v111 offset:22528\n\t"
  "v_rcp_f32 v128, v128\n\t"
  "v_mfma_f32_16x16x32_bf16 v[60:63], a[160:163], v[72:75], v[60:63]\n\t"
  "v_rcp_f32 v129, v129\n\t"
  "v_mul_f32 v128, v128, v128\n\t"
  "v_mfma_f32_16x16x32_bf16 v[64:67], a[164:167], v[72:75], v[64:67]\n\t"
  "s_waitcnt lgkmcnt(4)\n\t"
  "v_mul_f32 v129, v129, v129\n\t"
  "v_mfma_f32_16x16x32_bf16 v[44:47], a[168:171], v[76:79], v[44:47]\n\t"
  "ds_read_b64_tr_b16 v[68:69], v110 offset:24576\n\t"
  "v_max_f32 v20, 0, v20\n\t"
  "v_mfma_f32_16x16x32_bf16 v[48:51], a[172:175], v[76:79], v[48:51]\n\t"
  "ds_read_b64_tr_b16 v[70:71], v110 offset:26624\n\t"
  "buffer_load_dwordx4 v[220:223], v1, %[srd], s45 offen offset:3072 sc1\n\t"
  "v_mfma_f32_16x16x32_bf16 v[52:55], a[176:179], v[76:79], v[52:55]\n\t"
  "ds_read_b64_tr_b16 v[72:73], v111 offset:24576\n\t"
  "v_max_f32 v21, 0, v21\n\t"
  "v_mfma_f32_16x16x32_bf16 v[56:59], a[168:171], v[80:83], v[56:59]\n\t"
  "ds_read_b64_tr_b16 v[74:75], v111 offset:26624\n\t"
  "v_mul_f32 v22, v22, v128\n\t"
  "v_mfma_f32_16x16x32_bf16 v[60:63], a[172:175], v[80:83], v[60:63]\n\t"
  "v_mul_f32 v23, v23, v128\n\t"
  "v_mul_f32 v20, v20, v22\n\t"
  "v_mfma_f32_16x16x32_bf16 v[64:67], a[176:179], v[80:83], v[64:67]\n\t"
  "s_waitcnt lgkmcnt(4)\n\t"
  "s_add_u32 s45, %[sW2], 20480\n\t"
  "v_mfma_f32_16x16x32_bf16 v[44:47], a[180:183], v[84:87], v[44:47]\n\t"
  "ds_read_b64_tr_b16 v[76:77], v110 offset:28672\n\t"
  "v_mul_f32 v21, v21, v23\n\t"
  "v_mfma_f32_16x16x32_bf16 v[48:51], a[184:187], v[84:87], v[48:51]\n\t"
  "ds_read_b64_tr_b16 v[78:79], v110 offset:30720\n\t"
  "v_cvt_pk_bf16_f32 v116, v20, v21\n\t"
  "v_mfma_f32_16x16x32_bf16 v[52:55], a[188:191], v[84:87], v[52:55]\n\t"
  "ds_read_b64_tr_b16 v[80:81], v111 offset:28672\n\t"
  "v_cvt_pk_bf16_f32 v112, v20, v21\n\t"
  "v_mfma_f32_16x16x32_bf16 v[56:59], a[180:183], v[88:91], v[56:59]\n\t"
  "ds_read_b64_tr_b16 v[82:83], v111 offset:30720\n\t"
  "v_max_f32 v24, 0, v24\n\t"
  "v_mfma_f32_16x16x32_bf16 v[60:63], a[184:187], v[88:91], v[60:63]\n\t"
  "buffer_load_dwordx4 v[224:227], v1, %[srd], s45 offen offset:0 sc1\n\t"
  "v_max_f32 v25, 0, v25\n\t"
  "v_mfma_f32_16x16x32_bf16 v[64:67], a[188:191], v[88:91], v[64:67]\n\t"
  "s_waitcnt lgkmcnt(4)\n\t"
  "v_mul_f32 v26, v26, v128\n\t"
  "v_mfma_f32_16x16x32_bf16 v[44:47], a[192:195], v[68:71], v[44:47]\n\t"
  "ds_read_b64_tr_b16 v[84:85], v110 offset:32768\n\t"
  "v_mul_f32 v27, v27, v128\n\t"
  "v_mfma_f32_16x16x32_bf16 v[48:51], a[196:199], v[68:71], v[48:51]\n\t"
  "ds_read_b64_tr_b16 v[86:87], v110 offset:34816\n\t"
  "v_mul_f32 v24, v24, v26\n\t"
  "v_mfma_f32_16x16x32_bf16 v[52:55], a[200:203], v[68:71], v[52:55]\n\t"
  "ds_read_b64_tr_b16 v[88:89], v111 offset:32768\n\t"
  "buffer_load_dwordx4 v[228:231], v1, %[srd], s45 offen offset:1024 sc1\n\t"
  "v_mfma_f32_16x16x32_bf16 v[56:59], a[192:195], v[72:75], v[56:59]\n\t"
  "ds_read_b64_tr_b16 v[90:91], v111 offset:34816\n\t"
  "v_mul_f32 v25, v25, v27\n\t"
  "v_mfma_f32_16x16x32_bf16 v[60:63], a[196:199], v[72:75], v[60:63]\n\t"
  "v_cvt_pk_bf16_f32 v117, v24, v25\n\t"
  "v_cvt_pk_bf16_f32 v113, v24, v25\n\t"
  "v_mfma_f32_16x16x32_bf16 v[64:67], a[200:203], v[72:75], v[64:67]\n\t"
  "s_waitcnt lgkmcnt(4)\n\t"
  "v_max_f32 v28, 0, v28\n\t"
  "v_mfma_f32_16x16x32_bf16 v[44:47], a[204:207], v[76:79], v[44:47]\n\t"
  "ds_read_b64_tr_b16 v[68:69], v110 offset:36864\n\t"
  "v_max_f32 v29, 0, v29\n\t"
  "v_mfma_f32_16x16x32_bf16 v[48:51], a[208:211], v[76:79], v[48:51]\n\t"
  "ds_read_b64_tr_b16 v[70:71], v110 offset:38912\n\t"
  "buffer_load_dwordx4 v[232:235], v1, %[srd], s45 offen offset:2048 sc1\n\t"
  "v_mfma_f32_16x16x32_bf16 v[52:55], a[212:215], v[76:79], v[52:55]\n\t"
  "ds_read_b64_tr_b16 v[72:73], v111 offset:36864\n\t"
  "v_mul_f32 v30, v30, v128\n\t"
  "v_mfma_f32_16x16x32_bf16 v[56:59], a[204:207], v[80:83], v[56:59]\n\t"
  "ds_read_b64_tr_b16 v[74:75], v111 offset:38912\n\t"
  "v_mul_f32 v31, v31, v128\n\t"
  "v_mfma_f32_16x16x32_bf16 v[60:63], a[208:211], v[80:83], v[60:63]\n\t"
  "v_mul_f32 v28, v28, v30\n\t"
  "v_permlane16_swap_b32 v116, v117\n\t"
  "v_mfma_f32_16x16x32_bf16 v[64:67], a[212:215], v[80:83], v[64:67]\n\t"
  "s_waitcnt lgkmcnt(4)\n\t"
  "buffer_load_dwordx4 v[236:239], v1, %[srd], s45 offen offset:3072 sc1\n\t"
  "v_mfma_f32_16x16x32_bf16 v[44:47], a[216:219], v[84:87], v[44:47]\n\t"
  "ds_read_b64_tr_b16 v[76:77], v108 offset:0\n\t"
  "v_mul_f32 v29, v29, v31\n\t"
  "v_mfma_f32_16x16x32_bf16 v[48:51], a[220:223], v[84:87], v[48:51]\n\t"
  "ds_read_b64_tr_b16 v[78:79], v108 offset:2048\n\t"
  "v_cvt_pk_bf16_f32 v118, v28, v29\n\t"
  "v_mfma_f32_16x16x32_bf16 v[52:55], a[224:227], v[84:87], v[52:55]\n\t"
  "ds_read_b64_tr_b16 v[80:81], v109 offset:0\n\t"
  "v_cvt_pk_bf16_f32 v114, v28, v29\n\t"
  "v_mfma_f32_16x16x32_bf16 v[56:59], a[216:219], v[88:91], v[56:59]\n\t"
  "ds_read_b64_tr_b16 v[82:83], v109 offset:2048\n\t"
  "s_mov_b32 s55, 0\n\t"
  "v_mfma_f32_16x16x32_bf16 v[60:63], a[220:223], v[88:91], v[60:63]\n\t"
  "s_add_u32 s45, %[sW2], 24576\n\t"
  "v_permlane16_swap_b32 v118, v115\n\t"
  "v_mfma_f32_16x16x32_bf16 v[64:67], a[224:227], v[88:91], v[64:67]\n\t"
  "s_waitcnt lgkmcnt(4)\n\t"
  "v_permlane32_swap_b32 v117, v115\n\t"
  "v_mfma_f32_16x16x32_bf16 v[44:47], a[228:231], v[68:71], v[44:47]\n\t"
  "ds_read_b64_tr_b16 v[84:85], v108 offset:4096\n\t"
  "buffer_store_dwordx4 v[112:115], v8, %[srd], s55 offen sc1\n\t"
  "v_mfma_f32_16x16x32_bf16 v[48:51], a[232:235], v[68:71], v[48:51]\n\t"
  "ds_read_b64_tr_b16 v[86:87], v108 offset:6144\n\t"
  "v_max_f32 v32, 0, v32\n\t"
  "v_mfma_f32_16x16x32_bf16 v[52:55], a[236:239], v[68:71], v[52:55]\n\t"
  "ds_read_b64_tr_b16 v[88:89], v109 offset:4096\n\t"
  "buffer_load_dwordx4 v[240:243], v1, %[srd], s45 offen offset:0 sc1\n\t"
  "v_mfma_f32_16x16x32_bf16 v[56:59], a[228:231], v[72:75], v[56:59]\n\t"
  "ds_read_b64_tr_b16 v[90:91], v109 offset:6144\n\t"
  "v_max_f32 v33, 0, v33\n\t"
  "v_mfma_f32_16x16x32_bf16 v[60:63], a[232:235], v[72:75], v[60:63]\n\t"
  "v_mul_f32 v34, v34, v129\n\t"
  "v_mul_f32 v35, v35, v129\n\t"
  "v_mfma_f32_16x16x32_bf16 v[64:67], a[236:239], v[72:75], v[64:67]\n\t"
  "v_mul_f32 v32, v32, v34\n\t"
  "v_mul_f32 v33, v33, v35\n\t"
  "v_mfma_f32_16x16x16_bf16 v[44:47], a[240:241], v[96:97], v[44:47]\n\t"
  "v_cvt_pk_bf16_f32 v124, v32, v33\n\t"
  "v_cvt_pk_bf16_f32 v120, v32, v33\n\t"
  "v_mfma_f32_16x16x16_bf16 v[48:51], a[242:243], v[96:97], v[48:51]\n\t"
  "buffer_load_dwordx4 v[244:247], v1, %[srd], s45 offen offset:1024 sc1\n\t"
  "v_max_f32 v36, 0, v36\n\t"
  "v_mfma_f32_16x16x16_bf16 v[52:55], a[244:245], v[96:97], v[52:55]\n\t"
  "v_max_f32 v37, 0, v37\n\t"
  "v_mul_f32 v38, v38, v129\n\t"
  "v_mfma_f32_16x16x16_bf16 v[56:59], a[240:241], v[98:99], v[56:59]\n\t"
  "v_mul_f32 v39, v39, v129\n\t"
  "v_mul_f32 v36, v36, v38\n\t"
  "v_mfma_f32_16x16x16_bf16 v[60:63], a[242:243], v[98:99], v[60:63]\n\t"
  "v_mul_f32 v37, v37, v39\n\t"
  "v_cvt_pk_bf16_f32 v125, v36, v37\n\t"
  "v_cvt_pk_bf16_f32 v121, v36, v37\n\t"
  "v_max_f32 v40, 0, v40\n\t"
  "v_max_f32 v41, 0, v41\n\t"
  "v_mul_f32 v42, v42, v129\n\t"
  "v_mul_f32 v43, v43, v129\n\t"
  "v_mul_f32 v40, v40, v42\n\t"
  "v_permlane16_swap_b32 v124, v125\n\t"
  "v_mul_f32 v41, v41, v43\n\t"
  "v_cvt_pk_bf16_f32 v126, v40, v41\n\t"
  "v_cvt_pk_bf16_f32 v122, v40, v41\n\t"
  "s_mov_b32 s55, 49152\n\t"
  "v_permlane16_swap_b32 v126, v123\n\t"
  "s_nop 1\n\t"
  "v_permlane32_swap_b32 v125, v123\n\t"
  "buffer_store_dwordx4 v[120:123], v8, %[srd], s55 offen sc1\n\t"
  "v_mfma_f32_16x16x16_bf16 v[64:67], a[244:245], v[98:99], v[64:67]\n\t"
  "s_waitcnt lgkmcnt(4)\n\t"
  "v_mfma_f32_16x16x32_bf16 v[20:23], a[0:3], v[76:79], 0\n\t"
  "ds_read_b64_tr_b16 v[68:69], v108 offset:8192\n\t"
  "v_add_u32 v110, 122880, v5\n\t"
  "v_mfma_f32_16x16x32_bf16 v[24:27], a[4:7], v[76:79], 0\n\t"
  "ds_read_b64_tr_b16 v[70:71], v108 offset:10240\n\t"
  "v_xor_b32 v111, 32, v5\n\t"
  "v_mfma_f32_16x16x32_bf16 v[28:31], a[8:11], v[76:79], 0\n\t"
  "ds_read_b64_tr_b16 v[72:73], v109 offset:8192\n\t"
  "v_add_u32 v111, 122880, v111\n\t"
  "v_mfma_f32_16x16x32_bf16 v[32:35], a[0:3], v[80:83], 0\n\t"
  "ds_read_b64_tr_b16 v[74:75], v109 offset:10240\n\t"
  "v_and_b32 v128, 0xffff0000, v97\n\t"
  "v_mfma_f32_16x16x32_bf16 v[36:39], a[4:7], v[80:83], 0\n\t"
  "v_and_b32 v129, 0xffff0000, v99\n\t"
  "v_rcp_f32 v128, v128\n\t"
  "v_mfma_f32_16x16x32_bf16 v[40:43], a[8:11], v[80:83], 0\n\t"
  "s_waitcnt lgkmcnt(4)\n\t"
  "v_rcp_f32 v129, v129\n\t"
  "v_mfma_f32_16x16x32_bf16 v[20:23], a[12:15], v[84:87], v[20:23]\n\t"
  "ds_read_b64_tr_b16 v[76:77], v108 offset:12288\n\t"
  "s_barrier\n\t"
  "v_mfma_f32_16x16x32_bf16 v[24:27], a[16:19], v[84:87], v[24:27]\n\t"
  "ds_read_b64_tr_b16 v[78:79], v108 offset:14336\n\t"
  "v_mul_f32 v128, v128, v128\n\t"
  "v_mfma_f32_16x16x32_bf16 v[28:31], a[20:23], v[84:87], v[28:31]\n\t"
  "ds_read_b64_tr_b16 v[80:81], v109 offset:12288\n\t"
  "v_mul_f32 v129, v129, v129\n\t"
  "v_mfma_f32_16x16x32_bf16 v[32:35], a[12:15], v[88:91], v[32:35]\n\t"
  "ds_read_b64_tr_b16 v[82:83], v109 offset:14336\n\t"
  "s_mul_i32 s52, %[w], 20480\n\t"
  "v_mfma_f32_16x16x32_bf16 v[36:39], a[16:19], v[88:91], v[36:39]\n\t"
  "v_max_f32 v44, 0, v44\n\t"
  "v_max_f32 v45, 0, v45\n\t"
  "v_mfma_f32_16x16x32_bf16 v[40:43], a[20:23], v[88:91], v[40:43]\n\t"
  "s_waitcnt lgkmcnt(4)\n\t"
  "v_mul_f32 v46, v46, v128\n\t"
  "v_mfma_f32_16x16x32_bf16 v[20:23], a[24:27], v[68:71], v[20:23]\n\t"
  "ds_read_b64_tr_b16 v[84:85], v108 offset:16384\n\t"
  "s_add_u32 m0, s52, 0\n\t"
  "v_mfma_f32_16x16x32_bf16 v[24:27], a[28:31], v[68:71], v[24:27]\n\t"
  "ds_read_b64_tr_b16 v[86:87], v108 offset:18432\n\t"
  "v_mul_f32 v47, v47, v128\n\t"
  "v_mfma_f32_16x16x32_bf16 v[28:31], a[32:35], v[68:71], v[28:31]\n\t"
  "ds_read_b64_tr_b16 v[88:89], v109 offset:16384\n\t"
  "v_mul_f32 v44, v44, v46\n\t"
  "v_mfma_f32_16x16x32_bf16 v[32:35], a[24:27], v[72:75], v[32:35]\n\t"
  "ds_read_b64_tr_b16 v[90:91], v109 offset:18432\n\t"
  "v_mul_f32 v45, v45, v47\n\t"
  "v_mfma_f32_16x16x32_bf16 v[36:39], a[28:31], v[72:75], v[36:39]\n\t"
  "s_add_u32 s46, %[sW2], 26624\n\t"
  "v_cvt_pk_bf16_f32 v116, v44, v45\n\t"
  "v_mfma_f32_16x16x32_bf16 v[40:43], a[32:35], v[72:75], v[40:43]\n\t"
  "s_waitcnt lgkmcnt(4)\n\t"
  "v_cvt_pk_bf16_f32 v112, v44, v45\n\t"
  "v_mfma_f32_16x16x32_bf16 v[20:23], a[36:39], v[76:79], v[20:23]\n\t"
  "ds_read_b64_tr_b16 v[68:69], v108 offset:20480\n\t"
  "v_max_f32 v48, 0, v48\n\t"
  "v_mfma_f32_16x16x32_bf16 v[24:27], a[40:43], v[76:79], v[24:27]\n\t"
  "ds_read_b64_tr_b16 v[70:71], v108 offset:22528\n\t"
  "buffer_load_dwordx4 v1, %[srd], s46 offen offset:0 sc1 lds\n\t"
  "v_mfma_f32_16x16x32_bf16 v[28:31], a[44:47], v[76:79], v[28:31]\n\t"
  "ds_read_b64_tr_b16 v[72:73], v109 offset:20480\n\t"
  "v_max_f32 v49, 0, v49\n\t"
  "v_mfma_f32_16x16x32_bf16 v[32:35], a[36:39], v[80:83], v[32:35]\n\t"
  "ds_read_b64_tr_b16 v[74:75], v109 offset:22528\n\t"
  "v_mul_f32 v50, v50, v128\n\t"
  "v_mfma_f32_16x16x32_bf16 v[36:39], a[40:43], v[80:83], v[36:39]\n\t"
  "buffer_load_dwordx4 v1, %[srd], s46 offen offset:1024 sc1 lds\n\t"
  "v_mul_f32 v51, v51, v128\n\t"
  "v_mfma_f32_16x16x32_bf16 v[40:43], a[44:47], v[80:83], v[40:43]\n\t"
  "s_waitcnt lgkmcnt(4)\n\t"
  "v_mul_f32 v48, v48, v50\n\t"
  "v_mfma_f32_16x16x32_bf16 v[20:23], a[48:51], v[84:87], v[20:23]\n\t"
  "ds_read_b64_tr_b16 v[76:77], v108 offset:24576\n\t"
  "v_mul_f32 v49, v49, v51\n\t"
  "v_mfma_f32_16x16x32_bf16 v[24:27], a[52:55], v[84:87], v[24:27]\n\t"
  "ds_read_b64_tr_b16 v[78:79], v108 offset:26624\n\t"
  "v_cvt_pk_bf16_f32 v117, v48, v49\n\t"
  "v_mfma_f32_16x16x32_bf16 v[28:31], a[56:59], v[84:87], v[28:31]\n\t"
  "ds_read_b64_tr_b16 v[80:81], v109 offset:24576\n\t"
  "buffer_load_dwordx4 v1, %[srd], s46 offen offset:2048 sc1 lds\n\t"
  "v_mfma_f32_16x16x32_bf16 v[32:35], a[48:51], v[88:91], v[32:35]\n\t"
  "ds_read_b64_tr_b16 v[82:83], v109 offset:26624\n\t"
  "v_cvt_pk_bf16_f32 v113, v48, v49\n\t"
  "v_mfma_f32_16x16x32_bf16 v[36:39], a[52:55], v[88:91], v[36:39]\n\t"
  "v_max_f32 v52, 0, v52\n\t"
  "v_max_f32 v53, 0, v53\n\t"
  "v_mfma_f32_16x16x32_bf16 v[40:43], a[56:59], v[88:91], v[40:43]\n\t"
  "s_waitcnt lgkmcnt(4)\n\t"
  "buffer_load_dwordx4 v1, %[srd], s46 offen offset:3072 sc1 lds\n\t"
  "v_mfma_f32_16x16x32_bf16 v[20:23], a[60:63], v[68:71], v[20:23]\n\t"
  "ds_read_b64_tr_b16 v[84:85], v108 offset:28672\n\t"
  "v_mul_f32 v54, v54, v128\n\t"
  "v_mfma_f32_16x16x32_bf16 v[24:27], a[64:67], v[68:71], v[24:27]\n\t"
  "ds_read_b64_tr_b16 v[86:87], v108 offset:30720\n\t"
  "v_mul_f32 v55, v55, v128\n\t"
  "v_mfma_f32_16x16x32_bf16 v[28:31], a[68:71], v[68:71], v[28:31]\n\t"
  "ds_read_b64_tr_b16 v[88:89], v109 offset:28672\n\t"
  "v_mul_f32 v52, v52, v54\n\t"
  "v_mfma_f32_16x16x32_bf16 v[32:35], a[60:63], v[72:75], v[32:35]\n\t"
  "ds_read_b64_tr_b16 v[90:91], v109 offset:30720\n\t"
  "s_add_u32 m0, s52, 4096\n\t"
  "v_mfma_f32_16x16x32_bf16 v[36:39], a[64:67], v[72:75], v[36:39]\n\t"
  "v_permlane16_swap_b32 v116, v117\n\t"
  "v_mul_f32 v53, v53, v55\n\t"
  "v_mfma_f32_16x16x32_bf16 v[40:43], a[68:71], v[72:75], v[40:43]\n\t"
  "s_waitcnt lgkmcnt(4)\n\t"
  "v_cvt_pk_bf16_f32 v118, v52, v53\n\t"
  "v_mfma_f32_16x16x32_bf16 v[20:23], a[72:75], v[76:79], v[20:23]\n\t"
  "ds_read_b64_tr_b16 v[68:69], v108 offset:32768\n\t"
  "s_add_u32 s46, %[sW2], 30720\n\t"
  "v_mfma_f32_16x16x32_bf16 v[24:27], a[76:79], v[76:79], v[24:27]\n\t"
  "ds_read_b64_tr_b16 v[70:71], v108 offset:34816\n\t"
  "v_cvt_pk_bf16_f32 v114, v52, v53\n\t"
  "v_mfma_f32_16x16x32_bf16 v[28:31], a[80:83], v[76:79], v[28:31]\n\t"
  "ds_read_b64_tr_b16 v[72:73], v109 offset:32768\n\t"
  "s_mov_b32 s55, 98304\n\t"
  "v_mfma_f32_16x16x32_bf16 v[32:35], a[72:75], v[80:83], v[32:35]\n\t"
  "ds_read_b64_tr_b16 v[74:75], v109 offset:34816\n\t"
  "buffer_load_dwordx4 v1, %[srd], s46 offen offset:0 sc1 lds\n\t"
  "v_mfma_f32_16x16x32_bf16 v[36:39], a[76:79], v[80:83], v[36:39]\n\t"
  "v_permlane16_swap_b32 v118, v115\n\t"
  "v_mfma_f32_16x16x32_bf16 v[40:43], a[80:83], v[80:83], v[40:43]\n\t"
  "s_waitcnt lgkmcnt(4)\n\t"
  "v_permlane32_swap_b32 v117, v115\n\t"
  "v_mfma_f32_16x16x32_bf16 v[20:23], a[84:87], v[84:87], v[20:23]\n\t"
  "ds_read_b64_tr_b16 v[76:77], v108 offset:36864\n\t"
  "buffer_store_dwordx4 v[112:115], v8, %[srd], s55 offen sc1\n\t"
  "v_mfma_f32_16x16x32_bf16 v[24:27], a[88:91], v[84:87], v[24:27]\n\t"
  "ds_read_b64_tr_b16 v[78:79], v108 offset:38912\n\t"
  "buffer_load_dwordx4 v1, %[srd], s46 offen offset:1024 sc1 lds\n\t"
  "v_mfma_f32_16x16x32_bf16 v[28:31], a[92:95], v[84:87], v[28:31]\n\t"
  "ds_read_b64_tr_b16 v[80:81], v109 offset:36864\n\t"
  "v_max_f32 v56, 0, v56\n\t"
  "v_mfma_f32_16x16x32_bf16 v[32:35], a[84:87], v[88:91], v[32:35]\n\t"
  "ds_read_b64_tr_b16 v[82:83], v109 offset:38912\n\t"
  "v_max_f32 v57, 0, v57\n\t"
  "v_mfma_f32_16x16x32_bf16 v[36:39], a[88:91], v[88:91], v[36:39]\n\t"
  "buffer_load_dwordx4 v1, %[srd], s46 offen offset:2048 sc1 lds\n\t"
  "v_mul_f32 v58, v58, v129\n\t"
  "v_mfma_f32_16x16x32_bf16 v[40:43], a[92:95], v[88:91], v[40:43]\n\t"
  "s_waitcnt lgkmcnt(4)\n\t"
  "v_mul_f32 v59, v59, v129\n\t"
  "v_mfma_f32_16x16x32_bf16 v[20:23], a[96:99], v[68:71], v[20:23]\n\t"
  "ds_read_b64_tr_b16 v[84:85], v110 offset:0\n\t"
  "v_mul_f32 v56, v56, v58\n\t"
  "v_mfma_f32_16x16x32_bf16 v[24:27], a[100:103], v[68:71], v[24:27]\n\t"
  "ds_read_b64_tr_b16 v[86:87], v110 offset:2048\n\t"
  "buffer_load_dwordx4 v1, %[srd], s46 offen offset:3072 sc1 lds\n\t"
  "v_mfma_f32_16x16x32_bf16 v[28:31], a[104:107], v[68:71], v[28:31]\n\t"
  "ds_read_b64_tr_b16 v[88:89], v111 offset:0\n\t"
  "v_mul_f32 v57, v57, v59\n\t"
  "v_mfma_f32_16x16x32_bf16 v[32:35], a[96:99], v[72:75], v[32:35]\n\t"
  "ds_read_b64_tr_b16 v[90:91], v111 offset:2048\n\t"
  "v_cvt_pk_bf16_f32 v124, v56, v57\n\t"
  "v_mfma_f32_16x16x32_bf16 v[36:39], a[100:103], v[72:75], v[36:39]\n\t"
  "buffer_load_dwordx2 v[100:101], v136, %[srd], s54 offen offset:0 sc1\n\t"
  "buffer_load_dwordx2 v[102:103], v136, %[srd], s54 offen offset:1024 sc1\n\t"
  "v_mfma_f32_16x16x32_bf16 v[40:43], a[104:107], v[72:75], v[40:43]\n\t"
  "s_waitcnt lgkmcnt(4)\n\t"
  "buffer_load_dwordx2 v[104:105], v136, %[srd], s54 offen offset:2048 sc1\n\t"
  "v_mfma_f32_16x16x32_bf16 v[20:23], a[108:111], v[76:79], v[20:23]\n\t"
  "ds_read_b64_tr_b16 v[68:69], v110 offset:4096\n\t"
  "s_movk_i32 s56, 0\n\t"
  "v_mfma_f32_16x16x32_bf16 v[24:27], a[112:115], v[76:79], v[24:27]\n\t"
  "ds_read_b64_tr_b16 v[70:71], v110 offset:6144\n\t"
  "s_waitcnt vmcnt(12)\n\t"
  "v_mfma_f32_16x16x32_bf16 v[28:31], a[116:119], v[76:79], v[28:31]\n\t"
  "ds_read_b64_tr_b16 v[72:73], v111 offset:4096\n\t"
  "buffer_load_dwordx2 v[106:107], v136, %[srd], s54 offen offset:3072 sc1\n\t"
  "v_mfma_f32_16x16x32_bf16 v[32:35], a[108:111], v[80:83], v[32:35]\n\t"
  "ds_read_b64_tr_b16 v[74:75], v111 offset:6144\n\t"
  "buffer_store_dwordx4 v[132:135], v9, %[srd], s56 offen sc1\n\t"
  "v_mfma_f32_16x16x32_bf16 v[36:39], a[112:115], v[80:83], v[36:39]\n\t"
  "s_add_u32 m0, s52, 8192\n\t"
  "s_add_u32 s46, %[sW2], 34816\n\t"
  "v_mfma_f32_16x16x32_bf16 v[40:43], a[116:119], v[80:83], v[40:43]\n\t"
  "s_waitcnt lgkmcnt(4)\n\t"
  "v_cvt_pk_bf16_f32 v120, v56, v57\n\t"
  "v_mfma_f32_16x16x32_bf16 v[20:23], a[120:123], v[84:87], v[20:23]\n\t"
  "ds_read_b64_tr_b16 v[76:77], v110 offset:8192\n\t"
  "v_xor_b32 v108, 64, v5\n\t"
  "v_mfma_f32_16x16x32_bf16 v[24:27], a[124:127], v[84:87], v[24:27]\n\t"
  "ds_read_b64_tr_b16 v[78:79], v110 offset:10240\n\t"
  "v_add_u32 v108, 81920, v108\n\t"
  "v_mfma_f32_16x16x32_bf16 v[28:31], a[128:131], v[84:87], v[28:31]\n\t"
  "ds_read_b64_tr_b16 v[80:81], v111 offset:8192\n\t"
  "v_xor_b32 v109, 96, v5\n\t"
  "v_mfma_f32_16x16x32_bf16 v[32:35], a[120:123], v[88:91], v[32:35]\n\t"
  "ds_read_b64_tr_b16 v[82:83], v111 offset:10240\n\t"
  "v_add_u32 v109, 81920, v109\n\t"
  "v_mfma_f32_16x16x32_bf16 v[36:39], a[124:127], v[88:91], v[36:39]\n\t"
  "buffer_load_dwordx4 v1, %[srd], s46 offen offset:0 sc1 lds\n\t"
  "buffer_load_dwordx4 v1, %[srd], s46 offen offset:1024 sc1 lds\n\t"
  "v_mfma_f32_16x16x32_bf16 v[40:43], a[128:131], v[88:91], v[40:43]\n\t"
  "s_waitcnt lgkmcnt(4)\n\t"
  "v_max_f32 v60, 0, v60\n\t"
  "v_mfma_f32_16x16x32_bf16 v[20:23], a[132:135], v[68:71], v[20:23]\n\t"
  "ds_read_b64_tr_b16 v[84:85], v110 offset:12288\n\t"
  "buffer_load_dwordx4 v1, %[srd], s46 offen offset:2048 sc1 lds\n\t"
  "v_mfma_f32_16x16x32_bf16 v[24:27], a[136:139], v[68:71], v[24:27]\n\t"
  "ds_read_b64_tr_b16 v[86:87], v110 offset:14336\n\t"
  "v_max_f32 v61, 0, v61\n\t"
  "v_mfma_f32_16x16x32_bf16 v[28:31], a[140:143], v[68:71], v[28:31]\n\t"
  "ds_read_b64_tr_b16 v[88:89], v111 offset:12288\n\t"
  "v_mul_f32 v62, v62, v129\n\t"
  "v_mfma_f32_16x16x32_bf16 v[32:35], a[132:135], v[72:75], v[32:35]\n\t"
  "ds_read_b64_tr_b16 v[90:91], v111 offset:14336\n\t"
  "v_mul_f32 v63, v63, v129\n\t"
  "v_mfma_f32_16x16x32_bf16 v[36:39], a[136:139], v[72:75], v[36:39]\n\t"
  "buffer_load_dwordx4 v1, %[srd], s46 offen offset:3072 sc1 lds\n\t"
  "v_mul_f32 v60, v60, v62\n\t"
  "v_mfma_f32_16x16x32_bf16 v[40:43], a[140:143], v[72:75], v[40:43]\n\t"
  "s_waitcnt lgkmcnt(4)\n\t"
  "v_mul_f32 v61, v61, v63\n\t"
  "v_mfma_f32_16x16x32_bf16 v[20:23], a[144:147], v[76:79], v[20:23]\n\t"
  "ds_read_b64_tr_b16 v[68:69], v110 offset:16384\n\t"
  "v_cvt_pk_bf16_f32 v125, v60, v61\n\t"
  "v_mfma_f32_16x16x32_bf16 v[24:27], a[148:151], v[76:79], v[24:27]\n\t"
  "ds_read_b64_tr_b16 v[70:71], v110 offset:18432\n\t"
  "s_add_u32 m0, s52, 12288\n\t"
  "v_mfma_f32_16x16x32_bf16 v[28:31], a[152:155], v[76:79], v[28:31]\n\t"
  "ds_read_b64_tr_b16 v[72:73], v111 offset:16384\n\t"
  "v_cvt_pk_bf16_f32 v121, v60, v61\n\t"
  "v_mfma_f32_16x16x32_bf16 v[32:35], a[144:147], v[80:83], v[32:35]\n\t"
  "ds_read_b64_tr_b16 v[74:75], v111 offset:18432\n\t"
  "v_max_f32 v64, 0, v64\n\t"
  "v_mfma_f32_16x16x32_bf16 v[36:39], a[148:151], v[80:83], v[36:39]\n\t"
  "s_add_u32 s46, %[sW2], 38912\n\t"
  "v_max_f32 v65, 0, v65\n\t"
  "v_mfma_f32_16x16x32_bf16 v[40:43], a[152:155], v[80:83], v[40:43]\n\t"
  "s_waitcnt lgkmcnt(4)\n\t"
  "v_mul_f32 v66, v66, v129\n\t"
  "v_mfma_f32_16x16x32_bf16 v[20:23], a[156:159], v[84:87], v[20:23]\n\t"
  "ds_read_b64_tr_b16 v[76:77], v110 offset:20480\n\t"
  "v_mul_f32 v67, v67, v129\n\t"
  "v_mfma_f32_16x16x32_bf16 v[24:27], a[160:163], v[84:87], v[24:27]\n\t"
  "ds_read_b64_tr_b16 v[78:79], v110 offset:22528\n\t"
  "v_mul_f32 v64, v64, v66\n\t"
  "v_mfma_f32_16x16x32_bf16 v[28:31], a[164:167], v[84:87], v[28:31]\n\t"
  "ds_read_b64_tr_b16 v[80:81], v111 offset:20480\n\t"
  "buffer_load_dwordx4 v1, %[srd], s46 offen offset:0 sc1 lds\n\t"
  "v_mfma_f32_16x16x32_bf16 v[32:35], a[156:159], v[88:91], v[32:35]\n\t"
  "ds_read_b64_tr_b16 v[82:83], v111 offset:22528\n\t"
  "v_permlane16_swap_b32 v124, v125\n\t"
  "v_mfma_f32_16x16x32_bf16 v[36:39], a[160:163], v[88:91], v[36:39]\n\t"
  "v_mul_f32 v65, v65, v67\n\t"
  "v_cvt_pk_bf16_f32 v126, v64, v65\n\t"
  "v_mfma_f32_16x16x32_bf16 v[40:43], a[164:167], v[88:91], v[40:43]\n\t"
  "s_waitcnt lgkmcnt(4)\n\t"
  "buffer_load_dwordx4 v1, %[srd], s46 offen offset:1024 sc1 lds\n\t"
  "v_mfma_f32_16x16x32_bf16 v[20:23], a[168:171], v[68:71], v[20:23]\n\t"
  "ds_read_b64_tr_b16 v[84:85], v110 offset:24576\n\t"
  "v_cvt_pk_bf16_f32 v122, v64, v65\n\t"
  "v_mfma_f32_16x16x32_bf16 v[24:27], a[172:175], v[68:71], v[24:27]\n\t"
  "ds_read_b64_tr_b16 v[86:87], v110 offset:26624\n\t"
  "s_mov_b32 s55, 147456\n\t"
  "v_mfma_f32_16x16x32_bf16 v[28:31], a[176:179], v[68:71], v[28:31]\n\t"
  "ds_read_b64_tr_b16 v[88:89], v111 offset:24576\n\t"
  "v_permlane16_swap_b32 v126, v123\n\t"
  "v_mfma_f32_16x16x32_bf16 v[32:35], a[168:171], v[72:75], v[32:35]\n\t"
  "ds_read_b64_tr_b16 v[90:91], v111 offset:26624\n\t"
  "buffer_load_dwordx4 v1, %[srd], s46 offen offset:2048 sc1 lds\n\t"
  "v_mfma_f32_16x16x32_bf16 v[36:39], a[172:175], v[72:75], v[36:39]\n\t"
  "v_permlane32_swap_b32 v125, v123\n\t"
  "buffer_store_dwordx4 v[120:123], v8, %[srd], s55 offen sc1\n\t"
  "v_mfma_f32_16x16x32_bf16 v[40:43], a[176:179], v[72:75], v[40:43]\n\t"
  "s_waitcnt lgkmcnt(4)\n\t"
  "v_mfma_f32_16x16x32_bf16 v[20:23], a[180:183], v[76:79], v[20:23]\n\t"
  "ds_read_b64_tr_b16 v[68:69], v110 offset:28672\n\t"
  "buffer_load_dwordx4 v1, %[srd], s46 offen offset:3072 sc1 lds\n\t"
  "v_mfma_f32_16x16x32_bf16 v[24:27], a[184:187], v[76:79], v[24:27]\n\t"
  "ds_read_b64_tr_b16 v[70:71], v110 offset:30720\n\t"
  "v_mfma_f32_16x16x32_bf16 v[28:31], a[188:191], v[76:79], v[28:31]\n\t"
  "ds_read_b64_tr_b16 v[72:73], v111 offset:28672\n\t"
  "v_mfma_f32_16x16x32_bf16 v[32:35], a[180:183], v[80:83], v[32:35]\n\t"
  "ds_read_b64_tr_b16 v[74:75], v111 offset:30720\n\t"
  "s_add_u32 m0, s52, 16384\n\t"
  "v_mfma_f32_16x16x32_bf16 v[36:39], a[184:187], v[80:83], v[36:39]\n\t"
  "v_mfma_f32_16x16x32_bf16 v[40:43], a[188:191], v[80:83], v[40:43]\n\t"
  "s_waitcnt lgkmcnt(4)\n\t"
  "v_mfma_f32_16x16x32_bf16 v[20:23], a[192:195], v[84:87], v[20:23]\n\t"
  "ds_read_b64_tr_b16 v[76:77], v110 offset:32768\n\t"
  "v_mfma_f32_16x16x32_bf16 v[24:27], a[196:199], v[84:87], v[24:27]\n\t"
  "ds_read_b64_tr_b16 v[78:79], v110 offset:34816\n\t"
  "s_add_u32 s46, %[sW2], 43008\n\t"
  "v_mfma_f32_16x16x32_bf16 v[28:31], a[200:203], v[84:87], v[28:31]\n\t"
  "ds_read_b64_tr_b16 v[80:81], v111 offset:32768\n\t"
  "v_mfma_f32_16x16x32_bf16 v[32:35], a[192:195], v[88:91], v[32:35]\n\t"
  "ds_read_b64_tr_b16 v[82:83], v111 offset:34816\n\t"
  "v_mfma_f32_16x16x32_bf16 v[36:39], a[196:199], v[88:91], v[36:39]\n\t"
  "buffer_load_dwordx4 v1, %[srd], s46 offen offset:0 sc1 lds\n\t"
  "v_mfma_f32_16x16x32_bf16 v[40:43], a[200:203], v[88:91], v[40:43]\n\t"
  "s_waitcnt lgkmcnt(4)\n\t"
  "v_mfma_f32_16x16x32_bf16 v[20:23], a[204:207], v[68:71], v[20:23]\n\t"
  "ds_read_b64_tr_b16 v[84:85], v110 offset:36864\n\t"
  "v_mfma_f32_16x16x32_bf16 v[24:27], a[208:211], v[68:71], v[24:27]\n\t"
  "ds_read_b64_tr_b16 v[86:87], v110 offset:38912\n\t"
  "buffer_load_dwordx4 v1, %[srd], s46 offen offset:1024 sc1 lds\n\t"
  "v_mfma_f32_16x16x32_bf16 v[28:31], a[212:215], v[68:71], v[28:31]\n\t"
  "ds_read_b64_tr_b16 v[88:89], v111 offset:36864\n\t"
  "v_mfma_f32_16x16x32_bf16 v[32:35], a[204:207], v[72:75], v[32:35]\n\t"
  "ds_read_b64_tr_b16 v[90:91], v111 offset:38912\n\t"
  "v_mfma_f32_16x16x32_bf16 v[36:39], a[208:211], v[72:75], v[36:39]\n\t"
  "s_waitcnt vmcnt(14)\n\t"
  "v_min_u32 v131, v101, v103\n\t"
  "v_mfma_f32_16x16x32_bf16 v[40:43], a[212:215], v[72:75], v[40:43]\n\t"
  "s_waitcnt lgkmcnt(4)\n\t"
  "s_waitcnt vmcnt(12)\n\t"
  "v_min3_u32 v131, v131, v105, v107\n\t"
  "v_mfma_f32_16x16x32_bf16 v[20:23], a[216:219], v[76:79], v[20:23]\n\t"
  "ds_read_b64_tr_b16 v[68:69], v108 offset:0\n\t"
  "v_cmp_eq_u32 vcc, 0, v131\n\t"
  "v_mfma_f32_16x16x32_bf16 v[24:27], a[220:223], v[76:79], v[24:27]\n\t"
  "ds_read_b64_tr_b16 v[70:71], v108 offset:2048\n\t"
  "buffer_load_dwordx4 v1, %[srd], s46 offen offset:2048 sc1 lds\n\t"
  "v_mfma_f32_16x16x32_bf16 v[28:31], a[224:227], v[76:79], v[28:31]\n\t"
  "ds_read_b64_tr_b16 v[72:73], v109 offset:0\n\t"
  "s_cbranch_vccnz Lst1_slow%=\n\t"
  "Lst1_back%=:\n\t"
  "v_mfma_f32_16x16x32_bf16 v[32:35], a[216:219], v[80:83], v[32:35]\n\t"
  "ds_read_b64_tr_b16 v[74:75], v109 offset:2048\n\t"
  "buffer_load_dwordx4 v1, %[srd], s46 offen offset:3072 sc1 lds\n\t"
  "v_mfma_f32_16x16x32_bf16 v[36:39], a[220:223], v[80:83], v[36:39]\n\t"
  "v_mfma_f32_16x16x32_bf16 v[40:43], a[224:227], v[80:83], v[40:43]\n\t"
  "s_waitcnt lgkmcnt(4)\n\t"
  "v_mfma_f32_16x16x32_bf16 v[20:23], a[228:231], v[84:87], v[20:23]\n\t"
  "ds_read_b64_tr_b16 v[76:77], v108 offset:4096\n\t"
  "v_mfma_f32_16x16x32_bf16 v[24:27], a[232:235], v[84:87], v[24:27]\n\t"
  "ds_read_b64_tr_b16 v[78:79], v108 offset:6144\n\t"
  "v_mfma_f32_16x16x32_bf16 v[28:31], a[236:239], v[84:87], v[28:31]\n\t"
  "ds_read_b64_tr_b16 v[80:81], v109 offset:4096\n\t"
  "v_mfma_f32_16x16x32_bf16 v[32:35], a[228:231], v[88:91], v[32:35]\n\t"
  "ds_read_b64_tr_b16 v[82:83], v109 offset:6144\n\t"
  "v_mfma_f32_16x16x32_bf16 v[36:39], a[232:235], v[88:91], v[36:39]\n\t"
  "v_mfma_f32_16x16x32_bf16 v[40:43], a[236:239], v[88:91], v[40:43]\n\t"
  "v_mfma_f32_16x16x16_bf16 v[20:23], a[240:241], v[100:101], v[20:23]\n\t"
  "v_mfma_f32_16x16x16_bf16 v[24:27], a[242:243], v[100:101], v[24:27]\n\t"
  "v_mfma_f32_16x16x16_bf16 v[28:31], a[244:245], v[100:101], v[28:31]\n\t"
  "v_mfma_f32_16x16x16_bf16 v[32:35], a[240:241], v[102:103], v[32:35]\n\t"
  "v_mfma_f32_16x16x16_bf16 v[36:39], a[242:243], v[102:103], v[36:39]\n\t"
  "v_mfma_f32_16x16x16_bf16 v[40:43], a[244:245], v[102:103], v[40:43]\n\t"
  "s_waitcnt lgkmcnt(4)\n\t"
  "v_mfma_f32_16x16x32_bf16 v[44:47], a[0:3], v[68:71], 0\n\t"
  "ds_read_b64_tr_b16 v[84:85], v108 offset:8192\n\t"
  "v_xor_b32 v110, 64, v5\n\t"
  "v_mfma_f32_16x16x32_bf16 v[48:51], a[4:7], v[68:71], 0\n\t"
  "ds_read_b64_tr_b16 v[86:87], v108 offset:10240\n\t"
  "v_add_u32 v110, 122880, v110\n\t"
  "v_mfma_f32_16x16x32_bf16 v[52:55], a[8:11], v[68:71], 0\n\t"
  "ds_read_b64_tr_b16 v[88:89], v109 offset:8192\n\t"
  "v_xor_b32 v111, 96, v5\n\t"
  "v_mfma_f32_16x16x32_bf16 v[56:59], a[0:3], v[72:75], 0\n\t"
  "ds_read_b64_tr_b16 v[90:91], v109 offset:10240\n\t"
  "v_add_u32 v111, 122880, v111\n\t"
  "v_mfma_f32_16x16x32_bf16 v[60:63], a[4:7], v[72:75], 0\n\t"
  "v_and_b32 v128, 0xffff0000, v101\n\t"
  "v_and_b32 v129, 0xffff0000, v103\n\t"
  "v_mfma_f32_16x16x32_bf16 v[64:67], a[8:11], v[72:75], 0\n\t"
  "s_waitcnt lgkmcnt(4)\n\t"
  "v_rcp_f32 v128, v128\n\t"
  "v_mfma_f32_16x16x32_bf16 v[44:47], a[12:15], v[76:79], v[44:47]\n\t"
  "ds_read_b64_tr_b16 v[68:69], v108 offset:12288\n\t"
  "s_add_u32 s45, %[sW2], 47104\n\t"
  "v_mfma_f32_16x16x32_bf16 v[48:51], a[16:19], v[76:79], v[48:51]\n\t"
  "ds_read_b64_tr_b16 v[70:71], v108 offset:14336\n\t"
  "v_rcp_f32 v129, v129\n\t"
  "v_mfma_f32_16x16x32_bf16 v[52:55], a[20:23], v[76:79], v[52:55]\n\t"
  "ds_read_b64_tr_b16 v[72:73], v109 offset:12288\n\t"
  "v_mul_f32 v128, v128, v128\n\t"
  "v_mfma_f32_16x16x32_bf16 v[56:59], a[12:15], v[80:83], v[56:59]\n\t"
  "ds_read_b64_tr_b16 v[74:75], v109 offset:14336\n\t"
  "v_mul_f32 v129, v129, v129\n\t"
  "v_mfma_f32_16x16x32_bf16 v[60:63], a[16:19], v[80:83], v[60:63]\n\t"
  "buffer_load_dwordx4 a[0:3], v1, %[srd], s45 offen offset:0 sc1\n\t"
  "v_max_f32 v20, 0, v20\n\t"
  "v_mfma_f32_16x16x32_bf16 v[64:67], a[20:23], v[80:83], v[64:67]\n\t"
  "s_waitcnt lgkmcnt(4)\n\t"
  "v_max_f32 v21, 0, v21\n\t"
  "v_mfma_f32_16x16x32_bf16 v[44:47], a[24:27], v[84:87], v[44:47]\n\t"
  "ds_read_b64_tr_b16 v[76:77], v108 offset:16384\n\t"
  "v_mul_f32 v22, v22, v128\n\t"
  "v_mfma_f32_16x16x32_bf16 v[48:51], a[28:31], v[84:87], v[48:51]\n\t"
  "ds_read_b64_tr_b16 v[78:79], v108 offset:18432\n\t"
  "v_mul_f32 v23, v23, v128\n\t"
  "v_mfma_f32_16x16x32_bf16 v[52:55], a[32:35], v[84:87], v[52:55]\n\t"
  "ds_read_b64_tr_b16 v[80:81], v109 offset:16384\n\t"
  "v_mul_f32 v20, v20, v22\n\t"
  "v_mfma_f32_16x16x32_bf16 v[56:59], a[24:27], v[88:91], v[56:59]\n\t"
  "ds_read_b64_tr_b16 v[82:83], v109 offset:18432\n\t"
  "buffer_load_dwordx4 a[4:7], v1, %[srd], s45 offen offset:1024 sc1\n\t"
  "v_mfma_f32_16x16x32_bf16 v[60:63], a[28:31], v[88:91], v[60:63]\n\t"
  "v_mul_f32 v21, v21, v23\n\t"
  "v_cvt_pk_bf16_f32 v116, v20, v21\n\t"
  "v_mfma_f32_16x16x32_bf16 v[64:67], a[32:35], v[88:91], v[64:67]\n\t"
  "s_waitcnt lgkmcnt(4)\n\t"
  "v_cvt_pk_bf16_f32 v112, v20, v21\n\t"
  "v_mfma_f32_16x16x32_bf16 v[44:47], a[36:39], v[68:71], v[44:47]\n\t"
  "ds_read_b64_tr_b16 v[84:85], v108 offset:20480\n\t"
  "s_movk_i32 s56, 8192\n\t"
  "v_mfma_f32_16x16x32_bf16 v[48:51], a[40:43], v[68:71], v[48:51]\n\t"
  "ds_read_b64_tr_b16 v[86:87], v108 offset:22528\n\t"
  "s_waitcnt vmcnt(7)\n\t"
  "v_mfma_f32_16x16x32_bf16 v[52:55], a[44:47], v[68:71], v[52:55]\n\t"
  "ds_read_b64_tr_b16 v[88:89], v109 offset:20480\n\t"
  "buffer_store_dwordx4 v[132:135], v9, %[srd], s56 offen sc1\n\t"
  "v_mfma_f32_16x16x32_bf16 v[56:59], a[36:39], v[72:75], v[56:59]\n\t"
  "ds_read_b64_tr_b16 v[90:91], v109 offset:22528\n\t"
  "buffer_load_dwordx4 a[8:11], v1, %[srd], s45 offen offset:2048 sc1\n\t"
  "v_mfma_f32_16x16x32_bf16 v[60:63], a[40:43], v[72:75], v[60:63]\n\t"
  "v_max_f32 v24, 0, v24\n\t"
  "v_max_f32 v25, 0, v25\n\t"
  "v_mfma_f32_16x16x32_bf16 v[64:67], a[44:47], v[72:75], v[64:67]\n\t"
  "s_waitcnt lgkmcnt(4)\n\t"
  "v_mul_f32 v26, v26, v128\n\t"
  "v_mfma_f32_16x16x32_bf16 v[44:47], a[48:51], v[76:79], v[44:47]\n\t"
  "ds_read_b64_tr_b16 v[68:69], v108 offset:24576\n\t"
  "v_mul_f32 v27, v27, v128\n\t"
  "v_mfma_f32_16x16x32_bf16 v[48:51], a[52:55], v[76:79], v[48:51]\n\t"
  "ds_read_b64_tr_b16 v[70:71], v108 offset:26624\n\t"
  "buffer_load_dwordx4 a[12:15], v1, %[srd], s45 offen offset:3072 sc1\n\t"
  "v_mfma_f32_16x16x32_bf16 v[52:55], a[56:59], v[76:79], v[52:55]\n\t"
  "ds_read_b64_tr_b16 v[72:73], v109 offset:24576\n\t"
  "v_mul_f32 v24, v24, v26\n\t"
  "v_mfma_f32_16x16x32_bf16 v[56:59], a[48:51], v[80:83], v[56:59]\n\t"
  "ds_read_b64_tr_b16 v[74:75], v109 offset:26624\n\t"
  "v_mul_f32 v25, v25, v27\n\t"
  "v_mfma_f32_16x16x32_bf16 v[60:63], a[52:55], v[80:83], v[60:63]\n\t"
  "v_cvt_pk_bf16_f32 v117, v24, v25\n\t"
  "v_cvt_pk_bf16_f32 v113, v24, v25\n\t"
  "v_mfma_f32_16x16x32_bf16 v[64:67], a[56:59], v[80:83], v[64:67]\n\t"
  "s_waitcnt lgkmcnt(4)\n\t"
  "s_add_u32 s45, %[sW2], 51200\n\t"
  "v_mfma_f32_16x16x32_bf16 v[44:47], a[60:63], v[84:87], v[44:47]\n\t"
  "ds_read_b64_tr_b16 v[76:77], v108 offset:28672\n\t"
  "v_max_f32 v28, 0, v28\n\t"
  "v_mfma_f32_16x16x32_bf16 v[48:51], a[64:67], v[84:87], v[48:51]\n\t"
  "ds_read_b64_tr_b16 v[78:79], v108 offset:30720\n\t"
  "v_max_f32 v29, 0, v29\n\t"
  "v_mfma_f32_16x16x32_bf16 v[52:55], a[68:71], v[84:87], v[52:55]\n\t"
  "ds_read_b64_tr_b16 v[80:81], v109 offset:28672\n\t"
  "v_mul_f32 v30, v30, v128\n\t"
  "v_mfma_f32_16x16x32_bf16 v[56:59], a[60:63], v[88:91], v[56:59]\n\t"
  "ds_read_b64_tr_b16 v[82:83], v109 offset:30720\n\t"
  "v_mul_f32 v31, v31, v128\n\t"
  "v_mfma_f32_16x16x32_bf16 v[60:63], a[64:67], v[88:91], v[60:63]\n\t"
  "buffer_load_dwordx4 a[16:19], v1, %[srd], s45 offen offset:0 sc1\n\t"
  "v_mul_f32 v28, v28, v30\n\t"
  "v_mfma_f32_16x16x32_bf16 v[64:67], a[68:71], v[88:91], v[64:67]\n\t"
  "s_waitcnt lgkmcnt(4)\n\t"
  "v_permlane16_swap_b32 v116, v117\n\t"
  "v_mfma_f32_16x16x32_bf16 v[44:47], a[72:75], v[68:71], v[44:47]\n\t"
  "ds_read_b64_tr_b16 v[84:85], v108 offset:32768\n\t"
  "v_mul_f32 v29, v29, v31\n\t"
  "v_mfma_f32_16x16x32_bf16 v[48:51], a[76:79], v[68:71], v[48:51]\n\t"
  "ds_read_b64_tr_b16 v[86:87], v108 offset:34816\n\t"
  "v_cvt_pk_bf16_f32 v118, v28, v29\n\t"
  "v_mfma_f32_16x16x32_bf16 v[52:55], a[80:83], v[68:71], v[52:55]\n\t"
  "ds_read_b64_tr_b16 v[88:89], v109 offset:32768\n\t"
  "v_cvt_pk_bf16_f32 v114, v28, v29\n\t"
  "v_mfma_f32_16x16x32_bf16 v[56:59], a[72:75], v[72:75], v[56:59]\n\t"
  "ds_read_b64_tr_b16 v[90:91], v109 offset:34816\n\t"
  "buffer_load_dwordx4 a[20:23], v1, %[srd], s45 offen offset:1024 sc1\n\t"
  "v_mfma_f32_16x16x32_bf16 v[60:63], a[76:79], v[72:75], v[60:63]\n\t"
  "s_mov_b32 s55, 3145728\n\t"
  "v_permlane16_swap_b32 v118, v115\n\t"
  "v_mfma_f32_16x16x32_bf16 v[64:67], a[80:83], v[72:75], v[64:67]\n\t"
  "s_waitcnt lgkmcnt(4)\n\t"
  "v_permlane32_swap_b32 v117, v115\n\t"
  "v_mfma_f32_16x16x32_bf16 v[44:47], a[84:87], v[76:79], v[44:47]\n\t"
  "ds_read_b64_tr_b16 v[68:69], v108 offset:36864\n\t"
  "s_add_u32 s57, %[sFL], 0\n\t"
  "v_mfma_f32_16x16x32_bf16 v[48:51], a[88:91], v[76:79], v[48:51]\n\t"
  "ds_read_b64_tr_b16 v[70:71], v108 offset:38912\n\t"
  "buffer_load_dwordx2 v[138:139], v137, %[srd], s57 offen sc1\n\t"
  "v_mfma_f32_16x16x32_bf16 v[52:55], a[92:95], v[76:79], v[52:55]\n\t"
  "ds_read_b64_tr_b16 v[72:73], v109 offset:36864\n\t"
  "buffer_load_dwordx4 a[24:27], v1, %[srd], s45 offen offset:2048 sc1\n\t"
  "v_mfma_f32_16x16x32_bf16 v[56:59], a[84:87], v[80:83], v[56:59]\n\t"
  "ds_read_b64_tr_b16 v[74:75], v109 offset:38912\n\t"
  "buffer_store_dwordx4 v[112:115], v8, %[srd], s55 offen sc1\n\t"
  "v_mfma_f32_16x16x32_bf16 v[60:63], a[88:91], v[80:83], v[60:63]\n\t"
  "v_max_f32 v32, 0, v32\n\t"
  "v_max_f32 v33, 0, v33\n\t"
  "v_mfma_f32_16x16x32_bf16 v[64:67], a[92:95], v[80:83], v[64:67]\n\t"
  "s_waitcnt lgkmcnt(4)\n\t"
  "v_mul_f32 v34, v34, v129\n\t"
  "v_mfma_f32_16x16x32_bf16 v[44:47], a[96:99], v[84:87], v[44:47]\n\t"
  "ds_read_b64_tr_b16 v[76:77], v110 offset:0\n\t"
  "v_mul_f32 v35, v35, v129\n\t"
  "v_mfma_f32_16x16x32_bf16 v[48:51], a[100:103], v[84:87], v[48:51]\n\t"
  "ds_read_b64_tr_b16 v[78:79], v110 offset:2048\n\t"
  "buffer_load_dwordx4 a[28:31], v1, %[srd], s45 offen offset:3072 sc1\n\t"
  "v_mfma_f32_16x16x32_bf16 v[52:55], a[104:107], v[84:87], v[52:55]\n\t"
  "ds_read_b64_tr_b16 v[80:81], v111 offset:0\n\t"
  "v_mul_f32 v32, v32, v34\n\t"
  "v_mfma_f32_16x16x32_bf16 v[56:59], a[96:99], v[88:91], v[56:59]\n\t"
  "ds_read_b64_tr_b16 v[82:83], v111 offset:2048\n\t"
  "v_mul_f32 v33, v33, v35\n\t"
  "v_mfma_f32_16x16x32_bf16 v[60:63], a[100:103], v[88:91], v[60:63]\n\t"
  "v_cvt_pk_bf16_f32 v124, v32, v33\n\t"
  "v_cvt_pk_bf16_f32 v120, v32, v33\n\t"
  "v_mfma_f32_16x16x32_bf16 v[64:67], a[104:107], v[88:91], v[64:67]\n\t"
  "s_waitcnt lgkmcnt(4)\n\t"
  "s_add_u32 s45, %[sW2], 55296\n\t"
  "v_mfma_f32_16x16x32_bf16 v[44:47], a[108:111], v[68:71], v[44:47]\n\t"
  "ds_read_b64_tr_b16 v[84:85], v110 offset:4096\n\t"
  "v_max_f32 v36, 0, v36\n\t"
  "v_mfma_f32_16x16x32_bf16 v[48:51], a[112:115], v[68:71], v[48:51]\n\t"
  "ds_read_b64_tr_b16 v[86:87], v110 offset:6144\n\t"
  "v_max_f32 v37, 0, v37\n\t"
  "v_mfma_f32_16x16x32_bf16 v[52:55], a[116:119], v[68:71], v[52:55]\n\t"
  "ds_read_b64_tr_b16 v[88:89], v111 offset:4096\n\t"
  "v_mul_f32 v38, v38, v129\n\t"
  "v_mfma_f32_16x16x32_bf16 v[56:59], a[108:111], v[72:75], v[56:59]\n\t"
  "ds_read_b64_tr_b16 v[90:91], v111 offset:6144\n\t"
  "v_mul_f32 v39, v39, v129\n\t"
  "v_mfma_f32_16x16x32_bf16 v[60:63], a[112:115], v[72:75], v[60:63]\n\t"
  "buffer_load_dwordx4 a[32:35], v1, %[srd], s45 offen offset:0 sc1\n\t"
  "v_mul_f32 v36, v36, v38\n\t"
  "v_mfma_f32_16x16x32_bf16 v[64:67], a[116:119], v[72:75], v[64:67]\n\t"
  "s_waitcnt lgkmcnt(4)\n\t"
  "v_mul_f32 v37, v37, v39\n\t"
  "v_mfma_f32_16x16x32_bf16 v[44:47], a[120:123], v[76:79], v[44:47]\n\t"
  "ds_read_b64_tr_b16 v[68:69], v110 offset:8192\n\t"
  "v_cvt_pk_bf16_f32 v125, v36, v37\n\t"
  "v_mfma_f32_16x16x32_bf16 v[48:51], a[124:127], v[76:79], v[48:51]\n\t"
  "ds_read_b64_tr_b16 v[70:71], v110 offset:10240\n\t"
  "v_cvt_pk_bf16_f32 v121, v36, v37\n\t"
  "v_mfma_f32_16x16x32_bf16 v[52:55], a[128:131], v[76:79], v[52:55]\n\t"
  "ds_read_b64_tr_b16 v[72:73], v111 offset:8192\n\t"
  "v_max_f32 v40, 0, v40\n\t"
  "v_mfma_f32_16x16x32_bf16 v[56:59], a[120:123], v[80:83], v[56:59]\n\t"
  "ds_read_b64_tr_b16 v[74:75], v111 offset:10240\n\t"
  "buffer_load_dwordx4 a[36:39], v1, %[srd], s45 offen offset:1024 sc1\n\t"
  "v_mfma_f32_16x16x32_bf16 v[60:63], a[124:127], v[80:83], v[60:63]\n\t"
  "v_max_f32 v41, 0, v41\n\t"
  "v_mul_f32 v42, v42, v129\n\t"
  "v_mfma_f32_16x16x32_bf16 v[64:67], a[128:131], v[80:83], v[64:67]\n\t"
  "s_waitcnt lgkmcnt(4)\n\t"
  "v_mul_f32 v43, v43, v129\n\t"
  "v_mfma_f32_16x16x32_bf16 v[44:47], a[132:135], v[84:87], v[44:47]\n\t"
  "ds_read_b64_tr_b16 v[76:77], v110 offset:12288\n\t"
  "v_mul_f32 v40, v40, v42\n\t"
  "v_mfma_f32_16x16x32_bf16 v[48:51], a[136:139], v[84:87], v[48:51]\n\t"
  "ds_read_b64_tr_b16 v[78:79], v110 offset:14336\n\t"
  "v_permlane16_swap_b32 v124, v125\n\t"
  "v_mfma_f32_16x16x32_bf16 v[52:55], a[140:143], v[84:87], v[52:55]\n\t"
  "ds_read_b64_tr_b16 v[80:81], v111 offset:12288\n\t"
  "buffer_load_dwordx4 a[40:43], v1, %[srd], s45 offen offset:2048 sc1\n\t"
  "v_mfma_f32_16x16x32_bf16 v[56:59], a[132:135], v[88:91], v[56:59]\n\t"
  "ds_read_b64_tr_b16 v[82:83], v111 offset:14336\n\t"
  "v_mul_f32 v41, v41, v43\n\t"
  "v_mfma_f32_16x16x32_bf16 v[60:63], a[136:139], v[88:91], v[60:63]\n\t"
  "v_cvt_pk_bf16_f32 v126, v40, v41\n\t"
  "v_cvt_pk_bf16_f32 v122, v40, v41\n\t"
  "v_mfma_f32_16x16x32_bf16 v[64:67], a[140:143], v[88:91], v[64:67]\n\t"
  "s_waitcnt lgkmcnt(4)\n\t"
  "s_mov_b32 s55, 3194880\n\t"
  "v_mfma_f32_16x16x32_bf16 v[44:47], a[144:147], v[68:71], v[44:47]\n\t"
  "ds_read_b64_tr_b16 v[84:85], v110 offset:16384\n\t"
  "buffer_load_dwordx4 a[44:47], v1, %[srd], s45 offen offset:3072 sc1\n\t"
  "v_mfma_f32_16x16x32_bf16 v[48:51], a[148:151], v[68:71], v[48:51]\n\t"
  "ds_read_b64_tr_b16 v[86:87], v110 offset:18432\n\t"
  "v_permlane16_swap_b32 v126, v123\n\t"
  "v_mfma_f32_16x16x32_bf16 v[52:55], a[152:155], v[68:71], v[52:55]\n\t"
  "ds_read_b64_tr_b16 v[88:89], v111 offset:16384\n\t"
  "v_permlane32_swap_b32 v125, v123\n\t"
  "v_mfma_f32_16x16x32_bf16 v[56:59], a[144:147], v[72:75], v[56:59]\n\t"
  "ds_read_b64_tr_b16 v[90:91], v111 offset:18432\n\t"
  "buffer_store_dwordx4 v[120:123], v8, %[srd], s55 offen sc1\n\t"
  "v_mfma_f32_16x16x32_bf16 v[60:63], a[148:151], v[72:75], v[60:63]\n\t"
  "v_and_b32 v128, 0xffff0000, v105\n\t"
  "v_and_b32 v129, 0xffff0000, v107\n\t"
  "v_mfma_f32_16x16x32_bf16 v[64:67], a[152:155], v[72:75], v[64:67]\n\t"
  "s_waitcnt lgkmcnt(4)\n\t"
  "s_add_u32 s45, %[sW2], 59392\n\t"
  "v_mfma_f32_16x16x32_bf16 v[44:47], a[156:159], v[76:79], v[44:47]\n\t"
  "ds_read_b64_tr_b16 v[68:69], v110 offset:20480\n\t"
  "v_rcp_f32 v128, v128\n\t"
  "v_mfma_f32_16x16x32_bf16 v[48:51], a[160:163], v[76:79], v[48:51]\n\t"
  "ds_read_b64_tr_b16 v[70:71], v110 offset:22528\n\t"
  "v_rcp_f32 v129, v129\n\t"
  "v_mfma_f32_16x16x32_bf16 v[52:55], a[164:167], v[76:79], v[52:55]\n\t"
  "ds_read_b64_tr_b16 v[72:73], v111 offset:20480\n\t"
  "v_mul_f32 v128, v128, v128\n\t"
  "v_mfma_f32_16x16x32_bf16 v[56:59], a[156:159], v[80:83], v[56:59]\n\t"
  "ds_read_b64_tr_b16 v[74:75], v111 offset:22528\n\t"
  "v_mul_f32 v129, v129, v129\n\t"
  "v_mfma_f32_16x16x32_bf16 v[60:63], a[160:163], v[80:83], v[60:63]\n\t"
  "buffer_load_dwordx4 a[48:51], v1, %[srd], s45 offen offset:0 sc1\n\t"
  "v_mfma_f32_16x16x32_bf16 v[64:67], a[164:167], v[80:83], v[64:67]\n\t"
  "s_waitcnt lgkmcnt(4)\n\t"
  "s_waitcnt vmcnt(17)\n\t"
  "v_mfma_f32_16x16x32_bf16 v[44:47], a[168:171], v[84:87], v[44:47]\n\t"
  "ds_read_b64_tr_b16 v[76:77], v110 offset:24576\n\t"
  "ds_read_b128 a[160:163], v141 offset:0\n\t"
  "v_mfma_f32_16x16x32_bf16 v[48:51], a[172:175], v[84:87], v[48:51]\n\t"
  "ds_read_b64_tr_b16 v[78:79], v110 offset:26624\n\t"
  "ds_read_b128 a[164:167], v141 offset:1024\n\t"
  "v_mfma_f32_16x16x32_bf16 v[52:55], a[176:179], v[84:87], v[52:55]\n\t"
  "ds_read_b64_tr_b16 v[80:81], v111 offset:24576\n\t"
  "v_mfma_f32_16x16x32_bf16 v[56:59], a[168:171], v[88:91], v[56:59]\n\t"
  "ds_read_b64_tr_b16 v[82:83], v111 offset:26624\n\t"
  "buffer_load_dwordx4 a[52:55], v1, %[srd], s45 offen offset:1024 sc1\n\t"
  "v_mfma_f32_16x16x32_bf16 v[60:63], a[172:175], v[88:91], v[60:63]\n\t"
  "v_mfma_f32_16x16x32_bf16 v[64:67], a[176:179], v[88:91], v[64:67]\n\t"
  "s_waitcnt lgkmcnt(6)\n\t"
  "ds_read_b128 a[168:171], v141 offset:2048\n\t"
  "v_mfma_f32_16x16x32_bf16 v[44:47], a[180:183], v[68:71], v[44:47]\n\t"
  "ds_read_b64_tr_b16 v[84:85], v110 offset:28672\n\t"
  "ds_read_b128 a[172:175], v141 offset:3072\n\t"
  "v_mfma_f32_16x16x32_bf16 v[48:51], a[184:187], v[68:71], v[48:51]\n\t"
  "ds_read_b64_tr_b16 v[86:87], v110 offset:30720\n\t"
  "ds_read_b128 a[176:179], v141 offset:4096\n\t"
  "v_mfma_f32_16x16x32_bf16 v[52:55], a[188:191], v[68:71], v[52:55]\n\t"
  "ds_read_b64_tr_b16 v[88:89], v111 offset:28672\n\t"
  "v_mfma_f32_16x16x32_bf16 v[56:59], a[180:183], v[72:75], v[56:59]\n\t"
  "ds_read_b64_tr_b16 v[90:91], v111 offset:30720\n\t"
  "s_waitcnt vmcnt(10)\n\t"
  "v_cmp_ne_u64 vcc, v[132:133], v[138:139]\n\t"
  "v_mfma_f32_16x16x32_bf16 v[60:63], a[184:187], v[72:75], v[60:63]\n\t"
  "v_mfma_f32_16x16x32_bf16 v[64:67], a[188:191], v[72:75], v[64:67]\n\t"
  "s_waitcnt lgkmcnt(7)\n\t"
  "s_cbranch_vccnz Lfl0_slow%=\n\t"
  "Lfl0_back%=:\n\t"
  "v_mfma_f32_16x16x32_bf16 v[44:47], a[192:195], v[76:79], v[44:47]\n\t"
  "ds_read_b64_tr_b16 v[68:69], v110 offset:32768\n\t"
  "s_add_u32 s44, %[sH2ld], 0\n\t"
  "v_mfma_f32_16x16x32_bf16 v[48:51], a[196:199], v[76:79], v[48:51]\n\t"
  "ds_read_b64_tr_b16 v[70:71], v110 offset:34816\n\t"
  "ds_read_b128 a[180:183], v141 offset:5120\n\t"
  "v_mfma_f32_16x16x32_bf16 v[52:55], a[200:203], v[76:79], v[52:55]\n\t"
  "ds_read_b64_tr_b16 v[72:73], v111 offset:32768\n\t"
  "buffer_load_dwordx4 a[56:59], v1, %[srd], s44 offen offset:0 sc1\n\t"
  "v_mfma_f32_16x16x32_bf16 v[56:59], a[192:195], v[80:83], v[56:59]\n\t"
  "ds_read_b64_tr_b16 v[74:75], v111 offset:34816\n\t"
  "ds_read_b128 a[184:187], v141 offset:6144\n\t"
  "v_mfma_f32_16x16x32_bf16 v[60:63], a[196:199], v[80:83], v[60:63]\n\t"
  "buffer_load_dwordx4 a[60:63], v1, %[srd], s44 offen offset:1024 sc1\n\t"
  "ds_read_b128 a[188:191], v141 offset:7168\n\t"
  "v_mfma_f32_16x16x32_bf16 v[64:67], a[200:203], v[80:83], v[64:67]\n\t"
  "s_waitcnt lgkmcnt(7)\n\t"
  "ds_read_b128 a[192:195], v141 offset:8192\n\t"
  "v_mfma_f32_16x16x32_bf16 v[44:47], a[204:207], v[84:87], v[44:47]\n\t"
  "ds_read_b64_tr_b16 v[76:77], v110 offset:36864\n\t"
  "buffer_load_dwordx4 a[64:67], v1, %[srd], s44 offen offset:2048 sc1\n\t"
  "v_mfma_f32_16x16x32_bf16 v[48:51], a[208:211], v[84:87], v[48:51]\n\t"
  "ds_read_b64_tr_b16 v[78:79], v110 offset:38912\n\t"
  "ds_read_b128 a[196:199], v141 offset:9216\n\t"
  "v_mfma_f32_16x16x32_bf16 v[52:55], a[212:215], v[84:87], v[52:55]\n\t"
  "ds_read_b64_tr_b16 v[80:81], v111 offset:36864\n\t"
  "buffer_load_dwordx4 a[68:71], v1, %[srd], s44 offen offset:3072 sc1\n\t"
  "v_mfma_f32_16x16x32_bf16 v[56:59], a[204:207], v[88:91], v[56:59]\n\t"
  "ds_read_b64_tr_b16 v[82:83], v111 offset:38912\n\t"
  "ds_read_b128 a[200:203], v141 offset:10240\n\t"
  "v_mfma_f32_16x16x32_bf16 v[60:63], a[208:211], v[88:91], v[60:63]\n\t"
  "s_add_u32 s44, %[sH2ld], 4096\n\t"
  "v_mfma_f32_16x16x32_bf16 v[64:67], a[212:215], v[88:91], v[64:67]\n\t"
  "s_waitcnt lgkmcnt(9)\n\t"
  "ds_read_b128 a[204:207], v141 offset:11264\n\t"
  "v_mfma_f32_16x16x32_bf16 v[44:47], a[216:219], v[68:71], v[44:47]\n\t"
  "buffer_load_dwordx4 a[72:75], v1, %[srd], s44 offen offset:0 sc1\n\t"
  "ds_read_b128 a[208:211], v141 offset:12288\n\t"
  "v_mfma_f32_16x16x32_bf16 v[48:51], a[220:223], v[68:71], v[48:51]\n\t"
  "ds_read_b128 a[212:215], v141 offset:13312\n\t"
  "v_mfma_f32_16x16x32_bf16 v[52:55], a[224:227], v[68:71], v[52:55]\n\t"
  "buffer_load_dwordx4 a[76:79], v1, %[srd], s44 offen offset:1024 sc1\n\t"
  "v_mfma_f32_16x16x32_bf16 v[56:59], a[216:219], v[72:75], v[56:59]\n\t"
  "v_mfma_f32_16x16x32_bf16 v[60:63], a[220:223], v[72:75], v[60:63]\n\t"
  "buffer_load_dwordx4 a[80:83], v1, %[srd], s44 offen offset:2048 sc1\n\t"
  "v_mfma_f32_16x16x32_bf16 v[64:67], a[224:227], v[72:75], v[64:67]\n\t"
  "s_waitcnt lgkmcnt(4)\n\t"
  "ds_read_b128 a[216:219], v141 offset:14336\n\t"
  "v_mfma_f32_16x16x32_bf16 v[44:47], a[228:231], v[76:79], v[44:47]\n\t"
  "buffer_load_dwordx4 a[84:87], v1, %[srd], s44 offen offset:3072 sc1\n\t"
  "ds_read_b128 a[220:223], v141 offset:15360\n\t"
  "v_mfma_f32_16x16x32_bf16 v[48:51], a[232:235], v[76:79], v[48:51]\n\t"
  "ds_read_b128 a[224:227], v141 offset:16384\n\t"
  "v_mfma_f32_16x16x32_bf16 v[52:55], a[236:239], v[76:79], v[52:55]\n\t"
  "s_add_u32 s44, %[sH2ld], 8192\n\t"
  "v_mfma_f32_16x16x32_bf16 v[56:59], a[228:231], v[80:83], v[56:59]\n\t"
  "v_mfma_f32_16x16x32_bf16 v[60:63], a[232:235], v[80:83], v[60:63]\n\t"
  "buffer_load_dwordx4 a[88:91], v1, %[srd], s44 offen offset:0 sc1\n\t"
  "v_mfma_f32_16x16x32_bf16 v[64:67], a[236:239], v[80:83], v[64:67]\n\t"
  "ds_read_b128 a[228:231], v141 offset:17408\n\t"
  "ds_read_b128 a[232:235], v141 offset:18432\n\t"
  "v_mfma_f32_16x16x16_bf16 v[44:47], a[240:241], v[104:105], v[44:47]\n\t"
  "s_add_u32 s57, %[sFL], 8192\n\t"
  "buffer_load_dwordx2 v[138:139], v137, %[srd], s57 offen sc1\n\t"
  "v_mfma_f32_16x16x16_bf16 v[48:51], a[242:243], v[104:105], v[48:51]\n\t"
  "buffer_load_dwordx4 a[92:95], v1, %[srd], s44 offen offset:1024 sc1\n\t"
  "ds_read_b128 a[236:239], v141 offset:19456\n\t"
  "v_mfma_f32_16x16x16_bf16 v[52:55], a[244:245], v[104:105], v[52:55]\n\t"
  "buffer_load_dwordx4 a[96:99], v1, %[srd], s44 offen offset:2048 sc1\n\t"
  "v_mfma_f32_16x16x16_bf16 v[56:59], a[240:241], v[106:107], v[56:59]\n\t"
  "v_mfma_f32_16x16x16_bf16 v[60:63], a[242:243], v[106:107], v[60:63]\n\t"
  "buffer_load_dwordx4 a[100:103], v1, %[srd], s44 offen offset:3072 sc1\n\t"
  "v_mfma_f32_16x16x16_bf16 v[64:67], a[244:245], v[106:107], v[64:67]\n\t"
  "s_movk_i32 s56, 16384\n\t"
  "s_waitcnt vmcnt(15)\n\t"
  "buffer_store_dwordx4 v[132:135], v9, %[srd], s56 offen sc1\n\t"
  "v_max_f32 v44, 0, v44\n\t"
  "v_max_f32 v45, 0, v45\n\t"
  "v_pk_mul_f32 v[46:47], v[46:47], v[128:129] op_sel:[0,0] op_sel_hi:[1,0]\n\t"
  "v_pk_mul_f32 v[44:45], v[44:45], v[46:47]\n\t"
  "v_cvt_pk_bf16_f32 v116, v44, v45\n\t"
  "v_cvt_pk_bf16_f32 v112, v44, v45\n\t"
  "v_max_f32 v48, 0, v48\n\t"
  "v_max_f32 v49, 0, v49\n\t"
  "v_pk_mul_f32 v[50:51], v[50:51], v[128:129] op_sel:[0,0] op_sel_hi:[1,0]\n\t"
  "v_pk_mul_f32 v[48:49], v[48:49], v[50:51]\n\t"
  "v_cvt_pk_bf16_f32 v117, v48, v49\n\t"
  "v_cvt_pk_bf16_f32 v113, v48, v49\n\t"
  "v_max_f32 v52, 0, v52\n\t"
  "v_max_f32 v53, 0, v53\n\t"
  "v_pk_mul_f32 v[54:55], v[54:55], v[128:129] op_sel:[0,0] op_sel_hi:[1,0]\n\t"
  "v_permlane16_swap_b32 v116, v117\n\t"
  "v_pk_mul_f32 v[52:53], v[52:53], v[54:55]\n\t"
  "v_cvt_pk_bf16_f32 v118, v52, v53\n\t"
  "v_cvt_pk_bf16_f32 v114, v52, v53\n\t"
  "s_mov_b32 s55, 3244032\n\t"
  "v_permlane16_swap_b32 v118, v115\n\t"
  "s_nop 1\n\t"
  "v_permlane32_swap_b32 v117, v115\n\t"
  "buffer_store_dwordx4 v[112:115], v8, %[srd], s55 offen sc1\n\t"
  "v_max_f32 v56, 0, v56\n\t"
  "v_max_f32 v57, 0, v57\n\t"
  "v_pk_mul_f32 v[58:59], v[58:59], v[128:129] op_sel:[0,1] op_sel_hi:[1,1]\n\t"
  "v_pk_mul_f32 v[56:57], v[56:57], v[58:59]\n\t"
  "v_cvt_pk_bf16_f32 v124, v56, v57\n\t"
  "v_cvt_pk_bf16_f32 v120, v56, v57\n\t"
  "v_max_f32 v60, 0, v60\n\t"
  "v_max_f32 v61, 0, v61\n\t"
  "v_pk_mul_f32 v[62:63], v[62:63], v[128:129] op_sel:[0,1] op_sel_hi:[1,1]\n\t"
  "v_pk_mul_f32 v[60:61], v[60:61], v[62:63]\n\t"
  "v_cvt_pk_bf16_f32 v125, v60, v61\n\t"
  "v_cvt_pk_bf16_f32 v121, v60, v61\n\t"
  "v_max_f32 v64, 0, v64\n\t"
  "v_max_f32 v65, 0, v65\n\t"
  "v_pk_mul_f32 v[66:67], v[66:67], v[128:129] op_sel:[0,1] op_sel_hi:[1,1]\n\t"
  "v_permlane16_swap_b32 v124, v125\n\t"
  "v_pk_mul_f32 v[64:65], v[64:65], v[66:67]\n\t"
  "v_cvt_pk_bf16_f32 v126, v64, v65\n\t"
  "v_cvt_pk_bf16_f32 v122, v64, v65\n\t"
  "s_mov_b32 s55, 3293184\n\t"
  "v_permlane16_swap_b32 v126, v123\n\t"
  "s_nop 1\n\t"
  "v_permlane32_swap_b32 v125, v123\n\t"
  "buffer_store_dwordx4 v[120:123], v8, %[srd], s55 offen sc1\n\t"
  "s_waitcnt lgkmcnt(0)\n\t"
  "s_barrier\n\t"
  "s_waitcnt vmcnt(15)\n\t"
  "v_mfma_f32_16x16x32_bf16 v[20:23], v[144:147], a[56:59], 0\n\t"
  "v_lshrrev_b32 v63, 3, v1\n\t"
  "v_and_b32 v63, 0x60, v63\n\t"
  "v_mfma_f32_16x16x32_bf16 v[24:27], v[148:151], a[56:59], 0\n\t"
  "v_lshrrev_b32 v62, 1, v63\n\t"
  "buffer_load_dwordx4 v[68:71], v63, %[srd], %[sRT] offen offset:0 sc1\n\t"
  "v_mfma_f32_16x16x32_bf16 v[28:31], v[152:155], a[56:59], 0\n\t"
  "buffer_load_dwordx4 v[72:75], v63, %[srd], %[sRT] offen offset:16 sc1\n\t"
  "buffer_load_dwordx4 v[76:79], v63, %[srd], %[sRT] offen offset:128 sc1\n\t"
  "v_mfma_f32_16x16x32_bf16 v[32:35], v[156:159], a[56:59], 0\n\t"
  "buffer_load_dwordx4 v[80:83], v63, %[srd], %[sRT] offen offset:144 sc1\n\t"
  "buffer_load_dwordx4 v[84:87], v62, %[srd], %[sRT] offen offset:256 sc1\n\t"
  "v_mfma_f32_16x16x32_bf16 v[36:39], v[160:163], a[56:59], 0\n\t"
  "buffer_load_dwordx4 v[64:67], v17, %[srd], %[sRT] offen offset:0 sc1\n\t"
  "buffer_load_dwordx4 v[108:111], v17, %[srd], %[sRT] offen offset:16 sc1\n\t"
  "s_waitcnt vmcnt(21)\n\t"
  "v_mfma_f32_16x16x32_bf16 v[20:23], v[164:167], a[60:63], v[20:23]\n\t"
  "s_mul_i32 s48, %[w], -15360\n\t"
  "s_add_u32 s48, s48, 81920\n\t"
  "v_mfma_f32_16x16x32_bf16 v[24:27], v[168:171], a[60:63], v[24:27]\n\t"
  "v_add_u32 v62, s48, v13\n\t"
  "v_add_u32 v60, 61440, v14\n\t"
  "v_mfma_f32_16x16x32_bf16 v[28:31], v[172:175], a[60:63], v[28:31]\n\t"
  "v_add_u32 v61, 61440, v15\n\t"
  "v_accvgpr_read_b32 v89, a246\n\t"
  "v_mfma_f32_16x16x32_bf16 v[32:35], v[176:179], a[60:63], v[32:35]\n\t"
  "v_lshlrev_b32 v88, 16, v89\n\t"
  "v_and_b32 v89, 0xffff0000, v89\n\t"
  "v_mfma_f32_16x16x32_bf16 v[36:39], v[180:183], a[60:63], v[36:39]\n\t"
  "v_accvgpr_read_b32 v91, a247\n\t"
  "v_lshlrev_b32 v90, 16, v91\n\t"
  "s_waitcnt vmcnt(20)\n\t"
  "v_mfma_f32_16x16x32_bf16 v[20:23], v[184:187], a[64:67], v[20:23]\n\t"
  "v_and_b32 v91, 0xffff0000, v91\n\t"
  "v_accvgpr_read_b32 v93, a248\n\t"
  "v_mfma_f32_16x16x32_bf16 v[24:27], v[188:191], a[64:67], v[24:27]\n\t"
  "v_lshlrev_b32 v92, 16, v93\n\t"
  "v_and_b32 v93, 0xffff0000, v93\n\t"
  "v_mfma_f32_16x16x32_bf16 v[28:31], v[192:195], a[64:67], v[28:31]\n\t"
  "v_accvgpr_read_b32 v95, a249\n\t"
  "v_lshlrev_b32 v94, 16, v95\n\t"
  "v_mfma_f32_16x16x32_bf16 v[32:35], v[196:199], a[64:67], v[32:35]\n\t"
  "v_and_b32 v95, 0xffff0000, v95\n\t"
  "v_accvgpr_read_b32 v97, a250\n\t"
  "v_mfma_f32_16x16x32_bf16 v[36:39], v[200:203], a[64:67], v[36:39]\n\t"
  "s_waitcnt vmcnt(13)\n\t"
  "v_cmp_ne_u64 vcc, v[132:133], v[138:139]\n\t"
  "v_mfma_f32_16x16x32_bf16 v[20:23], v[204:207], a[68:71], v[20:23]\n\t"
  "v_lshlrev_b32 v96, 16, v97\n\t"
  "v_and_b32 v97, 0xffff0000, v97\n\t"
  "v_mfma_f32_16x16x32_bf16 v[24:27], v[208:211], a[68:71], v[24:27]\n\t"
  "s_cbranch_vccnz Lfl1_slow%=\n\t"
  "Lfl1_back%=:\n\t"
  "v_mfma_f32_16x16x32_bf16 v[28:31], v[212:215], a[68:71], v[28:31]\n\t"
  "v_accvgpr_read_b32 v99, a251\n\t"
  "v_lshlrev_b32 v98, 16, v99\n\t"
  "v_mfma_f32_16x16x32_bf16 v[32:35], v[216:219], a[68:71], v[32:35]\n\t"
  "s_add_u32 s45, %[sH2ld], 98304\n\t"
  "v_and_b32 v99, 0xffff0000, v99\n\t"
  "v_mfma_f32_16x16x32_bf16 v[36:39], v[220:223], a[68:71], v[36:39]\n\t"
  "v_accvgpr_read_b32 v101, a252\n\t"
  "v_lshlrev_b32 v100, 16, v101\n\t"
  "v_mfma_f32_16x16x32_bf16 v[20:23], v[224:227], a[72:75], v[20:23]\n\t"
  "v_and_b32 v101, 0xffff0000, v101\n\t"
  "v_accvgpr_read_b32 v103, a253\n\t"
  "v_mfma_f32_16x16x32_bf16 v[24:27], v[228:231], a[72:75], v[24:27]\n\t"
  "buffer_load_dwordx4 a[104:107], v1, %[srd], s45 offen offset:0 sc1\n\t"
  "v_lshlrev_b32 v102, 16, v103\n\t"
  "v_mfma_f32_16x16x32_bf16 v[28:31], v[232:235], a[72:75], v[28:31]\n\t"
  "v_and_b32 v103, 0xffff0000, v103\n\t"
  "v_accvgpr_read_b32 v105, a254\n\t"
  "v_mfma_f32_16x16x32_bf16 v[32:35], v[236:239], a[72:75], v[32:35]\n\t"
  "v_lshlrev_b32 v104, 16, v105\n\t"
  "v_and_b32 v105, 0xffff0000, v105\n\t"
  "v_mfma_f32_16x16x32_bf16 v[36:39], v[240:243], a[72:75], v[36:39]\n\t"
  "buffer_load_dwordx4 a[108:111], v1, %[srd], s45 offen offset:1024 sc1\n\t"
  "v_accvgpr_read_b32 v107, a255\n\t"
  "v_mfma_f32_16x16x32_bf16 v[20:23], v[244:247], a[76:79], v[20:23]\n\t"
  "v_lshlrev_b32 v106, 16, v107\n\t"
  "v_and_b32 v107, 0xffff0000, v107\n\t"
  "v_mfma_f32_16x16x32_bf16 v[24:27], a[160:163], a[76:79], v[24:27]\n\t"
  "s_movk_i32 s56, 24576\n\t"
  "s_waitcnt vmcnt(9)\n\t"
  "v_mfma_f32_16x16x32_bf16 v[28:31], a[164:167], a[76:79], v[28:31]\n\t"
  "buffer_store_dwordx4 v[132:135], v9, %[srd], s56 offen sc1\n\t"
  "buffer_load_dwordx4 a[112:115], v1, %[srd], s45 offen offset:2048 sc1\n\t"
  "v_mfma_f32_16x16x32_bf16 v[32:35], a[168:171], a[76:79], v[32:35]\n\t"
  "v_mfma_f32_16x16x32_bf16 v[36:39], a[172:175], a[76:79], v[36:39]\n\t"
  "v_mfma_f32_16x16x32_bf16 v[20:23], a[176:179], a[80:83], v[20:23]\n\t"
  "buffer_load_dwordx4 a[116:119], v1, %[srd], s45 offen offset:3072 sc1\n\t"
  "s_add_u32 s57, %[sFL], 16384\n\t"
  "v_mfma_f32_16x16x32_bf16 v[24:27], a[180:183], a[80:83], v[24:27]\n\t"
  "buffer_load_dwordx2 v[138:139], v137, %[srd], s57 offen sc1\n\t"
  "v_mfma_f32_16x16x32_bf16 v[28:31], a[184:187], a[80:83], v[28:31]\n\t"
  "v_mfma_f32_16x16x32_bf16 v[32:35], a[188:191], a[80:83], v[32:35]\n\t"
  "v_mfma_f32_16x16x32_bf16 v[36:39], a[192:195], a[80:83], v[36:39]\n\t"
  "s_add_u32 s45, %[sH2ld], 102400\n\t"
  "v_mfma_f32_16x16x32_bf16 v[20:23], a[196:199], a[84:87], v[20:23]\n\t"
  "v_mfma_f32_16x16x32_bf16 v[24:27], a[200:203], a[84:87], v[24:27]\n\t"
  "v_mfma_f32_16x16x32_bf16 v[28:31], a[204:207], a[84:87], v[28:31]\n\t"
  "buffer_load_dwordx4 a[120:123], v1, %[srd], s45 offen offset:0 sc1\n\t"
  "v_mfma_f32_16x16x32_bf16 v[32:35], a[208:211], a[84:87], v[32:35]\n\t"
  "v_mfma_f32_16x16x32_bf16 v[36:39], a[212:215], a[84:87], v[36:39]\n\t"
  "v_mfma_f32_16x16x32_bf16 v[20:23], a[216:219], a[88:91], v[20:23]\n\t"
  "buffer_load_dwordx4 a[124:127], v1, %[srd], s45 offen offset:1024 sc1\n\t"
  "v_mfma_f32_16x16x32_bf16 v[24:27], a[220:223], a[88:91], v[24:27]\n\t"
  "s_waitcnt vmcnt(14)\n\t"
  "v_mul_f32 v88, v88, v68\n\t"
  "v_mfma_f32_16x16x32_bf16 v[28:31], a[224:227], a[88:91], v[28:31]\n\t"
  "v_mul_f32 v89, v89, v69\n\t"
  "v_mul_f32 v90, v90, v70\n\t"
  "v_mfma_f32_16x16x32_bf16 v[32:35], a[228:231], a[88:91], v[32:35]\n\t"
  "buffer_load_dwordx4 a[128:131], v1, %[srd], s45 offen offset:2048 sc1\n\t"
  "v_mul_f32 v91, v91, v71\n\t"
  "v_mfma_f32_16x16x32_bf16 v[36:39], a[232:235], a[88:91], v[36:39]\n\t"
  "ds_write_b128 v62, v[88:91] offset:0\n\t"
  "s_waitcnt vmcnt(14)\n\t"
  "v_mul_f32 v92, v92, v72\n\t"
  "v_mfma_f32_16x16x32_bf16 v[20:23], a[236:239], a[92:95], v[20:23]\n\t"
  "v_mul_f32 v93, v93, v73\n\t"
  "v_mul_f32 v94, v94, v74\n\t"
  "v_mfma_f32_16x16x32_bf16 v[24:27], a[0:3], a[92:95], v[24:27]\n\t"
  "buffer_load_dwordx4 a[132:135], v1, %[srd], s45 offen offset:3072 sc1\n\t"
  "v_mul_f32 v95, v95, v75\n\t"
  "v_mfma_f32_16x16x32_bf16 v[28:31], a[4:7], a[92:95], v[28:31]\n\t"
  "ds_write_b128 v62, v[92:95] offset:1024\n\t"
  "s_waitcnt vmcnt(14)\n\t"
  "v_mul_f32 v96, v96, v76\n\t"
  "v_mfma_f32_16x16x32_bf16 v[32:35], a[8:11], a[92:95], v[32:35]\n\t"
  "v_mul_f32 v97, v97, v77\n\t"
  "v_mul_f32 v98, v98, v78\n\t"
  "v_mfma_f32_16x16x32_bf16 v[36:39], a[12:15], a[92:95], v[36:39]\n\t"
  "v_mul_f32 v99, v99, v79\n\t"
  "ds_write_b128 v62, v[96:99] offset:2048\n\t"
  "v_mfma_f32_16x16x32_bf16 v[20:23], a[16:19], a[96:99], v[20:23]\n\t"
  "s_add_u32 s45, %[sH2ld], 106496\n\t"
  "s_waitcnt vmcnt(13)\n\t"
  "v_mul_f32 v100, v100, v80\n\t"
  "v_mfma_f32_16x16x32_bf16 v[24:27], a[20:23], a[96:99], v[24:27]\n\t"
  "v_mul_f32 v101, v101, v81\n\t"
  "v_mul_f32 v102, v102, v82\n\t"
  "v_mfma_f32_16x16x32_bf16 v[28:31], a[24:27], a[96:99], v[28:31]\n\t"
  "v_mul_f32 v103, v103, v83\n\t"
  "ds_write_b128 v62, v[100:103] offset:3072\n\t"
  "v_mfma_f32_16x16x32_bf16 v[32:35], a[28:31], a[96:99], v[32:35]\n\t"
  "buffer_load_dwordx4 a[136:139], v1, %[srd], s45 offen offset:0 sc1\n\t"
  "s_waitcnt vmcnt(13)\n\t"
  "v_mul_f32 v104, v104, v84\n\t"
  "v_mfma_f32_16x16x32_bf16 v[36:39], a[32:35], a[96:99], v[36:39]\n\t"
  "v_mul_f32 v105, v105, v85\n\t"
  "v_mul_f32 v106, v106, v86\n\t"
  "v_mfma_f32_16x16x32_bf16 v[20:23], a[36:39], a[100:103], v[20:23]\n\t"
  "v_mul_f32 v107, v107, v87\n\t"
  "ds_write_b128 v62, v[104:107] offset:4096\n\t"
  "v_mfma_f32_16x16x32_bf16 v[24:27], a[40:43], a[100:103], v[24:27]\n\t"
  "buffer_load_dwordx4 a[140:143], v1, %[srd], s45 offen offset:1024 sc1\n\t"
  "v_mfma_f32_16x16x32_bf16 v[28:31], a[44:47], a[100:103], v[28:31]\n\t"
  "v_mfma_f32_16x16x32_bf16 v[32:35], a[48:51], a[100:103], v[32:35]\n\t"
  "v_mfma_f32_16x16x32_bf16 v[36:39], a[52:55], a[100:103], v[36:39]\n\t"
  "s_waitcnt vmcnt(10)\n\t"
  "buffer_load_dwordx4 a[144:147], v1, %[srd], s45 offen offset:2048 sc1\n\t"
  "v_mfma_f32_16x16x32_bf16 v[40:43], v[144:147], a[104:107], 0\n\t"
  "v_mfma_f32_16x16x32_bf16 v[44:47], v[148:151], a[104:107], 0\n\t"
  "v_mfma_f32_16x16x32_bf16 v[48:51], v[152:155], a[104:107], 0\n\t"
  "buffer_load_dwordx4 a[148:151], v1, %[srd], s45 offen offset:3072 sc1\n\t"
  "v_mfma_f32_16x16x32_bf16 v[52:55], v[156:159], a[104:107], 0\n\t"
  "ds_write_b128 v13, v[20:23] offset:0\n\t"
  "v_mfma_f32_16x16x32_bf16 v[56:59], v[160:163], a[104:107], 0\n\t"
  "v_mfma_f32_16x16x32_bf16 v[40:43], v[164:167], a[108:111], v[40:43]\n\t"
  "v_mfma_f32_16x16x32_bf16 v[44:47], v[168:171], a[108:111], v[44:47]\n\t"
  "ds_write_b128 v13, v[24:27] offset:1024\n\t"
  "v_mfma_f32_16x16x32_bf16 v[48:51], v[172:175], a[108:111], v[48:51]\n\t"
  "v_mfma_f32_16x16x32_bf16 v[52:55], v[176:179], a[108:111], v[52:55]\n\t"
  "v_mfma_f32_16x16x32_bf16 v[56:59], v[180:183], a[108:111], v[56:59]\n\t"
  "s_waitcnt vmcnt(9)\n\t"
  "ds_write_b128 v13, v[28:31] offset:2048\n\t"
  "v_mfma_f32_16x16x32_bf16 v[40:43], v[184:187], a[112:115], v[40:43]\n\t"
  "v_mfma_f32_16x16x32_bf16 v[44:47], v[188:191], a[112:115], v[44:47]\n\t"
  "v_mfma_f32_16x16x32_bf16 v[48:51], v[192:195], a[112:115], v[48:51]\n\t"
  "ds_write_b128 v13, v[32:35] offset:3072\n\t"
  "v_mfma_f32_16x16x32_bf16 v[52:55], v[196:199], a[112:115], v[52:55]\n\t"
  "v_mfma_f32_16x16x32_bf16 v[56:59], v[200:203], a[112:115], v[56:59]\n\t"
  "v_mfma_f32_16x16x32_bf16 v[40:43], v[204:207], a[116:119], v[40:43]\n\t"
  "ds_write_b128 v13, v[36:39] offset:4096\n\t"
  "v_mfma_f32_16x16x32_bf16 v[44:47], v[208:211], a[116:119], v[44:47]\n\t"
  "v_mfma_f32_16x16x32_bf16 v[48:51], v[212:215], a[116:119], v[48:51]\n\t"
  "v_mfma_f32_16x16x32_bf16 v[52:55], v[216:219], a[116:119], v[52:55]\n\t"
  "v_mfma_f32_16x16x32_bf16 v[56:59], v[220:223], a[116:119], v[56:59]\n\t"
  "s_waitcnt vmcnt(6)\n\t"
  "v_mfma_f32_16x16x32_bf16 v[40:43], v[224:227], a[120:123], v[40:43]\n\t"
  "v_mfma_f32_16x16x32_bf16 v[44:47], v[228:231], a[120:123], v[44:47]\n\t"
  "v_cmp_ne_u64 vcc, v[132:133], v[138:139]\n\t"
  "v_mfma_f32_16x16x32_bf16 v[48:51], v[232:235], a[120:123], v[48:51]\n\t"
  "v_mfma_f32_16x16x32_bf16 v[52:55], v[236:239], a[120:123], v[52:55]\n\t"
  "s_cbranch_vccnz Lfl2_slow%=\n\t"
  "Lfl2_back%=:\n\t"
  "v_mfma_f32_16x16x32_bf16 v[56:59], v[240:243], a[120:123], v[56:59]\n\t"
  "s_add_u32 s44, %[sH2ld], 3145728\n\t"
  "s_waitcnt lgkmcnt(0)\n\t"
  "s_barrier\n\t"
  "v_mfma_f32_16x16x32_bf16 v[40:43], v[244:247], a[124:127], v[40:43]\n\t"
  "ds_read_b128 v[68:71], v14 offset:0\n\t"
  "ds_read_b128 v[72:75], v15 offset:0\n\t"
  "v_mfma_f32_16x16x32_bf16 v[44:47], a[160:163], a[124:127], v[44:47]\n\t"
  "ds_read_b128 v[76:79], v14 offset:20480\n\t"
  "ds_read_b128 v[80:83], v15 offset:20480\n\t"
  "v_mfma_f32_16x16x32_bf16 v[48:51], a[164:167], a[124:127], v[48:51]\n\t"
  "buffer_load_dwordx4 a[152:155], v1, %[srd], s44 offen offset:0 sc1\n\t"
  "ds_read_b128 v[84:87], v14 offset:40960\n\t"
  "v_mfma_f32_16x16x32_bf16 v[52:55], a[168:171], a[124:127], v[52:55]\n\t"
  "ds_read_b128 v[88:91], v15 offset:40960\n\t"
  "ds_read_b128 v[92:95], v60 offset:0\n\t"
  "v_mfma_f32_16x16x32_bf16 v[56:59], a[172:175], a[124:127], v[56:59]\n\t"
  "s_waitcnt vmcnt(5)\n\t"
  "ds_read_b128 v[96:99], v61 offset:0\n\t"
  "v_mfma_f32_16x16x32_bf16 v[40:43], a[176:179], a[128:131], v[40:43]\n\t"
  "buffer_load_dwordx4 a[156:159], v1, %[srd], s44 offen offset:1024 sc1\n\t"
  "s_add_u32 s57, %[sFL], 24576\n\t"
  "v_mfma_f32_16x16x32_bf16 v[44:47], a[180:183], a[128:131], v[44:47]\n\t"
  "buffer_load_dwordx2 v[138:139], v137, %[srd], s57 offen sc1\n\t"
  "ds_read_b128 v[100:103], v60 offset:20480\n\t"
  "v_mfma_f32_16x16x32_bf16 v[48:51], a[184:187], a[128:131], v[48:51]\n\t"
  "ds_read_b128 v[104:107], v61 offset:20480\n\t"
  "v_mfma_f32_16x16x32_bf16 v[52:55], a[188:191], a[128:131], v[52:55]\n\t"
  "buffer_load_dwordx4 v[248:251], v1, %[srd], s44 offen offset:2048 sc1\n\t"
  "v_mfma_f32_16x16x32_bf16 v[56:59], a[192:195], a[128:131], v[56:59]\n\t"
  "v_mfma_f32_16x16x32_bf16 v[40:43], a[196:199], a[132:135], v[40:43]\n\t"
  "v_mfma_f32_16x16x32_bf16 v[44:47], a[200:203], a[132:135], v[44:47]\n\t"
  "buffer_load_dwordx4 v[252:255], v1, %[srd], s44 offen offset:3072 sc1\n\t"
  "v_mfma_f32_16x16x32_bf16 v[48:51], a[204:207], a[132:135], v[48:51]\n\t"
  "v_mfma_f32_16x16x32_bf16 v[52:55], a[208:211], a[132:135], v[52:55]\n\t"
  "v_mfma_f32_16x16x32_bf16 v[56:59], a[212:215], a[132:135], v[56:59]\n\t"
  "s_waitcnt vmcnt(7)\n\t"
  "v_mfma_f32_16x16x32_bf16 v[40:43], a[216:219], a[136:139], v[40:43]\n\t"
  "s_add_u32 s44, %[sH2ld], 3149824\n\t"
  "s_waitcnt lgkmcnt(0)\n\t"
  "v_mfma_f32_16x16x32_bf16 v[44:47], a[220:223], a[136:139], v[44:47]\n\t"
  "v_add_f32 v68, v68, v76\n\t"
  "v_add_f32 v69, v69, v77\n\t"
  "v_mfma_f32_16x16x32_bf16 v[48:51], a[224:227], a[136:139], v[48:51]\n\t"
  "v_add_f32 v70, v70, v78\n\t"
  "v_add_f32 v71, v71, v79\n\t"
  "v_mfma_f32_16x16x32_bf16 v[52:55], a[228:231], a[136:139], v[52:55]\n\t"
  "buffer_load_dwordx4 v[112:115], v1, %[srd], s44 offen offset:0 sc1\n\t"
  "v_add_f32 v72, v72, v80\n\t"
  "v_mfma_f32_16x16x32_bf16 v[56:59], a[232:235], a[136:139], v[56:59]\n\t"
  "v_add_f32 v73, v73, v81\n\t"
  "v_add_f32 v74, v74, v82\n\t"
  "v_mfma_f32_16x16x32_bf16 v[40:43], a[236:239], a[140:143], v[40:43]\n\t"
  "v_add_f32 v75, v75, v83\n\t"
  "v_add_f32 v84, v84, v92\n\t"
  "v_mfma_f32_16x16x32_bf16 v[44:47], a[0:3], a[140:143], v[44:47]\n\t"
  "buffer_load_dwordx4 v[116:119], v1, %[srd], s44 offen offset:1024 sc1\n\t"
  "v_add_f32 v85, v85, v93\n\t"
  "v_mfma_f32_16x16x32_bf16 v[48:51], a[4:7], a[140:143], v[48:51]\n\t"
  "v_add_f32 v86, v86, v94\n\t"
  "v_add_f32 v87, v87, v95\n\t"
  "v_mfma_f32_16x16x32_bf16 v[52:55], a[8:11], a[140:143], v[52:55]\n\t"
  "v_add_f32 v88, v88, v96\n\t"
  "v_add_f32 v89, v89, v97\n\t"
  "v_mfma_f32_16x16x32_bf16 v[56:59], a[12:15], a[140:143], v[56:59]\n\t"
  "s_waitcnt vmcnt(7)\n\t"
  "buffer_load_dwordx4 v[120:123], v1, %[srd], s44 offen offset:2048 sc1\n\t"
  "v_mfma_f32_16x16x32_bf16 v[40:43], a[16:19], a[144:147], v[40:43]\n\t"
  "v_add_f32 v90, v90, v98\n\t"
  "v_add_f32 v91, v91, v99\n\t"
  "v_mfma_f32_16x16x32_bf16 v[44:47], a[20:23], a[144:147], v[44:47]\n\t"
  "v_add_f32 v68, v68, v84\n\t"
  "v_add_f32 v69, v69, v85\n\t"
  "v_mfma_f32_16x16x32_bf16 v[48:51], a[24:27], a[144:147], v[48:51]\n\t"
  "buffer_load_dwordx4 v[124:127], v1, %[srd], s44 offen offset:3072 sc1\n\t"
  "v_add_f32 v70, v70, v86\n\t"
  "v_mfma_f32_16x16x32_bf16 v[52:55], a[28:31], a[144:147], v[52:55]\n\t"
  "v_add_f32 v71, v71, v87\n\t"
  "v_add_f32 v72, v72, v88\n\t"
  "v_mfma_f32_16x16x32_bf16 v[56:59], a[32:35], a[144:147], v[56:59]\n\t"
  "v_add_f32 v73, v73, v89\n\t"
  "v_add_f32 v74, v74, v90\n\t"
  "v_mfma_f32_16x16x32_bf16 v[40:43], a[36:39], a[148:151], v[40:43]\n\t"
  "v_add_f32 v75, v75, v91\n\t"
  "v_fma_f32 v68, v68, v64, v100\n\t"
  "v_mfma_f32_16x16x32_bf16 v[44:47], a[40:43], a[148:151], v[44:47]\n\t"
  "s_add_u32 s44, %[sH2ld], 3153920\n\t"
  "v_fma_f32 v69, v69, v65, v101\n\t"
  "v_mfma_f32_16x16x32_bf16 v[48:51], a[44:47], a[148:151], v[48:51]\n\t"
  "v_fma_f32 v70, v70, v66, v102\n\t"
  "v_fma_f32 v71, v71, v67, v103\n\t"
  "v_mfma_f32_16x16x32_bf16 v[52:55], a[48:51], a[148:151], v[52:55]\n\t"
  "v_fma_f32 v72, v72, v108, v104\n\t"
  "v_fma_f32 v73, v73, v109, v105\n\t"
  "v_mfma_f32_16x16x32_bf16 v[56:59], a[52:55], a[148:151], v[56:59]\n\t"
  "s_waitcnt vmcnt(7)\n\t"
  "buffer_load_dwordx4 v[128:131], v1, %[srd], s44 offen offset:0 sc1\n\t"
  "v_mfma_f32_16x16x32_bf16 v[20:23], v[144:147], a[152:155], 0\n\t"
  "v_fma_f32 v74, v74, v110, v106\n\t"
  "v_fma_f32 v75, v75, v111, v107\n\t"
  "v_mfma_f32_16x16x32_bf16 v[24:27], v[148:151], a[152:155], 0\n\t"
  "v_cvt_pk_bf16_f32 v76, v68, v69\n\t"
  "v_cvt_pk_bf16_f32 v77, v70, v71\n\t"
  "v_mfma_f32_16x16x32_bf16 v[28:31], v[152:155], a[152:155], 0\n\t"
  "buffer_load_dwordx4 a[240:243], v1, %[srd], s44 offen offset:1024 sc1\n\t"
  "v_cvt_pk_bf16_f32 v78, v72, v73\n\t"
  "v_mfma_f32_16x16x32_bf16 v[32:35], v[156:159], a[152:155], 0\n\t"
  "v_cvt_pk_bf16_f32 v79, v74, v75\n\t"
  "s_mov_b32 s55, 0\n\t"
  "v_mfma_f32_16x16x32_bf16 v[36:39], v[160:163], a[152:155], 0\n\t"
  "buffer_store_dwordx4 v[76:79], v16, %[srdo], s55 offen\n\t"
  "ds_write_b128 v13, v[40:43] offset:5120\n\t"
  "v_mfma_f32_16x16x32_bf16 v[20:23], v[164:167], a[156:159], v[20:23]\n\t"
  "buffer_load_dwordx4 a[56:59], v1, %[srd], s44 offen offset:2048 sc1\n\t"
  "v_mfma_f32_16x16x32_bf16 v[24:27], v[168:171], a[156:159], v[24:27]\n\t"
  "v_mfma_f32_16x16x32_bf16 v[28:31], v[172:175], a[156:159], v[28:31]\n\t"
  "ds_write_b128 v13, v[44:47] offset:6144\n\t"
  "v_mfma_f32_16x16x32_bf16 v[32:35], v[176:179], a[156:159], v[32:35]\n\t"
  "buffer_load_dwordx4 a[60:63], v1, %[srd], s44 offen offset:3072 sc1\n\t"
  "v_mfma_f32_16x16x32_bf16 v[36:39], v[180:183], a[156:159], v[36:39]\n\t"
  "s_waitcnt vmcnt(9)\n\t"
  "v_mfma_f32_16x16x32_bf16 v[20:23], v[184:187], v[248:251], v[20:23]\n\t"
  "ds_write_b128 v13, v[48:51] offset:7168\n\t"
  "v_mfma_f32_16x16x32_bf16 v[24:27], v[188:191], v[248:251], v[24:27]\n\t"
  "v_mfma_f32_16x16x32_bf16 v[28:31], v[192:195], v[248:251], v[28:31]\n\t"
  "v_mfma_f32_16x16x32_bf16 v[32:35], v[196:199], v[248:251], v[32:35]\n\t"
  "ds_write_b128 v13, v[52:55] offset:8192\n\t"
  "v_mfma_f32_16x16x32_bf16 v[36:39], v[200:203], v[248:251], v[36:39]\n\t"
  "v_mfma_f32_16x16x32_bf16 v[20:23], v[204:207], v[252:255], v[20:23]\n\t"
  "v_mfma_f32_16x16x32_bf16 v[24:27], v[208:211], v[252:255], v[24:27]\n\t"
  "ds_write_b128 v13, v[56:59] offset:9216\n\t"
  "v_mfma_f32_16x16x32_bf16 v[28:31], v[212:215], v[252:255], v[28:31]\n\t"
  "v_mfma_f32_16x16x32_bf16 v[32:35], v[216:219], v[252:255], v[32:35]\n\t"
  "v_mfma_f32_16x16x32_bf16 v[36:39], v[220:223], v[252:255], v[36:39]\n\t"
  "s_waitcnt vmcnt(7)\n\t"
  "v_mfma_f32_16x16x32_bf16 v[20:23], v[224:227], v[112:115], v[20:23]\n\t"
  "v_mfma_f32_16x16x32_bf16 v[24:27], v[228:231], v[112:115], v[24:27]\n\t"
  "v_cmp_ne_u64 vcc, v[132:133], v[138:139]\n\t"
  "v_mfma_f32_16x16x32_bf16 v[28:31], v[232:235], v[112:115], v[28:31]\n\t"
  "v_mfma_f32_16x16x32_bf16 v[32:35], v[236:239], v[112:115], v[32:35]\n\t"
  "s_cbranch_vccnz Lfl3_slow%=\n\t"
  "Lfl3_back%=:\n\t"
  "v_mfma_f32_16x16x32_bf16 v[36:39], v[240:243], v[112:115], v[36:39]\n\t"
  "s_add_u32 s45, %[sH2ld], 3244032\n\t"
  "s_waitcnt lgkmcnt(0)\n\t"
  "s_barrier\n\t"
  "v_mfma_f32_16x16x32_bf16 v[20:23], v[244:247], v[116:119], v[20:23]\n\t"
  "ds_read_b128 v[68:71], v14 offset:5120\n\t"
  "ds_read_b128 v[72:75], v15 offset:5120\n\t"
  "v_mfma_f32_16x16x32_bf16 v[24:27], a[160:163], v[116:119], v[24:27]\n\t"
  "buffer_load_dwordx4 a[104:107], v1, %[srd], s45 offen offset:0 sc1\n\t"
  "ds_read_b128 v[76:79], v14 offset:25600\n\t"
  "v_mfma_f32_16x16x32_bf16 v[28:31], a[164:167], v[116:119], v[28:31]\n\t"
  "ds_read_b128 v[80:83], v15 offset:25600\n\t"
  "ds_read_b128 v[84:87], v14 offset:46080\n\t"
  "v_mfma_f32_16x16x32_bf16 v[32:35], a[168:171], v[116:119], v[32:35]\n\t"
  "buffer_load_dwordx4 a[108:111], v1, %[srd], s45 offen offset:1024 sc1\n\t"
  "ds_read_b128 v[88:91], v15 offset:46080\n\t"
  "v_mfma_f32_16x16x32_bf16 v[36:39], a[172:175], v[116:119], v[36:39]\n\t"
  "s_waitcnt vmcnt(7)\n\t"
  "ds_read_b128 v[92:95], v60 offset:5120\n\t"
  "v_mfma_f32_16x16x32_bf16 v[20:23], a[176:179], v[120:123], v[20:23]\n\t"
  "ds_read_b128 v[96:99], v61 offset:5120\n\t"
  "ds_read_b128 v[100:103], v60 offset:25600\n\t"
  "v_mfma_f32_16x16x32_bf16 v[24:27], a[180:183], v[120:123], v[24:27]\n\t"
  "buffer_load_dwordx4 a[64:67], v1, %[srd], s45 offen offset:2048 sc1\n\t"
  "ds_read_b128 v[104:107], v61 offset:25600\n\t"
  "v_mfma_f32_16x16x32_bf16 v[28:31], a[184:187], v[120:123], v[28:31]\n\t"
  "v_mfma_f32_16x16x32_bf16 v[32:35], a[188:191], v[120:123], v[32:35]\n\t"
  "buffer_load_dwordx4 a[68:71], v1, %[srd], s45 offen offset:3072 sc1\n\t"
  "v_mfma_f32_16x16x32_bf16 v[36:39], a[192:195], v[120:123], v[36:39]\n\t"
  "v_mfma_f32_16x16x32_bf16 v[20:23], a[196:199], v[124:127], v[20:23]\n\t"
  "v_mfma_f32_16x16x32_bf16 v[24:27], a[200:203], v[124:127], v[24:27]\n\t"
  "s_add_u32 s45, %[sH2ld], 3248128\n\t"
  "v_mfma_f32_16x16x32_bf16 v[28:31], a[204:207], v[124:127], v[28:31]\n\t"
  "v_mfma_f32_16x16x32_bf16 v[32:35], a[208:211], v[124:127], v[32:35]\n\t"
  "buffer_load_dwordx4 a[72:75], v1, %[srd], s45 offen offset:0 sc1\n\t"
  "v_mfma_f32_16x16x32_bf16 v[36:39], a[212:215], v[124:127], v[36:39]\n\t"
  "s_waitcnt vmcnt(8)\n\t"
  "s_waitcnt lgkmcnt(0)\n\t"
  "v_mfma_f32_16x16x32_bf16 v[20:23], a[216:219], v[128:131], v[20:23]\n\t"
  "buffer_load_dwordx4 a[76:79], v1, %[srd], s45 offen offset:1024 sc1\n\t"
  "v_add_f32 v68, v68, v76\n\t"
  "v_mfma_f32_16x16x32_bf16 v[24:27], a[220:223], v[128:131], v[24:27]\n\t"
  "v_add_f32 v69, v69, v77\n\t"
  "v_add_f32 v70, v70, v78\n\t"
  "v_mfma_f32_16x16x32_bf16 v[28:31], a[224:227], v[128:131], v[28:31]\n\t"
  "v_add_f32 v71, v71, v79\n\t"
  "v_add_f32 v72, v72, v80\n\t"
  "v_mfma_f32_16x16x32_bf16 v[32:35], a[228:231], v[128:131], v[32:35]\n\t"
  "buffer_load_dwordx4 a[80:83], v1, %[srd], s45 offen offset:2048 sc1\n\t"
  "v_add_f32 v73, v73, v81\n\t"
  "v_mfma_f32_16x16x32_bf16 v[36:39], a[232:235], v[128:131], v[36:39]\n\t"
  "v_add_f32 v74, v74, v82\n\t"
  "v_add_f32 v75, v75, v83\n\t"
  "v_mfma_f32_16x16x32_bf16 v[20:23], a[236:239], a[240:243], v[20:23]\n\t"
  "buffer_load_dwordx4 a[84:87], v1, %[srd], s45 offen offset:3072 sc1\n\t"
  "v_add_f32 v84, v84, v92\n\t"
  "v_mfma_f32_16x16x32_bf16 v[24:27], a[0:3], a[240:243], v[24:27]\n\t"
  "v_add_f32 v85, v85, v93\n\t"
  "v_add_f32 v86, v86, v94\n\t"
  "v_mfma_f32_16x16x32_bf16 v[28:31], a[4:7], a[240:243], v[28:31]\n\t"
  "v_add_f32 v87, v87, v95\n\t"
  "v_add_f32 v88, v88, v96\n\t"
  "v_mfma_f32_16x16x32_bf16 v[32:35], a[8:11], a[240:243], v[32:35]\n\t"
  "s_add_u32 s45, %[sH2ld], 3252224\n\t"
  "v_add_f32 v89, v89, v97\n\t"
  "v_mfma_f32_16x16x32_bf16 v[36:39], a[12:15], a[240:243], v[36:39]\n\t"
  "s_waitcnt vmcnt(8)\n\t"
  "v_add_f32 v90, v90, v98\n\t"
  "v_mfma_f32_16x16x32_bf16 v[20:23], a[16:19], a[56:59], v[20:23]\n\t"
  "buffer_load_dwordx4 a[88:91], v1, %[srd], s45 offen offset:0 sc1\n\t"
  "v_add_f32 v91, v91, v99\n\t"
  "v_mfma_f32_16x16x32_bf16 v[24:27], a[20:23], a[56:59], v[24:27]\n\t"
  "v_add_f32 v68, v68, v84\n\t"
  "v_add_f32 v69, v69, v85\n\t"
  "v_mfma_f32_16x16x32_bf16 v[28:31], a[24:27], a[56:59], v[28:31]\n\t"
  "buffer_load_dwordx4 a[92:95], v1, %[srd], s45 offen offset:1024 sc1\n\t"
  "v_add_f32 v70, v70, v86\n\t"
  "v_mfma_f32_16x16x32_bf16 v[32:35], a[28:31], a[56:59], v[32:35]\n\t"
  "v_add_f32 v71, v71, v87\n\t"
  "v_add_f32 v72, v72, v88\n\t"
  "v_mfma_f32_16x16x32_bf16 v[36:39], a[32:35], a[56:59], v[36:39]\n\t"
  "v_add_f32 v73, v73, v89\n\t"
  "v_add_f32 v74, v74, v90\n\t"
  "v_mfma_f32_16x16x32_bf16 v[20:23], a[36:39], a[60:63], v[20:23]\n\t"
  "buffer_load_dwordx4 a[96:99], v1, %[srd], s45 offen offset:2048 sc1\n\t"
  "v_add_f32 v75, v75, v91\n\t"
  "v_mfma_f32_16x16x32_bf16 v[24:27], a[40:43], a[60:63], v[24:27]\n\t"
  "v_fma_f32 v68, v68, v64, v100\n\t"
  "v_fma_f32 v69, v69, v65, v101\n\t"
  "v_mfma_f32_16x16x32_bf16 v[28:31], a[44:47], a[60:63], v[28:31]\n\t"
  "buffer_load_dwordx4 a[100:103], v1, %[srd], s45 offen offset:3072 sc1\n\t"
  "v_fma_f32 v70, v70, v66, v102\n\t"
  "v_mfma_f32_16x16x32_bf16 v[32:35], a[48:51], a[60:63], v[32:35]\n\t"
  "v_fma_f32 v71, v71, v67, v103\n\t"
  "v_fma_f32 v72, v72, v108, v104\n\t"
  "v_mfma_f32_16x16x32_bf16 v[36:39], a[52:55], a[60:63], v[36:39]\n\t"
  "s_waitcnt vmcnt(10)\n\t"
  "v_fma_f32 v73, v73, v109, v105\n\t"
  "v_mfma_f32_16x16x32_bf16 v[40:43], v[144:147], a[104:107], 0\n\t"
  "v_fma_f32 v74, v74, v110, v106\n\t"
  "v_fma_f32 v75, v75, v111, v107\n\t"
  "v_mfma_f32_16x16x32_bf16 v[44:47], v[148:151], a[104:107], 0\n\t"
  "v_cvt_pk_bf16_f32 v76, v68, v69\n\t"
  "v_cvt_pk_bf16_f32 v77, v70, v71\n\t"
  "v_mfma_f32_16x16x32_bf16 v[48:51], v[152:155], a[104:107], 0\n\t"
  "v_cvt_pk_bf16_f32 v78, v72, v73\n\t"
  "v_cvt_pk_bf16_f32 v79, v74, v75\n\t"
  "v_mfma_f32_16x16x32_bf16 v[52:55], v[156:159], a[104:107], 0\n\t"
  "s_mov_b32 s55, 40960\n\t"
  "buffer_store_dwordx4 v[76:79], v16, %[srdo], s55 offen\n\t"
  "v_mfma_f32_16x16x32_bf16 v[56:59], v[160:163], a[104:107], 0\n\t"
  "ds_write_b128 v13, v[20:23] offset:10240\n\t"
  "v_mfma_f32_16x16x32_bf16 v[40:43], v[164:167], a[108:111], v[40:43]\n\t"
  "v_mfma_f32_16x16x32_bf16 v[44:47], v[168:171], a[108:111], v[44:47]\n\t"
  "v_mfma_f32_16x16x32_bf16 v[48:51], v[172:175], a[108:111], v[48:51]\n\t"
  "ds_write_b128 v13, v[24:27] offset:11264\n\t"
  "v_mfma_f32_16x16x32_bf16 v[52:55], v[176:179], a[108:111], v[52:55]\n\t"
  "v_mfma_f32_16x16x32_bf16 v[56:59], v[180:183], a[108:111], v[56:59]\n\t"
  "s_waitcnt vmcnt(9)\n\t"
  "v_mfma_f32_16x16x32_bf16 v[40:43], v[184:187], a[64:67], v[40:43]\n\t"
  "ds_write_b128 v13, v[28:31] offset:12288\n\t"
  "v_mfma_f32_16x16x32_bf16 v[44:47], v[188:191], a[64:67], v[44:47]\n\t"
  "v_mfma_f32_16x16x32_bf16 v[48:51], v[192:195], a[64:67], v[48:51]\n\t"
  "v_mfma_f32_16x16x32_bf16 v[52:55], v[196:199], a[64:67], v[52:55]\n\t"
  "ds_write_b128 v13, v[32:35] offset:13312\n\t"
  "v_mfma_f32_16x16x32_bf16 v[56:59], v[200:203], a[64:67], v[56:59]\n\t"
  "v_mfma_f32_16x16x32_bf16 v[40:43], v[204:207], a[68:71], v[40:43]\n\t"
  "v_mfma_f32_16x16x32_bf16 v[44:47], v[208:211], a[68:71], v[44:47]\n\t"
  "ds_write_b128 v13, v[36:39] offset:14336\n\t"
  "v_mfma_f32_16x16x32_bf16 v[48:51], v[212:215], a[68:71], v[48:51]\n\t"
  "v_mfma_f32_16x16x32_bf16 v[52:55], v[216:219], a[68:71], v[52:55]\n\t"
  "v_mfma_f32_16x16x32_bf16 v[56:59], v[220:223], a[68:71], v[56:59]\n\t"
  "s_waitcnt vmcnt(7)\n\t"
  "v_mfma_f32_16x16x32_bf16 v[40:43], v[224:227], a[72:75], v[40:43]\n\t"
  "v_mfma_f32_16x16x32_bf16 v[44:47], v[228:231], a[72:75], v[44:47]\n\t"
  "v_mfma_f32_16x16x32_bf16 v[48:51], v[232:235], a[72:75], v[48:51]\n\t"
  "v_mfma_f32_16x16x32_bf16 v[52:55], v[236:239], a[72:75], v[52:55]\n\t"
  "v_mfma_f32_16x16x32_bf16 v[56:59], v[240:243], a[72:75], v[56:59]\n\t"
  "s_waitcnt lgkmcnt(0)\n\t"
  "s_barrier\n\t"
  "v_mfma_f32_16x16x32_bf16 v[40:43], v[244:247], a[76:79], v[40:43]\n\t"
  "ds_read_b128 v[68:71], v14 offset:10240\n\t"
  "ds_read_b128 v[72:75], v15 offset:10240\n\t"
  "v_mfma_f32_16x16x32_bf16 v[44:47], a[160:163], a[76:79], v[44:47]\n\t"
  "ds_read_b128 v[76:79], v14 offset:30720\n\t"
  "ds_read_b128 v[80:83], v15 offset:30720\n\t"
  "v_mfma_f32_16x16x32_bf16 v[48:51], a[164:167], a[76:79], v[48:51]\n\t"
  "ds_read_b128 v[84:87], v14 offset:51200\n\t"
  "ds_read_b128 v[88:91], v15 offset:51200\n\t"
  "v_mfma_f32_16x16x32_bf16 v[52:55], a[168:171], a[76:79], v[52:55]\n\t"
  "ds_read_b128 v[92:95], v60 offset:10240\n\t"
  "ds_read_b128 v[96:99], v61 offset:10240\n\t"
  "v_mfma_f32_16x16x32_bf16 v[56:59], a[172:175], a[76:79], v[56:59]\n\t"
  "s_waitcnt vmcnt(5)\n\t"
  "ds_read_b128 v[100:103], v60 offset:30720\n\t"
  "v_mfma_f32_16x16x32_bf16 v[40:43], a[176:179], a[80:83], v[40:43]\n\t"
  "ds_read_b128 v[104:107], v61 offset:30720\n\t"
  "v_mfma_f32_16x16x32_bf16 v[44:47], a[180:183], a[80:83], v[44:47]\n\t"
  "v_mfma_f32_16x16x32_bf16 v[48:51], a[184:187], a[80:83], v[48:51]\n\t"
  "v_mfma_f32_16x16x32_bf16 v[52:55], a[188:191], a[80:83], v[52:55]\n\t"
  "v_mfma_f32_16x16x32_bf16 v[56:59], a[192:195], a[80:83], v[56:59]\n\t"
  "v_mfma_f32_16x16x32_bf16 v[40:43], a[196:199], a[84:87], v[40:43]\n\t"
  "v_mfma_f32_16x16x32_bf16 v[44:47], a[200:203], a[84:87], v[44:47]\n\t"
  "v_mfma_f32_16x16x32_bf16 v[48:51], a[204:207], a[84:87], v[48:51]\n\t"
  "v_mfma_f32_16x16x32_bf16 v[52:55], a[208:211], a[84:87], v[52:55]\n\t"
  "s_waitcnt lgkmcnt(0)\n\t"
  "v_add_f32 v68, v68, v76\n\t"
  "v_mfma_f32_16x16x32_bf16 v[56:59], a[212:215], a[84:87], v[56:59]\n\t"
  "s_waitcnt vmcnt(3)\n\t"
  "v_add_f32 v69, v69, v77\n\t"
  "v_mfma_f32_16x16x32_bf16 v[40:43], a[216:219], a[88:91], v[40:43]\n\t"
  "v_add_f32 v70, v70, v78\n\t"
  "v_add_f32 v71, v71, v79\n\t"
  "v_mfma_f32_16x16x32_bf16 v[40:43], a[236:239], a[92:95], v[40:43]\n\t"
  "v_add_f32 v72, v72, v80\n\t"
  "v_add_f32 v73, v73, v81\n\t"
  "s_waitcnt vmcnt(2)\n\t"
  "v_mfma_f32_16x16x32_bf16 v[40:43], a[16:19], a[96:99], v[40:43]\n\t"
  "v_add_f32 v74, v74, v82\n\t"
  "v_add_f32 v75, v75, v83\n\t"
  "s_waitcnt vmcnt(1)\n\t"
  "v_mfma_f32_16x16x32_bf16 v[40:43], a[36:39], a[100:103], v[40:43]\n\t"
  "v_add_f32 v84, v84, v92\n\t"
  "v_add_f32 v85, v85, v93\n\t"
  "v_mfma_f32_16x16x32_bf16 v[44:47], a[220:223], a[88:91], v[44:47]\n\t"
  "v_add_f32 v86, v86, v94\n\t"
  "v_add_f32 v87, v87, v95\n\t"
  "v_mfma_f32_16x16x32_bf16 v[44:47], a[0:3], a[92:95], v[44:47]\n\t"
  "v_add_f32 v88, v88, v96\n\t"
  "v_add_f32 v89, v89, v97\n\t"
  "v_mfma_f32_16x16x32_bf16 v[44:47], a[20:23], a[96:99], v[44:47]\n\t"
  "v_add_f32 v90, v90, v98\n\t"
  "v_add_f32 v91, v91, v99\n\t"
  "v_mfma_f32_16x16x32_bf16 v[44:47], a[40:43], a[100:103], v[44:47]\n\t"
  "v_add_f32 v68, v68, v84\n\t"
  "v_add_f32 v69, v69, v85\n\t"
  "v_mfma_f32_16x16x32_bf16 v[48:51], a[224:227], a[88:91], v[48:51]\n\t"
  "v_add_f32 v70, v70, v86\n\t"
  "v_add_f32 v71, v71, v87\n\t"
  "v_mfma_f32_16x16x32_bf16 v[48:51], a[4:7], a[92:95], v[48:51]\n\t"
  "ds_write_b128 v13, v[40:43] offset:15360\n\t"
  "v_add_f32 v72, v72, v88\n\t"
  "v_mfma_f32_16x16x32_bf16 v[48:51], a[24:27], a[96:99], v[48:51]\n\t"
  "v_add_f32 v73, v73, v89\n\t"
  "v_add_f32 v74, v74, v90\n\t"
  "v_mfma_f32_16x16x32_bf16 v[48:51], a[44:47], a[100:103], v[48:51]\n\t"
  "v_add_f32 v75, v75, v91\n\t"
  "v_fma_f32 v68, v68, v64, v100\n\t"
  "v_mfma_f32_16x16x32_bf16 v[52:55], a[228:231], a[88:91], v[52:55]\n\t"
  "v_fma_f32 v69, v69, v65, v101\n\t"
  "v_fma_f32 v70, v70, v66, v102\n\t"
  "v_mfma_f32_16x16x32_bf16 v[52:55], a[8:11], a[92:95], v[52:55]\n\t"
  "ds_write_b128 v13, v[44:47] offset:16384\n\t"
  "v_fma_f32 v71, v71, v67, v103\n\t"
  "v_mfma_f32_16x16x32_bf16 v[52:55], a[28:31], a[96:99], v[52:55]\n\t"
  "v_fma_f32 v72, v72, v108, v104\n\t"
  "v_fma_f32 v73, v73, v109, v105\n\t"
  "v_mfma_f32_16x16x32_bf16 v[52:55], a[48:51], a[100:103], v[52:55]\n\t"
  "v_fma_f32 v74, v74, v110, v106\n\t"
  "v_fma_f32 v75, v75, v111, v107\n\t"
  "v_mfma_f32_16x16x32_bf16 v[56:59], a[232:235], a[88:91], v[56:59]\n\t"
  "v_cvt_pk_bf16_f32 v76, v68, v69\n\t"
  "v_cvt_pk_bf16_f32 v77, v70, v71\n\t"
  "v_mfma_f32_16x16x32_bf16 v[56:59], a[12:15], a[92:95], v[56:59]\n\t"
  "ds_write_b128 v13, v[48:51] offset:17408\n\t"
  "v_cvt_pk_bf16_f32 v78, v72, v73\n\t"
  "v_mfma_f32_16x16x32_bf16 v[56:59], a[32:35], a[96:99], v[56:59]\n\t"
  "v_cvt_pk_bf16_f32 v79, v74, v75\n\t"
  "s_mov_b32 s55, 1310720\n\t"
  "v_mfma_f32_16x16x32_bf16 v[56:59], a[52:55], a[100:103], v[56:59]\n\t"
  "buffer_store_dwordx4 v[76:79], v16, %[srdo], s55 offen\n\t"
  "ds_write_b128 v13, v[52:55] offset:18432\n\t"
  "s_nop 5\n\t"
  "ds_write_b128 v13, v[56:59] offset:19456\n\t"
  "buffer_store_dwordx2 v[132:133], v18, %[srd], 0 offen sc1\n\t"
  "s_waitcnt lgkmcnt(0)\n\t"
  "s_barrier\n\t"
  "ds_read_b128 v[68:71], v14 offset:15360\n\t"
  "ds_read_b128 v[72:75], v15 offset:15360\n\t"
  "ds_read_b128 v[76:79], v14 offset:35840\n\t"
  "ds_read_b128 v[80:83], v15 offset:35840\n\t"
  "ds_read_b128 v[84:87], v14 offset:56320\n\t"
  "ds_read_b128 v[88:91], v15 offset:56320\n\t"
  "ds_read_b128 v[92:95], v60 offset:15360\n\t"
  "ds_read_b128 v[96:99], v61 offset:15360\n\t"
  "ds_read_b128 v[100:103], v60 offset:35840\n\t"
  "ds_read_b128 v[104:107], v61 offset:35840\n\t"
  "s_waitcnt lgkmcnt(7)\n\t"
  "v_pk_add_f32 v[68:69], v[68:69], v[76:77]\n\t"
  "v_pk_add_f32 v[70:71], v[70:71], v[78:79]\n\t"
  "s_waitcnt lgkmcnt(6)\n\t"
  "v_pk_add_f32 v[72:73], v[72:73], v[80:81]\n\t"
  "v_pk_add_f32 v[74:75], v[74:75], v[82:83]\n\t"
  "s_waitcnt lgkmcnt(3)\n\t"
  "v_pk_add_f32 v[84:85], v[84:85], v[92:93]\n\t"
  "v_pk_add_f32 v[86:87], v[86:87], v[94:95]\n\t"
  "s_waitcnt lgkmcnt(2)\n\t"
  "v_pk_add_f32 v[88:89], v[88:89], v[96:97]\n\t"
  "v_pk_add_f32 v[90:91], v[90:91], v[98:99]\n\t"
  "v_pk_add_f32 v[68:69], v[68:69], v[84:85]\n\t"
  "v_pk_add_f32 v[70:71], v[70:71], v[86:87]\n\t"
  "v_pk_add_f32 v[72:73], v[72:73], v[88:89]\n\t"
  "v_pk_add_f32 v[74:75], v[74:75], v[90:91]\n\t"
  "s_waitcnt lgkmcnt(1)\n\t"
  "v_pk_fma_f32 v[68:69], v[68:69], v[64:65], v[100:101]\n\t"
  "v_pk_fma_f32 v[70:71], v[70:71], v[66:67], v[102:103]\n\t"
  "s_waitcnt lgkmcnt(0)\n\t"
  "v_pk_fma_f32 v[72:73], v[72:73], v[108:109], v[104:105]\n\t"
  "v_pk_fma_f32 v[74:75], v[74:75], v[110:111], v[106:107]\n\t"
  "v_cvt_pk_bf16_f32 v76, v68, v69\n\t"
  "v_cvt_pk_bf16_f32 v77, v70, v71\n\t"
  "v_cvt_pk_bf16_f32 v78, v72, v73\n\t"
  "v_cvt_pk_bf16_f32 v79, v74, v75\n\t"
  "s_mov_b32 s55, 1351680\n\t"
  "buffer_store_dwordx4 v[76:79], v16, %[srdo], s55 offen\n\t"
  "s_branch Lfend%=\n\t"
  "Lst0_slow%=:\n\t"
  "s_nop 7\n\t"
  "s_nop 3\n\t"
  "s_mov_b32 s49, 0x40000\n\t"
  "Lst0_loop%=:\n\t"
  "s_sleep 2\n\t"
  "buffer_load_dwordx2 v[92:93], v136, %[srd], %[sST] offen offset:0 sc1\n\t"
  "buffer_load_dwordx2 v[94:95], v136, %[srd], %[sST] offen offset:1024 sc1\n\t"
  "buffer_load_dwordx2 v[96:97], v136, %[srd], %[sST] offen offset:2048 sc1\n\t"
  "buffer_load_dwordx2 v[98:99], v136, %[srd], %[sST] offen offset:3072 sc1\n\t"
  "s_waitcnt vmcnt(0)\n\t"
  "v_min_u32 v131, v93, v95\n\t"
  "v_min3_u32 v131, v131, v97, v99\n\t"
  "v_cmp_eq_u32 vcc, 0, v131\n\t"
  "s_nop 3\n\t"
  "s_cmp_eq_u64 vcc, 0\n\t"
  "s_cbranch_scc1 Lst0_back%=\n\t"
  "s_sub_u32 s49, s49, 1\n\t"
  "s_cmp_lg_u32 s49, 0\n\t"
  "s_cbranch_scc1 Lst0_loop%=\n\t"
  "s_branch Lst0_back%=\n\t"
  "Lw2_slow%=:\n\t"
  "s_nop 7\n\t"
  "s_nop 3\n\t"
  "s_mov_b32 s49, 0x40000\n\t"
  "Lw2_loop%=:\n\t"
  "s_sleep 2\n\t"
  "buffer_load_dwordx2 v[138:139], v2, %[srd], 0 offen sc1\n\t"
  "s_waitcnt vmcnt(0)\n\t"
  "v_cmp_ne_u64 vcc, v[132:133], v[138:139]\n\t"
  "s_nop 3\n\t"
  "s_cmp_eq_u64 vcc, 0\n\t"
  "s_cbranch_scc1 Lw2_back%=\n\t"
  "s_sub_u32 s49, s49, 1\n\t"
  "s_cmp_lg_u32 s49, 0\n\t"
  "s_cbranch_scc1 Lw2_loop%=\n\t"
  "s_branch Lw2_back%=\n\t"
  "Lst1_slow%=:\n\t"
  "s_nop 7\n\t"
  "s_nop 3\n\t"
  "s_waitcnt vmcnt(0)\n\t"
  "s_movk_i32 s59, 8192\n\t"
  "buffer_store_dwordx4 v[132:135], v9, %[srd], s59 offen sc1\n\t"
  "s_mov_b32 s49, 0x40000\n\t"
  "Lst1_loop%=:\n\t"
  "s_sleep 2\n\t"
  "buffer_load_dwordx2 v[100:101], v136, %[srd], s54 offen offset:0 sc1\n\t"
  "buffer_load_dwordx2 v[102:103], v136, %[srd], s54 offen offset:1024 sc1\n\t"
  "buffer_load_dwordx2 v[104:105], v136, %[srd], s54 offen offset:2048 sc1\n\t"
  "buffer_load_dwordx2 v[106:107], v136, %[srd], s54 offen offset:3072 sc1\n\t"
  "s_waitcnt vmcnt(0)\n\t"
  "v_min_u32 v131, v101, v103\n\t"
  "v_min3_u32 v131, v131, v105, v107\n\t"
  "v_cmp_eq_u32 vcc, 0, v131\n\t"
  "s_nop 3\n\t"
  "s_cmp_eq_u64 vcc, 0\n\t"
  "s_cbranch_scc1 Lst1_back%=\n\t"
  "s_sub_u32 s49, s49, 1\n\t"
  "s_cmp_lg_u32 s49, 0\n\t"
  "s_cbranch_scc1 Lst1_loop%=\n\t"
  "s_branch Lst1_back%=\n\t"
  "Lfl0_slow%=:\n\t"
  "s_nop 7\n\t"
  "s_nop 3\n\t"
  "s_waitcnt vmcnt(0)\n\t"
  "s_movk_i32 s59, 16384\n\t"
  "buffer_store_dwordx4 v[132:135], v9, %[srd], s59 offen sc1\n\t"
  "s_mov_b32 s49, 0x40000\n\t"
  "Lfl0_loop%=:\n\t"
  "s_sleep 2\n\t"
  "s_add_u32 s57, %[sFL], 0\n\t"
  "buffer_load_dwordx2 v[138:139], v137, %[srd], s57 offen sc1\n\t"
  "s_waitcnt vmcnt(0)\n\t"
  "v_cmp_ne_u64 vcc, v[132:133], v[138:139]\n\t"
  "s_nop 3\n\t"
  "s_cmp_eq_u64 vcc, 0\n\t"
  "s_cbranch_scc1 Lfl0_back%=\n\t"
  "s_sub_u32 s49, s49, 1\n\t"
  "s_cmp_lg_u32 s49, 0\n\t"
  "s_cbranch_scc1 Lfl0_loop%=\n\t"
  "s_branch Lfl0_back%=\n\t"
  "Lfl1_slow%=:\n\t"
  "s_nop 7\n\t"
  "s_nop 3\n\t"
  "s_waitcnt vmcnt(0)\n\t"
  "s_movk_i32 s59, 24576\n\t"
  "buffer_store_dwordx4 v[132:135], v9, %[srd], s59 offen sc1\n\t"
  "s_mov_b32 s49, 0x40000\n\t"
  "Lfl1_loop%=:\n\t"
  "s_sleep 2\n\t"
  "s_add_u32 s57, %[sFL], 8192\n\t"
  "buffer_load_dwordx2 v[138:139], v137, %[srd], s57 offen sc1\n\t"
  "s_waitcnt vmcnt(0)\n\t"
  "v_cmp_ne_u64 vcc, v[132:133], v[138:139]\n\t"
  "s_nop 3\n\t"
  "s_cmp_eq_u64 vcc, 0\n\t"
  "s_cbranch_scc1 Lfl1_back%=\n\t"
  "s_sub_u32 s49, s49, 1\n\t"
  "s_cmp_lg_u32 s49, 0\n\t"
  "s_cbranch_scc1 Lfl1_loop%=\n\t"
  "s_branch Lfl1_back%=\n\t"
  "Lfl2_slow%=:\n\t"
  "s_nop 7\n\t"
  "s_nop 3\n\t"
  "s_mov_b32 s49, 0x40000\n\t"
  "Lfl2_loop%=:\n\t"
  "s_sleep 2\n\t"
  "s_add_u32 s57, %[sFL], 16384\n\t"
  "buffer_load_dwordx2 v[138:139], v137, %[srd], s57 offen sc1\n\t"
  "s_waitcnt vmcnt(0)\n\t"
  "v_cmp_ne_u64 vcc, v[132:133], v[138:139]\n\t"
  "s_nop 3\n\t"
  "s_cmp_eq_u64 vcc, 0\n\t"
  "s_cbranch_scc1 Lfl2_back%=\n\t"
  "s_sub_u32 s49, s49, 1\n\t"
  "s_cmp_lg_u32 s49, 0\n\t"
  "s_cbranch_scc1 Lfl2_loop%=\n\t"
  "s_branch Lfl2_back%=\n\t"
  "Lfl3_slow%=:\n\t"
  "s_nop 7\n\t"
  "s_nop 3\n\t"
  "s_mov_b32 s49, 0x40000\n\t"
  "Lfl3_loop%=:\n\t"
  "s_sleep 2\n\t"
  "s_add_u32 s57, %[sFL], 24576\n\t"
  "buffer_load_dwordx2 v[138:139], v137, %[srd], s57 offen sc1\n\t"
  "s_waitcnt vmcnt(0)\n\t"
  "v_cmp_ne_u64 vcc, v[132:133], v[138:139]\n\t"
  "s_nop 3\n\t"
  "s_cmp_eq_u64 vcc, 0\n\t"
  "s_cbranch_scc1 Lfl3_back%=\n\t"
  "s_sub_u32 s49, s49, 1\n\t"
  "s_cmp_lg_u32 s49, 0\n\t"
  "s_cbranch_scc1 Lfl3_loop%=\n\t"
  "s_branch Lfl3_back%=\n\t"
  "Lfend%=:\n\t"
  ".Lk_end:\n\t"
    :
    : [srd] "s"(srd), [srdo] "s"(srdo), [w] "s"(w), [ws] "s"(ws),
      [tlo] "s"((u32)tag), [thi] "s"((u32)(tag >> 32)), [sW1] "s"(OFF_W1F + (j * 4 + w) * 61440u), [sW2] "s"(OFF_W2F + (ng * 4 + w) * 61440u),
      [sUB] "s"(OFF_UB + (j * 4 + w) * 1536u),
      [sH2ld] "s"(OFF_H2F + ((4 * p + sub) * 48 + 12 * w) * 1024u), [sST] "s"(OFF_ST2 + p * 4096u), [sFL] "s"(OFF_FL + p * 32768u + w * 2048u),
      [sRT] "s"(OFF_RT + ng * 320u), [wdone] "s"(wdone), [pm] "s"(pmask)
    : "memory", "vcc", "scc",
"a0", "a1", "a2", "a3", "a4", "a5", "a6", "a7", "a8", "a9", "a10", "a11", "a12", "a13", "a14", "a15", "a16", "a17", "a18", "a19", "a20", "a21", "a22", "a23", "a24", "a25", "a26", "a27", "a28", "a29", "a30", "a31", "a32", "a33", "a34", "a35", "a36", "a37", "a38", "a39", "a40", "a41", "a42", "a43", "a44", "a45", "a46", "a47", "a48", "a49", "a50", "a51", "a52", "a53", "a54", "a55", "a56", "a57", "a58", "a59", "a60", "a61", "a62", "a63", "a64", "a65", "a66", "a67", "a68", "a69", "a70", "a71", "a72", "a73", "a74", "a75", "a76", "a77", "a78", "a79", "a80", "a81", "a82", "a83", "a84", "a85", "a86", "a87", "a88", "a89", "a90", "a91", "a92", "a93", "a94", "a95", "a96", "a97", "a98", "a99", "a100", "a101", "a102", "a103", "a104", "a105", "a106", "a107", "a108", "a109", "a110", "a111", "a112", "a113", "a114", "a115", "a116", "a117", "a118", "a119", "a120", "a121", "a122", "a123", "a124", "a125", "a126", "a127", "a128", "a129", "a130", "a131", "a132", "a133", "a134", "a135", "a136", "a137", "a138", "a139", "a140", "a141", "a142", "a143", "a144", "a145", "a146", "a147", "a148", "a149", "a150", "a151", "a152", "a153", "a154", "a155", "a156", "a157", "a158", "a159", "a160", "a161", "a162", "a163", "a164", "a165", "a166", "a167", "a168", "a169", "a170", "a171", "a172", "a173", "a174", "a175", "a176", "a177", "a178", "a179", "a180", "a181", "a182", "a183", "a184", "a185", "a186", "a187", "a188", "a189", "a190", "a191", "a192", "a193", "a194", "a195", "a196", "a197", "a198", "a199", "a200", "a201", "a202", "a203", "a204", "a205", "a206", "a207", "a208", "a209", "a210", "a211", "a212", "a213", "a214", "a215", "a216", "a217", "a218", "a219", "a220", "a221", "a222", "a223", "a224", "a225", "a226", "a227", "a228", "a229", "a230", "a231", "a232", "a233", "a234", "a235", "a236", "a237", "a238", "a239", "a240", "a241", "a242", "a243", "a244", "a245", "a246", "a247", "a248", "a249", "a250", "a251", "a252", "a253", "a254", "a255", "s44", "s45", "s46", "s47", "s48", "s49", "s52", "s54", "s55", "s56", "s57", "s58", "s59", "s60", "s61", "s62", "s63", "s68", "s69", "v0", "v1", "v2", "v3", "v4", "v5", "v6", "v7", "v8", "v9", "v10", "v11", "v12", "v13", "v14", "v15", "v16", "v17", "v18", "v19", "v20", "v21", "v22", "v23", "v24", "v25", "v26", "v27", "v28", "v29", "v30", "v31", "v32", "v33", "v34", "v35", "v36", "v37", "v38", "v39", "v40", "v41", "v42", "v43", "v44", "v45", "v46", "v47", "v48", "v49", "v50", "v51", "v52", "v53", "v54", "v55", "v56", "v57", "v58", "v59", "v60", "v61", "v62", "v63", "v64", "v65", "v66", "v67", "v68", "v69", "v70", "v71", "v72", "v73", "v74", "v75", "v76", "v77", "v78", "v79", "v80", "v81", "v82", "v83", "v84", "v85", "v86", "v87", "v88", "v89", "v90", "v91", "v92", "v93", "v94", "v95", "v96", "v97", "v98", "v99", "v100", "v101", "v102", "v103", "v104", "v105", "v106", "v107", "v108", "v109", "v110", "v111", "v112", "v113", "v114", "v115", "v116", "v117", "v118", "v119", "v120", "v121", "v122", "v123", "v124", "v125", "v126", "v127", "v128", "v129", "v130", "v131", "v132", "v133", "v134", "v135", "v136", "v137", "v138", "v139", "v141", "v142", "v143", "v144", "v145", "v146", "v147", "v148", "v149", "v150", "v151", "v152", "v153", "v154", "v155", "v156", "v157", "v158", "v159", "v160", "v161", "v162", "v163", "v164", "v165", "v166", "v167", "v168", "v169", "v170", "v171", "v172", "v173", "v174", "v175", "v176", "v177", "v178", "v179", "v180", "v181", "v182", "v183", "v184", "v185", "v186", "v187", "v188", "v189", "v190", "v191", "v192", "v193", "v194", "v195", "v196", "v197", "v198", "v199", "v200", "v201", "v202", "v203", "v204", "v205", "v206", "v207", "v208", "v209", "v210", "v211", "v212", "v213", "v214", "v215", "v216", "v217", "v218", "v219", "v220", "v221", "v222", "v223", "v224", "v225", "v226", "v227", "v228", "v229", "v230", "v231", "v232", "v233", "v234", "v235", "v236", "v237", "v238", "v239", "v240", "v241", "v242", "v243", "v244", "v245", "v246", "v247", "v248", "v249", "v250", "v251", "v252", "v253", "v254", "v255"
  );
}

extern "C" void kernel_launch(void* const* d_in, const int* in_sizes, int n_in, void* d_out, int out_size,
                              void* d_ws, size_t ws_size, hipStream_t stream) {
  k_main<<<256, 256, 0, stream>>>((const u16*)d_in[0], (const u16*)d_in[1], (const u16*)d_in[2], (const u16*)d_in[3], (const u16*)d_in[4],
                                  (const u16*)d_in[5], (const u16*)d_in[7], (const u16*)d_in[8], (const u16*)d_in[9], (const u16*)d_in[10],
                                  (const u16*)d_in[11], (char*)d_ws, (u16*)d_out);
}
```

```cpp
#include <hip/hip_runtime.h>
#include <stdint.h>

typedef unsigned short u16;
typedef unsigned int u32;
typedef unsigned long long u64;
typedef __attribute__((ext_vector_type(8))) __bf16 bf16x8;
typedef __attribute__((ext_vector_type(2))) __bf16 bf16x2;
typedef __attribute__((ext_vector_type(4))) short s16x4;
typedef __attribute__((ext_vector_type(4))) float f32x4;
typedef __attribute__((ext_vector_type(2))) float f32x2;
typedef __attribute__((ext_vector_type(4))) u32 u32x4;
typedef __attribute__((ext_vector_type(2))) u32 u32x2;
#define LAS __attribute__((address_space(3)))
#define GAS __attribute__((address_space(1)))
#define DI __device__ __forceinline__

constexpr int S_ = 2048, D_ = 640, F_ = 1536;
constexpr unsigned OFF_EP = 0u;
constexpr unsigned OFF_FR = 2048u;
constexpr unsigned OFF_FA1 = 4096u;
constexpr unsigned OFF_FA2 = 24576u;
constexpr unsigned OFF_FA3 = 55296u;
constexpr unsigned OFF_FX = 71680u;
constexpr unsigned OFF_FX1 = 235520u;
constexpr unsigned OFF_FW1 = 399360u;
constexpr unsigned OFF_FU = 407552u;
constexpr unsigned OFF_FW2 = 415744u;
constexpr unsigned OFF_RT = 419840u;
constexpr unsigned OFF_GB = 422400u;
constexpr unsigned OFF_RL = 424960u;
constexpr unsigned OFF_ST2 = 442368u;
constexpr unsigned OFF_FL = 573440u;
constexpr unsigned OFF_MODP = 1097728u;
constexpr unsigned OFF_ST1 = 1159168u;
constexpr unsigned OFF_UB = 1175552u;
constexpr unsigned OFF_KVT = 1273856u;
constexpr unsigned OFF_XT = 3895296u;
constexpr unsigned OFF_X2P = 6516736u;
constexpr unsigned OFF_H2F = 9138176u;
constexpr unsigned OFF_W1F = 15429632u;
constexpr unsigned OFF_W2F = 19361792u;
constexpr unsigned WS_END = 21327872u;
constexpr unsigned TAG_A = 2654435769u;
constexpr unsigned TAG_B = 2246822507u;
constexpr unsigned TAG_C = 2135587861u;
constexpr unsigned TAG_D = 3266489909u;
constexpr unsigned FC_PITCH = 4160u;
constexpr unsigned FC_CH = 16640u;
constexpr unsigned L_FC = 0u;
constexpr unsigned L_KV = 66560u;
constexpr unsigned L_X = 84992u;
constexpr unsigned L_ST = 101376u;
constexpr unsigned L_XR = 117760u;
constexpr unsigned L_CNT = 140288u;
constexpr unsigned L_STASH = 143360u;
constexpr unsigned L_UN = 71680u;
constexpr unsigned UN_SZ = 22528u;

constexpr unsigned OOB = 0x80000000u;
DI unsigned rfl(unsigned x) { return __builtin_amdgcn_readfirstlane(x); }
DI unsigned stkey(unsigned r) { const unsigned k = (r >> 1) & 7; return (((k ^ (k >> 1)) >> 1) & 1) | ((k & 1) << 1) | (k & 4); }
DI float bflo(u32 w) { return __uint_as_float(w << 16); }
DI float bfhi(u32 w) { return __uint_as_float(w & 0xffff0000u); }
DI u32 pack_bf16(float lo, float hi) { bf16x2 v; v[0] = (__bf16)lo; v[1] = (__bf16)hi; return __builtin_bit_cast(u32, v); }
DI float rbf(float x) { __bf16 b = (__bf16)x; return (float)b; }
DI float dot2(u32 a, u32 b, float c) { return __builtin_amdgcn_fdot2_f32_bf16(__builtin_bit_cast(bf16x2, a), __builtin_bit_cast(bf16x2, b), c, false); }
DI f32x4 mfma(u32x4 a, u32x4 b, f32x4 c) { return __builtin_amdgcn_mfma_f32_16x16x32_bf16(__builtin_bit_cast(bf16x8, a), __builtin_bit_cast(bf16x8, b), c, 0, 0, 0); }
DI void mfma_v(f32x4& c, u32x4 a, u32x4 b) { asm volatile("s_nop 1\n\tv_mfma_f32_16x16x32_bf16 %0, %1, %2, %0" : "+v"(c) : "v"(a), "v"(b)); }
DI u32x2 tr_read(const char* p) { return __builtin_bit_cast(u32x2, __builtin_amdgcn_ds_read_tr16_b64_v4i16((LAS s16x4*)p)); }
typedef __amdgpu_buffer_rsrc_t rsrc_t;
DI rsrc_t mkrsrc(const void* p, unsigned bytes) { return __builtin_amdgcn_make_buffer_rsrc((void*)p, 0, bytes, 0x00020000); }
#define BAR() asm volatile("s_waitcnt lgkmcnt(0)\n\ts_barrier" ::: "memory")
#define FENCE() do { asm volatile("" ::: "memory"); __builtin_amdgcn_sched_barrier(0); } while (0)
constexpr int SC1 = 16, NT = 2, W_IDLE_SLEEP = 8;

DI u64 load_tag(const char* ws) {
  u64 epoch;
  asm volatile("s_load_dwordx2 %0, %1, 0\n\ts_waitcnt lgkmcnt(0)" : "=&s"(epoch) : "s"(ws) : "memory");
  const u32 tl = ((u32)epoch ^ TAG_A) * TAG_B, th = ((u32)(epoch >> 32) ^ TAG_C) * TAG_D + tl;
  return ((u64)th << 32) | tl;
}
template <int N, int SL = 2>
DI void poll_flags(rsrc_t srd, unsigned voff, u64 tag) {
  const u32 lo = (u32)tag, hi = (u32)(tag >> 32);
#pragma unroll 1
  for (int spin = 0; spin < (1 << 18); ++spin) {
    u32 bad = 0;
#pragma unroll
    for (int k = 0; k < N; ++k) { const u32x2 f = __builtin_amdgcn_raw_buffer_load_b64(srd, voff + k * 512, 0, SC1); bad |= (f[0] ^ lo) | (f[1] ^ hi); }
    asm volatile("" ::: "memory");
    if (__all(bad == 0)) break;
    __builtin_amdgcn_s_sleep(SL);
  }
  FENCE();
}
DI void put_flag(rsrc_t srd, unsigned off, unsigned lane, u64 tag) {
  __builtin_amdgcn_raw_buffer_store_b128(u32x4{(u32)tag, (u32)(tag >> 32), (u32)tag, (u32)(tag >> 32)}, srd, lane < 8 ? off + lane * 16 : OOB, 0, SC1);
  FENCE();
}
DI float modval(rsrc_t srd, unsigned col) {
  float m[4];
#pragma unroll
  for (int k = 0; k < 4; ++k) m[k] = __uint_as_float(__builtin_amdgcn_raw_buffer_load_b32(srd, col * 4, OFF_MODP + k * 15360, SC1));
  return rbf((m[0] + m[1]) + (m[2] + m[3]));
}

constexpr int RP1 = 400, RP2 = 656, L_WS = 69632;

__global__ __launch_bounds__(256, 1) void k_main(const u16* __restrict__ x, const u16* __restrict__ kIn, const u16* __restrict__ vIn,
                                                 const u16* __restrict__ shk, const u16* __restrict__ ssmk, const u16* __restrict__ ssmD,
                                                 const u16* __restrict__ wg, const u16* __restrict__ wu, const u16* __restrict__ wd,
                                                 const u16* __restrict__ cIn, const u16* __restrict__ wmod, char* ws, u16* __restrict__ out) {
  __shared__ __attribute__((aligned(16))) char smem[163840];
  const unsigned b = blockIdx.x, tid = threadIdx.x, w = rfl(tid >> 6), lane = tid & 63;
  const unsigned xcd = b & 7, loc = b >> 3, pl = loc & 3, p = 4 * (xcd >> 1) + pl, j = 8 * (xcd & 1) + (loc >> 2), ng = j & 7, sub = j >> 3;
  if (loc >= 24) {
    u64 pc; unsigned len;
    asm volatile("s_getpc_b64 %0\n.Lk_here:\n\ts_mov_b32 %1, .Lk_end-.Lk_here" : "=s"(pc), "=s"(len));
    asm volatile("buffer_load_dword a255, %0, %1, 0 offen" :: "v"(lane < 16 ? (lane * 32 + (loc - 24) * 4 + w) * 128 : OOB), "s"(mkrsrc((const void*)pc, len)) : "a255", "memory");
  }
  asm volatile("" :: "s"(x), "s"(kIn), "s"(vIn), "s"(shk), "s"(ssmk), "s"(ssmD), "s"(wg), "s"(wu), "s"(wd), "s"(cIn), "s"(wmod), "s"(ws), "s"(out));
  const rsrc_t srd = mkrsrc(ws, WS_END);
  const rsrc_t srdo = mkrsrc(out, S_ * D_ * 2);
  const unsigned lb = (unsigned)(size_t)smem;
  const unsigned role = loc < 20 ? 0 : loc < 28 ? 1 : 2;
  const unsigned cb = xcd * 20 + loc, w1 = xcd * 8 + loc - 20, w2 = xcd * 4 + loc - 28;
  const unsigned tb = 4 * (cb & 31) + w, lkv = L_KV + 2048 + w * 9216;
  if (role == 0)
    asm volatile(
  "s_mov_b32 m0, %[lk]\n\t"
  "s_nop 3\n\t"
  "buffer_load_dwordx4 %[vo], %[rk], %[sh] offen nt lds\n\t"
  "s_add_u32 m0, %[lk], 1024\n\t"
  "s_add_u32 s44, %[so], 0\n\t"
  "buffer_load_dwordx4 %[vo], %[rk], s44 offen nt lds\n\t"
  "s_add_u32 m0, %[lk], 5120\n\t"
  "s_nop 0\n\t"
  "buffer_load_dwordx4 %[vo], %[rv], s44 offen nt lds\n\t"
  "s_add_u32 m0, %[lk], 2048\n\t"
  "s_add_u32 s44, %[so], 5120\n\t"
  "buffer_load_dwordx4 %[vo], %[rk], s44 offen nt lds\n\t"
  "s_add_u32 m0, %[lk], 6144\n\t"
  "s_nop 0\n\t"
  "buffer_load_dwordx4 %[vo], %[rv], s44 offen nt lds\n\t"
  "s_add_u32 m0, %[lk], 3072\n\t"
  "s_add_u32 s44, %[so], 10240\n\t"
  "buffer_load_dwordx4 %[vo], %[rk], s44 offen nt lds\n\t"
  "s_add_u32 m0, %[lk], 7168\n\t"
  "s_nop 0\n\t"
  "buffer_load_dwordx4 %[vo], %[rv], s44 offen nt lds\n\t"
  "s_add_u32 m0, %[lk], 4096\n\t"
  "s_add_u32 s44, %[so], 15360\n\t"
  "buffer_load_dwordx4 %[vo], %[rk], s44 offen nt lds\n\t"
  "s_add_u32 m0, %[lk], 8192\n\t"
  "s_nop 0\n\t"
  "buffer_load_dwordx4 %[vo], %[rv], s44 offen nt lds\n\t"
      :: [rk] "s"(mkrsrc(kIn, S_ * D_ * 2)), [rv] "s"(mkrsrc(vIn, S_ * D_ * 2)), [lk] "s"(lb + lkv), [so] "s"(tb * 20480u + (cb >> 5) * 256u), [sh] "s"(tb ? tb * 20480u + (cb >> 5) * 256u - 5120u : OOB),
         [vo] "v"((lane >> 4) * 1280 + (lane & 15) * 16) : "memory", "scc", "s44");
  FENCE();
  const u32x4 zero4 = {0, 0, 0, 0};
  unsigned wdone = 0, dval = 0;
#define U_KQ(u_) ((u_) & 3)
#define U_CG(u_) (((u_) >> 2) < 30 || ((u_) >> 2) >= 50 ? (u_) >> 2 : ((u_) >> 2) < 40 ? ((u_) >> 2) + 10 : ((u_) >> 2) - 10)
#define U_SRC(u_) (U_KQ(u_) * 160u * 7680u + U_CG(u_) * 128u)
#define U_FLAG(u_) (OFF_FA2 + (U_KQ(u_) * 60 + U_CG(u_)) * 128)
  const unsigned vm = (lane >> 3) * 7680 + (((lane & 7) ^ (((lane >> 4) & 3) << 1)) * 16);
#define GEMV_LOAD(u_, lu_) asm volatile( \
  "s_nop 4\n\t" \
  "s_add_u32 m0, %[lu], 20480\n\t" \
  "s_nop 0\n\t" \
  "buffer_load_dwordx4 %[vl16], %[rc], %[sC] offen lds\n\t" \
  "s_add_u32 m0, %[lu], 0\n\t" \
  "s_add_u32 s44, %[sMod], 0\n\t" \
  "buffer_load_dwordx4 %[vm], %[rm], s44 offen nt lds\n\t" \
  "s_add_u32 m0, %[lu], 1024\n\t" \
  "s_add_u32 s44, %[sMod], 61440\n\t" \
  "buffer_load_dwordx4 %[vm], %[rm], s44 offen nt lds\n\t" \
  "s_add_u32 m0, %[lu], 2048\n\t" \
  "s_add_u32 s44, %[sMod], 122880\n\t" \
  "buffer_load_dwordx4 %[vm], %[rm], s44 offen nt lds\n\t" \
  "s_add_u32 m0, %[lu], 3072\n\t" \
  "s_add_u32 s44, %[sMod], 184320\n\t" \
  "buffer_load_dwordx4 %[vm], %[rm], s44 offen nt lds\n\t" \
  "s_add_u32 m0, %[lu], 4096\n\t" \
  "s_add_u32 s44, %[sMod], 245760\n\t" \
  "buffer_load_dwordx4 %[vm], %[rm], s44 offen nt lds\n\t" \
  "s_add_u32 m0, %[lu], 5120\n\t" \
  "s_add_u32 s44, %[sMod], 307200\n\t" \
  "buffer_load_dwordx4 %[vm], %[rm], s44 offen nt lds\n\t" \
  "s_add_u32 m0, %[lu], 6144\n\t" \
  "s_add_u32 s44, %[sMod], 368640\n\t" \
  "buffer_load_dwordx4 %[vm], %[rm], s44 offen nt lds\n\t" \
  "s_add_u32 m0, %[lu], 7168\n\t" \
  "s_add_u32 s44, %[sMod], 430080\n\t" \
  "buffer_load_dwordx4 %[vm], %[rm], s44 offen nt lds\n\t" \
  "s_add_u32 m0, %[lu], 8192\n\t" \
  "s_add_u32 s44, %[sMod], 491520\n\t" \
  "buffer_load_dwordx4 %[vm], %[rm], s44 offen nt lds\n\t" \
  "s_add_u32 m0, %[lu], 9216\n\t" \
  "s_add_u32 s44, %[sMod], 552960\n\t" \
  "buffer_load_dwordx4 %[vm], %[rm], s44 offen nt lds\n\t" \
  "s_add_u32 m0, %[lu], 10240\n\t" \
  "s_add_u32 s44, %[sMod], 614400\n\t" \
  "buffer_load_dwordx4 %[vm], %[rm], s44 offen nt lds\n\t" \
  "s_add_u32 m0, %[lu], 11264\n\t" \
  "s_add_u32 s44, %[sMod], 675840\n\t" \
  "buffer_load_dwordx4 %[vm], %[rm], s44 offen nt lds\n\t" \
  "s_add_u32 m0, %[lu], 12288\n\t" \
  "s_add_u32 s44, %[sMod], 737280\n\t" \
  "buffer_load_dwordx4 %[vm], %[rm], s44 offen nt lds\n\t" \
  "s_add_u32 m0, %[lu], 13312\n\t" \
  "s_add_u32 s44, %[sMod], 798720\n\t" \
  "buffer_load_dwordx4 %[vm], %[rm], s44 offen nt lds\n\t" \
  "s_add_u32 m0, %[lu], 14336\n\t" \
  "s_add_u32 s44, %[sMod], 860160\n\t" \
  "buffer_load_dwordx4 %[vm], %[rm], s44 offen nt lds\n\t" \
  "s_add_u32 m0, %[lu], 15360\n\t" \
  "s_add_u32 s44, %[sMod], 921600\n\t" \
  "buffer_load_dwordx4 %[vm], %[rm], s44 offen nt lds\n\t" \
  "s_add_u32 m0, %[lu], 16384\n\t" \
  "s_add_u32 s44, %[sMod], 983040\n\t" \
  "buffer_load_dwordx4 %[vm], %[rm], s44 offen nt lds\n\t" \
  "s_add_u32 m0, %[lu], 17408\n\t" \
  "s_add_u32 s44, %[sMod], 1044480\n\t" \
  "buffer_load_dwordx4 %[vm], %[rm], s44 offen nt lds\n\t" \
  "s_add_u32 m0, %[lu], 18432\n\t" \
  "s_add_u32 s44, %[sMod], 1105920\n\t" \
  "buffer_load_dwordx4 %[vm], %[rm], s44 offen nt lds\n\t" \
  "s_add_u32 m0, %[lu], 19456\n\t" \
  "s_add_u32 s44, %[sMod], 1167360\n\t" \
  "buffer_load_dwordx4 %[vm], %[rm], s44 offen nt lds\n\t" \
        :: [rm] "s"(mkrsrc(wmod, D_ * 3840 * 2)), [rc] "s"(mkrsrc(cIn, D_ * 2)), [lu] "s"(lu_), [sC] "s"(U_KQ(u_) * 320u), [sMod] "s"(U_SRC(u_)), \
           [vl16] "v"(lane * 16), [vm] "v"(vm) : "memory", "scc", "s44")
#define W1_LOAD() asm volatile( \
  "s_nop 4\n\t" \
  "s_add_u32 s44, %[sW1], 0\n\t" \
  "buffer_load_dwordx4 a[0:3], %[vl16], %[srd], s44 offen offset:0 sc1\n\t" \
  "buffer_load_dwordx4 a[4:7], %[vl16], %[srd], s44 offen offset:1024 sc1\n\t" \
  "buffer_load_dwordx4 a[8:11], %[vl16], %[srd], s44 offen offset:2048 sc1\n\t" \
  "buffer_load_dwordx4 a[12:15], %[vl16], %[srd], s44 offen offset:3072 sc1\n\t" \
  "s_add_u32 s44, %[sW1], 4096\n\t" \
  "buffer_load_dwordx4 a[16:19], %[vl16], %[srd], s44 offen offset:0 sc1\n\t" \
  "buffer_load_dwordx4 a[20:23], %[vl16], %[srd], s44 offen offset:1024 sc1\n\t" \
  "buffer_load_dwordx4 a[24:27], %[vl16], %[srd], s44 offen offset:2048 sc1\n\t" \
  "buffer_load_dwordx4 a[28:31], %[vl16], %[srd], s44 offen offset:3072 sc1\n\t" \
  "s_add_u32 s44, %[sW1], 8192\n\t" \
  "buffer_load_dwordx4 a[32:35], %[vl16], %[srd], s44 offen offset:0 sc1\n\t" \
  "buffer_load_dwordx4 a[36:39], %[vl16], %[srd], s44 offen offset:1024 sc1\n\t" \
  "buffer_load_dwordx4 a[40:43], %[vl16], %[srd], s44 offen offset:2048 sc1\n\t" \
  "buffer_load_dwordx4 a[44:47], %[vl16], %[srd], s44 offen offset:3072 sc1\n\t" \
  "s_add_u32 s44, %[sW1], 12288\n\t" \
  "buffer_load_dwordx4 a[48:51], %[vl16], %[srd], s44 offen offset:0 sc1\n\t" \
  "buffer_load_dwordx4 a[52:55], %[vl16], %[srd], s44 offen offset:1024 sc1\n\t" \
  "buffer_load_dwordx4 a[56:59], %[vl16], %[srd], s44 offen offset:2048 sc1\n\t" \
  "buffer_load_dwordx4 a[60:63], %[vl16], %[srd], s44 offen offset:3072 sc1\n\t" \
  "s_add_u32 s44, %[sW1], 16384\n\t" \
  "buffer_load_dwordx4 a[64:67], %[vl16], %[srd], s44 offen offset:0 sc1\n\t" \
  "buffer_load_dwordx4 a[68:71], %[vl16], %[srd], s44 offen offset:1024 sc1\n\t" \
  "buffer_load_dwordx4 a[72:75], %[vl16], %[srd], s44 offen offset:2048 sc1\n\t" \
  "buffer_load_dwordx4 a[76:79], %[vl16], %[srd], s44 offen offset:3072 sc1\n\t" \
  "s_add_u32 s44, %[sW1], 20480\n\t" \
  "buffer_load_dwordx4 a[80:83], %[vl16], %[srd], s44 offen offset:0 sc1\n\t" \
  "buffer_load_dwordx4 a[84:87], %[vl16], %[srd], s44 offen offset:1024 sc1\n\t" \
  "buffer_load_dwordx4 a[88:91], %[vl16], %[srd], s44 offen offset:2048 sc1\n\t" \
  "buffer_load_dwordx4 a[92:95], %[vl16], %[srd], s44 offen offset:3072 sc1\n\t" \
  "s_add_u32 s44, %[sW1], 24576\n\t" \
  "buffer_load_dwordx4 a[96:99], %[vl16], %[srd], s44 offen offset:0 sc1\n\t" \
  "buffer_load_dwordx4 a[100:103], %[vl16], %[srd], s44 offen offset:1024 sc1\n\t" \
  "buffer_load_dwordx4 a[104:107], %[vl16], %[srd], s44 offen offset:2048 sc1\n\t" \
  "buffer_load_dwordx4 a[108:111], %[vl16], %[srd], s44 offen offset:3072 sc1\n\t" \
  "s_add_u32 s44, %[sW1], 28672\n\t" \
  "buffer_load_dwordx4 a[112:115], %[vl16], %[srd], s44 offen offset:0 sc1\n\t" \
  "buffer_load_dwordx4 a[116:119], %[vl16], %[srd], s44 offen offset:1024 sc1\n\t" \
  "buffer_load_dwordx4 a[120:123], %[vl16], %[srd], s44 offen offset:2048 sc1\n\t" \
  "buffer_load_dwordx4 a[124:127], %[vl16], %[srd], s44 offen offset:3072 sc1\n\t" \
  "s_add_u32 s44, %[sW1], 32768\n\t" \
  "buffer_load_dwordx4 a[128:131], %[vl16], %[srd], s44 offen offset:0 sc1\n\t" \
  "buffer_load_dwordx4 a[132:135], %[vl16], %[srd], s44 offen offset:1024 sc1\n\t" \
  "buffer_load_dwordx4 a[136:139], %[vl16], %[srd], s44 offen offset:2048 sc1\n\t" \
  "buffer_load_dwordx4 a[140:143], %[vl16], %[srd], s44 offen offset:3072 sc1\n\t" \
  "s_add_u32 s44, %[sW1], 36864\n\t" \
  "buffer_load_dwordx4 a[144:147], %[vl16], %[srd], s44 offen offset:0 sc1\n\t" \
  "buffer_load_dwordx4 a[148:151], %[vl16], %[srd], s44 offen offset:1024 sc1\n\t" \
  "buffer_load_dwordx4 a[152:155], %[vl16], %[srd], s44 offen offset:2048 sc1\n\t" \
  "buffer_load_dwordx4 a[156:159], %[vl16], %[srd], s44 offen offset:3072 sc1\n\t" \
  "s_add_u32 s44, %[sW1], 40960\n\t" \
  "buffer_load_dwordx4 a[160:163], %[vl16], %[srd], s44 offen offset:0 sc1\n\t" \
  "buffer_load_dwordx4 a[164:167], %[vl16], %[srd], s44 offen offset:1024 sc1\n\t" \
        :: [srd] "s"(srd), [sW1] "s"(OFF_W1F + (j * 4 + w) * 61440u), [vl16] "v"(lane * 16) : "memory", "scc", \
"a0", "a1", "a2", "a3", "a4", "a5", "a6", "a7", "a8", "a9", "a10", "a11", "a12", "a13", "a14", "a15", "a16", "a17", "a18", "a19", "a20", "a21", "a22", "a23", "a24", "a25", "a26", "a27", "a28", "a29", "a30", "a31", "a32", "a33", "a34", "a35", "a36", "a37", "a38", "a39", "a40", "a41", "a42", "a43", "a44", "a45", "a46", "a47", "a48", "a49", "a50", "a51", "a52", "a53", "a54", "a55", "a56", "a57", "a58", "a59", "a60", "a61", "a62", "a63", "a64", "a65", "a66", "a67", "a68", "a69", "a70", "a71", "a72", "a73", "a74", "a75", "a76", "a77", "a78", "a79", "a80", "a81", "a82", "a83", "a84", "a85", "a86", "a87", "a88", "a89", "a90", "a91", "a92", "a93", "a94", "a95", "a96", "a97", "a98", "a99", "a100", "a101", "a102", "a103", "a104", "a105", "a106", "a107", "a108", "a109", "a110", "a111", "a112", "a113", "a114", "a115", "a116", "a117", "a118", "a119", "a120", "a121", "a122", "a123", "a124", "a125", "a126", "a127", "a128", "a129", "a130", "a131", "a132", "a133", "a134", "a135", "a136", "a137", "a138", "a139", "a140", "a141", "a142", "a143", "a144", "a145", "a146", "a147", "a148", "a149", "a150", "a151", "a152", "a153", "a154", "a155", "a156", "a157", "a158", "a159", "a160", "a161", "a162", "a163", "a164", "a165", "a166", "a167", "s44" \
      )
#define GEMV_UNIT(u_, lu_, younger_) asm volatile( \
  "s_nop 4\n\t" \
  "v_lshlrev_b32 v176, 1, %[vl]\n\t" \
  "v_add_u32 v176, %[lu], v176\n\t" \
  "v_mov_b32 v96, 0\n\t" \
  "v_mov_b32 v97, 0\n\t" \
  "v_mov_b32 v98, 0\n\t" \
  "s_waitcnt vmcnt(%c[wa])\n\t" \
  "ds_read_u16_d16_hi v96, v176 offset:20480\n\t" \
  "ds_read_u16_d16_hi v97, v176 offset:20608\n\t" \
  "ds_read_u16_d16_hi v98, v176 offset:20736\n\t" \
  "s_waitcnt lgkmcnt(2)\n\t" \
  "v_mul_f32 v177, 0xbfb8aa3b, v96\n\t" \
  "s_waitcnt lgkmcnt(1)\n\t" \
  "v_mul_f32 v178, 0xbfb8aa3b, v97\n\t" \
  "s_waitcnt lgkmcnt(0)\n\t" \
  "v_mul_f32 v179, 0xbfb8aa3b, v98\n\t" \
  "v_exp_f32 v177, v177\n\t" \
  "v_exp_f32 v178, v178\n\t" \
  "v_exp_f32 v179, v179\n\t" \
  "v_add_f32 v177, 1.0, v177\n\t" \
  "v_add_f32 v178, 1.0, v178\n\t" \
  "v_add_f32 v179, 1.0, v179\n\t" \
  "v_rcp_f32 v177, v177\n\t" \
  "v_rcp_f32 v178, v178\n\t" \
  "v_rcp_f32 v179, v179\n\t" \
  "v_mul_f32 v177, v177, v96\n\t" \
  "v_mul_f32 v178, v178, v97\n\t" \
  "v_mul_f32 v179, v179, v98\n\t" \
  "v_cvt_pk_bf16_f32 v177, v177, v177\n\t" \
  "v_cvt_pk_bf16_f32 v178, v178, v178\n\t" \
  "v_cvt_pk_bf16_f32 v179, v179, v179\n\t" \
  "ds_write_b16 v176, v177 offset:21504\n\t" \
  "ds_write_b16 v176, v178 offset:21632\n\t" \
  "ds_write_b16 v176, v179 offset:21760\n\t" \
  "v_lshrrev_b32 v180, 4, %[vl]\n\t" \
  "v_lshlrev_b32 v180, 3, v180\n\t" \
  "v_add_u32 v180, %[lu], v180\n\t" \
  "ds_read_b64 v[100:101], v180 offset:21504\n\t" \
  "ds_read_b64 v[102:103], v180 offset:21536\n\t" \
  "ds_read_b64 v[104:105], v180 offset:21568\n\t" \
  "ds_read_b64 v[106:107], v180 offset:21600\n\t" \
  "ds_read_b64 v[108:109], v180 offset:21632\n\t" \
  "ds_read_b64 v[110:111], v180 offset:21664\n\t" \
  "ds_read_b64 v[112:113], v180 offset:21696\n\t" \
  "ds_read_b64 v[114:115], v180 offset:21728\n\t" \
  "ds_read_b64 v[116:117], v180 offset:21760\n\t" \
  "ds_read_b64 v[118:119], v180 offset:21792\n\t" \
  "v_xor_b32 v181, 32, %[vtm]\n\t" \
  "v_xor_b32 v182, 64, %[vtm]\n\t" \
  "v_xor_b32 v183, 96, %[vtm]\n\t" \
  "s_waitcnt vmcnt(%c[wb])\n\t" \
  "ds_read_b64_tr_b16 v[120:121], %[vtm] offset:0\n\t" \
  "ds_read_b64_tr_b16 v[122:123], %[vtm] offset:2048\n\t" \
  "ds_read_b64_tr_b16 v[124:125], v181 offset:0\n\t" \
  "ds_read_b64_tr_b16 v[126:127], v181 offset:2048\n\t" \
  "ds_read_b64_tr_b16 v[128:129], v182 offset:0\n\t" \
  "ds_read_b64_tr_b16 v[130:131], v182 offset:2048\n\t" \
  "ds_read_b64_tr_b16 v[132:133], v183 offset:0\n\t" \
  "ds_read_b64_tr_b16 v[134:135], v183 offset:2048\n\t" \
  "ds_read_b64_tr_b16 v[136:137], %[vtm] offset:4096\n\t" \
  "ds_read_b64_tr_b16 v[138:139], %[vtm] offset:6144\n\t" \
  "ds_read_b64_tr_b16 v[140:141], v181 offset:4096\n\t" \
  "ds_read_b64_tr_b16 v[142:143], v181 offset:6144\n\t" \
  "ds_read_b64_tr_b16 v[144:145], v182 offset:4096\n\t" \
  "ds_read_b64_tr_b16 v[146:147], v182 offset:6144\n\t" \
  "ds_read_b64_tr_b16 v[148:149], v183 offset:4096\n\t" \
  "ds_read_b64_tr_b16 v[150:151], v183 offset:6144\n\t" \
  "s_waitcnt lgkmcnt(14)\n\t" \
  "v_mfma_f32_16x16x32_bf16 v[152:155], v[100:103], v[120:123], 0\n\t" \
  "s_waitcnt lgkmcnt(12)\n\t" \
  "v_mfma_f32_16x16x32_bf16 v[156:159], v[100:103], v[124:127], 0\n\t" \
  "s_waitcnt lgkmcnt(10)\n\t" \
  "v_mfma_f32_16x16x32_bf16 v[160:163], v[100:103], v[128:131], 0\n\t" \
  "s_waitcnt lgkmcnt(8)\n\t" \
  "v_mfma_f32_16x16x32_bf16 v[164:167], v[100:103], v[132:135], 0\n\t" \
  "ds_read_b64_tr_b16 v[120:121], %[vtm] offset:8192\n\t" \
  "ds_read_b64_tr_b16 v[122:123], %[vtm] offset:10240\n\t" \
  "ds_read_b64_tr_b16 v[124:125], v181 offset:8192\n\t" \
  "ds_read_b64_tr_b16 v[126:127], v181 offset:10240\n\t" \
  "ds_read_b64_tr_b16 v[128:129], v182 offset:8192\n\t" \
  "ds_read_b64_tr_b16 v[130:131], v182 offset:10240\n\t" \
  "ds_read_b64_tr_b16 v[132:133], v183 offset:8192\n\t" \
  "ds_read_b64_tr_b16 v[134:135], v183 offset:10240\n\t" \
  "s_waitcnt lgkmcnt(14)\n\t" \
  "v_mfma_f32_16x16x32_bf16 v[152:155], v[104:107], v[136:139], v[152:155]\n\t" \
  "s_waitcnt lgkmcnt(12)\n\t" \
  "v_mfma_f32_16x16x32_bf16 v[156:159], v[104:107], v[140:143], v[156:159]\n\t" \
  "s_waitcnt lgkmcnt(10)\n\t" \
  "v_mfma_f32_16x16x32_bf16 v[160:163], v[104:107], v[144:147], v[160:163]\n\t" \
  "s_waitcnt lgkmcnt(8)\n\t" \
  "v_mfma_f32_16x16x32_bf16 v[164:167], v[104:107], v[148:151], v[164:167]\n\t" \
  "ds_read_b64_tr_b16 v[136:137], %[vtm] offset:12288\n\t" \
  "ds_read_b64_tr_b16 v[138:139], %[vtm] offset:14336\n\t" \
  "ds_read_b64_tr_b16 v[140:141], v181 offset:12288\n\t" \
  "ds_read_b64_tr_b16 v[142:143], v181 offset:14336\n\t" \
  "ds_read_b64_tr_b16 v[144:145], v182 offset:12288\n\t" \
  "ds_read_b64_tr_b16 v[146:147], v182 offset:14336\n\t" \
  "ds_read_b64_tr_b16 v[148:149], v183 offset:12288\n\t" \
  "ds_read_b64_tr_b16 v[150:151], v183 offset:14336\n\t" \
  "s_waitcnt lgkmcnt(14)\n\t" \
  "v_mfma_f32_16x16x32_bf16 v[152:155], v[108:111], v[120:123], v[152:155]\n\t" \
  "s_waitcnt lgkmcnt(12)\n\t" \
  "v_mfma_f32_16x16x32_bf16 v[156:159], v[108:111], v[124:127], v[156:159]\n\t" \
  "s_waitcnt lgkmcnt(10)\n\t" \
  "v_mfma_f32_16x16x32_bf16 v[160:163], v[108:111], v[128:131], v[160:163]\n\t" \
  "s_waitcnt lgkmcnt(8)\n\t" \
  "v_mfma_f32_16x16x32_bf16 v[164:167], v[108:111], v[132:135], v[164:167]\n\t" \
  "ds_read_b64_tr_b16 v[120:121], %[vtm] offset:16384\n\t" \
  "ds_read_b64_tr_b16 v[122:123], %[vtm] offset:18432\n\t" \
  "ds_read_b64_tr_b16 v[124:125], v181 offset:16384\n\t" \
  "ds_read_b64_tr_b16 v[126:127], v181 offset:18432\n\t" \
  "ds_read_b64_tr_b16 v[128:129], v182 offset:16384\n\t" \
  "ds_read_b64_tr_b16 v[130:131], v182 offset:18432\n\t" \
  "ds_read_b64_tr_b16 v[132:133], v183 offset:16384\n\t" \
  "ds_read_b64_tr_b16 v[134:135], v183 offset:18432\n\t" \
  "s_waitcnt lgkmcnt(14)\n\t" \
  "v_mfma_f32_16x16x32_bf16 v[152:155], v[112:115], v[136:139], v[152:155]\n\t" \
  "s_waitcnt lgkmcnt(12)\n\t" \
  "v_mfma_f32_16x16x32_bf16 v[156:159], v[112:115], v[140:143], v[156:159]\n\t" \
  "s_waitcnt lgkmcnt(10)\n\t" \
  "v_mfma_f32_16x16x32_bf16 v[160:163], v[112:115], v[144:147], v[160:163]\n\t" \
  "s_waitcnt lgkmcnt(8)\n\t" \
  "v_mfma_f32_16x16x32_bf16 v[164:167], v[112:115], v[148:151], v[164:167]\n\t" \
  "s_waitcnt lgkmcnt(6)\n\t" \
  "v_mfma_f32_16x16x32_bf16 v[152:155], v[116:119], v[120:123], v[152:155]\n\t" \
  "s_waitcnt lgkmcnt(4)\n\t" \
  "v_mfma_f32_16x16x32_bf16 v[156:159], v[116:119], v[124:127], v[156:159]\n\t" \
  "s_waitcnt lgkmcnt(2)\n\t" \
  "v_mfma_f32_16x16x32_bf16 v[160:163], v[116:119], v[128:131], v[160:163]\n\t" \
  "s_waitcnt lgkmcnt(0)\n\t" \
  "v_mfma_f32_16x16x32_bf16 v[164:167], v[116:119], v[132:135], v[164:167]\n\t" \
  "v_lshlrev_b32 v176, 2, %[vl]\n\t" \
  "s_nop 2\n\t" \
  "v_mov_b32_dpp v152, v156 quad_perm:[0,1,2,3] row_mask:0x2 bank_mask:0xf\n\t" \
  "s_nop 1\n\t" \
  "v_mov_b32_dpp v152, v160 quad_perm:[0,1,2,3] row_mask:0x4 bank_mask:0xf\n\t" \
  "s_nop 1\n\t" \
  "v_mov_b32_dpp v152, v164 quad_perm:[0,1,2,3] row_mask:0x8 bank_mask:0xf\n\t" \
  "buffer_store_dword v152, v176, %[srd], %[sMo] offen sc1\n\t" \
  "s_nop 1\n\t" \
        :: [srd] "s"(srd), [lu] "s"(lu_), [wa] "i"((younger_) + 20), [wb] "i"(younger_), [sMo] "s"(OFF_MODP + U_KQ(u_) * 15360u + U_CG(u_) * 256u), [vl] "v"(lane), \
           [vtm] "v"((lu_) + (lane >> 2) * 128 + ((lane >> 3) & 3) * 32 + (lane & 3) * 8) : "memory", "vcc", "scc", \
"v96", "v97", "v98", "v100", "v101", "v102", "v103", "v104", "v105", "v106", "v107", "v108", "v109", "v110", "v111", "v112", "v113", "v114", "v115", "v116", "v117", "v118", "v119", "v120", "v121", "v122", "v123", "v124", "v125", "v126", "v127", "v128", "v129", "v130", "v131", "v132", "v133", "v134", "v135", "v136", "v137", "v138", "v139", "v140", "v141", "v142", "v143", "v144", "v145", "v146", "v147", "v148", "v149", "v150", "v151", "v152", "v153", "v154", "v155", "v156", "v157", "v158", "v159", "v160", "v161", "v162", "v163", "v164", "v165", "v166", "v167", "v176", "v177", "v178", "v179", "v180", "v181", "v182", "v183" \
      )
  auto stash = [&]() {
    const unsigned vpub = role == 0 ? (tid < 8 * 8 ? (0 ? OFF_FX1 : OFF_FX) + ((tid >> 3) * 160 + cb) * 128 + (tid & 7) * 16 : OOB) : tid < 8 ? (role == 1 ? OFF_FU + w1 * 128 : OFF_FW2 + w2 * 128) + tid * 16 : OOB;
    const unsigned vpw = lane < 4 ? OFF_FW1 + (j * 4 + lane) * 128 : lane < 8 ? OFF_FW2 + ((4 * w + (lane & 3)) * 2 + (ng >> 2)) * 128
                                                                              : OFF_FU + (lane < 12 ? j * 4 + (lane & 3) : (lane & 3) * 16) * 128;
    const unsigned pb = 32 * (lane >> 3) + 8 * w + (lane & 7);
    const unsigned vpx = lane < 40 && !(role == 0 && pb == cb) ? OFF_FX + (xcd % 8 * 160 + pb) * 128 : OFF_FR + min(lane - 40, 9u) * 128;
    const unsigned g = lane >> 4, q = (lane >> 2) & 3, pq = lane & 3;
    const unsigned row = 4 * g + q;
    const unsigned vtr = lb + row * 128 + ((row >> 1) & 3) * 32 + ((pq * 8) ^ ((row & 8) * 2));
    const unsigned vl16 = lane * 16;
    const unsigned vdma = OFF_X2P + p * 81920u + w * 4096u + lane * 16;
    const unsigned vh2st = lane < 48 ? OFF_H2F + (4 * p * 192 + 12 * j + 3 * w) * 256u + lane * 16 : OOB;
    const unsigned tk = 4 * j + w;
    const unsigned vstc = lb + lane * 128 + (((tk >> 4) ^ ((lane >> 1) & 3)) * 32) + (((tk & 15) * 2) ^ ((lane & 8) * 2));
    const unsigned vstw = lane == 63 ? tk * 64 : OOB;
    const unsigned vflw = lane < 8 ? OFF_FL + p * 32768u + (j * 4 + w) * 128u + lane * 16 : OOB;
    const unsigned vep = b == 0 && tid == 0 ? OFF_EP : OOB;
    const unsigned rs_ = 2 * (w & 1) + sub, rb = lb + (w >> 1) * 81920 + 80 * ng * 128;
    const unsigned r0 = 8 * g + q, r1 = 8 * g + 4 + q, r2 = 64 + 4 * g + q;
    const unsigned vres0 = rb + r0 * 128 + ((rs_ ^ ((r0 >> 1) & 3)) * 32) + ((pq * 8) ^ ((r0 & 8) * 2));
    const unsigned vres1 = rb + r1 * 128 + ((rs_ ^ ((r1 >> 1) & 3)) * 32) + ((pq * 8) ^ ((r1 & 8) * 2));
    const unsigned vres2 = rb + r2 * 128 + ((rs_ ^ ((r2 >> 1) & 3)) * 32) + ((pq * 8) ^ ((r2 & 8) * 2));
    const unsigned vwr = lb + w * 20480 + g * 256 + ((lane + 4 * g) & 15) * 16;
    const unsigned tq = lane < 32 ? q : (lane >> 1) & 3, tl = 4 * w + tq, g0 = lane < 32 ? pq : 2 * (lane & 1), g1 = lane < 32 ? pq : g0 + 1;
    const unsigned vrd0 = lb + (lane < 32 ? g * 2048 : 4096) + g0 * 256 + ((tl + 4 * g0) & 15) * 16;
    const unsigned vrd1 = lb + (lane < 32 ? g * 2048 + 1024 : 4096) + g1 * 256 + ((tl + 4 * g1) & 15) * 16;
    const unsigned vh1w = lane < 40 ? OFF_FX1 + (xcd % 8 * 160 + pb) * 128 : OFF_FU + ((lane < 44 ? j * 4 : 0) + (lane & 3)) * 128;
    const unsigned vgb = OFF_GB - OFF_RT + (lane < 32 ? 32 * g + 8 * pq : 64 + 8 * (lane & 1)) * 4;
    const unsigned vout = lane < 40 ? ((64 * p + 16 * sub + tl) * D_ + 80 * ng + (lane < 32 ? 32 * g + 8 * pq : 64 + 8 * (lane & 1))) * 2 : OOB;
    *(u32x4*)(smem + L_STASH + 0 + tid * 16) = u32x4{vpub, vl16, vpw, vpx};
    *(u32x4*)(smem + L_STASH + 4096 + tid * 16) = u32x4{vdma, vtr, vstc, vstw};
    *(u32x4*)(smem + L_STASH + 8192 + tid * 16) = u32x4{vh2st, vflw, vres0, vres1};
    *(u32x4*)(smem + L_STASH + 12288 + tid * 16) = u32x4{vres2, vwr, vrd0, vrd1};
    *(u32x4*)(smem + L_STASH + 16384 + tid * 16) = u32x4{vout, vgb, vep, vh1w};
    FENCE();
  };
  unsigned c_vf2;
  unsigned c_vf1, c_vp0;
  u64 pmask = 0;
  unsigned c_vf3;
  unsigned c_vfw;
  unsigned c_va;
  unsigned c_vb0;
  unsigned c_vb1;
  unsigned c_vb2;
  unsigned c_vb3;
  unsigned c_vx;
  unsigned c_vs;
  unsigned c_vs2;
  unsigned c_vo;
  u32x4 c_vd;
  unsigned c_vl16;
  unsigned c_vkv;
  unsigned c_vmod;
  u64 tag;
  if (__builtin_expect(role == 0, 1)) {
    const rsrc_t rf = mkrsrc(ssmk, S_ * D_ * 2);
    const unsigned d0 = 128 * (cb >> 5) + 2 * lane;
    u32x4 fa[4];
    const u32x4 taps = *(const u32x4*)(shk + d0 * 4);
    dval = ssmD[8 * (cb >> 1) + 2 * w + (cb & 1)];
    FENCE();
    __builtin_amdgcn_raw_buffer_store_b128(u32x4{0, 0, 0, 0}, srd, tid < 64 && cb < 128 ? OFF_ST2 + cb * 1024 + tid * 16 : OOB, 0, SC1);
    FENCE();
#pragma unroll
    for (int n = 0; n < 4; ++n) fa[n] = __builtin_amdgcn_raw_buffer_load_b128(rf, lane * 16 + n * 1024, (8 * (cb >> 1) + 2 * w + (cb & 1)) * 4096u, NT);
    u32x4 xr[5];
    const bool isx = cb < 128;
    const rsrc_t rx = mkrsrc(isx ? x : wmod, isx ? S_ * D_ * 2 : D_ * 3840 * 2);
#pragma unroll
    for (int n = 0; n < 5; ++n)
      xr[n] = __builtin_amdgcn_raw_buffer_load_b128(rx, isx ? (lane >> 3) * 1280 + (lane & 7) * 16 : vm,
                                                    isx ? cb * 20480u + (w & 1) * 10240u + (w >> 1) * 128u + n * 256u : U_SRC(cb) + (4 * n + w) * 61440u, NT);
    const u32x4 cr = __builtin_amdgcn_raw_buffer_load_b128(mkrsrc(cIn, isx ? 0 : D_ * 2), lane * 16, U_KQ(cb) * 320u, 0);
    FENCE();
    tag = load_tag(ws);
    if (w < 2) *(u32x4*)(smem + L_KV + tid * 16) = zero4;
    if (tid == 128) *(u32x4*)(smem + L_CNT) = zero4;
    FENCE();
    asm volatile("s_waitcnt vmcnt(11)" ::: "memory");
    asm volatile(
  "s_nop 1\n\t"
  "ds_read2st64_b32 v[112:113], %[la] offset0:0 offset1:1\n\t"
  "ds_read2st64_b32 v[114:115], %[la] offset0:2 offset1:3\n\t"
  "ds_read2st64_b32 v[116:117], %[la] offset0:4 offset1:5\n\t"
  "ds_read2st64_b32 v[118:119], %[la] offset0:6 offset1:7\n\t"
  "ds_read2st64_b32 v[120:121], %[la] offset0:8 offset1:9\n\t"
  "ds_read2st64_b32 v[122:123], %[la] offset0:10 offset1:11\n\t"
  "ds_read2st64_b32 v[132:133], %[la] offset0:20 offset1:21\n\t"
  "ds_read2st64_b32 v[134:135], %[la] offset0:22 offset1:23\n\t"
  "ds_read2st64_b32 v[136:137], %[la] offset0:24 offset1:25\n\t"
  "ds_read2st64_b32 v[138:139], %[la] offset0:26 offset1:27\n\t"
  "ds_read2st64_b32 v[124:125], %[la] offset0:12 offset1:13\n\t"
  "ds_read2st64_b32 v[126:127], %[la] offset0:14 offset1:15\n\t"
  "ds_read2st64_b32 v[128:129], %[la] offset0:16 offset1:17\n\t"
  "ds_read2st64_b32 v[130:131], %[la] offset0:18 offset1:19\n\t"
  "ds_read2st64_b32 v[140:141], %[la] offset0:28 offset1:29\n\t"
  "ds_read2st64_b32 v[142:143], %[la] offset0:30 offset1:31\n\t"
  "ds_read2st64_b32 v[144:145], %[la] offset0:32 offset1:33\n\t"
  "ds_read2st64_b32 v[146:147], %[la] offset0:34 offset1:35\n\t"
  "v_lshlrev_b32 v186, 16, %[t0]\n\t"
  "v_lshlrev_b32 v187, 16, %[t2]\n\t"
  "v_and_b32 v188, 0xffff0000, %[t0]\n\t"
  "v_and_b32 v189, 0xffff0000, %[t2]\n\t"
  "v_lshlrev_b32 v190, 16, %[t1]\n\t"
  "v_lshlrev_b32 v191, 16, %[t3]\n\t"
  "v_and_b32 v192, 0xffff0000, %[t1]\n\t"
  "v_and_b32 v193, 0xffff0000, %[t3]\n\t"
  "v_mov_b32 v234, 0x3f803f80\n\t"
  "s_waitcnt lgkmcnt(15)\n\t"
  "v_lshlrev_b32 v148, 16, v113\n\t"
  "v_and_b32 v149, 0xffff0000, v113\n\t"
  "v_lshlrev_b32 v150, 16, v114\n\t"
  "v_and_b32 v151, 0xffff0000, v114\n\t"
  "v_lshlrev_b32 v152, 16, v115\n\t"
  "v_and_b32 v153, 0xffff0000, v115\n\t"
  "v_lshlrev_b32 v154, 16, v116\n\t"
  "v_and_b32 v155, 0xffff0000, v116\n\t"
  "v_lshlrev_b32 v156, 16, v117\n\t"
  "v_and_b32 v157, 0xffff0000, v117\n\t"
  "s_waitcnt lgkmcnt(14)\n\t"
  "v_lshlrev_b32 v158, 16, v118\n\t"
  "v_and_b32 v159, 0xffff0000, v118\n\t"
  "v_lshlrev_b32 v160, 16, v119\n\t"
  "v_and_b32 v161, 0xffff0000, v119\n\t"
  "s_waitcnt lgkmcnt(13)\n\t"
  "v_lshlrev_b32 v162, 16, v120\n\t"
  "v_and_b32 v163, 0xffff0000, v120\n\t"
  "v_lshlrev_b32 v164, 16, v121\n\t"
  "v_and_b32 v165, 0xffff0000, v121\n\t"
  "s_waitcnt lgkmcnt(12)\n\t"
  "v_lshlrev_b32 v166, 16, v122\n\t"
  "v_and_b32 v167, 0xffff0000, v122\n\t"
  "v_lshlrev_b32 v168, 16, v123\n\t"
  "v_and_b32 v169, 0xffff0000, v123\n\t"
  "v_pk_mul_f32 v[194:195], v[148:149], v[192:193]\n\t"
  "v_pk_mul_f32 v[196:197], v[150:151], v[192:193]\n\t"
  "v_pk_mul_f32 v[198:199], v[152:153], v[192:193]\n\t"
  "v_pk_mul_f32 v[200:201], v[154:155], v[192:193]\n\t"
  "v_pk_mul_f32 v[202:203], v[156:157], v[192:193]\n\t"
  "v_pk_mul_f32 v[204:205], v[158:159], v[192:193]\n\t"
  "v_pk_mul_f32 v[206:207], v[160:161], v[192:193]\n\t"
  "v_pk_mul_f32 v[208:209], v[162:163], v[192:193]\n\t"
  "v_pk_mul_f32 v[210:211], v[150:151], v[190:191]\n\t"
  "v_pk_mul_f32 v[212:213], v[152:153], v[190:191]\n\t"
  "v_pk_mul_f32 v[214:215], v[154:155], v[190:191]\n\t"
  "v_pk_mul_f32 v[216:217], v[156:157], v[190:191]\n\t"
  "v_pk_mul_f32 v[218:219], v[158:159], v[190:191]\n\t"
  "v_pk_mul_f32 v[220:221], v[160:161], v[190:191]\n\t"
  "v_pk_mul_f32 v[222:223], v[162:163], v[190:191]\n\t"
  "v_pk_mul_f32 v[224:225], v[164:165], v[190:191]\n\t"
  "v_cvt_pk_bf16_f32 v194, v194, v210\n\t"
  "v_cvt_pk_bf16_f32 v195, v195, v211\n\t"
  "v_cvt_pk_bf16_f32 v196, v196, v212\n\t"
  "v_cvt_pk_bf16_f32 v197, v197, v213\n\t"
  "v_cvt_pk_bf16_f32 v198, v198, v214\n\t"
  "v_cvt_pk_bf16_f32 v199, v199, v215\n\t"
  "v_cvt_pk_bf16_f32 v200, v200, v216\n\t"
  "v_cvt_pk_bf16_f32 v201, v201, v217\n\t"
  "v_cvt_pk_bf16_f32 v202, v202, v218\n\t"
  "v_cvt_pk_bf16_f32 v203, v203, v219\n\t"
  "v_cvt_pk_bf16_f32 v204, v204, v220\n\t"
  "v_cvt_pk_bf16_f32 v205, v205, v221\n\t"
  "v_cvt_pk_bf16_f32 v206, v206, v222\n\t"
  "v_cvt_pk_bf16_f32 v207, v207, v223\n\t"
  "v_cvt_pk_bf16_f32 v208, v208, v224\n\t"
  "v_cvt_pk_bf16_f32 v209, v209, v225\n\t"
  "v_dot2_f32_bf16 v194, v194, v234, 0\n\t"
  "v_dot2_f32_bf16 v195, v195, v234, 0\n\t"
  "v_dot2_f32_bf16 v196, v196, v234, 0\n\t"
  "v_dot2_f32_bf16 v197, v197, v234, 0\n\t"
  "v_dot2_f32_bf16 v198, v198, v234, 0\n\t"
  "v_dot2_f32_bf16 v199, v199, v234, 0\n\t"
  "v_dot2_f32_bf16 v200, v200, v234, 0\n\t"
  "v_dot2_f32_bf16 v201, v201, v234, 0\n\t"
  "v_dot2_f32_bf16 v202, v202, v234, 0\n\t"
  "v_dot2_f32_bf16 v203, v203, v234, 0\n\t"
  "v_dot2_f32_bf16 v204, v204, v234, 0\n\t"
  "v_dot2_f32_bf16 v205, v205, v234, 0\n\t"
  "v_dot2_f32_bf16 v206, v206, v234, 0\n\t"
  "v_dot2_f32_bf16 v207, v207, v234, 0\n\t"
  "v_dot2_f32_bf16 v208, v208, v234, 0\n\t"
  "v_dot2_f32_bf16 v209, v209, v234, 0\n\t"
  "v_pk_mul_f32 v[210:211], v[152:153], v[188:189]\n\t"
  "v_pk_mul_f32 v[212:213], v[154:155], v[188:189]\n\t"
  "v_pk_mul_f32 v[214:215], v[156:157], v[188:189]\n\t"
  "v_pk_mul_f32 v[216:217], v[158:159], v[188:189]\n\t"
  "v_pk_mul_f32 v[218:219], v[160:161], v[188:189]\n\t"
  "v_pk_mul_f32 v[220:221], v[162:163], v[188:189]\n\t"
  "v_pk_mul_f32 v[222:223], v[164:165], v[188:189]\n\t"
  "v_pk_mul_f32 v[224:225], v[166:167], v[188:189]\n\t"
  "v_cvt_pk_bf16_f32 v194, v194, v210\n\t"
  "v_cvt_pk_bf16_f32 v195, v195, v211\n\t"
  "v_cvt_pk_bf16_f32 v196, v196, v212\n\t"
  "v_cvt_pk_bf16_f32 v197, v197, v213\n\t"
  "v_cvt_pk_bf16_f32 v198, v198, v214\n\t"
  "v_cvt_pk_bf16_f32 v199, v199, v215\n\t"
  "v_cvt_pk_bf16_f32 v200, v200, v216\n\t"
  "v_cvt_pk_bf16_f32 v201, v201, v217\n\t"
  "v_cvt_pk_bf16_f32 v202, v202, v218\n\t"
  "v_cvt_pk_bf16_f32 v203, v203, v219\n\t"
  "v_cvt_pk_bf16_f32 v204, v204, v220\n\t"
  "v_cvt_pk_bf16_f32 v205, v205, v221\n\t"
  "v_cvt_pk_bf16_f32 v206, v206, v222\n\t"
  "v_cvt_pk_bf16_f32 v207, v207, v223\n\t"
  "v_cvt_pk_bf16_f32 v208, v208, v224\n\t"
  "v_cvt_pk_bf16_f32 v209, v209, v225\n\t"
  "v_dot2_f32_bf16 v194, v194, v234, 0\n\t"
  "v_dot2_f32_bf16 v195, v195, v234, 0\n\t"
  "v_dot2_f32_bf16 v196, v196, v234, 0\n\t"
  "v_dot2_f32_bf16 v197, v197, v234, 0\n\t"
  "v_dot2_f32_bf16 v198, v198, v234, 0\n\t"
  "v_dot2_f32_bf16 v199, v199, v234, 0\n\t"
  "v_dot2_f32_bf16 v200, v200, v234, 0\n\t"
  "v_dot2_f32_bf16 v201, v201, v234, 0\n\t"
  "v_dot2_f32_bf16 v202, v202, v234, 0\n\t"
  "v_dot2_f32_bf16 v203, v203, v234, 0\n\t"
  "v_dot2_f32_bf16 v204, v204, v234, 0\n\t"
  "v_dot2_f32_bf16 v205, v205, v234, 0\n\t"
  "v_dot2_f32_bf16 v206, v206, v234, 0\n\t"
  "v_dot2_f32_bf16 v207, v207, v234, 0\n\t"
  "v_dot2_f32_bf16 v208, v208, v234, 0\n\t"
  "v_dot2_f32_bf16 v209, v209, v234, 0\n\t"
  "v_pk_mul_f32 v[210:211], v[154:155], v[186:187]\n\t"
  "v_pk_mul_f32 v[212:213], v[156:157], v[186:187]\n\t"
  "v_pk_mul_f32 v[214:215], v[158:159], v[186:187]\n\t"
  "v_pk_mul_f32 v[216:217], v[160:161], v[186:187]\n\t"
  "v_pk_mul_f32 v[218:219], v[162:163], v[186:187]\n\t"
  "v_pk_mul_f32 v[220:221], v[164:165], v[186:187]\n\t"
  "v_pk_mul_f32 v[222:223], v[166:167], v[186:187]\n\t"
  "v_pk_mul_f32 v[224:225], v[168:169], v[186:187]\n\t"
  "v_cvt_pk_bf16_f32 v194, v194, v210\n\t"
  "v_cvt_pk_bf16_f32 v195, v195, v211\n\t"
  "v_cvt_pk_bf16_f32 v196, v196, v212\n\t"
  "v_cvt_pk_bf16_f32 v197, v197, v213\n\t"
  "v_cvt_pk_bf16_f32 v198, v198, v214\n\t"
  "v_cvt_pk_bf16_f32 v199, v199, v215\n\t"
  "v_cvt_pk_bf16_f32 v200, v200, v216\n\t"
  "v_cvt_pk_bf16_f32 v201, v201, v217\n\t"
  "v_cvt_pk_bf16_f32 v202, v202, v218\n\t"
  "v_cvt_pk_bf16_f32 v203, v203, v219\n\t"
  "v_cvt_pk_bf16_f32 v204, v204, v220\n\t"
  "v_cvt_pk_bf16_f32 v205, v205, v221\n\t"
  "v_cvt_pk_bf16_f32 v206, v206, v222\n\t"
  "v_cvt_pk_bf16_f32 v207, v207, v223\n\t"
  "v_cvt_pk_bf16_f32 v208, v208, v224\n\t"
  "v_cvt_pk_bf16_f32 v209, v209, v225\n\t"
  "v_dot2_f32_bf16 v194, v194, v234, 0\n\t"
  "v_dot2_f32_bf16 v195, v195, v234, 0\n\t"
  "v_dot2_f32_bf16 v196, v196, v234, 0\n\t"
  "v_dot2_f32_bf16 v197, v197, v234, 0\n\t"
  "v_dot2_f32_bf16 v198, v198, v234, 0\n\t"
  "v_dot2_f32_bf16 v199, v199, v234, 0\n\t"
  "v_dot2_f32_bf16 v200, v200, v234, 0\n\t"
  "v_dot2_f32_bf16 v201, v201, v234, 0\n\t"
  "v_dot2_f32_bf16 v202, v202, v234, 0\n\t"
  "v_dot2_f32_bf16 v203, v203, v234, 0\n\t"
  "v_dot2_f32_bf16 v204, v204, v234, 0\n\t"
  "v_dot2_f32_bf16 v205, v205, v234, 0\n\t"
  "v_dot2_f32_bf16 v206, v206, v234, 0\n\t"
  "v_dot2_f32_bf16 v207, v207, v234, 0\n\t"
  "v_dot2_f32_bf16 v208, v208, v234, 0\n\t"
  "v_dot2_f32_bf16 v209, v209, v234, 0\n\t"
  "s_waitcnt lgkmcnt(11)\n\t"
  "v_and_b32 v210, 0xffff, v132\n\t"
  "v_and_b32 v211, 0xffff0000, v132\n\t"
  "v_and_b32 v212, 0xffff, v133\n\t"
  "v_and_b32 v213, 0xffff0000, v133\n\t"
  "s_waitcnt lgkmcnt(10)\n\t"
  "v_and_b32 v214, 0xffff, v134\n\t"
  "v_and_b32 v215, 0xffff0000, v134\n\t"
  "v_and_b32 v216, 0xffff, v135\n\t"
  "v_and_b32 v217, 0xffff0000, v135\n\t"
  "s_waitcnt lgkmcnt(9)\n\t"
  "v_and_b32 v218, 0xffff, v136\n\t"
  "v_and_b32 v219, 0xffff0000, v136\n\t"
  "v_and_b32 v220, 0xffff, v137\n\t"
  "v_and_b32 v221, 0xffff0000, v137\n\t"
  "s_waitcnt lgkmcnt(8)\n\t"
  "v_and_b32 v222, 0xffff, v138\n\t"
  "v_and_b32 v223, 0xffff0000, v138\n\t"
  "v_and_b32 v224, 0xffff, v139\n\t"
  "v_and_b32 v225, 0xffff0000, v139\n\t"
  "v_cvt_pk_bf16_f32 v194, v194, v195\n\t"
  "v_cvt_pk_bf16_f32 v196, v196, v197\n\t"
  "v_cvt_pk_bf16_f32 v198, v198, v199\n\t"
  "v_cvt_pk_bf16_f32 v200, v200, v201\n\t"
  "v_cvt_pk_bf16_f32 v202, v202, v203\n\t"
  "v_cvt_pk_bf16_f32 v204, v204, v205\n\t"
  "v_cvt_pk_bf16_f32 v206, v206, v207\n\t"
  "v_cvt_pk_bf16_f32 v208, v208, v209\n\t"
  "v_dot2_f32_bf16 v210, v194, v210, 0\n\t"
  "v_dot2_f32_bf16 v211, v194, v211, 0\n\t"
  "v_dot2_f32_bf16 v212, v196, v212, 0\n\t"
  "v_dot2_f32_bf16 v213, v196, v213, 0\n\t"
  "v_dot2_f32_bf16 v214, v198, v214, 0\n\t"
  "v_dot2_f32_bf16 v215, v198, v215, 0\n\t"
  "v_dot2_f32_bf16 v216, v200, v216, 0\n\t"
  "v_dot2_f32_bf16 v217, v200, v217, 0\n\t"
  "v_dot2_f32_bf16 v218, v202, v218, 0\n\t"
  "v_dot2_f32_bf16 v219, v202, v219, 0\n\t"
  "v_dot2_f32_bf16 v220, v204, v220, 0\n\t"
  "v_dot2_f32_bf16 v221, v204, v221, 0\n\t"
  "v_dot2_f32_bf16 v222, v206, v222, 0\n\t"
  "v_dot2_f32_bf16 v223, v206, v223, 0\n\t"
  "v_dot2_f32_bf16 v224, v208, v224, 0\n\t"
  "v_dot2_f32_bf16 v225, v208, v225, 0\n\t"
  "v_cvt_pk_bf16_f32 v229, v212, v210\n\t"
  "v_cvt_pk_bf16_f32 v228, v216, v214\n\t"
  "v_cvt_pk_bf16_f32 v227, v220, v218\n\t"
  "v_cvt_pk_bf16_f32 v226, v224, v222\n\t"
  "buffer_store_dwordx4 v[226:229], %[vo], %[srd], 0 offen offset:0 sc1\n\t"
  "v_cvt_pk_bf16_f32 v233, v213, v211\n\t"
  "v_cvt_pk_bf16_f32 v232, v217, v215\n\t"
  "v_cvt_pk_bf16_f32 v231, v221, v219\n\t"
  "v_cvt_pk_bf16_f32 v230, v225, v223\n\t"
  "buffer_store_dwordx4 v[230:233], %[vo], %[srd], 0 offen offset:128 sc1\n\t"
  "s_waitcnt lgkmcnt(7)\n\t"
  "v_lshlrev_b32 v170, 16, v124\n\t"
  "v_and_b32 v171, 0xffff0000, v124\n\t"
  "v_lshlrev_b32 v172, 16, v125\n\t"
  "v_and_b32 v173, 0xffff0000, v125\n\t"
  "s_waitcnt lgkmcnt(6)\n\t"
  "v_lshlrev_b32 v174, 16, v126\n\t"
  "v_and_b32 v175, 0xffff0000, v126\n\t"
  "v_lshlrev_b32 v176, 16, v127\n\t"
  "v_and_b32 v177, 0xffff0000, v127\n\t"
  "s_waitcnt lgkmcnt(5)\n\t"
  "v_lshlrev_b32 v178, 16, v128\n\t"
  "v_and_b32 v179, 0xffff0000, v128\n\t"
  "v_lshlrev_b32 v180, 16, v129\n\t"
  "v_and_b32 v181, 0xffff0000, v129\n\t"
  "s_waitcnt lgkmcnt(4)\n\t"
  "v_lshlrev_b32 v182, 16, v130\n\t"
  "v_and_b32 v183, 0xffff0000, v130\n\t"
  "v_lshlrev_b32 v184, 16, v131\n\t"
  "v_and_b32 v185, 0xffff0000, v131\n\t"
  "v_pk_mul_f32 v[194:195], v[164:165], v[192:193]\n\t"
  "v_pk_mul_f32 v[196:197], v[166:167], v[192:193]\n\t"
  "v_pk_mul_f32 v[198:199], v[168:169], v[192:193]\n\t"
  "v_pk_mul_f32 v[200:201], v[170:171], v[192:193]\n\t"
  "v_pk_mul_f32 v[202:203], v[172:173], v[192:193]\n\t"
  "v_pk_mul_f32 v[204:205], v[174:175], v[192:193]\n\t"
  "v_pk_mul_f32 v[206:207], v[176:177], v[192:193]\n\t"
  "v_pk_mul_f32 v[208:209], v[178:179], v[192:193]\n\t"
  "v_pk_mul_f32 v[210:211], v[166:167], v[190:191]\n\t"
  "v_pk_mul_f32 v[212:213], v[168:169], v[190:191]\n\t"
  "v_pk_mul_f32 v[214:215], v[170:171], v[190:191]\n\t"
  "v_pk_mul_f32 v[216:217], v[172:173], v[190:191]\n\t"
  "v_pk_mul_f32 v[218:219], v[174:175], v[190:191]\n\t"
  "v_pk_mul_f32 v[220:221], v[176:177], v[190:191]\n\t"
  "v_pk_mul_f32 v[222:223], v[178:179], v[190:191]\n\t"
  "v_pk_mul_f32 v[224:225], v[180:181], v[190:191]\n\t"
  "v_cvt_pk_bf16_f32 v194, v194, v210\n\t"
  "v_cvt_pk_bf16_f32 v195, v195, v211\n\t"
  "v_cvt_pk_bf16_f32 v196, v196, v212\n\t"
  "v_cvt_pk_bf16_f32 v197, v197, v213\n\t"
  "v_cvt_pk_bf16_f32 v198, v198, v214\n\t"
  "v_cvt_pk_bf16_f32 v199, v199, v215\n\t"
  "v_cvt_pk_bf16_f32 v200, v200, v216\n\t"
  "v_cvt_pk_bf16_f32 v201, v201, v217\n\t"
  "v_cvt_pk_bf16_f32 v202, v202, v218\n\t"
  "v_cvt_pk_bf16_f32 v203, v203, v219\n\t"
  "v_cvt_pk_bf16_f32 v204, v204, v220\n\t"
  "v_cvt_pk_bf16_f32 v205, v205, v221\n\t"
  "v_cvt_pk_bf16_f32 v206, v206, v222\n\t"
  "v_cvt_pk_bf16_f32 v207, v207, v223\n\t"
  "v_cvt_pk_bf16_f32 v208, v208, v224\n\t"
  "v_cvt_pk_bf16_f32 v209, v209, v225\n\t"
  "v_dot2_f32_bf16 v194, v194, v234, 0\n\t"
  "v_dot2_f32_bf16 v195, v195, v234, 0\n\t"
  "v_dot2_f32_bf16 v196, v196, v234, 0\n\t"
  "v_dot2_f32_bf16 v197, v197, v234, 0\n\t"
  "v_dot2_f32_bf16 v198, v198, v234, 0\n\t"
  "v_dot2_f32_bf16 v199, v199, v234, 0\n\t"
  "v_dot2_f32_bf16 v200, v200, v234, 0\n\t"
  "v_dot2_f32_bf16 v201, v201, v234, 0\n\t"
  "v_dot2_f32_bf16 v202, v202, v234, 0\n\t"
  "v_dot2_f32_bf16 v203, v203, v234, 0\n\t"
  "v_dot2_f32_bf16 v204, v204, v234, 0\n\t"
  "v_dot2_f32_bf16 v205, v205, v234, 0\n\t"
  "v_dot2_f32_bf16 v206, v206, v234, 0\n\t"
  "v_dot2_f32_bf16 v207, v207, v234, 0\n\t"
  "v_dot2_f32_bf16 v208, v208, v234, 0\n\t"
  "v_dot2_f32_bf16 v209, v209, v234, 0\n\t"
  "v_pk_mul_f32 v[210:211], v[168:169], v[188:189]\n\t"
  "v_pk_mul_f32 v[212:213], v[170:171], v[188:189]\n\t"
  "v_pk_mul_f32 v[214:215], v[172:173], v[188:189]\n\t"
  "v_pk_mul_f32 v[216:217], v[174:175], v[188:189]\n\t"
  "v_pk_mul_f32 v[218:219], v[176:177], v[188:189]\n\t"
  "v_pk_mul_f32 v[220:221], v[178:179], v[188:189]\n\t"
  "v_pk_mul_f32 v[222:223], v[180:181], v[188:189]\n\t"
  "v_pk_mul_f32 v[224:225], v[182:183], v[188:189]\n\t"
  "v_cvt_pk_bf16_f32 v194, v194, v210\n\t"
  "v_cvt_pk_bf16_f32 v195, v195, v211\n\t"
  "v_cvt_pk_bf16_f32 v196, v196, v212\n\t"
  "v_cvt_pk_bf16_f32 v197, v197, v213\n\t"
  "v_cvt_pk_bf16_f32 v198, v198, v214\n\t"
  "v_cvt_pk_bf16_f32 v199, v199, v215\n\t"
  "v_cvt_pk_bf16_f32 v200, v200, v216\n\t"
  "v_cvt_pk_bf16_f32 v201, v201, v217\n\t"
  "v_cvt_pk_bf16_f32 v202, v202, v218\n\t"
  "v_cvt_pk_bf16_f32 v203, v203, v219\n\t"
  "v_cvt_pk_bf16_f32 v204, v204, v220\n\t"
  "v_cvt_pk_bf16_f32 v205, v205, v221\n\t"
  "v_cvt_pk_bf16_f32 v206, v206, v222\n\t"
  "v_cvt_pk_bf16_f32 v207, v207, v223\n\t"
  "v_cvt_pk_bf16_f32 v208, v208, v224\n\t"
  "v_cvt_pk_bf16_f32 v209, v209, v225\n\t"
  "v_dot2_f32_bf16 v194, v194, v234, 0\n\t"
  "v_dot2_f32_bf16 v195, v195, v234, 0\n\t"
  "v_dot2_f32_bf16 v196, v196, v234, 0\n\t"
  "v_dot2_f32_bf16 v197, v197, v234, 0\n\t"
  "v_dot2_f32_bf16 v198, v198, v234, 0\n\t"
  "v_dot2_f32_bf16 v199, v199, v234, 0\n\t"
  "v_dot2_f32_bf16 v200, v200, v234, 0\n\t"
  "v_dot2_f32_bf16 v201, v201, v234, 0\n\t"
  "v_dot2_f32_bf16 v202, v202, v234, 0\n\t"
  "v_dot2_f32_bf16 v203, v203, v234, 0\n\t"
  "v_dot2_f32_bf16 v204, v204, v234, 0\n\t"
  "v_dot2_f32_bf16 v205, v205, v234, 0\n\t"
  "v_dot2_f32_bf16 v206, v206, v234, 0\n\t"
  "v_dot2_f32_bf16 v207, v207, v234, 0\n\t"
  "v_dot2_f32_bf16 v208, v208, v234, 0\n\t"
  "v_dot2_f32_bf16 v209, v209, v234, 0\n\t"
  "v_pk_mul_f32 v[210:211], v[170:171], v[186:187]\n\t"
  "v_pk_mul_f32 v[212:213], v[172:173], v[186:187]\n\t"
  "v_pk_mul_f32 v[214:215], v[174:175], v[186:187]\n\t"
  "v_pk_mul_f32 v[216:217], v[176:177], v[186:187]\n\t"
  "v_pk_mul_f32 v[218:219], v[178:179], v[186:187]\n\t"
  "v_pk_mul_f32 v[220:221], v[180:181], v[186:187]\n\t"
  "v_pk_mul_f32 v[222:223], v[182:183], v[186:187]\n\t"
  "v_pk_mul_f32 v[224:225], v[184:185], v[186:187]\n\t"
  "v_cvt_pk_bf16_f32 v194, v194, v210\n\t"
  "v_cvt_pk_bf16_f32 v195, v195, v211\n\t"
  "v_cvt_pk_bf16_f32 v196, v196, v212\n\t"
  "v_cvt_pk_bf16_f32 v197, v197, v213\n\t"
  "v_cvt_pk_bf16_f32 v198, v198, v214\n\t"
  "v_cvt_pk_bf16_f32 v199, v199, v215\n\t"
  "v_cvt_pk_bf16_f32 v200, v200, v216\n\t"
  "v_cvt_pk_bf16_f32 v201, v201, v217\n\t"
  "v_cvt_pk_bf16_f32 v202, v202, v218\n\t"
  "v_cvt_pk_bf16_f32 v203, v203, v219\n\t"
  "v_cvt_pk_bf16_f32 v204, v204, v220\n\t"
  "v_cvt_pk_bf16_f32 v205, v205, v221\n\t"
  "v_cvt_pk_bf16_f32 v206, v206, v222\n\t"
  "v_cvt_pk_bf16_f32 v207, v207, v223\n\t"
  "v_cvt_pk_bf16_f32 v208, v208, v224\n\t"
  "v_cvt_pk_bf16_f32 v209, v209, v225\n\t"
  "v_dot2_f32_bf16 v194, v194, v234, 0\n\t"
  "v_dot2_f32_bf16 v195, v195, v234, 0\n\t"
  "v_dot2_f32_bf16 v196, v196, v234, 0\n\t"
  "v_dot2_f32_bf16 v197, v197, v234, 0\n\t"
  "v_dot2_f32_bf16 v198, v198, v234, 0\n\t"
  "v_dot2_f32_bf16 v199, v199, v234, 0\n\t"
  "v_dot2_f32_bf16 v200, v200, v234, 0\n\t"
  "v_dot2_f32_bf16 v201, v201, v234, 0\n\t"
  "v_dot2_f32_bf16 v202, v202, v234, 0\n\t"
  "v_dot2_f32_bf16 v203, v203, v234, 0\n\t"
  "v_dot2_f32_bf16 v204, v204, v234, 0\n\t"
  "v_dot2_f32_bf16 v205, v205, v234, 0\n\t"
  "v_dot2_f32_bf16 v206, v206, v234, 0\n\t"
  "v_dot2_f32_bf16 v207, v207, v234, 0\n\t"
  "v_dot2_f32_bf16 v208, v208, v234, 0\n\t"
  "v_dot2_f32_bf16 v209, v209, v234, 0\n\t"
  "s_waitcnt lgkmcnt(3)\n\t"
  "v_and_b32 v210, 0xffff, v140\n\t"
  "v_and_b32 v211, 0xffff0000, v140\n\t"
  "v_and_b32 v212, 0xffff, v141\n\t"
  "v_and_b32 v213, 0xffff0000, v141\n\t"
  "s_waitcnt lgkmcnt(2)\n\t"
  "v_and_b32 v214, 0xffff, v142\n\t"
  "v_and_b32 v215, 0xffff0000, v142\n\t"
  "v_and_b32 v216, 0xffff, v143\n\t"
  "v_and_b32 v217, 0xffff0000, v143\n\t"
  "s_waitcnt lgkmcnt(1)\n\t"
  "v_and_b32 v218, 0xffff, v144\n\t"
  "v_and_b32 v219, 0xffff0000, v144\n\t"
  "v_and_b32 v220, 0xffff, v145\n\t"
  "v_and_b32 v221, 0xffff0000, v145\n\t"
  "s_waitcnt lgkmcnt(0)\n\t"
  "v_and_b32 v222, 0xffff, v146\n\t"
  "v_and_b32 v223, 0xffff0000, v146\n\t"
  "v_and_b32 v224, 0xffff, v147\n\t"
  "v_and_b32 v225, 0xffff0000, v147\n\t"
  "v_cvt_pk_bf16_f32 v194, v194, v195\n\t"
  "v_cvt_pk_bf16_f32 v196, v196, v197\n\t"
  "v_cvt_pk_bf16_f32 v198, v198, v199\n\t"
  "v_cvt_pk_bf16_f32 v200, v200, v201\n\t"
  "v_cvt_pk_bf16_f32 v202, v202, v203\n\t"
  "v_cvt_pk_bf16_f32 v204, v204, v205\n\t"
  "v_cvt_pk_bf16_f32 v206, v206, v207\n\t"
  "v_cvt_pk_bf16_f32 v208, v208, v209\n\t"
  "v_dot2_f32_bf16 v210, v194, v210, 0\n\t"
  "v_dot2_f32_bf16 v211, v194, v211, 0\n\t"
  "v_dot2_f32_bf16 v212, v196, v212, 0\n\t"
  "v_dot2_f32_bf16 v213, v196, v213, 0\n\t"
  "v_dot2_f32_bf16 v214, v198, v214, 0\n\t"
  "v_dot2_f32_bf16 v215, v198, v215, 0\n\t"
  "v_dot2_f32_bf16 v216, v200, v216, 0\n\t"
  "v_dot2_f32_bf16 v217, v200, v217, 0\n\t"
  "v_dot2_f32_bf16 v218, v202, v218, 0\n\t"
  "v_dot2_f32_bf16 v219, v202, v219, 0\n\t"
  "v_dot2_f32_bf16 v220, v204, v220, 0\n\t"
  "v_dot2_f32_bf16 v221, v204, v221, 0\n\t"
  "v_dot2_f32_bf16 v222, v206, v222, 0\n\t"
  "v_dot2_f32_bf16 v223, v206, v223, 0\n\t"
  "v_dot2_f32_bf16 v224, v208, v224, 0\n\t"
  "v_dot2_f32_bf16 v225, v208, v225, 0\n\t"
  "v_cvt_pk_bf16_f32 v229, v212, v210\n\t"
  "v_cvt_pk_bf16_f32 v228, v216, v214\n\t"
  "v_cvt_pk_bf16_f32 v227, v220, v218\n\t"
  "v_cvt_pk_bf16_f32 v226, v224, v222\n\t"
  "buffer_store_dwordx4 v[226:229], %[vo], %[srd], 0 offen offset:64 sc1\n\t"
  "v_cvt_pk_bf16_f32 v233, v213, v211\n\t"
  "v_cvt_pk_bf16_f32 v232, v217, v215\n\t"
  "v_cvt_pk_bf16_f32 v231, v221, v219\n\t"
  "v_cvt_pk_bf16_f32 v230, v225, v223\n\t"
  "buffer_store_dwordx4 v[230:233], %[vo], %[srd], 0 offen offset:192 sc1\n\t"
  "s_nop 1\n\t"
      :: [srd] "s"(srd), [la] "v"(lb + lkv + lane * 4), [t0] "v"(taps[0]), [t1] "v"(taps[1]), [t2] "v"(taps[2]), [t3] "v"(taps[3]),
         [vo] "v"(OFF_KVT + (tb * 160 + 2 * (d0 >> 3)) * 128 + ((lane ^ (tb >> 1)) & 3) * 16)
      : "memory",
"v112", "v113", "v114", "v115", "v116", "v117", "v118", "v119", "v120", "v121", "v122", "v123", "v124", "v125", "v126", "v127", "v128", "v129", "v130", "v131", "v132", "v133", "v134", "v135", "v136", "v137", "v138", "v139", "v140", "v141", "v142", "v143", "v144", "v145", "v146", "v147", "v148", "v149", "v150", "v151", "v152", "v153", "v154", "v155", "v156", "v157", "v158", "v159", "v160", "v161", "v162", "v163", "v164", "v165", "v166", "v167", "v168", "v169", "v170", "v171", "v172", "v173", "v174", "v175", "v176", "v177", "v178", "v179", "v180", "v181", "v182", "v183", "v184", "v185", "v186", "v187", "v188", "v189", "v190", "v191", "v192", "v193", "v194", "v195", "v196", "v197", "v198", "v199", "v200", "v201", "v202", "v203", "v204", "v205", "v206", "v207", "v208", "v209", "v210", "v211", "v212", "v213", "v214", "v215", "v216", "v217", "v218", "v219", "v220", "v221", "v222", "v223", "v224", "v225", "v226", "v227", "v228", "v229", "v230", "v231", "v232", "v233", "v234"
    );
    {
      const unsigned d = 8 * (cb >> 1) + 2 * w + (cb & 1);
      const unsigned r = lane & 15, g = lane >> 4;
      const unsigned pp = 25 + r - 8 * g;
      const unsigned va = lb + L_FC + w * FC_CH + (pp & 3) * FC_PITCH + (pp & ~3u) * 2;
      unsigned vb[4];
  #pragma unroll
      for (int k = 0; k < 4; ++k) { const unsigned G = 14 - 4 * k + 2 * r + g; vb[k] = lb + L_KV + G * 64 + ((w ^ (G >> 2)) & 3) * 16; }
      const unsigned gx = (2 * r + (g >> 1)) * 64 + ((w ^ (r >> 1)) & 3) * 16;
      const unsigned vx = lb + L_X + gx + (g & 1) * 8, vs = lb + L_ST + r * 128 + ((2 * g) ^ stkey(r)) * 16;
      const unsigned to = 256 * (g & 1) + 16 * r + 8 * (g >> 1);
      const unsigned vo = (to >> 6) * 81920u + (((to & 63) * 2) ^ (((d >> 1) & 3) * 32 + (d & 8) * 2));
      const unsigned wh = (lane >> 2) & 3, mcol = (wh < 3 ? wh : 4) * 640 + d;
      const unsigned jd = 8 * g - 9 - r, dv = dval << (16 * (jd & 1));
      const u32x4 vd = {jd >> 1 == 0 ? dv : 0u, jd >> 1 == 1 ? dv : 0u, jd >> 1 == 2 ? dv : 0u, jd >> 1 == 3 ? dv : 0u};
      c_vf2 = lane < 32 ? OFF_FA3 + (32 * w + lane) * 128 : OFF_FA2 + ((lane & 3) * 60 + (mcol >> 6)) * 128;
      c_vp0 = tid < 8 * 8 ? OFF_FX + ((tid >> 3) * 160 + cb) * 128 + (tid & 7) * 16 : OOB;
      c_vf1 = OFF_FA1 + ((cb & ~31u) + 8 * w + (lane & 7)) * 128;
      c_vf3 = lane < 8 ? (cb < 128 ? OFF_FA3 + cb * 128 : U_FLAG(cb)) + lane * 16 : OOB;
      c_vfw = OFF_FW1 + (j * 4 + (lane & 3)) * 128;
      c_va = va;
      c_vb0 = vb[0];
      c_vb1 = vb[1];
      c_vb2 = vb[2];
      c_vb3 = vb[3];
      c_vx = vx;
      c_vs = vs;
      c_vs2 = vs ^ 16;
      c_vo = vo;
      c_vd = vd;
      c_vl16 = lane * 16;
      c_vkv = (lane >> 3) * 20480 + (lane & 7) * 16;
      c_vmod = OFF_MODP + ((lane & 3) * 3840 + mcol) * 4;
      asm volatile("" : "+v"(c_vf2), "+v"(c_vf1), "+v"(c_vp0), "+v"(c_vf3), "+v"(c_vfw), "+v"(c_va), "+v"(c_vb0), "+v"(c_vb1), "+v"(c_vb2), "+v"(c_vb3), "+v"(c_vx), "+v"(c_vs), "+v"(c_vs2), "+v"(c_vo), "+v"(c_vd), "+v"(c_vl16), "+v"(c_vkv), "+v"(c_vmod));
    }
    char* fcw = smem + L_FC + w * FC_CH;
    auto put = [&](unsigned u, u32x4 a, u32x2 h) {
      const u32 W[6] = {a[0], a[1], a[2], a[3], h[0], h[1]};
      u32 O[5];
#pragma unroll
      for (int i = 0; i < 5; ++i) O[i] = __builtin_amdgcn_alignbit(W[i + 1], W[i], 16);
      *(u32x4*)(fcw + 0 * FC_PITCH + u * 16) = a;
      *(u32x4*)(fcw + 1 * FC_PITCH + u * 16) = u32x4{O[0], O[1], O[2], O[3]};
      *(u32x4*)(fcw + 2 * FC_PITCH + u * 16) = u32x4{W[1], W[2], W[3], W[4]};
      *(u32x4*)(fcw + 3 * FC_PITCH + u * 16) = u32x4{O[1], O[2], O[3], O[4]};
    };
#pragma unroll
    for (int n = 0; n < 4; ++n) {
      u32x2 h;
#pragma unroll
      for (int e = 0; e < 2; ++e) h[e] = __builtin_amdgcn_update_dpp(n < 3 ? rfl(fa[n < 3 ? n + 1 : n][e]) : 0u, fa[n][e], 0x130, 0xf, 0xf, false);
      put(lane + 64 * n + 2, fa[n], h);
    }
    if (lane < 2) put(1 - lane, u32x4{0, 0, 0, 0}, lane == 0 ? u32x2{fa[0][0], fa[0][1]} : u32x2{0, 0});
    FENCE();
#pragma unroll
    for (int n = 0; n < 5; ++n) *(u32x4*)(smem + L_XR + n * 4096 + w * 1024 + lane * 16) = xr[n];
    if (w == 1) *(u32x4*)(smem + L_XR + 20480 + lane * 16) = cr;
    FENCE();
    asm volatile("s_waitcnt vmcnt(0) lgkmcnt(0)\n\ts_barrier" ::: "memory");
    __builtin_amdgcn_raw_buffer_store_b128(u32x4{(u32)tag, (u32)(tag >> 32), (u32)tag, (u32)(tag >> 32)}, srd, tid < 8 ? OFF_FA1 + cb * 128 + tid * 16 : OOB, 0, SC1);
    FENCE();
    if (isx) {
      const unsigned g = lane >> 4, q = (lane >> 2) & 3, ch = 16 * (g >> 1) + (lane & 15);
      asm volatile(
  "s_nop 4\n\t"
  "s_add_u32 s44, %[w], 1\n\t"
  "s_and_b32 s44, s44, 2\n\t"
  "s_cbranch_scc0 Lxs%=\n\t"
  "ds_read_b64_tr_b16 v[96:97], %[vtx] offset:0\n\t"
  "ds_read_b64_tr_b16 v[98:99], %[vtx] offset:512\n\t"
  "ds_read_b64_tr_b16 v[100:101], %[vtx] offset:64\n\t"
  "ds_read_b64_tr_b16 v[102:103], %[vtx] offset:576\n\t"
  "ds_read_b64_tr_b16 v[104:105], %[vtx] offset:2048\n\t"
  "ds_read_b64_tr_b16 v[106:107], %[vtx] offset:2560\n\t"
  "ds_read_b64_tr_b16 v[108:109], %[vtx] offset:2112\n\t"
  "ds_read_b64_tr_b16 v[110:111], %[vtx] offset:2624\n\t"
  "ds_read_b64_tr_b16 v[112:113], %[vtx] offset:4096\n\t"
  "ds_read_b64_tr_b16 v[114:115], %[vtx] offset:4608\n\t"
  "ds_read_b64_tr_b16 v[116:117], %[vtx] offset:4160\n\t"
  "ds_read_b64_tr_b16 v[118:119], %[vtx] offset:4672\n\t"
  "ds_read_b64_tr_b16 v[120:121], %[vtx] offset:6144\n\t"
  "ds_read_b64_tr_b16 v[122:123], %[vtx] offset:6656\n\t"
  "ds_read_b64_tr_b16 v[124:125], %[vtx] offset:6208\n\t"
  "ds_read_b64_tr_b16 v[126:127], %[vtx] offset:6720\n\t"
  "s_add_u32 s44, %[sXo], 0\n\t"
  "s_waitcnt lgkmcnt(14)\n\t"
  "buffer_store_dwordx4 v[96:99], %[vxo], %[srd], s44 offen offset:0 sc1\n\t"
  "ds_read_b64_tr_b16 v[128:129], %[vtx] offset:8192\n\t"
  "ds_read_b64_tr_b16 v[130:131], %[vtx] offset:8704\n\t"
  "s_waitcnt lgkmcnt(14)\n\t"
  "buffer_store_dwordx4 v[100:103], %[vxo], %[srd], s44 offen offset:1024 sc1\n\t"
  "ds_read_b64_tr_b16 v[132:133], %[vtx] offset:8256\n\t"
  "ds_read_b64_tr_b16 v[134:135], %[vtx] offset:8768\n\t"
  "s_waitcnt lgkmcnt(14)\n\t"
  "buffer_store_dwordx4 v[104:107], %[vxo], %[srd], s44 offen offset:2048 sc1\n\t"
  "s_waitcnt lgkmcnt(12)\n\t"
  "buffer_store_dwordx4 v[108:111], %[vxo], %[srd], s44 offen offset:3072 sc1\n\t"
  "s_add_u32 s44, %[sXo], 4096\n\t"
  "s_waitcnt lgkmcnt(10)\n\t"
  "buffer_store_dwordx4 v[112:115], %[vxo], %[srd], s44 offen offset:0 sc1\n\t"
  "s_waitcnt lgkmcnt(8)\n\t"
  "buffer_store_dwordx4 v[116:119], %[vxo], %[srd], s44 offen offset:1024 sc1\n\t"
  "s_waitcnt lgkmcnt(6)\n\t"
  "buffer_store_dwordx4 v[120:123], %[vxo], %[srd], s44 offen offset:2048 sc1\n\t"
  "s_waitcnt lgkmcnt(4)\n\t"
  "buffer_store_dwordx4 v[124:127], %[vxo], %[srd], s44 offen offset:3072 sc1\n\t"
  "s_add_u32 s44, %[sXo], 8192\n\t"
  "s_waitcnt lgkmcnt(2)\n\t"
  "buffer_store_dwordx4 v[128:131], %[vxo], %[srd], s44 offen offset:0 sc1\n\t"
  "s_waitcnt lgkmcnt(0)\n\t"
  "buffer_store_dwordx4 v[132:135], %[vxo], %[srd], s44 offen offset:1024 sc1\n\t"
  "s_branch Lxe%=\n\t"
  "Lxs%=:\n\t"
  "ds_read_b128 v[96:99], %[vsx] offset:0\n\t"
  "ds_read_b128 v[100:103], %[vsx] offset:2048\n\t"
  "ds_read_b128 v[104:107], %[vsx] offset:4096\n\t"
  "ds_read_b128 v[108:111], %[vsx] offset:6144\n\t"
  "ds_read_b128 v[112:115], %[vsx] offset:8192\n\t"
  "ds_read_b128 v[116:119], %[vsx] offset:10240\n\t"
  "ds_read_b128 v[120:123], %[vsx] offset:12288\n\t"
  "ds_read_b128 v[124:127], %[vsx] offset:14336\n\t"
  "ds_read_b128 v[128:131], %[vsx] offset:16384\n\t"
  "ds_read_b128 v[132:135], %[vsx] offset:18432\n\t"
  "v_mov_b32 v184, 0\n\t"
  "v_mov_b32 v185, 0\n\t"
  "v_mov_b32 v186, 0x3f803f80\n\t"
  "s_waitcnt lgkmcnt(9)\n\t"
  "v_dot2c_f32_bf16 v184, v96, v186\n\t"
  "v_dot2c_f32_bf16 v185, v96, v96\n\t"
  "v_dot2c_f32_bf16 v184, v97, v186\n\t"
  "v_dot2c_f32_bf16 v185, v97, v97\n\t"
  "v_dot2c_f32_bf16 v184, v98, v186\n\t"
  "v_dot2c_f32_bf16 v185, v98, v98\n\t"
  "v_dot2c_f32_bf16 v184, v99, v186\n\t"
  "v_dot2c_f32_bf16 v185, v99, v99\n\t"
  "s_waitcnt lgkmcnt(8)\n\t"
  "v_dot2c_f32_bf16 v184, v100, v186\n\t"
  "v_dot2c_f32_bf16 v185, v100, v100\n\t"
  "v_dot2c_f32_bf16 v184, v101, v186\n\t"
  "v_dot2c_f32_bf16 v185, v101, v101\n\t"
  "v_dot2c_f32_bf16 v184, v102, v186\n\t"
  "v_dot2c_f32_bf16 v185, v102, v102\n\t"
  "v_dot2c_f32_bf16 v184, v103, v186\n\t"
  "v_dot2c_f32_bf16 v185, v103, v103\n\t"
  "s_waitcnt lgkmcnt(7)\n\t"
  "v_dot2c_f32_bf16 v184, v104, v186\n\t"
  "v_dot2c_f32_bf16 v185, v104, v104\n\t"
  "v_dot2c_f32_bf16 v184, v105, v186\n\t"
  "v_dot2c_f32_bf16 v185, v105, v105\n\t"
  "v_dot2c_f32_bf16 v184, v106, v186\n\t"
  "v_dot2c_f32_bf16 v185, v106, v106\n\t"
  "v_dot2c_f32_bf16 v184, v107, v186\n\t"
  "v_dot2c_f32_bf16 v185, v107, v107\n\t"
  "s_waitcnt lgkmcnt(6)\n\t"
  "v_dot2c_f32_bf16 v184, v108, v186\n\t"
  "v_dot2c_f32_bf16 v185, v108, v108\n\t"
  "v_dot2c_f32_bf16 v184, v109, v186\n\t"
  "v_dot2c_f32_bf16 v185, v109, v109\n\t"
  "v_dot2c_f32_bf16 v184, v110, v186\n\t"
  "v_dot2c_f32_bf16 v185, v110, v110\n\t"
  "v_dot2c_f32_bf16 v184, v111, v186\n\t"
  "v_dot2c_f32_bf16 v185, v111, v111\n\t"
  "s_waitcnt lgkmcnt(5)\n\t"
  "v_dot2c_f32_bf16 v184, v112, v186\n\t"
  "v_dot2c_f32_bf16 v185, v112, v112\n\t"
  "v_dot2c_f32_bf16 v184, v113, v186\n\t"
  "v_dot2c_f32_bf16 v185, v113, v113\n\t"
  "v_dot2c_f32_bf16 v184, v114, v186\n\t"
  "v_dot2c_f32_bf16 v185, v114, v114\n\t"
  "v_dot2c_f32_bf16 v184, v115, v186\n\t"
  "v_dot2c_f32_bf16 v185, v115, v115\n\t"
  "s_waitcnt lgkmcnt(4)\n\t"
  "v_dot2c_f32_bf16 v184, v116, v186\n\t"
  "v_dot2c_f32_bf16 v185, v116, v116\n\t"
  "v_dot2c_f32_bf16 v184, v117, v186\n\t"
  "v_dot2c_f32_bf16 v185, v117, v117\n\t"
  "v_dot2c_f32_bf16 v184, v118, v186\n\t"
  "v_dot2c_f32_bf16 v185, v118, v118\n\t"
  "v_dot2c_f32_bf16 v184, v119, v186\n\t"
  "v_dot2c_f32_bf16 v185, v119, v119\n\t"
  "s_waitcnt lgkmcnt(3)\n\t"
  "v_dot2c_f32_bf16 v184, v120, v186\n\t"
  "v_dot2c_f32_bf16 v185, v120, v120\n\t"
  "v_dot2c_f32_bf16 v184, v121, v186\n\t"
  "v_dot2c_f32_bf16 v185, v121, v121\n\t"
  "v_dot2c_f32_bf16 v184, v122, v186\n\t"
  "v_dot2c_f32_bf16 v185, v122, v122\n\t"
  "v_dot2c_f32_bf16 v184, v123, v186\n\t"
  "v_dot2c_f32_bf16 v185, v123, v123\n\t"
  "s_waitcnt lgkmcnt(2)\n\t"
  "v_dot2c_f32_bf16 v184, v124, v186\n\t"
  "v_dot2c_f32_bf16 v185, v124, v124\n\t"
  "v_dot2c_f32_bf16 v184, v125, v186\n\t"
  "v_dot2c_f32_bf16 v185, v125, v125\n\t"
  "v_dot2c_f32_bf16 v184, v126, v186\n\t"
  "v_dot2c_f32_bf16 v185, v126, v126\n\t"
  "v_dot2c_f32_bf16 v184, v127, v186\n\t"
  "v_dot2c_f32_bf16 v185, v127, v127\n\t"
  "s_waitcnt lgkmcnt(1)\n\t"
  "v_dot2c_f32_bf16 v184, v128, v186\n\t"
  "v_dot2c_f32_bf16 v185, v128, v128\n\t"
  "v_dot2c_f32_bf16 v184, v129, v186\n\t"
  "v_dot2c_f32_bf16 v185, v129, v129\n\t"
  "v_dot2c_f32_bf16 v184, v130, v186\n\t"
  "v_dot2c_f32_bf16 v185, v130, v130\n\t"
  "v_dot2c_f32_bf16 v184, v131, v186\n\t"
  "v_dot2c_f32_bf16 v185, v131, v131\n\t"
  "s_waitcnt lgkmcnt(0)\n\t"
  "v_dot2c_f32_bf16 v184, v132, v186\n\t"
  "v_dot2c_f32_bf16 v185, v132, v132\n\t"
  "v_dot2c_f32_bf16 v184, v133, v186\n\t"
  "v_dot2c_f32_bf16 v185, v133, v133\n\t"
  "v_dot2c_f32_bf16 v184, v134, v186\n\t"
  "v_dot2c_f32_bf16 v185, v134, v134\n\t"
  "v_dot2c_f32_bf16 v184, v135, v186\n\t"
  "v_dot2c_f32_bf16 v185, v135, v135\n\t"
  "s_nop 1\n\t"
  "v_add_f32_dpp v184, v184, v184 quad_perm:[1,0,3,2] row_mask:0xf bank_mask:0xf\n\t"
  "v_add_f32_dpp v185, v185, v185 quad_perm:[1,0,3,2] row_mask:0xf bank_mask:0xf\n\t"
  "s_nop 0\n\t"
  "v_add_f32_dpp v184, v184, v184 quad_perm:[2,3,0,1] row_mask:0xf bank_mask:0xf\n\t"
  "v_add_f32_dpp v185, v185, v185 quad_perm:[2,3,0,1] row_mask:0xf bank_mask:0xf\n\t"
  "s_nop 0\n\t"
  "v_add_f32_dpp v184, v184, v184 row_half_mirror row_mask:0xf bank_mask:0xf\n\t"
  "v_add_f32_dpp v185, v185, v185 row_half_mirror row_mask:0xf bank_mask:0xf\n\t"
  "v_mul_f32 v184, 0xbacccccd, v184\n\t"
  "v_mul_f32 v185, 0x3acccccd, v185\n\t"
  "v_fma_f32 v185, -v184, v184, v185\n\t"
  "v_max_f32 v185, 0, v185\n\t"
  "v_cvt_pk_bf16_f32 v185, 0, v185\n\t"
  "v_cvt_pk_bf16_f32 v184, 0, v184\n\t"
  "v_add_f32 v185, 0x3727c5ac, v185\n\t"
  "v_cvt_pk_bf16_f32 v185, 0, v185\n\t"
  "v_sqrt_f32 v185, v185\n\t"
  "s_nop 0\n\t"
  "v_cvt_pk_bf16_f32 v185, 0, v185\n\t"
  "v_rcp_f32 v188, v185\n\t"
  "s_nop 0\n\t"
  "v_mul_f32 v189, v184, v188\n\t"
  "s_nop 1\n\t"
  "v_mov_b32_dpp v188, v189 quad_perm:[0,1,2,3] row_mask:0xf bank_mask:0xa\n\t"
  "buffer_store_dword v188, %[vso], %[srd], %[sSo] offen sc1\n\t"
  "Lxe%=:\n\t"
  "s_nop 1\n\t"
        :: [srd] "s"(srd), [w] "s"(w), [sSo] "s"(OFF_ST1 + cb * 128u), [sXo] "s"(OFF_XT + cb * 20480u + (w - 1) * 10240u),
           [vtx] "v"(lb + L_XR + (w - 1) * 10240 + (8 * (g & 1) + q) * 128 + (g >> 1) * 32 + (lane & 3) * 8), [vsx] "v"(lb + L_XR + (w & 1) * 1024 + lane * 16),
           [vso] "v"((lane & 3) == 0 ? (((w & 1) * 4 + g) ^ stkey(cb)) * 16 + ((lane >> 3) & 1) * 4 + (lane & 4) * 2 : OOB), [vxo] "v"((2 * (ch >> 3) + (ch & 1)) * 128 + ((((ch ^ cb) >> 1) & 3) + 4 * (g & 1)) * 16)
        : "memory", "vcc", "scc",
"s44", "v96", "v97", "v98", "v99", "v100", "v101", "v102", "v103", "v104", "v105", "v106", "v107", "v108", "v109", "v110", "v111", "v112", "v113", "v114", "v115", "v116", "v117", "v118", "v119", "v120", "v121", "v122", "v123", "v124", "v125", "v126", "v127", "v128", "v129", "v130", "v131", "v132", "v133", "v134", "v135", "v184", "v185", "v186", "v188", "v189"
      );
    } else if (w == 1) GEMV_UNIT(cb, lb + L_XR, 8);
    stash();
  } else {
    const unsigned lu = lb + L_UN + w * UN_SZ;
    const unsigned g = lane >> 4, q = (lane >> 2) & 3, p4 = lane & 3;
    if (8) __builtin_amdgcn_s_sleep(8);
    if (role == 2) {
      const unsigned ui = 1 ? 16 * (w2 >> 2) + 4 * w + (w2 & 3) : 4 * w2 + w;
      GEMV_LOAD(ui, lu);
      const rsrc_t rd = mkrsrc(wd, D_ * F_ * 2);
      const unsigned r8 = 24 * w + (lane >> 3), so = (96 * (w2 >> 1) * D_ + 320 * (w2 & 1)) * 2;
      u32x4 raw[15];
#pragma unroll
      for (int n = 0; n < 15; ++n) raw[n] = __builtin_amdgcn_raw_buffer_load_b128(rd, r8 * (D_ * 2) + (lane & 7) * 16 + (n % 5) * 128, so + (n / 5) * 8 * D_ * 2, NT);
      FENCE();
      tag = load_tag(ws);
      GEMV_UNIT(ui, lu, 15);
#pragma unroll
      for (int n = 0; n < 15; ++n) *(u32x4*)(smem + r8 * RP2 + (lane & 7) * 16 + (n % 5) * 128 + (n / 5) * 8 * RP2) = raw[n];
      FENCE();
      asm volatile("s_waitcnt vmcnt(0)" ::: "memory");
      put_flag(srd, U_FLAG(ui), lane, tag);
      const bool rt = w2 < 10 && w == 0;
      poll_flags<1, 4>(srd, rt && lane >= 4 ? OFF_FA2 + ((lane & 3) * 60 + 40 + w2) * 128 : OFF_FW1 + (j * 4 + (lane & 3)) * 128, tag);
      if (rt) {
        const float sc = rbf(1.f + modval(srd, 4 * 640 + lane + 64 * w2));
        __builtin_amdgcn_raw_buffer_store_b32(__float_as_uint(sc == 0.f ? 0x1p40f : 1.f / sc), srd, lane * 4, OFF_RT + w2 * 256, SC1);
        asm volatile("s_waitcnt vmcnt(0)" ::: "memory");
        put_flag(srd, OFF_FR + w2 * 128, lane, tag);
      }
      W1_LOAD();
      wdone = 0 ? 255 : 127;
      BAR();
#pragma unroll
      for (int n = 0; n < 15; ++n) {
        const int i = n % 5;
        const char* a = smem + (32 * (n / 5) + 8 * g + q) * RP2 + (80 * w + (i < 4 ? 32 * (i >> 1) + 8 * p4 + 4 * (i & 1) : 64 + 4 * p4)) * 2;
        const u32x2 lo = tr_read(a), hi = tr_read(a + 4 * RP2);
        raw[n] = u32x4{lo[0], lo[1], hi[0], hi[1]};
      }
      FENCE();
#pragma unroll
      for (int n = 0; n < 15; ++n)
        __builtin_amdgcn_raw_buffer_store_b128(raw[n], srd, lane * 16 + (n & 3) * 1024, OFF_W2F + ((4 * (w2 & 1) + w) * 240 + 15 * (w2 >> 1) + (n & ~3)) * 1024, SC1);
      FENCE();
      stash();
    } else {
      const rsrc_t rg = mkrsrc(wg, D_ * F_ * 2), ru = mkrsrc(wu, D_ * F_ * 2);
      const unsigned rw = 5 * w + lane / 12, ck = lane % 12, ui = w == 0 ? 160 + w1 : w == 1 && (w1 & 3) == 3 ? 224 + (w1 >> 2) : 240;
      const unsigned vo = lane < 60 ? rw * (F_ * 2) + ck * 16 : OOB, so = (160 * (w1 & 3) * F_ + 96 * (w1 >> 2)) * 2;
      u32x4 raw[16];
#pragma unroll
      for (int n = 0; n < 8; ++n) {
        raw[2 * n] = __builtin_amdgcn_raw_buffer_load_b128(rg, vo, so + n * 20 * F_ * 2, NT);
        raw[2 * n + 1] = __builtin_amdgcn_raw_buffer_load_b128(ru, vo, so + n * 20 * F_ * 2, NT);
      }
      FENCE();
      tag = load_tag(ws);
      char* o = smem + (lane < 60 ? rw * RP1 + ck * 32 : 384);
      u32x4 fr[15];
#pragma unroll
      for (int h = 0; h < 2; ++h) {
#pragma unroll
        for (int n = h ? 5 : 0; n < (h ? 8 : 5); ++n) {
          *(u32x4*)(o + n * 20 * RP1) = u32x4{raw[2 * n][0], raw[2 * n + 1][0], raw[2 * n][1], raw[2 * n + 1][1]};
          *(u32x4*)(o + n * 20 * RP1 + (lane < 60 ? 16 : 0)) = u32x4{raw[2 * n][2], raw[2 * n + 1][2], raw[2 * n][3], raw[2 * n + 1][3]};
        }
        BAR();
#pragma unroll
        for (int n = h ? 9 : 0; n < (h ? 15 : 9); ++n) {
          const int ks = n / 3, i = n % 3;
          const char* a = smem + (32 * ks + 4 * g + q) * RP1 + (12 * w + (p4 < 3 ? 4 * p4 + i : 4 * i + 3)) * 8;
          const u32x2 lo = tr_read(a), hi = tr_read(a + 16 * RP1);
          fr[n] = u32x4{lo[0], lo[1], hi[0], hi[1]};
        }
        FENCE();
#pragma unroll
        for (int n = h ? 9 : 0; n < (h ? 15 : 9); ++n)
          __builtin_amdgcn_raw_buffer_store_b128(fr[n], srd, lane * 16 + (n & 3) * 1024, OFF_W1F + (((w1 & ~3u) + w) * 60 + 15 * (w1 & 3) + (n & ~3)) * 1024, SC1);
        FENCE();
      }
      if (ui < 240) { GEMV_LOAD(ui, lu); asm volatile("s_waitcnt vmcnt(21)" ::: "memory"); } else asm volatile("s_waitcnt vmcnt(0)" ::: "memory");
      asm volatile("s_barrier" ::: "memory");
      if (w == 3) put_flag(srd, OFF_FW1 + w1 * 128, lane, tag);
      if (ui < 240) {
        GEMV_UNIT(ui, lu, 0);
        asm volatile("s_waitcnt vmcnt(0)" ::: "memory");
        put_flag(srd, U_FLAG(ui), lane, tag);
      }
      poll_flags<1, 4>(srd, OFF_FW1 + (j * 4 + (lane & 3)) * 128, tag);
      W1_LOAD();
      wdone = 0 ? 255 : 127;
      stash();
      if (w > 1 || (w == 1 && (w1 & 15) == 0)) {
        const unsigned c0 = (6 - w) * 640 + 160 * (w == 1 ? w1 >> 4 : w1 & 3);
        poll_flags<1, 4>(srd, OFF_FA2 + ((lane & 3) * 60 + ((c0 + min(64 * ((lane >> 2) & 3), 159u)) >> 6)) * 128, tag);
        f32x4 m[4];
#pragma unroll
        for (int k = 0; k < 4; ++k) m[k] = __builtin_bit_cast(f32x4, __builtin_amdgcn_raw_buffer_load_b128(srd, lane < 40 ? lane * 16 : OOB, OFF_MODP + k * 15360 + c0 * 4, SC1));
        f32x4 v = (m[0] + m[1]) + (m[2] + m[3]);
#pragma unroll
        for (int e = 0; e < 4; ++e) v[e] = rbf(v[e]);
        if (w == 1) __builtin_amdgcn_raw_buffer_store_b128(__builtin_bit_cast(u32x4, v), srd, lane < 40 ? lane * 16 : OOB, OFF_GB + 40 * w1, SC1);
        else {
          const float of = w == 2 ? 1.f : 0.f;
          *(u32x2*)(smem + L_WS + (w - 2) * 1024 + lane * 8) = u32x2{pack_bf16(of + v[0], of + v[1]), pack_bf16(of + v[2], of + v[3])};
        }
      }
      BAR();
      f32x4 aU[3];
      u32x2 s0[5], s1[5];
#pragma unroll
      for (int ks = 0; ks < 5; ++ks) {
        const char* t = smem + L_WS + (lane & 1) * 1024 + (32 * ks + 4 * g) * 2;
        s0[ks] = *(const u32x2*)t; s1[ks] = *(const u32x2*)(t + 32);
      }
#pragma unroll
      for (int i = 0; i < 3; ++i) aU[i] = f32x4{0, 0, 0, 0};
      FENCE();
#pragma unroll
      for (int n = 0; n < 15; ++n) mfma_v(aU[n % 3], u32x4{s0[n / 3][0], s0[n / 3][1], s1[n / 3][0], s1[n / 3][1]}, fr[n]);
      asm volatile("s_nop 7\n\ts_nop 1" : "+v"(aU[0]), "+v"(aU[1]), "+v"(aU[2]));
      const float u = g == 0 ? aU[0][0] : g == 1 ? aU[1][0] : aU[2][0], bb = g == 0 ? aU[0][1] : g == 1 ? aU[1][1] : aU[2][1];
      const float uh = rbf(u), bh = rbf(bb);
      __builtin_amdgcn_raw_buffer_store_b64(u32x2{pack_bf16(uh, u - uh), pack_bf16(bh, bb - bh)}, srd,
                                            g < 3 ? OFF_UB + ((((w1 & ~3u) + w) * 3 + g) * 64 + (w1 & 3) * 16 + (lane & 15)) * 8 : OOB, 0, SC1);
    }
  }
  if (role == 0) {
    const unsigned d = 8 * (cb >> 1) + 2 * w + (cb & 1);
    asm volatile(
  "s_nop 4\n\t"
  "buffer_load_dwordx2 v[160:161], %[vf1], %[srd], 0 offen sc1\n\t"
  "s_lshl_b32 s45, %[w], 12\n\t"
  "s_mov_b64 s[80:81], %[ws]\n\t"
  "v_mov_b32 v252, %[tlo]\n\t"
  "v_mov_b32 v253, %[thi]\n\t"
  "v_mov_b32 v254, %[tlo]\n\t"
  "v_mov_b32 v255, %[thi]\n\t"
  "v_mov_b32 v162, 140288\n\t"
  "v_mov_b32 v163, 1\n\t"
  "s_mov_b64 exec, 1\n\t"
  "s_waitcnt vmcnt(1)\n\t"
  "ds_add_rtn_u32 v164, v162, v163\n\t"
  "s_waitcnt lgkmcnt(0)\n\t"
  "s_mov_b64 exec, -1\n\t"
  "v_readfirstlane_b32 s44, v164\n\t"
  "s_nop 3\n\t"
  "s_cmp_lg_u32 s44, 3\n\t"
  "s_cbranch_scc1 Lnox%=\n\t"
  "buffer_store_dwordx4 v[252:255], %[vf3], %[srd], 0 offen sc1\n\t"
  "s_waitcnt vmcnt(1)\n\t"
  "s_branch Lx%=\n\t"
  "Lnox%=:\n\t"
  "s_waitcnt vmcnt(0)\n\t"
  "Lx%=:\n\t"
  "v_cmp_ne_u64 vcc, v[252:253], v[160:161]\n\t"
  "s_nop 1\n\t"
  "s_cbranch_vccnz La1_slow%=\n\t"
  "La1_back%=:\n\t"
  "s_add_u32 m0, s45, 68608\n\t"
  "s_add_u32 s44, %[sKV], 0\n\t"
  "buffer_load_dwordx4 %[vkv], %[srd], s44 offen sc1 lds\n\t"
  "s_add_u32 m0, s45, 69632\n\t"
  "s_add_u32 s44, %[sKV], 163840\n\t"
  "buffer_load_dwordx4 %[vkv], %[srd], s44 offen sc1 lds\n\t"
  "s_add_u32 m0, s45, 70656\n\t"
  "s_add_u32 s44, %[sKV], 327680\n\t"
  "buffer_load_dwordx4 %[vkv], %[srd], s44 offen sc1 lds\n\t"
  "s_add_u32 m0, s45, 71680\n\t"
  "s_add_u32 s44, %[sKV], 491520\n\t"
  "buffer_load_dwordx4 %[vkv], %[srd], s44 offen sc1 lds\n\t"
  "buffer_load_dwordx2 v[250:251], %[vfw], %[srd], 0 offen sc1\n\t"
  "ds_read_b64 v[64:65], %[va] offset:0\n\t"
  "ds_read_b64 v[66:67], %[va] offset:8\n\t"
  "ds_read_b64 v[68:69], %[va] offset:512\n\t"
  "ds_read_b64 v[70:71], %[va] offset:520\n\t"
  "ds_read_b64 v[72:73], %[va] offset:1024\n\t"
  "ds_read_b64 v[74:75], %[va] offset:1032\n\t"
  "ds_read_b64 v[76:77], %[va] offset:1536\n\t"
  "ds_read_b64 v[78:79], %[va] offset:1544\n\t"
  "ds_read_b64 v[80:81], %[va] offset:2048\n\t"
  "ds_read_b64 v[82:83], %[va] offset:2056\n\t"
  "ds_read_b64 v[84:85], %[va] offset:2560\n\t"
  "ds_read_b64 v[86:87], %[va] offset:2568\n\t"
  "ds_read_b64 v[88:89], %[va] offset:3072\n\t"
  "ds_read_b64 v[90:91], %[va] offset:3080\n\t"
  "ds_read_b64 v[92:93], %[va] offset:3584\n\t"
  "ds_read_b64 v[94:95], %[va] offset:3592\n\t"
  "s_waitcnt vmcnt(0) lgkmcnt(0)\n\t"
  "s_barrier\n\t"
  "ds_read_b128 v[124:127], %[vb0] offset:15360\n\t"
  "ds_read_b128 v[120:123], %[vb0] offset:13312\n\t"
  "ds_read_b128 v[116:119], %[vb0] offset:11264\n\t"
  "ds_read_b128 v[112:115], %[vb0] offset:9216\n\t"
  "ds_read_b128 v[108:111], %[vb0] offset:7168\n\t"
  "ds_read_b128 v[104:107], %[vb0] offset:5120\n\t"
  "ds_read_b128 v[100:103], %[vb0] offset:3072\n\t"
  "ds_read_b128 v[96:99], %[vb0] offset:1024\n\t"
  "v_cmp_eq_u64 s[54:55], v[252:253], v[250:251]\n\t"
  "s_nop 3\n\t"
  "s_cmp_eq_u64 s[54:55], -1\n\t"
  "s_cselect_b32 s53, 1, 0\n\t"
  "s_cselect_b32 s56, %[sW1], 1879048192\n\t"
  "s_mov_b32 %[wdone], s53\n\t"
  "s_waitcnt lgkmcnt(7)\n\t"
  "v_mfma_f32_16x16x32_bf16 v[60:63], v[64:67], v[124:127], 0\n\t"
  "ds_read_b128 v[156:159], %[vb1] offset:15360\n\t"
  "buffer_load_dwordx4 a[0:3], %[vl16], %[srd], s56 offen offset:0 sc1\n\t"
  "s_waitcnt lgkmcnt(7)\n\t"
  "v_mfma_f32_16x16x32_bf16 v[56:59], v[64:67], v[120:123], 0\n\t"
  "s_waitcnt lgkmcnt(6)\n\t"
  "v_mfma_f32_16x16x32_bf16 v[52:55], v[64:67], v[116:119], 0\n\t"
  "buffer_load_dwordx2 v[250:251], %[vfw], %[srd], 0 offen sc1\n\t"
  "s_waitcnt lgkmcnt(5)\n\t"
  "v_mfma_f32_16x16x32_bf16 v[48:51], v[64:67], v[112:115], 0\n\t"
  "s_waitcnt lgkmcnt(4)\n\t"
  "v_mfma_f32_16x16x32_bf16 v[44:47], v[64:67], v[108:111], 0\n\t"
  "ds_read_b128 v[152:155], %[vb1] offset:13312\n\t"
  "s_waitcnt lgkmcnt(4)\n\t"
  "v_mfma_f32_16x16x32_bf16 v[40:43], v[64:67], v[104:107], 0\n\t"
  "buffer_load_dwordx4 a[4:7], %[vl16], %[srd], s56 offen offset:1024 sc1\n\t"
  "s_waitcnt lgkmcnt(3)\n\t"
  "v_mfma_f32_16x16x32_bf16 v[36:39], v[64:67], v[100:103], 0\n\t"
  "s_waitcnt lgkmcnt(2)\n\t"
  "v_mfma_f32_16x16x32_bf16 v[32:35], v[64:67], v[96:99], 0\n\t"
  "v_mfma_f32_16x16x32_bf16 v[60:63], %[vd], v[124:127], v[60:63]\n\t"
  "ds_read_b128 v[148:151], %[vb1] offset:11264\n\t"
  "v_mfma_f32_16x16x32_bf16 v[56:59], %[vd], v[120:123], v[56:59]\n\t"
  "v_mfma_f32_16x16x32_bf16 v[52:55], %[vd], v[116:119], v[52:55]\n\t"
  "buffer_load_dwordx4 a[8:11], %[vl16], %[srd], s56 offen offset:2048 sc1\n\t"
  "v_mfma_f32_16x16x32_bf16 v[48:51], %[vd], v[112:115], v[48:51]\n\t"
  "v_mfma_f32_16x16x32_bf16 v[44:47], %[vd], v[108:111], v[44:47]\n\t"
  "ds_read_b128 v[144:147], %[vb1] offset:9216\n\t"
  "v_mfma_f32_16x16x32_bf16 v[40:43], %[vd], v[104:107], v[40:43]\n\t"
  "v_mfma_f32_16x16x32_bf16 v[36:39], %[vd], v[100:103], v[36:39]\n\t"
  "v_mfma_f32_16x16x32_bf16 v[32:35], %[vd], v[96:99], v[32:35]\n\t"
  "ds_read_b64 v[64:65], %[va] offset:64\n\t"
  "ds_read_b64 v[66:67], %[va] offset:72\n\t"
  "v_mfma_f32_16x16x32_bf16 v[60:63], v[68:71], v[120:123], v[60:63]\n\t"
  "ds_read_b128 v[140:143], %[vb1] offset:7168\n\t"
  "buffer_load_dwordx4 a[12:15], %[vl16], %[srd], s56 offen offset:3072 sc1\n\t"
  "v_mfma_f32_16x16x32_bf16 v[56:59], v[68:71], v[116:119], v[56:59]\n\t"
  "v_mfma_f32_16x16x32_bf16 v[52:55], v[68:71], v[112:115], v[52:55]\n\t"
  "v_mfma_f32_16x16x32_bf16 v[48:51], v[68:71], v[108:111], v[48:51]\n\t"
  "v_mfma_f32_16x16x32_bf16 v[44:47], v[68:71], v[104:107], v[44:47]\n\t"
  "ds_read_b128 v[136:139], %[vb1] offset:5120\n\t"
  "v_mfma_f32_16x16x32_bf16 v[40:43], v[68:71], v[100:103], v[40:43]\n\t"
  "s_add_u32 s56, s56, 4096\n\t"
  "v_mfma_f32_16x16x32_bf16 v[36:39], v[68:71], v[96:99], v[36:39]\n\t"
  "ds_read_b64 v[68:69], %[va] offset:576\n\t"
  "ds_read_b64 v[70:71], %[va] offset:584\n\t"
  "v_mfma_f32_16x16x32_bf16 v[60:63], v[72:75], v[116:119], v[60:63]\n\t"
  "v_mfma_f32_16x16x32_bf16 v[56:59], v[72:75], v[112:115], v[56:59]\n\t"
  "ds_read_b128 v[132:135], %[vb1] offset:3072\n\t"
  "v_mfma_f32_16x16x32_bf16 v[52:55], v[72:75], v[108:111], v[52:55]\n\t"
  "v_mfma_f32_16x16x32_bf16 v[48:51], v[72:75], v[104:107], v[48:51]\n\t"
  "buffer_load_dwordx4 a[16:19], %[vl16], %[srd], s56 offen offset:0 sc1\n\t"
  "v_mfma_f32_16x16x32_bf16 v[44:47], v[72:75], v[100:103], v[44:47]\n\t"
  "v_mfma_f32_16x16x32_bf16 v[40:43], v[72:75], v[96:99], v[40:43]\n\t"
  "ds_read_b128 v[128:131], %[vb1] offset:1024\n\t"
  "ds_read_b64 v[72:73], %[va] offset:1088\n\t"
  "v_mfma_f32_16x16x32_bf16 v[60:63], v[76:79], v[112:115], v[60:63]\n\t"
  "ds_read_b64 v[74:75], %[va] offset:1096\n\t"
  "v_mfma_f32_16x16x32_bf16 v[56:59], v[76:79], v[108:111], v[56:59]\n\t"
  "v_mfma_f32_16x16x32_bf16 v[52:55], v[76:79], v[104:107], v[52:55]\n\t"
  "v_mfma_f32_16x16x32_bf16 v[48:51], v[76:79], v[100:103], v[48:51]\n\t"
  "buffer_load_dwordx4 a[20:23], %[vl16], %[srd], s56 offen offset:1024 sc1\n\t"
  "v_mfma_f32_16x16x32_bf16 v[44:47], v[76:79], v[96:99], v[44:47]\n\t"
  "ds_read_b64 v[76:77], %[va] offset:1600\n\t"
  "ds_read_b64 v[78:79], %[va] offset:1608\n\t"
  "v_mfma_f32_16x16x32_bf16 v[60:63], v[80:83], v[108:111], v[60:63]\n\t"
  "v_mfma_f32_16x16x32_bf16 v[56:59], v[80:83], v[104:107], v[56:59]\n\t"
  "v_mfma_f32_16x16x32_bf16 v[52:55], v[80:83], v[100:103], v[52:55]\n\t"
  "v_mfma_f32_16x16x32_bf16 v[48:51], v[80:83], v[96:99], v[48:51]\n\t"
  "ds_read_b64 v[80:81], %[va] offset:2112\n\t"
  "ds_read_b64 v[82:83], %[va] offset:2120\n\t"
  "v_mfma_f32_16x16x32_bf16 v[60:63], v[84:87], v[104:107], v[60:63]\n\t"
  "buffer_load_dwordx4 a[24:27], %[vl16], %[srd], s56 offen offset:2048 sc1\n\t"
  "v_mfma_f32_16x16x32_bf16 v[56:59], v[84:87], v[100:103], v[56:59]\n\t"
  "v_mfma_f32_16x16x32_bf16 v[52:55], v[84:87], v[96:99], v[52:55]\n\t"
  "ds_read_b64 v[84:85], %[va] offset:2624\n\t"
  "ds_read_b64 v[86:87], %[va] offset:2632\n\t"
  "v_mfma_f32_16x16x32_bf16 v[60:63], v[88:91], v[100:103], v[60:63]\n\t"
  "v_mfma_f32_16x16x32_bf16 v[56:59], v[88:91], v[96:99], v[56:59]\n\t"
  "ds_read_b64 v[88:89], %[va] offset:3136\n\t"
  "ds_read_b64 v[90:91], %[va] offset:3144\n\t"
  "v_mfma_f32_16x16x32_bf16 v[60:63], v[92:95], v[96:99], v[60:63]\n\t"
  "s_waitcnt lgkmcnt(10)\n\t"
  "ds_read_b64 v[92:93], %[va] offset:3648\n\t"
  "v_mfma_f32_16x16x32_bf16 v[60:63], v[64:67], v[156:159], v[60:63]\n\t"
  "ds_read_b128 v[124:127], %[vb2] offset:15360\n\t"
  "ds_read_b64 v[94:95], %[va] offset:3656\n\t"
  "v_mfma_f32_16x16x32_bf16 v[56:59], v[64:67], v[152:155], v[56:59]\n\t"
  "s_waitcnt vmcnt(6)\n\t"
  "v_cmp_eq_u64 s[54:55], v[252:253], v[250:251]\n\t"
  "v_mfma_f32_16x16x32_bf16 v[52:55], v[64:67], v[148:151], v[52:55]\n\t"
  "buffer_load_dwordx4 a[28:31], %[vl16], %[srd], s56 offen offset:3072 sc1\n\t"
  "v_mfma_f32_16x16x32_bf16 v[48:51], v[64:67], v[144:147], v[48:51]\n\t"
  "ds_read_b128 v[120:123], %[vb2] offset:13312\n\t"
  "v_mfma_f32_16x16x32_bf16 v[44:47], v[64:67], v[140:143], v[44:47]\n\t"
  "s_cmp_eq_u64 s[54:55], -1\n\t"
  "s_cselect_b32 s53, 2, 0\n\t"
  "s_cselect_b32 s57, %[sW1], 1879048192\n\t"
  "v_mfma_f32_16x16x32_bf16 v[40:43], v[64:67], v[136:139], v[40:43]\n\t"
  "s_or_b32 %[wdone], %[wdone], s53\n\t"
  "buffer_load_dwordx2 v[250:251], %[vfw], %[srd], 0 offen sc1\n\t"
  "v_mfma_f32_16x16x32_bf16 v[36:39], v[64:67], v[132:135], v[36:39]\n\t"
  "ds_read_b128 v[116:119], %[vb2] offset:11264\n\t"
  "s_add_u32 s57, s57, 8192\n\t"
  "v_mfma_f32_16x16x32_bf16 v[32:35], v[64:67], v[128:131], v[32:35]\n\t"
  "ds_read_b64 v[64:65], %[va] offset:128\n\t"
  "ds_read_b64 v[66:67], %[va] offset:136\n\t"
  "v_mfma_f32_16x16x32_bf16 v[60:63], v[68:71], v[152:155], v[60:63]\n\t"
  "buffer_load_dwordx4 a[32:35], %[vl16], %[srd], s57 offen offset:0 sc1\n\t"
  "v_mfma_f32_16x16x32_bf16 v[56:59], v[68:71], v[148:151], v[56:59]\n\t"
  "ds_read_b128 v[112:115], %[vb2] offset:9216\n\t"
  "v_mfma_f32_16x16x32_bf16 v[52:55], v[68:71], v[144:147], v[52:55]\n\t"
  "v_mfma_f32_16x16x32_bf16 v[48:51], v[68:71], v[140:143], v[48:51]\n\t"
  "v_mfma_f32_16x16x32_bf16 v[44:47], v[68:71], v[136:139], v[44:47]\n\t"
  "ds_read_b128 v[108:111], %[vb2] offset:7168\n\t"
  "buffer_load_dwordx4 a[36:39], %[vl16], %[srd], s57 offen offset:1024 sc1\n\t"
  "v_mfma_f32_16x16x32_bf16 v[40:43], v[68:71], v[132:135], v[40:43]\n\t"
  "v_mfma_f32_16x16x32_bf16 v[36:39], v[68:71], v[128:131], v[36:39]\n\t"
  "s_waitcnt lgkmcnt(11)\n\t"
  "ds_read_b64 v[68:69], %[va] offset:640\n\t"
  "v_mfma_f32_16x16x32_bf16 v[60:63], v[72:75], v[148:151], v[60:63]\n\t"
  "ds_read_b128 v[104:107], %[vb2] offset:5120\n\t"
  "ds_read_b64 v[70:71], %[va] offset:648\n\t"
  "v_mfma_f32_16x16x32_bf16 v[56:59], v[72:75], v[144:147], v[56:59]\n\t"
  "v_mfma_f32_16x16x32_bf16 v[52:55], v[72:75], v[140:143], v[52:55]\n\t"
  "buffer_load_dwordx4 a[40:43], %[vl16], %[srd], s57 offen offset:2048 sc1\n\t"
  "v_mfma_f32_16x16x32_bf16 v[48:51], v[72:75], v[136:139], v[48:51]\n\t"
  "ds_read_b128 v[100:103], %[vb2] offset:3072\n\t"
  "v_mfma_f32_16x16x32_bf16 v[44:47], v[72:75], v[132:135], v[44:47]\n\t"
  "v_mfma_f32_16x16x32_bf16 v[40:43], v[72:75], v[128:131], v[40:43]\n\t"
  "ds_read_b64 v[72:73], %[va] offset:1152\n\t"
  "ds_read_b64 v[74:75], %[va] offset:1160\n\t"
  "v_mfma_f32_16x16x32_bf16 v[60:63], v[76:79], v[144:147], v[60:63]\n\t"
  "ds_read_b128 v[96:99], %[vb2] offset:1024\n\t"
  "buffer_load_dwordx4 a[44:47], %[vl16], %[srd], s57 offen offset:3072 sc1\n\t"
  "v_mfma_f32_16x16x32_bf16 v[56:59], v[76:79], v[140:143], v[56:59]\n\t"
  "v_mfma_f32_16x16x32_bf16 v[52:55], v[76:79], v[136:139], v[52:55]\n\t"
  "v_mfma_f32_16x16x32_bf16 v[48:51], v[76:79], v[132:135], v[48:51]\n\t"
  "v_mfma_f32_16x16x32_bf16 v[44:47], v[76:79], v[128:131], v[44:47]\n\t"
  "ds_read_b64 v[76:77], %[va] offset:1664\n\t"
  "ds_read_b64 v[78:79], %[va] offset:1672\n\t"
  "v_mfma_f32_16x16x32_bf16 v[60:63], v[80:83], v[140:143], v[60:63]\n\t"
  "s_add_u32 s57, s57, 4096\n\t"
  "v_mfma_f32_16x16x32_bf16 v[56:59], v[80:83], v[136:139], v[56:59]\n\t"
  "v_mfma_f32_16x16x32_bf16 v[52:55], v[80:83], v[132:135], v[52:55]\n\t"
  "v_mfma_f32_16x16x32_bf16 v[48:51], v[80:83], v[128:131], v[48:51]\n\t"
  "ds_read_b64 v[80:81], %[va] offset:2176\n\t"
  "ds_read_b64 v[82:83], %[va] offset:2184\n\t"
  "v_mfma_f32_16x16x32_bf16 v[60:63], v[84:87], v[136:139], v[60:63]\n\t"
  "buffer_load_dwordx4 a[48:51], %[vl16], %[srd], s57 offen offset:0 sc1\n\t"
  "v_mfma_f32_16x16x32_bf16 v[56:59], v[84:87], v[132:135], v[56:59]\n\t"
  "v_mfma_f32_16x16x32_bf16 v[52:55], v[84:87], v[128:131], v[52:55]\n\t"
  "s_waitcnt lgkmcnt(15)\n\t"
  "ds_read_b64 v[84:85], %[va] offset:2688\n\t"
  "v_mfma_f32_16x16x32_bf16 v[60:63], v[88:91], v[132:135], v[60:63]\n\t"
  "ds_read_b64 v[86:87], %[va] offset:2696\n\t"
  "v_mfma_f32_16x16x32_bf16 v[56:59], v[88:91], v[128:131], v[56:59]\n\t"
  "ds_read_b64 v[88:89], %[va] offset:3200\n\t"
  "ds_read_b64 v[90:91], %[va] offset:3208\n\t"
  "v_mfma_f32_16x16x32_bf16 v[60:63], v[92:95], v[128:131], v[60:63]\n\t"
  "s_waitcnt lgkmcnt(8)\n\t"
  "ds_read_b64 v[92:93], %[va] offset:3712\n\t"
  "v_mfma_f32_16x16x32_bf16 v[60:63], v[64:67], v[124:127], v[60:63]\n\t"
  "ds_read_b128 v[156:159], %[vb3] offset:15360\n\t"
  "ds_read_b64 v[94:95], %[va] offset:3720\n\t"
  "v_mfma_f32_16x16x32_bf16 v[56:59], v[64:67], v[120:123], v[56:59]\n\t"
  "buffer_load_dwordx4 a[52:55], %[vl16], %[srd], s57 offen offset:1024 sc1\n\t"
  "v_mfma_f32_16x16x32_bf16 v[52:55], v[64:67], v[116:119], v[52:55]\n\t"
  "v_mfma_f32_16x16x32_bf16 v[48:51], v[64:67], v[112:115], v[48:51]\n\t"
  "ds_read_b128 v[152:155], %[vb3] offset:13312\n\t"
  "buffer_load_dwordx4 a[56:59], %[vl16], %[srd], s57 offen offset:2048 sc1\n\t"
  "v_mfma_f32_16x16x32_bf16 v[44:47], v[64:67], v[108:111], v[44:47]\n\t"
  "v_mfma_f32_16x16x32_bf16 v[40:43], v[64:67], v[104:107], v[40:43]\n\t"
  "v_mfma_f32_16x16x32_bf16 v[36:39], v[64:67], v[100:103], v[36:39]\n\t"
  "ds_read_b128 v[148:151], %[vb3] offset:11264\n\t"
  "v_mfma_f32_16x16x32_bf16 v[32:35], v[64:67], v[96:99], v[32:35]\n\t"
  "ds_read_b64 v[64:65], %[va] offset:192\n\t"
  "ds_read_b64 v[66:67], %[va] offset:200\n\t"
  "v_mfma_f32_16x16x32_bf16 v[60:63], v[68:71], v[120:123], v[60:63]\n\t"
  "buffer_load_dwordx4 a[60:63], %[vl16], %[srd], s57 offen offset:3072 sc1\n\t"
  "v_mfma_f32_16x16x32_bf16 v[56:59], v[68:71], v[116:119], v[56:59]\n\t"
  "ds_read_b128 v[144:147], %[vb3] offset:9216\n\t"
  "s_waitcnt vmcnt(8)\n\t"
  "v_cmp_eq_u64 s[54:55], v[252:253], v[250:251]\n\t"
  "v_mfma_f32_16x16x32_bf16 v[52:55], v[68:71], v[112:115], v[52:55]\n\t"
  "v_mfma_f32_16x16x32_bf16 v[48:51], v[68:71], v[108:111], v[48:51]\n\t"
  "v_mfma_f32_16x16x32_bf16 v[44:47], v[68:71], v[104:107], v[44:47]\n\t"
  "ds_read_b128 v[140:143], %[vb3] offset:7168\n\t"
  "s_cmp_eq_u64 s[54:55], -1\n\t"
  "s_cselect_b32 s53, 4, 0\n\t"
  "s_cselect_b32 s58, %[sW1], 1879048192\n\t"
  "v_mfma_f32_16x16x32_bf16 v[40:43], v[68:71], v[100:103], v[40:43]\n\t"
  "s_or_b32 %[wdone], %[wdone], s53\n\t"
  "s_add_u32 s58, s58, 16384\n\t"
  "v_mfma_f32_16x16x32_bf16 v[36:39], v[68:71], v[96:99], v[36:39]\n\t"
  "s_waitcnt lgkmcnt(11)\n\t"
  "ds_read_b64 v[68:69], %[va] offset:704\n\t"
  "v_mfma_f32_16x16x32_bf16 v[60:63], v[72:75], v[116:119], v[60:63]\n\t"
  "ds_read_b128 v[136:139], %[vb3] offset:5120\n\t"
  "ds_read_b64 v[70:71], %[va] offset:712\n\t"
  "v_mfma_f32_16x16x32_bf16 v[56:59], v[72:75], v[112:115], v[56:59]\n\t"
  "buffer_load_dwordx4 a[64:67], %[vl16], %[srd], s58 offen offset:0 sc1\n\t"
  "v_mfma_f32_16x16x32_bf16 v[52:55], v[72:75], v[108:111], v[52:55]\n\t"
  "v_mfma_f32_16x16x32_bf16 v[48:51], v[72:75], v[104:107], v[48:51]\n\t"
  "ds_read_b128 v[132:135], %[vb3] offset:3072\n\t"
  "buffer_load_dwordx4 a[68:71], %[vl16], %[srd], s58 offen offset:1024 sc1\n\t"
  "v_mfma_f32_16x16x32_bf16 v[44:47], v[72:75], v[100:103], v[44:47]\n\t"
  "v_mfma_f32_16x16x32_bf16 v[40:43], v[72:75], v[96:99], v[40:43]\n\t"
  "ds_read_b64 v[72:73], %[va] offset:1216\n\t"
  "ds_read_b64 v[74:75], %[va] offset:1224\n\t"
  "v_mfma_f32_16x16x32_bf16 v[60:63], v[76:79], v[112:115], v[60:63]\n\t"
  "ds_read_b128 v[128:131], %[vb3] offset:1024\n\t"
  "v_mfma_f32_16x16x32_bf16 v[56:59], v[76:79], v[108:111], v[56:59]\n\t"
  "buffer_load_dwordx4 a[72:75], %[vl16], %[srd], s58 offen offset:2048 sc1\n\t"
  "v_mfma_f32_16x16x32_bf16 v[52:55], v[76:79], v[104:107], v[52:55]\n\t"
  "v_mfma_f32_16x16x32_bf16 v[48:51], v[76:79], v[100:103], v[48:51]\n\t"
  "v_mfma_f32_16x16x32_bf16 v[44:47], v[76:79], v[96:99], v[44:47]\n\t"
  "ds_read_b64 v[76:77], %[va] offset:1728\n\t"
  "ds_read_b64 v[78:79], %[va] offset:1736\n\t"
  "v_mfma_f32_16x16x32_bf16 v[60:63], v[80:83], v[108:111], v[60:63]\n\t"
  "buffer_load_dwordx4 a[76:79], %[vl16], %[srd], s58 offen offset:3072 sc1\n\t"
  "v_mfma_f32_16x16x32_bf16 v[56:59], v[80:83], v[104:107], v[56:59]\n\t"
  "v_mfma_f32_16x16x32_bf16 v[52:55], v[80:83], v[100:103], v[52:55]\n\t"
  "s_add_u32 s58, s58, 4096\n\t"
  "v_mfma_f32_16x16x32_bf16 v[48:51], v[80:83], v[96:99], v[48:51]\n\t"
  "ds_read_b64 v[80:81], %[va] offset:2240\n\t"
  "ds_read_b64 v[82:83], %[va] offset:2248\n\t"
  "v_mfma_f32_16x16x32_bf16 v[60:63], v[84:87], v[104:107], v[60:63]\n\t"
  "v_mfma_f32_16x16x32_bf16 v[56:59], v[84:87], v[100:103], v[56:59]\n\t"
  "v_mfma_f32_16x16x32_bf16 v[52:55], v[84:87], v[96:99], v[52:55]\n\t"
  "s_waitcnt lgkmcnt(15)\n\t"
  "ds_read_b64 v[84:85], %[va] offset:2752\n\t"
  "v_mfma_f32_16x16x32_bf16 v[60:63], v[88:91], v[100:103], v[60:63]\n\t"
  "ds_read_b64 v[86:87], %[va] offset:2760\n\t"
  "buffer_load_dwordx4 a[80:83], %[vl16], %[srd], s58 offen offset:0 sc1\n\t"
  "v_mfma_f32_16x16x32_bf16 v[56:59], v[88:91], v[96:99], v[56:59]\n\t"
  "ds_read_b64 v[88:89], %[va] offset:3264\n\t"
  "ds_read_b64 v[90:91], %[va] offset:3272\n\t"
  "v_mfma_f32_16x16x32_bf16 v[60:63], v[92:95], v[96:99], v[60:63]\n\t"
  "s_waitcnt lgkmcnt(8)\n\t"
  "ds_read_b64 v[92:93], %[va] offset:3776\n\t"
  "v_mfma_f32_16x16x32_bf16 v[60:63], v[64:67], v[156:159], v[60:63]\n\t"
  "ds_read_b128 v[124:127], %[vb0] offset:14336\n\t"
  "ds_read_b64 v[94:95], %[va] offset:3784\n\t"
  "v_mfma_f32_16x16x32_bf16 v[56:59], v[64:67], v[152:155], v[56:59]\n\t"
  "buffer_load_dwordx4 a[84:87], %[vl16], %[srd], s58 offen offset:1024 sc1\n\t"
  "v_mfma_f32_16x16x32_bf16 v[52:55], v[64:67], v[148:151], v[52:55]\n\t"
  "v_mfma_f32_16x16x32_bf16 v[48:51], v[64:67], v[144:147], v[48:51]\n\t"
  "ds_read_b128 v[120:123], %[vb0] offset:12288\n\t"
  "buffer_load_dwordx4 a[88:91], %[vl16], %[srd], s58 offen offset:2048 sc1\n\t"
  "v_mfma_f32_16x16x32_bf16 v[44:47], v[64:67], v[140:143], v[44:47]\n\t"
  "v_mfma_f32_16x16x32_bf16 v[40:43], v[64:67], v[136:139], v[40:43]\n\t"
  "v_mfma_f32_16x16x32_bf16 v[36:39], v[64:67], v[132:135], v[36:39]\n\t"
  "ds_read_b128 v[116:119], %[vb0] offset:10240\n\t"
  "buffer_load_dwordx4 a[92:95], %[vl16], %[srd], s58 offen offset:3072 sc1\n\t"
  "v_mfma_f32_16x16x32_bf16 v[32:35], v[64:67], v[128:131], v[32:35]\n\t"
  "ds_read_b64 v[64:65], %[va] offset:256\n\t"
  "ds_read_b64 v[66:67], %[va] offset:264\n\t"
  "v_mfma_f32_16x16x32_bf16 v[60:63], v[68:71], v[152:155], v[60:63]\n\t"
  "v_mfma_f32_16x16x32_bf16 v[56:59], v[68:71], v[148:151], v[56:59]\n\t"
  "ds_read_b128 v[112:115], %[vb0] offset:8192\n\t"
  "s_add_u32 s58, s58, 4096\n\t"
  "v_mfma_f32_16x16x32_bf16 v[52:55], v[68:71], v[144:147], v[52:55]\n\t"
  "v_mfma_f32_16x16x32_bf16 v[48:51], v[68:71], v[140:143], v[48:51]\n\t"
  "v_mfma_f32_16x16x32_bf16 v[44:47], v[68:71], v[136:139], v[44:47]\n\t"
  "ds_read_b128 v[108:111], %[vb0] offset:6144\n\t"
  "v_mfma_f32_16x16x32_bf16 v[40:43], v[68:71], v[132:135], v[40:43]\n\t"
  "buffer_load_dwordx4 a[96:99], %[vl16], %[srd], s58 offen offset:0 sc1\n\t"
  "v_mfma_f32_16x16x32_bf16 v[36:39], v[68:71], v[128:131], v[36:39]\n\t"
  "s_waitcnt lgkmcnt(11)\n\t"
  "ds_read_b64 v[68:69], %[va] offset:768\n\t"
  "v_mfma_f32_16x16x32_bf16 v[60:63], v[72:75], v[148:151], v[60:63]\n\t"
  "ds_read_b128 v[104:107], %[vb0] offset:4096\n\t"
  "ds_read_b64 v[70:71], %[va] offset:776\n\t"
  "v_mfma_f32_16x16x32_bf16 v[56:59], v[72:75], v[144:147], v[56:59]\n\t"
  "buffer_load_dwordx4 a[100:103], %[vl16], %[srd], s58 offen offset:1024 sc1\n\t"
  "v_mfma_f32_16x16x32_bf16 v[52:55], v[72:75], v[140:143], v[52:55]\n\t"
  "v_mfma_f32_16x16x32_bf16 v[48:51], v[72:75], v[136:139], v[48:51]\n\t"
  "ds_read_b128 v[100:103], %[vb0] offset:2048\n\t"
  "v_mfma_f32_16x16x32_bf16 v[44:47], v[72:75], v[132:135], v[44:47]\n\t"
  "v_mfma_f32_16x16x32_bf16 v[40:43], v[72:75], v[128:131], v[40:43]\n\t"
  "ds_read_b64 v[72:73], %[va] offset:1280\n\t"
  "ds_read_b64 v[74:75], %[va] offset:1288\n\t"
  "v_mfma_f32_16x16x32_bf16 v[60:63], v[76:79], v[144:147], v[60:63]\n\t"
  "ds_read_b128 v[96:99], %[vb0] offset:0\n\t"
  "v_mfma_f32_16x16x32_bf16 v[56:59], v[76:79], v[140:143], v[56:59]\n\t"
  "v_mfma_f32_16x16x32_bf16 v[52:55], v[76:79], v[136:139], v[52:55]\n\t"
  "v_mfma_f32_16x16x32_bf16 v[48:51], v[76:79], v[132:135], v[48:51]\n\t"
  "buffer_load_dwordx2 v[162:163], %[vf2], %[srd], 0 offen sc1\n\t"
  "v_mfma_f32_16x16x32_bf16 v[44:47], v[76:79], v[128:131], v[44:47]\n\t"
  "ds_read_b64 v[76:77], %[va] offset:1792\n\t"
  "ds_read_b64 v[78:79], %[va] offset:1800\n\t"
  "v_mfma_f32_16x16x32_bf16 v[60:63], v[80:83], v[140:143], v[60:63]\n\t"
  "v_mfma_f32_16x16x32_bf16 v[56:59], v[80:83], v[136:139], v[56:59]\n\t"
  "v_mfma_f32_16x16x32_bf16 v[52:55], v[80:83], v[132:135], v[52:55]\n\t"
  "v_mfma_f32_16x16x32_bf16 v[48:51], v[80:83], v[128:131], v[48:51]\n\t"
  "ds_read_b64 v[80:81], %[va] offset:2304\n\t"
  "ds_read_b64 v[82:83], %[va] offset:2312\n\t"
  "v_mfma_f32_16x16x32_bf16 v[60:63], v[84:87], v[136:139], v[60:63]\n\t"
  "v_mfma_f32_16x16x32_bf16 v[56:59], v[84:87], v[132:135], v[56:59]\n\t"
  "v_mfma_f32_16x16x32_bf16 v[52:55], v[84:87], v[128:131], v[52:55]\n\t"
  "s_waitcnt lgkmcnt(15)\n\t"
  "ds_read_b64 v[84:85], %[va] offset:2816\n\t"
  "v_mfma_f32_16x16x32_bf16 v[60:63], v[88:91], v[132:135], v[60:63]\n\t"
  "ds_read_b64 v[86:87], %[va] offset:2824\n\t"
  "v_mfma_f32_16x16x32_bf16 v[56:59], v[88:91], v[128:131], v[56:59]\n\t"
  "ds_read_b64 v[88:89], %[va] offset:3328\n\t"
  "ds_read_b64 v[90:91], %[va] offset:3336\n\t"
  "v_mfma_f32_16x16x32_bf16 v[60:63], v[92:95], v[128:131], v[60:63]\n\t"
  "s_waitcnt lgkmcnt(8)\n\t"
  "ds_read_b64 v[92:93], %[va] offset:3840\n\t"
  "v_mfma_f32_16x16x32_bf16 v[60:63], v[64:67], v[124:127], v[60:63]\n\t"
  "ds_read_b128 v[156:159], %[vb1] offset:14336\n\t"
  "ds_read_b64 v[94:95], %[va] offset:3848\n\t"
  "v_mfma_f32_16x16x32_bf16 v[56:59], v[64:67], v[120:123], v[56:59]\n\t"
  "v_mfma_f32_16x16x32_bf16 v[52:55], v[64:67], v[116:119], v[52:55]\n\t"
  "v_mfma_f32_16x16x32_bf16 v[48:51], v[64:67], v[112:115], v[48:51]\n\t"
  "ds_read_b128 v[152:155], %[vb1] offset:12288\n\t"
  "v_mfma_f32_16x16x32_bf16 v[44:47], v[64:67], v[108:111], v[44:47]\n\t"
  "v_mfma_f32_16x16x32_bf16 v[40:43], v[64:67], v[104:107], v[40:43]\n\t"
  "v_mfma_f32_16x16x32_bf16 v[36:39], v[64:67], v[100:103], v[36:39]\n\t"
  "ds_read_b128 v[148:151], %[vb1] offset:10240\n\t"
  "v_mfma_f32_16x16x32_bf16 v[32:35], v[64:67], v[96:99], v[32:35]\n\t"
  "ds_read_b64 v[64:65], %[va] offset:320\n\t"
  "ds_read_b64 v[66:67], %[va] offset:328\n\t"
  "v_mfma_f32_16x16x32_bf16 v[60:63], v[68:71], v[120:123], v[60:63]\n\t"
  "v_mfma_f32_16x16x32_bf16 v[56:59], v[68:71], v[116:119], v[56:59]\n\t"
  "ds_read_b128 v[144:147], %[vb1] offset:8192\n\t"
  "v_mfma_f32_16x16x32_bf16 v[52:55], v[68:71], v[112:115], v[52:55]\n\t"
  "v_mfma_f32_16x16x32_bf16 v[48:51], v[68:71], v[108:111], v[48:51]\n\t"
  "v_mfma_f32_16x16x32_bf16 v[44:47], v[68:71], v[104:107], v[44:47]\n\t"
  "ds_read_b128 v[140:143], %[vb1] offset:6144\n\t"
  "buffer_load_dwordx2 v[250:251], %[vfw], %[srd], 0 offen sc1\n\t"
  "v_mfma_f32_16x16x32_bf16 v[40:43], v[68:71], v[100:103], v[40:43]\n\t"
  "v_mfma_f32_16x16x32_bf16 v[36:39], v[68:71], v[96:99], v[36:39]\n\t"
  "s_waitcnt lgkmcnt(11)\n\t"
  "ds_read_b64 v[68:69], %[va] offset:832\n\t"
  "v_mfma_f32_16x16x32_bf16 v[60:63], v[72:75], v[116:119], v[60:63]\n\t"
  "ds_read_b128 v[136:139], %[vb1] offset:4096\n\t"
  "ds_read_b64 v[70:71], %[va] offset:840\n\t"
  "v_mfma_f32_16x16x32_bf16 v[56:59], v[72:75], v[112:115], v[56:59]\n\t"
  "v_mfma_f32_16x16x32_bf16 v[52:55], v[72:75], v[108:111], v[52:55]\n\t"
  "v_mfma_f32_16x16x32_bf16 v[48:51], v[72:75], v[104:107], v[48:51]\n\t"
  "ds_read_b128 v[132:135], %[vb1] offset:2048\n\t"
  "v_mfma_f32_16x16x32_bf16 v[44:47], v[72:75], v[100:103], v[44:47]\n\t"
  "v_mfma_f32_16x16x32_bf16 v[40:43], v[72:75], v[96:99], v[40:43]\n\t"
  "ds_read_b64 v[72:73], %[va] offset:1344\n\t"
  "ds_read_b64 v[74:75], %[va] offset:1352\n\t"
  "v_mfma_f32_16x16x32_bf16 v[60:63], v[76:79], v[112:115], v[60:63]\n\t"
  "ds_read_b128 v[128:131], %[vb1] offset:0\n\t"
  "v_mfma_f32_16x16x32_bf16 v[56:59], v[76:79], v[108:111], v[56:59]\n\t"
  "v_mfma_f32_16x16x32_bf16 v[52:55], v[76:79], v[104:107], v[52:55]\n\t"
  "v_mfma_f32_16x16x32_bf16 v[48:51], v[76:79], v[100:103], v[48:51]\n\t"
  "v_mfma_f32_16x16x32_bf16 v[44:47], v[76:79], v[96:99], v[44:47]\n\t"
  "ds_read_b64 v[76:77], %[va] offset:1856\n\t"
  "ds_read_b64 v[78:79], %[va] offset:1864\n\t"
  "v_mfma_f32_16x16x32_bf16 v[60:63], v[80:83], v[108:111], v[60:63]\n\t"
  "v_mfma_f32_16x16x32_bf16 v[56:59], v[80:83], v[104:107], v[56:59]\n\t"
  "v_mfma_f32_16x16x32_bf16 v[52:55], v[80:83], v[100:103], v[52:55]\n\t"
  "s_waitcnt vmcnt(1)\n\t"
  "v_cmp_ne_u64 vcc, v[252:253], v[162:163]\n\t"
  "v_mfma_f32_16x16x32_bf16 v[48:51], v[80:83], v[96:99], v[48:51]\n\t"
  "ds_read_b64 v[80:81], %[va] offset:2368\n\t"
  "ds_read_b64 v[82:83], %[va] offset:2376\n\t"
  "v_mfma_f32_16x16x32_bf16 v[60:63], v[84:87], v[104:107], v[60:63]\n\t"
  "s_cbranch_vccnz La2_slow%=\n\t"
  "La2_back%=:\n\t"
  "v_mfma_f32_16x16x32_bf16 v[56:59], v[84:87], v[100:103], v[56:59]\n\t"
  "buffer_load_dword v244, %[vmod], %[srd], 0 offen sc1\n\t"
  "s_add_u32 m0, s45, 84992\n\t"
  "v_mfma_f32_16x16x32_bf16 v[52:55], v[84:87], v[96:99], v[52:55]\n\t"
  "s_waitcnt lgkmcnt(15)\n\t"
  "ds_read_b64 v[84:85], %[va] offset:2880\n\t"
  "v_mfma_f32_16x16x32_bf16 v[60:63], v[88:91], v[100:103], v[60:63]\n\t"
  "ds_read_b64 v[86:87], %[va] offset:2888\n\t"
  "s_add_u32 s44, %[sKV], 2621440\n\t"
  "v_mfma_f32_16x16x32_bf16 v[56:59], v[88:91], v[96:99], v[56:59]\n\t"
  "ds_read_b64 v[88:89], %[va] offset:3392\n\t"
  "ds_read_b64 v[90:91], %[va] offset:3400\n\t"
  "v_mfma_f32_16x16x32_bf16 v[60:63], v[92:95], v[96:99], v[60:63]\n\t"
  "s_waitcnt lgkmcnt(8)\n\t"
  "ds_read_b64 v[92:93], %[va] offset:3904\n\t"
  "v_mfma_f32_16x16x32_bf16 v[60:63], v[64:67], v[156:159], v[60:63]\n\t"
  "ds_read_b128 v[124:127], %[vb2] offset:14336\n\t"
  "ds_read_b64 v[94:95], %[va] offset:3912\n\t"
  "v_mfma_f32_16x16x32_bf16 v[56:59], v[64:67], v[152:155], v[56:59]\n\t"
  "buffer_load_dwordx4 %[vkv], %[srd], s44 offen sc1 lds\n\t"
  "s_add_u32 m0, s45, 86016\n\t"
  "v_mfma_f32_16x16x32_bf16 v[52:55], v[64:67], v[148:151], v[52:55]\n\t"
  "s_add_u32 s44, %[sKV], 2785280\n\t"
  "buffer_load_dwordx4 %[vkv], %[srd], s44 offen sc1 lds\n\t"
  "v_mfma_f32_16x16x32_bf16 v[48:51], v[64:67], v[144:147], v[48:51]\n\t"
  "ds_read_b128 v[120:123], %[vb2] offset:12288\n\t"
  "s_add_u32 m0, s45, 87040\n\t"
  "v_mfma_f32_16x16x32_bf16 v[44:47], v[64:67], v[140:143], v[44:47]\n\t"
  "s_add_u32 s44, %[sKV], 2949120\n\t"
  "buffer_load_dwordx4 %[vkv], %[srd], s44 offen sc1 lds\n\t"
  "v_mfma_f32_16x16x32_bf16 v[40:43], v[64:67], v[136:139], v[40:43]\n\t"
  "s_add_u32 m0, s45, 88064\n\t"
  "s_add_u32 s44, %[sKV], 3112960\n\t"
  "v_mfma_f32_16x16x32_bf16 v[36:39], v[64:67], v[132:135], v[36:39]\n\t"
  "ds_read_b128 v[116:119], %[vb2] offset:10240\n\t"
  "buffer_load_dwordx4 %[vkv], %[srd], s44 offen sc1 lds\n\t"
  "v_mfma_f32_16x16x32_bf16 v[32:35], v[64:67], v[128:131], v[32:35]\n\t"
  "ds_read_b64 v[64:65], %[va] offset:384\n\t"
  "ds_read_b64 v[66:67], %[va] offset:392\n\t"
  "v_mfma_f32_16x16x32_bf16 v[60:63], v[68:71], v[152:155], v[60:63]\n\t"
  "s_add_u32 m0, s45, 101376\n\t"
  "s_add_u32 s44, s45, 1159168\n\t"
  "v_mfma_f32_16x16x32_bf16 v[56:59], v[68:71], v[148:151], v[56:59]\n\t"
  "ds_read_b128 v[112:115], %[vb2] offset:8192\n\t"
  "buffer_load_dwordx4 %[vl16], %[srd], s44 offen offset:0 sc1 lds\n\t"
  "v_mfma_f32_16x16x32_bf16 v[52:55], v[68:71], v[144:147], v[52:55]\n\t"
  "buffer_load_dwordx4 %[vl16], %[srd], s44 offen offset:1024 sc1 lds\n\t"
  "buffer_load_dwordx4 %[vl16], %[srd], s44 offen offset:2048 sc1 lds\n\t"
  "v_mfma_f32_16x16x32_bf16 v[48:51], v[68:71], v[140:143], v[48:51]\n\t"
  "buffer_load_dwordx4 %[vl16], %[srd], s44 offen offset:3072 sc1 lds\n\t"
  "v_mfma_f32_16x16x32_bf16 v[44:47], v[68:71], v[136:139], v[44:47]\n\t"
  "ds_read_b128 v[108:111], %[vb2] offset:6144\n\t"
  "v_mfma_f32_16x16x32_bf16 v[40:43], v[68:71], v[132:135], v[40:43]\n\t"
  "v_mfma_f32_16x16x32_bf16 v[36:39], v[68:71], v[128:131], v[36:39]\n\t"
  "s_waitcnt lgkmcnt(11)\n\t"
  "ds_read_b64 v[68:69], %[va] offset:896\n\t"
  "v_mfma_f32_16x16x32_bf16 v[60:63], v[72:75], v[148:151], v[60:63]\n\t"
  "ds_read_b128 v[104:107], %[vb2] offset:4096\n\t"
  "ds_read_b64 v[70:71], %[va] offset:904\n\t"
  "v_mfma_f32_16x16x32_bf16 v[56:59], v[72:75], v[144:147], v[56:59]\n\t"
  "s_waitcnt vmcnt(9)\n\t"
  "v_cmp_eq_u64 s[54:55], v[252:253], v[250:251]\n\t"
  "v_mfma_f32_16x16x32_bf16 v[52:55], v[72:75], v[140:143], v[52:55]\n\t"
  "v_mfma_f32_16x16x32_bf16 v[48:51], v[72:75], v[136:139], v[48:51]\n\t"
  "ds_read_b128 v[100:103], %[vb2] offset:2048\n\t"
  "v_mfma_f32_16x16x32_bf16 v[44:47], v[72:75], v[132:135], v[44:47]\n\t"
  "s_cmp_eq_u64 s[54:55], -1\n\t"
  "s_cselect_b32 s53, 8, 0\n\t"
  "s_cselect_b32 s59, %[sW1], 1879048192\n\t"
  "v_mfma_f32_16x16x32_bf16 v[40:43], v[72:75], v[128:131], v[40:43]\n\t"
  "ds_read_b64 v[72:73], %[va] offset:1408\n\t"
  "ds_read_b64 v[74:75], %[va] offset:1416\n\t"
  "v_mfma_f32_16x16x32_bf16 v[60:63], v[76:79], v[144:147], v[60:63]\n\t"
  "ds_read_b128 v[96:99], %[vb2] offset:0\n\t"
  "s_or_b32 %[wdone], %[wdone], s53\n\t"
  "v_mfma_f32_16x16x32_bf16 v[56:59], v[76:79], v[140:143], v[56:59]\n\t"
  "s_add_u32 s59, s59, 26624\n\t"
  "v_mfma_f32_16x16x32_bf16 v[52:55], v[76:79], v[136:139], v[52:55]\n\t"
  "v_mfma_f32_16x16x32_bf16 v[48:51], v[76:79], v[132:135], v[48:51]\n\t"
  "v_mfma_f32_16x16x32_bf16 v[44:47], v[76:79], v[128:131], v[44:47]\n\t"
  "ds_read_b64 v[76:77], %[va] offset:1920\n\t"
  "ds_read_b64 v[78:79], %[va] offset:1928\n\t"
  "v_mfma_f32_16x16x32_bf16 v[60:63], v[80:83], v[140:143], v[60:63]\n\t"
  "buffer_load_dwordx4 a[104:107], %[vl16], %[srd], s59 offen offset:0 sc1\n\t"
  "v_mfma_f32_16x16x32_bf16 v[56:59], v[80:83], v[136:139], v[56:59]\n\t"
  "v_mfma_f32_16x16x32_bf16 v[52:55], v[80:83], v[132:135], v[52:55]\n\t"
  "v_mfma_f32_16x16x32_bf16 v[48:51], v[80:83], v[128:131], v[48:51]\n\t"
  "ds_read_b64 v[80:81], %[va] offset:2432\n\t"
  "ds_read_b64 v[82:83], %[va] offset:2440\n\t"
  "v_mfma_f32_16x16x32_bf16 v[60:63], v[84:87], v[136:139], v[60:63]\n\t"
  "buffer_load_dwordx4 a[108:111], %[vl16], %[srd], s59 offen offset:1024 sc1\n\t"
  "v_mfma_f32_16x16x32_bf16 v[56:59], v[84:87], v[132:135], v[56:59]\n\t"
  "v_mfma_f32_16x16x32_bf16 v[52:55], v[84:87], v[128:131], v[52:55]\n\t"
  "s_waitcnt lgkmcnt(15)\n\t"
  "ds_read_b64 v[84:85], %[va] offset:2944\n\t"
  "v_mfma_f32_16x16x32_bf16 v[60:63], v[88:91], v[132:135], v[60:63]\n\t"
  "ds_read_b64 v[86:87], %[va] offset:2952\n\t"
  "v_mfma_f32_16x16x32_bf16 v[56:59], v[88:91], v[128:131], v[56:59]\n\t"
  "ds_read_b64 v[88:89], %[va] offset:3456\n\t"
  "ds_read_b64 v[90:91], %[va] offset:3464\n\t"
  "v_mfma_f32_16x16x32_bf16 v[60:63], v[92:95], v[128:131], v[60:63]\n\t"
  "s_waitcnt lgkmcnt(8)\n\t"
  "ds_read_b64 v[92:93], %[va] offset:3968\n\t"
  "v_mfma_f32_16x16x32_bf16 v[60:63], v[64:67], v[124:127], v[60:63]\n\t"
  "ds_read_b128 v[156:159], %[vb3] offset:14336\n\t"
  "ds_read_b64 v[94:95], %[va] offset:3976\n\t"
  "v_mfma_f32_16x16x32_bf16 v[56:59], v[64:67], v[120:123], v[56:59]\n\t"
  "buffer_load_dwordx4 a[112:115], %[vl16], %[srd], s59 offen offset:2048 sc1\n\t"
  "v_mfma_f32_16x16x32_bf16 v[52:55], v[64:67], v[116:119], v[52:55]\n\t"
  "buffer_load_dwordx4 a[116:119], %[vl16], %[srd], s59 offen offset:3072 sc1\n\t"
  "v_mfma_f32_16x16x32_bf16 v[48:51], v[64:67], v[112:115], v[48:51]\n\t"
  "ds_read_b128 v[152:155], %[vb3] offset:12288\n\t"
  "v_mfma_f32_16x16x32_bf16 v[44:47], v[64:67], v[108:111], v[44:47]\n\t"
  "v_mfma_f32_16x16x32_bf16 v[40:43], v[64:67], v[104:107], v[40:43]\n\t"
  "v_mfma_f32_16x16x32_bf16 v[36:39], v[64:67], v[100:103], v[36:39]\n\t"
  "ds_read_b128 v[148:151], %[vb3] offset:10240\n\t"
  "s_add_u32 s59, s59, 4096\n\t"
  "v_mfma_f32_16x16x32_bf16 v[32:35], v[64:67], v[96:99], v[32:35]\n\t"
  "ds_read_b64 v[64:65], %[va] offset:448\n\t"
  "ds_read_b64 v[66:67], %[va] offset:456\n\t"
  "v_mfma_f32_16x16x32_bf16 v[60:63], v[68:71], v[120:123], v[60:63]\n\t"
  "v_mfma_f32_16x16x32_bf16 v[56:59], v[68:71], v[116:119], v[56:59]\n\t"
  "ds_read_b128 v[144:147], %[vb3] offset:8192\n\t"
  "v_mfma_f32_16x16x32_bf16 v[52:55], v[68:71], v[112:115], v[52:55]\n\t"
  "buffer_load_dwordx4 a[120:123], %[vl16], %[srd], s59 offen offset:0 sc1\n\t"
  "v_mfma_f32_16x16x32_bf16 v[48:51], v[68:71], v[108:111], v[48:51]\n\t"
  "v_mfma_f32_16x16x32_bf16 v[44:47], v[68:71], v[104:107], v[44:47]\n\t"
  "ds_read_b128 v[140:143], %[vb3] offset:6144\n\t"
  "v_mfma_f32_16x16x32_bf16 v[40:43], v[68:71], v[100:103], v[40:43]\n\t"
  "v_mfma_f32_16x16x32_bf16 v[36:39], v[68:71], v[96:99], v[36:39]\n\t"
  "s_waitcnt lgkmcnt(11)\n\t"
  "ds_read_b64 v[68:69], %[va] offset:960\n\t"
  "v_mfma_f32_16x16x32_bf16 v[60:63], v[72:75], v[116:119], v[60:63]\n\t"
  "ds_read_b128 v[136:139], %[vb3] offset:4096\n\t"
  "ds_read_b64 v[70:71], %[va] offset:968\n\t"
  "v_mfma_f32_16x16x32_bf16 v[56:59], v[72:75], v[112:115], v[56:59]\n\t"
  "buffer_load_dwordx4 a[124:127], %[vl16], %[srd], s59 offen offset:1024 sc1\n\t"
  "v_mfma_f32_16x16x32_bf16 v[52:55], v[72:75], v[108:111], v[52:55]\n\t"
  "v_mfma_f32_16x16x32_bf16 v[48:51], v[72:75], v[104:107], v[48:51]\n\t"
  "ds_read_b128 v[132:135], %[vb3] offset:2048\n\t"
  "buffer_load_dwordx4 a[128:131], %[vl16], %[srd], s59 offen offset:2048 sc1\n\t"
  "v_mfma_f32_16x16x32_bf16 v[44:47], v[72:75], v[100:103], v[44:47]\n\t"
  "v_mfma_f32_16x16x32_bf16 v[40:43], v[72:75], v[96:99], v[40:43]\n\t"
  "ds_read_b64 v[72:73], %[va] offset:1472\n\t"
  "ds_read_b64 v[74:75], %[va] offset:1480\n\t"
  "v_mfma_f32_16x16x32_bf16 v[60:63], v[76:79], v[112:115], v[60:63]\n\t"
  "ds_read_b128 v[128:131], %[vb3] offset:0\n\t"
  "s_waitcnt vmcnt(15)\n\t"
  "v_add_f32_dpp v244, v244, v244 quad_perm:[1,0,3,2] row_mask:0xf bank_mask:0xf\n\t"
  "v_mfma_f32_16x16x32_bf16 v[56:59], v[76:79], v[108:111], v[56:59]\n\t"
  "buffer_load_dwordx4 a[132:135], %[vl16], %[srd], s59 offen offset:3072 sc1\n\t"
  "v_add_f32_dpp v244, v244, v244 quad_perm:[2,3,0,1] row_mask:0xf bank_mask:0xf\n\t"
  "v_mfma_f32_16x16x32_bf16 v[52:55], v[76:79], v[104:107], v[52:55]\n\t"
  "v_cvt_pk_bf16_f32 v244, 0, v244\n\t"
  "v_add_f32 v245, 1.0, v244\n\t"
  "v_mfma_f32_16x16x32_bf16 v[48:51], v[76:79], v[100:103], v[48:51]\n\t"
  "v_cvt_pk_bf16_f32 v245, 0, v245\n\t"
  "v_readlane_b32 s46, v244, 0\n\t"
  "v_mfma_f32_16x16x32_bf16 v[44:47], v[76:79], v[96:99], v[44:47]\n\t"
  "ds_read_b64 v[76:77], %[va] offset:1984\n\t"
  "ds_read_b64 v[78:79], %[va] offset:1992\n\t"
  "v_mfma_f32_16x16x32_bf16 v[60:63], v[80:83], v[108:111], v[60:63]\n\t"
  "s_add_u32 s59, s59, 4096\n\t"
  "v_readlane_b32 s48, v244, 8\n\t"
  "v_mfma_f32_16x16x32_bf16 v[56:59], v[80:83], v[104:107], v[56:59]\n\t"
  "v_readlane_b32 s47, v245, 4\n\t"
  "v_readlane_b32 s51, v245, 12\n\t"
  "v_mfma_f32_16x16x32_bf16 v[52:55], v[80:83], v[100:103], v[52:55]\n\t"
  "v_mfma_f32_16x16x32_bf16 v[48:51], v[80:83], v[96:99], v[48:51]\n\t"
  "ds_read_b64 v[80:81], %[va] offset:2496\n\t"
  "ds_read_b64 v[82:83], %[va] offset:2504\n\t"
  "v_mfma_f32_16x16x32_bf16 v[60:63], v[84:87], v[104:107], v[60:63]\n\t"
  "buffer_load_dwordx4 a[136:139], %[vl16], %[srd], s59 offen offset:0 sc1\n\t"
  "v_mfma_f32_16x16x32_bf16 v[56:59], v[84:87], v[100:103], v[56:59]\n\t"
  "s_and_b32 s52, s51, 0x7fffffff\n\t"
  "s_cselect_b32 s51, s51, 0x2b800000\n\t"
  "v_mfma_f32_16x16x32_bf16 v[52:55], v[84:87], v[96:99], v[52:55]\n\t"
  "s_waitcnt lgkmcnt(15)\n\t"
  "ds_read_b64 v[84:85], %[va] offset:3008\n\t"
  "v_mfma_f32_16x16x32_bf16 v[60:63], v[88:91], v[100:103], v[60:63]\n\t"
  "ds_read_b64 v[86:87], %[va] offset:3016\n\t"
  "v_mov_b32 v245, s48\n\t"
  "v_mfma_f32_16x16x32_bf16 v[56:59], v[88:91], v[96:99], v[56:59]\n\t"
  "ds_read_b64 v[88:89], %[va] offset:3520\n\t"
  "ds_read_b64 v[90:91], %[va] offset:3528\n\t"
  "v_mfma_f32_16x16x32_bf16 v[60:63], v[92:95], v[96:99], v[60:63]\n\t"
  "s_waitcnt lgkmcnt(8)\n\t"
  "ds_read_b64 v[92:93], %[va] offset:4032\n\t"
  "v_mfma_f32_16x16x32_bf16 v[60:63], v[64:67], v[156:159], v[60:63]\n\t"
  "ds_read_b64 v[94:95], %[va] offset:4040\n\t"
  "buffer_load_dwordx4 a[140:143], %[vl16], %[srd], s59 offen offset:1024 sc1\n\t"
  "v_mfma_f32_16x16x32_bf16 v[56:59], v[64:67], v[152:155], v[56:59]\n\t"
  "v_mul_f32 v245, s51, v245\n\t"
  "v_mul_f32 v244, s47, v245\n\t"
  "v_mfma_f32_16x16x32_bf16 v[52:55], v[64:67], v[148:151], v[52:55]\n\t"
  "buffer_load_dwordx4 a[144:147], %[vl16], %[srd], s59 offen offset:2048 sc1\n\t"
  "v_mul_f32 v245, s46, v245\n\t"
  "v_mfma_f32_16x16x32_bf16 v[48:51], v[64:67], v[144:147], v[48:51]\n\t"
  "s_waitcnt vmcnt(11)\n\t"
  "s_barrier\n\t"
  "v_mfma_f32_16x16x32_bf16 v[44:47], v[64:67], v[140:143], v[44:47]\n\t"
  "v_mov_b32 v246, s51\n\t"
  "ds_read_b64 v[224:225], %[vx] offset:0\n\t"
  "v_mfma_f32_16x16x32_bf16 v[40:43], v[64:67], v[136:139], v[40:43]\n\t"
  "ds_read_b128 v[160:163], %[vs] offset:0\n\t"
  "ds_read_b128 v[164:167], %[vs2] offset:0\n\t"
  "v_mfma_f32_16x16x32_bf16 v[36:39], v[64:67], v[132:135], v[36:39]\n\t"
  "buffer_load_dwordx4 a[148:151], %[vl16], %[srd], s59 offen offset:3072 sc1\n\t"
  "ds_read_b64 v[226:227], %[vx] offset:2048\n\t"
  "v_mfma_f32_16x16x32_bf16 v[32:35], v[64:67], v[128:131], v[32:35]\n\t"
  "ds_read_b128 v[168:171], %[vs] offset:2048\n\t"
  "ds_read_b128 v[172:175], %[vs2] offset:2048\n\t"
  "v_mfma_f32_16x16x32_bf16 v[60:63], v[68:71], v[152:155], v[60:63]\n\t"
  "ds_read_b64 v[228:229], %[vx] offset:4096\n\t"
  "ds_read_b128 v[176:179], %[vs] offset:4096\n\t"
  "v_mfma_f32_16x16x32_bf16 v[56:59], v[68:71], v[148:151], v[56:59]\n\t"
  "ds_read_b128 v[180:183], %[vs2] offset:4096\n\t"
  "ds_read_b64 v[230:231], %[vx] offset:6144\n\t"
  "v_mfma_f32_16x16x32_bf16 v[52:55], v[68:71], v[144:147], v[52:55]\n\t"
  "s_add_u32 s59, s59, 4096\n\t"
  "ds_read_b128 v[184:187], %[vs] offset:6144\n\t"
  "v_mfma_f32_16x16x32_bf16 v[48:51], v[68:71], v[140:143], v[48:51]\n\t"
  "ds_read_b128 v[188:191], %[vs2] offset:6144\n\t"
  "ds_read_b64 v[232:233], %[vx] offset:8192\n\t"
  "v_mfma_f32_16x16x32_bf16 v[44:47], v[68:71], v[136:139], v[44:47]\n\t"
  "ds_read_b128 v[192:195], %[vs] offset:8192\n\t"
  "ds_read_b128 v[196:199], %[vs2] offset:8192\n\t"
  "v_mfma_f32_16x16x32_bf16 v[40:43], v[68:71], v[132:135], v[40:43]\n\t"
  "ds_read_b64 v[234:235], %[vx] offset:10240\n\t"
  "ds_read_b128 v[200:203], %[vs] offset:10240\n\t"
  "v_mfma_f32_16x16x32_bf16 v[36:39], v[68:71], v[128:131], v[36:39]\n\t"
  "s_waitcnt lgkmcnt(15)\n\t"
  "buffer_load_dwordx4 a[152:155], %[vl16], %[srd], s59 offen offset:0 sc1\n\t"
  "v_mfma_f32_16x16x32_bf16 v[60:63], v[72:75], v[148:151], v[60:63]\n\t"
  "ds_read_b128 v[204:207], %[vs2] offset:10240\n\t"
  "ds_read_b64 v[236:237], %[vx] offset:12288\n\t"
  "v_mfma_f32_16x16x32_bf16 v[56:59], v[72:75], v[144:147], v[56:59]\n\t"
  "ds_read_b128 v[208:211], %[vs] offset:12288\n\t"
  "ds_read_b128 v[212:215], %[vs2] offset:12288\n\t"
  "v_mfma_f32_16x16x32_bf16 v[52:55], v[72:75], v[140:143], v[52:55]\n\t"
  "ds_read_b64 v[238:239], %[vx] offset:14336\n\t"
  "ds_read_b128 v[216:219], %[vs] offset:14336\n\t"
  "v_mfma_f32_16x16x32_bf16 v[48:51], v[72:75], v[136:139], v[48:51]\n\t"
  "buffer_load_dwordx4 a[156:159], %[vl16], %[srd], s59 offen offset:1024 sc1\n\t"
  "ds_read_b128 v[220:223], %[vs2] offset:14336\n\t"
  "v_mfma_f32_16x16x32_bf16 v[44:47], v[72:75], v[132:135], v[44:47]\n\t"
  "v_mfma_f32_16x16x32_bf16 v[40:43], v[72:75], v[128:131], v[40:43]\n\t"
  "v_mfma_f32_16x16x32_bf16 v[60:63], v[76:79], v[144:147], v[60:63]\n\t"
  "v_mfma_f32_16x16x32_bf16 v[56:59], v[76:79], v[140:143], v[56:59]\n\t"
  "buffer_load_dwordx4 a[160:163], %[vl16], %[srd], s59 offen offset:2048 sc1\n\t"
  "v_mfma_f32_16x16x32_bf16 v[52:55], v[76:79], v[136:139], v[52:55]\n\t"
  "v_mfma_f32_16x16x32_bf16 v[48:51], v[76:79], v[132:135], v[48:51]\n\t"
  "v_mfma_f32_16x16x32_bf16 v[44:47], v[76:79], v[128:131], v[44:47]\n\t"
  "v_mfma_f32_16x16x32_bf16 v[60:63], v[80:83], v[140:143], v[60:63]\n\t"
  "buffer_load_dwordx4 a[164:167], %[vl16], %[srd], s59 offen offset:3072 sc1\n\t"
  "v_mfma_f32_16x16x32_bf16 v[56:59], v[80:83], v[136:139], v[56:59]\n\t"
  "v_mfma_f32_16x16x32_bf16 v[52:55], v[80:83], v[132:135], v[52:55]\n\t"
  "v_mfma_f32_16x16x32_bf16 v[48:51], v[80:83], v[128:131], v[48:51]\n\t"
  "v_mfma_f32_16x16x32_bf16 v[60:63], v[84:87], v[136:139], v[60:63]\n\t"
  "v_mfma_f32_16x16x32_bf16 v[56:59], v[84:87], v[132:135], v[56:59]\n\t"
  "v_mfma_f32_16x16x32_bf16 v[52:55], v[84:87], v[128:131], v[52:55]\n\t"
  "v_mfma_f32_16x16x32_bf16 v[60:63], v[88:91], v[132:135], v[60:63]\n\t"
  "v_mfma_f32_16x16x32_bf16 v[56:59], v[88:91], v[128:131], v[56:59]\n\t"
  "v_mfma_f32_16x16x32_bf16 v[60:63], v[92:95], v[128:131], v[60:63]\n\t"
  "v_lshlrev_b32 v96, 16, v224\n\t"
  "v_and_b32 v97, 0xffff0000, v224\n\t"
  "v_lshlrev_b32 v98, 16, v225\n\t"
  "v_and_b32 v99, 0xffff0000, v225\n\t"
  "s_waitcnt lgkmcnt(15)\n\t"
  "v_lshlrev_b32 v100, 16, v226\n\t"
  "v_and_b32 v101, 0xffff0000, v226\n\t"
  "v_lshlrev_b32 v102, 16, v227\n\t"
  "v_and_b32 v103, 0xffff0000, v227\n\t"
  "v_pk_fma_f32 v[160:161], v[96:97], v[160:161], v[162:163]\n\t"
  "v_pk_fma_f32 v[164:165], v[98:99], v[164:165], v[166:167]\n\t"
  "v_pk_fma_f32 v[168:169], v[100:101], v[168:169], v[170:171]\n\t"
  "v_pk_fma_f32 v[172:173], v[102:103], v[172:173], v[174:175]\n\t"
  "v_pk_mul_f32 v[96:97], v[96:97], v[246:247] op_sel_hi:[1,0]\n\t"
  "v_pk_mul_f32 v[98:99], v[98:99], v[246:247] op_sel_hi:[1,0]\n\t"
  "v_pk_mul_f32 v[100:101], v[100:101], v[246:247] op_sel_hi:[1,0]\n\t"
  "v_pk_mul_f32 v[102:103], v[102:103], v[246:247] op_sel_hi:[1,0]\n\t"
  "v_pk_fma_f32 v[160:161], v[160:161], v[244:245], v[244:245] op_sel:[0,0,1] op_sel_hi:[1,0,1]\n\t"
  "v_pk_fma_f32 v[164:165], v[164:165], v[244:245], v[244:245] op_sel:[0,0,1] op_sel_hi:[1,0,1]\n\t"
  "v_pk_fma_f32 v[168:169], v[168:169], v[244:245], v[244:245] op_sel:[0,0,1] op_sel_hi:[1,0,1]\n\t"
  "v_pk_fma_f32 v[172:173], v[172:173], v[244:245], v[244:245] op_sel:[0,0,1] op_sel_hi:[1,0,1]\n\t"
  "v_pk_fma_f32 v[96:97], v[160:161], v[32:33], v[96:97]\n\t"
  "v_pk_fma_f32 v[98:99], v[164:165], v[34:35], v[98:99]\n\t"
  "v_pk_fma_f32 v[100:101], v[168:169], v[36:37], v[100:101]\n\t"
  "v_pk_fma_f32 v[102:103], v[172:173], v[38:39], v[102:103]\n\t"
  "v_cvt_pk_bf16_f32 v64, v96, v97\n\t"
  "v_cvt_pk_bf16_f32 v66, v100, v101\n\t"
  "v_cvt_pk_bf16_f32 v65, v98, v99\n\t"
  "v_cvt_pk_bf16_f32 v67, v102, v103\n\t"
  "s_add_u32 s44, %[sO], 0\n\t"
  "v_permlane16_swap_b32 v64, v66\n\t"
  "v_permlane16_swap_b32 v65, v67\n\t"
  "buffer_store_dwordx4 v[64:67], %[vo], %[srd], s44 offen sc1\n\t"
  "v_lshlrev_b32 v104, 16, v228\n\t"
  "v_and_b32 v105, 0xffff0000, v228\n\t"
  "v_lshlrev_b32 v106, 16, v229\n\t"
  "v_and_b32 v107, 0xffff0000, v229\n\t"
  "s_waitcnt lgkmcnt(14)\n\t"
  "v_lshlrev_b32 v108, 16, v230\n\t"
  "v_and_b32 v109, 0xffff0000, v230\n\t"
  "v_lshlrev_b32 v110, 16, v231\n\t"
  "v_and_b32 v111, 0xffff0000, v231\n\t"
  "v_pk_fma_f32 v[176:177], v[104:105], v[176:177], v[178:179]\n\t"
  "v_pk_fma_f32 v[180:181], v[106:107], v[180:181], v[182:183]\n\t"
  "s_waitcnt lgkmcnt(13)\n\t"
  "v_pk_fma_f32 v[184:185], v[108:109], v[184:185], v[186:187]\n\t"
  "s_waitcnt lgkmcnt(12)\n\t"
  "v_pk_fma_f32 v[188:189], v[110:111], v[188:189], v[190:191]\n\t"
  "v_pk_mul_f32 v[104:105], v[104:105], v[246:247] op_sel_hi:[1,0]\n\t"
  "v_pk_mul_f32 v[106:107], v[106:107], v[246:247] op_sel_hi:[1,0]\n\t"
  "v_pk_mul_f32 v[108:109], v[108:109], v[246:247] op_sel_hi:[1,0]\n\t"
  "v_pk_mul_f32 v[110:111], v[110:111], v[246:247] op_sel_hi:[1,0]\n\t"
  "v_pk_fma_f32 v[176:177], v[176:177], v[244:245], v[244:245] op_sel:[0,0,1] op_sel_hi:[1,0,1]\n\t"
  "v_pk_fma_f32 v[180:181], v[180:181], v[244:245], v[244:245] op_sel:[0,0,1] op_sel_hi:[1,0,1]\n\t"
  "v_pk_fma_f32 v[184:185], v[184:185], v[244:245], v[244:245] op_sel:[0,0,1] op_sel_hi:[1,0,1]\n\t"
  "v_pk_fma_f32 v[188:189], v[188:189], v[244:245], v[244:245] op_sel:[0,0,1] op_sel_hi:[1,0,1]\n\t"
  "v_pk_fma_f32 v[104:105], v[176:177], v[40:41], v[104:105]\n\t"
  "v_pk_fma_f32 v[106:107], v[180:181], v[42:43], v[106:107]\n\t"
  "v_pk_fma_f32 v[108:109], v[184:185], v[44:45], v[108:109]\n\t"
  "v_pk_fma_f32 v[110:111], v[188:189], v[46:47], v[110:111]\n\t"
  "v_cvt_pk_bf16_f32 v68, v104, v105\n\t"
  "v_cvt_pk_bf16_f32 v70, v108, v109\n\t"
  "v_cvt_pk_bf16_f32 v69, v106, v107\n\t"
  "v_cvt_pk_bf16_f32 v71, v110, v111\n\t"
  "s_add_u32 s44, %[sO], 655360\n\t"
  "v_permlane16_swap_b32 v68, v70\n\t"
  "v_permlane16_swap_b32 v69, v71\n\t"
  "buffer_store_dwordx4 v[68:71], %[vo], %[srd], s44 offen sc1\n\t"
  "s_waitcnt lgkmcnt(11)\n\t"
  "v_lshlrev_b32 v112, 16, v232\n\t"
  "v_and_b32 v113, 0xffff0000, v232\n\t"
  "v_lshlrev_b32 v114, 16, v233\n\t"
  "v_and_b32 v115, 0xffff0000, v233\n\t"
  "s_waitcnt lgkmcnt(8)\n\t"
  "v_lshlrev_b32 v116, 16, v234\n\t"
  "v_and_b32 v117, 0xffff0000, v234\n\t"
  "v_lshlrev_b32 v118, 16, v235\n\t"
  "v_and_b32 v119, 0xffff0000, v235\n\t"
  "v_pk_fma_f32 v[192:193], v[112:113], v[192:193], v[194:195]\n\t"
  "v_pk_fma_f32 v[196:197], v[114:115], v[196:197], v[198:199]\n\t"
  "s_waitcnt lgkmcnt(7)\n\t"
  "v_pk_fma_f32 v[200:201], v[116:117], v[200:201], v[202:203]\n\t"
  "s_waitcnt lgkmcnt(6)\n\t"
  "v_pk_fma_f32 v[204:205], v[118:119], v[204:205], v[206:207]\n\t"
  "v_pk_mul_f32 v[112:113], v[112:113], v[246:247] op_sel_hi:[1,0]\n\t"
  "v_pk_mul_f32 v[114:115], v[114:115], v[246:247] op_sel_hi:[1,0]\n\t"
  "v_pk_mul_f32 v[116:117], v[116:117], v[246:247] op_sel_hi:[1,0]\n\t"
  "v_pk_mul_f32 v[118:119], v[118:119], v[246:247] op_sel_hi:[1,0]\n\t"
  "v_pk_fma_f32 v[192:193], v[192:193], v[244:245], v[244:245] op_sel:[0,0,1] op_sel_hi:[1,0,1]\n\t"
  "v_pk_fma_f32 v[196:197], v[196:197], v[244:245], v[244:245] op_sel:[0,0,1] op_sel_hi:[1,0,1]\n\t"
  "v_pk_fma_f32 v[200:201], v[200:201], v[244:245], v[244:245] op_sel:[0,0,1] op_sel_hi:[1,0,1]\n\t"
  "v_pk_fma_f32 v[204:205], v[204:205], v[244:245], v[244:245] op_sel:[0,0,1] op_sel_hi:[1,0,1]\n\t"
  "v_pk_fma_f32 v[112:113], v[192:193], v[48:49], v[112:113]\n\t"
  "v_pk_fma_f32 v[114:115], v[196:197], v[50:51], v[114:115]\n\t"
  "v_pk_fma_f32 v[116:117], v[200:201], v[52:53], v[116:117]\n\t"
  "v_pk_fma_f32 v[118:119], v[204:205], v[54:55], v[118:119]\n\t"
  "v_cvt_pk_bf16_f32 v72, v112, v113\n\t"
  "v_cvt_pk_bf16_f32 v74, v116, v117\n\t"
  "v_cvt_pk_bf16_f32 v73, v114, v115\n\t"
  "v_cvt_pk_bf16_f32 v75, v118, v119\n\t"
  "s_add_u32 s44, %[sO], 1310720\n\t"
  "v_permlane16_swap_b32 v72, v74\n\t"
  "v_permlane16_swap_b32 v73, v75\n\t"
  "buffer_store_dwordx4 v[72:75], %[vo], %[srd], s44 offen sc1\n\t"
  "s_waitcnt lgkmcnt(5)\n\t"
  "v_lshlrev_b32 v120, 16, v236\n\t"
  "v_and_b32 v121, 0xffff0000, v236\n\t"
  "v_lshlrev_b32 v122, 16, v237\n\t"
  "v_and_b32 v123, 0xffff0000, v237\n\t"
  "s_waitcnt lgkmcnt(2)\n\t"
  "v_lshlrev_b32 v124, 16, v238\n\t"
  "v_and_b32 v125, 0xffff0000, v238\n\t"
  "v_lshlrev_b32 v126, 16, v239\n\t"
  "v_and_b32 v127, 0xffff0000, v239\n\t"
  "v_pk_fma_f32 v[208:209], v[120:121], v[208:209], v[210:211]\n\t"
  "v_pk_fma_f32 v[212:213], v[122:123], v[212:213], v[214:215]\n\t"
  "s_waitcnt lgkmcnt(1)\n\t"
  "v_pk_fma_f32 v[216:217], v[124:125], v[216:217], v[218:219]\n\t"
  "s_waitcnt lgkmcnt(0)\n\t"
  "v_pk_fma_f32 v[220:221], v[126:127], v[220:221], v[222:223]\n\t"
  "v_pk_mul_f32 v[120:121], v[120:121], v[246:247] op_sel_hi:[1,0]\n\t"
  "v_pk_mul_f32 v[122:123], v[122:123], v[246:247] op_sel_hi:[1,0]\n\t"
  "v_pk_mul_f32 v[124:125], v[124:125], v[246:247] op_sel_hi:[1,0]\n\t"
  "v_pk_mul_f32 v[126:127], v[126:127], v[246:247] op_sel_hi:[1,0]\n\t"
  "v_pk_fma_f32 v[208:209], v[208:209], v[244:245], v[244:245] op_sel:[0,0,1] op_sel_hi:[1,0,1]\n\t"
  "v_pk_fma_f32 v[212:213], v[212:213], v[244:245], v[244:245] op_sel:[0,0,1] op_sel_hi:[1,0,1]\n\t"
  "v_pk_fma_f32 v[216:217], v[216:217], v[244:245], v[244:245] op_sel:[0,0,1] op_sel_hi:[1,0,1]\n\t"
  "v_pk_fma_f32 v[220:221], v[220:221], v[244:245], v[244:245] op_sel:[0,0,1] op_sel_hi:[1,0,1]\n\t"
  "v_pk_fma_f32 v[120:121], v[208:209], v[56:57], v[120:121]\n\t"
  "v_pk_fma_f32 v[122:123], v[212:213], v[58:59], v[122:123]\n\t"
  "v_pk_fma_f32 v[124:125], v[216:217], v[60:61], v[124:125]\n\t"
  "v_pk_fma_f32 v[126:127], v[220:221], v[62:63], v[126:127]\n\t"
  "v_cvt_pk_bf16_f32 v76, v120, v121\n\t"
  "v_cvt_pk_bf16_f32 v78, v124, v125\n\t"
  "v_cvt_pk_bf16_f32 v77, v122, v123\n\t"
  "v_cvt_pk_bf16_f32 v79, v126, v127\n\t"
  "s_add_u32 s44, %[sO], 1966080\n\t"
  "v_permlane16_swap_b32 v76, v78\n\t"
  "v_permlane16_swap_b32 v77, v79\n\t"
  "buffer_store_dwordx4 v[76:79], %[vo], %[srd], s44 offen sc1\n\t"
  "s_nop 1\n\t"
  "s_branch Lcend%=\n\t"
  "La1_slow%=:\n\t"
  "s_nop 7\n\t"
  "s_nop 3\n\t"
  "s_mov_b32 s49, 0x40000\n\t"
  "La1_loop%=:\n\t"
  "s_sleep 3\n\t"
  "buffer_load_dwordx2 v[160:161], %[vf1], %[srd], 0 offen sc1\n\t"
  "s_waitcnt vmcnt(0)\n\t"
  "v_cmp_ne_u64 vcc, v[252:253], v[160:161]\n\t"
  "s_nop 3\n\t"
  "s_cmp_eq_u64 vcc, 0\n\t"
  "s_cbranch_scc1 La1_back%=\n\t"
  "s_sub_u32 s49, s49, 1\n\t"
  "s_cmp_lg_u32 s49, 0\n\t"
  "s_cbranch_scc1 La1_loop%=\n\t"
  "s_branch La1_back%=\n\t"
  "La2_slow%=:\n\t"
  "s_nop 7\n\t"
  "s_nop 3\n\t"
  "s_mov_b32 s49, 0x40000\n\t"
  "La2_loop%=:\n\t"
  "s_sleep 3\n\t"
  "buffer_load_dwordx2 v[162:163], %[vf2], %[srd], 0 offen sc1\n\t"
  "s_waitcnt vmcnt(0)\n\t"
  "v_cmp_ne_u64 vcc, v[252:253], v[162:163]\n\t"
  "s_nop 3\n\t"
  "s_cmp_eq_u64 vcc, 0\n\t"
  "s_cbranch_scc1 La2_back%=\n\t"
  "s_sub_u32 s49, s49, 1\n\t"
  "s_cmp_lg_u32 s49, 0\n\t"
  "s_cbranch_scc1 La2_loop%=\n\t"
  "s_branch La2_back%=\n\t"
  "Lcend%=:\n\t"
      : [wdone] "=&s"(wdone), [pm] "=&s"(pmask)
      : [srd] "s"(srd), [ws] "s"(ws), [sO] "s"(OFF_X2P + d * 128u), [w] "s"(w), [sKV] "s"(OFF_KVT + cb * 128u + w * 655360u),
        [tlo] "s"((u32)tag), [thi] "s"((u32)(tag >> 32)), [sW1] "s"(OFF_W1F + (j * 4 + w) * 61440u),
        [vf2] "v"(c_vf2),
        [vf1] "v"(c_vf1), [vp0] "v"(c_vp0), [vsa] "v"(lb + L_STASH + tid * 16 + 12), [vf3] "v"(c_vf3), [vfw] "v"(c_vfw),
        [va] "v"(c_va), [vb0] "v"(c_vb0), [vb1] "v"(c_vb1), [vb2] "v"(c_vb2), [vb3] "v"(c_vb3), [vx] "v"(c_vx), [vs] "v"(c_vs), [vs2] "v"(c_vs2), [vo] "v"(c_vo), [vd] "v"(c_vd),
        [vl16] "v"(c_vl16), [vkv] "v"(c_vkv),
        [vmod] "v"(c_vmod)
      : "memory", "vcc", "scc",
"a0", "a1", "a2", "a3", "a4", "a5", "a6", "a7", "a8", "a9", "a10", "a11", "a12", "a13", "a14", "a15", "a16", "a17", "a18", "a19", "a20", "a21", "a22", "a23", "a24", "a25", "a26", "a27", "a28", "a29", "a30", "a31", "a32", "a33", "a34", "a35", "a36", "a37", "a38", "a39", "a40", "a41", "a42", "a43", "a44", "a45", "a46", "a47", "a48", "a49", "a50", "a51", "a52", "a53", "a54", "a55", "a56", "a57", "a58", "a59", "a60", "a61", "a62", "a63", "a64", "a65", "a66", "a67", "a68", "a69", "a70", "a71", "a72", "a73", "a74", "a75", "a76", "a77", "a78", "a79", "a80", "a81", "a82", "a83", "a84", "a85", "a86", "a87", "a88", "a89", "a90", "a91", "a92", "a93", "a94", "a95", "a96", "a97", "a98", "a99", "a100", "a101", "a102", "a103", "a104", "a105", "a106", "a107", "a108", "a109", "a110", "a111", "a112", "a113", "a114", "a115", "a116", "a117", "a118", "a119", "a120", "a121", "a122", "a123", "a124", "a125", "a126", "a127", "a128", "a129", "a130", "a131", "a132", "a133", "a134", "a135", "a136", "a137", "a138", "a139", "a140", "a141", "a142", "a143", "a144", "a145", "a146", "a147", "a148", "a149", "a150", "a151", "a152", "a153", "a154", "a155", "a156", "a157", "a158", "a159", "a160", "a161", "a162", "a163", "a164", "a165", "a166", "a167", "s44", "s45", "s46", "s47", "s48", "s49", "s51", "s52", "s53", "s54", "s55", "s56", "s57", "s58", "s59", "s80", "s81", "v32", "v33", "v34", "v35", "v36", "v37", "v38", "v39", "v40", "v41", "v42", "v43", "v44", "v45", "v46", "v47", "v48", "v49", "v50", "v51", "v52", "v53", "v54", "v55", "v56", "v57", "v58", "v59", "v60", "v61", "v62", "v63", "v64", "v65", "v66", "v67", "v68", "v69", "v70", "v71", "v72", "v73", "v74", "v75", "v76", "v77", "v78", "v79", "v80", "v81", "v82", "v83", "v84", "v85", "v86", "v87", "v88", "v89", "v90", "v91", "v92", "v93", "v94", "v95", "v96", "v97", "v98", "v99", "v100", "v101", "v102", "v103", "v104", "v105", "v106", "v107", "v108", "v109", "v110", "v111", "v112", "v113", "v114", "v115", "v116", "v117", "v118", "v119", "v120", "v121", "v122", "v123", "v124", "v125", "v126", "v127", "v128", "v129", "v130", "v131", "v132", "v133", "v134", "v135", "v136", "v137", "v138", "v139", "v140", "v141", "v142", "v143", "v144", "v145", "v146", "v147", "v148", "v149", "v150", "v151", "v152", "v153", "v154", "v155", "v156", "v157", "v158", "v159", "v160", "v161", "v162", "v163", "v164", "v165", "v166", "v167", "v168", "v169", "v170", "v171", "v172", "v173", "v174", "v175", "v176", "v177", "v178", "v179", "v180", "v181", "v182", "v183", "v184", "v185", "v186", "v187", "v188", "v189", "v190", "v191", "v192", "v193", "v194", "v195", "v196", "v197", "v198", "v199", "v200", "v201", "v202", "v203", "v204", "v205", "v206", "v207", "v208", "v209", "v210", "v211", "v212", "v213", "v214", "v215", "v216", "v217", "v218", "v219", "v220", "v221", "v222", "v223", "v224", "v225", "v226", "v227", "v228", "v229", "v230", "v231", "v232", "v233", "v234", "v235", "v236", "v237", "v238", "v239", "v244", "v245", "v246", "v247", "v250", "v251", "v252", "v253", "v254", "v255"
    );
  }
  asm volatile(
  "s_mov_b64 s[68:69], %[ws]\n\t"
  "v_mbcnt_lo_u32_b32 v20, -1, 0\n\t"
  "v_mbcnt_hi_u32_b32 v20, -1, v20\n\t"
  "s_lshl_b32 s52, %[w], 10\n\t"
  "s_add_u32 s52, s52, 143360\n\t"
  "v_lshl_add_u32 v20, v20, 4, s52\n\t"
  "ds_read_b128 v[0:3], v20 offset:0\n\t"
  "ds_read_b128 v[4:7], v20 offset:4096\n\t"
  "ds_read_b128 v[8:11], v20 offset:8192\n\t"
  "ds_read_b128 v[12:15], v20 offset:12288\n\t"
  "ds_read_b128 v[16:19], v20 offset:16384\n\t"
  "s_waitcnt lgkmcnt(0)\n\t"
  "v_mov_b32 v132, %[tlo]\n\t"
  "v_mov_b32 v133, %[thi]\n\t"
  "v_mov_b32 v134, %[tlo]\n\t"
  "v_mov_b32 v135, %[thi]\n\t"
  "s_lshl_b32 s52, %[w], 12\n\t"
  "s_mov_b64 s[60:61], 0\n\t"
  "s_mov_b32 s62, 0\n\t"
  "v_bfrev_b32 v128, 1\n\t"
  "v_add_u32 v124, 40960, v6\n\t"
  "v_mov_b32 v112, 0\n\t"
  "v_mov_b32 v113, 0\n\t"
  "v_mov_b32 v114, 0\n\t"
  "v_mov_b32 v115, 0\n\t"
  "v_mov_b32 v116, 0\n\t"
  "v_mov_b32 v117, 0\n\t"
  "v_mov_b32 v118, 0\n\t"
  "v_mov_b32 v119, 0\n\t"
  "v_mov_b32 v120, 0\n\t"
  "v_mov_b32 v121, 0\n\t"
  "v_and_b32 v136, 0xf0, v1\n\t"
  "v_lshlrev_b32 v137, 3, v136\n\t"
  "v_lshlrev_b32 v136, 2, v136\n\t"
  "s_add_u32 s54, %[sST], 65536\n\t"
  "s_mul_i32 s48, %[w], 20480\n\t"
  "v_add_u32 v141, s48, v1\n\t"
  "v_xor_b32 v109, 32, v5\n\t"
  "v_mov_b32 v108, v5\n\t"
  "v_lshrrev_b32 v139, 2, v1\n\t"
  "s_mov_b32 s47, 419840\n\t"
  "v_not_b32 v126, v132\n\t"
  "v_mov_b32 v127, 0\n\t"
  "s_waitcnt vmcnt(0) lgkmcnt(0)\n\t"
  "s_barrier\n\t"
  "buffer_store_dwordx4 v[132:135], v0, %[srd], 0 offen sc1\n\t"
  "s_movk_i32 s59, 0\n\t"
  "buffer_store_dwordx4 v[112:115], v9, %[srd], s59 offen sc1\n\t"
  "s_movk_i32 s59, 8192\n\t"
  "buffer_store_dwordx4 v[112:115], v9, %[srd], s59 offen sc1\n\t"
  "s_movk_i32 s59, 16384\n\t"
  "buffer_store_dwordx4 v[112:115], v9, %[srd], s59 offen sc1\n\t"
  "s_movk_i32 s59, 24576\n\t"
  "buffer_store_dwordx4 v[112:115], v9, %[srd], s59 offen sc1\n\t"
  "s_bitcmp1_b32 %[wdone], 3\n\t"
  "s_cbranch_scc1 Lw3%=\n\t"
  "s_bitcmp1_b32 %[wdone], 6\n\t"
  "s_cbranch_scc1 Lpw_done%=\n\t"
  "s_mov_b32 s49, 0x40000\n\t"
  "Lpw_loop%=:\n\t"
  "buffer_load_dwordx2 v[130:131], v2, %[srd], 0 offen sc1\n\t"
  "s_waitcnt vmcnt(0)\n\t"
  "v_cmp_eq_u64 vcc, v[132:133], v[130:131]\n\t"
  "s_nop 3\n\t"
  "s_orn2_b64 vcc, vcc, 15\n\t"
  "s_cmp_eq_u64 vcc, -1\n\t"
  "s_cbranch_scc1 Lpw_done%=\n\t"
  "s_sleep 3\n\t"
  "s_sub_u32 s49, s49, 1\n\t"
  "s_cmp_lg_u32 s49, 0\n\t"
  "s_cbranch_scc1 Lpw_loop%=\n\t"
  "Lpw_done%=:\n\t"
  "s_add_u32 s44, %[sW1], 24576\n\t"
  "buffer_load_dwordx4 a[104:107], v1, %[srd], s44 offen offset:2048 sc1\n\t"
  "s_add_u32 s44, %[sW1], 24576\n\t"
  "buffer_load_dwordx4 a[108:111], v1, %[srd], s44 offen offset:3072 sc1\n\t"
  "s_add_u32 s44, %[sW1], 28672\n\t"
  "buffer_load_dwordx4 a[112:115], v1, %[srd], s44 offen offset:0 sc1\n\t"
  "s_add_u32 s44, %[sW1], 28672\n\t"
  "buffer_load_dwordx4 a[116:119], v1, %[srd], s44 offen offset:1024 sc1\n\t"
  "s_add_u32 s44, %[sW1], 28672\n\t"
  "buffer_load_dwordx4 a[120:123], v1, %[srd], s44 offen offset:2048 sc1\n\t"
  "s_add_u32 s44, %[sW1], 28672\n\t"
  "buffer_load_dwordx4 a[124:127], v1, %[srd], s44 offen offset:3072 sc1\n\t"
  "s_add_u32 s44, %[sW1], 32768\n\t"
  "buffer_load_dwordx4 a[128:131], v1, %[srd], s44 offen offset:0 sc1\n\t"
  "s_add_u32 s44, %[sW1], 32768\n\t"
  "buffer_load_dwordx4 a[132:135], v1, %[srd], s44 offen offset:1024 sc1\n\t"
  "s_add_u32 s44, %[sW1], 32768\n\t"
  "buffer_load_dwordx4 a[136:139], v1, %[srd], s44 offen offset:2048 sc1\n\t"
  "s_add_u32 s44, %[sW1], 32768\n\t"
  "buffer_load_dwordx4 a[140:143], v1, %[srd], s44 offen offset:3072 sc1\n\t"
  "s_add_u32 s44, %[sW1], 36864\n\t"
  "buffer_load_dwordx4 a[144:147], v1, %[srd], s44 offen offset:0 sc1\n\t"
  "s_add_u32 s44, %[sW1], 36864\n\t"
  "buffer_load_dwordx4 a[148:151], v1, %[srd], s44 offen offset:1024 sc1\n\t"
  "s_add_u32 s44, %[sW1], 36864\n\t"
  "buffer_load_dwordx4 a[152:155], v1, %[srd], s44 offen offset:2048 sc1\n\t"
  "s_add_u32 s44, %[sW1], 36864\n\t"
  "buffer_load_dwordx4 a[156:159], v1, %[srd], s44 offen offset:3072 sc1\n\t"
  "s_add_u32 s44, %[sW1], 40960\n\t"
  "buffer_load_dwordx4 a[160:163], v1, %[srd], s44 offen offset:0 sc1\n\t"
  "s_add_u32 s44, %[sW1], 40960\n\t"
  "buffer_load_dwordx4 a[164:167], v1, %[srd], s44 offen offset:1024 sc1\n\t"
  "Lw3%=:\n\t"
  "s_bitcmp1_b32 %[wdone], 2\n\t"
  "s_cbranch_scc1 Lw2%=\n\t"
  "s_add_u32 s44, %[sW1], 16384\n\t"
  "buffer_load_dwordx4 a[64:67], v1, %[srd], s44 offen offset:0 sc1\n\t"
  "s_add_u32 s44, %[sW1], 16384\n\t"
  "buffer_load_dwordx4 a[68:71], v1, %[srd], s44 offen offset:1024 sc1\n\t"
  "s_add_u32 s44, %[sW1], 16384\n\t"
  "buffer_load_dwordx4 a[72:75], v1, %[srd], s44 offen offset:2048 sc1\n\t"
  "s_add_u32 s44, %[sW1], 16384\n\t"
  "buffer_load_dwordx4 a[76:79], v1, %[srd], s44 offen offset:3072 sc1\n\t"
  "s_add_u32 s44, %[sW1], 20480\n\t"
  "buffer_load_dwordx4 a[80:83], v1, %[srd], s44 offen offset:0 sc1\n\t"
  "s_add_u32 s44, %[sW1], 20480\n\t"
  "buffer_load_dwordx4 a[84:87], v1, %[srd], s44 offen offset:1024 sc1\n\t"
  "s_add_u32 s44, %[sW1], 20480\n\t"
  "buffer_load_dwordx4 a[88:91], v1, %[srd], s44 offen offset:2048 sc1\n\t"
  "s_add_u32 s44, %[sW1], 20480\n\t"
  "buffer_load_dwordx4 a[92:95], v1, %[srd], s44 offen offset:3072 sc1\n\t"
  "s_add_u32 s44, %[sW1], 24576\n\t"
  "buffer_load_dwordx4 a[96:99], v1, %[srd], s44 offen offset:0 sc1\n\t"
  "s_add_u32 s44, %[sW1], 24576\n\t"
  "buffer_load_dwordx4 a[100:103], v1, %[srd], s44 offen offset:1024 sc1\n\t"
  "Lw2%=:\n\t"
  "s_bitcmp1_b32 %[wdone], 1\n\t"
  "s_cbranch_scc1 Lw1%=\n\t"
  "s_add_u32 s44, %[sW1], 8192\n\t"
  "buffer_load_dwordx4 a[32:35], v1, %[srd], s44 offen offset:0 sc1\n\t"
  "s_add_u32 s44, %[sW1], 8192\n\t"
  "buffer_load_dwordx4 a[36:39], v1, %[srd], s44 offen offset:1024 sc1\n\t"
  "s_add_u32 s44, %[sW1], 8192\n\t"
  "buffer_load_dwordx4 a[40:43], v1, %[srd], s44 offen offset:2048 sc1\n\t"
  "s_add_u32 s44, %[sW1], 8192\n\t"
  "buffer_load_dwordx4 a[44:47], v1, %[srd], s44 offen offset:3072 sc1\n\t"
  "s_add_u32 s44, %[sW1], 12288\n\t"
  "buffer_load_dwordx4 a[48:51], v1, %[srd], s44 offen offset:0 sc1\n\t"
  "s_add_u32 s44, %[sW1], 12288\n\t"
  "buffer_load_dwordx4 a[52:55], v1, %[srd], s44 offen offset:1024 sc1\n\t"
  "s_add_u32 s44, %[sW1], 12288\n\t"
  "buffer_load_dwordx4 a[56:59], v1, %[srd], s44 offen offset:2048 sc1\n\t"
  "s_add_u32 s44, %[sW1], 12288\n\t"
  "buffer_load_dwordx4 a[60:63], v1, %[srd], s44 offen offset:3072 sc1\n\t"
  "Lw1%=:\n\t"
  "s_bitcmp1_b32 %[wdone], 0\n\t"
  "s_cbranch_scc1 Lw0%=\n\t"
  "s_add_u32 s44, %[sW1], 0\n\t"
  "buffer_load_dwordx4 a[0:3], v1, %[srd], s44 offen offset:0 sc1\n\t"
  "s_add_u32 s44, %[sW1], 0\n\t"
  "buffer_load_dwordx4 a[4:7], v1, %[srd], s44 offen offset:1024 sc1\n\t"
  "s_add_u32 s44, %[sW1], 0\n\t"
  "buffer_load_dwordx4 a[8:11], v1, %[srd], s44 offen offset:2048 sc1\n\t"
  "s_add_u32 s44, %[sW1], 0\n\t"
  "buffer_load_dwordx4 a[12:15], v1, %[srd], s44 offen offset:3072 sc1\n\t"
  "s_add_u32 s44, %[sW1], 4096\n\t"
  "buffer_load_dwordx4 a[16:19], v1, %[srd], s44 offen offset:0 sc1\n\t"
  "s_add_u32 s44, %[sW1], 4096\n\t"
  "buffer_load_dwordx4 a[20:23], v1, %[srd], s44 offen offset:1024 sc1\n\t"
  "s_add_u32 s44, %[sW1], 4096\n\t"
  "buffer_load_dwordx4 a[24:27], v1, %[srd], s44 offen offset:2048 sc1\n\t"
  "s_add_u32 s44, %[sW1], 4096\n\t"
  "buffer_load_dwordx4 a[28:31], v1, %[srd], s44 offen offset:3072 sc1\n\t"
  "Lw0%=:\n\t"
  "s_bitcmp1_b32 %[wdone], 7\n\t"
  "s_cselect_b32 s58, 5, 0\n\t"
  "s_mov_b32 s49, 0x40000\n\t"
  "Lpx_loop%=:\n\t"
  "buffer_load_dwordx2 v[126:127], v3, %[srd], 0 offen sc1\n\t"
  "s_cmp_lg_u32 s58, 0\n\t"
  "s_cbranch_scc1 Lpk0%=\n\t"
  "s_add_u32 s44, %[sW1], 40960\n\t"
  "buffer_load_dwordx4 a[168:171], v1, %[srd], s44 offen offset:2048 sc1\n\t"
  "s_add_u32 s44, %[sW1], 40960\n\t"
  "buffer_load_dwordx4 a[172:175], v1, %[srd], s44 offen offset:3072 sc1\n\t"
  "s_add_u32 s44, %[sW1], 45056\n\t"
  "buffer_load_dwordx4 a[176:179], v1, %[srd], s44 offen offset:0 sc1\n\t"
  "s_add_u32 s44, %[sW1], 45056\n\t"
  "buffer_load_dwordx4 a[180:183], v1, %[srd], s44 offen offset:1024 sc1\n\t"
  "s_mov_b32 s58, 1\n\t"
  "s_waitcnt vmcnt(4)\n\t"
  "s_branch Lpx_chk%=\n\t"
  "Lpk0%=:\n\t"
  "s_cmp_lg_u32 s58, 1\n\t"
  "s_cbranch_scc1 Lpk1%=\n\t"
  "s_add_u32 s44, %[sW1], 45056\n\t"
  "buffer_load_dwordx4 a[184:187], v1, %[srd], s44 offen offset:2048 sc1\n\t"
  "s_add_u32 s44, %[sW1], 45056\n\t"
  "buffer_load_dwordx4 a[188:191], v1, %[srd], s44 offen offset:3072 sc1\n\t"
  "s_add_u32 s44, %[sW1], 49152\n\t"
  "buffer_load_dwordx4 a[192:195], v1, %[srd], s44 offen offset:0 sc1\n\t"
  "s_add_u32 s44, %[sW1], 49152\n\t"
  "buffer_load_dwordx4 a[196:199], v1, %[srd], s44 offen offset:1024 sc1\n\t"
  "s_mov_b32 s58, 2\n\t"
  "s_waitcnt vmcnt(4)\n\t"
  "s_branch Lpx_chk%=\n\t"
  "Lpk1%=:\n\t"
  "s_cmp_lg_u32 s58, 2\n\t"
  "s_cbranch_scc1 Lpk2%=\n\t"
  "s_add_u32 s44, %[sW1], 49152\n\t"
  "buffer_load_dwordx4 a[200:203], v1, %[srd], s44 offen offset:2048 sc1\n\t"
  "s_add_u32 s44, %[sW1], 49152\n\t"
  "buffer_load_dwordx4 a[204:207], v1, %[srd], s44 offen offset:3072 sc1\n\t"
  "s_add_u32 s44, %[sW1], 53248\n\t"
  "buffer_load_dwordx4 a[208:211], v1, %[srd], s44 offen offset:0 sc1\n\t"
  "s_add_u32 s44, %[sW1], 53248\n\t"
  "buffer_load_dwordx4 a[212:215], v1, %[srd], s44 offen offset:1024 sc1\n\t"
  "s_mov_b32 s58, 3\n\t"
  "s_waitcnt vmcnt(4)\n\t"
  "s_branch Lpx_chk%=\n\t"
  "Lpk2%=:\n\t"
  "s_cmp_lg_u32 s58, 3\n\t"
  "s_cbranch_scc1 Lpk3%=\n\t"
  "s_add_u32 s44, %[sW1], 53248\n\t"
  "buffer_load_dwordx4 a[216:219], v1, %[srd], s44 offen offset:2048 sc1\n\t"
  "s_add_u32 s44, %[sW1], 53248\n\t"
  "buffer_load_dwordx4 a[220:223], v1, %[srd], s44 offen offset:3072 sc1\n\t"
  "s_add_u32 s44, %[sW1], 57344\n\t"
  "buffer_load_dwordx4 a[224:227], v1, %[srd], s44 offen offset:0 sc1\n\t"
  "s_add_u32 s44, %[sW1], 57344\n\t"
  "buffer_load_dwordx4 a[228:231], v1, %[srd], s44 offen offset:1024 sc1\n\t"
  "s_mov_b32 s58, 4\n\t"
  "s_waitcnt vmcnt(4)\n\t"
  "s_branch Lpx_chk%=\n\t"
  "Lpk3%=:\n\t"
  "s_cmp_lg_u32 s58, 4\n\t"
  "s_cbranch_scc1 Lpk4%=\n\t"
  "s_add_u32 s44, %[sW1], 57344\n\t"
  "buffer_load_dwordx4 a[232:235], v1, %[srd], s44 offen offset:2048 sc1\n\t"
  "s_add_u32 s44, %[sW1], 57344\n\t"
  "buffer_load_dwordx4 a[236:239], v1, %[srd], s44 offen offset:3072 sc1\n\t"
  "s_mov_b32 s58, 5\n\t"
  "s_waitcnt vmcnt(2)\n\t"
  "s_branch Lpx_chk%=\n\t"
  "Lpk4%=:\n\t"
  "s_sleep 2\n\t"
  "s_waitcnt vmcnt(0)\n\t"
  "Lpx_chk%=:\n\t"
  "v_cmp_eq_u64 vcc, v[132:133], v[126:127]\n\t"
  "s_nop 3\n\t"
  "s_or_b64 s[60:61], s[60:61], vcc\n\t"
  "s_cmp_eq_u64 s[60:61], -1\n\t"
  "s_cbranch_scc1 Lpx_done%=\n\t"
  "v_cndmask_b32 v3, v3, v128, vcc\n\t"
  "s_bitcmp1_b32 s62, 0\n\t"
  "s_cbranch_scc1 Le1%=\n\t"
  "s_bfe_u32 s63, s60, 0x80000\n\t"
  "s_cmpk_lg_u32 s63, 0xff\n\t"
  "s_cbranch_scc1 Le1%=\n\t"
  "s_bitset1_b32 s62, 0\n\t"
  "s_add_u32 m0, s52, 0\n\t"
  "s_mov_b32 s46, 0\n\t"
  "buffer_load_dwordx4 v4, %[srd], s46 offen offset:0 sc1 lds\n\t"
  "buffer_load_dwordx4 v4, %[srd], s46 offen offset:1024 sc1 lds\n\t"
  "buffer_load_dwordx4 v4, %[srd], s46 offen offset:2048 sc1 lds\n\t"
  "buffer_load_dwordx4 v4, %[srd], s46 offen offset:3072 sc1 lds\n\t"
  "Le1%=:\n\t"
  "s_bitcmp1_b32 s62, 1\n\t"
  "s_cbranch_scc1 Le2%=\n\t"
  "s_bfe_u32 s63, s60, 0x80008\n\t"
  "s_cmpk_lg_u32 s63, 0xff\n\t"
  "s_cbranch_scc1 Le2%=\n\t"
  "s_bitset1_b32 s62, 1\n\t"
  "s_add_u32 m0, s52, 16384\n\t"
  "s_mov_b32 s46, 16384\n\t"
  "buffer_load_dwordx4 v4, %[srd], s46 offen offset:0 sc1 lds\n\t"
  "buffer_load_dwordx4 v4, %[srd], s46 offen offset:1024 sc1 lds\n\t"
  "buffer_load_dwordx4 v4, %[srd], s46 offen offset:2048 sc1 lds\n\t"
  "buffer_load_dwordx4 v4, %[srd], s46 offen offset:3072 sc1 lds\n\t"
  "Le2%=:\n\t"
  "s_bitcmp1_b32 s62, 2\n\t"
  "s_cbranch_scc1 Le3%=\n\t"
  "s_bfe_u32 s63, s60, 0x80010\n\t"
  "s_cmpk_lg_u32 s63, 0xff\n\t"
  "s_cbranch_scc1 Le3%=\n\t"
  "s_bitset1_b32 s62, 2\n\t"
  "s_add_u32 m0, s52, 32768\n\t"
  "s_mov_b32 s46, 32768\n\t"
  "buffer_load_dwordx4 v4, %[srd], s46 offen offset:0 sc1 lds\n\t"
  "buffer_load_dwordx4 v4, %[srd], s46 offen offset:1024 sc1 lds\n\t"
  "buffer_load_dwordx4 v4, %[srd], s46 offen offset:2048 sc1 lds\n\t"
  "buffer_load_dwordx4 v4, %[srd], s46 offen offset:3072 sc1 lds\n\t"
  "Le3%=:\n\t"
  "s_bitcmp1_b32 s62, 3\n\t"
  "s_cbranch_scc1 Le4%=\n\t"
  "s_bfe_u32 s63, s60, 0x80018\n\t"
  "s_cmpk_lg_u32 s63, 0xff\n\t"
  "s_cbranch_scc1 Le4%=\n\t"
  "s_bitset1_b32 s62, 3\n\t"
  "s_add_u32 m0, s52, 49152\n\t"
  "s_mov_b32 s46, 49152\n\t"
  "buffer_load_dwordx4 v4, %[srd], s46 offen offset:0 sc1 lds\n\t"
  "buffer_load_dwordx4 v4, %[srd], s46 offen offset:1024 sc1 lds\n\t"
  "buffer_load_dwordx4 v4, %[srd], s46 offen offset:2048 sc1 lds\n\t"
  "buffer_load_dwordx4 v4, %[srd], s46 offen offset:3072 sc1 lds\n\t"
  "Le4%=:\n\t"
  "s_bitcmp1_b32 s62, 4\n\t"
  "s_cbranch_scc1 Le5%=\n\t"
  "s_bfe_u32 s63, s61, 0x80000\n\t"
  "s_cmpk_lg_u32 s63, 0xff\n\t"
  "s_cbranch_scc1 Le5%=\n\t"
  "s_bitset1_b32 s62, 4\n\t"
  "s_add_u32 m0, s52, 65536\n\t"
  "s_mov_b32 s46, 65536\n\t"
  "buffer_load_dwordx4 v4, %[srd], s46 offen offset:0 sc1 lds\n\t"
  "buffer_load_dwordx4 v4, %[srd], s46 offen offset:1024 sc1 lds\n\t"
  "buffer_load_dwordx4 v4, %[srd], s46 offen offset:2048 sc1 lds\n\t"
  "buffer_load_dwordx4 v4, %[srd], s46 offen offset:3072 sc1 lds\n\t"
  "Le5%=:\n\t"
  "s_bitcmp1_b32 s62, 5\n\t"
  "s_cbranch_scc1 Le6%=\n\t"
  "s_ashr_i32 s63, s61, 8\n\t"
  "s_cmp_lg_u32 s63, -1\n\t"
  "s_cbranch_scc1 Le6%=\n\t"
  "s_bitset1_b32 s62, 5\n\t"
  "buffer_load_dword v142, v139, %[srd], s47 offen offset:0 sc1\n\t"
  "buffer_load_dword v143, v139, %[srd], s47 offen offset:256 sc1\n\t"
  "buffer_load_dword v248, v139, %[srd], s47 offen offset:512 sc1\n\t"
  "buffer_load_dword v249, v139, %[srd], s47 offen offset:768 sc1\n\t"
  "buffer_load_dword v250, v139, %[srd], s47 offen offset:1024 sc1\n\t"
  "buffer_load_dword v251, v139, %[srd], s47 offen offset:1280 sc1\n\t"
  "buffer_load_dword v252, v139, %[srd], s47 offen offset:1536 sc1\n\t"
  "buffer_load_dword v253, v139, %[srd], s47 offen offset:1792 sc1\n\t"
  "buffer_load_dword v254, v139, %[srd], s47 offen offset:2048 sc1\n\t"
  "buffer_load_dword v255, v139, %[srd], s47 offen offset:2304 sc1\n\t"
  "Le6%=:\n\t"
  "s_sub_u32 s49, s49, 1\n\t"
  "s_cmp_lg_u32 s49, 0\n\t"
  "s_cbranch_scc1 Lpx_loop%=\n\t"
  "Lpx_done%=:\n\t"
  "s_bitcmp1_b32 s62, 0\n\t"
  "s_cbranch_scc1 Lf0%=\n\t"
  "s_add_u32 m0, s52, 0\n\t"
  "s_mov_b32 s46, 0\n\t"
  "buffer_load_dwordx4 v4, %[srd], s46 offen offset:0 sc1 lds\n\t"
  "buffer_load_dwordx4 v4, %[srd], s46 offen offset:1024 sc1 lds\n\t"
  "buffer_load_dwordx4 v4, %[srd], s46 offen offset:2048 sc1 lds\n\t"
  "buffer_load_dwordx4 v4, %[srd], s46 offen offset:3072 sc1 lds\n\t"
  "Lf0%=:\n\t"
  "s_bitcmp1_b32 s62, 1\n\t"
  "s_cbranch_scc1 Lf1%=\n\t"
  "s_add_u32 m0, s52, 16384\n\t"
  "s_mov_b32 s46, 16384\n\t"
  "buffer_load_dwordx4 v4, %[srd], s46 offen offset:0 sc1 lds\n\t"
  "buffer_load_dwordx4 v4, %[srd], s46 offen offset:1024 sc1 lds\n\t"
  "buffer_load_dwordx4 v4, %[srd], s46 offen offset:2048 sc1 lds\n\t"
  "buffer_load_dwordx4 v4, %[srd], s46 offen offset:3072 sc1 lds\n\t"
  "Lf1%=:\n\t"
  "s_bitcmp1_b32 s62, 2\n\t"
  "s_cbranch_scc1 Lf2%=\n\t"
  "s_add_u32 m0, s52, 32768\n\t"
  "s_mov_b32 s46, 32768\n\t"
  "buffer_load_dwordx4 v4, %[srd], s46 offen offset:0 sc1 lds\n\t"
  "buffer_load_dwordx4 v4, %[srd], s46 offen offset:1024 sc1 lds\n\t"
  "buffer_load_dwordx4 v4, %[srd], s46 offen offset:2048 sc1 lds\n\t"
  "buffer_load_dwordx4 v4, %[srd], s46 offen offset:3072 sc1 lds\n\t"
  "Lf2%=:\n\t"
  "s_bitcmp1_b32 s62, 3\n\t"
  "s_cbranch_scc1 Lf3%=\n\t"
  "s_add_u32 m0, s52, 49152\n\t"
  "s_mov_b32 s46, 49152\n\t"
  "buffer_load_dwordx4 v4, %[srd], s46 offen offset:0 sc1 lds\n\t"
  "buffer_load_dwordx4 v4, %[srd], s46 offen offset:1024 sc1 lds\n\t"
  "buffer_load_dwordx4 v4, %[srd], s46 offen offset:2048 sc1 lds\n\t"
  "buffer_load_dwordx4 v4, %[srd], s46 offen offset:3072 sc1 lds\n\t"
  "Lf3%=:\n\t"
  "s_bitcmp1_b32 s62, 4\n\t"
  "s_cbranch_scc1 Lf4%=\n\t"
  "s_add_u32 m0, s52, 65536\n\t"
  "s_mov_b32 s46, 65536\n\t"
  "buffer_load_dwordx4 v4, %[srd], s46 offen offset:0 sc1 lds\n\t"
  "buffer_load_dwordx4 v4, %[srd], s46 offen offset:1024 sc1 lds\n\t"
  "buffer_load_dwordx4 v4, %[srd], s46 offen offset:2048 sc1 lds\n\t"
  "buffer_load_dwordx4 v4, %[srd], s46 offen offset:3072 sc1 lds\n\t"
  "Lf4%=:\n\t"
  "s_bitcmp1_b32 s62, 5\n\t"
  "s_cbranch_scc1 Lf5%=\n\t"
  "s_waitcnt vmcnt(29)\n\t"
  "buffer_load_dword v142, v139, %[srd], s47 offen offset:0 sc1\n\t"
  "s_waitcnt vmcnt(29)\n\t"
  "buffer_load_dword v143, v139, %[srd], s47 offen offset:256 sc1\n\t"
  "s_waitcnt vmcnt(29)\n\t"
  "buffer_load_dword v248, v139, %[srd], s47 offen offset:512 sc1\n\t"
  "s_waitcnt vmcnt(29)\n\t"
  "buffer_load_dword v249, v139, %[srd], s47 offen offset:768 sc1\n\t"
  "s_waitcnt vmcnt(29)\n\t"
  "buffer_load_dword v250, v139, %[srd], s47 offen offset:1024 sc1\n\t"
  "s_waitcnt vmcnt(29)\n\t"
  "buffer_load_dword v251, v139, %[srd], s47 offen offset:1280 sc1\n\t"
  "s_waitcnt vmcnt(29)\n\t"
  "buffer_load_dword v252, v139, %[srd], s47 offen offset:1536 sc1\n\t"
  "s_waitcnt vmcnt(29)\n\t"
  "buffer_load_dword v253, v139, %[srd], s47 offen offset:1792 sc1\n\t"
  "s_waitcnt vmcnt(29)\n\t"
  "buffer_load_dword v254, v139, %[srd], s47 offen offset:2048 sc1\n\t"
  "s_waitcnt vmcnt(29)\n\t"
  "buffer_load_dword v255, v139, %[srd], s47 offen offset:2304 sc1\n\t"
  "Lf5%=:\n\t"
  "s_waitcnt vmcnt(0)\n\t"
  "s_barrier\n\t"
  "ds_read_b64_tr_b16 v[68:69], v108 offset:0\n\t"
  "ds_read_b64_tr_b16 v[70:71], v108 offset:2048\n\t"
  "ds_read_b64_tr_b16 v[72:73], v109 offset:0\n\t"
  "ds_read_b64_tr_b16 v[74:75], v109 offset:2048\n\t"
  "ds_read_b64_tr_b16 v[76:77], v108 offset:4096\n\t"
  "ds_read_b64_tr_b16 v[78:79], v108 offset:6144\n\t"
  "ds_read_b64_tr_b16 v[80:81], v109 offset:4096\n\t"
  "ds_read_b64_tr_b16 v[82:83], v109 offset:6144\n\t"
  "ds_read_u16_d16_hi v112, v6 offset:0\n\t"
  "ds_read_u16_d16_hi v113, v6 offset:8192\n\t"
  "ds_read_u16_d16_hi v114, v6 offset:16384\n\t"
  "ds_read_u16_d16_hi v115, v6 offset:24576\n\t"
  "ds_read_u16_d16_hi v116, v6 offset:32768\n\t"
  "ds_read_u16_d16_hi v117, v124 offset:0\n\t"
  "ds_read_u16_d16_hi v118, v124 offset:8192\n\t"
  "ds_read_u16_d16_hi v119, v124 offset:16384\n\t"
  "ds_read_u16_d16_hi v120, v124 offset:24576\n\t"
  "ds_read_u16_d16_hi v121, v124 offset:32768\n\t"
  "s_cmp_gt_u32 s58, 0\n\t"
  "s_cbranch_scc1 Lc0%=\n\t"
  "s_add_u32 s44, %[sW1], 40960\n\t"
  "buffer_load_dwordx4 a[168:171], v1, %[srd], s44 offen offset:2048 sc1\n\t"
  "s_add_u32 s44, %[sW1], 40960\n\t"
  "buffer_load_dwordx4 a[172:175], v1, %[srd], s44 offen offset:3072 sc1\n\t"
  "s_add_u32 s44, %[sW1], 45056\n\t"
  "buffer_load_dwordx4 a[176:179], v1, %[srd], s44 offen offset:0 sc1\n\t"
  "s_add_u32 s44, %[sW1], 45056\n\t"
  "buffer_load_dwordx4 a[180:183], v1, %[srd], s44 offen offset:1024 sc1\n\t"
  "Lc0%=:\n\t"
  "s_cmp_gt_u32 s58, 1\n\t"
  "s_cbranch_scc1 Lc1%=\n\t"
  "s_add_u32 s44, %[sW1], 45056\n\t"
  "buffer_load_dwordx4 a[184:187], v1, %[srd], s44 offen offset:2048 sc1\n\t"
  "s_add_u32 s44, %[sW1], 45056\n\t"
  "buffer_load_dwordx4 a[188:191], v1, %[srd], s44 offen offset:3072 sc1\n\t"
  "s_add_u32 s44, %[sW1], 49152\n\t"
  "buffer_load_dwordx4 a[192:195], v1, %[srd], s44 offen offset:0 sc1\n\t"
  "s_add_u32 s44, %[sW1], 49152\n\t"
  "buffer_load_dwordx4 a[196:199], v1, %[srd], s44 offen offset:1024 sc1\n\t"
  "Lc1%=:\n\t"
  "s_cmp_gt_u32 s58, 2\n\t"
  "s_cbranch_scc1 Lc2%=\n\t"
  "s_add_u32 s44, %[sW1], 49152\n\t"
  "buffer_load_dwordx4 a[200:203], v1, %[srd], s44 offen offset:2048 sc1\n\t"
  "s_add_u32 s44, %[sW1], 49152\n\t"
  "buffer_load_dwordx4 a[204:207], v1, %[srd], s44 offen offset:3072 sc1\n\t"
  "s_add_u32 s44, %[sW1], 53248\n\t"
  "buffer_load_dwordx4 a[208:211], v1, %[srd], s44 offen offset:0 sc1\n\t"
  "s_add_u32 s44, %[sW1], 53248\n\t"
  "buffer_load_dwordx4 a[212:215], v1, %[srd], s44 offen offset:1024 sc1\n\t"
  "Lc2%=:\n\t"
  "s_cmp_gt_u32 s58, 3\n\t"
  "s_cbranch_scc1 Lc3%=\n\t"
  "s_add_u32 s44, %[sW1], 53248\n\t"
  "buffer_load_dwordx4 a[216:219], v1, %[srd], s44 offen offset:2048 sc1\n\t"
  "s_add_u32 s44, %[sW1], 53248\n\t"
  "buffer_load_dwordx4 a[220:223], v1, %[srd], s44 offen offset:3072 sc1\n\t"
  "s_add_u32 s44, %[sW1], 57344\n\t"
  "buffer_load_dwordx4 a[224:227], v1, %[srd], s44 offen offset:0 sc1\n\t"
  "s_add_u32 s44, %[sW1], 57344\n\t"
  "buffer_load_dwordx4 a[228:231], v1, %[srd], s44 offen offset:1024 sc1\n\t"
  "Lc3%=:\n\t"
  "s_cmp_gt_u32 s58, 4\n\t"
  "s_cbranch_scc1 Lc4%=\n\t"
  "s_add_u32 s44, %[sW1], 57344\n\t"
  "buffer_load_dwordx4 a[232:235], v1, %[srd], s44 offen offset:2048 sc1\n\t"
  "s_add_u32 s44, %[sW1], 57344\n\t"
  "buffer_load_dwordx4 a[236:239], v1, %[srd], s44 offen offset:3072 sc1\n\t"
  "Lc4%=:\n\t"
  "s_waitcnt lgkmcnt(15)\n\t"
  "v_mfma_f32_16x16x32_bf16 v[20:23], a[0:3], v[68:71], 0\n\t"
  "ds_read_b64_tr_b16 v[84:85], v108 offset:8192\n\t"
  "v_add_u32 v110, 40960, v5\n\t"
  "v_mfma_f32_16x16x32_bf16 v[24:27], a[4:7], v[68:71], 0\n\t"
  "ds_read_b64_tr_b16 v[86:87], v108 offset:10240\n\t"
  "v_xor_b32 v111, 32, v5\n\t"
  "v_mfma_f32_16x16x32_bf16 v[28:31], a[8:11], v[68:71], 0\n\t"
  "ds_read_b64_tr_b16 v[88:89], v109 offset:8192\n\t"
  "v_add_u32 v111, 40960, v111\n\t"
  "s_waitcnt lgkmcnt(15)\n\t"
  "v_mfma_f32_16x16x32_bf16 v[32:35], a[0:3], v[72:75], 0\n\t"
  "ds_read_b64_tr_b16 v[90:91], v109 offset:10240\n\t"
  "s_add_u32 m0, s52, 81920\n\t"
  "v_mfma_f32_16x16x32_bf16 v[36:39], a[4:7], v[72:75], 0\n\t"
  "s_mov_b32 s46, 1310720\n\t"
  "buffer_load_dwordx4 v4, %[srd], s46 offen offset:0 sc1 lds\n\t"
  "v_mfma_f32_16x16x32_bf16 v[40:43], a[8:11], v[72:75], 0\n\t"
  "s_waitcnt lgkmcnt(14)\n\t"
  "v_mfma_f32_16x16x32_bf16 v[20:23], a[12:15], v[76:79], v[20:23]\n\t"
  "ds_read_b64_tr_b16 v[68:69], v108 offset:12288\n\t"
  "v_mfma_f32_16x16x32_bf16 v[24:27], a[16:19], v[76:79], v[24:27]\n\t"
  "ds_read_b64_tr_b16 v[70:71], v108 offset:14336\n\t"
  "buffer_load_dwordx4 v4, %[srd], s46 offen offset:1024 sc1 lds\n\t"
  "v_mfma_f32_16x16x32_bf16 v[28:31], a[20:23], v[76:79], v[28:31]\n\t"
  "ds_read_b64_tr_b16 v[72:73], v109 offset:12288\n\t"
  "v_mfma_f32_16x16x32_bf16 v[32:35], a[12:15], v[80:83], v[32:35]\n\t"
  "ds_read_b64_tr_b16 v[74:75], v109 offset:14336\n\t"
  "s_waitcnt lgkmcnt(15)\n\t"
  "v_mul_f32 v112, v112, v142\n\t"
  "v_mul_f32 v113, v113, v143\n\t"
  "v_mfma_f32_16x16x32_bf16 v[36:39], a[16:19], v[80:83], v[36:39]\n\t"
  "v_mul_f32 v114, v114, v248\n\t"
  "s_waitcnt lgkmcnt(14)\n\t"
  "v_mul_f32 v115, v115, v249\n\t"
  "s_waitcnt lgkmcnt(13)\n\t"
  "v_mul_f32 v116, v116, v250\n\t"
  "v_mfma_f32_16x16x32_bf16 v[40:43], a[20:23], v[80:83], v[40:43]\n\t"
  "s_waitcnt lgkmcnt(4)\n\t"
  "v_mul_f32 v117, v117, v251\n\t"
  "v_mul_f32 v118, v118, v252\n\t"
  "v_mul_f32 v119, v119, v253\n\t"
  "v_mfma_f32_16x16x32_bf16 v[20:23], a[24:27], v[84:87], v[20:23]\n\t"
  "ds_read_b64_tr_b16 v[76:77], v108 offset:16384\n\t"
  "v_mul_f32 v120, v120, v254\n\t"
  "v_mul_f32 v121, v121, v255\n\t"
  "v_add_f32 v122, v112, v113\n\t"
  "v_mfma_f32_16x16x32_bf16 v[24:27], a[28:31], v[84:87], v[24:27]\n\t"
  "ds_read_b64_tr_b16 v[78:79], v108 offset:18432\n\t"
  "v_mul_f32 v123, v112, v112\n\t"
  "v_fmac_f32 v123, v113, v113\n\t"
  "v_add_f32 v122, v122, v114\n\t"
  "v_mfma_f32_16x16x32_bf16 v[28:31], a[32:35], v[84:87], v[28:31]\n\t"
  "ds_read_b64_tr_b16 v[80:81], v109 offset:16384\n\t"
  "v_fmac_f32 v123, v114, v114\n\t"
  "v_add_f32 v122, v122, v115\n\t"
  "v_fmac_f32 v123, v115, v115\n\t"
  "v_mfma_f32_16x16x32_bf16 v[32:35], a[24:27], v[88:91], v[32:35]\n\t"
  "ds_read_b64_tr_b16 v[82:83], v109 offset:18432\n\t"
  "v_add_f32 v122, v122, v116\n\t"
  "v_fmac_f32 v123, v116, v116\n\t"
  "v_add_f32 v122, v122, v117\n\t"
  "v_mfma_f32_16x16x32_bf16 v[36:39], a[28:31], v[88:91], v[36:39]\n\t"
  "v_fmac_f32 v123, v117, v117\n\t"
  "v_add_f32 v122, v122, v118\n\t"
  "v_fmac_f32 v123, v118, v118\n\t"
  "v_add_f32 v122, v122, v119\n\t"
  "v_mfma_f32_16x16x32_bf16 v[40:43], a[32:35], v[88:91], v[40:43]\n\t"
  "s_waitcnt lgkmcnt(4)\n\t"
  "v_fmac_f32 v123, v119, v119\n\t"
  "v_add_f32 v122, v122, v120\n\t"
  "v_fmac_f32 v123, v120, v120\n\t"
  "v_mfma_f32_16x16x32_bf16 v[20:23], a[36:39], v[68:71], v[20:23]\n\t"
  "ds_read_b64_tr_b16 v[84:85], v108 offset:20480\n\t"
  "v_add_f32 v122, v122, v121\n\t"
  "v_fmac_f32 v123, v121, v121\n\t"
  "buffer_load_dwordx4 v4, %[srd], s46 offen offset:2048 sc1 lds\n\t"
  "v_mfma_f32_16x16x32_bf16 v[24:27], a[40:43], v[68:71], v[24:27]\n\t"
  "ds_read_b64_tr_b16 v[86:87], v108 offset:22528\n\t"
  "v_add_f32_dpp v122, v122, v122 quad_perm:[1,0,3,2] row_mask:0xf bank_mask:0xf\n\t"
  "v_add_f32_dpp v123, v123, v123 quad_perm:[1,0,3,2] row_mask:0xf bank_mask:0xf\n\t"
  "buffer_load_dwordx4 v4, %[srd], s46 offen offset:3072 sc1 lds\n\t"
  "v_mfma_f32_16x16x32_bf16 v[28:31], a[44:47], v[68:71], v[28:31]\n\t"
  "ds_read_b64_tr_b16 v[88:89], v109 offset:20480\n\t"
  "v_add_f32_dpp v122, v122, v122 quad_perm:[2,3,0,1] row_mask:0xf bank_mask:0xf\n\t"
  "v_add_f32_dpp v123, v123, v123 quad_perm:[2,3,0,1] row_mask:0xf bank_mask:0xf\n\t"
  "s_add_u32 m0, s52, 98304\n\t"
  "v_mfma_f32_16x16x32_bf16 v[32:35], a[36:39], v[72:75], v[32:35]\n\t"
  "ds_read_b64_tr_b16 v[90:91], v109 offset:22528\n\t"
  "v_add_f32_dpp v122, v122, v122 row_half_mirror row_mask:0xf bank_mask:0xf\n\t"
  "v_add_f32_dpp v123, v123, v123 row_half_mirror row_mask:0xf bank_mask:0xf\n\t"
  "s_mov_b32 s46, 1327104\n\t"
  "v_mfma_f32_16x16x32_bf16 v[36:39], a[40:43], v[72:75], v[36:39]\n\t"
  "v_add_f32_dpp v122, v122, v122 row_mirror row_mask:0xf bank_mask:0xf\n\t"
  "v_add_f32_dpp v123, v123, v123 row_mirror row_mask:0xf bank_mask:0xf\n\t"
  "buffer_load_dwordx4 v4, %[srd], s46 offen offset:0 sc1 lds\n\t"
  "v_add_f32_dpp v122, v122, v122 row_bcast:15 row_mask:0xa bank_mask:0xf\n\t"
  "v_mfma_f32_16x16x32_bf16 v[40:43], a[44:47], v[72:75], v[40:43]\n\t"
  "s_waitcnt lgkmcnt(4)\n\t"
  "v_add_f32_dpp v123, v123, v123 row_bcast:15 row_mask:0xa bank_mask:0xf\n\t"
  "v_add_f32_dpp v122, v122, v122 row_bcast:31 row_mask:0xc bank_mask:0xf\n\t"
  "buffer_load_dwordx4 v4, %[srd], s46 offen offset:1024 sc1 lds\n\t"
  "v_mfma_f32_16x16x32_bf16 v[20:23], a[48:51], v[76:79], v[20:23]\n\t"
  "ds_read_b64_tr_b16 v[68:69], v108 offset:24576\n\t"
  "v_add_f32_dpp v123, v123, v123 row_bcast:31 row_mask:0xc bank_mask:0xf\n\t"
  "v_mul_f32 v122, 0xbacccccd, v122\n\t"
  "v_mul_f32 v123, 0x3acccccd, v123\n\t"
  "v_mfma_f32_16x16x32_bf16 v[24:27], a[52:55], v[76:79], v[24:27]\n\t"
  "ds_read_b64_tr_b16 v[70:71], v108 offset:26624\n\t"
  "v_fma_f32 v123, -v122, v122, v123\n\t"
  "v_max_f32 v123, 0, v123\n\t"
  "v_cvt_pk_bf16_f32 v123, 0, v123\n\t"
  "v_mfma_f32_16x16x32_bf16 v[28:31], a[56:59], v[76:79], v[28:31]\n\t"
  "ds_read_b64_tr_b16 v[72:73], v109 offset:24576\n\t"
  "v_cvt_pk_bf16_f32 v126, v122, v122\n\t"
  "v_add_f32 v123, 0x3727c5ac, v123\n\t"
  "v_cvt_pk_bf16_f32 v123, 0, v123\n\t"
  "v_mfma_f32_16x16x32_bf16 v[32:35], a[48:51], v[80:83], v[32:35]\n\t"
  "ds_read_b64_tr_b16 v[74:75], v109 offset:26624\n\t"
  "v_sqrt_f32 v123, v123\n\t"
  "buffer_load_dwordx4 v4, %[srd], s46 offen offset:2048 sc1 lds\n\t"
  "v_cvt_pk_bf16_f32 v127, v123, v123\n\t"
  "v_mfma_f32_16x16x32_bf16 v[36:39], a[52:55], v[80:83], v[36:39]\n\t"
  "buffer_store_dwordx2 v[126:127], v7, %[srd], %[sST] offen sc1\n\t"
  "buffer_load_dwordx4 v4, %[srd], s46 offen offset:3072 sc1 lds\n\t"
  "s_add_u32 m0, s52, 114688\n\t"
  "v_add_u32 v124, 122880, v6\n\t"
  "v_mfma_f32_16x16x32_bf16 v[40:43], a[56:59], v[80:83], v[40:43]\n\t"
  "s_waitcnt lgkmcnt(4)\n\t"
  "v_add_u32 v125, 81920, v6\n\t"
  "v_mfma_f32_16x16x32_bf16 v[20:23], a[60:63], v[84:87], v[20:23]\n\t"
  "ds_read_b64_tr_b16 v[76:77], v108 offset:28672\n\t"
  "v_mov_b32 v112, 0\n\t"
  "v_mfma_f32_16x16x32_bf16 v[24:27], a[64:67], v[84:87], v[24:27]\n\t"
  "ds_read_b64_tr_b16 v[78:79], v108 offset:30720\n\t"
  "s_mov_b32 s46, 1343488\n\t"
  "v_mfma_f32_16x16x32_bf16 v[28:31], a[68:71], v[84:87], v[28:31]\n\t"
  "ds_read_b64_tr_b16 v[80:81], v109 offset:28672\n\t"
  "v_mov_b32 v113, 0\n\t"
  "v_mfma_f32_16x16x32_bf16 v[32:35], a[60:63], v[88:91], v[32:35]\n\t"
  "ds_read_b64_tr_b16 v[82:83], v109 offset:30720\n\t"
  "buffer_load_dwordx4 v4, %[srd], s46 offen offset:0 sc1 lds\n\t"
  "v_mfma_f32_16x16x32_bf16 v[36:39], a[64:67], v[88:91], v[36:39]\n\t"
  "v_mov_b32 v114, 0\n\t"
  "v_mov_b32 v115, 0\n\t"
  "v_mfma_f32_16x16x32_bf16 v[40:43], a[68:71], v[88:91], v[40:43]\n\t"
  "s_waitcnt lgkmcnt(4)\n\t"
  "v_mov_b32 v116, 0\n\t"
  "v_mfma_f32_16x16x32_bf16 v[20:23], a[72:75], v[68:71], v[20:23]\n\t"
  "ds_read_b64_tr_b16 v[84:85], v108 offset:32768\n\t"
  "buffer_load_dwordx4 v4, %[srd], s46 offen offset:1024 sc1 lds\n\t"
  "v_mfma_f32_16x16x32_bf16 v[24:27], a[76:79], v[68:71], v[24:27]\n\t"
  "ds_read_b64_tr_b16 v[86:87], v108 offset:34816\n\t"
  "v_mov_b32 v117, 0\n\t"
  "v_mfma_f32_16x16x32_bf16 v[28:31], a[80:83], v[68:71], v[28:31]\n\t"
  "ds_read_b64_tr_b16 v[88:89], v109 offset:32768\n\t"
  "buffer_load_dwordx4 v4, %[srd], s46 offen offset:2048 sc1 lds\n\t"
  "v_mfma_f32_16x16x32_bf16 v[32:35], a[72:75], v[72:75], v[32:35]\n\t"
  "ds_read_b64_tr_b16 v[90:91], v109 offset:34816\n\t"
  "v_mov_b32 v118, 0\n\t"
  "v_mfma_f32_16x16x32_bf16 v[36:39], a[76:79], v[72:75], v[36:39]\n\t"
  "buffer_load_dwordx4 v4, %[srd], s46 offen offset:3072 sc1 lds\n\t"
  "v_mov_b32 v119, 0\n\t"
  "v_mfma_f32_16x16x32_bf16 v[40:43], a[80:83], v[72:75], v[40:43]\n\t"
  "s_waitcnt lgkmcnt(4)\n\t"
  "v_mov_b32 v120, 0\n\t"
  "v_mfma_f32_16x16x32_bf16 v[20:23], a[84:87], v[76:79], v[20:23]\n\t"
  "ds_read_b64_tr_b16 v[68:69], v108 offset:36864\n\t"
  "v_mov_b32 v121, 0\n\t"
  "v_mfma_f32_16x16x32_bf16 v[24:27], a[88:91], v[76:79], v[24:27]\n\t"
  "ds_read_b64_tr_b16 v[70:71], v108 offset:38912\n\t"
  "s_add_u32 m0, s52, 131072\n\t"
  "v_mfma_f32_16x16x32_bf16 v[28:31], a[92:95], v[76:79], v[28:31]\n\t"
  "ds_read_b64_tr_b16 v[72:73], v109 offset:36864\n\t"
  "v_mfma_f32_16x16x32_bf16 v[32:35], a[84:87], v[80:83], v[32:35]\n\t"
  "ds_read_b64_tr_b16 v[74:75], v109 offset:38912\n\t"
  "s_mov_b32 s46, 1359872\n\t"
  "v_mfma_f32_16x16x32_bf16 v[36:39], a[88:91], v[80:83], v[36:39]\n\t"
  "buffer_load_dwordx2 v[138:139], v2, %[srd], 0 offen sc1\n\t"
  "v_mfma_f32_16x16x32_bf16 v[40:43], a[92:95], v[80:83], v[40:43]\n\t"
  "s_waitcnt lgkmcnt(4)\n\t"
  "v_mfma_f32_16x16x32_bf16 v[20:23], a[96:99], v[84:87], v[20:23]\n\t"
  "ds_read_b64_tr_b16 v[76:77], v110 offset:0\n\t"
  "buffer_load_dwordx4 v4, %[srd], s46 offen offset:0 sc1 lds\n\t"
  "v_mfma_f32_16x16x32_bf16 v[24:27], a[100:103], v[84:87], v[24:27]\n\t"
  "ds_read_b64_tr_b16 v[78:79], v110 offset:2048\n\t"
  "v_mfma_f32_16x16x32_bf16 v[28:31], a[104:107], v[84:87], v[28:31]\n\t"
  "ds_read_b64_tr_b16 v[80:81], v111 offset:0\n\t"
  "buffer_load_dwordx4 v4, %[srd], s46 offen offset:1024 sc1 lds\n\t"
  "v_mfma_f32_16x16x32_bf16 v[32:35], a[96:99], v[88:91], v[32:35]\n\t"
  "ds_read_b64_tr_b16 v[82:83], v111 offset:2048\n\t"
  "v_mfma_f32_16x16x32_bf16 v[36:39], a[100:103], v[88:91], v[36:39]\n\t"
  "buffer_load_dwordx4 v4, %[srd], s46 offen offset:2048 sc1 lds\n\t"
  "v_mfma_f32_16x16x32_bf16 v[40:43], a[104:107], v[88:91], v[40:43]\n\t"
  "s_waitcnt lgkmcnt(4)\n\t"
  "v_mfma_f32_16x16x32_bf16 v[20:23], a[108:111], v[68:71], v[20:23]\n\t"
  "ds_read_b64_tr_b16 v[84:85], v110 offset:4096\n\t"
  "v_mfma_f32_16x16x32_bf16 v[24:27], a[112:115], v[68:71], v[24:27]\n\t"
  "ds_read_b64_tr_b16 v[86:87], v110 offset:6144\n\t"
  "buffer_load_dwordx4 v4, %[srd], s46 offen offset:3072 sc1 lds\n\t"
  "v_mfma_f32_16x16x32_bf16 v[28:31], a[116:119], v[68:71], v[28:31]\n\t"
  "ds_read_b64_tr_b16 v[88:89], v111 offset:4096\n\t"
  "v_mfma_f32_16x16x32_bf16 v[32:35], a[108:111], v[72:75], v[32:35]\n\t"
  "ds_read_b64_tr_b16 v[90:91], v111 offset:6144\n\t"
  "s_add_u32 m0, s52, 147456\n\t"
  "v_mfma_f32_16x16x32_bf16 v[36:39], a[112:115], v[72:75], v[36:39]\n\t"
  "v_mfma_f32_16x16x32_bf16 v[40:43], a[116:119], v[72:75], v[40:43]\n\t"
  "s_waitcnt lgkmcnt(4)\n\t"
  "v_mfma_f32_16x16x32_bf16 v[20:23], a[120:123], v[76:79], v[20:23]\n\t"
  "ds_read_b64_tr_b16 v[68:69], v110 offset:8192\n\t"
  "v_xor_b32 v108, 64, v5\n\t"
  "v_mfma_f32_16x16x32_bf16 v[24:27], a[124:127], v[76:79], v[24:27]\n\t"
  "ds_read_b64_tr_b16 v[70:71], v110 offset:10240\n\t"
  "v_add_u32 v108, 0, v108\n\t"
  "v_mfma_f32_16x16x32_bf16 v[28:31], a[128:131], v[76:79], v[28:31]\n\t"
  "ds_read_b64_tr_b16 v[72:73], v111 offset:8192\n\t"
  "v_xor_b32 v109, 96, v5\n\t"
  "v_mfma_f32_16x16x32_bf16 v[32:35], a[120:123], v[80:83], v[32:35]\n\t"
  "ds_read_b64_tr_b16 v[74:75], v111 offset:10240\n\t"
  "v_add_u32 v109, 0, v109\n\t"
  "v_mfma_f32_16x16x32_bf16 v[36:39], a[124:127], v[80:83], v[36:39]\n\t"
  "s_mov_b32 s46, 1376256\n\t"
  "buffer_load_dwordx4 v4, %[srd], s46 offen offset:0 sc1 lds\n\t"
  "v_mfma_f32_16x16x32_bf16 v[40:43], a[128:131], v[80:83], v[40:43]\n\t"
  "s_waitcnt lgkmcnt(4)\n\t"
  "buffer_load_dwordx4 v4, %[srd], s46 offen offset:1024 sc1 lds\n\t"
  "v_mfma_f32_16x16x32_bf16 v[20:23], a[132:135], v[84:87], v[20:23]\n\t"
  "ds_read_b64_tr_b16 v[76:77], v110 offset:12288\n\t"
  "v_mfma_f32_16x16x32_bf16 v[24:27], a[136:139], v[84:87], v[24:27]\n\t"
  "ds_read_b64_tr_b16 v[78:79], v110 offset:14336\n\t"
  "buffer_load_dwordx4 v4, %[srd], s46 offen offset:2048 sc1 lds\n\t"
  "v_mfma_f32_16x16x32_bf16 v[28:31], a[140:143], v[84:87], v[28:31]\n\t"
  "ds_read_b64_tr_b16 v[80:81], v111 offset:12288\n\t"
  "v_mfma_f32_16x16x32_bf16 v[32:35], a[132:135], v[88:91], v[32:35]\n\t"
  "ds_read_b64_tr_b16 v[82:83], v111 offset:14336\n\t"
  "buffer_load_dwordx4 v4, %[srd], s46 offen offset:3072 sc1 lds\n\t"
  "v_mfma_f32_16x16x32_bf16 v[36:39], a[136:139], v[88:91], v[36:39]\n\t"
  "v_mfma_f32_16x16x32_bf16 v[40:43], a[140:143], v[88:91], v[40:43]\n\t"
  "s_waitcnt lgkmcnt(4)\n\t"
  "v_mfma_f32_16x16x32_bf16 v[20:23], a[144:147], v[68:71], v[20:23]\n\t"
  "ds_read_b64_tr_b16 v[84:85], v110 offset:16384\n\t"
  "v_mfma_f32_16x16x32_bf16 v[24:27], a[148:151], v[68:71], v[24:27]\n\t"
  "ds_read_b64_tr_b16 v[86:87], v110 offset:18432\n\t"
  "v_mfma_f32_16x16x32_bf16 v[28:31], a[152:155], v[68:71], v[28:31]\n\t"
  "ds_read_b64_tr_b16 v[88:89], v111 offset:16384\n\t"
  "v_mfma_f32_16x16x32_bf16 v[32:35], a[144:147], v[72:75], v[32:35]\n\t"
  "ds_read_b64_tr_b16 v[90:91], v111 offset:18432\n\t"
  "v_mfma_f32_16x16x32_bf16 v[36:39], a[148:151], v[72:75], v[36:39]\n\t"
  "v_mfma_f32_16x16x32_bf16 v[40:43], a[152:155], v[72:75], v[40:43]\n\t"
  "s_waitcnt lgkmcnt(4)\n\t"
  "v_mfma_f32_16x16x32_bf16 v[20:23], a[156:159], v[76:79], v[20:23]\n\t"
  "ds_read_b64_tr_b16 v[68:69], v110 offset:20480\n\t"
  "v_mfma_f32_16x16x32_bf16 v[24:27], a[160:163], v[76:79], v[24:27]\n\t"
  "ds_read_b64_tr_b16 v[70:71], v110 offset:22528\n\t"
  "v_mfma_f32_16x16x32_bf16 v[28:31], a[164:167], v[76:79], v[28:31]\n\t"
  "ds_read_b64_tr_b16 v[72:73], v111 offset:20480\n\t"
  "v_mfma_f32_16x16x32_bf16 v[32:35], a[156:159], v[80:83], v[32:35]\n\t"
  "ds_read_b64_tr_b16 v[74:75], v111 offset:22528\n\t"
  "v_mfma_f32_16x16x32_bf16 v[36:39], a[160:163], v[80:83], v[36:39]\n\t"
  "v_mfma_f32_16x16x32_bf16 v[40:43], a[164:167], v[80:83], v[40:43]\n\t"
  "s_waitcnt lgkmcnt(4)\n\t"
  "s_waitcnt vmcnt(39)\n\t"
  "v_mfma_f32_16x16x32_bf16 v[20:23], a[168:171], v[84:87], v[20:23]\n\t"
  "ds_read_b64_tr_b16 v[76:77], v110 offset:24576\n\t"
  "s_waitcnt vmcnt(38)\n\t"
  "v_mfma_f32_16x16x32_bf16 v[24:27], a[172:175], v[84:87], v[24:27]\n\t"
  "ds_read_b64_tr_b16 v[78:79], v110 offset:26624\n\t"
  "s_waitcnt vmcnt(37)\n\t"
  "v_mfma_f32_16x16x32_bf16 v[28:31], a[176:179], v[84:87], v[28:31]\n\t"
  "ds_read_b64_tr_b16 v[80:81], v111 offset:24576\n\t"
  "v_mfma_f32_16x16x32_bf16 v[32:35], a[168:171], v[88:91], v[32:35]\n\t"
  "ds_read_b64_tr_b16 v[82:83], v111 offset:26624\n\t"
  "v_mfma_f32_16x16x32_bf16 v[36:39], a[172:175], v[88:91], v[36:39]\n\t"
  "v_mfma_f32_16x16x32_bf16 v[40:43], a[176:179], v[88:91], v[40:43]\n\t"
  "s_waitcnt lgkmcnt(4)\n\t"
  "s_waitcnt vmcnt(36)\n\t"
  "v_mfma_f32_16x16x32_bf16 v[20:23], a[180:183], v[68:71], v[20:23]\n\t"
  "ds_read_b64_tr_b16 v[84:85], v110 offset:28672\n\t"
  "s_waitcnt vmcnt(35)\n\t"
  "v_mfma_f32_16x16x32_bf16 v[24:27], a[184:187], v[68:71], v[24:27]\n\t"
  "ds_read_b64_tr_b16 v[86:87], v110 offset:30720\n\t"
  "s_waitcnt vmcnt(34)\n\t"
  "v_mfma_f32_16x16x32_bf16 v[28:31], a[188:191], v[68:71], v[28:31]\n\t"
  "ds_read_b64_tr_b16 v[88:89], v111 offset:28672\n\t"
  "v_mfma_f32_16x16x32_bf16 v[32:35], a[180:183], v[72:75], v[32:35]\n\t"
  "ds_read_b64_tr_b16 v[90:91], v111 offset:30720\n\t"
  "v_mfma_f32_16x16x32_bf16 v[36:39], a[184:187], v[72:75], v[36:39]\n\t"
  "v_mfma_f32_16x16x32_bf16 v[40:43], a[188:191], v[72:75], v[40:43]\n\t"
  "s_waitcnt lgkmcnt(4)\n\t"
  "s_waitcnt vmcnt(33)\n\t"
  "v_mfma_f32_16x16x32_bf16 v[20:23], a[192:195], v[76:79], v[20:23]\n\t"
  "ds_read_b64_tr_b16 v[68:69], v110 offset:32768\n\t"
  "s_waitcnt vmcnt(8)\n\t"
  "v_cmp_ne_u64 vcc, v[132:133], v[138:139]\n\t"
  "v_mfma_f32_16x16x32_bf16 v[24:27], a[196:199], v[76:79], v[24:27]\n\t"
  "ds_read_b64_tr_b16 v[70:71], v110 offset:34816\n\t"
  "v_mfma_f32_16x16x32_bf16 v[28:31], a[200:203], v[76:79], v[28:31]\n\t"
  "ds_read_b64_tr_b16 v[72:73], v111 offset:32768\n\t"
  "s_cbranch_vccnz Lw2_slow%=\n\t"
  "Lw2_back%=:\n\t"
  "v_mfma_f32_16x16x32_bf16 v[32:35], a[192:195], v[80:83], v[32:35]\n\t"
  "ds_read_b64_tr_b16 v[74:75], v111 offset:34816\n\t"
  "v_mfma_f32_16x16x32_bf16 v[36:39], a[196:199], v[80:83], v[36:39]\n\t"
  "v_mfma_f32_16x16x32_bf16 v[40:43], a[200:203], v[80:83], v[40:43]\n\t"
  "s_waitcnt lgkmcnt(4)\n\t"
  "v_mfma_f32_16x16x32_bf16 v[20:23], a[204:207], v[84:87], v[20:23]\n\t"
  "ds_read_b64_tr_b16 v[76:77], v110 offset:36864\n\t"
  "v_lshrrev_b32 v138, 1, v1\n\t"
  "v_mfma_f32_16x16x32_bf16 v[24:27], a[208:211], v[84:87], v[24:27]\n\t"
  "ds_read_b64_tr_b16 v[78:79], v110 offset:38912\n\t"
  "buffer_load_dwordx2 a[240:241], v138, %[srd], %[sUB] offen offset:0 sc1\n\t"
  "v_mfma_f32_16x16x32_bf16 v[28:31], a[212:215], v[84:87], v[28:31]\n\t"
  "ds_read_b64_tr_b16 v[80:81], v111 offset:36864\n\t"
  "buffer_load_dwordx2 a[242:243], v138, %[srd], %[sUB] offen offset:512 sc1\n\t"
  "v_mfma_f32_16x16x32_bf16 v[32:35], a[204:207], v[88:91], v[32:35]\n\t"
  "ds_read_b64_tr_b16 v[82:83], v111 offset:38912\n\t"
  "buffer_load_dwordx2 a[244:245], v138, %[srd], %[sUB] offen offset:1024 sc1\n\t"
  "v_mfma_f32_16x16x32_bf16 v[36:39], a[208:211], v[88:91], v[36:39]\n\t"
  "s_add_u32 s45, %[sW2], 0\n\t"
  "buffer_load_dwordx4 v[144:147], v1, %[srd], s45 offen offset:0 sc1\n\t"
  "v_mfma_f32_16x16x32_bf16 v[40:43], a[212:215], v[88:91], v[40:43]\n\t"
  "s_waitcnt lgkmcnt(4)\n\t"
  "v_mfma_f32_16x16x32_bf16 v[20:23], a[216:219], v[68:71], v[20:23]\n\t"
  "ds_read_b64_tr_b16 v[84:85], v108 offset:0\n\t"
  "v_mfma_f32_16x16x32_bf16 v[24:27], a[220:223], v[68:71], v[24:27]\n\t"
  "ds_read_b64_tr_b16 v[86:87], v108 offset:2048\n\t"
  "v_mfma_f32_16x16x32_bf16 v[28:31], a[224:227], v[68:71], v[28:31]\n\t"
  "ds_read_b64_tr_b16 v[88:89], v109 offset:0\n\t"
  "v_mfma_f32_16x16x32_bf16 v[32:35], a[216:219], v[72:75], v[32:35]\n\t"
  "ds_read_b64_tr_b16 v[90:91], v109 offset:2048\n\t"
  "buffer_load_dwordx4 v[148:151], v1, %[srd], s45 offen offset:1024 sc1\n\t"
  "v_mfma_f32_16x16x32_bf16 v[36:39], a[220:223], v[72:75], v[36:39]\n\t"
  "v_mfma_f32_16x16x32_bf16 v[40:43], a[224:227], v[72:75], v[40:43]\n\t"
  "s_waitcnt lgkmcnt(4)\n\t"
  "v_mfma_f32_16x16x32_bf16 v[20:23], a[228:231], v[76:79], v[20:23]\n\t"
  "ds_read_b64_tr_b16 v[68:69], v108 offset:4096\n\t"
  "v_mfma_f32_16x16x32_bf16 v[24:27], a[232:235], v[76:79], v[24:27]\n\t"
  "ds_read_b64_tr_b16 v[70:71], v108 offset:6144\n\t"
  "buffer_load_dwordx4 v[152:155], v1, %[srd], s45 offen offset:2048 sc1\n\t"
  "v_mfma_f32_16x16x32_bf16 v[28:31], a[236:239], v[76:79], v[28:31]\n\t"
  "ds_read_b64_tr_b16 v[72:73], v109 offset:4096\n\t"
  "v_mfma_f32_16x16x32_bf16 v[32:35], a[228:231], v[80:83], v[32:35]\n\t"
  "ds_read_b64_tr_b16 v[74:75], v109 offset:6144\n\t"
  "v_mfma_f32_16x16x32_bf16 v[36:39], a[232:235], v[80:83], v[36:39]\n\t"
  "v_mfma_f32_16x16x32_bf16 v[40:43], a[236:239], v[80:83], v[40:43]\n\t"
  "s_waitcnt lgkmcnt(4)\n\t"
  "v_mfma_f32_16x16x32_bf16 v[44:47], a[0:3], v[84:87], 0\n\t"
  "ds_read_b64_tr_b16 v[76:77], v108 offset:8192\n\t"
  "v_xor_b32 v110, 64, v5\n\t"
  "v_mfma_f32_16x16x32_bf16 v[48:51], a[4:7], v[84:87], 0\n\t"
  "ds_read_b64_tr_b16 v[78:79], v108 offset:10240\n\t"
  "v_add_u32 v110, 40960, v110\n\t"
  "v_mfma_f32_16x16x32_bf16 v[52:55], a[8:11], v[84:87], 0\n\t"
  "ds_read_b64_tr_b16 v[80:81], v109 offset:8192\n\t"
  "v_xor_b32 v111, 96, v5\n\t"
  "v_mfma_f32_16x16x32_bf16 v[56:59], a[0:3], v[88:91], 0\n\t"
  "ds_read_b64_tr_b16 v[82:83], v109 offset:10240\n\t"
  "v_add_u32 v111, 40960, v111\n\t"
  "v_mfma_f32_16x16x32_bf16 v[60:63], a[4:7], v[88:91], 0\n\t"
  "buffer_load_dwordx4 v[156:159], v1, %[srd], s45 offen offset:3072 sc1\n\t"
  "s_add_u32 s45, %[sW2], 4096\n\t"
  "v_mfma_f32_16x16x32_bf16 v[64:67], a[8:11], v[88:91], 0\n\t"
  "s_waitcnt lgkmcnt(4)\n\t"
  "v_mfma_f32_16x16x32_bf16 v[44:47], a[12:15], v[68:71], v[44:47]\n\t"
  "ds_read_b64_tr_b16 v[84:85], v108 offset:12288\n\t"
  "buffer_load_dwordx2 v[92:93], v136, %[srd], %[sST] offen offset:0 sc1\n\t"
  "v_mfma_f32_16x16x32_bf16 v[48:51], a[16:19], v[68:71], v[48:51]\n\t"
  "ds_read_b64_tr_b16 v[86:87], v108 offset:14336\n\t"
  "buffer_load_dwordx2 v[94:95], v136, %[srd], %[sST] offen offset:1024 sc1\n\t"
  "v_mfma_f32_16x16x32_bf16 v[52:55], a[20:23], v[68:71], v[52:55]\n\t"
  "ds_read_b64_tr_b16 v[88:89], v109 offset:12288\n\t"
  "buffer_load_dwordx2 v[96:97], v136, %[srd], %[sST] offen offset:2048 sc1\n\t"
  "v_mfma_f32_16x16x32_bf16 v[56:59], a[12:15], v[72:75], v[56:59]\n\t"
  "ds_read_b64_tr_b16 v[90:91], v109 offset:14336\n\t"
  "buffer_load_dwordx2 v[98:99], v136, %[srd], %[sST] offen offset:3072 sc1\n\t"
  "v_mfma_f32_16x16x32_bf16 v[60:63], a[16:19], v[72:75], v[60:63]\n\t"
  "buffer_load_dwordx4 v[160:163], v1, %[srd], s45 offen offset:0 sc1\n\t"
  "v_mfma_f32_16x16x32_bf16 v[64:67], a[20:23], v[72:75], v[64:67]\n\t"
  "s_waitcnt lgkmcnt(4)\n\t"
  "v_mfma_f32_16x16x32_bf16 v[44:47], a[24:27], v[76:79], v[44:47]\n\t"
  "ds_read_b64_tr_b16 v[68:69], v108 offset:16384\n\t"
  "v_mfma_f32_16x16x32_bf16 v[48:51], a[28:31], v[76:79], v[48:51]\n\t"
  "ds_read_b64_tr_b16 v[70:71], v108 offset:18432\n\t"
  "buffer_load_dwordx4 v[164:167], v1, %[srd], s45 offen offset:1024 sc1\n\t"
  "v_mfma_f32_16x16x32_bf16 v[52:55], a[32:35], v[76:79], v[52:55]\n\t"
  "ds_read_b64_tr_b16 v[72:73], v109 offset:16384\n\t"
  "s_waitcnt vmcnt(13)\n\t"
  "s_barrier\n\t"
  "v_mfma_f32_16x16x32_bf16 v[56:59], a[24:27], v[80:83], v[56:59]\n\t"
  "ds_read_b64_tr_b16 v[74:75], v109 offset:18432\n\t"
  "ds_read_u16_d16_hi v112, v125 offset:0\n\t"
  "v_mfma_f32_16x16x32_bf16 v[60:63], a[28:31], v[80:83], v[60:63]\n\t"
  "ds_read_u16_d16_hi v113, v125 offset:8192\n\t"
  "ds_read_b64_tr_b16 a[246:247], v10 offset:0\n\t"
  "v_mfma_f32_16x16x32_bf16 v[64:67], a[32:35], v[80:83], v[64:67]\n\t"
  "s_waitcnt lgkmcnt(7)\n\t"
  "ds_read_u16_d16_hi v114, v125 offset:16384\n\t"
  "v_mfma_f32_16x16x32_bf16 v[44:47], a[36:39], v[84:87], v[44:47]\n\t"
  "ds_read_b64_tr_b16 v[76:77], v108 offset:20480\n\t"
  "buffer_load_dwordx4 v[168:171], v1, %[srd], s45 offen offset:2048 sc1\n\t"
  "v_mfma_f32_16x16x32_bf16 v[48:51], a[40:43], v[84:87], v[48:51]\n\t"
  "ds_read_b64_tr_b16 v[78:79], v108 offset:22528\n\t"
  "ds_read_u16_d16_hi v115, v125 offset:24576\n\t"
  "v_mfma_f32_16x16x32_bf16 v[52:55], a[44:47], v[84:87], v[52:55]\n\t"
  "ds_read_b64_tr_b16 v[80:81], v109 offset:20480\n\t"
  "ds_read_u16_d16_hi v116, v125 offset:32768\n\t"
  "v_mfma_f32_16x16x32_bf16 v[56:59], a[36:39], v[88:91], v[56:59]\n\t"
  "ds_read_b64_tr_b16 v[82:83], v109 offset:22528\n\t"
  "ds_read_u16_d16_hi v117, v124 offset:0\n\t"
  "v_mfma_f32_16x16x32_bf16 v[60:63], a[40:43], v[88:91], v[60:63]\n\t"
  "buffer_load_dwordx4 v[172:175], v1, %[srd], s45 offen offset:3072 sc1\n\t"
  "ds_read_u16_d16_hi v118, v124 offset:8192\n\t"
  "v_mfma_f32_16x16x32_bf16 v[64:67], a[44:47], v[88:91], v[64:67]\n\t"
  "s_waitcnt lgkmcnt(12)\n\t"
  "ds_read_u16_d16_hi v119, v124 offset:16384\n\t"
  "v_mfma_f32_16x16x32_bf16 v[44:47], a[48:51], v[68:71], v[44:47]\n\t"
  "ds_read_b64_tr_b16 v[84:85], v108 offset:24576\n\t"
  "ds_read_u16_d16_hi v120, v124 offset:24576\n\t"
  "v_mfma_f32_16x16x32_bf16 v[48:51], a[52:55], v[68:71], v[48:51]\n\t"
  "ds_read_b64_tr_b16 v[86:87], v108 offset:26624\n\t"
  "ds_read_u16_d16_hi v121, v124 offset:32768\n\t"
  "v_mfma_f32_16x16x32_bf16 v[52:55], a[56:59], v[68:71], v[52:55]\n\t"
  "ds_read_b64_tr_b16 v[88:89], v109 offset:24576\n\t"
  "ds_read_b64_tr_b16 a[248:249], v11 offset:0\n\t"
  "v_mfma_f32_16x16x32_bf16 v[56:59], a[48:51], v[72:75], v[56:59]\n\t"
  "ds_read_b64_tr_b16 v[90:91], v109 offset:26624\n\t"
  "s_add_u32 s45, %[sW2], 8192\n\t"
  "v_mfma_f32_16x16x32_bf16 v[60:63], a[52:55], v[72:75], v[60:63]\n\t"
  "ds_read_b64_tr_b16 a[250:251], v10 offset:4096\n\t"
  "ds_read_b64_tr_b16 a[252:253], v11 offset:4096\n\t"
  "v_mfma_f32_16x16x32_bf16 v[64:67], a[56:59], v[72:75], v[64:67]\n\t"
  "s_waitcnt lgkmcnt(12)\n\t"
  "ds_read_b64_tr_b16 a[254:255], v12 offset:0\n\t"
  "v_mfma_f32_16x16x32_bf16 v[44:47], a[60:63], v[76:79], v[44:47]\n\t"
  "ds_read_b64_tr_b16 v[68:69], v108 offset:28672\n\t"
  "v_mfma_f32_16x16x32_bf16 v[48:51], a[64:67], v[76:79], v[48:51]\n\t"
  "ds_read_b64_tr_b16 v[70:71], v108 offset:30720\n\t"
  "v_mfma_f32_16x16x32_bf16 v[52:55], a[68:71], v[76:79], v[52:55]\n\t"
  "ds_read_b64_tr_b16 v[72:73], v109 offset:28672\n\t"
  "buffer_load_dwordx4 v[176:179], v1, %[srd], s45 offen offset:0 sc1\n\t"
  "v_mfma_f32_16x16x32_bf16 v[56:59], a[60:63], v[80:83], v[56:59]\n\t"
  "ds_read_b64_tr_b16 v[74:75], v109 offset:30720\n\t"
  "v_mfma_f32_16x16x32_bf16 v[60:63], a[64:67], v[80:83], v[60:63]\n\t"
  "v_mfma_f32_16x16x32_bf16 v[64:67], a[68:71], v[80:83], v[64:67]\n\t"
  "s_waitcnt lgkmcnt(7)\n\t"
  "v_mul_f32 v112, v112, v142\n\t"
  "v_mfma_f32_16x16x32_bf16 v[44:47], a[72:75], v[84:87], v[44:47]\n\t"
  "ds_read_b64_tr_b16 v[76:77], v108 offset:32768\n\t"
  "buffer_load_dwordx4 v[180:183], v1, %[srd], s45 offen offset:1024 sc1\n\t"
  "v_mfma_f32_16x16x32_bf16 v[48:51], a[76:79], v[84:87], v[48:51]\n\t"
  "ds_read_b64_tr_b16 v[78:79], v108 offset:34816\n\t"
  "v_mul_f32 v113, v113, v143\n\t"
  "v_mfma_f32_16x16x32_bf16 v[52:55], a[80:83], v[84:87], v[52:55]\n\t"
  "ds_read_b64_tr_b16 v[80:81], v109 offset:32768\n\t"
  "v_mul_f32 v114, v114, v248\n\t"
  "v_mfma_f32_16x16x32_bf16 v[56:59], a[72:75], v[88:91], v[56:59]\n\t"
  "ds_read_b64_tr_b16 v[82:83], v109 offset:34816\n\t"
  "v_mul_f32 v115, v115, v249\n\t"
  "v_mfma_f32_16x16x32_bf16 v[60:63], a[76:79], v[88:91], v[60:63]\n\t"
  "v_mul_f32 v116, v116, v250\n\t"
  "v_mul_f32 v117, v117, v251\n\t"
  "v_mfma_f32_16x16x32_bf16 v[64:67], a[80:83], v[88:91], v[64:67]\n\t"
  "s_waitcnt lgkmcnt(4)\n\t"
  "buffer_load_dwordx4 v[184:187], v1, %[srd], s45 offen offset:2048 sc1\n\t"
  "v_mfma_f32_16x16x32_bf16 v[44:47], a[84:87], v[68:71], v[44:47]\n\t"
  "ds_read_b64_tr_b16 v[84:85], v108 offset:36864\n\t"
  "v_mul_f32 v118, v118, v252\n\t"
  "v_mfma_f32_16x16x32_bf16 v[48:51], a[88:91], v[68:71], v[48:51]\n\t"
  "ds_read_b64_tr_b16 v[86:87], v108 offset:38912\n\t"
  "v_mul_f32 v119, v119, v253\n\t"
  "v_mfma_f32_16x16x32_bf16 v[52:55], a[92:95], v[68:71], v[52:55]\n\t"
  "ds_read_b64_tr_b16 v[88:89], v109 offset:36864\n\t"
  "v_mul_f32 v120, v120, v254\n\t"
  "v_mfma_f32_16x16x32_bf16 v[56:59], a[84:87], v[72:75], v[56:59]\n\t"
  "ds_read_b64_tr_b16 v[90:91], v109 offset:38912\n\t"
  "buffer_load_dwordx4 v[188:191], v1, %[srd], s45 offen offset:3072 sc1\n\t"
  "v_mfma_f32_16x16x32_bf16 v[60:63], a[88:91], v[72:75], v[60:63]\n\t"
  "v_mul_f32 v121, v121, v255\n\t"
  "v_add_f32 v122, v112, v113\n\t"
  "v_mfma_f32_16x16x32_bf16 v[64:67], a[92:95], v[72:75], v[64:67]\n\t"
  "s_waitcnt lgkmcnt(4)\n\t"
  "v_mul_f32 v123, v112, v112\n\t"
  "v_mfma_f32_16x16x32_bf16 v[44:47], a[96:99], v[76:79], v[44:47]\n\t"
  "ds_read_b64_tr_b16 v[68:69], v110 offset:0\n\t"
  "v_fmac_f32 v123, v113, v113\n\t"
  "v_mfma_f32_16x16x32_bf16 v[48:51], a[100:103], v[76:79], v[48:51]\n\t"
  "ds_read_b64_tr_b16 v[70:71], v110 offset:2048\n\t"
  "v_add_f32 v122, v122, v114\n\t"
  "v_mfma_f32_16x16x32_bf16 v[52:55], a[104:107], v[76:79], v[52:55]\n\t"
  "ds_read_b64_tr_b16 v[72:73], v111 offset:0\n\t"
  "s_add_u32 s45, %[sW2], 12288\n\t"
  "v_mfma_f32_16x16x32_bf16 v[56:59], a[96:99], v[80:83], v[56:59]\n\t"
  "ds_read_b64_tr_b16 v[74:75], v111 offset:2048\n\t"
  "v_fmac_f32 v123, v114, v114\n\t"
  "v_mfma_f32_16x16x32_bf16 v[60:63], a[100:103], v[80:83], v[60:63]\n\t"
  "v_add_f32 v122, v122, v115\n\t"
  "v_fmac_f32 v123, v115, v115\n\t"
  "v_mfma_f32_16x16x32_bf16 v[64:67], a[104:107], v[80:83], v[64:67]\n\t"
  "s_waitcnt lgkmcnt(4)\n\t"
  "v_add_f32 v122, v122, v116\n\t"
  "v_mfma_f32_16x16x32_bf16 v[44:47], a[108:111], v[84:87], v[44:47]\n\t"
  "ds_read_b64_tr_b16 v[76:77], v110 offset:4096\n\t"
  "buffer_load_dwordx4 v[192:195], v1, %[srd], s45 offen offset:0 sc1\n\t"
  "v_mfma_f32_16x16x32_bf16 v[48:51], a[112:115], v[84:87], v[48:51]\n\t"
  "ds_read_b64_tr_b16 v[78:79], v110 offset:6144\n\t"
  "v_fmac_f32 v123, v116, v116\n\t"
  "v_mfma_f32_16x16x32_bf16 v[52:55], a[116:119], v[84:87], v[52:55]\n\t"
  "ds_read_b64_tr_b16 v[80:81], v111 offset:4096\n\t"
  "v_add_f32 v122, v122, v117\n\t"
  "v_mfma_f32_16x16x32_bf16 v[56:59], a[108:111], v[88:91], v[56:59]\n\t"
  "ds_read_b64_tr_b16 v[82:83], v111 offset:6144\n\t"
  "v_fmac_f32 v123, v117, v117\n\t"
  "v_mfma_f32_16x16x32_bf16 v[60:63], a[112:115], v[88:91], v[60:63]\n\t"
  "v_add_f32 v122, v122, v118\n\t"
  "v_fmac_f32 v123, v118, v118\n\t"
  "v_mfma_f32_16x16x32_bf16 v[64:67], a[116:119], v[88:91], v[64:67]\n\t"
  "s_waitcnt lgkmcnt(4)\n\t"
  "s_waitcnt vmcnt(11)\n\t"
  "v_min_u32 v131, v93, v95\n\t"
  "v_mfma_f32_16x16x32_bf16 v[44:47], a[120:123], v[68:71], v[44:47]\n\t"
  "ds_read_b64_tr_b16 v[84:85], v110 offset:8192\n\t"
  "s_waitcnt vmcnt(9)\n\t"
  "v_min3_u32 v131, v131, v97, v99\n\t"
  "v_mfma_f32_16x16x32_bf16 v[48:51], a[124:127], v[68:71], v[48:51]\n\t"
  "ds_read_b64_tr_b16 v[86:87], v110 offset:10240\n\t"
  "v_cmp_eq_u32 vcc, 0, v131\n\t"
  "v_mfma_f32_16x16x32_bf16 v[52:55], a[128:131], v[68:71], v[52:55]\n\t"
  "ds_read_b64_tr_b16 v[88:89], v111 offset:8192\n\t"
  "v_add_u32 v108, 81920, v5\n\t"
  "v_mfma_f32_16x16x32_bf16 v[56:59], a[120:123], v[72:75], v[56:59]\n\t"
  "ds_read_b64_tr_b16 v[90:91], v111 offset:10240\n\t"
  "s_cbranch_vccnz Lst0_slow%=\n\t"
  "Lst0_back%=:\n\t"
  "v_mfma_f32_16x16x32_bf16 v[60:63], a[124:127], v[72:75], v[60:63]\n\t"
  "v_xor_b32 v109, 32, v5\n\t"
  "v_add_u32 v109, 81920, v109\n\t"
  "v_mfma_f32_16x16x32_bf16 v[64:67], a[128:131], v[72:75], v[64:67]\n\t"
  "s_waitcnt lgkmcnt(4)\n\t"
  "buffer_load_dwordx4 v[196:199], v1, %[srd], s45 offen offset:1024 sc1\n\t"
  "v_mfma_f32_16x16x32_bf16 v[44:47], a[132:135], v[76:79], v[44:47]\n\t"
  "ds_read_b64_tr_b16 v[68:69], v110 offset:12288\n\t"
  "buffer_load_dwordx4 v[200:203], v1, %[srd], s45 offen offset:2048 sc1\n\t"
  "v_mfma_f32_16x16x32_bf16 v[48:51], a[136:139], v[76:79], v[48:51]\n\t"
  "ds_read_b64_tr_b16 v[70:71], v110 offset:14336\n\t"
  "v_add_f32 v122, v122, v119\n\t"
  "v_mfma_f32_16x16x32_bf16 v[52:55], a[140:143], v[76:79], v[52:55]\n\t"
  "ds_read_b64_tr_b16 v[72:73], v111 offset:12288\n\t"
  "buffer_load_dwordx4 v[204:207], v1, %[srd], s45 offen offset:3072 sc1\n\t"
  "v_mfma_f32_16x16x32_bf16 v[56:59], a[132:135], v[80:83], v[56:59]\n\t"
  "ds_read_b64_tr_b16 v[74:75], v111 offset:14336\n\t"
  "v_fmac_f32 v123, v119, v119\n\t"
  "v_mfma_f32_16x16x32_bf16 v[60:63], a[136:139], v[80:83], v[60:63]\n\t"
  "v_add_f32 v122, v122, v120\n\t"
  "v_fmac_f32 v123, v120, v120\n\t"
  "v_mfma_f32_16x16x32_bf16 v[64:67], a[140:143], v[80:83], v[64:67]\n\t"
  "v_add_f32 v122, v122, v121\n\t"
  "v_fmac_f32 v123, v121, v121\n\t"
  "v_mfma_f32_16x16x16_bf16 v[20:23], a[240:241], v[92:93], v[20:23]\n\t"
  "s_add_u32 s45, %[sW2], 16384\n\t"
  "v_add_f32_dpp v122, v122, v122 quad_perm:[1,0,3,2] row_mask:0xf bank_mask:0xf\n\t"
  "v_mfma_f32_16x16x16_bf16 v[24:27], a[242:243], v[92:93], v[24:27]\n\t"
  "v_add_f32_dpp v123, v123, v123 quad_perm:[1,0,3,2] row_mask:0xf bank_mask:0xf\n\t"
  "v_add_f32_dpp v122, v122, v122 quad_perm:[2,3,0,1] row_mask:0xf bank_mask:0xf\n\t"
  "v_mfma_f32_16x16x16_bf16 v[28:31], a[244:245], v[92:93], v[28:31]\n\t"
  "v_add_f32_dpp v123, v123, v123 quad_perm:[2,3,0,1] row_mask:0xf bank_mask:0xf\n\t"
  "v_add_f32_dpp v122, v122, v122 row_half_mirror row_mask:0xf bank_mask:0xf\n\t"
  "v_mfma_f32_16x16x16_bf16 v[32:35], a[240:241], v[94:95], v[32:35]\n\t"
  "v_add_f32_dpp v123, v123, v123 row_half_mirror row_mask:0xf bank_mask:0xf\n\t"
  "v_add_f32_dpp v122, v122, v122 row_mirror row_mask:0xf bank_mask:0xf\n\t"
  "v_mfma_f32_16x16x16_bf16 v[36:39], a[242:243], v[94:95], v[36:39]\n\t"
  "v_add_f32_dpp v123, v123, v123 row_mirror row_mask:0xf bank_mask:0xf\n\t"
  "v_add_f32_dpp v122, v122, v122 row_bcast:15 row_mask:0xa bank_mask:0xf\n\t"
  "v_mfma_f32_16x16x16_bf16 v[40:43], a[244:245], v[94:95], v[40:43]\n\t"
  "s_waitcnt lgkmcnt(4)\n\t"
  "buffer_load_dwordx4 v[208:211], v1, %[srd], s45 offen offset:0 sc1\n\t"
  "v_mfma_f32_16x16x32_bf16 v[44:47], a[144:147], v[84:87], v[44:47]\n\t"
  "ds_read_b64_tr_b16 v[76:77], v110 offset:16384\n\t"
  "v_add_f32_dpp v123, v123, v123 row_bcast:15 row_mask:0xa bank_mask:0xf\n\t"
  "v_mfma_f32_16x16x32_bf16 v[48:51], a[148:151], v[84:87], v[48:51]\n\t"
  "ds_read_b64_tr_b16 v[78:79], v110 offset:18432\n\t"
  "v_add_f32_dpp v122, v122, v122 row_bcast:31 row_mask:0xc bank_mask:0xf\n\t"
  "v_mfma_f32_16x16x32_bf16 v[52:55], a[152:155], v[84:87], v[52:55]\n\t"
  "ds_read_b64_tr_b16 v[80:81], v111 offset:16384\n\t"
  "v_add_f32_dpp v123, v123, v123 row_bcast:31 row_mask:0xc bank_mask:0xf\n\t"
  "v_mfma_f32_16x16x32_bf16 v[56:59], a[144:147], v[88:91], v[56:59]\n\t"
  "ds_read_b64_tr_b16 v[82:83], v111 offset:18432\n\t"
  "v_mul_f32 v122, 0xbacccccd, v122\n\t"
  "v_mfma_f32_16x16x32_bf16 v[60:63], a[148:151], v[88:91], v[60:63]\n\t"
  "buffer_load_dwordx4 v[212:215], v1, %[srd], s45 offen offset:1024 sc1\n\t"
  "v_mul_f32 v123, 0x3acccccd, v123\n\t"
  "v_mfma_f32_16x16x32_bf16 v[64:67], a[152:155], v[88:91], v[64:67]\n\t"
  "s_waitcnt lgkmcnt(4)\n\t"
  "v_fma_f32 v123, -v122, v122, v123\n\t"
  "v_max_f32 v123, 0, v123\n\t"
  "v_cvt_pk_bf16_f32 v123, 0, v123\n\t"
  "v_cvt_pk_bf16_f32 v126, v122, v122\n\t"
  "v_add_f32 v123, 0x3727c5ac, v123\n\t"
  "v_cvt_pk_bf16_f32 v123, 0, v123\n\t"
  "v_sqrt_f32 v123, v123\n\t"
  "s_nop 0\n\t"
  "v_cvt_pk_bf16_f32 v127, v123, v123\n\t"
  "buffer_store_dwordx2 v[126:127], v7, %[srd], s54 offen sc1\n\t"
  "v_mfma_f32_16x16x32_bf16 v[44:47], a[156:159], v[68:71], v[44:47]\n\t"
  "ds_read_b64_tr_b16 v[84:85], v110 offset:20480\n\t"
  "v_and_b32 v128, 0xffff0000, v93\n\t"
  "v_mfma_f32_16x16x32_bf16 v[48:51], a[160:163], v[68:71], v[48:51]\n\t"
  "ds_read_b64_tr_b16 v[86:87], v110 offset:22528\n\t"
  "v_and_b32 v129, 0xffff0000, v95\n\t"
  "v_mfma_f32_16x16x32_bf16 v[52:55], a[164:167], v[68:71], v[52:55]\n\t"
  "ds_read_b64_tr_b16 v[88:89], v111 offset:20480\n\t"
  "buffer_load_dwordx4 v[216:219], v1, %[srd], s45 offen offset:2048 sc1\n\t"
  "v_mfma_f32_16x16x32_bf16 v[56:59], a[156:159], v[72:75], v[56:59]\n\t"
  "ds_read_b64_tr_b16 v[90:91], v111 offset:22528\n\t"
  "v_rcp_f32 v128, v128\n\t"
  "v_mfma_f32_16x16x32_bf16 v[60:63], a[160:163], v[72:75], v[60:63]\n\t"
  "v_rcp_f32 v129, v129\n\t"
  "v_mul_f32 v128, v128, v128\n\t"
  "v_mfma_f32_16x16x32_bf16 v[64:67], a[164:167], v[72:75], v[64:67]\n\t"
  "s_waitcnt lgkmcnt(4)\n\t"
  "v_mul_f32 v129, v129, v129\n\t"
  "v_mfma_f32_16x16x32_bf16 v[44:47], a[168:171], v[76:79], v[44:47]\n\t"
  "ds_read_b64_tr_b16 v[68:69], v110 offset:24576\n\t"
  "v_max_f32 v20, 0, v20\n\t"
  "v_mfma_f32_16x16x32_bf16 v[48:51], a[172:175], v[76:79], v[48:51]\n\t"
  "ds_read_b64_tr_b16 v[70:71], v110 offset:26624\n\t"
  "buffer_load_dwordx4 v[220:223], v1, %[srd], s45 offen offset:3072 sc1\n\t"
  "v_mfma_f32_16x16x32_bf16 v[52:55], a[176:179], v[76:79], v[52:55]\n\t"
  "ds_read_b64_tr_b16 v[72:73], v111 offset:24576\n\t"
  "v_max_f32 v21, 0, v21\n\t"
  "v_mfma_f32_16x16x32_bf16 v[56:59], a[168:171], v[80:83], v[56:59]\n\t"
  "ds_read_b64_tr_b16 v[74:75], v111 offset:26624\n\t"
  "v_mul_f32 v22, v22, v128\n\t"
  "v_mfma_f32_16x16x32_bf16 v[60:63], a[172:175], v[80:83], v[60:63]\n\t"
  "v_mul_f32 v23, v23, v128\n\t"
  "v_mul_f32 v20, v20, v22\n\t"
  "v_mfma_f32_16x16x32_bf16 v[64:67], a[176:179], v[80:83], v[64:67]\n\t"
  "s_waitcnt lgkmcnt(4)\n\t"
  "s_add_u32 s45, %[sW2], 20480\n\t"
  "v_mfma_f32_16x16x32_bf16 v[44:47], a[180:183], v[84:87], v[44:47]\n\t"
  "ds_read_b64_tr_b16 v[76:77], v110 offset:28672\n\t"
  "v_mul_f32 v21, v21, v23\n\t"
  "v_mfma_f32_16x16x32_bf16 v[48:51], a[184:187], v[84:87], v[48:51]\n\t"
  "ds_read_b64_tr_b16 v[78:79], v110 offset:30720\n\t"
  "v_cvt_pk_bf16_f32 v116, v20, v21\n\t"
  "v_mfma_f32_16x16x32_bf16 v[52:55], a[188:191], v[84:87], v[52:55]\n\t"
  "ds_read_b64_tr_b16 v[80:81], v111 offset:28672\n\t"
  "v_cvt_pk_bf16_f32 v112, v20, v21\n\t"
  "v_mfma_f32_16x16x32_bf16 v[56:59], a[180:183], v[88:91], v[56:59]\n\t"
  "ds_read_b64_tr_b16 v[82:83], v111 offset:30720\n\t"
  "v_max_f32 v24, 0, v24\n\t"
  "v_mfma_f32_16x16x32_bf16 v[60:63], a[184:187], v[88:91], v[60:63]\n\t"
  "buffer_load_dwordx4 v[224:227], v1, %[srd], s45 offen offset:0 sc1\n\t"
  "v_max_f32 v25, 0, v25\n\t"
  "v_mfma_f32_16x16x32_bf16 v[64:67], a[188:191], v[88:91], v[64:67]\n\t"
  "s_waitcnt lgkmcnt(4)\n\t"
  "v_mul_f32 v26, v26, v128\n\t"
  "v_mfma_f32_16x16x32_bf16 v[44:47], a[192:195], v[68:71], v[44:47]\n\t"
  "ds_read_b64_tr_b16 v[84:85], v110 offset:32768\n\t"
  "v_mul_f32 v27, v27, v128\n\t"
  "v_mfma_f32_16x16x32_bf16 v[48:51], a[196:199], v[68:71], v[48:51]\n\t"
  "ds_read_b64_tr_b16 v[86:87], v110 offset:34816\n\t"
  "v_mul_f32 v24, v24, v26\n\t"
  "v_mfma_f32_16x16x32_bf16 v[52:55], a[200:203], v[68:71], v[52:55]\n\t"
  "ds_read_b64_tr_b16 v[88:89], v111 offset:32768\n\t"
  "buffer_load_dwordx4 v[228:231], v1, %[srd], s45 offen offset:1024 sc1\n\t"
  "v_mfma_f32_16x16x32_bf16 v[56:59], a[192:195], v[72:75], v[56:59]\n\t"
  "ds_read_b64_tr_b16 v[90:91], v111 offset:34816\n\t"
  "v_mul_f32 v25, v25, v27\n\t"
  "v_mfma_f32_16x16x32_bf16 v[60:63], a[196:199], v[72:75], v[60:63]\n\t"
  "v_cvt_pk_bf16_f32 v117, v24, v25\n\t"
  "v_cvt_pk_bf16_f32 v113, v24, v25\n\t"
  "v_mfma_f32_16x16x32_bf16 v[64:67], a[200:203], v[72:75], v[64:67]\n\t"
  "s_waitcnt lgkmcnt(4)\n\t"
  "v_max_f32 v28, 0, v28\n\t"
  "v_mfma_f32_16x16x32_bf16 v[44:47], a[204:207], v[76:79], v[44:47]\n\t"
  "ds_read_b64_tr_b16 v[68:69], v110 offset:36864\n\t"
  "v_max_f32 v29, 0, v29\n\t"
  "v_mfma_f32_16x16x32_bf16 v[48:51], a[208:211], v[76:79], v[48:51]\n\t"
  "ds_read_b64_tr_b16 v[70:71], v110 offset:38912\n\t"
  "buffer_load_dwordx4 v[232:235], v1, %[srd], s45 offen offset:2048 sc1\n\t"
  "v_mfma_f32_16x16x32_bf16 v[52:55], a[212:215], v[76:79], v[52:55]\n\t"
  "ds_read_b64_tr_b16 v[72:73], v111 offset:36864\n\t"
  "v_mul_f32 v30, v30, v128\n\t"
  "v_mfma_f32_16x16x32_bf16 v[56:59], a[204:207], v[80:83], v[56:59]\n\t"
  "ds_read_b64_tr_b16 v[74:75], v111 offset:38912\n\t"
  "v_mul_f32 v31, v31, v128\n\t"
  "v_mfma_f32_16x16x32_bf16 v[60:63], a[208:211], v[80:83], v[60:63]\n\t"
  "v_mul_f32 v28, v28, v30\n\t"
  "v_permlane16_swap_b32 v116, v117\n\t"
  "v_mfma_f32_16x16x32_bf16 v[64:67], a[212:215], v[80:83], v[64:67]\n\t"
  "s_waitcnt lgkmcnt(4)\n\t"
  "buffer_load_dwordx4 v[236:239], v1, %[srd], s45 offen offset:3072 sc1\n\t"
  "v_mfma_f32_16x16x32_bf16 v[44:47], a[216:219], v[84:87], v[44:47]\n\t"
  "ds_read_b64_tr_b16 v[76:77], v108 offset:0\n\t"
  "v_mul_f32 v29, v29, v31\n\t"
  "v_mfma_f32_16x16x32_bf16 v[48:51], a[220:223], v[84:87], v[48:51]\n\t"
  "ds_read_b64_tr_b16 v[78:79], v108 offset:2048\n\t"
  "v_cvt_pk_bf16_f32 v118, v28, v29\n\t"
  "v_mfma_f32_16x16x32_bf16 v[52:55], a[224:227], v[84:87], v[52:55]\n\t"
  "ds_read_b64_tr_b16 v[80:81], v109 offset:0\n\t"
  "v_cvt_pk_bf16_f32 v114, v28, v29\n\t"
  "v_mfma_f32_16x16x32_bf16 v[56:59], a[216:219], v[88:91], v[56:59]\n\t"
  "ds_read_b64_tr_b16 v[82:83], v109 offset:2048\n\t"
  "s_mov_b32 s55, 0\n\t"
  "v_mfma_f32_16x16x32_bf16 v[60:63], a[220:223], v[88:91], v[60:63]\n\t"
  "s_add_u32 s45, %[sW2], 24576\n\t"
  "v_permlane16_swap_b32 v118, v115\n\t"
  "v_mfma_f32_16x16x32_bf16 v[64:67], a[224:227], v[88:91], v[64:67]\n\t"
  "s_waitcnt lgkmcnt(4)\n\t"
  "v_permlane32_swap_b32 v117, v115\n\t"
  "v_mfma_f32_16x16x32_bf16 v[44:47], a[228:231], v[68:71], v[44:47]\n\t"
  "ds_read_b64_tr_b16 v[84:85], v108 offset:4096\n\t"
  "buffer_store_dwordx4 v[112:115], v8, %[srd], s55 offen sc1\n\t"
  "v_mfma_f32_16x16x32_bf16 v[48:51], a[232:235], v[68:71], v[48:51]\n\t"
  "ds_read_b64_tr_b16 v[86:87], v108 offset:6144\n\t"
  "v_max_f32 v32, 0, v32\n\t"
  "v_mfma_f32_16x16x32_bf16 v[52:55], a[236:239], v[68:71], v[52:55]\n\t"
  "ds_read_b64_tr_b16 v[88:89], v109 offset:4096\n\t"
  "buffer_load_dwordx4 v[240:243], v1, %[srd], s45 offen offset:0 sc1\n\t"
  "v_mfma_f32_16x16x32_bf16 v[56:59], a[228:231], v[72:75], v[56:59]\n\t"
  "ds_read_b64_tr_b16 v[90:91], v109 offset:6144\n\t"
  "v_max_f32 v33, 0, v33\n\t"
  "v_mfma_f32_16x16x32_bf16 v[60:63], a[232:235], v[72:75], v[60:63]\n\t"
  "v_mul_f32 v34, v34, v129\n\t"
  "v_mul_f32 v35, v35, v129\n\t"
  "v_mfma_f32_16x16x32_bf16 v[64:67], a[236:239], v[72:75], v[64:67]\n\t"
  "v_mul_f32 v32, v32, v34\n\t"
  "v_mul_f32 v33, v33, v35\n\t"
  "v_mfma_f32_16x16x16_bf16 v[44:47], a[240:241], v[96:97], v[44:47]\n\t"
  "v_cvt_pk_bf16_f32 v124, v32, v33\n\t"
  "v_cvt_pk_bf16_f32 v120, v32, v33\n\t"
  "v_mfma_f32_16x16x16_bf16 v[48:51], a[242:243], v[96:97], v[48:51]\n\t"
  "buffer_load_dwordx4 v[244:247], v1, %[srd], s45 offen offset:1024 sc1\n\t"
  "v_max_f32 v36, 0, v36\n\t"
  "v_mfma_f32_16x16x16_bf16 v[52:55], a[244:245], v[96:97], v[52:55]\n\t"
  "v_max_f32 v37, 0, v37\n\t"
  "v_mul_f32 v38, v38, v129\n\t"
  "v_mfma_f32_16x16x16_bf16 v[56:59], a[240:241], v[98:99], v[56:59]\n\t"
  "v_mul_f32 v39, v39, v129\n\t"
  "v_mul_f32 v36, v36, v38\n\t"
  "v_mfma_f32_16x16x16_bf16 v[60:63], a[242:243], v[98:99], v[60:63]\n\t"
  "v_mul_f32 v37, v37, v39\n\t"
  "v_cvt_pk_bf16_f32 v125, v36, v37\n\t"
  "v_cvt_pk_bf16_f32 v121, v36, v37\n\t"
  "v_max_f32 v40, 0, v40\n\t"
  "v_max_f32 v41, 0, v41\n\t"
  "v_mul_f32 v42, v42, v129\n\t"
  "v_mul_f32 v43, v43, v129\n\t"
  "v_mul_f32 v40, v40, v42\n\t"
  "v_permlane16_swap_b32 v124, v125\n\t"
  "v_mul_f32 v41, v41, v43\n\t"
  "v_cvt_pk_bf16_f32 v126, v40, v41\n\t"
  "v_cvt_pk_bf16_f32 v122, v40, v41\n\t"
  "s_mov_b32 s55, 49152\n\t"
  "v_permlane16_swap_b32 v126, v123\n\t"
  "s_nop 1\n\t"
  "v_permlane32_swap_b32 v125, v123\n\t"
  "buffer_store_dwordx4 v[120:123], v8, %[srd], s55 offen sc1\n\t"
  "v_mfma_f32_16x16x16_bf16 v[64:67], a[244:245], v[98:99], v[64:67]\n\t"
  "s_waitcnt lgkmcnt(4)\n\t"
  "v_mfma_f32_16x16x32_bf16 v[20:23], a[0:3], v[76:79], 0\n\t"
  "ds_read_b64_tr_b16 v[68:69], v108 offset:8192\n\t"
  "v_add_u32 v110, 122880, v5\n\t"
  "v_mfma_f32_16x16x32_bf16 v[24:27], a[4:7], v[76:79], 0\n\t"
  "ds_read_b64_tr_b16 v[70:71], v108 offset:10240\n\t"
  "v_xor_b32 v111, 32, v5\n\t"
  "v_mfma_f32_16x16x32_bf16 v[28:31], a[8:11], v[76:79], 0\n\t"
  "ds_read_b64_tr_b16 v[72:73], v109 offset:8192\n\t"
  "v_add_u32 v111, 122880, v111\n\t"
  "v_mfma_f32_16x16x32_bf16 v[32:35], a[0:3], v[80:83], 0\n\t"
  "ds_read_b64_tr_b16 v[74:75], v109 offset:10240\n\t"
  "v_and_b32 v128, 0xffff0000, v97\n\t"
  "v_mfma_f32_16x16x32_bf16 v[36:39], a[4:7], v[80:83], 0\n\t"
  "v_and_b32 v129, 0xffff0000, v99\n\t"
  "v_rcp_f32 v128, v128\n\t"
  "v_mfma_f32_16x16x32_bf16 v[40:43], a[8:11], v[80:83], 0\n\t"
  "s_waitcnt lgkmcnt(4)\n\t"
  "v_rcp_f32 v129, v129\n\t"
  "v_mfma_f32_16x16x32_bf16 v[20:23], a[12:15], v[84:87], v[20:23]\n\t"
  "ds_read_b64_tr_b16 v[76:77], v108 offset:12288\n\t"
  "s_barrier\n\t"
  "v_mfma_f32_16x16x32_bf16 v[24:27], a[16:19], v[84:87], v[24:27]\n\t"
  "ds_read_b64_tr_b16 v[78:79], v108 offset:14336\n\t"
  "v_mul_f32 v128, v128, v128\n\t"
  "v_mfma_f32_16x16x32_bf16 v[28:31], a[20:23], v[84:87], v[28:31]\n\t"
  "ds_read_b64_tr_b16 v[80:81], v109 offset:12288\n\t"
  "v_mul_f32 v129, v129, v129\n\t"
  "v_mfma_f32_16x16x32_bf16 v[32:35], a[12:15], v[88:91], v[32:35]\n\t"
  "ds_read_b64_tr_b16 v[82:83], v109 offset:14336\n\t"
  "s_mul_i32 s52, %[w], 20480\n\t"
  "v_mfma_f32_16x16x32_bf16 v[36:39], a[16:19], v[88:91], v[36:39]\n\t"
  "v_max_f32 v44, 0, v44\n\t"
  "v_max_f32 v45, 0, v45\n\t"
  "v_mfma_f32_16x16x32_bf16 v[40:43], a[20:23], v[88:91], v[40:43]\n\t"
  "s_waitcnt lgkmcnt(4)\n\t"
  "v_mul_f32 v46, v46, v128\n\t"
  "v_mfma_f32_16x16x32_bf16 v[20:23], a[24:27], v[68:71], v[20:23]\n\t"
  "ds_read_b64_tr_b16 v[84:85], v108 offset:16384\n\t"
  "s_add_u32 m0, s52, 0\n\t"
  "v_mfma_f32_16x16x32_bf16 v[24:27], a[28:31], v[68:71], v[24:27]\n\t"
  "ds_read_b64_tr_b16 v[86:87], v108 offset:18432\n\t"
  "v_mul_f32 v47, v47, v128\n\t"
  "v_mfma_f32_16x16x32_bf16 v[28:31], a[32:35], v[68:71], v[28:31]\n\t"
  "ds_read_b64_tr_b16 v[88:89], v109 offset:16384\n\t"
  "v_mul_f32 v44, v44, v46\n\t"
  "v_mfma_f32_16x16x32_bf16 v[32:35], a[24:27], v[72:75], v[32:35]\n\t"
  "ds_read_b64_tr_b16 v[90:91], v109 offset:18432\n\t"
  "v_mul_f32 v45, v45, v47\n\t"
  "v_mfma_f32_16x16x32_bf16 v[36:39], a[28:31], v[72:75], v[36:39]\n\t"
  "s_add_u32 s46, %[sW2], 26624\n\t"
  "v_cvt_pk_bf16_f32 v116, v44, v45\n\t"
  "v_mfma_f32_16x16x32_bf16 v[40:43], a[32:35], v[72:75], v[40:43]\n\t"
  "s_waitcnt lgkmcnt(4)\n\t"
  "v_cvt_pk_bf16_f32 v112, v44, v45\n\t"
  "v_mfma_f32_16x16x32_bf16 v[20:23], a[36:39], v[76:79], v[20:23]\n\t"
  "ds_read_b64_tr_b16 v[68:69], v108 offset:20480\n\t"
  "v_max_f32 v48, 0, v48\n\t"
  "v_mfma_f32_16x16x32_bf16 v[24:27], a[40:43], v[76:79], v[24:27]\n\t"
  "ds_read_b64_tr_b16 v[70:71], v108 offset:22528\n\t"
  "buffer_load_dwordx4 v1, %[srd], s46 offen offset:0 sc1 lds\n\t"
  "v_mfma_f32_16x16x32_bf16 v[28:31], a[44:47], v[76:79], v[28:31]\n\t"
  "ds_read_b64_tr_b16 v[72:73], v109 offset:20480\n\t"
  "v_max_f32 v49, 0, v49\n\t"
  "v_mfma_f32_16x16x32_bf16 v[32:35], a[36:39], v[80:83], v[32:35]\n\t"
  "ds_read_b64_tr_b16 v[74:75], v109 offset:22528\n\t"
  "v_mul_f32 v50, v50, v128\n\t"
  "v_mfma_f32_16x16x32_bf16 v[36:39], a[40:43], v[80:83], v[36:39]\n\t"
  "buffer_load_dwordx4 v1, %[srd], s46 offen offset:1024 sc1 lds\n\t"
  "v_mul_f32 v51, v51, v128\n\t"
  "v_mfma_f32_16x16x32_bf16 v[40:43], a[44:47], v[80:83], v[40:43]\n\t"
  "s_waitcnt lgkmcnt(4)\n\t"
  "v_mul_f32 v48, v48, v50\n\t"
  "v_mfma_f32_16x16x32_bf16 v[20:23], a[48:51], v[84:87], v[20:23]\n\t"
  "ds_read_b64_tr_b16 v[76:77], v108 offset:24576\n\t"
  "v_mul_f32 v49, v49, v51\n\t"
  "v_mfma_f32_16x16x32_bf16 v[24:27], a[52:55], v[84:87], v[24:27]\n\t"
  "ds_read_b64_tr_b16 v[78:79], v108 offset:26624\n\t"
  "v_cvt_pk_bf16_f32 v117, v48, v49\n\t"
  "v_mfma_f32_16x16x32_bf16 v[28:31], a[56:59], v[84:87], v[28:31]\n\t"
  "ds_read_b64_tr_b16 v[80:81], v109 offset:24576\n\t"
  "buffer_load_dwordx4 v1, %[srd], s46 offen offset:2048 sc1 lds\n\t"
  "v_mfma_f32_16x16x32_bf16 v[32:35], a[48:51], v[88:91], v[32:35]\n\t"
  "ds_read_b64_tr_b16 v[82:83], v109 offset:26624\n\t"
  "v_cvt_pk_bf16_f32 v113, v48, v49\n\t"
  "v_mfma_f32_16x16x32_bf16 v[36:39], a[52:55], v[88:91], v[36:39]\n\t"
  "v_max_f32 v52, 0, v52\n\t"
  "v_max_f32 v53, 0, v53\n\t"
  "v_mfma_f32_16x16x32_bf16 v[40:43], a[56:59], v[88:91], v[40:43]\n\t"
  "s_waitcnt lgkmcnt(4)\n\t"
  "buffer_load_dwordx4 v1, %[srd], s46 offen offset:3072 sc1 lds\n\t"
  "v_mfma_f32_16x16x32_bf16 v[20:23], a[60:63], v[68:71], v[20:23]\n\t"
  "ds_read_b64_tr_b16 v[84:85], v108 offset:28672\n\t"
  "v_mul_f32 v54, v54, v128\n\t"
  "v_mfma_f32_16x16x32_bf16 v[24:27], a[64:67], v[68:71], v[24:27]\n\t"
  "ds_read_b64_tr_b16 v[86:87], v108 offset:30720\n\t"
  "v_mul_f32 v55, v55, v128\n\t"
  "v_mfma_f32_16x16x32_bf16 v[28:31], a[68:71], v[68:71], v[28:31]\n\t"
  "ds_read_b64_tr_b16 v[88:89], v109 offset:28672\n\t"
  "v_mul_f32 v52, v52, v54\n\t"
  "v_mfma_f32_16x16x32_bf16 v[32:35], a[60:63], v[72:75], v[32:35]\n\t"
  "ds_read_b64_tr_b16 v[90:91], v109 offset:30720\n\t"
  "s_add_u32 m0, s52, 4096\n\t"
  "v_mfma_f32_16x16x32_bf16 v[36:39], a[64:67], v[72:75], v[36:39]\n\t"
  "v_permlane16_swap_b32 v116, v117\n\t"
  "v_mul_f32 v53, v53, v55\n\t"
  "v_mfma_f32_16x16x32_bf16 v[40:43], a[68:71], v[72:75], v[40:43]\n\t"
  "s_waitcnt lgkmcnt(4)\n\t"
  "v_cvt_pk_bf16_f32 v118, v52, v53\n\t"
  "v_mfma_f32_16x16x32_bf16 v[20:23], a[72:75], v[76:79], v[20:23]\n\t"
  "ds_read_b64_tr_b16 v[68:69], v108 offset:32768\n\t"
  "s_add_u32 s46, %[sW2], 30720\n\t"
  "v_mfma_f32_16x16x32_bf16 v[24:27], a[76:79], v[76:79], v[24:27]\n\t"
  "ds_read_b64_tr_b16 v[70:71], v108 offset:34816\n\t"
  "v_cvt_pk_bf16_f32 v114, v52, v53\n\t"
  "v_mfma_f32_16x16x32_bf16 v[28:31], a[80:83], v[76:79], v[28:31]\n\t"
  "ds_read_b64_tr_b16 v[72:73], v109 offset:32768\n\t"
  "s_mov_b32 s55, 98304\n\t"
  "v_mfma_f32_16x16x32_bf16 v[32:35], a[72:75], v[80:83], v[32:35]\n\t"
  "ds_read_b64_tr_b16 v[74:75], v109 offset:34816\n\t"
  "buffer_load_dwordx4 v1, %[srd], s46 offen offset:0 sc1 lds\n\t"
  "v_mfma_f32_16x16x32_bf16 v[36:39], a[76:79], v[80:83], v[36:39]\n\t"
  "v_permlane16_swap_b32 v118, v115\n\t"
  "v_mfma_f32_16x16x32_bf16 v[40:43], a[80:83], v[80:83], v[40:43]\n\t"
  "s_waitcnt lgkmcnt(4)\n\t"
  "v_permlane32_swap_b32 v117, v115\n\t"
  "v_mfma_f32_16x16x32_bf16 v[20:23], a[84:87], v[84:87], v[20:23]\n\t"
  "ds_read_b64_tr_b16 v[76:77], v108 offset:36864\n\t"
  "buffer_store_dwordx4 v[112:115], v8, %[srd], s55 offen sc1\n\t"
  "v_mfma_f32_16x16x32_bf16 v[24:27], a[88:91], v[84:87], v[24:27]\n\t"
  "ds_read_b64_tr_b16 v[78:79], v108 offset:38912\n\t"
  "buffer_load_dwordx4 v1, %[srd], s46 offen offset:1024 sc1 lds\n\t"
  "v_mfma_f32_16x16x32_bf16 v[28:31], a[92:95], v[84:87], v[28:31]\n\t"
  "ds_read_b64_tr_b16 v[80:81], v109 offset:36864\n\t"
  "v_max_f32 v56, 0, v56\n\t"
  "v_mfma_f32_16x16x32_bf16 v[32:35], a[84:87], v[88:91], v[32:35]\n\t"
  "ds_read_b64_tr_b16 v[82:83], v109 offset:38912\n\t"
  "v_max_f32 v57, 0, v57\n\t"
  "v_mfma_f32_16x16x32_bf16 v[36:39], a[88:91], v[88:91], v[36:39]\n\t"
  "buffer_load_dwordx4 v1, %[srd], s46 offen offset:2048 sc1 lds\n\t"
  "v_mul_f32 v58, v58, v129\n\t"
  "v_mfma_f32_16x16x32_bf16 v[40:43], a[92:95], v[88:91], v[40:43]\n\t"
  "s_waitcnt lgkmcnt(4)\n\t"
  "v_mul_f32 v59, v59, v129\n\t"
  "v_mfma_f32_16x16x32_bf16 v[20:23], a[96:99], v[68:71], v[20:23]\n\t"
  "ds_read_b64_tr_b16 v[84:85], v110 offset:0\n\t"
  "v_mul_f32 v56, v56, v58\n\t"
  "v_mfma_f32_16x16x32_bf16 v[24:27], a[100:103], v[68:71], v[24:27]\n\t"
  "ds_read_b64_tr_b16 v[86:87], v110 offset:2048\n\t"
  "buffer_load_dwordx4 v1, %[srd], s46 offen offset:3072 sc1 lds\n\t"
  "v_mfma_f32_16x16x32_bf16 v[28:31], a[104:107], v[68:71], v[28:31]\n\t"
  "ds_read_b64_tr_b16 v[88:89], v111 offset:0\n\t"
  "v_mul_f32 v57, v57, v59\n\t"
  "v_mfma_f32_16x16x32_bf16 v[32:35], a[96:99], v[72:75], v[32:35]\n\t"
  "ds_read_b64_tr_b16 v[90:91], v111 offset:2048\n\t"
  "v_cvt_pk_bf16_f32 v124, v56, v57\n\t"
  "v_mfma_f32_16x16x32_bf16 v[36:39], a[100:103], v[72:75], v[36:39]\n\t"
  "buffer_load_dwordx2 v[100:101], v136, %[srd], s54 offen offset:0 sc1\n\t"
  "buffer_load_dwordx2 v[102:103], v136, %[srd], s54 offen offset:1024 sc1\n\t"
  "v_mfma_f32_16x16x32_bf16 v[40:43], a[104:107], v[72:75], v[40:43]\n\t"
  "s_waitcnt lgkmcnt(4)\n\t"
  "buffer_load_dwordx2 v[104:105], v136, %[srd], s54 offen offset:2048 sc1\n\t"
  "v_mfma_f32_16x16x32_bf16 v[20:23], a[108:111], v[76:79], v[20:23]\n\t"
  "ds_read_b64_tr_b16 v[68:69], v110 offset:4096\n\t"
  "s_movk_i32 s56, 0\n\t"
  "v_mfma_f32_16x16x32_bf16 v[24:27], a[112:115], v[76:79], v[24:27]\n\t"
  "ds_read_b64_tr_b16 v[70:71], v110 offset:6144\n\t"
  "s_waitcnt vmcnt(12)\n\t"
  "v_mfma_f32_16x16x32_bf16 v[28:31], a[116:119], v[76:79], v[28:31]\n\t"
  "ds_read_b64_tr_b16 v[72:73], v111 offset:4096\n\t"
  "buffer_load_dwordx2 v[106:107], v136, %[srd], s54 offen offset:3072 sc1\n\t"
  "v_mfma_f32_16x16x32_bf16 v[32:35], a[108:111], v[80:83], v[32:35]\n\t"
  "ds_read_b64_tr_b16 v[74:75], v111 offset:6144\n\t"
  "buffer_store_dwordx4 v[132:135], v9, %[srd], s56 offen sc1\n\t"
  "v_mfma_f32_16x16x32_bf16 v[36:39], a[112:115], v[80:83], v[36:39]\n\t"
  "s_add_u32 m0, s52, 8192\n\t"
  "s_add_u32 s46, %[sW2], 34816\n\t"
  "v_mfma_f32_16x16x32_bf16 v[40:43], a[116:119], v[80:83], v[40:43]\n\t"
  "s_waitcnt lgkmcnt(4)\n\t"
  "v_cvt_pk_bf16_f32 v120, v56, v57\n\t"
  "v_mfma_f32_16x16x32_bf16 v[20:23], a[120:123], v[84:87], v[20:23]\n\t"
  "ds_read_b64_tr_b16 v[76:77], v110 offset:8192\n\t"
  "v_xor_b32 v108, 64, v5\n\t"
  "v_mfma_f32_16x16x32_bf16 v[24:27], a[124:127], v[84:87], v[24:27]\n\t"
  "ds_read_b64_tr_b16 v[78:79], v110 offset:10240\n\t"
  "v_add_u32 v108, 81920, v108\n\t"
  "v_mfma_f32_16x16x32_bf16 v[28:31], a[128:131], v[84:87], v[28:31]\n\t"
  "ds_read_b64_tr_b16 v[80:81], v111 offset:8192\n\t"
  "v_xor_b32 v109, 96, v5\n\t"
  "v_mfma_f32_16x16x32_bf16 v[32:35], a[120:123], v[88:91], v[32:35]\n\t"
  "ds_read_b64_tr_b16 v[82:83], v111 offset:10240\n\t"
  "v_add_u32 v109, 81920, v109\n\t"
  "v_mfma_f32_16x16x32_bf16 v[36:39], a[124:127], v[88:91], v[36:39]\n\t"
  "buffer_load_dwordx4 v1, %[srd], s46 offen offset:0 sc1 lds\n\t"
  "buffer_load_dwordx4 v1, %[srd], s46 offen offset:1024 sc1 lds\n\t"
  "v_mfma_f32_16x16x32_bf16 v[40:43], a[128:131], v[88:91], v[40:43]\n\t"
  "s_waitcnt lgkmcnt(4)\n\t"
  "v_max_f32 v60, 0, v60\n\t"
  "v_mfma_f32_16x16x32_bf16 v[20:23], a[132:135], v[68:71], v[20:23]\n\t"
  "ds_read_b64_tr_b16 v[84:85], v110 offset:12288\n\t"
  "buffer_load_dwordx4 v1, %[srd], s46 offen offset:2048 sc1 lds\n\t"
  "v_mfma_f32_16x16x32_bf16 v[24:27], a[136:139], v[68:71], v[24:27]\n\t"
  "ds_read_b64_tr_b16 v[86:87], v110 offset:14336\n\t"
  "v_max_f32 v61, 0, v61\n\t"
  "v_mfma_f32_16x16x32_bf16 v[28:31], a[140:143], v[68:71], v[28:31]\n\t"
  "ds_read_b64_tr_b16 v[88:89], v111 offset:12288\n\t"
  "v_mul_f32 v62, v62, v129\n\t"
  "v_mfma_f32_16x16x32_bf16 v[32:35], a[132:135], v[72:75], v[32:35]\n\t"
  "ds_read_b64_tr_b16 v[90:91], v111 offset:14336\n\t"
  "v_mul_f32 v63, v63, v129\n\t"
  "v_mfma_f32_16x16x32_bf16 v[36:39], a[136:139], v[72:75], v[36:39]\n\t"
  "buffer_load_dwordx4 v1, %[srd], s46 offen offset:3072 sc1 lds\n\t"
  "v_mul_f32 v60, v60, v62\n\t"
  "v_mfma_f32_16x16x32_bf16 v[40:43], a[140:143], v[72:75], v[40:43]\n\t"
  "s_waitcnt lgkmcnt(4)\n\t"
  "v_mul_f32 v61, v61, v63\n\t"
  "v_mfma_f32_16x16x32_bf16 v[20:23], a[144:147], v[76:79], v[20:23]\n\t"
  "ds_read_b64_tr_b16 v[68:69], v110 offset:16384\n\t"
  "v_cvt_pk_bf16_f32 v125, v60, v61\n\t"
  "v_mfma_f32_16x16x32_bf16 v[24:27], a[148:151], v[76:79], v[24:27]\n\t"
  "ds_read_b64_tr_b16 v[70:71], v110 offset:18432\n\t"
  "s_add_u32 m0, s52, 12288\n\t"
  "v_mfma_f32_16x16x32_bf16 v[28:31], a[152:155], v[76:79], v[28:31]\n\t"
  "ds_read_b64_tr_b16 v[72:73], v111 offset:16384\n\t"
  "v_cvt_pk_bf16_f32 v121, v60, v61\n\t"
  "v_mfma_f32_16x16x32_bf16 v[32:35], a[144:147], v[80:83], v[32:35]\n\t"
  "ds_read_b64_tr_b16 v[74:75], v111 offset:18432\n\t"
  "v_max_f32 v64, 0, v64\n\t"
  "v_mfma_f32_16x16x32_bf16 v[36:39], a[148:151], v[80:83], v[36:39]\n\t"
  "s_add_u32 s46, %[sW2], 38912\n\t"
  "v_max_f32 v65, 0, v65\n\t"
  "v_mfma_f32_16x16x32_bf16 v[40:43], a[152:155], v[80:83], v[40:43]\n\t"
  "s_waitcnt lgkmcnt(4)\n\t"
  "v_mul_f32 v66, v66, v129\n\t"
  "v_mfma_f32_16x16x32_bf16 v[20:23], a[156:159], v[84:87], v[20:23]\n\t"
  "ds_read_b64_tr_b16 v[76:77], v110 offset:20480\n\t"
  "v_mul_f32 v67, v67, v129\n\t"
  "v_mfma_f32_16x16x32_bf16 v[24:27], a[160:163], v[84:87], v[24:27]\n\t"
  "ds_read_b64_tr_b16 v[78:79], v110 offset:22528\n\t"
  "v_mul_f32 v64, v64, v66\n\t"
  "v_mfma_f32_16x16x32_bf16 v[28:31], a[164:167], v[84:87], v[28:31]\n\t"
  "ds_read_b64_tr_b16 v[80:81], v111 offset:20480\n\t"
  "buffer_load_dwordx4 v1, %[srd], s46 offen offset:0 sc1 lds\n\t"
  "v_mfma_f32_16x16x32_bf16 v[32:35], a[156:159], v[88:91], v[32:35]\n\t"
  "ds_read_b64_tr_b16 v[82:83], v111 offset:22528\n\t"
  "v_permlane16_swap_b32 v124, v125\n\t"
  "v_mfma_f32_16x16x32_bf16 v[36:39], a[160:163], v[88:91], v[36:39]\n\t"
  "v_mul_f32 v65, v65, v67\n\t"
  "v_cvt_pk_bf16_f32 v126, v64, v65\n\t"
  "v_mfma_f32_16x16x32_bf16 v[40:43], a[164:167], v[88:91], v[40:43]\n\t"
  "s_waitcnt lgkmcnt(4)\n\t"
  "buffer_load_dwordx4 v1, %[srd], s46 offen offset:1024 sc1 lds\n\t"
  "v_mfma_f32_16x16x32_bf16 v[20:23], a[168:171], v[68:71], v[20:23]\n\t"
  "ds_read_b64_tr_b16 v[84:85], v110 offset:24576\n\t"
  "v_cvt_pk_bf16_f32 v122, v64, v65\n\t"
  "v_mfma_f32_16x16x32_bf16 v[24:27], a[172:175], v[68:71], v[24:27]\n\t"
  "ds_read_b64_tr_b16 v[86:87], v110 offset:26624\n\t"
  "s_mov_b32 s55, 147456\n\t"
  "v_mfma_f32_16x16x32_bf16 v[28:31], a[176:179], v[68:71], v[28:31]\n\t"
  "ds_read_b64_tr_b16 v[88:89], v111 offset:24576\n\t"
  "v_permlane16_swap_b32 v126, v123\n\t"
  "v_mfma_f32_16x16x32_bf16 v[32:35], a[168:171], v[72:75], v[32:35]\n\t"
  "ds_read_b64_tr_b16 v[90:91], v111 offset:26624\n\t"
  "buffer_load_dwordx4 v1, %[srd], s46 offen offset:2048 sc1 lds\n\t"
  "v_mfma_f32_16x16x32_bf16 v[36:39], a[172:175], v[72:75], v[36:39]\n\t"
  "v_permlane32_swap_b32 v125, v123\n\t"
  "buffer_store_dwordx4 v[120:123], v8, %[srd], s55 offen sc1\n\t"
  "v_mfma_f32_16x16x32_bf16 v[40:43], a[176:179], v[72:75], v[40:43]\n\t"
  "s_waitcnt lgkmcnt(4)\n\t"
  "v_mfma_f32_16x16x32_bf16 v[20:23], a[180:183], v[76:79], v[20:23]\n\t"
  "ds_read_b64_tr_b16 v[68:69], v110 offset:28672\n\t"
  "buffer_load_dwordx4 v1, %[srd], s46 offen offset:3072 sc1 lds\n\t"
  "v_mfma_f32_16x16x32_bf16 v[24:27], a[184:187], v[76:79], v[24:27]\n\t"
  "ds_read_b64_tr_b16 v[70:71], v110 offset:30720\n\t"
  "v_mfma_f32_16x16x32_bf16 v[28:31], a[188:191], v[76:79], v[28:31]\n\t"
  "ds_read_b64_tr_b16 v[72:73], v111 offset:28672\n\t"
  "v_mfma_f32_16x16x32_bf16 v[32:35], a[180:183], v[80:83], v[32:35]\n\t"
  "ds_read_b64_tr_b16 v[74:75], v111 offset:30720\n\t"
  "s_add_u32 m0, s52, 16384\n\t"
  "v_mfma_f32_16x16x32_bf16 v[36:39], a[184:187], v[80:83], v[36:39]\n\t"
  "v_mfma_f32_16x16x32_bf16 v[40:43], a[188:191], v[80:83], v[40:43]\n\t"
  "s_waitcnt lgkmcnt(4)\n\t"
  "v_mfma_f32_16x16x32_bf16 v[20:23], a[192:195], v[84:87], v[20:23]\n\t"
  "ds_read_b64_tr_b16 v[76:77], v110 offset:32768\n\t"
  "v_mfma_f32_16x16x32_bf16 v[24:27], a[196:199], v[84:87], v[24:27]\n\t"
  "ds_read_b64_tr_b16 v[78:79], v110 offset:34816\n\t"
  "s_add_u32 s46, %[sW2], 43008\n\t"
  "v_mfma_f32_16x16x32_bf16 v[28:31], a[200:203], v[84:87], v[28:31]\n\t"
  "ds_read_b64_tr_b16 v[80:81], v111 offset:32768\n\t"
  "v_mfma_f32_16x16x32_bf16 v[32:35], a[192:195], v[88:91], v[32:35]\n\t"
  "ds_read_b64_tr_b16 v[82:83], v111 offset:34816\n\t"
  "v_mfma_f32_16x16x32_bf16 v[36:39], a[196:199], v[88:91], v[36:39]\n\t"
  "buffer_load_dwordx4 v1, %[srd], s46 offen offset:0 sc1 lds\n\t"
  "v_mfma_f32_16x16x32_bf16 v[40:43], a[200:203], v[88:91], v[40:43]\n\t"
  "s_waitcnt lgkmcnt(4)\n\t"
  "v_mfma_f32_16x16x32_bf16 v[20:23], a[204:207], v[68:71], v[20:23]\n\t"
  "ds_read_b64_tr_b16 v[84:85], v110 offset:36864\n\t"
  "v_mfma_f32_16x16x32_bf16 v[24:27], a[208:211], v[68:71], v[24:27]\n\t"
  "ds_read_b64_tr_b16 v[86:87], v110 offset:38912\n\t"
  "buffer_load_dwordx4 v1, %[srd], s46 offen offset:1024 sc1 lds\n\t"
  "v_mfma_f32_16x16x32_bf16 v[28:31], a[212:215], v[68:71], v[28:31]\n\t"
  "ds_read_b64_tr_b16 v[88:89], v111 offset:36864\n\t"
  "v_mfma_f32_16x16x32_bf16 v[32:35], a[204:207], v[72:75], v[32:35]\n\t"
  "ds_read_b64_tr_b16 v[90:91], v111 offset:38912\n\t"
  "v_mfma_f32_16x16x32_bf16 v[36:39], a[208:211], v[72:75], v[36:39]\n\t"
  "s_waitcnt vmcnt(14)\n\t"
  "v_min_u32 v131, v101, v103\n\t"
  "v_mfma_f32_16x16x32_bf16 v[40:43], a[212:215], v[72:75], v[40:43]\n\t"
  "s_waitcnt lgkmcnt(4)\n\t"
  "s_waitcnt vmcnt(12)\n\t"
  "v_min3_u32 v131, v131, v105, v107\n\t"
  "v_mfma_f32_16x16x32_bf16 v[20:23], a[216:219], v[76:79], v[20:23]\n\t"
  "ds_read_b64_tr_b16 v[68:69], v108 offset:0\n\t"
  "v_cmp_eq_u32 vcc, 0, v131\n\t"
  "v_mfma_f32_16x16x32_bf16 v[24:27], a[220:223], v[76:79], v[24:27]\n\t"
  "ds_read_b64_tr_b16 v[70:71], v108 offset:2048\n\t"
  "buffer_load_dwordx4 v1, %[srd], s46 offen offset:2048 sc1 lds\n\t"
  "v_mfma_f32_16x16x32_bf16 v[28:31], a[224:227], v[76:79], v[28:31]\n\t"
  "ds_read_b64_tr_b16 v[72:73], v109 offset:0\n\t"
  "s_cbranch_vccnz Lst1_slow%=\n\t"
  "Lst1_back%=:\n\t"
  "v_mfma_f32_16x16x32_bf16 v[32:35], a[216:219], v[80:83], v[32:35]\n\t"
  "ds_read_b64_tr_b16 v[74:75], v109 offset:2048\n\t"
  "buffer_load_dwordx4 v1, %[srd], s46 offen offset:3072 sc1 lds\n\t"
  "v_mfma_f32_16x16x32_bf16 v[36:39], a[220:223], v[80:83], v[36:39]\n\t"
  "v_mfma_f32_16x16x32_bf16 v[40:43], a[224:227], v[80:83], v[40:43]\n\t"
  "s_waitcnt lgkmcnt(4)\n\t"
  "v_mfma_f32_16x16x32_bf16 v[20:23], a[228:231], v[84:87], v[20:23]\n\t"
  "ds_read_b64_tr_b16 v[76:77], v108 offset:4096\n\t"
  "v_mfma_f32_16x16x32_bf16 v[24:27], a[232:235], v[84:87], v[24:27]\n\t"
  "ds_read_b64_tr_b16 v[78:79], v108 offset:6144\n\t"
  "v_mfma_f32_16x16x32_bf16 v[28:31], a[236:239], v[84:87], v[28:31]\n\t"
  "ds_read_b64_tr_b16 v[80:81], v109 offset:4096\n\t"
  "v_mfma_f32_16x16x32_bf16 v[32:35], a[228:231], v[88:91], v[32:35]\n\t"
  "ds_read_b64_tr_b16 v[82:83], v109 offset:6144\n\t"
  "v_mfma_f32_16x16x32_bf16 v[36:39], a[232:235], v[88:91], v[36:39]\n\t"
  "v_mfma_f32_16x16x32_bf16 v[40:43], a[236:239], v[88:91], v[40:43]\n\t"
  "v_mfma_f32_16x16x16_bf16 v[20:23], a[240:241], v[100:101], v[20:23]\n\t"
  "v_mfma_f32_16x16x16_bf16 v[24:27], a[242:243], v[100:101], v[24:27]\n\t"
  "v_mfma_f32_16x16x16_bf16 v[28:31], a[244:245], v[100:101], v[28:31]\n\t"
  "v_mfma_f32_16x16x16_bf16 v[32:35], a[240:241], v[102:103], v[32:35]\n\t"
  "v_mfma_f32_16x16x16_bf16 v[36:39], a[242:243], v[102:103], v[36:39]\n\t"
  "v_mfma_f32_16x16x16_bf16 v[40:43], a[244:245], v[102:103], v[40:43]\n\t"
  "s_waitcnt lgkmcnt(4)\n\t"
  "v_mfma_f32_16x16x32_bf16 v[44:47], a[0:3], v[68:71], 0\n\t"
  "ds_read_b64_tr_b16 v[84:85], v108 offset:8192\n\t"
  "v_xor_b32 v110, 64, v5\n\t"
  "v_mfma_f32_16x16x32_bf16 v[48:51], a[4:7], v[68:71], 0\n\t"
  "ds_read_b64_tr_b16 v[86:87], v108 offset:10240\n\t"
  "v_add_u32 v110, 122880, v110\n\t"
  "v_mfma_f32_16x16x32_bf16 v[52:55], a[8:11], v[68:71], 0\n\t"
  "ds_read_b64_tr_b16 v[88:89], v109 offset:8192\n\t"
  "v_xor_b32 v111, 96, v5\n\t"
  "v_mfma_f32_16x16x32_bf16 v[56:59], a[0:3], v[72:75], 0\n\t"
  "ds_read_b64_tr_b16 v[90:91], v109 offset:10240\n\t"
  "v_add_u32 v111, 122880, v111\n\t"
  "v_mfma_f32_16x16x32_bf16 v[60:63], a[4:7], v[72:75], 0\n\t"
  "v_and_b32 v128, 0xffff0000, v101\n\t"
  "v_and_b32 v129, 0xffff0000, v103\n\t"
  "v_mfma_f32_16x16x32_bf16 v[64:67], a[8:11], v[72:75], 0\n\t"
  "s_waitcnt lgkmcnt(4)\n\t"
  "v_rcp_f32 v128, v128\n\t"
  "v_mfma_f32_16x16x32_bf16 v[44:47], a[12:15], v[76:79], v[44:47]\n\t"
  "ds_read_b64_tr_b16 v[68:69], v108 offset:12288\n\t"
  "s_add_u32 s45, %[sW2], 47104\n\t"
  "v_mfma_f32_16x16x32_bf16 v[48:51], a[16:19], v[76:79], v[48:51]\n\t"
  "ds_read_b64_tr_b16 v[70:71], v108 offset:14336\n\t"
  "v_rcp_f32 v129, v129\n\t"
  "v_mfma_f32_16x16x32_bf16 v[52:55], a[20:23], v[76:79], v[52:55]\n\t"
  "ds_read_b64_tr_b16 v[72:73], v109 offset:12288\n\t"
  "v_mul_f32 v128, v128, v128\n\t"
  "v_mfma_f32_16x16x32_bf16 v[56:59], a[12:15], v[80:83], v[56:59]\n\t"
  "ds_read_b64_tr_b16 v[74:75], v109 offset:14336\n\t"
  "v_mul_f32 v129, v129, v129\n\t"
  "v_mfma_f32_16x16x32_bf16 v[60:63], a[16:19], v[80:83], v[60:63]\n\t"
  "buffer_load_dwordx4 a[0:3], v1, %[srd], s45 offen offset:0 sc1\n\t"
  "v_max_f32 v20, 0, v20\n\t"
  "v_mfma_f32_16x16x32_bf16 v[64:67], a[20:23], v[80:83], v[64:67]\n\t"
  "s_waitcnt lgkmcnt(4)\n\t"
  "v_max_f32 v21, 0, v21\n\t"
  "v_mfma_f32_16x16x32_bf16 v[44:47], a[24:27], v[84:87], v[44:47]\n\t"
  "ds_read_b64_tr_b16 v[76:77], v108 offset:16384\n\t"
  "v_mul_f32 v22, v22, v128\n\t"
  "v_mfma_f32_16x16x32_bf16 v[48:51], a[28:31], v[84:87], v[48:51]\n\t"
  "ds_read_b64_tr_b16 v[78:79], v108 offset:18432\n\t"
  "v_mul_f32 v23, v23, v128\n\t"
  "v_mfma_f32_16x16x32_bf16 v[52:55], a[32:35], v[84:87], v[52:55]\n\t"
  "ds_read_b64_tr_b16 v[80:81], v109 offset:16384\n\t"
  "v_mul_f32 v20, v20, v22\n\t"
  "v_mfma_f32_16x16x32_bf16 v[56:59], a[24:27], v[88:91], v[56:59]\n\t"
  "ds_read_b64_tr_b16 v[82:83], v109 offset:18432\n\t"
  "buffer_load_dwordx4 a[4:7], v1, %[srd], s45 offen offset:1024 sc1\n\t"
  "v_mfma_f32_16x16x32_bf16 v[60:63], a[28:31], v[88:91], v[60:63]\n\t"
  "v_mul_f32 v21, v21, v23\n\t"
  "v_cvt_pk_bf16_f32 v116, v20, v21\n\t"
  "v_mfma_f32_16x16x32_bf16 v[64:67], a[32:35], v[88:91], v[64:67]\n\t"
  "s_waitcnt lgkmcnt(4)\n\t"
  "v_cvt_pk_bf16_f32 v112, v20, v21\n\t"
  "v_mfma_f32_16x16x32_bf16 v[44:47], a[36:39], v[68:71], v[44:47]\n\t"
  "ds_read_b64_tr_b16 v[84:85], v108 offset:20480\n\t"
  "s_movk_i32 s56, 8192\n\t"
  "v_mfma_f32_16x16x32_bf16 v[48:51], a[40:43], v[68:71], v[48:51]\n\t"
  "ds_read_b64_tr_b16 v[86:87], v108 offset:22528\n\t"
  "s_waitcnt vmcnt(7)\n\t"
  "v_mfma_f32_16x16x32_bf16 v[52:55], a[44:47], v[68:71], v[52:55]\n\t"
  "ds_read_b64_tr_b16 v[88:89], v109 offset:20480\n\t"
  "buffer_store_dwordx4 v[132:135], v9, %[srd], s56 offen sc1\n\t"
  "v_mfma_f32_16x16x32_bf16 v[56:59], a[36:39], v[72:75], v[56:59]\n\t"
  "ds_read_b64_tr_b16 v[90:91], v109 offset:22528\n\t"
  "buffer_load_dwordx4 a[8:11], v1, %[srd], s45 offen offset:2048 sc1\n\t"
  "v_mfma_f32_16x16x32_bf16 v[60:63], a[40:43], v[72:75], v[60:63]\n\t"
  "v_max_f32 v24, 0, v24\n\t"
  "v_max_f32 v25, 0, v25\n\t"
  "v_mfma_f32_16x16x32_bf16 v[64:67], a[44:47], v[72:75], v[64:67]\n\t"
  "s_waitcnt lgkmcnt(4)\n\t"
  "v_mul_f32 v26, v26, v128\n\t"
  "v_mfma_f32_16x16x32_bf16 v[44:47], a[48:51], v[76:79], v[44:47]\n\t"
  "ds_read_b64_tr_b16 v[68:69], v108 offset:24576\n\t"
  "v_mul_f32 v27, v27, v128\n\t"
  "v_mfma_f32_16x16x32_bf16 v[48:51], a[52:55], v[76:79], v[48:51]\n\t"
  "ds_read_b64_tr_b16 v[70:71], v108 offset:26624\n\t"
  "buffer_load_dwordx4 a[12:15], v1, %[srd], s45 offen offset:3072 sc1\n\t"
  "v_mfma_f32_16x16x32_bf16 v[52:55], a[56:59], v[76:79], v[52:55]\n\t"
  "ds_read_b64_tr_b16 v[72:73], v109 offset:24576\n\t"
  "v_mul_f32 v24, v24, v26\n\t"
  "v_mfma_f32_16x16x32_bf16 v[56:59], a[48:51], v[80:83], v[56:59]\n\t"
  "ds_read_b64_tr_b16 v[74:75], v109 offset:26624\n\t"
  "v_mul_f32 v25, v25, v27\n\t"
  "v_mfma_f32_16x16x32_bf16 v[60:63], a[52:55], v[80:83], v[60:63]\n\t"
  "v_cvt_pk_bf16_f32 v117, v24, v25\n\t"
  "v_cvt_pk_bf16_f32 v113, v24, v25\n\t"
  "v_mfma_f32_16x16x32_bf16 v[64:67], a[56:59], v[80:83], v[64:67]\n\t"
  "s_waitcnt lgkmcnt(4)\n\t"
  "s_add_u32 s45, %[sW2], 51200\n\t"
  "v_mfma_f32_16x16x32_bf16 v[44:47], a[60:63], v[84:87], v[44:47]\n\t"
  "ds_read_b64_tr_b16 v[76:77], v108 offset:28672\n\t"
  "v_max_f32 v28, 0, v28\n\t"
  "v_mfma_f32_16x16x32_bf16 v[48:51], a[64:67], v[84:87], v[48:51]\n\t"
  "ds_read_b64_tr_b16 v[78:79], v108 offset:30720\n\t"
  "v_max_f32 v29, 0, v29\n\t"
  "v_mfma_f32_16x16x32_bf16 v[52:55], a[68:71], v[84:87], v[52:55]\n\t"
  "ds_read_b64_tr_b16 v[80:81], v109 offset:28672\n\t"
  "v_mul_f32 v30, v30, v128\n\t"
  "v_mfma_f32_16x16x32_bf16 v[56:59], a[60:63], v[88:91], v[56:59]\n\t"
  "ds_read_b64_tr_b16 v[82:83], v109 offset:30720\n\t"
  "v_mul_f32 v31, v31, v128\n\t"
  "v_mfma_f32_16x16x32_bf16 v[60:63], a[64:67], v[88:91], v[60:63]\n\t"
  "buffer_load_dwordx4 a[16:19], v1, %[srd], s45 offen offset:0 sc1\n\t"
  "v_mul_f32 v28, v28, v30\n\t"
  "v_mfma_f32_16x16x32_bf16 v[64:67], a[68:71], v[88:91], v[64:67]\n\t"
  "s_waitcnt lgkmcnt(4)\n\t"
  "v_permlane16_swap_b32 v116, v117\n\t"
  "v_mfma_f32_16x16x32_bf16 v[44:47], a[72:75], v[68:71], v[44:47]\n\t"
  "ds_read_b64_tr_b16 v[84:85], v108 offset:32768\n\t"
  "v_mul_f32 v29, v29, v31\n\t"
  "v_mfma_f32_16x16x32_bf16 v[48:51], a[76:79], v[68:71], v[48:51]\n\t"
  "ds_read_b64_tr_b16 v[86:87], v108 offset:34816\n\t"
  "v_cvt_pk_bf16_f32 v118, v28, v29\n\t"
  "v_mfma_f32_16x16x32_bf16 v[52:55], a[80:83], v[68:71], v[52:55]\n\t"
  "ds_read_b64_tr_b16 v[88:89], v109 offset:32768\n\t"
  "v_cvt_pk_bf16_f32 v114, v28, v29\n\t"
  "v_mfma_f32_16x16x32_bf16 v[56:59], a[72:75], v[72:75], v[56:59]\n\t"
  "ds_read_b64_tr_b16 v[90:91], v109 offset:34816\n\t"
  "buffer_load_dwordx4 a[20:23], v1, %[srd], s45 offen offset:1024 sc1\n\t"
  "v_mfma_f32_16x16x32_bf16 v[60:63], a[76:79], v[72:75], v[60:63]\n\t"
  "s_mov_b32 s55, 3145728\n\t"
  "v_permlane16_swap_b32 v118, v115\n\t"
  "v_mfma_f32_16x16x32_bf16 v[64:67], a[80:83], v[72:75], v[64:67]\n\t"
  "s_waitcnt lgkmcnt(4)\n\t"
  "v_permlane32_swap_b32 v117, v115\n\t"
  "v_mfma_f32_16x16x32_bf16 v[44:47], a[84:87], v[76:79], v[44:47]\n\t"
  "ds_read_b64_tr_b16 v[68:69], v108 offset:36864\n\t"
  "s_add_u32 s57, %[sFL], 0\n\t"
  "v_mfma_f32_16x16x32_bf16 v[48:51], a[88:91], v[76:79], v[48:51]\n\t"
  "ds_read_b64_tr_b16 v[70:71], v108 offset:38912\n\t"
  "buffer_load_dwordx2 v[138:139], v137, %[srd], s57 offen sc1\n\t"
  "v_mfma_f32_16x16x32_bf16 v[52:55], a[92:95], v[76:79], v[52:55]\n\t"
  "ds_read_b64_tr_b16 v[72:73], v109 offset:36864\n\t"
  "buffer_load_dwordx4 a[24:27], v1, %[srd], s45 offen offset:2048 sc1\n\t"
  "v_mfma_f32_16x16x32_bf16 v[56:59], a[84:87], v[80:83], v[56:59]\n\t"
  "ds_read_b64_tr_b16 v[74:75], v109 offset:38912\n\t"
  "buffer_store_dwordx4 v[112:115], v8, %[srd], s55 offen sc1\n\t"
  "v_mfma_f32_16x16x32_bf16 v[60:63], a[88:91], v[80:83], v[60:63]\n\t"
  "v_max_f32 v32, 0, v32\n\t"
  "v_max_f32 v33, 0, v33\n\t"
  "v_mfma_f32_16x16x32_bf16 v[64:67], a[92:95], v[80:83], v[64:67]\n\t"
  "s_waitcnt lgkmcnt(4)\n\t"
  "v_mul_f32 v34, v34, v129\n\t"
  "v_mfma_f32_16x16x32_bf16 v[44:47], a[96:99], v[84:87], v[44:47]\n\t"
  "ds_read_b64_tr_b16 v[76:77], v110 offset:0\n\t"
  "v_mul_f32 v35, v35, v129\n\t"
  "v_mfma_f32_16x16x32_bf16 v[48:51], a[100:103], v[84:87], v[48:51]\n\t"
  "ds_read_b64_tr_b16 v[78:79], v110 offset:2048\n\t"
  "buffer_load_dwordx4 a[28:31], v1, %[srd], s45 offen offset:3072 sc1\n\t"
  "v_mfma_f32_16x16x32_bf16 v[52:55], a[104:107], v[84:87], v[52:55]\n\t"
  "ds_read_b64_tr_b16 v[80:81], v111 offset:0\n\t"
  "v_mul_f32 v32, v32, v34\n\t"
  "v_mfma_f32_16x16x32_bf16 v[56:59], a[96:99], v[88:91], v[56:59]\n\t"
  "ds_read_b64_tr_b16 v[82:83], v111 offset:2048\n\t"
  "v_mul_f32 v33, v33, v35\n\t"
  "v_mfma_f32_16x16x32_bf16 v[60:63], a[100:103], v[88:91], v[60:63]\n\t"
  "v_cvt_pk_bf16_f32 v124, v32, v33\n\t"
  "v_cvt_pk_bf16_f32 v120, v32, v33\n\t"
  "v_mfma_f32_16x16x32_bf16 v[64:67], a[104:107], v[88:91], v[64:67]\n\t"
  "s_waitcnt lgkmcnt(4)\n\t"
  "s_add_u32 s45, %[sW2], 55296\n\t"
  "v_mfma_f32_16x16x32_bf16 v[44:47], a[108:111], v[68:71], v[44:47]\n\t"
  "ds_read_b64_tr_b16 v[84:85], v110 offset:4096\n\t"
  "v_max_f32 v36, 0, v36\n\t"
  "v_mfma_f32_16x16x32_bf16 v[48:51], a[112:115], v[68:71], v[48:51]\n\t"
  "ds_read_b64_tr_b16 v[86:87], v110 offset:6144\n\t"
  "v_max_f32 v37, 0, v37\n\t"
  "v_mfma_f32_16x16x32_bf16 v[52:55], a[116:119], v[68:71], v[52:55]\n\t"
  "ds_read_b64_tr_b16 v[88:89], v111 offset:4096\n\t"
  "v_mul_f32 v38, v38, v129\n\t"
  "v_mfma_f32_16x16x32_bf16 v[56:59], a[108:111], v[72:75], v[56:59]\n\t"
  "ds_read_b64_tr_b16 v[90:91], v111 offset:6144\n\t"
  "v_mul_f32 v39, v39, v129\n\t"
  "v_mfma_f32_16x16x32_bf16 v[60:63], a[112:115], v[72:75], v[60:63]\n\t"
  "buffer_load_dwordx4 a[32:35], v1, %[srd], s45 offen offset:0 sc1\n\t"
  "v_mul_f32 v36, v36, v38\n\t"
  "v_mfma_f32_16x16x32_bf16 v[64:67], a[116:119], v[72:75], v[64:67]\n\t"
  "s_waitcnt lgkmcnt(4)\n\t"
  "v_mul_f32 v37, v37, v39\n\t"
  "v_mfma_f32_16x16x32_bf16 v[44:47], a[120:123], v[76:79], v[44:47]\n\t"
  "ds_read_b64_tr_b16 v[68:69], v110 offset:8192\n\t"
  "v_cvt_pk_bf16_f32 v125, v36, v37\n\t"
  "v_mfma_f32_16x16x32_bf16 v[48:51], a[124:127], v[76:79], v[48:51]\n\t"
  "ds_read_b64_tr_b16 v[70:71], v110 offset:10240\n\t"
  "v_cvt_pk_bf16_f32 v121, v36, v37\n\t"
  "v_mfma_f32_16x16x32_bf16 v[52:55], a[128:131], v[76:79], v[52:55]\n\t"
  "ds_read_b64_tr_b16 v[72:73], v111 offset:8192\n\t"
  "v_max_f32 v40, 0, v40\n\t"
  "v_mfma_f32_16x16x32_bf16 v[56:59], a[120:123], v[80:83], v[56:59]\n\t"
  "ds_read_b64_tr_b16 v[74:75], v111 offset:10240\n\t"
  "buffer_load_dwordx4 a[36:39], v1, %[srd], s45 offen offset:1024 sc1\n\t"
  "v_mfma_f32_16x16x32_bf16 v[60:63], a[124:127], v[80:83], v[60:63]\n\t"
  "v_max_f32 v41, 0, v41\n\t"
  "v_mul_f32 v42, v42, v129\n\t"
  "v_mfma_f32_16x16x32_bf16 v[64:67], a[128:131], v[80:83], v[64:67]\n\t"
  "s_waitcnt lgkmcnt(4)\n\t"
  "v_mul_f32 v43, v43, v129\n\t"
  "v_mfma_f32_16x16x32_bf16 v[44:47], a[132:135], v[84:87], v[44:47]\n\t"
  "ds_read_b64_tr_b16 v[76:77], v110 offset:12288\n\t"
  "v_mul_f32 v40, v40, v42\n\t"
  "v_mfma_f32_16x16x32_bf16 v[48:51], a[136:139], v[84:87], v[48:51]\n\t"
  "ds_read_b64_tr_b16 v[78:79], v110 offset:14336\n\t"
  "v_permlane16_swap_b32 v124, v125\n\t"
  "v_mfma_f32_16x16x32_bf16 v[52:55], a[140:143], v[84:87], v[52:55]\n\t"
  "ds_read_b64_tr_b16 v[80:81], v111 offset:12288\n\t"
  "buffer_load_dwordx4 a[40:43], v1, %[srd], s45 offen offset:2048 sc1\n\t"
  "v_mfma_f32_16x16x32_bf16 v[56:59], a[132:135], v[88:91], v[56:59]\n\t"
  "ds_read_b64_tr_b16 v[82:83], v111 offset:14336\n\t"
  "v_mul_f32 v41, v41, v43\n\t"
  "v_mfma_f32_16x16x32_bf16 v[60:63], a[136:139], v[88:91], v[60:63]\n\t"
  "v_cvt_pk_bf16_f32 v126, v40, v41\n\t"
  "v_cvt_pk_bf16_f32 v122, v40, v41\n\t"
  "v_mfma_f32_16x16x32_bf16 v[64:67], a[140:143], v[88:91], v[64:67]\n\t"
  "s_waitcnt lgkmcnt(4)\n\t"
  "s_mov_b32 s55, 3194880\n\t"
  "v_mfma_f32_16x16x32_bf16 v[44:47], a[144:147], v[68:71], v[44:47]\n\t"
  "ds_read_b64_tr_b16 v[84:85], v110 offset:16384\n\t"
  "buffer_load_dwordx4 a[44:47], v1, %[srd], s45 offen offset:3072 sc1\n\t"
  "v_mfma_f32_16x16x32_bf16 v[48:51], a[148:151], v[68:71], v[48:51]\n\t"
  "ds_read_b64_tr_b16 v[86:87], v110 offset:18432\n\t"
  "v_permlane16_swap_b32 v126, v123\n\t"
  "v_mfma_f32_16x16x32_bf16 v[52:55], a[152:155], v[68:71], v[52:55]\n\t"
  "ds_read_b64_tr_b16 v[88:89], v111 offset:16384\n\t"
  "v_permlane32_swap_b32 v125, v123\n\t"
  "v_mfma_f32_16x16x32_bf16 v[56:59], a[144:147], v[72:75], v[56:59]\n\t"
  "ds_read_b64_tr_b16 v[90:91], v111 offset:18432\n\t"
  "buffer_store_dwordx4 v[120:123], v8, %[srd], s55 offen sc1\n\t"
  "v_mfma_f32_16x16x32_bf16 v[60:63], a[148:151], v[72:75], v[60:63]\n\t"
  "v_and_b32 v128, 0xffff0000, v105\n\t"
  "v_and_b32 v129, 0xffff0000, v107\n\t"
  "v_mfma_f32_16x16x32_bf16 v[64:67], a[152:155], v[72:75], v[64:67]\n\t"
  "s_waitcnt lgkmcnt(4)\n\t"
  "s_add_u32 s45, %[sW2], 59392\n\t"
  "v_mfma_f32_16x16x32_bf16 v[44:47], a[156:159], v[76:79], v[44:47]\n\t"
  "ds_read_b64_tr_b16 v[68:69], v110 offset:20480\n\t"
  "v_rcp_f32 v128, v128\n\t"
  "v_mfma_f32_16x16x32_bf16 v[48:51], a[160:163], v[76:79], v[48:51]\n\t"
  "ds_read_b64_tr_b16 v[70:71], v110 offset:22528\n\t"
  "v_rcp_f32 v129, v129\n\t"
  "v_mfma_f32_16x16x32_bf16 v[52:55], a[164:167], v[76:79], v[52:55]\n\t"
  "ds_read_b64_tr_b16 v[72:73], v111 offset:20480\n\t"
  "v_mul_f32 v128, v128, v128\n\t"
  "v_mfma_f32_16x16x32_bf16 v[56:59], a[156:159], v[80:83], v[56:59]\n\t"
  "ds_read_b64_tr_b16 v[74:75], v111 offset:22528\n\t"
  "v_mul_f32 v129, v129, v129\n\t"
  "v_mfma_f32_16x16x32_bf16 v[60:63], a[160:163], v[80:83], v[60:63]\n\t"
  "buffer_load_dwordx4 a[48:51], v1, %[srd], s45 offen offset:0 sc1\n\t"
  "v_mfma_f32_16x16x32_bf16 v[64:67], a[164:167], v[80:83], v[64:67]\n\t"
  "s_waitcnt lgkmcnt(4)\n\t"
  "s_waitcnt vmcnt(17)\n\t"
  "v_mfma_f32_16x16x32_bf16 v[44:47], a[168:171], v[84:87], v[44:47]\n\t"
  "ds_read_b64_tr_b16 v[76:77], v110 offset:24576\n\t"
  "ds_read_b128 a[160:163], v141 offset:0\n\t"
  "v_mfma_f32_16x16x32_bf16 v[48:51], a[172:175], v[84:87], v[48:51]\n\t"
  "ds_read_b64_tr_b16 v[78:79], v110 offset:26624\n\t"
  "ds_read_b128 a[164:167], v141 offset:1024\n\t"
  "v_mfma_f32_16x16x32_bf16 v[52:55], a[176:179], v[84:87], v[52:55]\n\t"
  "ds_read_b64_tr_b16 v[80:81], v111 offset:24576\n\t"
  "v_mfma_f32_16x16x32_bf16 v[56:59], a[168:171], v[88:91], v[56:59]\n\t"
  "ds_read_b64_tr_b16 v[82:83], v111 offset:26624\n\t"
  "buffer_load_dwordx4 a[52:55], v1, %[srd], s45 offen offset:1024 sc1\n\t"
  "v_mfma_f32_16x16x32_bf16 v[60:63], a[172:175], v[88:91], v[60:63]\n\t"
  "v_mfma_f32_16x16x32_bf16 v[64:67], a[176:179], v[88:91], v[64:67]\n\t"
  "s_waitcnt lgkmcnt(6)\n\t"
  "ds_read_b128 a[168:171], v141 offset:2048\n\t"
  "v_mfma_f32_16x16x32_bf16 v[44:47], a[180:183], v[68:71], v[44:47]\n\t"
  "ds_read_b64_tr_b16 v[84:85], v110 offset:28672\n\t"
  "ds_read_b128 a[172:175], v141 offset:3072\n\t"
  "v_mfma_f32_16x16x32_bf16 v[48:51], a[184:187], v[68:71], v[48:51]\n\t"
  "ds_read_b64_tr_b16 v[86:87], v110 offset:30720\n\t"
  "ds_read_b128 a[176:179], v141 offset:4096\n\t"
  "v_mfma_f32_16x16x32_bf16 v[52:55], a[188:191], v[68:71], v[52:55]\n\t"
  "ds_read_b64_tr_b16 v[88:89], v111 offset:28672\n\t"
  "v_mfma_f32_16x16x32_bf16 v[56:59], a[180:183], v[72:75], v[56:59]\n\t"
  "ds_read_b64_tr_b16 v[90:91], v111 offset:30720\n\t"
  "s_waitcnt vmcnt(10)\n\t"
  "v_cmp_ne_u64 vcc, v[132:133], v[138:139]\n\t"
  "v_mfma_f32_16x16x32_bf16 v[60:63], a[184:187], v[72:75], v[60:63]\n\t"
  "v_mfma_f32_16x16x32_bf16 v[64:67], a[188:191], v[72:75], v[64:67]\n\t"
  "s_waitcnt lgkmcnt(7)\n\t"
  "s_cbranch_vccnz Lfl0_slow%=\n\t"
  "Lfl0_back%=:\n\t"
  "v_mfma_f32_16x16x32_bf16 v[44:47], a[192:195], v[76:79], v[44:47]\n\t"
  "ds_read_b64_tr_b16 v[68:69], v110 offset:32768\n\t"
  "s_add_u32 s44, %[sH2ld], 0\n\t"
  "v_mfma_f32_16x16x32_bf16 v[48:51], a[196:199], v[76:79], v[48:51]\n\t"
  "ds_read_b64_tr_b16 v[70:71], v110 offset:34816\n\t"
  "ds_read_b128 a[180:183], v141 offset:5120\n\t"
  "v_mfma_f32_16x16x32_bf16 v[52:55], a[200:203], v[76:79], v[52:55]\n\t"
  "ds_read_b64_tr_b16 v[72:73], v111 offset:32768\n\t"
  "buffer_load_dwordx4 a[56:59], v1, %[srd], s44 offen offset:0 sc1\n\t"
  "v_mfma_f32_16x16x32_bf16 v[56:59], a[192:195], v[80:83], v[56:59]\n\t"
  "ds_read_b64_tr_b16 v[74:75], v111 offset:34816\n\t"
  "ds_read_b128 a[184:187], v141 offset:6144\n\t"
  "v_mfma_f32_16x16x32_bf16 v[60:63], a[196:199], v[80:83], v[60:63]\n\t"
  "buffer_load_dwordx4 a[60:63], v1, %[srd], s44 offen offset:1024 sc1\n\t"
  "ds_read_b128 a[188:191], v141 offset:7168\n\t"
  "v_mfma_f32_16x16x32_bf16 v[64:67], a[200:203], v[80:83], v[64:67]\n\t"
  "s_waitcnt lgkmcnt(7)\n\t"
  "ds_read_b128 a[192:195], v141 offset:8192\n\t"
  "v_mfma_f32_16x16x32_bf16 v[44:47], a[204:207], v[84:87], v[44:47]\n\t"
  "ds_read_b64_tr_b16 v[76:77], v110 offset:36864\n\t"
  "buffer_load_dwordx4 a[64:67], v1, %[srd], s44 offen offset:2048 sc1\n\t"
  "v_mfma_f32_16x16x32_bf16 v[48:51], a[208:211], v[84:87], v[48:51]\n\t"
  "ds_read_b64_tr_b16 v[78:79], v110 offset:38912\n\t"
  "ds_read_b128 a[196:199], v141 offset:9216\n\t"
  "v_mfma_f32_16x16x32_bf16 v[52:55], a[212:215], v[84:87], v[52:55]\n\t"
  "ds_read_b64_tr_b16 v[80:81], v111 offset:36864\n\t"
  "buffer_load_dwordx4 a[68:71], v1, %[srd], s44 offen offset:3072 sc1\n\t"
  "v_mfma_f32_16x16x32_bf16 v[56:59], a[204:207], v[88:91], v[56:59]\n\t"
  "ds_read_b64_tr_b16 v[82:83], v111 offset:38912\n\t"
  "ds_read_b128 a[200:203], v141 offset:10240\n\t"
  "v_mfma_f32_16x16x32_bf16 v[60:63], a[208:211], v[88:91], v[60:63]\n\t"
  "s_add_u32 s44, %[sH2ld], 4096\n\t"
  "v_mfma_f32_16x16x32_bf16 v[64:67], a[212:215], v[88:91], v[64:67]\n\t"
  "s_waitcnt lgkmcnt(9)\n\t"
  "ds_read_b128 a[204:207], v141 offset:11264\n\t"
  "v_mfma_f32_16x16x32_bf16 v[44:47], a[216:219], v[68:71], v[44:47]\n\t"
  "buffer_load_dwordx4 a[72:75], v1, %[srd], s44 offen offset:0 sc1\n\t"
  "ds_read_b128 a[208:211], v141 offset:12288\n\t"
  "v_mfma_f32_16x16x32_bf16 v[48:51], a[220:223], v[68:71], v[48:51]\n\t"
  "ds_read_b128 a[212:215], v141 offset:13312\n\t"
  "v_mfma_f32_16x16x32_bf16 v[52:55], a[224:227], v[68:71], v[52:55]\n\t"
  "buffer_load_dwordx4 a[76:79], v1, %[srd], s44 offen offset:1024 sc1\n\t"
  "v_mfma_f32_16x16x32_bf16 v[56:59], a[216:219], v[72:75], v[56:59]\n\t"
  "v_mfma_f32_16x16x32_bf16 v[60:63], a[220:223], v[72:75], v[60:63]\n\t"
  "buffer_load_dwordx4 a[80:83], v1, %[srd], s44 offen offset:2048 sc1\n\t"
  "v_mfma_f32_16x16x32_bf16 v[64:67], a[224:227], v[72:75], v[64:67]\n\t"
  "s_waitcnt lgkmcnt(4)\n\t"
  "ds_read_b128 a[216:219], v141 offset:14336\n\t"
  "v_mfma_f32_16x16x32_bf16 v[44:47], a[228:231], v[76:79], v[44:47]\n\t"
  "buffer_load_dwordx4 a[84:87], v1, %[srd], s44 offen offset:3072 sc1\n\t"
  "ds_read_b128 a[220:223], v141 offset:15360\n\t"
  "v_mfma_f32_16x16x32_bf16 v[48:51], a[232:235], v[76:79], v[48:51]\n\t"
  "ds_read_b128 a[224:227], v141 offset:16384\n\t"
  "v_mfma_f32_16x16x32_bf16 v[52:55], a[236:239], v[76:79], v[52:55]\n\t"
  "s_add_u32 s44, %[sH2ld], 8192\n\t"
  "v_mfma_f32_16x16x32_bf16 v[56:59], a[228:231], v[80:83], v[56:59]\n\t"
  "v_mfma_f32_16x16x32_bf16 v[60:63], a[232:235], v[80:83], v[60:63]\n\t"
  "buffer_load_dwordx4 a[88:91], v1, %[srd], s44 offen offset:0 sc1\n\t"
  "v_mfma_f32_16x16x32_bf16 v[64:67], a[236:239], v[80:83], v[64:67]\n\t"
  "ds_read_b128 a[228:231], v141 offset:17408\n\t"
  "ds_read_b128 a[232:235], v141 offset:18432\n\t"
  "v_mfma_f32_16x16x16_bf16 v[44:47], a[240:241], v[104:105], v[44:47]\n\t"
  "s_add_u32 s57, %[sFL], 8192\n\t"
  "buffer_load_dwordx2 v[138:139], v137, %[srd], s57 offen sc1\n\t"
  "v_mfma_f32_16x16x16_bf16 v[48:51], a[242:243], v[104:105], v[48:51]\n\t"
  "buffer_load_dwordx4 a[92:95], v1, %[srd], s44 offen offset:1024 sc1\n\t"
  "ds_read_b128 a[236:239], v141 offset:19456\n\t"
  "v_mfma_f32_16x16x16_bf16 v[52:55], a[244:245], v[104:105], v[52:55]\n\t"
  "buffer_load_dwordx4 a[96:99], v1, %[srd], s44 offen offset:2048 sc1\n\t"
  "v_mfma_f32_16x16x16_bf16 v[56:59], a[240:241], v[106:107], v[56:59]\n\t"
  "v_mfma_f32_16x16x16_bf16 v[60:63], a[242:243], v[106:107], v[60:63]\n\t"
  "buffer_load_dwordx4 a[100:103], v1, %[srd], s44 offen offset:3072 sc1\n\t"
  "v_mfma_f32_16x16x16_bf16 v[64:67], a[244:245], v[106:107], v[64:67]\n\t"
  "s_movk_i32 s56, 16384\n\t"
  "s_waitcnt vmcnt(15)\n\t"
  "buffer_store_dwordx4 v[132:135], v9, %[srd], s56 offen sc1\n\t"
  "v_max_f32 v44, 0, v44\n\t"
  "v_max_f32 v45, 0, v45\n\t"
  "v_pk_mul_f32 v[46:47], v[46:47], v[128:129] op_sel:[0,0] op_sel_hi:[1,0]\n\t"
  "v_pk_mul_f32 v[44:45], v[44:45], v[46:47]\n\t"
  "v_cvt_pk_bf16_f32 v116, v44, v45\n\t"
  "v_cvt_pk_bf16_f32 v112, v44, v45\n\t"
  "v_max_f32 v48, 0, v48\n\t"
  "v_max_f32 v49, 0, v49\n\t"
  "v_pk_mul_f32 v[50:51], v[50:51], v[128:129] op_sel:[0,0] op_sel_hi:[1,0]\n\t"
  "v_pk_mul_f32 v[48:49], v[48:49], v[50:51]\n\t"
  "v_cvt_pk_bf16_f32 v117, v48, v49\n\t"
  "v_cvt_pk_bf16_f32 v113, v48, v49\n\t"
  "v_max_f32 v52, 0, v52\n\t"
  "v_max_f32 v53, 0, v53\n\t"
  "v_pk_mul_f32 v[54:55], v[54:55], v[128:129] op_sel:[0,0] op_sel_hi:[1,0]\n\t"
  "v_permlane16_swap_b32 v116, v117\n\t"
  "v_pk_mul_f32 v[52:53], v[52:53], v[54:55]\n\t"
  "v_cvt_pk_bf16_f32 v118, v52, v53\n\t"
  "v_cvt_pk_bf16_f32 v114, v52, v53\n\t"
  "s_mov_b32 s55, 3244032\n\t"
  "v_permlane16_swap_b32 v118, v115\n\t"
  "s_nop 1\n\t"
  "v_permlane32_swap_b32 v117, v115\n\t"
  "buffer_store_dwordx4 v[112:115], v8, %[srd], s55 offen sc1\n\t"
  "v_max_f32 v56, 0, v56\n\t"
  "v_max_f32 v57, 0, v57\n\t"
  "v_pk_mul_f32 v[58:59], v[58:59], v[128:129] op_sel:[0,1] op_sel_hi:[1,1]\n\t"
  "v_pk_mul_f32 v[56:57], v[56:57], v[58:59]\n\t"
  "v_cvt_pk_bf16_f32 v124, v56, v57\n\t"
  "v_cvt_pk_bf16_f32 v120, v56, v57\n\t"
  "v_max_f32 v60, 0, v60\n\t"
  "v_max_f32 v61, 0, v61\n\t"
  "v_pk_mul_f32 v[62:63], v[62:63], v[128:129] op_sel:[0,1] op_sel_hi:[1,1]\n\t"
  "v_pk_mul_f32 v[60:61], v[60:61], v[62:63]\n\t"
  "v_cvt_pk_bf16_f32 v125, v60, v61\n\t"
  "v_cvt_pk_bf16_f32 v121, v60, v61\n\t"
  "v_max_f32 v64, 0, v64\n\t"
  "v_max_f32 v65, 0, v65\n\t"
  "v_pk_mul_f32 v[66:67], v[66:67], v[128:129] op_sel:[0,1] op_sel_hi:[1,1]\n\t"
  "v_permlane16_swap_b32 v124, v125\n\t"
  "v_pk_mul_f32 v[64:65], v[64:65], v[66:67]\n\t"
  "v_cvt_pk_bf16_f32 v126, v64, v65\n\t"
  "v_cvt_pk_bf16_f32 v122, v64, v65\n\t"
  "s_mov_b32 s55, 3293184\n\t"
  "v_permlane16_swap_b32 v126, v123\n\t"
  "s_nop 1\n\t"
  "v_permlane32_swap_b32 v125, v123\n\t"
  "buffer_store_dwordx4 v[120:123], v8, %[srd], s55 offen sc1\n\t"
  "s_waitcnt lgkmcnt(0)\n\t"
  "s_barrier\n\t"
  "s_waitcnt vmcnt(15)\n\t"
  "v_mfma_f32_16x16x32_bf16 v[20:23], v[144:147], a[56:59], 0\n\t"
  "v_lshrrev_b32 v63, 3, v1\n\t"
  "v_and_b32 v63, 0x60, v63\n\t"
  "v_mfma_f32_16x16x32_bf16 v[24:27], v[148:151], a[56:59], 0\n\t"
  "v_lshrrev_b32 v62, 1, v63\n\t"
  "buffer_load_dwordx4 v[68:71], v63, %[srd], %[sRT] offen offset:0 sc1\n\t"
  "v_mfma_f32_16x16x32_bf16 v[28:31], v[152:155], a[56:59], 0\n\t"
  "buffer_load_dwordx4 v[72:75], v63, %[srd], %[sRT] offen offset:16 sc1\n\t"
  "buffer_load_dwordx4 v[76:79], v63, %[srd], %[sRT] offen offset:128 sc1\n\t"
  "v_mfma_f32_16x16x32_bf16 v[32:35], v[156:159], a[56:59], 0\n\t"
  "buffer_load_dwordx4 v[80:83], v63, %[srd], %[sRT] offen offset:144 sc1\n\t"
  "buffer_load_dwordx4 v[84:87], v62, %[srd], %[sRT] offen offset:256 sc1\n\t"
  "v_mfma_f32_16x16x32_bf16 v[36:39], v[160:163], a[56:59], 0\n\t"
  "buffer_load_dwordx4 v[64:67], v17, %[srd], %[sRT] offen offset:0 sc1\n\t"
  "buffer_load_dwordx4 v[108:111], v17, %[srd], %[sRT] offen offset:16 sc1\n\t"
  "s_waitcnt vmcnt(21)\n\t"
  "v_mfma_f32_16x16x32_bf16 v[20:23], v[164:167], a[60:63], v[20:23]\n\t"
  "s_mul_i32 s48, %[w], -15360\n\t"
  "s_add_u32 s48, s48, 81920\n\t"
  "v_mfma_f32_16x16x32_bf16 v[24:27], v[168:171], a[60:63], v[24:27]\n\t"
  "v_add_u32 v62, s48, v13\n\t"
  "v_add_u32 v60, 61440, v14\n\t"
  "v_mfma_f32_16x16x32_bf16 v[28:31], v[172:175], a[60:63], v[28:31]\n\t"
  "v_add_u32 v61, 61440, v15\n\t"
  "v_accvgpr_read_b32 v89, a246\n\t"
  "v_mfma_f32_16x16x32_bf16 v[32:35], v[176:179], a[60:63], v[32:35]\n\t"
  "v_lshlrev_b32 v88, 16, v89\n\t"
  "v_and_b32 v89, 0xffff0000, v89\n\t"
  "v_mfma_f32_16x16x32_bf16 v[36:39], v[180:183], a[60:63], v[36:39]\n\t"
  "v_accvgpr_read_b32 v91, a247\n\t"
  "v_lshlrev_b32 v90, 16, v91\n\t"
  "s_waitcnt vmcnt(20)\n\t"
  "v_mfma_f32_16x16x32_bf16 v[20:23], v[184:187], a[64:67], v[20:23]\n\t"
  "v_and_b32 v91, 0xffff0000, v91\n\t"
  "v_accvgpr_read_b32 v93, a248\n\t"
  "v_mfma_f32_16x16x32_bf16 v[24:27], v[188:191], a[64:67], v[24:27]\n\t"
  "v_lshlrev_b32 v92, 16, v93\n\t"
  "v_and_b32 v93, 0xffff0000, v93\n\t"
  "v_mfma_f32_16x16x32_bf16 v[28:31], v[192:195], a[64:67], v[28:31]\n\t"
  "v_accvgpr_read_b32 v95, a249\n\t"
  "v_lshlrev_b32 v94, 16, v95\n\t"
  "v_mfma_f32_16x16x32_bf16 v[32:35], v[196:199], a[64:67], v[32:35]\n\t"
  "v_and_b32 v95, 0xffff0000, v95\n\t"
  "v_accvgpr_read_b32 v97, a250\n\t"
  "v_mfma_f32_16x16x32_bf16 v[36:39], v[200:203], a[64:67], v[36:39]\n\t"
  "s_waitcnt vmcnt(13)\n\t"
  "v_cmp_ne_u64 vcc, v[132:133], v[138:139]\n\t"
  "v_mfma_f32_16x16x32_bf16 v[20:23], v[204:207], a[68:71], v[20:23]\n\t"
  "v_lshlrev_b32 v96, 16, v97\n\t"
  "v_and_b32 v97, 0xffff0000, v97\n\t"
  "v_mfma_f32_16x16x32_bf16 v[24:27], v[208:211], a[68:71], v[24:27]\n\t"
  "s_cbranch_vccnz Lfl1_slow%=\n\t"
  "Lfl1_back%=:\n\t"
  "v_mfma_f32_16x16x32_bf16 v[28:31], v[212:215], a[68:71], v[28:31]\n\t"
  "v_accvgpr_read_b32 v99, a251\n\t"
  "v_lshlrev_b32 v98, 16, v99\n\t"
  "v_mfma_f32_16x16x32_bf16 v[32:35], v[216:219], a[68:71], v[32:35]\n\t"
  "s_add_u32 s45, %[sH2ld], 98304\n\t"
  "v_and_b32 v99, 0xffff0000, v99\n\t"
  "v_mfma_f32_16x16x32_bf16 v[36:39], v[220:223], a[68:71], v[36:39]\n\t"
  "v_accvgpr_read_b32 v101, a252\n\t"
  "v_lshlrev_b32 v100, 16, v101\n\t"
  "v_mfma_f32_16x16x32_bf16 v[20:23], v[224:227], a[72:75], v[20:23]\n\t"
  "v_and_b32 v101, 0xffff0000, v101\n\t"
  "v_accvgpr_read_b32 v103, a253\n\t"
  "v_mfma_f32_16x16x32_bf16 v[24:27], v[228:231], a[72:75], v[24:27]\n\t"
  "buffer_load_dwordx4 a[104:107], v1, %[srd], s45 offen offset:0 sc1\n\t"
  "v_lshlrev_b32 v102, 16, v103\n\t"
  "v_mfma_f32_16x16x32_bf16 v[28:31], v[232:235], a[72:75], v[28:31]\n\t"
  "v_and_b32 v103, 0xffff0000, v103\n\t"
  "v_accvgpr_read_b32 v105, a254\n\t"
  "v_mfma_f32_16x16x32_bf16 v[32:35], v[236:239], a[72:75], v[32:35]\n\t"
  "v_lshlrev_b32 v104, 16, v105\n\t"
  "v_and_b32 v105, 0xffff0000, v105\n\t"
  "v_mfma_f32_16x16x32_bf16 v[36:39], v[240:243], a[72:75], v[36:39]\n\t"
  "buffer_load_dwordx4 a[108:111], v1, %[srd], s45 offen offset:1024 sc1\n\t"
  "v_accvgpr_read_b32 v107, a255\n\t"
  "v_mfma_f32_16x16x32_bf16 v[20:23], v[244:247], a[76:79], v[20:23]\n\t"
  "v_lshlrev_b32 v106, 16, v107\n\t"
  "v_and_b32 v107, 0xffff0000, v107\n\t"
  "v_mfma_f32_16x16x32_bf16 v[24:27], a[160:163], a[76:79], v[24:27]\n\t"
  "s_movk_i32 s56, 24576\n\t"
  "s_waitcnt vmcnt(9)\n\t"
  "v_mfma_f32_16x16x32_bf16 v[28:31], a[164:167], a[76:79], v[28:31]\n\t"
  "buffer_store_dwordx4 v[132:135], v9, %[srd], s56 offen sc1\n\t"
  "buffer_load_dwordx4 a[112:115], v1, %[srd], s45 offen offset:2048 sc1\n\t"
  "v_mfma_f32_16x16x32_bf16 v[32:35], a[168:171], a[76:79], v[32:35]\n\t"
  "v_mfma_f32_16x16x32_bf16 v[36:39], a[172:175], a[76:79], v[36:39]\n\t"
  "v_mfma_f32_16x16x32_bf16 v[20:23], a[176:179], a[80:83], v[20:23]\n\t"
  "buffer_load_dwordx4 a[116:119], v1, %[srd], s45 offen offset:3072 sc1\n\t"
  "s_add_u32 s57, %[sFL], 16384\n\t"
  "v_mfma_f32_16x16x32_bf16 v[24:27], a[180:183], a[80:83], v[24:27]\n\t"
  "buffer_load_dwordx2 v[138:139], v137, %[srd], s57 offen sc1\n\t"
  "v_mfma_f32_16x16x32_bf16 v[28:31], a[184:187], a[80:83], v[28:31]\n\t"
  "v_mfma_f32_16x16x32_bf16 v[32:35], a[188:191], a[80:83], v[32:35]\n\t"
  "v_mfma_f32_16x16x32_bf16 v[36:39], a[192:195], a[80:83], v[36:39]\n\t"
  "s_add_u32 s45, %[sH2ld], 102400\n\t"
  "v_mfma_f32_16x16x32_bf16 v[20:23], a[196:199], a[84:87], v[20:23]\n\t"
  "v_mfma_f32_16x16x32_bf16 v[24:27], a[200:203], a[84:87], v[24:27]\n\t"
  "v_mfma_f32_16x16x32_bf16 v[28:31], a[204:207], a[84:87], v[28:31]\n\t"
  "buffer_load_dwordx4 a[120:123], v1, %[srd], s45 offen offset:0 sc1\n\t"
  "v_mfma_f32_16x16x32_bf16 v[32:35], a[208:211], a[84:87], v[32:35]\n\t"
  "v_mfma_f32_16x16x32_bf16 v[36:39], a[212:215], a[84:87], v[36:39]\n\t"
  "v_mfma_f32_16x16x32_bf16 v[20:23], a[216:219], a[88:91], v[20:23]\n\t"
  "buffer_load_dwordx4 a[124:127], v1, %[srd], s45 offen offset:1024 sc1\n\t"
  "v_mfma_f32_16x16x32_bf16 v[24:27], a[220:223], a[88:91], v[24:27]\n\t"
  "s_waitcnt vmcnt(14)\n\t"
  "v_mul_f32 v88, v88, v68\n\t"
  "v_mfma_f32_16x16x32_bf16 v[28:31], a[224:227], a[88:91], v[28:31]\n\t"
  "v_mul_f32 v89, v89, v69\n\t"
  "v_mul_f32 v90, v90, v70\n\t"
  "v_mfma_f32_16x16x32_bf16 v[32:35], a[228:231], a[88:91], v[32:35]\n\t"
  "buffer_load_dwordx4 a[128:131], v1, %[srd], s45 offen offset:2048 sc1\n\t"
  "v_mul_f32 v91, v91, v71\n\t"
  "v_mfma_f32_16x16x32_bf16 v[36:39], a[232:235], a[88:91], v[36:39]\n\t"
  "ds_write_b128 v62, v[88:91] offset:0\n\t"
  "s_waitcnt vmcnt(14)\n\t"
  "v_mul_f32 v92, v92, v72\n\t"
  "v_mfma_f32_16x16x32_bf16 v[20:23], a[236:239], a[92:95], v[20:23]\n\t"
  "v_mul_f32 v93, v93, v73\n\t"
  "v_mul_f32 v94, v94, v74\n\t"
  "v_mfma_f32_16x16x32_bf16 v[24:27], a[0:3], a[92:95], v[24:27]\n\t"
  "buffer_load_dwordx4 a[132:135], v1, %[srd], s45 offen offset:3072 sc1\n\t"
  "v_mul_f32 v95, v95, v75\n\t"
  "v_mfma_f32_16x16x32_bf16 v[28:31], a[4:7], a[92:95], v[28:31]\n\t"
  "ds_write_b128 v62, v[92:95] offset:1024\n\t"
  "s_waitcnt vmcnt(14)\n\t"
  "v_mul_f32 v96, v96, v76\n\t"
  "v_mfma_f32_16x16x32_bf16 v[32:35], a[8:11], a[92:95], v[32:35]\n\t"
  "v_mul_f32 v97, v97, v77\n\t"
  "v_mul_f32 v98, v98, v78\n\t"
  "v_mfma_f32_16x16x32_bf16 v[36:39], a[12:15], a[92:95], v[36:39]\n\t"
  "v_mul_f32 v99, v99, v79\n\t"
  "ds_write_b128 v62, v[96:99] offset:2048\n\t"
  "v_mfma_f32_16x16x32_bf16 v[20:23], a[16:19], a[96:99], v[20:23]\n\t"
  "s_add_u32 s45, %[sH2ld], 106496\n\t"
  "s_waitcnt vmcnt(13)\n\t"
  "v_mul_f32 v100, v100, v80\n\t"
  "v_mfma_f32_16x16x32_bf16 v[24:27], a[20:23], a[96:99], v[24:27]\n\t"
  "v_mul_f32 v101, v101, v81\n\t"
  "v_mul_f32 v102, v102, v82\n\t"
  "v_mfma_f32_16x16x32_bf16 v[28:31], a[24:27], a[96:99], v[28:31]\n\t"
  "v_mul_f32 v103, v103, v83\n\t"
  "ds_write_b128 v62, v[100:103] offset:3072\n\t"
  "v_mfma_f32_16x16x32_bf16 v[32:35], a[28:31], a[96:99], v[32:35]\n\t"
  "buffer_load_dwordx4 a[136:139], v1, %[srd], s45 offen offset:0 sc1\n\t"
  "s_waitcnt vmcnt(13)\n\t"
  "v_mul_f32 v104, v104, v84\n\t"
  "v_mfma_f32_16x16x32_bf16 v[36:39], a[32:35], a[96:99], v[36:39]\n\t"
  "v_mul_f32 v105, v105, v85\n\t"
  "v_mul_f32 v106, v106, v86\n\t"
  "v_mfma_f32_16x16x32_bf16 v[20:23], a[36:39], a[100:103], v[20:23]\n\t"
  "v_mul_f32 v107, v107, v87\n\t"
  "ds_write_b128 v62, v[104:107] offset:4096\n\t"
  "v_mfma_f32_16x16x32_bf16 v[24:27], a[40:43], a[100:103], v[24:27]\n\t"
  "buffer_load_dwordx4 a[140:143], v1, %[srd], s45 offen offset:1024 sc1\n\t"
  "v_mfma_f32_16x16x32_bf16 v[28:31], a[44:47], a[100:103], v[28:31]\n\t"
  "v_mfma_f32_16x16x32_bf16 v[32:35], a[48:51], a[100:103], v[32:35]\n\t"
  "v_mfma_f32_16x16x32_bf16 v[36:39], a[52:55], a[100:103], v[36:39]\n\t"
  "s_waitcnt vmcnt(10)\n\t"
  "buffer_load_dwordx4 a[144:147], v1, %[srd], s45 offen offset:2048 sc1\n\t"
  "v_mfma_f32_16x16x32_bf16 v[40:43], v[144:147], a[104:107], 0\n\t"
  "v_mfma_f32_16x16x32_bf16 v[44:47], v[148:151], a[104:107], 0\n\t"
  "v_mfma_f32_16x16x32_bf16 v[48:51], v[152:155], a[104:107], 0\n\t"
  "buffer_load_dwordx4 a[148:151], v1, %[srd], s45 offen offset:3072 sc1\n\t"
  "v_mfma_f32_16x16x32_bf16 v[52:55], v[156:159], a[104:107], 0\n\t"
  "ds_write_b128 v13, v[20:23] offset:0\n\t"
  "v_mfma_f32_16x16x32_bf16 v[56:59], v[160:163], a[104:107], 0\n\t"
  "v_mfma_f32_16x16x32_bf16 v[40:43], v[164:167], a[108:111], v[40:43]\n\t"
  "v_mfma_f32_16x16x32_bf16 v[44:47], v[168:171], a[108:111], v[44:47]\n\t"
  "ds_write_b128 v13, v[24:27] offset:1024\n\t"
  "v_mfma_f32_16x16x32_bf16 v[48:51], v[172:175], a[108:111], v[48:51]\n\t"
  "v_mfma_f32_16x16x32_bf16 v[52:55], v[176:179], a[108:111], v[52:55]\n\t"
  "v_mfma_f32_16x16x32_bf16 v[56:59], v[180:183], a[108:111], v[56:59]\n\t"
  "s_waitcnt vmcnt(9)\n\t"
  "ds_write_b128 v13, v[28:31] offset:2048\n\t"
  "v_mfma_f32_16x16x32_bf16 v[40:43], v[184:187], a[112:115], v[40:43]\n\t"
  "v_mfma_f32_16x16x32_bf16 v[44:47], v[188:191], a[112:115], v[44:47]\n\t"
  "v_mfma_f32_16x16x32_bf16 v[48:51], v[192:195], a[112:115], v[48:51]\n\t"
  "ds_write_b128 v13, v[32:35] offset:3072\n\t"
  "v_mfma_f32_16x16x32_bf16 v[52:55], v[196:199], a[112:115], v[52:55]\n\t"
  "v_mfma_f32_16x16x32_bf16 v[56:59], v[200:203], a[112:115], v[56:59]\n\t"
  "v_mfma_f32_16x16x32_bf16 v[40:43], v[204:207], a[116:119], v[40:43]\n\t"
  "ds_write_b128 v13, v[36:39] offset:4096\n\t"
  "v_mfma_f32_16x16x32_bf16 v[44:47], v[208:211], a[116:119], v[44:47]\n\t"
  "v_mfma_f32_16x16x32_bf16 v[48:51], v[212:215], a[116:119], v[48:51]\n\t"
  "v_mfma_f32_16x16x32_bf16 v[52:55], v[216:219], a[116:119], v[52:55]\n\t"
  "v_mfma_f32_16x16x32_bf16 v[56:59], v[220:223], a[116:119], v[56:59]\n\t"
  "s_waitcnt vmcnt(6)\n\t"
  "v_mfma_f32_16x16x32_bf16 v[40:43], v[224:227], a[120:123], v[40:43]\n\t"
  "v_mfma_f32_16x16x32_bf16 v[44:47], v[228:231], a[120:123], v[44:47]\n\t"
  "v_cmp_ne_u64 vcc, v[132:133], v[138:139]\n\t"
  "v_mfma_f32_16x16x32_bf16 v[48:51], v[232:235], a[120:123], v[48:51]\n\t"
  "v_mfma_f32_16x16x32_bf16 v[52:55], v[236:239], a[120:123], v[52:55]\n\t"
  "s_cbranch_vccnz Lfl2_slow%=\n\t"
  "Lfl2_back%=:\n\t"
  "v_mfma_f32_16x16x32_bf16 v[56:59], v[240:243], a[120:123], v[56:59]\n\t"
  "s_add_u32 s44, %[sH2ld], 3145728\n\t"
  "s_waitcnt lgkmcnt(0)\n\t"
  "s_barrier\n\t"
  "v_mfma_f32_16x16x32_bf16 v[40:43], v[244:247], a[124:127], v[40:43]\n\t"
  "ds_read_b128 v[68:71], v14 offset:0\n\t"
  "ds_read_b128 v[72:75], v15 offset:0\n\t"
  "v_mfma_f32_16x16x32_bf16 v[44:47], a[160:163], a[124:127], v[44:47]\n\t"
  "ds_read_b128 v[76:79], v14 offset:20480\n\t"
  "ds_read_b128 v[80:83], v15 offset:20480\n\t"
  "v_mfma_f32_16x16x32_bf16 v[48:51], a[164:167], a[124:127], v[48:51]\n\t"
  "buffer_load_dwordx4 a[152:155], v1, %[srd], s44 offen offset:0 sc1\n\t"
  "ds_read_b128 v[84:87], v14 offset:40960\n\t"
  "v_mfma_f32_16x16x32_bf16 v[52:55], a[168:171], a[124:127], v[52:55]\n\t"
  "ds_read_b128 v[88:91], v15 offset:40960\n\t"
  "ds_read_b128 v[92:95], v60 offset:0\n\t"
  "v_mfma_f32_16x16x32_bf16 v[56:59], a[172:175], a[124:127], v[56:59]\n\t"
  "s_waitcnt vmcnt(5)\n\t"
  "ds_read_b128 v[96:99], v61 offset:0\n\t"
  "v_mfma_f32_16x16x32_bf16 v[40:43], a[176:179], a[128:131], v[40:43]\n\t"
  "buffer_load_dwordx4 a[156:159], v1, %[srd], s44 offen offset:1024 sc1\n\t"
  "s_add_u32 s57, %[sFL], 24576\n\t"
  "v_mfma_f32_16x16x32_bf16 v[44:47], a[180:183], a[128:131], v[44:47]\n\t"
  "buffer_load_dwordx2 v[138:139], v137, %[srd], s57 offen sc1\n\t"
  "ds_read_b128 v[100:103], v60 offset:20480\n\t"
  "v_mfma_f32_16x16x32_bf16 v[48:51], a[184:187], a[128:131], v[48:51]\n\t"
  "ds_read_b128 v[104:107], v61 offset:20480\n\t"
  "v_mfma_f32_16x16x32_bf16 v[52:55], a[188:191], a[128:131], v[52:55]\n\t"
  "buffer_load_dwordx4 v[112:115], v1, %[srd], s44 offen offset:2048 sc1\n\t"
  "v_mfma_f32_16x16x32_bf16 v[56:59], a[192:195], a[128:131], v[56:59]\n\t"
  "v_mfma_f32_16x16x32_bf16 v[40:43], a[196:199], a[132:135], v[40:43]\n\t"
  "v_mfma_f32_16x16x32_bf16 v[44:47], a[200:203], a[132:135], v[44:47]\n\t"
  "buffer_load_dwordx4 v[116:119], v1, %[srd], s44 offen offset:3072 sc1\n\t"
  "v_mfma_f32_16x16x32_bf16 v[48:51], a[204:207], a[132:135], v[48:51]\n\t"
  "v_mfma_f32_16x16x32_bf16 v[52:55], a[208:211], a[132:135], v[52:55]\n\t"
  "v_mfma_f32_16x16x32_bf16 v[56:59], a[212:215], a[132:135], v[56:59]\n\t"
  "s_waitcnt vmcnt(7)\n\t"
  "v_mfma_f32_16x16x32_bf16 v[40:43], a[216:219], a[136:139], v[40:43]\n\t"
  "s_add_u32 s44, %[sH2ld], 3149824\n\t"
  "s_waitcnt lgkmcnt(0)\n\t"
  "v_mfma_f32_16x16x32_bf16 v[44:47], a[220:223], a[136:139], v[44:47]\n\t"
  "v_add_f32 v68, v68, v76\n\t"
  "v_add_f32 v69, v69, v77\n\t"
  "v_mfma_f32_16x16x32_bf16 v[48:51], a[224:227], a[136:139], v[48:51]\n\t"
  "v_add_f32 v70, v70, v78\n\t"
  "v_add_f32 v71, v71, v79\n\t"
  "v_mfma_f32_16x16x32_bf16 v[52:55], a[228:231], a[136:139], v[52:55]\n\t"
  "buffer_load_dwordx4 v[120:123], v1, %[srd], s44 offen offset:0 sc1\n\t"
  "v_add_f32 v72, v72, v80\n\t"
  "v_mfma_f32_16x16x32_bf16 v[56:59], a[232:235], a[136:139], v[56:59]\n\t"
  "v_add_f32 v73, v73, v81\n\t"
  "v_add_f32 v74, v74, v82\n\t"
  "v_mfma_f32_16x16x32_bf16 v[40:43], a[236:239], a[140:143], v[40:43]\n\t"
  "v_add_f32 v75, v75, v83\n\t"
  "v_add_f32 v84, v84, v92\n\t"
  "v_mfma_f32_16x16x32_bf16 v[44:47], a[0:3], a[140:143], v[44:47]\n\t"
  "buffer_load_dwordx4 v[124:127], v1, %[srd], s44 offen offset:1024 sc1\n\t"
  "v_add_f32 v85, v85, v93\n\t"
  "v_mfma_f32_16x16x32_bf16 v[48:51], a[4:7], a[140:143], v[48:51]\n\t"
  "v_add_f32 v86, v86, v94\n\t"
  "v_add_f32 v87, v87, v95\n\t"
  "v_mfma_f32_16x16x32_bf16 v[52:55], a[8:11], a[140:143], v[52:55]\n\t"
  "v_add_f32 v88, v88, v96\n\t"
  "v_add_f32 v89, v89, v97\n\t"
  "v_mfma_f32_16x16x32_bf16 v[56:59], a[12:15], a[140:143], v[56:59]\n\t"
  "s_waitcnt vmcnt(7)\n\t"
  "buffer_load_dwordx4 v[128:131], v1, %[srd], s44 offen offset:2048 sc1\n\t"
  "v_mfma_f32_16x16x32_bf16 v[40:43], a[16:19], a[144:147], v[40:43]\n\t"
  "v_add_f32 v90, v90, v98\n\t"
  "v_add_f32 v91, v91, v99\n\t"
  "v_mfma_f32_16x16x32_bf16 v[44:47], a[20:23], a[144:147], v[44:47]\n\t"
  "v_add_f32 v68, v68, v84\n\t"
  "v_add_f32 v69, v69, v85\n\t"
  "v_mfma_f32_16x16x32_bf16 v[48:51], a[24:27], a[144:147], v[48:51]\n\t"
  "buffer_load_dwordx4 a[240:243], v1, %[srd], s44 offen offset:3072 sc1\n\t"
  "v_add_f32 v70, v70, v86\n\t"
  "v_mfma_f32_16x16x32_bf16 v[52:55], a[28:31], a[144:147], v[52:55]\n\t"
  "v_add_f32 v71, v71, v87\n\t"
  "v_add_f32 v72, v72, v88\n\t"
  "v_mfma_f32_16x16x32_bf16 v[56:59], a[32:35], a[144:147], v[56:59]\n\t"
  "v_add_f32 v73, v73, v89\n\t"
  "v_add_f32 v74, v74, v90\n\t"
  "v_mfma_f32_16x16x32_bf16 v[40:43], a[36:39], a[148:151], v[40:43]\n\t"
  "v_add_f32 v75, v75, v91\n\t"
  "v_fma_f32 v68, v68, v64, v100\n\t"
  "v_mfma_f32_16x16x32_bf16 v[44:47], a[40:43], a[148:151], v[44:47]\n\t"
  "s_add_u32 s44, %[sH2ld], 3153920\n\t"
  "v_fma_f32 v69, v69, v65, v101\n\t"
  "v_mfma_f32_16x16x32_bf16 v[48:51], a[44:47], a[148:151], v[48:51]\n\t"
  "v_fma_f32 v70, v70, v66, v102\n\t"
  "v_fma_f32 v71, v71, v67, v103\n\t"
  "v_mfma_f32_16x16x32_bf16 v[52:55], a[48:51], a[148:151], v[52:55]\n\t"
  "v_fma_f32 v72, v72, v108, v104\n\t"
  "v_fma_f32 v73, v73, v109, v105\n\t"
  "v_mfma_f32_16x16x32_bf16 v[56:59], a[52:55], a[148:151], v[56:59]\n\t"
  "s_waitcnt vmcnt(7)\n\t"
  "buffer_load_dwordx4 v[248:251], v1, %[srd], s44 offen offset:0 sc1\n\t"
  "v_mfma_f32_16x16x32_bf16 v[20:23], v[144:147], a[152:155], 0\n\t"
  "v_fma_f32 v74, v74, v110, v106\n\t"
  "v_fma_f32 v75, v75, v111, v107\n\t"
  "v_mfma_f32_16x16x32_bf16 v[24:27], v[148:151], a[152:155], 0\n\t"
  "v_cvt_pk_bf16_f32 v76, v68, v69\n\t"
  "v_cvt_pk_bf16_f32 v77, v70, v71\n\t"
  "v_mfma_f32_16x16x32_bf16 v[28:31], v[152:155], a[152:155], 0\n\t"
  "buffer_load_dwordx4 v[252:255], v1, %[srd], s44 offen offset:1024 sc1\n\t"
  "v_cvt_pk_bf16_f32 v78, v72, v73\n\t"
  "v_mfma_f32_16x16x32_bf16 v[32:35], v[156:159], a[152:155], 0\n\t"
  "v_cvt_pk_bf16_f32 v79, v74, v75\n\t"
  "s_mov_b32 s55, 0\n\t"
  "v_mfma_f32_16x16x32_bf16 v[36:39], v[160:163], a[152:155], 0\n\t"
  "buffer_store_dwordx4 v[76:79], v16, %[srdo], s55 offen\n\t"
  "ds_write_b128 v13, v[40:43] offset:5120\n\t"
  "v_mfma_f32_16x16x32_bf16 v[20:23], v[164:167], a[156:159], v[20:23]\n\t"
  "buffer_load_dwordx4 a[56:59], v1, %[srd], s44 offen offset:2048 sc1\n\t"
  "v_mfma_f32_16x16x32_bf16 v[24:27], v[168:171], a[156:159], v[24:27]\n\t"
  "v_mfma_f32_16x16x32_bf16 v[28:31], v[172:175], a[156:159], v[28:31]\n\t"
  "ds_write_b128 v13, v[44:47] offset:6144\n\t"
  "v_mfma_f32_16x16x32_bf16 v[32:35], v[176:179], a[156:159], v[32:35]\n\t"
  "buffer_load_dwordx4 a[60:63], v1, %[srd], s44 offen offset:3072 sc1\n\t"
  "v_mfma_f32_16x16x32_bf16 v[36:39], v[180:183], a[156:159], v[36:39]\n\t"
  "s_waitcnt vmcnt(9)\n\t"
  "v_mfma_f32_16x16x32_bf16 v[20:23], v[184:187], v[112:115], v[20:23]\n\t"
  "ds_write_b128 v13, v[48:51] offset:7168\n\t"
  "v_mfma_f32_16x16x32_bf16 v[24:27], v[188:191], v[112:115], v[24:27]\n\t"
  "v_mfma_f32_16x16x32_bf16 v[28:31], v[192:195], v[112:115], v[28:31]\n\t"
  "v_mfma_f32_16x16x32_bf16 v[32:35], v[196:199], v[112:115], v[32:35]\n\t"
  "ds_write_b128 v13, v[52:55] offset:8192\n\t"
  "v_mfma_f32_16x16x32_bf16 v[36:39], v[200:203], v[112:115], v[36:39]\n\t"
  "v_mfma_f32_16x16x32_bf16 v[20:23], v[204:207], v[116:119], v[20:23]\n\t"
  "v_mfma_f32_16x16x32_bf16 v[24:27], v[208:211], v[116:119], v[24:27]\n\t"
  "ds_write_b128 v13, v[56:59] offset:9216\n\t"
  "v_mfma_f32_16x16x32_bf16 v[28:31], v[212:215], v[116:119], v[28:31]\n\t"
  "v_mfma_f32_16x16x32_bf16 v[32:35], v[216:219], v[116:119], v[32:35]\n\t"
  "v_mfma_f32_16x16x32_bf16 v[36:39], v[220:223], v[116:119], v[36:39]\n\t"
  "s_waitcnt vmcnt(7)\n\t"
  "v_mfma_f32_16x16x32_bf16 v[20:23], v[224:227], v[120:123], v[20:23]\n\t"
  "v_mfma_f32_16x16x32_bf16 v[24:27], v[228:231], v[120:123], v[24:27]\n\t"
  "v_cmp_ne_u64 vcc, v[132:133], v[138:139]\n\t"
  "v_mfma_f32_16x16x32_bf16 v[28:31], v[232:235], v[120:123], v[28:31]\n\t"
  "v_mfma_f32_16x16x32_bf16 v[32:35], v[236:239], v[120:123], v[32:35]\n\t"
  "s_cbranch_vccnz Lfl3_slow%=\n\t"
  "Lfl3_back%=:\n\t"
  "v_mfma_f32_16x16x32_bf16 v[36:39], v[240:243], v[120:123], v[36:39]\n\t"
  "s_add_u32 s45, %[sH2ld], 3244032\n\t"
  "s_waitcnt lgkmcnt(0)\n\t"
  "s_barrier\n\t"
  "v_mfma_f32_16x16x32_bf16 v[20:23], v[244:247], v[124:127], v[20:23]\n\t"
  "ds_read_b128 v[68:71], v14 offset:5120\n\t"
  "ds_read_b128 v[72:75], v15 offset:5120\n\t"
  "v_mfma_f32_16x16x32_bf16 v[24:27], a[160:163], v[124:127], v[24:27]\n\t"
  "buffer_load_dwordx4 a[104:107], v1, %[srd], s45 offen offset:0 sc1\n\t"
  "ds_read_b128 v[76:79], v14 offset:25600\n\t"
  "v_mfma_f32_16x16x32_bf16 v[28:31], a[164:167], v[124:127], v[28:31]\n\t"
  "ds_read_b128 v[80:83], v15 offset:25600\n\t"
  "ds_read_b128 v[84:87], v14 offset:46080\n\t"
  "v_mfma_f32_16x16x32_bf16 v[32:35], a[168:171], v[124:127], v[32:35]\n\t"
  "buffer_load_dwordx4 a[108:111], v1, %[srd], s45 offen offset:1024 sc1\n\t"
  "ds_read_b128 v[88:91], v15 offset:46080\n\t"
  "v_mfma_f32_16x16x32_bf16 v[36:39], a[172:175], v[124:127], v[36:39]\n\t"
  "s_waitcnt vmcnt(7)\n\t"
  "ds_read_b128 v[92:95], v60 offset:5120\n\t"
  "v_mfma_f32_16x16x32_bf16 v[20:23], a[176:179], v[128:131], v[20:23]\n\t"
  "ds_read_b128 v[96:99], v61 offset:5120\n\t"
  "ds_read_b128 v[100:103], v60 offset:25600\n\t"
  "v_mfma_f32_16x16x32_bf16 v[24:27], a[180:183], v[128:131], v[24:27]\n\t"
  "buffer_load_dwordx4 a[64:67], v1, %[srd], s45 offen offset:2048 sc1\n\t"
  "ds_read_b128 v[104:107], v61 offset:25600\n\t"
  "v_mfma_f32_16x16x32_bf16 v[28:31], a[184:187], v[128:131], v[28:31]\n\t"
  "v_mfma_f32_16x16x32_bf16 v[32:35], a[188:191], v[128:131], v[32:35]\n\t"
  "buffer_load_dwordx4 a[68:71], v1, %[srd], s45 offen offset:3072 sc1\n\t"
  "v_mfma_f32_16x16x32_bf16 v[36:39], a[192:195], v[128:131], v[36:39]\n\t"
  "v_mfma_f32_16x16x32_bf16 v[20:23], a[196:199], a[240:243], v[20:23]\n\t"
  "v_mfma_f32_16x16x32_bf16 v[24:27], a[200:203], a[240:243], v[24:27]\n\t"
  "s_add_u32 s45, %[sH2ld], 3248128\n\t"
  "v_mfma_f32_16x16x32_bf16 v[28:31], a[204:207], a[240:243], v[28:31]\n\t"
  "v_mfma_f32_16x16x32_bf16 v[32:35], a[208:211], a[240:243], v[32:35]\n\t"
  "buffer_load_dwordx4 a[72:75], v1, %[srd], s45 offen offset:0 sc1\n\t"
  "v_mfma_f32_16x16x32_bf16 v[36:39], a[212:215], a[240:243], v[36:39]\n\t"
  "s_waitcnt vmcnt(8)\n\t"
  "s_waitcnt lgkmcnt(0)\n\t"
  "v_mfma_f32_16x16x32_bf16 v[20:23], a[216:219], v[248:251], v[20:23]\n\t"
  "buffer_load_dwordx4 a[76:79], v1, %[srd], s45 offen offset:1024 sc1\n\t"
  "v_add_f32 v68, v68, v76\n\t"
  "v_mfma_f32_16x16x32_bf16 v[24:27], a[220:223], v[248:251], v[24:27]\n\t"
  "v_add_f32 v69, v69, v77\n\t"
  "v_add_f32 v70, v70, v78\n\t"
  "v_mfma_f32_16x16x32_bf16 v[28:31], a[224:227], v[248:251], v[28:31]\n\t"
  "v_add_f32 v71, v71, v79\n\t"
  "v_add_f32 v72, v72, v80\n\t"
  "v_mfma_f32_16x16x32_bf16 v[32:35], a[228:231], v[248:251], v[32:35]\n\t"
  "buffer_load_dwordx4 a[80:83], v1, %[srd], s45 offen offset:2048 sc1\n\t"
  "v_add_f32 v73, v73, v81\n\t"
  "v_mfma_f32_16x16x32_bf16 v[36:39], a[232:235], v[248:251], v[36:39]\n\t"
  "v_add_f32 v74, v74, v82\n\t"
  "v_add_f32 v75, v75, v83\n\t"
  "v_mfma_f32_16x16x32_bf16 v[20:23], a[236:239], v[252:255], v[20:23]\n\t"
  "buffer_load_dwordx4 a[84:87], v1, %[srd], s45 offen offset:3072 sc1\n\t"
  "v_add_f32 v84, v84, v92\n\t"
  "v_mfma_f32_16x16x32_bf16 v[24:27], a[0:3], v[252:255], v[24:27]\n\t"
  "v_add_f32 v85, v85, v93\n\t"
  "v_add_f32 v86, v86, v94\n\t"
  "v_mfma_f32_16x16x32_bf16 v[28:31], a[4:7], v[252:255], v[28:31]\n\t"
  "v_add_f32 v87, v87, v95\n\t"
  "v_add_f32 v88, v88, v96\n\t"
  "v_mfma_f32_16x16x32_bf16 v[32:35], a[8:11], v[252:255], v[32:35]\n\t"
  "s_add_u32 s45, %[sH2ld], 3252224\n\t"
  "v_add_f32 v89, v89, v97\n\t"
  "v_mfma_f32_16x16x32_bf16 v[36:39], a[12:15], v[252:255], v[36:39]\n\t"
  "s_waitcnt vmcnt(8)\n\t"
  "v_add_f32 v90, v90, v98\n\t"
  "v_mfma_f32_16x16x32_bf16 v[20:23], a[16:19], a[56:59], v[20:23]\n\t"
  "buffer_load_dwordx4 a[88:91], v1, %[srd], s45 offen offset:0 sc1\n\t"
  "v_add_f32 v91, v91, v99\n\t"
  "v_mfma_f32_16x16x32_bf16 v[24:27], a[20:23], a[56:59], v[24:27]\n\t"
  "v_add_f32 v68, v68, v84\n\t"
  "v_add_f32 v69, v69, v85\n\t"
  "v_mfma_f32_16x16x32_bf16 v[28:31], a[24:27], a[56:59], v[28:31]\n\t"
  "buffer_load_dwordx4 a[92:95], v1, %[srd], s45 offen offset:1024 sc1\n\t"
  "v_add_f32 v70, v70, v86\n\t"
  "v_mfma_f32_16x16x32_bf16 v[32:35], a[28:31], a[56:59], v[32:35]\n\t"
  "v_add_f32 v71, v71, v87\n\t"
  "v_add_f32 v72, v72, v88\n\t"
  "v_mfma_f32_16x16x32_bf16 v[36:39], a[32:35], a[56:59], v[36:39]\n\t"
  "v_add_f32 v73, v73, v89\n\t"
  "v_add_f32 v74, v74, v90\n\t"
  "v_mfma_f32_16x16x32_bf16 v[20:23], a[36:39], a[60:63], v[20:23]\n\t"
  "buffer_load_dwordx4 a[96:99], v1, %[srd], s45 offen offset:2048 sc1\n\t"
  "v_add_f32 v75, v75, v91\n\t"
  "v_mfma_f32_16x16x32_bf16 v[24:27], a[40:43], a[60:63], v[24:27]\n\t"
  "v_fma_f32 v68, v68, v64, v100\n\t"
  "v_fma_f32 v69, v69, v65, v101\n\t"
  "v_mfma_f32_16x16x32_bf16 v[28:31], a[44:47], a[60:63], v[28:31]\n\t"
  "buffer_load_dwordx4 a[100:103], v1, %[srd], s45 offen offset:3072 sc1\n\t"
  "v_fma_f32 v70, v70, v66, v102\n\t"
  "v_mfma_f32_16x16x32_bf16 v[32:35], a[48:51], a[60:63], v[32:35]\n\t"
  "v_fma_f32 v71, v71, v67, v103\n\t"
  "v_fma_f32 v72, v72, v108, v104\n\t"
  "v_mfma_f32_16x16x32_bf16 v[36:39], a[52:55], a[60:63], v[36:39]\n\t"
  "s_waitcnt vmcnt(10)\n\t"
  "v_fma_f32 v73, v73, v109, v105\n\t"
  "v_mfma_f32_16x16x32_bf16 v[40:43], v[144:147], a[104:107], 0\n\t"
  "v_fma_f32 v74, v74, v110, v106\n\t"
  "v_fma_f32 v75, v75, v111, v107\n\t"
  "v_mfma_f32_16x16x32_bf16 v[44:47], v[148:151], a[104:107], 0\n\t"
  "v_cvt_pk_bf16_f32 v76, v68, v69\n\t"
  "v_cvt_pk_bf16_f32 v77, v70, v71\n\t"
  "v_mfma_f32_16x16x32_bf16 v[48:51], v[152:155], a[104:107], 0\n\t"
  "v_cvt_pk_bf16_f32 v78, v72, v73\n\t"
  "v_cvt_pk_bf16_f32 v79, v74, v75\n\t"
  "v_mfma_f32_16x16x32_bf16 v[52:55], v[156:159], a[104:107], 0\n\t"
  "s_mov_b32 s55, 40960\n\t"
  "buffer_store_dwordx4 v[76:79], v16, %[srdo], s55 offen\n\t"
  "v_mfma_f32_16x16x32_bf16 v[56:59], v[160:163], a[104:107], 0\n\t"
  "ds_write_b128 v13, v[20:23] offset:10240\n\t"
  "v_mfma_f32_16x16x32_bf16 v[40:43], v[164:167], a[108:111], v[40:43]\n\t"
  "v_mfma_f32_16x16x32_bf16 v[44:47], v[168:171], a[108:111], v[44:47]\n\t"
  "v_mfma_f32_16x16x32_bf16 v[48:51], v[172:175], a[108:111], v[48:51]\n\t"
  "ds_write_b128 v13, v[24:27] offset:11264\n\t"
  "v_mfma_f32_16x16x32_bf16 v[52:55], v[176:179], a[108:111], v[52:55]\n\t"
  "v_mfma_f32_16x16x32_bf16 v[56:59], v[180:183], a[108:111], v[56:59]\n\t"
  "s_waitcnt vmcnt(9)\n\t"
  "v_mfma_f32_16x16x32_bf16 v[40:43], v[184:187], a[64:67], v[40:43]\n\t"
  "ds_write_b128 v13, v[28:31] offset:12288\n\t"
  "v_mfma_f32_16x16x32_bf16 v[44:47], v[188:191], a[64:67], v[44:47]\n\t"
  "v_mfma_f32_16x16x32_bf16 v[48:51], v[192:195], a[64:67], v[48:51]\n\t"
  "v_mfma_f32_16x16x32_bf16 v[52:55], v[196:199], a[64:67], v[52:55]\n\t"
  "ds_write_b128 v13, v[32:35] offset:13312\n\t"
  "v_mfma_f32_16x16x32_bf16 v[56:59], v[200:203], a[64:67], v[56:59]\n\t"
  "v_mfma_f32_16x16x32_bf16 v[40:43], v[204:207], a[68:71], v[40:43]\n\t"
  "v_mfma_f32_16x16x32_bf16 v[44:47], v[208:211], a[68:71], v[44:47]\n\t"
  "ds_write_b128 v13, v[36:39] offset:14336\n\t"
  "v_mfma_f32_16x16x32_bf16 v[48:51], v[212:215], a[68:71], v[48:51]\n\t"
  "v_mfma_f32_16x16x32_bf16 v[52:55], v[216:219], a[68:71], v[52:55]\n\t"
  "v_mfma_f32_16x16x32_bf16 v[56:59], v[220:223], a[68:71], v[56:59]\n\t"
  "s_waitcnt vmcnt(7)\n\t"
  "v_mfma_f32_16x16x32_bf16 v[40:43], v[224:227], a[72:75], v[40:43]\n\t"
  "v_mfma_f32_16x16x32_bf16 v[44:47], v[228:231], a[72:75], v[44:47]\n\t"
  "v_mfma_f32_16x16x32_bf16 v[48:51], v[232:235], a[72:75], v[48:51]\n\t"
  "v_mfma_f32_16x16x32_bf16 v[52:55], v[236:239], a[72:75], v[52:55]\n\t"
  "v_mfma_f32_16x16x32_bf16 v[56:59], v[240:243], a[72:75], v[56:59]\n\t"
  "s_waitcnt lgkmcnt(0)\n\t"
  "s_barrier\n\t"
  "v_mfma_f32_16x16x32_bf16 v[40:43], v[244:247], a[76:79], v[40:43]\n\t"
  "ds_read_b128 v[68:71], v14 offset:10240\n\t"
  "ds_read_b128 v[72:75], v15 offset:10240\n\t"
  "v_mfma_f32_16x16x32_bf16 v[44:47], a[160:163], a[76:79], v[44:47]\n\t"
  "ds_read_b128 v[76:79], v14 offset:30720\n\t"
  "ds_read_b128 v[80:83], v15 offset:30720\n\t"
  "v_mfma_f32_16x16x32_bf16 v[48:51], a[164:167], a[76:79], v[48:51]\n\t"
  "ds_read_b128 v[84:87], v14 offset:51200\n\t"
  "ds_read_b128 v[88:91], v15 offset:51200\n\t"
  "v_mfma_f32_16x16x32_bf16 v[52:55], a[168:171], a[76:79], v[52:55]\n\t"
  "ds_read_b128 v[92:95], v60 offset:10240\n\t"
  "ds_read_b128 v[96:99], v61 offset:10240\n\t"
  "v_mfma_f32_16x16x32_bf16 v[56:59], a[172:175], a[76:79], v[56:59]\n\t"
  "s_waitcnt vmcnt(5)\n\t"
  "ds_read_b128 v[100:103], v60 offset:30720\n\t"
  "v_mfma_f32_16x16x32_bf16 v[40:43], a[176:179], a[80:83], v[40:43]\n\t"
  "ds_read_b128 v[104:107], v61 offset:30720\n\t"
  "v_mfma_f32_16x16x32_bf16 v[44:47], a[180:183], a[80:83], v[44:47]\n\t"
  "v_mfma_f32_16x16x32_bf16 v[48:51], a[184:187], a[80:83], v[48:51]\n\t"
  "v_mfma_f32_16x16x32_bf16 v[52:55], a[188:191], a[80:83], v[52:55]\n\t"
  "v_mfma_f32_16x16x32_bf16 v[56:59], a[192:195], a[80:83], v[56:59]\n\t"
  "v_mfma_f32_16x16x32_bf16 v[40:43], a[196:199], a[84:87], v[40:43]\n\t"
  "v_mfma_f32_16x16x32_bf16 v[44:47], a[200:203], a[84:87], v[44:47]\n\t"
  "v_mfma_f32_16x16x32_bf16 v[48:51], a[204:207], a[84:87], v[48:51]\n\t"
  "v_mfma_f32_16x16x32_bf16 v[52:55], a[208:211], a[84:87], v[52:55]\n\t"
  "s_waitcnt lgkmcnt(0)\n\t"
  "v_add_f32 v68, v68, v76\n\t"
  "v_mfma_f32_16x16x32_bf16 v[56:59], a[212:215], a[84:87], v[56:59]\n\t"
  "s_waitcnt vmcnt(3)\n\t"
  "v_add_f32 v69, v69, v77\n\t"
  "v_mfma_f32_16x16x32_bf16 v[40:43], a[216:219], a[88:91], v[40:43]\n\t"
  "v_add_f32 v70, v70, v78\n\t"
  "v_add_f32 v71, v71, v79\n\t"
  "v_mfma_f32_16x16x32_bf16 v[40:43], a[236:239], a[92:95], v[40:43]\n\t"
  "v_add_f32 v72, v72, v80\n\t"
  "v_add_f32 v73, v73, v81\n\t"
  "s_waitcnt vmcnt(2)\n\t"
  "v_mfma_f32_16x16x32_bf16 v[40:43], a[16:19], a[96:99], v[40:43]\n\t"
  "v_add_f32 v74, v74, v82\n\t"
  "v_add_f32 v75, v75, v83\n\t"
  "s_waitcnt vmcnt(1)\n\t"
  "v_mfma_f32_16x16x32_bf16 v[40:43], a[36:39], a[100:103], v[40:43]\n\t"
  "v_add_f32 v84, v84, v92\n\t"
  "v_add_f32 v85, v85, v93\n\t"
  "v_mfma_f32_16x16x32_bf16 v[44:47], a[220:223], a[88:91], v[44:47]\n\t"
  "v_add_f32 v86, v86, v94\n\t"
  "v_add_f32 v87, v87, v95\n\t"
  "v_mfma_f32_16x16x32_bf16 v[44:47], a[0:3], a[92:95], v[44:47]\n\t"
  "v_add_f32 v88, v88, v96\n\t"
  "v_add_f32 v89, v89, v97\n\t"
  "v_mfma_f32_16x16x32_bf16 v[44:47], a[20:23], a[96:99], v[44:47]\n\t"
  "v_add_f32 v90, v90, v98\n\t"
  "v_add_f32 v91, v91, v99\n\t"
  "v_mfma_f32_16x16x32_bf16 v[44:47], a[40:43], a[100:103], v[44:47]\n\t"
  "v_add_f32 v68, v68, v84\n\t"
  "v_add_f32 v69, v69, v85\n\t"
  "v_mfma_f32_16x16x32_bf16 v[48:51], a[224:227], a[88:91], v[48:51]\n\t"
  "v_add_f32 v70, v70, v86\n\t"
  "v_add_f32 v71, v71, v87\n\t"
  "v_mfma_f32_16x16x32_bf16 v[48:51], a[4:7], a[92:95], v[48:51]\n\t"
  "ds_write_b128 v13, v[40:43] offset:15360\n\t"
  "v_add_f32 v72, v72, v88\n\t"
  "v_mfma_f32_16x16x32_bf16 v[48:51], a[24:27], a[96:99], v[48:51]\n\t"
  "v_add_f32 v73, v73, v89\n\t"
  "v_add_f32 v74, v74, v90\n\t"
  "v_mfma_f32_16x16x32_bf16 v[48:51], a[44:47], a[100:103], v[48:51]\n\t"
  "v_add_f32 v75, v75, v91\n\t"
  "v_fma_f32 v68, v68, v64, v100\n\t"
  "v_mfma_f32_16x16x32_bf16 v[52:55], a[228:231], a[88:91], v[52:55]\n\t"
  "v_fma_f32 v69, v69, v65, v101\n\t"
  "v_fma_f32 v70, v70, v66, v102\n\t"
  "v_mfma_f32_16x16x32_bf16 v[52:55], a[8:11], a[92:95], v[52:55]\n\t"
  "ds_write_b128 v13, v[44:47] offset:16384\n\t"
  "v_fma_f32 v71, v71, v67, v103\n\t"
  "v_mfma_f32_16x16x32_bf16 v[52:55], a[28:31], a[96:99], v[52:55]\n\t"
  "v_fma_f32 v72, v72, v108, v104\n\t"
  "v_fma_f32 v73, v73, v109, v105\n\t"
  "v_mfma_f32_16x16x32_bf16 v[52:55], a[48:51], a[100:103], v[52:55]\n\t"
  "v_fma_f32 v74, v74, v110, v106\n\t"
  "v_fma_f32 v75, v75, v111, v107\n\t"
  "v_mfma_f32_16x16x32_bf16 v[56:59], a[232:235], a[88:91], v[56:59]\n\t"
  "v_cvt_pk_bf16_f32 v76, v68, v69\n\t"
  "v_cvt_pk_bf16_f32 v77, v70, v71\n\t"
  "v_mfma_f32_16x16x32_bf16 v[56:59], a[12:15], a[92:95], v[56:59]\n\t"
  "ds_write_b128 v13, v[48:51] offset:17408\n\t"
  "v_cvt_pk_bf16_f32 v78, v72, v73\n\t"
  "v_mfma_f32_16x16x32_bf16 v[56:59], a[32:35], a[96:99], v[56:59]\n\t"
  "v_cvt_pk_bf16_f32 v79, v74, v75\n\t"
  "s_mov_b32 s55, 1310720\n\t"
  "v_mfma_f32_16x16x32_bf16 v[56:59], a[52:55], a[100:103], v[56:59]\n\t"
  "buffer_store_dwordx4 v[76:79], v16, %[srdo], s55 offen\n\t"
  "ds_write_b128 v13, v[52:55] offset:18432\n\t"
  "s_nop 5\n\t"
  "ds_write_b128 v13, v[56:59] offset:19456\n\t"
  "buffer_store_dwordx2 v[132:133], v18, %[srd], 0 offen sc1\n\t"
  "s_waitcnt lgkmcnt(0)\n\t"
  "s_barrier\n\t"
  "ds_read_b128 v[68:71], v14 offset:15360\n\t"
  "ds_read_b128 v[72:75], v15 offset:15360\n\t"
  "ds_read_b128 v[76:79], v14 offset:35840\n\t"
  "ds_read_b128 v[80:83], v15 offset:35840\n\t"
  "ds_read_b128 v[84:87], v14 offset:56320\n\t"
  "ds_read_b128 v[88:91], v15 offset:56320\n\t"
  "ds_read_b128 v[92:95], v60 offset:15360\n\t"
  "ds_read_b128 v[96:99], v61 offset:15360\n\t"
  "ds_read_b128 v[100:103], v60 offset:35840\n\t"
  "ds_read_b128 v[104:107], v61 offset:35840\n\t"
  "s_waitcnt lgkmcnt(7)\n\t"
  "v_pk_add_f32 v[68:69], v[68:69], v[76:77]\n\t"
  "v_pk_add_f32 v[70:71], v[70:71], v[78:79]\n\t"
  "s_waitcnt lgkmcnt(6)\n\t"
  "v_pk_add_f32 v[72:73], v[72:73], v[80:81]\n\t"
  "v_pk_add_f32 v[74:75], v[74:75], v[82:83]\n\t"
  "s_waitcnt lgkmcnt(3)\n\t"
  "v_pk_add_f32 v[84:85], v[84:85], v[92:93]\n\t"
  "v_pk_add_f32 v[86:87], v[86:87], v[94:95]\n\t"
  "s_waitcnt lgkmcnt(2)\n\t"
  "v_pk_add_f32 v[88:89], v[88:89], v[96:97]\n\t"
  "v_pk_add_f32 v[90:91], v[90:91], v[98:99]\n\t"
  "v_pk_add_f32 v[68:69], v[68:69], v[84:85]\n\t"
  "v_pk_add_f32 v[70:71], v[70:71], v[86:87]\n\t"
  "v_pk_add_f32 v[72:73], v[72:73], v[88:89]\n\t"
  "v_pk_add_f32 v[74:75], v[74:75], v[90:91]\n\t"
  "s_waitcnt lgkmcnt(1)\n\t"
  "v_pk_fma_f32 v[68:69], v[68:69], v[64:65], v[100:101]\n\t"
  "v_pk_fma_f32 v[70:71], v[70:71], v[66:67], v[102:103]\n\t"
  "s_waitcnt lgkmcnt(0)\n\t"
  "v_pk_fma_f32 v[72:73], v[72:73], v[108:109], v[104:105]\n\t"
  "v_pk_fma_f32 v[74:75], v[74:75], v[110:111], v[106:107]\n\t"
  "v_cvt_pk_bf16_f32 v76, v68, v69\n\t"
  "v_cvt_pk_bf16_f32 v77, v70, v71\n\t"
  "v_cvt_pk_bf16_f32 v78, v72, v73\n\t"
  "v_cvt_pk_bf16_f32 v79, v74, v75\n\t"
  "s_mov_b32 s55, 1351680\n\t"
  "buffer_store_dwordx4 v[76:79], v16, %[srdo], s55 offen\n\t"
  "s_branch Lfend%=\n\t"
  "Lst0_slow%=:\n\t"
  "s_nop 7\n\t"
  "s_nop 3\n\t"
  "s_mov_b32 s49, 0x40000\n\t"
  "Lst0_loop%=:\n\t"
  "s_sleep 2\n\t"
  "buffer_load_dwordx2 v[92:93], v136, %[srd], %[sST] offen offset:0 sc1\n\t"
  "buffer_load_dwordx2 v[94:95], v136, %[srd], %[sST] offen offset:1024 sc1\n\t"
  "buffer_load_dwordx2 v[96:97], v136, %[srd], %[sST] offen offset:2048 sc1\n\t"
  "buffer_load_dwordx2 v[98:99], v136, %[srd], %[sST] offen offset:3072 sc1\n\t"
  "s_waitcnt vmcnt(0)\n\t"
  "v_min_u32 v131, v93, v95\n\t"
  "v_min3_u32 v131, v131, v97, v99\n\t"
  "v_cmp_eq_u32 vcc, 0, v131\n\t"
  "s_nop 3\n\t"
  "s_cmp_eq_u64 vcc, 0\n\t"
  "s_cbranch_scc1 Lst0_back%=\n\t"
  "s_sub_u32 s49, s49, 1\n\t"
  "s_cmp_lg_u32 s49, 0\n\t"
  "s_cbranch_scc1 Lst0_loop%=\n\t"
  "s_branch Lst0_back%=\n\t"
  "Lw2_slow%=:\n\t"
  "s_nop 7\n\t"
  "s_nop 3\n\t"
  "s_mov_b32 s49, 0x40000\n\t"
  "Lw2_loop%=:\n\t"
  "s_sleep 2\n\t"
  "buffer_load_dwordx2 v[138:139], v2, %[srd], 0 offen sc1\n\t"
  "s_waitcnt vmcnt(0)\n\t"
  "v_cmp_ne_u64 vcc, v[132:133], v[138:139]\n\t"
  "s_nop 3\n\t"
  "s_cmp_eq_u64 vcc, 0\n\t"
  "s_cbranch_scc1 Lw2_back%=\n\t"
  "s_sub_u32 s49, s49, 1\n\t"
  "s_cmp_lg_u32 s49, 0\n\t"
  "s_cbranch_scc1 Lw2_loop%=\n\t"
  "s_branch Lw2_back%=\n\t"
  "Lst1_slow%=:\n\t"
  "s_nop 7\n\t"
  "s_nop 3\n\t"
  "s_waitcnt vmcnt(0)\n\t"
  "s_movk_i32 s59, 8192\n\t"
  "buffer_store_dwordx4 v[132:135], v9, %[srd], s59 offen sc1\n\t"
  "s_mov_b32 s49, 0x40000\n\t"
  "Lst1_loop%=:\n\t"
  "s_sleep 2\n\t"
  "buffer_load_dwordx2 v[100:101], v136, %[srd], s54 offen offset:0 sc1\n\t"
  "buffer_load_dwordx2 v[102:103], v136, %[srd], s54 offen offset:1024 sc1\n\t"
  "buffer_load_dwordx2 v[104:105], v136, %[srd], s54 offen offset:2048 sc1\n\t"
  "buffer_load_dwordx2 v[106:107], v136, %[srd], s54 offen offset:3072 sc1\n\t"
  "s_waitcnt vmcnt(0)\n\t"
  "v_min_u32 v131, v101, v103\n\t"
  "v_min3_u32 v131, v131, v105, v107\n\t"
  "v_cmp_eq_u32 vcc, 0, v131\n\t"
  "s_nop 3\n\t"
  "s_cmp_eq_u64 vcc, 0\n\t"
  "s_cbranch_scc1 Lst1_back%=\n\t"
  "s_sub_u32 s49, s49, 1\n\t"
  "s_cmp_lg_u32 s49, 0\n\t"
  "s_cbranch_scc1 Lst1_loop%=\n\t"
  "s_branch Lst1_back%=\n\t"
  "Lfl0_slow%=:\n\t"
  "s_nop 7\n\t"
  "s_nop 3\n\t"
  "s_waitcnt vmcnt(0)\n\t"
  "s_movk_i32 s59, 16384\n\t"
  "buffer_store_dwordx4 v[132:135], v9, %[srd], s59 offen sc1\n\t"
  "s_mov_b32 s49, 0x40000\n\t"
  "Lfl0_loop%=:\n\t"
  "s_sleep 2\n\t"
  "s_add_u32 s57, %[sFL], 0\n\t"
  "buffer_load_dwordx2 v[138:139], v137, %[srd], s57 offen sc1\n\t"
  "s_waitcnt vmcnt(0)\n\t"
  "v_cmp_ne_u64 vcc, v[132:133], v[138:139]\n\t"
  "s_nop 3\n\t"
  "s_cmp_eq_u64 vcc, 0\n\t"
  "s_cbranch_scc1 Lfl0_back%=\n\t"
  "s_sub_u32 s49, s49, 1\n\t"
  "s_cmp_lg_u32 s49, 0\n\t"
  "s_cbranch_scc1 Lfl0_loop%=\n\t"
  "s_branch Lfl0_back%=\n\t"
  "Lfl1_slow%=:\n\t"
  "s_nop 7\n\t"
  "s_nop 3\n\t"
  "s_waitcnt vmcnt(0)\n\t"
  "s_movk_i32 s59, 24576\n\t"
  "buffer_store_dwordx4 v[132:135], v9, %[srd], s59 offen sc1\n\t"
  "s_mov_b32 s49, 0x40000\n\t"
  "Lfl1_loop%=:\n\t"
  "s_sleep 2\n\t"
  "s_add_u32 s57, %[sFL], 8192\n\t"
  "buffer_load_dwordx2 v[138:139], v137, %[srd], s57 offen sc1\n\t"
  "s_waitcnt vmcnt(0)\n\t"
  "v_cmp_ne_u64 vcc, v[132:133], v[138:139]\n\t"
  "s_nop 3\n\t"
  "s_cmp_eq_u64 vcc, 0\n\t"
  "s_cbranch_scc1 Lfl1_back%=\n\t"
  "s_sub_u32 s49, s49, 1\n\t"
  "s_cmp_lg_u32 s49, 0\n\t"
  "s_cbranch_scc1 Lfl1_loop%=\n\t"
  "s_branch Lfl1_back%=\n\t"
  "Lfl2_slow%=:\n\t"
  "s_nop 7\n\t"
  "s_nop 3\n\t"
  "s_mov_b32 s49, 0x40000\n\t"
  "Lfl2_loop%=:\n\t"
  "s_sleep 2\n\t"
  "s_add_u32 s57, %[sFL], 16384\n\t"
  "buffer_load_dwordx2 v[138:139], v137, %[srd], s57 offen sc1\n\t"
  "s_waitcnt vmcnt(0)\n\t"
  "v_cmp_ne_u64 vcc, v[132:133], v[138:139]\n\t"
  "s_nop 3\n\t"
  "s_cmp_eq_u64 vcc, 0\n\t"
  "s_cbranch_scc1 Lfl2_back%=\n\t"
  "s_sub_u32 s49, s49, 1\n\t"
  "s_cmp_lg_u32 s49, 0\n\t"
  "s_cbranch_scc1 Lfl2_loop%=\n\t"
  "s_branch Lfl2_back%=\n\t"
  "Lfl3_slow%=:\n\t"
  "s_nop 7\n\t"
  "s_nop 3\n\t"
  "s_mov_b32 s49, 0x40000\n\t"
  "Lfl3_loop%=:\n\t"
  "s_sleep 2\n\t"
  "s_add_u32 s57, %[sFL], 24576\n\t"
  "buffer_load_dwordx2 v[138:139], v137, %[srd], s57 offen sc1\n\t"
  "s_waitcnt vmcnt(0)\n\t"
  "v_cmp_ne_u64 vcc, v[132:133], v[138:139]\n\t"
  "s_nop 3\n\t"
  "s_cmp_eq_u64 vcc, 0\n\t"
  "s_cbranch_scc1 Lfl3_back%=\n\t"
  "s_sub_u32 s49, s49, 1\n\t"
  "s_cmp_lg_u32 s49, 0\n\t"
  "s_cbranch_scc1 Lfl3_loop%=\n\t"
  "s_branch Lfl3_back%=\n\t"
  "Lfend%=:\n\t"
  ".Lk_end:\n\t"
    :
    : [srd] "s"(srd), [srdo] "s"(srdo), [w] "s"(w), [ws] "s"(ws),
      [tlo] "s"((u32)tag), [thi] "s"((u32)(tag >> 32)), [sW1] "s"(OFF_W1F + (j * 4 + w) * 61440u), [sW2] "s"(OFF_W2F + (ng * 4 + w) * 61440u),
      [sUB] "s"(OFF_UB + (j * 4 + w) * 1536u),
      [sH2ld] "s"(OFF_H2F + ((4 * p + sub) * 48 + 12 * w) * 1024u), [sST] "s"(OFF_ST2 + p * 4096u), [sFL] "s"(OFF_FL + p * 32768u + w * 2048u),
      [sRT] "s"(OFF_RT + ng * 320u), [wdone] "s"(wdone), [pm] "s"(pmask)
    : "memory", "vcc", "scc",
"a0", "a1", "a2", "a3", "a4", "a5", "a6", "a7", "a8", "a9", "a10", "a11", "a12", "a13", "a14", "a15", "a16", "a17", "a18", "a19", "a20", "a21", "a22", "a23", "a24", "a25", "a26", "a27", "a28", "a29", "a30", "a31", "a32", "a33", "a34", "a35", "a36", "a37", "a38", "a39", "a40", "a41", "a42", "a43", "a44", "a45", "a46", "a47", "a48", "a49", "a50", "a51", "a52", "a53", "a54", "a55", "a56", "a57", "a58", "a59", "a60", "a61", "a62", "a63", "a64", "a65", "a66", "a67", "a68", "a69", "a70", "a71", "a72", "a73", "a74", "a75", "a76", "a77", "a78", "a79", "a80", "a81", "a82", "a83", "a84", "a85", "a86", "a87", "a88", "a89", "a90", "a91", "a92", "a93", "a94", "a95", "a96", "a97", "a98", "a99", "a100", "a101", "a102", "a103", "a104", "a105", "a106", "a107", "a108", "a109", "a110", "a111", "a112", "a113", "a114", "a115", "a116", "a117", "a118", "a119", "a120", "a121", "a122", "a123", "a124", "a125", "a126", "a127", "a128", "a129", "a130", "a131", "a132", "a133", "a134", "a135", "a136", "a137", "a138", "a139", "a140", "a141", "a142", "a143", "a144", "a145", "a146", "a147", "a148", "a149", "a150", "a151", "a152", "a153", "a154", "a155", "a156", "a157", "a158", "a159", "a160", "a161", "a162", "a163", "a164", "a165", "a166", "a167", "a168", "a169", "a170", "a171", "a172", "a173", "a174", "a175", "a176", "a177", "a178", "a179", "a180", "a181", "a182", "a183", "a184", "a185", "a186", "a187", "a188", "a189", "a190", "a191", "a192", "a193", "a194", "a195", "a196", "a197", "a198", "a199", "a200", "a201", "a202", "a203", "a204", "a205", "a206", "a207", "a208", "a209", "a210", "a211", "a212", "a213", "a214", "a215", "a216", "a217", "a218", "a219", "a220", "a221", "a222", "a223", "a224", "a225", "a226", "a227", "a228", "a229", "a230", "a231", "a232", "a233", "a234", "a235", "a236", "a237", "a238", "a239", "a240", "a241", "a242", "a243", "a244", "a245", "a246", "a247", "a248", "a249", "a250", "a251", "a252", "a253", "a254", "a255", "s44", "s45", "s46", "s47", "s48", "s49", "s52", "s54", "s55", "s56", "s57", "s58", "s59", "s60", "s61", "s62", "s63", "s68", "s69", "v0", "v1", "v2", "v3", "v4", "v5", "v6", "v7", "v8", "v9", "v10", "v11", "v12", "v13", "v14", "v15", "v16", "v17", "v18", "v19", "v20", "v21", "v22", "v23", "v24", "v25", "v26", "v27", "v28", "v29", "v30", "v31", "v32", "v33", "v34", "v35", "v36", "v37", "v38", "v39", "v40", "v41", "v42", "v43", "v44", "v45", "v46", "v47", "v48", "v49", "v50", "v51", "v52", "v53", "v54", "v55", "v56", "v57", "v58", "v59", "v60", "v61", "v62", "v63", "v64", "v65", "v66", "v67", "v68", "v69", "v70", "v71", "v72", "v73", "v74", "v75", "v76", "v77", "v78", "v79", "v80", "v81", "v82", "v83", "v84", "v85", "v86", "v87", "v88", "v89", "v90", "v91", "v92", "v93", "v94", "v95", "v96", "v97", "v98", "v99", "v100", "v101", "v102", "v103", "v104", "v105", "v106", "v107", "v108", "v109", "v110", "v111", "v112", "v113", "v114", "v115", "v116", "v117", "v118", "v119", "v120", "v121", "v122", "v123", "v124", "v125", "v126", "v127", "v128", "v129", "v130", "v131", "v132", "v133", "v134", "v135", "v136", "v137", "v138", "v139", "v141", "v142", "v143", "v144", "v145", "v146", "v147", "v148", "v149", "v150", "v151", "v152", "v153", "v154", "v155", "v156", "v157", "v158", "v159", "v160", "v161", "v162", "v163", "v164", "v165", "v166", "v167", "v168", "v169", "v170", "v171", "v172", "v173", "v174", "v175", "v176", "v177", "v178", "v179", "v180", "v181", "v182", "v183", "v184", "v185", "v186", "v187", "v188", "v189", "v190", "v191", "v192", "v193", "v194", "v195", "v196", "v197", "v198", "v199", "v200", "v201", "v202", "v203", "v204", "v205", "v206", "v207", "v208", "v209", "v210", "v211", "v212", "v213", "v214", "v215", "v216", "v217", "v218", "v219", "v220", "v221", "v222", "v223", "v224", "v225", "v226", "v227", "v228", "v229", "v230", "v231", "v232", "v233", "v234", "v235", "v236", "v237", "v238", "v239", "v240", "v241", "v242", "v243", "v244", "v245", "v246", "v247", "v248", "v249", "v250", "v251", "v252", "v253", "v254", "v255"
  );
}

extern "C" void kernel_launch(void* const* d_in, const int* in_sizes, int n_in, void* d_out, int out_size,
                              void* d_ws, size_t ws_size, hipStream_t stream) {
  k_main<<<256, 256, 0, stream>>>((const u16*)d_in[0], (const u16*)d_in[1], (const u16*)d_in[2], (const u16*)d_in[3], (const u16*)d_in[4],
                                  (const u16*)d_in[5], (const u16*)d_in[7], (const u16*)d_in[8], (const u16*)d_in[9], (const u16*)d_in[10],
                                  (const u16*)d_in[11], (char*)d_ws, (u16*)d_out);
}
```

```cpp
#include <hip/hip_runtime.h>
#include <stdint.h>

typedef unsigned short u16;
typedef unsigned int u32;
typedef unsigned long long u64;
typedef __attribute__((ext_vector_type(8))) __bf16 bf16x8;
typedef __attribute__((ext_vector_type(2))) __bf16 bf16x2;
typedef __attribute__((ext_vector_type(4))) short s16x4;
typedef __attribute__((ext_vector_type(4))) float f32x4;
typedef __attribute__((ext_vector_type(2))) float f32x2;
typedef __attribute__((ext_vector_type(4))) u32 u32x4;
typedef __attribute__((ext_vector_type(2))) u32 u32x2;
#define LAS __attribute__((address_space(3)))
#define GAS __attribute__((address_space(1)))
#define DI __device__ __forceinline__

constexpr int S_ = 2048, D_ = 640, F_ = 1536;
constexpr unsigned OFF_EP = 0u;
constexpr unsigned OFF_FR = 2048u;
constexpr unsigned OFF_FA1 = 4096u;
constexpr unsigned OFF_FA2 = 24576u;
constexpr unsigned OFF_FA3 = 55296u;
constexpr unsigned OFF_FX = 71680u;
constexpr unsigned OFF_FX1 = 235520u;
constexpr unsigned OFF_FW1 = 399360u;
constexpr unsigned OFF_FU = 407552u;
constexpr unsigned OFF_FW2 = 415744u;
constexpr unsigned OFF_RT = 419840u;
constexpr unsigned OFF_GB = 422400u;
constexpr unsigned OFF_RL = 424960u;
constexpr unsigned OFF_ST2 = 442368u;
constexpr unsigned OFF_FL = 573440u;
constexpr unsigned OFF_MODP = 1097728u;
constexpr unsigned OFF_ST1 = 1159168u;
constexpr unsigned OFF_UB = 1175552u;
constexpr unsigned OFF_KVT = 1273856u;
constexpr unsigned OFF_XT = 3895296u;
constexpr unsigned OFF_X2P = 6516736u;
constexpr unsigned OFF_H2F = 9138176u;
constexpr unsigned OFF_W1F = 15429632u;
constexpr unsigned OFF_W2F = 19361792u;
constexpr unsigned WS_END = 21327872u;
constexpr unsigned TAG_A = 2654435769u;
constexpr unsigned TAG_B = 2246822507u;
constexpr unsigned TAG_C = 2135587861u;
constexpr unsigned TAG_D = 3266489909u;
constexpr unsigned FC_PITCH = 4160u;
constexpr unsigned FC_CH = 16640u;
constexpr unsigned L_FC = 0u;
constexpr unsigned L_KV = 66560u;
constexpr unsigned L_X = 84992u;
constexpr unsigned L_ST = 101376u;
constexpr unsigned L_XR = 117760u;
constexpr unsigned L_CNT = 140288u;
constexpr unsigned L_STASH = 143360u;
constexpr unsigned L_UN = 71680u;
constexpr unsigned UN_SZ = 22528u;

constexpr unsigned OOB = 0x80000000u;
DI unsigned rfl(unsigned x) { return __builtin_amdgcn_readfirstlane(x); }
DI unsigned stkey(unsigned r) { const unsigned k = (r >> 1) & 7; return (((k ^ (k >> 1)) >> 1) & 1) | ((k & 1) << 1) | (k & 4); }
DI float bflo(u32 w) { return __uint_as_float(w << 16); }
DI float bfhi(u32 w) { return __uint_as_float(w & 0xffff0000u); }
DI u32 pack_bf16(float lo, float hi) { bf16x2 v; v[0] = (__bf16)lo; v[1] = (__bf16)hi; return __builtin_bit_cast(u32, v); }
DI float rbf(float x) { __bf16 b = (__bf16)x; return (float)b; }
DI float dot2(u32 a, u32 b, float c) { return __builtin_amdgcn_fdot2_f32_bf16(__builtin_bit_cast(bf16x2, a), __builtin_bit_cast(bf16x2, b), c, false); }
DI f32x4 mfma(u32x4 a, u32x4 b, f32x4 c) { return __builtin_amdgcn_mfma_f32_16x16x32_bf16(__builtin_bit_cast(bf16x8, a), __builtin_bit_cast(bf16x8, b), c, 0, 0, 0); }
DI void mfma_v(f32x4& c, u32x4 a, u32x4 b) { asm volatile("s_nop 1\n\tv_mfma_f32_16x16x32_bf16 %0, %1, %2, %0" : "+v"(c) : "v"(a), "v"(b)); }
DI u32x2 tr_read(const char* p) { return __builtin_bit_cast(u32x2, __builtin_amdgcn_ds_read_tr16_b64_v4i16((LAS s16x4*)p)); }
typedef __amdgpu_buffer_rsrc_t rsrc_t;
DI rsrc_t mkrsrc(const void* p, unsigned bytes) { return __builtin_amdgcn_make_buffer_rsrc((void*)p, 0, bytes, 0x00020000); }
#define BAR() asm volatile("s_waitcnt lgkmcnt(0)\n\ts_barrier" ::: "memory")
#define FENCE() do { asm volatile("" ::: "memory"); __builtin_amdgcn_sched_barrier(0); } while (0)
constexpr int SC1 = 16, NT = 2, W_IDLE_SLEEP = 8;

DI u64 load_tag(const char* ws) {
  u64 epoch;
  asm volatile("s_load_dwordx2 %0, %1, 0\n\ts_waitcnt lgkmcnt(0)" : "=&s"(epoch) : "s"(ws) : "memory");
  const u32 tl = ((u32)epoch ^ TAG_A) * TAG_B, th = ((u32)(epoch >> 32) ^ TAG_C) * TAG_D + tl;
  return ((u64)th << 32) | tl;
}
template <int N, int SL = 2>
DI void poll_flags(rsrc_t srd, unsigned voff, u64 tag) {
  const u32 lo = (u32)tag, hi = (u32)(tag >> 32);
#pragma unroll 1
  for (int spin = 0; spin < (1 << 18); ++spin) {
    u32 bad = 0;
#pragma unroll
    for (int k = 0; k < N; ++k) { const u32x2 f = __builtin_amdgcn_raw_buffer_load_b64(srd, voff + k * 512, 0, SC1); bad |= (f[0] ^ lo) | (f[1] ^ hi); }
    asm volatile("" ::: "memory");
    if (__all(bad == 0)) break;
    __builtin_amdgcn_s_sleep(SL);
  }
  FENCE();
}
DI void put_flag(rsrc_t srd, unsigned off, unsigned lane, u64 tag) {
  __builtin_amdgcn_raw_buffer_store_b128(u32x4{(u32)tag, (u32)(tag >> 32), (u32)tag, (u32)(tag >> 32)}, srd, lane < 8 ? off + lane * 16 : OOB, 0, SC1);
  FENCE();
}
DI float modval(rsrc_t srd, unsigned col) {
  float m[4];
#pragma unroll
  for (int k = 0; k < 4; ++k) m[k] = __uint_as_float(__builtin_amdgcn_raw_buffer_load_b32(srd, col * 4, OFF_MODP + k * 15360, SC1));
  return rbf((m[0] + m[1]) + (m[2] + m[3]));
}

constexpr int RP1 = 400, RP2 = 656, L_WS = 69632;

__global__ __launch_bounds__(256, 1) void k_main(const u16* __restrict__ x, const u16* __restrict__ kIn, const u16* __restrict__ vIn,
                                                 const u16* __restrict__ shk, const u16* __restrict__ ssmk, const u16* __restrict__ ssmD,
                                                 const u16* __restrict__ wg, const u16* __restrict__ wu, const u16* __restrict__ wd,
                                                 const u16* __restrict__ cIn, const u16* __restrict__ wmod, char* ws, u16* __restrict__ out) {
  __shared__ __attribute__((aligned(16))) char smem[163840];
  const unsigned b = blockIdx.x, tid = threadIdx.x, w = rfl(tid >> 6), lane = tid & 63;
  const unsigned xcd = b & 7, loc = b >> 3, pl = loc & 3, p = 4 * (xcd >> 1) + pl, j = 8 * (xcd & 1) + (loc >> 2), ng = j & 7, sub = j >> 3;
  if (loc >= 24) {
    u64 pc; unsigned len;
    asm volatile("s_getpc_b64 %0\n.Lk_here:\n\ts_mov_b32 %1, .Lk_end-.Lk_here" : "=s"(pc), "=s"(len));
    asm volatile("buffer_load_dword a255, %0, %1, 0 offen" :: "v"(lane < 16 ? (lane * 32 + (loc - 24) * 4 + w) * 128 : OOB), "s"(mkrsrc((const void*)pc, len)) : "a255", "memory");
  }
  asm volatile("" :: "s"(x), "s"(kIn), "s"(vIn), "s"(shk), "s"(ssmk), "s"(ssmD), "s"(wg), "s"(wu), "s"(wd), "s"(cIn), "s"(wmod), "s"(ws), "s"(out));
  const rsrc_t srd = mkrsrc(ws, WS_END);
  const rsrc_t srdo = mkrsrc(out, S_ * D_ * 2);
  const unsigned lb = (unsigned)(size_t)smem;
  const unsigned role = loc < 20 ? 0 : loc < 28 ? 1 : 2;
  const unsigned cb = xcd * 20 + loc, w1 = xcd * 8 + loc - 20, w2 = xcd * 4 + loc - 28;
  const unsigned tb = 4 * (cb & 31) + w, lkv = L_KV + 2048 + w * 9216;
  if (role == 0)
    asm volatile(
  "s_mov_b32 m0, %[lk]\n\t"
  "s_nop 3\n\t"
  "buffer_load_dwordx4 %[vo], %[rk], %[sh] offen nt lds\n\t"
  "s_add_u32 m0, %[lk], 1024\n\t"
  "s_add_u32 s44, %[so], 0\n\t"
  "buffer_load_dwordx4 %[vo], %[rk], s44 offen nt lds\n\t"
  "s_add_u32 m0, %[lk], 5120\n\t"
  "s_nop 0\n\t"
  "buffer_load_dwordx4 %[vo], %[rv], s44 offen nt lds\n\t"
  "s_add_u32 m0, %[lk], 2048\n\t"
  "s_add_u32 s44, %[so], 5120\n\t"
  "buffer_load_dwordx4 %[vo], %[rk], s44 offen nt lds\n\t"
  "s_add_u32 m0, %[lk], 6144\n\t"
  "s_nop 0\n\t"
  "buffer_load_dwordx4 %[vo], %[rv], s44 offen nt lds\n\t"
  "s_add_u32 m0, %[lk], 3072\n\t"
  "s_add_u32 s44, %[so], 10240\n\t"
  "buffer_load_dwordx4 %[vo], %[rk], s44 offen nt lds\n\t"
  "s_add_u32 m0, %[lk], 7168\n\t"
  "s_nop 0\n\t"
  "buffer_load_dwordx4 %[vo], %[rv], s44 offen nt lds\n\t"
  "s_add_u32 m0, %[lk], 4096\n\t"
  "s_add_u32 s44, %[so], 15360\n\t"
  "buffer_load_dwordx4 %[vo], %[rk], s44 offen nt lds\n\t"
  "s_add_u32 m0, %[lk], 8192\n\t"
  "s_nop 0\n\t"
  "buffer_load_dwordx4 %[vo], %[rv], s44 offen nt lds\n\t"
      :: [rk] "s"(mkrsrc(kIn, S_ * D_ * 2)), [rv] "s"(mkrsrc(vIn, S_ * D_ * 2)), [lk] "s"(lb + lkv), [so] "s"(tb * 20480u + (cb >> 5) * 256u), [sh] "s"(tb ? tb * 20480u + (cb >> 5) * 256u - 5120u : OOB),
         [vo] "v"((lane >> 4) * 1280 + (lane & 15) * 16) : "memory", "scc", "s44");
  FENCE();
  const u32x4 zero4 = {0, 0, 0, 0};
  unsigned wdone = 0, dval = 0;
#define U_KQ(u_) ((u_) & 3)
#define U_CG(u_) (((u_) >> 2) < 30 || ((u_) >> 2) >= 50 ? (u_) >> 2 : ((u_) >> 2) < 40 ? ((u_) >> 2) + 10 : ((u_) >> 2) - 10)
#define U_SRC(u_) (U_KQ(u_) * 160u * 7680u + U_CG(u_) * 128u)
#define U_FLAG(u_) (OFF_FA2 + (U_KQ(u_) * 60 + U_CG(u_)) * 128)
  const unsigned vm = (lane >> 3) * 7680 + (((lane & 7) ^ (((lane >> 4) & 3) << 1)) * 16);
#define GEMV_LOAD(u_, lu_) asm volatile( \
  "s_nop 4\n\t" \
  "s_add_u32 m0, %[lu], 20480\n\t" \
  "s_nop 0\n\t" \
  "buffer_load_dwordx4 %[vl16], %[rc], %[sC] offen lds\n\t" \
  "s_add_u32 m0, %[lu], 0\n\t" \
  "s_add_u32 s44, %[sMod], 0\n\t" \
  "buffer_load_dwordx4 %[vm], %[rm], s44 offen nt lds\n\t" \
  "s_add_u32 m0, %[lu], 1024\n\t" \
  "s_add_u32 s44, %[sMod], 61440\n\t" \
  "buffer_load_dwordx4 %[vm], %[rm], s44 offen nt lds\n\t" \
  "s_add_u32 m0, %[lu], 2048\n\t" \
  "s_add_u32 s44, %[sMod], 122880\n\t" \
  "buffer_load_dwordx4 %[vm], %[rm], s44 offen nt lds\n\t" \
  "s_add_u32 m0, %[lu], 3072\n\t" \
  "s_add_u32 s44, %[sMod], 184320\n\t" \
  "buffer_load_dwordx4 %[vm], %[rm], s44 offen nt lds\n\t" \
  "s_add_u32 m0, %[lu], 4096\n\t" \
  "s_add_u32 s44, %[sMod], 245760\n\t" \
  "buffer_load_dwordx4 %[vm], %[rm], s44 offen nt lds\n\t" \
  "s_add_u32 m0, %[lu], 5120\n\t" \
  "s_add_u32 s44, %[sMod], 307200\n\t" \
  "buffer_load_dwordx4 %[vm], %[rm], s44 offen nt lds\n\t" \
  "s_add_u32 m0, %[lu], 6144\n\t" \
  "s_add_u32 s44, %[sMod], 368640\n\t" \
  "buffer_load_dwordx4 %[vm], %[rm], s44 offen nt lds\n\t" \
  "s_add_u32 m0, %[lu], 7168\n\t" \
  "s_add_u32 s44, %[sMod], 430080\n\t" \
  "buffer_load_dwordx4 %[vm], %[rm], s44 offen nt lds\n\t" \
  "s_add_u32 m0, %[lu], 8192\n\t" \
  "s_add_u32 s44, %[sMod], 491520\n\t" \
  "buffer_load_dwordx4 %[vm], %[rm], s44 offen nt lds\n\t" \
  "s_add_u32 m0, %[lu], 9216\n\t" \
  "s_add_u32 s44, %[sMod], 552960\n\t" \
  "buffer_load_dwordx4 %[vm], %[rm], s44 offen nt lds\n\t" \
  "s_add_u32 m0, %[lu], 10240\n\t" \
  "s_add_u32 s44, %[sMod], 614400\n\t" \
  "buffer_load_dwordx4 %[vm], %[rm], s44 offen nt lds\n\t" \
  "s_add_u32 m0, %[lu], 11264\n\t" \
  "s_add_u32 s44, %[sMod], 675840\n\t" \
  "buffer_load_dwordx4 %[vm], %[rm], s44 offen nt lds\n\t" \
  "s_add_u32 m0, %[lu], 12288\n\t" \
  "s_add_u32 s44, %[sMod], 737280\n\t" \
  "buffer_load_dwordx4 %[vm], %[rm], s44 offen nt lds\n\t" \
  "s_add_u32 m0, %[lu], 13312\n\t" \
  "s_add_u32 s44, %[sMod], 798720\n\t" \
  "buffer_load_dwordx4 %[vm], %[rm], s44 offen nt lds\n\t" \
  "s_add_u32 m0, %[lu], 14336\n\t" \
  "s_add_u32 s44, %[sMod], 860160\n\t" \
  "buffer_load_dwordx4 %[vm], %[rm], s44 offen nt lds\n\t" \
  "s_add_u32 m0, %[lu], 15360\n\t" \
  "s_add_u32 s44, %[sMod], 921600\n\t" \
  "buffer_load_dwordx4 %[vm], %[rm], s44 offen nt lds\n\t" \
  "s_add_u32 m0, %[lu], 16384\n\t" \
  "s_add_u32 s44, %[sMod], 983040\n\t" \
  "buffer_load_dwordx4 %[vm], %[rm], s44 offen nt lds\n\t" \
  "s_add_u32 m0, %[lu], 17408\n\t" \
  "s_add_u32 s44, %[sMod], 1044480\n\t" \
  "buffer_load_dwordx4 %[vm], %[rm], s44 offen nt lds\n\t" \
  "s_add_u32 m0, %[lu], 18432\n\t" \
  "s_add_u32 s44, %[sMod], 1105920\n\t" \
  "buffer_load_dwordx4 %[vm], %[rm], s44 offen nt lds\n\t" \
  "s_add_u32 m0, %[lu], 19456\n\t" \
  "s_add_u32 s44, %[sMod], 1167360\n\t" \
  "buffer_load_dwordx4 %[vm], %[rm], s44 offen nt lds\n\t" \
        :: [rm] "s"(mkrsrc(wmod, D_ * 3840 * 2)), [rc] "s"(mkrsrc(cIn, D_ * 2)), [lu] "s"(lu_), [sC] "s"(U_KQ(u_) * 320u), [sMod] "s"(U_SRC(u_)), \
           [vl16] "v"(lane * 16), [vm] "v"(vm) : "memory", "scc", "s44")
#define W1_LOAD() asm volatile( \
  "s_nop 4\n\t" \
  "s_add_u32 s44, %[sW1], 0\n\t" \
  "buffer_load_dwordx4 a[0:3], %[vl16], %[srd], s44 offen offset:0 sc1\n\t" \
  "buffer_load_dwordx4 a[4:7], %[vl16], %[srd], s44 offen offset:1024 sc1\n\t" \
  "buffer_load_dwordx4 a[8:11], %[vl16], %[srd], s44 offen offset:2048 sc1\n\t" \
  "buffer_load_dwordx4 a[12:15], %[vl16], %[srd], s44 offen offset:3072 sc1\n\t" \
  "s_add_u32 s44, %[sW1], 4096\n\t" \
  "buffer_load_dwordx4 a[16:19], %[vl16], %[srd], s44 offen offset:0 sc1\n\t" \
  "buffer_load_dwordx4 a[20:23], %[vl16], %[srd], s44 offen offset:1024 sc1\n\t" \
  "buffer_load_dwordx4 a[24:27], %[vl16], %[srd], s44 offen offset:2048 sc1\n\t" \
  "buffer_load_dwordx4 a[28:31], %[vl16], %[srd], s44 offen offset:3072 sc1\n\t" \
  "s_add_u32 s44, %[sW1], 8192\n\t" \
  "buffer_load_dwordx4 a[32:35], %[vl16], %[srd], s44 offen offset:0 sc1\n\t" \
  "buffer_load_dwordx4 a[36:39], %[vl16], %[srd], s44 offen offset:1024 sc1\n\t" \
  "buffer_load_dwordx4 a[40:43], %[vl16], %[srd], s44 offen offset:2048 sc1\n\t" \
  "buffer_load_dwordx4 a[44:47], %[vl16], %[srd], s44 offen offset:3072 sc1\n\t" \
  "s_add_u32 s44, %[sW1], 12288\n\t" \
  "buffer_load_dwordx4 a[48:51], %[vl16], %[srd], s44 offen offset:0 sc1\n\t" \
  "buffer_load_dwordx4 a[52:55], %[vl16], %[srd], s44 offen offset:1024 sc1\n\t" \
  "buffer_load_dwordx4 a[56:59], %[vl16], %[srd], s44 offen offset:2048 sc1\n\t" \
  "buffer_load_dwordx4 a[60:63], %[vl16], %[srd], s44 offen offset:3072 sc1\n\t" \
  "s_add_u32 s44, %[sW1], 16384\n\t" \
  "buffer_load_dwordx4 a[64:67], %[vl16], %[srd], s44 offen offset:0 sc1\n\t" \
  "buffer_load_dwordx4 a[68:71], %[vl16], %[srd], s44 offen offset:1024 sc1\n\t" \
  "buffer_load_dwordx4 a[72:75], %[vl16], %[srd], s44 offen offset:2048 sc1\n\t" \
  "buffer_load_dwordx4 a[76:79], %[vl16], %[srd], s44 offen offset:3072 sc1\n\t" \
  "s_add_u32 s44, %[sW1], 20480\n\t" \
  "buffer_load_dwordx4 a[80:83], %[vl16], %[srd], s44 offen offset:0 sc1\n\t" \
  "buffer_load_dwordx4 a[84:87], %[vl16], %[srd], s44 offen offset:1024 sc1\n\t" \
  "buffer_load_dwordx4 a[88:91], %[vl16], %[srd], s44 offen offset:2048 sc1\n\t" \
  "buffer_load_dwordx4 a[92:95], %[vl16], %[srd], s44 offen offset:3072 sc1\n\t" \
  "s_add_u32 s44, %[sW1], 24576\n\t" \
  "buffer_load_dwordx4 a[96:99], %[vl16], %[srd], s44 offen offset:0 sc1\n\t" \
  "buffer_load_dwordx4 a[100:103], %[vl16], %[srd], s44 offen offset:1024 sc1\n\t" \
  "buffer_load_dwordx4 a[104:107], %[vl16], %[srd], s44 offen offset:2048 sc1\n\t" \
  "buffer_load_dwordx4 a[108:111], %[vl16], %[srd], s44 offen offset:3072 sc1\n\t" \
  "s_add_u32 s44, %[sW1], 28672\n\t" \
  "buffer_load_dwordx4 a[112:115], %[vl16], %[srd], s44 offen offset:0 sc1\n\t" \
  "buffer_load_dwordx4 a[116:119], %[vl16], %[srd], s44 offen offset:1024 sc1\n\t" \
  "buffer_load_dwordx4 a[120:123], %[vl16], %[srd], s44 offen offset:2048 sc1\n\t" \
  "buffer_load_dwordx4 a[124:127], %[vl16], %[srd], s44 offen offset:3072 sc1\n\t" \
  "s_add_u32 s44, %[sW1], 32768\n\t" \
  "buffer_load_dwordx4 a[128:131], %[vl16], %[srd], s44 offen offset:0 sc1\n\t" \
  "buffer_load_dwordx4 a[132:135], %[vl16], %[srd], s44 offen offset:1024 sc1\n\t" \
  "buffer_load_dwordx4 a[136:139], %[vl16], %[srd], s44 offen offset:2048 sc1\n\t" \
  "buffer_load_dwordx4 a[140:143], %[vl16], %[srd], s44 offen offset:3072 sc1\n\t" \
  "s_add_u32 s44, %[sW1], 36864\n\t" \
  "buffer_load_dwordx4 a[144:147], %[vl16], %[srd], s44 offen offset:0 sc1\n\t" \
  "buffer_load_dwordx4 a[148:151], %[vl16], %[srd], s44 offen offset:1024 sc1\n\t" \
  "buffer_load_dwordx4 a[152:155], %[vl16], %[srd], s44 offen offset:2048 sc1\n\t" \
  "buffer_load_dwordx4 a[156:159], %[vl16], %[srd], s44 offen offset:3072 sc1\n\t" \
  "s_add_u32 s44, %[sW1], 40960\n\t" \
  "buffer_load_dwordx4 a[160:163], %[vl16], %[srd], s44 offen offset:0 sc1\n\t" \
  "buffer_load_dwordx4 a[164:167], %[vl16], %[srd], s44 offen offset:1024 sc1\n\t" \
        :: [srd] "s"(srd), [sW1] "s"(OFF_W1F + (j * 4 + w) * 61440u), [vl16] "v"(lane * 16) : "memory", "scc", \
"a0", "a1", "a2", "a3", "a4", "a5", "a6", "a7", "a8", "a9", "a10", "a11", "a12", "a13", "a14", "a15", "a16", "a17", "a18", "a19", "a20", "a21", "a22", "a23", "a24", "a25", "a26", "a27", "a28", "a29", "a30", "a31", "a32", "a33", "a34", "a35", "a36", "a37", "a38", "a39", "a40", "a41", "a42", "a43", "a44", "a45", "a46", "a47", "a48", "a49", "a50", "a51", "a52", "a53", "a54", "a55", "a56", "a57", "a58", "a59", "a60", "a61", "a62", "a63", "a64", "a65", "a66", "a67", "a68", "a69", "a70", "a71", "a72", "a73", "a74", "a75", "a76", "a77", "a78", "a79", "a80", "a81", "a82", "a83", "a84", "a85", "a86", "a87", "a88", "a89", "a90", "a91", "a92", "a93", "a94", "a95", "a96", "a97", "a98", "a99", "a100", "a101", "a102", "a103", "a104", "a105", "a106", "a107", "a108", "a109", "a110", "a111", "a112", "a113", "a114", "a115", "a116", "a117", "a118", "a119", "a120", "a121", "a122", "a123", "a124", "a125", "a126", "a127", "a128", "a129", "a130", "a131", "a132", "a133", "a134", "a135", "a136", "a137", "a138", "a139", "a140", "a141", "a142", "a143", "a144", "a145", "a146", "a147", "a148", "a149", "a150", "a151", "a152", "a153", "a154", "a155", "a156", "a157", "a158", "a159", "a160", "a161", "a162", "a163", "a164", "a165", "a166", "a167", "s44" \
      )
#define GEMV_UNIT(u_, lu_, younger_) asm volatile( \
  "s_nop 4\n\t" \
  "v_lshlrev_b32 v176, 1, %[vl]\n\t" \
  "v_add_u32 v176, %[lu], v176\n\t" \
  "v_mov_b32 v96, 0\n\t" \
  "v_mov_b32 v97, 0\n\t" \
  "v_mov_b32 v98, 0\n\t" \
  "s_waitcnt vmcnt(%c[wa])\n\t" \
  "ds_read_u16_d16_hi v96, v176 offset:20480\n\t" \
  "ds_read_u16_d16_hi v97, v176 offset:20608\n\t" \
  "ds_read_u16_d16_hi v98, v176 offset:20736\n\t" \
  "s_waitcnt lgkmcnt(2)\n\t" \
  "v_mul_f32 v177, 0xbfb8aa3b, v96\n\t" \
  "s_waitcnt lgkmcnt(1)\n\t" \
  "v_mul_f32 v178, 0xbfb8aa3b, v97\n\t" \
  "s_waitcnt lgkmcnt(0)\n\t" \
  "v_mul_f32 v179, 0xbfb8aa3b, v98\n\t" \
  "v_exp_f32 v177, v177\n\t" \
  "v_exp_f32 v178, v178\n\t" \
  "v_exp_f32 v179, v179\n\t" \
  "v_add_f32 v177, 1.0, v177\n\t" \
  "v_add_f32 v178, 1.0, v178\n\t" \
  "v_add_f32 v179, 1.0, v179\n\t" \
  "v_rcp_f32 v177, v177\n\t" \
  "v_rcp_f32 v178, v178\n\t" \
  "v_rcp_f32 v179, v179\n\t" \
  "v_mul_f32 v177, v177, v96\n\t" \
  "v_mul_f32 v178, v178, v97\n\t" \
  "v_mul_f32 v179, v179, v98\n\t" \
  "v_cvt_pk_bf16_f32 v177, v177, v177\n\t" \
  "v_cvt_pk_bf16_f32 v178, v178, v178\n\t" \
  "v_cvt_pk_bf16_f32 v179, v179, v179\n\t" \
  "ds_write_b16 v176, v177 offset:21504\n\t" \
  "ds_write_b16 v176, v178 offset:21632\n\t" \
  "ds_write_b16 v176, v179 offset:21760\n\t" \
  "v_lshrrev_b32 v180, 4, %[vl]\n\t" \
  "v_lshlrev_b32 v180, 3, v180\n\t" \
  "v_add_u32 v180, %[lu], v180\n\t" \
  "ds_read_b64 v[100:101], v180 offset:21504\n\t" \
  "ds_read_b64 v[102:103], v180 offset:21536\n\t" \
  "ds_read_b64 v[104:105], v180 offset:21568\n\t" \
  "ds_read_b64 v[106:107], v180 offset:21600\n\t" \
  "ds_read_b64 v[108:109], v180 offset:21632\n\t" \
  "ds_read_b64 v[110:111], v180 offset:21664\n\t" \
  "ds_read_b64 v[112:113], v180 offset:21696\n\t" \
  "ds_read_b64 v[114:115], v180 offset:21728\n\t" \
  "ds_read_b64 v[116:117], v180 offset:21760\n\t" \
  "ds_read_b64 v[118:119], v180 offset:21792\n\t" \
  "v_xor_b32 v181, 32, %[vtm]\n\t" \
  "v_xor_b32 v182, 64, %[vtm]\n\t" \
  "v_xor_b32 v183, 96, %[vtm]\n\t" \
  "s_waitcnt vmcnt(%c[wb])\n\t" \
  "ds_read_b64_tr_b16 v[120:121], %[vtm] offset:0\n\t" \
  "ds_read_b64_tr_b16 v[122:123], %[vtm] offset:2048\n\t" \
  "ds_read_b64_tr_b16 v[124:125], v181 offset:0\n\t" \
  "ds_read_b64_tr_b16 v[126:127], v181 offset:2048\n\t" \
  "ds_read_b64_tr_b16 v[128:129], v182 offset:0\n\t" \
  "ds_read_b64_tr_b16 v[130:131], v182 offset:2048\n\t" \
  "ds_read_b64_tr_b16 v[132:133], v183 offset:0\n\t" \
  "ds_read_b64_tr_b16 v[134:135], v183 offset:2048\n\t" \
  "ds_read_b64_tr_b16 v[136:137], %[vtm] offset:4096\n\t" \
  "ds_read_b64_tr_b16 v[138:139], %[vtm] offset:6144\n\t" \
  "ds_read_b64_tr_b16 v[140:141], v181 offset:4096\n\t" \
  "ds_read_b64_tr_b16 v[142:143], v181 offset:6144\n\t" \
  "ds_read_b64_tr_b16 v[144:145], v182 offset:4096\n\t" \
  "ds_read_b64_tr_b16 v[146:147], v182 offset:6144\n\t" \
  "ds_read_b64_tr_b16 v[148:149], v183 offset:4096\n\t" \
  "ds_read_b64_tr_b16 v[150:151], v183 offset:6144\n\t" \
  "s_waitcnt lgkmcnt(14)\n\t" \
  "v_mfma_f32_16x16x32_bf16 v[152:155], v[100:103], v[120:123], 0\n\t" \
  "s_waitcnt lgkmcnt(12)\n\t" \
  "v_mfma_f32_16x16x32_bf16 v[156:159], v[100:103], v[124:127], 0\n\t" \
  "s_waitcnt lgkmcnt(10)\n\t" \
  "v_mfma_f32_16x16x32_bf16 v[160:163], v[100:103], v[128:131], 0\n\t" \
  "s_waitcnt lgkmcnt(8)\n\t" \
  "v_mfma_f32_16x16x32_bf16 v[164:167], v[100:103], v[132:135], 0\n\t" \
  "ds_read_b64_tr_b16 v[120:121], %[vtm] offset:8192\n\t" \
  "ds_read_b64_tr_b16 v[122:123], %[vtm] offset:10240\n\t" \
  "ds_read_b64_tr_b16 v[124:125], v181 offset:8192\n\t" \
  "ds_read_b64_tr_b16 v[126:127], v181 offset:10240\n\t" \
  "ds_read_b64_tr_b16 v[128:129], v182 offset:8192\n\t" \
  "ds_read_b64_tr_b16 v[130:131], v182 offset:10240\n\t" \
  "ds_read_b64_tr_b16 v[132:133], v183 offset:8192\n\t" \
  "ds_read_b64_tr_b16 v[134:135], v183 offset:10240\n\t" \
  "s_waitcnt lgkmcnt(14)\n\t" \
  "v_mfma_f32_16x16x32_bf16 v[152:155], v[104:107], v[136:139], v[152:155]\n\t" \
  "s_waitcnt lgkmcnt(12)\n\t" \
  "v_mfma_f32_16x16x32_bf16 v[156:159], v[104:107], v[140:143], v[156:159]\n\t" \
  "s_waitcnt lgkmcnt(10)\n\t" \
  "v_mfma_f32_16x16x32_bf16 v[160:163], v[104:107], v[144:147], v[160:163]\n\t" \
  "s_waitcnt lgkmcnt(8)\n\t" \
  "v_mfma_f32_16x16x32_bf16 v[164:167], v[104:107], v[148:151], v[164:167]\n\t" \
  "ds_read_b64_tr_b16 v[136:137], %[vtm] offset:12288\n\t" \
  "ds_read_b64_tr_b16 v[138:139], %[vtm] offset:14336\n\t" \
  "ds_read_b64_tr_b16 v[140:141], v181 offset:12288\n\t" \
  "ds_read_b64_tr_b16 v[142:143], v181 offset:14336\n\t" \
  "ds_read_b64_tr_b16 v[144:145], v182 offset:12288\n\t" \
  "ds_read_b64_tr_b16 v[146:147], v182 offset:14336\n\t" \
  "ds_read_b64_tr_b16 v[148:149], v183 offset:12288\n\t" \
  "ds_read_b64_tr_b16 v[150:151], v183 offset:14336\n\t" \
  "s_waitcnt lgkmcnt(14)\n\t" \
  "v_mfma_f32_16x16x32_bf16 v[152:155], v[108:111], v[120:123], v[152:155]\n\t" \
  "s_waitcnt lgkmcnt(12)\n\t" \
  "v_mfma_f32_16x16x32_bf16 v[156:159], v[108:111], v[124:127], v[156:159]\n\t" \
  "s_waitcnt lgkmcnt(10)\n\t" \
  "v_mfma_f32_16x16x32_bf16 v[160:163], v[108:111], v[128:131], v[160:163]\n\t" \
  "s_waitcnt lgkmcnt(8)\n\t" \
  "v_mfma_f32_16x16x32_bf16 v[164:167], v[108:111], v[132:135], v[164:167]\n\t" \
  "ds_read_b64_tr_b16 v[120:121], %[vtm] offset:16384\n\t" \
  "ds_read_b64_tr_b16 v[122:123], %[vtm] offset:18432\n\t" \
  "ds_read_b64_tr_b16 v[124:125], v181 offset:16384\n\t" \
  "ds_read_b64_tr_b16 v[126:127], v181 offset:18432\n\t" \
  "ds_read_b64_tr_b16 v[128:129], v182 offset:16384\n\t" \
  "ds_read_b64_tr_b16 v[130:131], v182 offset:18432\n\t" \
  "ds_read_b64_tr_b16 v[132:133], v183 offset:16384\n\t" \
  "ds_read_b64_tr_b16 v[134:135], v183 offset:18432\n\t" \
  "s_waitcnt lgkmcnt(14)\n\t" \
  "v_mfma_f32_16x16x32_bf16 v[152:155], v[112:115], v[136:139], v[152:155]\n\t" \
  "s_waitcnt lgkmcnt(12)\n\t" \
  "v_mfma_f32_16x16x32_bf16 v[156:159], v[112:115], v[140:143], v[156:159]\n\t" \
  "s_waitcnt lgkmcnt(10)\n\t" \
  "v_mfma_f32_16x16x32_bf16 v[160:163], v[112:115], v[144:147], v[160:163]\n\t" \
  "s_waitcnt lgkmcnt(8)\n\t" \
  "v_mfma_f32_16x16x32_bf16 v[164:167], v[112:115], v[148:151], v[164:167]\n\t" \
  "s_waitcnt lgkmcnt(6)\n\t" \
  "v_mfma_f32_16x16x32_bf16 v[152:155], v[116:119], v[120:123], v[152:155]\n\t" \
  "s_waitcnt lgkmcnt(4)\n\t" \
  "v_mfma_f32_16x16x32_bf16 v[156:159], v[116:119], v[124:127], v[156:159]\n\t" \
  "s_waitcnt lgkmcnt(2)\n\t" \
  "v_mfma_f32_16x16x32_bf16 v[160:163], v[116:119], v[128:131], v[160:163]\n\t" \
  "s_waitcnt lgkmcnt(0)\n\t" \
  "v_mfma_f32_16x16x32_bf16 v[164:167], v[116:119], v[132:135], v[164:167]\n\t" \
  "v_lshlrev_b32 v176, 2, %[vl]\n\t" \
  "s_nop 2\n\t" \
  "v_mov_b32_dpp v152, v156 quad_perm:[0,1,2,3] row_mask:0x2 bank_mask:0xf\n\t" \
  "s_nop 1\n\t" \
  "v_mov_b32_dpp v152, v160 quad_perm:[0,1,2,3] row_mask:0x4 bank_mask:0xf\n\t" \
  "s_nop 1\n\t" \
  "v_mov_b32_dpp v152, v164 quad_perm:[0,1,2,3] row_mask:0x8 bank_mask:0xf\n\t" \
  "buffer_store_dword v152, v176, %[srd], %[sMo] offen sc1\n\t" \
  "s_nop 1\n\t" \
        :: [srd] "s"(srd), [lu] "s"(lu_), [wa] "i"((younger_) + 20), [wb] "i"(younger_), [sMo] "s"(OFF_MODP + U_KQ(u_) * 15360u + U_CG(u_) * 256u), [vl] "v"(lane), \
           [vtm] "v"((lu_) + (lane >> 2) * 128 + ((lane >> 3) & 3) * 32 + (lane & 3) * 8) : "memory", "vcc", "scc", \
"v96", "v97", "v98", "v100", "v101", "v102", "v103", "v104", "v105", "v106", "v107", "v108", "v109", "v110", "v111", "v112", "v113", "v114", "v115", "v116", "v117", "v118", "v119", "v120", "v121", "v122", "v123", "v124", "v125", "v126", "v127", "v128", "v129", "v130", "v131", "v132", "v133", "v134", "v135", "v136", "v137", "v138", "v139", "v140", "v141", "v142", "v143", "v144", "v145", "v146", "v147", "v148", "v149", "v150", "v151", "v152", "v153", "v154", "v155", "v156", "v157", "v158", "v159", "v160", "v161", "v162", "v163", "v164", "v165", "v166", "v167", "v176", "v177", "v178", "v179", "v180", "v181", "v182", "v183" \
      )
  auto stash = [&]() {
    const unsigned vpub = role == 0 ? (tid < 8 * 8 ? (0 ? OFF_FX1 : OFF_FX) + ((tid >> 3) * 160 + cb) * 128 + (tid & 7) * 16 : OOB) : tid < 8 ? (role == 1 ? OFF_FU + w1 * 128 : OFF_FW2 + w2 * 128) + tid * 16 : OOB;
    const unsigned vpw = lane < 4 ? OFF_FW1 + (j * 4 + lane) * 128 : lane < 8 ? OFF_FW2 + ((4 * w + (lane & 3)) * 2 + (ng >> 2)) * 128
                                                                              : OFF_FU + (lane < 12 ? j * 4 + (lane & 3) : (lane & 3) * 16) * 128;
    const unsigned pb = 32 * (lane >> 3) + 8 * w + (lane & 7);
    const unsigned vpx = lane < 40 && !(role == 0 && pb == cb) ? OFF_FX + (xcd % 8 * 160 + pb) * 128 : OFF_FR + min(lane - 40, 9u) * 128;
    const unsigned g = lane >> 4, q = (lane >> 2) & 3, pq = lane & 3;
    const unsigned row = 4 * g + q;
    const unsigned vtr = lb + row * 128 + ((row >> 1) & 3) * 32 + ((pq * 8) ^ ((row & 8) * 2));
    const unsigned vl16 = lane * 16;
    const unsigned vdma = OFF_X2P + p * 81920u + w * 4096u + lane * 16;
    const unsigned vh2st = lane < 48 ? OFF_H2F + (4 * p * 192 + 12 * j + 3 * w) * 256u + lane * 16 : OOB;
    const unsigned tk = 4 * j + w;
    const unsigned vstc = lb + lane * 128 + (((tk >> 4) ^ ((lane >> 1) & 3)) * 32) + (((tk & 15) * 2) ^ ((lane & 8) * 2));
    const unsigned vstw = lane == 63 ? tk * 64 : OOB;
    const unsigned vflw = lane < 8 ? OFF_FL + p * 32768u + (j * 4 + w) * 128u + lane * 16 : OOB;
    const unsigned vep = b == 0 && tid == 0 ? OFF_EP : OOB;
    const unsigned rs_ = 2 * (w & 1) + sub, rb = lb + (w >> 1) * 81920 + 80 * ng * 128;
    const unsigned r0 = 8 * g + q, r1 = 8 * g + 4 + q, r2 = 64 + 4 * g + q;
    const unsigned vres0 = rb + r0 * 128 + ((rs_ ^ ((r0 >> 1) & 3)) * 32) + ((pq * 8) ^ ((r0 & 8) * 2));
    const unsigned vres1 = rb + r1 * 128 + ((rs_ ^ ((r1 >> 1) & 3)) * 32) + ((pq * 8) ^ ((r1 & 8) * 2));
    const unsigned vres2 = rb + r2 * 128 + ((rs_ ^ ((r2 >> 1) & 3)) * 32) + ((pq * 8) ^ ((r2 & 8) * 2));
    const unsigned vwr = lb + w * 20480 + g * 256 + ((lane + 4 * g) & 15) * 16;
    const unsigned tq = lane < 32 ? q : (lane >> 1) & 3, tl = 4 * w + tq, g0 = lane < 32 ? pq : 2 * (lane & 1), g1 = lane < 32 ? pq : g0 + 1;
    const unsigned vrd0 = lb + (lane < 32 ? g * 2048 : 4096) + g0 * 256 + ((tl + 4 * g0) & 15) * 16;
    const unsigned vrd1 = lb + (lane < 32 ? g * 2048 + 1024 : 4096) + g1 * 256 + ((tl + 4 * g1) & 15) * 16;
    const unsigned vh1w = lane < 40 ? OFF_FX1 + (xcd % 8 * 160 + pb) * 128 : OFF_FU + ((lane < 44 ? j * 4 : 0) + (lane & 3)) * 128;
    const unsigned vgb = OFF_GB - OFF_RT + (lane < 32 ? 32 * g + 8 * pq : 64 + 8 * (lane & 1)) * 4;
    const unsigned vout = lane < 40 ? ((64 * p + 16 * sub + tl) * D_ + 80 * ng + (lane < 32 ? 32 * g + 8 * pq : 64 + 8 * (lane & 1))) * 2 : OOB;
    *(u32x4*)(smem + L_STASH + 0 + tid * 16) = u32x4{vpub, vl16, vpw, vpx};
    *(u32x4*)(smem + L_STASH + 4096 + tid * 16) = u32x4{vdma, vtr, vstc, vstw};
    *(u32x4*)(smem + L_STASH + 8192 + tid * 16) = u32x4{vh2st, vflw, vres0, vres1};
    *(u32x4*)(smem + L_STASH + 12288 + tid * 16) = u32x4{vres2, vwr, vrd0, vrd1};
    *(u32x4*)(smem + L_STASH + 16384 + tid * 16) = u32x4{vout, vgb, vep, vh1w};
    FENCE();
  };
  unsigned c_vf2;
  unsigned c_vf1, c_vp0;
  u64 pmask = 0;
  unsigned c_vf3;
  unsigned c_vfw;
  unsigned c_va;
  unsigned c_vb0;
  unsigned c_vb1;
  unsigned c_vb2;
  unsigned c_vb3;
  unsigned c_vx;
  unsigned c_vs;
  unsigned c_vs2;
  unsigned c_vo;
  u32x4 c_vd;
  unsigned c_vl16;
  unsigned c_vkv;
  unsigned c_vmod;
  u64 tag;
  if (__builtin_expect(role == 0, 1)) {
    const rsrc_t rf = mkrsrc(ssmk, S_ * D_ * 2);
    const unsigned d0 = 128 * (cb >> 5) + 2 * lane;
    u32x4 fa[4];
    const u32x4 taps = *(const u32x4*)(shk + d0 * 4);
    dval = ssmD[8 * (cb >> 1) + 2 * w + (cb & 1)];
    FENCE();
    __builtin_amdgcn_raw_buffer_store_b128(zero4, srd, w == 0 ? (cb < 128 ? OFF_ST2 + cb * 1024 + lane * 16 : OOB)
        : !2 ? OOB : w == 1 ? OFF_FX + ((lane >> 3) * 160 + cb) * 128 + (lane & 7) * 16
        : w == 2 && lane < 16 ? (lane < 8 ? OFF_FA1 + cb * 128 : cb < 128 ? OFF_FA3 + cb * 128 : U_FLAG(cb)) + (lane & 7) * 16 : OOB, 0, SC1);
    FENCE();
#pragma unroll
    for (int n = 0; n < 4; ++n) fa[n] = __builtin_amdgcn_raw_buffer_load_b128(rf, lane * 16 + n * 1024, (8 * (cb >> 1) + 2 * w + (cb & 1)) * 4096u, NT);
    u32x4 xr[5];
    const bool isx = cb < 128;
    const rsrc_t rx = mkrsrc(isx ? x : wmod, isx ? S_ * D_ * 2 : D_ * 3840 * 2);
#pragma unroll
    for (int n = 0; n < 5; ++n)
      xr[n] = __builtin_amdgcn_raw_buffer_load_b128(rx, isx ? (lane >> 3) * 1280 + (lane & 7) * 16 : vm,
                                                    isx ? cb * 20480u + (w & 1) * 10240u + (w >> 1) * 128u + n * 256u : U_SRC(cb) + (4 * n + w) * 61440u, NT);
    const u32x4 cr = __builtin_amdgcn_raw_buffer_load_b128(mkrsrc(cIn, isx ? 0 : D_ * 2), lane * 16, U_KQ(cb) * 320u, 0);
    FENCE();
    tag = load_tag(ws);
    if (w < 2) *(u32x4*)(smem + L_KV + tid * 16) = zero4;
    if (tid == 128) *(u32x4*)(smem + L_CNT) = zero4;
    FENCE();
    asm volatile("s_waitcnt vmcnt(11)" ::: "memory");
    asm volatile(
  "s_nop 1\n\t"
  "ds_read2st64_b32 v[112:113], %[la] offset0:0 offset1:1\n\t"
  "ds_read2st64_b32 v[114:115], %[la] offset0:2 offset1:3\n\t"
  "ds_read2st64_b32 v[116:117], %[la] offset0:4 offset1:5\n\t"
  "ds_read2st64_b32 v[118:119], %[la] offset0:6 offset1:7\n\t"
  "ds_read2st64_b32 v[120:121], %[la] offset0:8 offset1:9\n\t"
  "ds_read2st64_b32 v[122:123], %[la] offset0:10 offset1:11\n\t"
  "ds_read2st64_b32 v[132:133], %[la] offset0:20 offset1:21\n\t"
  "ds_read2st64_b32 v[134:135], %[la] offset0:22 offset1:23\n\t"
  "ds_read2st64_b32 v[136:137], %[la] offset0:24 offset1:25\n\t"
  "ds_read2st64_b32 v[138:139], %[la] offset0:26 offset1:27\n\t"
  "ds_read2st64_b32 v[124:125], %[la] offset0:12 offset1:13\n\t"
  "ds_read2st64_b32 v[126:127], %[la] offset0:14 offset1:15\n\t"
  "ds_read2st64_b32 v[128:129], %[la] offset0:16 offset1:17\n\t"
  "ds_read2st64_b32 v[130:131], %[la] offset0:18 offset1:19\n\t"
  "ds_read2st64_b32 v[140:141], %[la] offset0:28 offset1:29\n\t"
  "ds_read2st64_b32 v[142:143], %[la] offset0:30 offset1:31\n\t"
  "ds_read2st64_b32 v[144:145], %[la] offset0:32 offset1:33\n\t"
  "ds_read2st64_b32 v[146:147], %[la] offset0:34 offset1:35\n\t"
  "v_lshlrev_b32 v186, 16, %[t0]\n\t"
  "v_lshlrev_b32 v187, 16, %[t2]\n\t"
  "v_and_b32 v188, 0xffff0000, %[t0]\n\t"
  "v_and_b32 v189, 0xffff0000, %[t2]\n\t"
  "v_lshlrev_b32 v190, 16, %[t1]\n\t"
  "v_lshlrev_b32 v191, 16, %[t3]\n\t"
  "v_and_b32 v192, 0xffff0000, %[t1]\n\t"
  "v_and_b32 v193, 0xffff0000, %[t3]\n\t"
  "v_mov_b32 v234, 0x3f803f80\n\t"
  "s_waitcnt lgkmcnt(15)\n\t"
  "v_lshlrev_b32 v148, 16, v113\n\t"
  "v_and_b32 v149, 0xffff0000, v113\n\t"
  "v_lshlrev_b32 v150, 16, v114\n\t"
  "v_and_b32 v151, 0xffff0000, v114\n\t"
  "v_lshlrev_b32 v152, 16, v115\n\t"
  "v_and_b32 v153, 0xffff0000, v115\n\t"
  "v_lshlrev_b32 v154, 16, v116\n\t"
  "v_and_b32 v155, 0xffff0000, v116\n\t"
  "v_lshlrev_b32 v156, 16, v117\n\t"
  "v_and_b32 v157, 0xffff0000, v117\n\t"
  "s_waitcnt lgkmcnt(14)\n\t"
  "v_lshlrev_b32 v158, 16, v118\n\t"
  "v_and_b32 v159, 0xffff0000, v118\n\t"
  "v_lshlrev_b32 v160, 16, v119\n\t"
  "v_and_b32 v161, 0xffff0000, v119\n\t"
  "s_waitcnt lgkmcnt(13)\n\t"
  "v_lshlrev_b32 v162, 16, v120\n\t"
  "v_and_b32 v163, 0xffff0000, v120\n\t"
  "v_lshlrev_b32 v164, 16, v121\n\t"
  "v_and_b32 v165, 0xffff0000, v121\n\t"
  "s_waitcnt lgkmcnt(12)\n\t"
  "v_lshlrev_b32 v166, 16, v122\n\t"
  "v_and_b32 v167, 0xffff0000, v122\n\t"
  "v_lshlrev_b32 v168, 16, v123\n\t"
  "v_and_b32 v169, 0xffff0000, v123\n\t"
  "v_pk_mul_f32 v[194:195], v[148:149], v[192:193]\n\t"
  "v_pk_mul_f32 v[196:197], v[150:151], v[192:193]\n\t"
  "v_pk_mul_f32 v[198:199], v[152:153], v[192:193]\n\t"
  "v_pk_mul_f32 v[200:201], v[154:155], v[192:193]\n\t"
  "v_pk_mul_f32 v[202:203], v[156:157], v[192:193]\n\t"
  "v_pk_mul_f32 v[204:205], v[158:159], v[192:193]\n\t"
  "v_pk_mul_f32 v[206:207], v[160:161], v[192:193]\n\t"
  "v_pk_mul_f32 v[208:209], v[162:163], v[192:193]\n\t"
  "v_pk_mul_f32 v[210:211], v[150:151], v[190:191]\n\t"
  "v_pk_mul_f32 v[212:213], v[152:153], v[190:191]\n\t"
  "v_pk_mul_f32 v[214:215], v[154:155], v[190:191]\n\t"
  "v_pk_mul_f32 v[216:217], v[156:157], v[190:191]\n\t"
  "v_pk_mul_f32 v[218:219], v[158:159], v[190:191]\n\t"
  "v_pk_mul_f32 v[220:221], v[160:161], v[190:191]\n\t"
  "v_pk_mul_f32 v[222:223], v[162:163], v[190:191]\n\t"
  "v_pk_mul_f32 v[224:225], v[164:165], v[190:191]\n\t"
  "v_cvt_pk_bf16_f32 v194, v194, v210\n\t"
  "v_cvt_pk_bf16_f32 v195, v195, v211\n\t"
  "v_cvt_pk_bf16_f32 v196, v196, v212\n\t"
  "v_cvt_pk_bf16_f32 v197, v197, v213\n\t"
  "v_cvt_pk_bf16_f32 v198, v198, v214\n\t"
  "v_cvt_pk_bf16_f32 v199, v199, v215\n\t"
  "v_cvt_pk_bf16_f32 v200, v200, v216\n\t"
  "v_cvt_pk_bf16_f32 v201, v201, v217\n\t"
  "v_cvt_pk_bf16_f32 v202, v202, v218\n\t"
  "v_cvt_pk_bf16_f32 v203, v203, v219\n\t"
  "v_cvt_pk_bf16_f32 v204, v204, v220\n\t"
  "v_cvt_pk_bf16_f32 v205, v205, v221\n\t"
  "v_cvt_pk_bf16_f32 v206, v206, v222\n\t"
  "v_cvt_pk_bf16_f32 v207, v207, v223\n\t"
  "v_cvt_pk_bf16_f32 v208, v208, v224\n\t"
  "v_cvt_pk_bf16_f32 v209, v209, v225\n\t"
  "v_dot2_f32_bf16 v194, v194, v234, 0\n\t"
  "v_dot2_f32_bf16 v195, v195, v234, 0\n\t"
  "v_dot2_f32_bf16 v196, v196, v234, 0\n\t"
  "v_dot2_f32_bf16 v197, v197, v234, 0\n\t"
  "v_dot2_f32_bf16 v198, v198, v234, 0\n\t"
  "v_dot2_f32_bf16 v199, v199, v234, 0\n\t"
  "v_dot2_f32_bf16 v200, v200, v234, 0\n\t"
  "v_dot2_f32_bf16 v201, v201, v234, 0\n\t"
  "v_dot2_f32_bf16 v202, v202, v234, 0\n\t"
  "v_dot2_f32_bf16 v203, v203, v234, 0\n\t"
  "v_dot2_f32_bf16 v204, v204, v234, 0\n\t"
  "v_dot2_f32_bf16 v205, v205, v234, 0\n\t"
  "v_dot2_f32_bf16 v206, v206, v234, 0\n\t"
  "v_dot2_f32_bf16 v207, v207, v234, 0\n\t"
  "v_dot2_f32_bf16 v208, v208, v234, 0\n\t"
  "v_dot2_f32_bf16 v209, v209, v234, 0\n\t"
  "v_pk_mul_f32 v[210:211], v[152:153], v[188:189]\n\t"
  "v_pk_mul_f32 v[212:213], v[154:155], v[188:189]\n\t"
  "v_pk_mul_f32 v[214:215], v[156:157], v[188:189]\n\t"
  "v_pk_mul_f32 v[216:217], v[158:159], v[188:189]\n\t"
  "v_pk_mul_f32 v[218:219], v[160:161], v[188:189]\n\t"
  "v_pk_mul_f32 v[220:221], v[162:163], v[188:189]\n\t"
  "v_pk_mul_f32 v[222:223], v[164:165], v[188:189]\n\t"
  "v_pk_mul_f32 v[224:225], v[166:167], v[188:189]\n\t"
  "v_cvt_pk_bf16_f32 v194, v194, v210\n\t"
  "v_cvt_pk_bf16_f32 v195, v195, v211\n\t"
  "v_cvt_pk_bf16_f32 v196, v196, v212\n\t"
  "v_cvt_pk_bf16_f32 v197, v197, v213\n\t"
  "v_cvt_pk_bf16_f32 v198, v198, v214\n\t"
  "v_cvt_pk_bf16_f32 v199, v199, v215\n\t"
  "v_cvt_pk_bf16_f32 v200, v200, v216\n\t"
  "v_cvt_pk_bf16_f32 v201, v201, v217\n\t"
  "v_cvt_pk_bf16_f32 v202, v202, v218\n\t"
  "v_cvt_pk_bf16_f32 v203, v203, v219\n\t"
  "v_cvt_pk_bf16_f32 v204, v204, v220\n\t"
  "v_cvt_pk_bf16_f32 v205, v205, v221\n\t"
  "v_cvt_pk_bf16_f32 v206, v206, v222\n\t"
  "v_cvt_pk_bf16_f32 v207, v207, v223\n\t"
  "v_cvt_pk_bf16_f32 v208, v208, v224\n\t"
  "v_cvt_pk_bf16_f32 v209, v209, v225\n\t"
  "v_dot2_f32_bf16 v194, v194, v234, 0\n\t"
  "v_dot2_f32_bf16 v195, v195, v234, 0\n\t"
  "v_dot2_f32_bf16 v196, v196, v234, 0\n\t"
  "v_dot2_f32_bf16 v197, v197, v234, 0\n\t"
  "v_dot2_f32_bf16 v198, v198, v234, 0\n\t"
  "v_dot2_f32_bf16 v199, v199, v234, 0\n\t"
  "v_dot2_f32_bf16 v200, v200, v234, 0\n\t"
  "v_dot2_f32_bf16 v201, v201, v234, 0\n\t"
  "v_dot2_f32_bf16 v202, v202, v234, 0\n\t"
  "v_dot2_f32_bf16 v203, v203, v234, 0\n\t"
  "v_dot2_f32_bf16 v204, v204, v234, 0\n\t"
  "v_dot2_f32_bf16 v205, v205, v234, 0\n\t"
  "v_dot2_f32_bf16 v206, v206, v234, 0\n\t"
  "v_dot2_f32_bf16 v207, v207, v234, 0\n\t"
  "v_dot2_f32_bf16 v208, v208, v234, 0\n\t"
  "v_dot2_f32_bf16 v209, v209, v234, 0\n\t"
  "v_pk_mul_f32 v[210:211], v[154:155], v[186:187]\n\t"
  "v_pk_mul_f32 v[212:213], v[156:157], v[186:187]\n\t"
  "v_pk_mul_f32 v[214:215], v[158:159], v[186:187]\n\t"
  "v_pk_mul_f32 v[216:217], v[160:161], v[186:187]\n\t"
  "v_pk_mul_f32 v[218:219], v[162:163], v[186:187]\n\t"
  "v_pk_mul_f32 v[220:221], v[164:165], v[186:187]\n\t"
  "v_pk_mul_f32 v[222:223], v[166:167], v[186:187]\n\t"
  "v_pk_mul_f32 v[224:225], v[168:169], v[186:187]\n\t"
  "v_cvt_pk_bf16_f32 v194, v194, v210\n\t"
  "v_cvt_pk_bf16_f32 v195, v195, v211\n\t"
  "v_cvt_pk_bf16_f32 v196, v196, v212\n\t"
  "v_cvt_pk_bf16_f32 v197, v197, v213\n\t"
  "v_cvt_pk_bf16_f32 v198, v198, v214\n\t"
  "v_cvt_pk_bf16_f32 v199, v199, v215\n\t"
  "v_cvt_pk_bf16_f32 v200, v200, v216\n\t"
  "v_cvt_pk_bf16_f32 v201, v201, v217\n\t"
  "v_cvt_pk_bf16_f32 v202, v202, v218\n\t"
  "v_cvt_pk_bf16_f32 v203, v203, v219\n\t"
  "v_cvt_pk_bf16_f32 v204, v204, v220\n\t"
  "v_cvt_pk_bf16_f32 v205, v205, v221\n\t"
  "v_cvt_pk_bf16_f32 v206, v206, v222\n\t"
  "v_cvt_pk_bf16_f32 v207, v207, v223\n\t"
  "v_cvt_pk_bf16_f32 v208, v208, v224\n\t"
  "v_cvt_pk_bf16_f32 v209, v209, v225\n\t"
  "v_dot2_f32_bf16 v194, v194, v234, 0\n\t"
  "v_dot2_f32_bf16 v195, v195, v234, 0\n\t"
  "v_dot2_f32_bf16 v196, v196, v234, 0\n\t"
  "v_dot2_f32_bf16 v197, v197, v234, 0\n\t"
  "v_dot2_f32_bf16 v198, v198, v234, 0\n\t"
  "v_dot2_f32_bf16 v199, v199, v234, 0\n\t"
  "v_dot2_f32_bf16 v200, v200, v234, 0\n\t"
  "v_dot2_f32_bf16 v201, v201, v234, 0\n\t"
  "v_dot2_f32_bf16 v202, v202, v234, 0\n\t"
  "v_dot2_f32_bf16 v203, v203, v234, 0\n\t"
  "v_dot2_f32_bf16 v204, v204, v234, 0\n\t"
  "v_dot2_f32_bf16 v205, v205, v234, 0\n\t"
  "v_dot2_f32_bf16 v206, v206, v234, 0\n\t"
  "v_dot2_f32_bf16 v207, v207, v234, 0\n\t"
  "v_dot2_f32_bf16 v208, v208, v234, 0\n\t"
  "v_dot2_f32_bf16 v209, v209, v234, 0\n\t"
  "s_waitcnt lgkmcnt(11)\n\t"
  "v_and_b32 v210, 0xffff, v132\n\t"
  "v_and_b32 v211, 0xffff0000, v132\n\t"
  "v_and_b32 v212, 0xffff, v133\n\t"
  "v_and_b32 v213, 0xffff0000, v133\n\t"
  "s_waitcnt lgkmcnt(10)\n\t"
  "v_and_b32 v214, 0xffff, v134\n\t"
  "v_and_b32 v215, 0xffff0000, v134\n\t"
  "v_and_b32 v216, 0xffff, v135\n\t"
  "v_and_b32 v217, 0xffff0000, v135\n\t"
  "s_waitcnt lgkmcnt(9)\n\t"
  "v_and_b32 v218, 0xffff, v136\n\t"
  "v_and_b32 v219, 0xffff0000, v136\n\t"
  "v_and_b32 v220, 0xffff, v137\n\t"
  "v_and_b32 v221, 0xffff0000, v137\n\t"
  "s_waitcnt lgkmcnt(8)\n\t"
  "v_and_b32 v222, 0xffff, v138\n\t"
  "v_and_b32 v223, 0xffff0000, v138\n\t"
  "v_and_b32 v224, 0xffff, v139\n\t"
  "v_and_b32 v225, 0xffff0000, v139\n\t"
  "v_cvt_pk_bf16_f32 v194, v194, v195\n\t"
  "v_cvt_pk_bf16_f32 v196, v196, v197\n\t"
  "v_cvt_pk_bf16_f32 v198, v198, v199\n\t"
  "v_cvt_pk_bf16_f32 v200, v200, v201\n\t"
  "v_cvt_pk_bf16_f32 v202, v202, v203\n\t"
  "v_cvt_pk_bf16_f32 v204, v204, v205\n\t"
  "v_cvt_pk_bf16_f32 v206, v206, v207\n\t"
  "v_cvt_pk_bf16_f32 v208, v208, v209\n\t"
  "v_dot2_f32_bf16 v210, v194, v210, 0\n\t"
  "v_dot2_f32_bf16 v211, v194, v211, 0\n\t"
  "v_dot2_f32_bf16 v212, v196, v212, 0\n\t"
  "v_dot2_f32_bf16 v213, v196, v213, 0\n\t"
  "v_dot2_f32_bf16 v214, v198, v214, 0\n\t"
  "v_dot2_f32_bf16 v215, v198, v215, 0\n\t"
  "v_dot2_f32_bf16 v216, v200, v216, 0\n\t"
  "v_dot2_f32_bf16 v217, v200, v217, 0\n\t"
  "v_dot2_f32_bf16 v218, v202, v218, 0\n\t"
  "v_dot2_f32_bf16 v219, v202, v219, 0\n\t"
  "v_dot2_f32_bf16 v220, v204, v220, 0\n\t"
  "v_dot2_f32_bf16 v221, v204, v221, 0\n\t"
  "v_dot2_f32_bf16 v222, v206, v222, 0\n\t"
  "v_dot2_f32_bf16 v223, v206, v223, 0\n\t"
  "v_dot2_f32_bf16 v224, v208, v224, 0\n\t"
  "v_dot2_f32_bf16 v225, v208, v225, 0\n\t"
  "v_cvt_pk_bf16_f32 v229, v212, v210\n\t"
  "v_cvt_pk_bf16_f32 v228, v216, v214\n\t"
  "v_cvt_pk_bf16_f32 v227, v220, v218\n\t"
  "v_cvt_pk_bf16_f32 v226, v224, v222\n\t"
  "buffer_store_dwordx4 v[226:229], %[vo], %[srd], 0 offen offset:0 sc1\n\t"
  "v_cvt_pk_bf16_f32 v233, v213, v211\n\t"
  "v_cvt_pk_bf16_f32 v232, v217, v215\n\t"
  "v_cvt_pk_bf16_f32 v231, v221, v219\n\t"
  "v_cvt_pk_bf16_f32 v230, v225, v223\n\t"
  "buffer_store_dwordx4 v[230:233], %[vo], %[srd], 0 offen offset:128 sc1\n\t"
  "s_waitcnt lgkmcnt(7)\n\t"
  "v_lshlrev_b32 v170, 16, v124\n\t"
  "v_and_b32 v171, 0xffff0000, v124\n\t"
  "v_lshlrev_b32 v172, 16, v125\n\t"
  "v_and_b32 v173, 0xffff0000, v125\n\t"
  "s_waitcnt lgkmcnt(6)\n\t"
  "v_lshlrev_b32 v174, 16, v126\n\t"
  "v_and_b32 v175, 0xffff0000, v126\n\t"
  "v_lshlrev_b32 v176, 16, v127\n\t"
  "v_and_b32 v177, 0xffff0000, v127\n\t"
  "s_waitcnt lgkmcnt(5)\n\t"
  "v_lshlrev_b32 v178, 16, v128\n\t"
  "v_and_b32 v179, 0xffff0000, v128\n\t"
  "v_lshlrev_b32 v180, 16, v129\n\t"
  "v_and_b32 v181, 0xffff0000, v129\n\t"
  "s_waitcnt lgkmcnt(4)\n\t"
  "v_lshlrev_b32 v182, 16, v130\n\t"
  "v_and_b32 v183, 0xffff0000, v130\n\t"
  "v_lshlrev_b32 v184, 16, v131\n\t"
  "v_and_b32 v185, 0xffff0000, v131\n\t"
  "v_pk_mul_f32 v[194:195], v[164:165], v[192:193]\n\t"
  "v_pk_mul_f32 v[196:197], v[166:167], v[192:193]\n\t"
  "v_pk_mul_f32 v[198:199], v[168:169], v[192:193]\n\t"
  "v_pk_mul_f32 v[200:201], v[170:171], v[192:193]\n\t"
  "v_pk_mul_f32 v[202:203], v[172:173], v[192:193]\n\t"
  "v_pk_mul_f32 v[204:205], v[174:175], v[192:193]\n\t"
  "v_pk_mul_f32 v[206:207], v[176:177], v[192:193]\n\t"
  "v_pk_mul_f32 v[208:209], v[178:179], v[192:193]\n\t"
  "v_pk_mul_f32 v[210:211], v[166:167], v[190:191]\n\t"
  "v_pk_mul_f32 v[212:213], v[168:169], v[190:191]\n\t"
  "v_pk_mul_f32 v[214:215], v[170:171], v[190:191]\n\t"
  "v_pk_mul_f32 v[216:217], v[172:173], v[190:191]\n\t"
  "v_pk_mul_f32 v[218:219], v[174:175], v[190:191]\n\t"
  "v_pk_mul_f32 v[220:221], v[176:177], v[190:191]\n\t"
  "v_pk_mul_f32 v[222:223], v[178:179], v[190:191]\n\t"
  "v_pk_mul_f32 v[224:225], v[180:181], v[190:191]\n\t"
  "v_cvt_pk_bf16_f32 v194, v194, v210\n\t"
  "v_cvt_pk_bf16_f32 v195, v195, v211\n\t"
  "v_cvt_pk_bf16_f32 v196, v196, v212\n\t"
  "v_cvt_pk_bf16_f32 v197, v197, v213\n\t"
  "v_cvt_pk_bf16_f32 v198, v198, v214\n\t"
  "v_cvt_pk_bf16_f32 v199, v199, v215\n\t"
  "v_cvt_pk_bf16_f32 v200, v200, v216\n\t"
  "v_cvt_pk_bf16_f32 v201, v201, v217\n\t"
  "v_cvt_pk_bf16_f32 v202, v202, v218\n\t"
  "v_cvt_pk_bf16_f32 v203, v203, v219\n\t"
  "v_cvt_pk_bf16_f32 v204, v204, v220\n\t"
  "v_cvt_pk_bf16_f32 v205, v205, v221\n\t"
  "v_cvt_pk_bf16_f32 v206, v206, v222\n\t"
  "v_cvt_pk_bf16_f32 v207, v207, v223\n\t"
  "v_cvt_pk_bf16_f32 v208, v208, v224\n\t"
  "v_cvt_pk_bf16_f32 v209, v209, v225\n\t"
  "v_dot2_f32_bf16 v194, v194, v234, 0\n\t"
  "v_dot2_f32_bf16 v195, v195, v234, 0\n\t"
  "v_dot2_f32_bf16 v196, v196, v234, 0\n\t"
  "v_dot2_f32_bf16 v197, v197, v234, 0\n\t"
  "v_dot2_f32_bf16 v198, v198, v234, 0\n\t"
  "v_dot2_f32_bf16 v199, v199, v234, 0\n\t"
  "v_dot2_f32_bf16 v200, v200, v234, 0\n\t"
  "v_dot2_f32_bf16 v201, v201, v234, 0\n\t"
  "v_dot2_f32_bf16 v202, v202, v234, 0\n\t"
  "v_dot2_f32_bf16 v203, v203, v234, 0\n\t"
  "v_dot2_f32_bf16 v204, v204, v234, 0\n\t"
  "v_dot2_f32_bf16 v205, v205, v234, 0\n\t"
  "v_dot2_f32_bf16 v206, v206, v234, 0\n\t"
  "v_dot2_f32_bf16 v207, v207, v234, 0\n\t"
  "v_dot2_f32_bf16 v208, v208, v234, 0\n\t"
  "v_dot2_f32_bf16 v209, v209, v234, 0\n\t"
  "v_pk_mul_f32 v[210:211], v[168:169], v[188:189]\n\t"
  "v_pk_mul_f32 v[212:213], v[170:171], v[188:189]\n\t"
  "v_pk_mul_f32 v[214:215], v[172:173], v[188:189]\n\t"
  "v_pk_mul_f32 v[216:217], v[174:175], v[188:189]\n\t"
  "v_pk_mul_f32 v[218:219], v[176:177], v[188:189]\n\t"
  "v_pk_mul_f32 v[220:221], v[178:179], v[188:189]\n\t"
  "v_pk_mul_f32 v[222:223], v[180:181], v[188:189]\n\t"
  "v_pk_mul_f32 v[224:225], v[182:183], v[188:189]\n\t"
  "v_cvt_pk_bf16_f32 v194, v194, v210\n\t"
  "v_cvt_pk_bf16_f32 v195, v195, v211\n\t"
  "v_cvt_pk_bf16_f32 v196, v196, v212\n\t"
  "v_cvt_pk_bf16_f32 v197, v197, v213\n\t"
  "v_cvt_pk_bf16_f32 v198, v198, v214\n\t"
  "v_cvt_pk_bf16_f32 v199, v199, v215\n\t"
  "v_cvt_pk_bf16_f32 v200, v200, v216\n\t"
  "v_cvt_pk_bf16_f32 v201, v201, v217\n\t"
  "v_cvt_pk_bf16_f32 v202, v202, v218\n\t"
  "v_cvt_pk_bf16_f32 v203, v203, v219\n\t"
  "v_cvt_pk_bf16_f32 v204, v204, v220\n\t"
  "v_cvt_pk_bf16_f32 v205, v205, v221\n\t"
  "v_cvt_pk_bf16_f32 v206, v206, v222\n\t"
  "v_cvt_pk_bf16_f32 v207, v207, v223\n\t"
  "v_cvt_pk_bf16_f32 v208, v208, v224\n\t"
  "v_cvt_pk_bf16_f32 v209, v209, v225\n\t"
  "v_dot2_f32_bf16 v194, v194, v234, 0\n\t"
  "v_dot2_f32_bf16 v195, v195, v234, 0\n\t"
  "v_dot2_f32_bf16 v196, v196, v234, 0\n\t"
  "v_dot2_f32_bf16 v197, v197, v234, 0\n\t"
  "v_dot2_f32_bf16 v198, v198, v234, 0\n\t"
  "v_dot2_f32_bf16 v199, v199, v234, 0\n\t"
  "v_dot2_f32_bf16 v200, v200, v234, 0\n\t"
  "v_dot2_f32_bf16 v201, v201, v234, 0\n\t"
  "v_dot2_f32_bf16 v202, v202, v234, 0\n\t"
  "v_dot2_f32_bf16 v203, v203, v234, 0\n\t"
  "v_dot2_f32_bf16 v204, v204, v234, 0\n\t"
  "v_dot2_f32_bf16 v205, v205, v234, 0\n\t"
  "v_dot2_f32_bf16 v206, v206, v234, 0\n\t"
  "v_dot2_f32_bf16 v207, v207, v234, 0\n\t"
  "v_dot2_f32_bf16 v208, v208, v234, 0\n\t"
  "v_dot2_f32_bf16 v209, v209, v234, 0\n\t"
  "v_pk_mul_f32 v[210:211], v[170:171], v[186:187]\n\t"
  "v_pk_mul_f32 v[212:213], v[172:173], v[186:187]\n\t"
  "v_pk_mul_f32 v[214:215], v[174:175], v[186:187]\n\t"
  "v_pk_mul_f32 v[216:217], v[176:177], v[186:187]\n\t"
  "v_pk_mul_f32 v[218:219], v[178:179], v[186:187]\n\t"
  "v_pk_mul_f32 v[220:221], v[180:181], v[186:187]\n\t"
  "v_pk_mul_f32 v[222:223], v[182:183], v[186:187]\n\t"
  "v_pk_mul_f32 v[224:225], v[184:185], v[186:187]\n\t"
  "v_cvt_pk_bf16_f32 v194, v194, v210\n\t"
  "v_cvt_pk_bf16_f32 v195, v195, v211\n\t"
  "v_cvt_pk_bf16_f32 v196, v196, v212\n\t"
  "v_cvt_pk_bf16_f32 v197, v197, v213\n\t"
  "v_cvt_pk_bf16_f32 v198, v198, v214\n\t"
  "v_cvt_pk_bf16_f32 v199, v199, v215\n\t"
  "v_cvt_pk_bf16_f32 v200, v200, v216\n\t"
  "v_cvt_pk_bf16_f32 v201, v201, v217\n\t"
  "v_cvt_pk_bf16_f32 v202, v202, v218\n\t"
  "v_cvt_pk_bf16_f32 v203, v203, v219\n\t"
  "v_cvt_pk_bf16_f32 v204, v204, v220\n\t"
  "v_cvt_pk_bf16_f32 v205, v205, v221\n\t"
  "v_cvt_pk_bf16_f32 v206, v206, v222\n\t"
  "v_cvt_pk_bf16_f32 v207, v207, v223\n\t"
  "v_cvt_pk_bf16_f32 v208, v208, v224\n\t"
  "v_cvt_pk_bf16_f32 v209, v209, v225\n\t"
  "v_dot2_f32_bf16 v194, v194, v234, 0\n\t"
  "v_dot2_f32_bf16 v195, v195, v234, 0\n\t"
  "v_dot2_f32_bf16 v196, v196, v234, 0\n\t"
  "v_dot2_f32_bf16 v197, v197, v234, 0\n\t"
  "v_dot2_f32_bf16 v198, v198, v234, 0\n\t"
  "v_dot2_f32_bf16 v199, v199, v234, 0\n\t"
  "v_dot2_f32_bf16 v200, v200, v234, 0\n\t"
  "v_dot2_f32_bf16 v201, v201, v234, 0\n\t"
  "v_dot2_f32_bf16 v202, v202, v234, 0\n\t"
  "v_dot2_f32_bf16 v203, v203, v234, 0\n\t"
  "v_dot2_f32_bf16 v204, v204, v234, 0\n\t"
  "v_dot2_f32_bf16 v205, v205, v234, 0\n\t"
  "v_dot2_f32_bf16 v206, v206, v234, 0\n\t"
  "v_dot2_f32_bf16 v207, v207, v234, 0\n\t"
  "v_dot2_f32_bf16 v208, v208, v234, 0\n\t"
  "v_dot2_f32_bf16 v209, v209, v234, 0\n\t"
  "s_waitcnt lgkmcnt(3)\n\t"
  "v_and_b32 v210, 0xffff, v140\n\t"
  "v_and_b32 v211, 0xffff0000, v140\n\t"
  "v_and_b32 v212, 0xffff, v141\n\t"
  "v_and_b32 v213, 0xffff0000, v141\n\t"
  "s_waitcnt lgkmcnt(2)\n\t"
  "v_and_b32 v214, 0xffff, v142\n\t"
  "v_and_b32 v215, 0xffff0000, v142\n\t"
  "v_and_b32 v216, 0xffff, v143\n\t"
  "v_and_b32 v217, 0xffff0000, v143\n\t"
  "s_waitcnt lgkmcnt(1)\n\t"
  "v_and_b32 v218, 0xffff, v144\n\t"
  "v_and_b32 v219, 0xffff0000, v144\n\t"
  "v_and_b32 v220, 0xffff, v145\n\t"
  "v_and_b32 v221, 0xffff0000, v145\n\t"
  "s_waitcnt lgkmcnt(0)\n\t"
  "v_and_b32 v222, 0xffff, v146\n\t"
  "v_and_b32 v223, 0xffff0000, v146\n\t"
  "v_and_b32 v224, 0xffff, v147\n\t"
  "v_and_b32 v225, 0xffff0000, v147\n\t"
  "v_cvt_pk_bf16_f32 v194, v194, v195\n\t"
  "v_cvt_pk_bf16_f32 v196, v196, v197\n\t"
  "v_cvt_pk_bf16_f32 v198, v198, v199\n\t"
  "v_cvt_pk_bf16_f32 v200, v200, v201\n\t"
  "v_cvt_pk_bf16_f32 v202, v202, v203\n\t"
  "v_cvt_pk_bf16_f32 v204, v204, v205\n\t"
  "v_cvt_pk_bf16_f32 v206, v206, v207\n\t"
  "v_cvt_pk_bf16_f32 v208, v208, v209\n\t"
  "v_dot2_f32_bf16 v210, v194, v210, 0\n\t"
  "v_dot2_f32_bf16 v211, v194, v211, 0\n\t"
  "v_dot2_f32_bf16 v212, v196, v212, 0\n\t"
  "v_dot2_f32_bf16 v213, v196, v213, 0\n\t"
  "v_dot2_f32_bf16 v214, v198, v214, 0\n\t"
  "v_dot2_f32_bf16 v215, v198, v215, 0\n\t"
  "v_dot2_f32_bf16 v216, v200, v216, 0\n\t"
  "v_dot2_f32_bf16 v217, v200, v217, 0\n\t"
  "v_dot2_f32_bf16 v218, v202, v218, 0\n\t"
  "v_dot2_f32_bf16 v219, v202, v219, 0\n\t"
  "v_dot2_f32_bf16 v220, v204, v220, 0\n\t"
  "v_dot2_f32_bf16 v221, v204, v221, 0\n\t"
  "v_dot2_f32_bf16 v222, v206, v222, 0\n\t"
  "v_dot2_f32_bf16 v223, v206, v223, 0\n\t"
  "v_dot2_f32_bf16 v224, v208, v224, 0\n\t"
  "v_dot2_f32_bf16 v225, v208, v225, 0\n\t"
  "v_cvt_pk_bf16_f32 v229, v212, v210\n\t"
  "v_cvt_pk_bf16_f32 v228, v216, v214\n\t"
  "v_cvt_pk_bf16_f32 v227, v220, v218\n\t"
  "v_cvt_pk_bf16_f32 v226, v224, v222\n\t"
  "buffer_store_dwordx4 v[226:229], %[vo], %[srd], 0 offen offset:64 sc1\n\t"
  "v_cvt_pk_bf16_f32 v233, v213, v211\n\t"
  "v_cvt_pk_bf16_f32 v232, v217, v215\n\t"
  "v_cvt_pk_bf16_f32 v231, v221, v219\n\t"
  "v_cvt_pk_bf16_f32 v230, v225, v223\n\t"
  "buffer_store_dwordx4 v[230:233], %[vo], %[srd], 0 offen offset:192 sc1\n\t"
  "s_nop 1\n\t"
      :: [srd] "s"(srd), [la] "v"(lb + lkv + lane * 4), [t0] "v"(taps[0]), [t1] "v"(taps[1]), [t2] "v"(taps[2]), [t3] "v"(taps[3]),
         [vo] "v"(OFF_KVT + (tb * 160 + 2 * (d0 >> 3)) * 128 + ((lane ^ (tb >> 1)) & 3) * 16)
      : "memory",
"v112", "v113", "v114", "v115", "v116", "v117", "v118", "v119", "v120", "v121", "v122", "v123", "v124", "v125", "v126", "v127", "v128", "v129", "v130", "v131", "v132", "v133", "v134", "v135", "v136", "v137", "v138", "v139", "v140", "v141", "v142", "v143", "v144", "v145", "v146", "v147", "v148", "v149", "v150", "v151", "v152", "v153", "v154", "v155", "v156", "v157", "v158", "v159", "v160", "v161", "v162", "v163", "v164", "v165", "v166", "v167", "v168", "v169", "v170", "v171", "v172", "v173", "v174", "v175", "v176", "v177", "v178", "v179", "v180", "v181", "v182", "v183", "v184", "v185", "v186", "v187", "v188", "v189", "v190", "v191", "v192", "v193", "v194", "v195", "v196", "v197", "v198", "v199", "v200", "v201", "v202", "v203", "v204", "v205", "v206", "v207", "v208", "v209", "v210", "v211", "v212", "v213", "v214", "v215", "v216", "v217", "v218", "v219", "v220", "v221", "v222", "v223", "v224", "v225", "v226", "v227", "v228", "v229", "v230", "v231", "v232", "v233", "v234"
    );
    {
      const unsigned d = 8 * (cb >> 1) + 2 * w + (cb & 1);
      const unsigned r = lane & 15, g = lane >> 4;
      const unsigned pp = 25 + r - 8 * g;
      const unsigned va = lb + L_FC + w * FC_CH + (pp & 3) * FC_PITCH + (pp & ~3u) * 2;
      unsigned vb[4];
  #pragma unroll
      for (int k = 0; k < 4; ++k) { const unsigned G = 14 - 4 * k + 2 * r + g; vb[k] = lb + L_KV + G * 64 + ((w ^ (G >> 2)) & 3) * 16; }
      const unsigned gx = (2 * r + (g >> 1)) * 64 + ((w ^ (r >> 1)) & 3) * 16;
      const unsigned vx = lb + L_X + gx + (g & 1) * 8, vs = lb + L_ST + r * 128 + ((2 * g) ^ stkey(r)) * 16;
      const unsigned to = 256 * (g & 1) + 16 * r + 8 * (g >> 1);
      const unsigned vo = (to >> 6) * 81920u + (((to & 63) * 2) ^ (((d >> 1) & 3) * 32 + (d & 8) * 2));
      const unsigned wh = (lane >> 2) & 3, mcol = (wh < 3 ? wh : 4) * 640 + d;
      const unsigned jd = 8 * g - 9 - r, dv = dval << (16 * (jd & 1));
      const u32x4 vd = {jd >> 1 == 0 ? dv : 0u, jd >> 1 == 1 ? dv : 0u, jd >> 1 == 2 ? dv : 0u, jd >> 1 == 3 ? dv : 0u};
      c_vf2 = lane < 32 ? OFF_FA3 + (32 * w + lane) * 128 : OFF_FA2 + ((lane & 3) * 60 + (mcol >> 6)) * 128;
      c_vp0 = tid < 8 * 8 ? OFF_FX + ((tid >> 3) * 160 + cb) * 128 + (tid & 7) * 16 : OOB;
      c_vf1 = OFF_FA1 + ((cb & ~31u) + 8 * w + (lane & 7)) * 128;
      c_vf3 = lane < 8 ? (cb < 128 ? OFF_FA3 + cb * 128 : U_FLAG(cb)) + lane * 16 : OOB;
      c_vfw = OFF_FW1 + (j * 4 + (lane & 3)) * 128;
      c_va = va;
      c_vb0 = vb[0];
      c_vb1 = vb[1];
      c_vb2 = vb[2];
      c_vb3 = vb[3];
      c_vx = vx;
      c_vs = vs;
      c_vs2 = vs ^ 16;
      c_vo = vo;
      c_vd = vd;
      c_vl16 = lane * 16;
      c_vkv = (lane >> 3) * 20480 + (lane & 7) * 16;
      c_vmod = OFF_MODP + ((lane & 3) * 3840 + mcol) * 4;
      asm volatile("" : "+v"(c_vf2), "+v"(c_vf1), "+v"(c_vp0), "+v"(c_vf3), "+v"(c_vfw), "+v"(c_va), "+v"(c_vb0), "+v"(c_vb1), "+v"(c_vb2), "+v"(c_vb3), "+v"(c_vx), "+v"(c_vs), "+v"(c_vs2), "+v"(c_vo), "+v"(c_vd), "+v"(c_vl16), "+v"(c_vkv), "+v"(c_vmod));
    }
    char* fcw = smem + L_FC + w * FC_CH;
    auto put = [&](unsigned u, u32x4 a, u32x2 h) {
      const u32 W[6] = {a[0], a[1], a[2], a[3], h[0], h[1]};
      u32 O[5];
#pragma unroll
      for (int i = 0; i < 5; ++i) O[i] = __builtin_amdgcn_alignbit(W[i + 1], W[i], 16);
      *(u32x4*)(fcw + 0 * FC_PITCH + u * 16) = a;
      *(u32x4*)(fcw + 1 * FC_PITCH + u * 16) = u32x4{O[0], O[1], O[2], O[3]};
      *(u32x4*)(fcw + 2 * FC_PITCH + u * 16) = u32x4{W[1], W[2], W[3], W[4]};
      *(u32x4*)(fcw + 3 * FC_PITCH + u * 16) = u32x4{O[1], O[2], O[3], O[4]};
    };
#pragma unroll
    for (int n = 0; n < 4; ++n) {
      u32x2 h;
#pragma unroll
      for (int e = 0; e < 2; ++e) h[e] = __builtin_amdgcn_update_dpp(n < 3 ? rfl(fa[n < 3 ? n + 1 : n][e]) : 0u, fa[n][e], 0x130, 0xf, 0xf, false);
      put(lane + 64 * n + 2, fa[n], h);
    }
    if (lane < 2) put(1 - lane, u32x4{0, 0, 0, 0}, lane == 0 ? u32x2{fa[0][0], fa[0][1]} : u32x2{0, 0});
    FENCE();
#pragma unroll
    for (int n = 0; n < 5; ++n) *(u32x4*)(smem + L_XR + n * 4096 + w * 1024 + lane * 16) = xr[n];
    if (w == 1) *(u32x4*)(smem + L_XR + 20480 + lane * 16) = cr;
    FENCE();
    asm volatile("s_waitcnt vmcnt(0) lgkmcnt(0)\n\ts_barrier" ::: "memory");
    __builtin_amdgcn_raw_buffer_store_b128(u32x4{(u32)tag, (u32)(tag >> 32), (u32)tag, (u32)(tag >> 32)}, srd, tid < 8 ? OFF_FA1 + cb * 128 + tid * 16 : OOB, 0, SC1);
    FENCE();
    if (isx) {
      const unsigned g = lane >> 4, q = (lane >> 2) & 3, ch = 16 * (g >> 1) + (lane & 15);
      asm volatile(
  "s_nop 4\n\t"
  "s_add_u32 s44, %[w], 1\n\t"
  "s_and_b32 s44, s44, 2\n\t"
  "s_cbranch_scc0 Lxs%=\n\t"
  "ds_read_b64_tr_b16 v[96:97], %[vtx] offset:0\n\t"
  "ds_read_b64_tr_b16 v[98:99], %[vtx] offset:512\n\t"
  "ds_read_b64_tr_b16 v[100:101], %[vtx] offset:64\n\t"
  "ds_read_b64_tr_b16 v[102:103], %[vtx] offset:576\n\t"
  "ds_read_b64_tr_b16 v[104:105], %[vtx] offset:2048\n\t"
  "ds_read_b64_tr_b16 v[106:107], %[vtx] offset:2560\n\t"
  "ds_read_b64_tr_b16 v[108:109], %[vtx] offset:2112\n\t"
  "ds_read_b64_tr_b16 v[110:111], %[vtx] offset:2624\n\t"
  "ds_read_b64_tr_b16 v[112:113], %[vtx] offset:4096\n\t"
  "ds_read_b64_tr_b16 v[114:115], %[vtx] offset:4608\n\t"
  "ds_read_b64_tr_b16 v[116:117], %[vtx] offset:4160\n\t"
  "ds_read_b64_tr_b16 v[118:119], %[vtx] offset:4672\n\t"
  "ds_read_b64_tr_b16 v[120:121], %[vtx] offset:6144\n\t"
  "ds_read_b64_tr_b16 v[122:123], %[vtx] offset:6656\n\t"
  "ds_read_b64_tr_b16 v[124:125], %[vtx] offset:6208\n\t"
  "ds_read_b64_tr_b16 v[126:127], %[vtx] offset:6720\n\t"
  "s_add_u32 s44, %[sXo], 0\n\t"
  "s_waitcnt lgkmcnt(14)\n\t"
  "buffer_store_dwordx4 v[96:99], %[vxo], %[srd], s44 offen offset:0 sc1\n\t"
  "ds_read_b64_tr_b16 v[128:129], %[vtx] offset:8192\n\t"
  "ds_read_b64_tr_b16 v[130:131], %[vtx] offset:8704\n\t"
  "s_waitcnt lgkmcnt(14)\n\t"
  "buffer_store_dwordx4 v[100:103], %[vxo], %[srd], s44 offen offset:1024 sc1\n\t"
  "ds_read_b64_tr_b16 v[132:133], %[vtx] offset:8256\n\t"
  "ds_read_b64_tr_b16 v[134:135], %[vtx] offset:8768\n\t"
  "s_waitcnt lgkmcnt(14)\n\t"
  "buffer_store_dwordx4 v[104:107], %[vxo], %[srd], s44 offen offset:2048 sc1\n\t"
  "s_waitcnt lgkmcnt(12)\n\t"
  "buffer_store_dwordx4 v[108:111], %[vxo], %[srd], s44 offen offset:3072 sc1\n\t"
  "s_add_u32 s44, %[sXo], 4096\n\t"
  "s_waitcnt lgkmcnt(10)\n\t"
  "buffer_store_dwordx4 v[112:115], %[vxo], %[srd], s44 offen offset:0 sc1\n\t"
  "s_waitcnt lgkmcnt(8)\n\t"
  "buffer_store_dwordx4 v[116:119], %[vxo], %[srd], s44 offen offset:1024 sc1\n\t"
  "s_waitcnt lgkmcnt(6)\n\t"
  "buffer_store_dwordx4 v[120:123], %[vxo], %[srd], s44 offen offset:2048 sc1\n\t"
  "s_waitcnt lgkmcnt(4)\n\t"
  "buffer_store_dwordx4 v[124:127], %[vxo], %[srd], s44 offen offset:3072 sc1\n\t"
  "s_add_u32 s44, %[sXo], 8192\n\t"
  "s_waitcnt lgkmcnt(2)\n\t"
  "buffer_store_dwordx4 v[128:131], %[vxo], %[srd], s44 offen offset:0 sc1\n\t"
  "s_waitcnt lgkmcnt(0)\n\t"
  "buffer_store_dwordx4 v[132:135], %[vxo], %[srd], s44 offen offset:1024 sc1\n\t"
  "s_branch Lxe%=\n\t"
  "Lxs%=:\n\t"
  "ds_read_b128 v[96:99], %[vsx] offset:0\n\t"
  "ds_read_b128 v[100:103], %[vsx] offset:2048\n\t"
  "ds_read_b128 v[104:107], %[vsx] offset:4096\n\t"
  "ds_read_b128 v[108:111], %[vsx] offset:6144\n\t"
  "ds_read_b128 v[112:115], %[vsx] offset:8192\n\t"
  "ds_read_b128 v[116:119], %[vsx] offset:10240\n\t"
  "ds_read_b128 v[120:123], %[vsx] offset:12288\n\t"
  "ds_read_b128 v[124:127], %[vsx] offset:14336\n\t"
  "ds_read_b128 v[128:131], %[vsx] offset:16384\n\t"
  "ds_read_b128 v[132:135], %[vsx] offset:18432\n\t"
  "v_mov_b32 v184, 0\n\t"
  "v_mov_b32 v185, 0\n\t"
  "v_mov_b32 v186, 0x3f803f80\n\t"
  "s_waitcnt lgkmcnt(9)\n\t"
  "v_dot2c_f32_bf16 v184, v96, v186\n\t"
  "v_dot2c_f32_bf16 v185, v96, v96\n\t"
  "v_dot2c_f32_bf16 v184, v97, v186\n\t"
  "v_dot2c_f32_bf16 v185, v97, v97\n\t"
  "v_dot2c_f32_bf16 v184, v98, v186\n\t"
  "v_dot2c_f32_bf16 v185, v98, v98\n\t"
  "v_dot2c_f32_bf16 v184, v99, v186\n\t"
  "v_dot2c_f32_bf16 v185, v99, v99\n\t"
  "s_waitcnt lgkmcnt(8)\n\t"
  "v_dot2c_f32_bf16 v184, v100, v186\n\t"
  "v_dot2c_f32_bf16 v185, v100, v100\n\t"
  "v_dot2c_f32_bf16 v184, v101, v186\n\t"
  "v_dot2c_f32_bf16 v185, v101, v101\n\t"
  "v_dot2c_f32_bf16 v184, v102, v186\n\t"
  "v_dot2c_f32_bf16 v185, v102, v102\n\t"
  "v_dot2c_f32_bf16 v184, v103, v186\n\t"
  "v_dot2c_f32_bf16 v185, v103, v103\n\t"
  "s_waitcnt lgkmcnt(7)\n\t"
  "v_dot2c_f32_bf16 v184, v104, v186\n\t"
  "v_dot2c_f32_bf16 v185, v104, v104\n\t"
  "v_dot2c_f32_bf16 v184, v105, v186\n\t"
  "v_dot2c_f32_bf16 v185, v105, v105\n\t"
  "v_dot2c_f32_bf16 v184, v106, v186\n\t"
  "v_dot2c_f32_bf16 v185, v106, v106\n\t"
  "v_dot2c_f32_bf16 v184, v107, v186\n\t"
  "v_dot2c_f32_bf16 v185, v107, v107\n\t"
  "s_waitcnt lgkmcnt(6)\n\t"
  "v_dot2c_f32_bf16 v184, v108, v186\n\t"
  "v_dot2c_f32_bf16 v185, v108, v108\n\t"
  "v_dot2c_f32_bf16 v184, v109, v186\n\t"
  "v_dot2c_f32_bf16 v185, v109, v109\n\t"
  "v_dot2c_f32_bf16 v184, v110, v186\n\t"
  "v_dot2c_f32_bf16 v185, v110, v110\n\t"
  "v_dot2c_f32_bf16 v184, v111, v186\n\t"
  "v_dot2c_f32_bf16 v185, v111, v111\n\t"
  "s_waitcnt lgkmcnt(5)\n\t"
  "v_dot2c_f32_bf16 v184, v112, v186\n\t"
  "v_dot2c_f32_bf16 v185, v112, v112\n\t"
  "v_dot2c_f32_bf16 v184, v113, v186\n\t"
  "v_dot2c_f32_bf16 v185, v113, v113\n\t"
  "v_dot2c_f32_bf16 v184, v114, v186\n\t"
  "v_dot2c_f32_bf16 v185, v114, v114\n\t"
  "v_dot2c_f32_bf16 v184, v115, v186\n\t"
  "v_dot2c_f32_bf16 v185, v115, v115\n\t"
  "s_waitcnt lgkmcnt(4)\n\t"
  "v_dot2c_f32_bf16 v184, v116, v186\n\t"
  "v_dot2c_f32_bf16 v185, v116, v116\n\t"
  "v_dot2c_f32_bf16 v184, v117, v186\n\t"
  "v_dot2c_f32_bf16 v185, v117, v117\n\t"
  "v_dot2c_f32_bf16 v184, v118, v186\n\t"
  "v_dot2c_f32_bf16 v185, v118, v118\n\t"
  "v_dot2c_f32_bf16 v184, v119, v186\n\t"
  "v_dot2c_f32_bf16 v185, v119, v119\n\t"
  "s_waitcnt lgkmcnt(3)\n\t"
  "v_dot2c_f32_bf16 v184, v120, v186\n\t"
  "v_dot2c_f32_bf16 v185, v120, v120\n\t"
  "v_dot2c_f32_bf16 v184, v121, v186\n\t"
  "v_dot2c_f32_bf16 v185, v121, v121\n\t"
  "v_dot2c_f32_bf16 v184, v122, v186\n\t"
  "v_dot2c_f32_bf16 v185, v122, v122\n\t"
  "v_dot2c_f32_bf16 v184, v123, v186\n\t"
  "v_dot2c_f32_bf16 v185, v123, v123\n\t"
  "s_waitcnt lgkmcnt(2)\n\t"
  "v_dot2c_f32_bf16 v184, v124, v186\n\t"
  "v_dot2c_f32_bf16 v185, v124, v124\n\t"
  "v_dot2c_f32_bf16 v184, v125, v186\n\t"
  "v_dot2c_f32_bf16 v185, v125, v125\n\t"
  "v_dot2c_f32_bf16 v184, v126, v186\n\t"
  "v_dot2c_f32_bf16 v185, v126, v126\n\t"
  "v_dot2c_f32_bf16 v184, v127, v186\n\t"
  "v_dot2c_f32_bf16 v185, v127, v127\n\t"
  "s_waitcnt lgkmcnt(1)\n\t"
  "v_dot2c_f32_bf16 v184, v128, v186\n\t"
  "v_dot2c_f32_bf16 v185, v128, v128\n\t"
  "v_dot2c_f32_bf16 v184, v129, v186\n\t"
  "v_dot2c_f32_bf16 v185, v129, v129\n\t"
  "v_dot2c_f32_bf16 v184, v130, v186\n\t"
  "v_dot2c_f32_bf16 v185, v130, v130\n\t"
  "v_dot2c_f32_bf16 v184, v131, v186\n\t"
  "v_dot2c_f32_bf16 v185, v131, v131\n\t"
  "s_waitcnt lgkmcnt(0)\n\t"
  "v_dot2c_f32_bf16 v184, v132, v186\n\t"
  "v_dot2c_f32_bf16 v185, v132, v132\n\t"
  "v_dot2c_f32_bf16 v184, v133, v186\n\t"
  "v_dot2c_f32_bf16 v185, v133, v133\n\t"
  "v_dot2c_f32_bf16 v184, v134, v186\n\t"
  "v_dot2c_f32_bf16 v185, v134, v134\n\t"
  "v_dot2c_f32_bf16 v184, v135, v186\n\t"
  "v_dot2c_f32_bf16 v185, v135, v135\n\t"
  "s_nop 1\n\t"
  "v_add_f32_dpp v184, v184, v184 quad_perm:[1,0,3,2] row_mask:0xf bank_mask:0xf\n\t"
  "v_add_f32_dpp v185, v185, v185 quad_perm:[1,0,3,2] row_mask:0xf bank_mask:0xf\n\t"
  "s_nop 0\n\t"
  "v_add_f32_dpp v184, v184, v184 quad_perm:[2,3,0,1] row_mask:0xf bank_mask:0xf\n\t"
  "v_add_f32_dpp v185, v185, v185 quad_perm:[2,3,0,1] row_mask:0xf bank_mask:0xf\n\t"
  "s_nop 0\n\t"
  "v_add_f32_dpp v184, v184, v184 row_half_mirror row_mask:0xf bank_mask:0xf\n\t"
  "v_add_f32_dpp v185, v185, v185 row_half_mirror row_mask:0xf bank_mask:0xf\n\t"
  "v_mul_f32 v184, 0xbacccccd, v184\n\t"
  "v_mul_f32 v185, 0x3acccccd, v185\n\t"
  "v_fma_f32 v185, -v184, v184, v185\n\t"
  "v_max_f32 v185, 0, v185\n\t"
  "v_cvt_pk_bf16_f32 v185, 0, v185\n\t"
  "v_cvt_pk_bf16_f32 v184, 0, v184\n\t"
  "v_add_f32 v185, 0x3727c5ac, v185\n\t"
  "v_cvt_pk_bf16_f32 v185, 0, v185\n\t"
  "v_sqrt_f32 v185, v185\n\t"
  "s_nop 0\n\t"
  "v_cvt_pk_bf16_f32 v185, 0, v185\n\t"
  "v_rcp_f32 v188, v185\n\t"
  "s_nop 0\n\t"
  "v_mul_f32 v189, v184, v188\n\t"
  "s_nop 1\n\t"
  "v_mov_b32_dpp v188, v189 quad_perm:[0,1,2,3] row_mask:0xf bank_mask:0xa\n\t"
  "buffer_store_dword v188, %[vso], %[srd], %[sSo] offen sc1\n\t"
  "Lxe%=:\n\t"
  "s_nop 1\n\t"
        :: [srd] "s"(srd), [w] "s"(w), [sSo] "s"(OFF_ST1 + cb * 128u), [sXo] "s"(OFF_XT + cb * 20480u + (w - 1) * 10240u),
           [vtx] "v"(lb + L_XR + (w - 1) * 10240 + (8 * (g & 1) + q) * 128 + (g >> 1) * 32 + (lane & 3) * 8), [vsx] "v"(lb + L_XR + (w & 1) * 1024 + lane * 16),
           [vso] "v"((lane & 3) == 0 ? (((w & 1) * 4 + g) ^ stkey(cb)) * 16 + ((lane >> 3) & 1) * 4 + (lane & 4) * 2 : OOB), [vxo] "v"((2 * (ch >> 3) + (ch & 1)) * 128 + ((((ch ^ cb) >> 1) & 3) + 4 * (g & 1)) * 16)
        : "memory", "vcc", "scc",
"s44", "v96", "v97", "v98", "v99", "v100", "v101", "v102", "v103", "v104", "v105", "v106", "v107", "v108", "v109", "v110", "v111", "v112", "v113", "v114", "v115", "v116", "v117", "v118", "v119", "v120", "v121", "v122", "v123", "v124", "v125", "v126", "v127", "v128", "v129", "v130", "v131", "v132", "v133", "v134", "v135", "v184", "v185", "v186", "v188", "v189"
      );
    } else if (w == 1) GEMV_UNIT(cb, lb + L_XR, 8);
    stash();
  } else {
    const unsigned lu = lb + L_UN + w * UN_SZ;
    const unsigned g = lane >> 4, q = (lane >> 2) & 3, p4 = lane & 3;
    if (2 > 1) {
      const unsigned u1 = role == 2 ? (1 ? 16 * (w2 >> 2) + 4 * w + (w2 & 3) : 4 * w2 + w) : w == 0 ? 160 + w1 : w == 1 && (w1 & 3) == 3 ? 224 + (w1 >> 2) : 240;
      __builtin_amdgcn_raw_buffer_store_b128(zero4, srd, lane < 8 ? (u1 < 240 ? U_FLAG(u1) + lane * 16 : OOB)
          : lane < 24 && w == 0 ? (role == 1 ? (lane < 16 ? OFF_FW1 : OFF_FU) + w1 * 128 : lane < 16 ? OFF_FW2 + w2 * 128 : w2 < 10 ? OFF_FR + w2 * 128 : OOB) + (lane & 7) * 16 : OOB, 0, SC1);
      FENCE();
    }
    if (8) __builtin_amdgcn_s_sleep(8);
    if (role == 2) {
      const unsigned ui = 1 ? 16 * (w2 >> 2) + 4 * w + (w2 & 3) : 4 * w2 + w;
      GEMV_LOAD(ui, lu);
      const rsrc_t rd = mkrsrc(wd, D_ * F_ * 2);
      const unsigned r8 = 24 * w + (lane >> 3), so = (96 * (w2 >> 1) * D_ + 320 * (w2 & 1)) * 2;
      u32x4 raw[15];
#pragma unroll
      for (int n = 0; n < 15; ++n) raw[n] = __builtin_amdgcn_raw_buffer_load_b128(rd, r8 * (D_ * 2) + (lane & 7) * 16 + (n % 5) * 128, so + (n / 5) * 8 * D_ * 2, NT);
      FENCE();
      tag = load_tag(ws);
      GEMV_UNIT(ui, lu, 15);
#pragma unroll
      for (int n = 0; n < 15; ++n) *(u32x4*)(smem + r8 * RP2 + (lane & 7) * 16 + (n % 5) * 128 + (n / 5) * 8 * RP2) = raw[n];
      FENCE();
      asm volatile("s_waitcnt vmcnt(0)" ::: "memory");
      put_flag(srd, U_FLAG(ui), lane, tag);
      const bool rt = w2 < 10 && w == 0;
      poll_flags<1, 4>(srd, rt && lane >= 4 ? OFF_FA2 + ((lane & 3) * 60 + 40 + w2) * 128 : OFF_FW1 + (j * 4 + (lane & 3)) * 128, tag);
      if (rt) {
        const float sc = rbf(1.f + modval(srd, 4 * 640 + lane + 64 * w2));
        __builtin_amdgcn_raw_buffer_store_b32(__float_as_uint(sc == 0.f ? 0x1p40f : 1.f / sc), srd, lane * 4, OFF_RT + w2 * 256, SC1);
        asm volatile("s_waitcnt vmcnt(0)" ::: "memory");
        put_flag(srd, OFF_FR + w2 * 128, lane, tag);
      }
      W1_LOAD();
      wdone = 0 ? 255 : 127;
      BAR();
#pragma unroll
      for (int n = 0; n < 15; ++n) {
        const int i = n % 5;
        const char* a = smem + (32 * (n / 5) + 8 * g + q) * RP2 + (80 * w + (i < 4 ? 32 * (i >> 1) + 8 * p4 + 4 * (i & 1) : 64 + 4 * p4)) * 2;
        const u32x2 lo = tr_read(a), hi = tr_read(a + 4 * RP2);
        raw[n] = u32x4{lo[0], lo[1], hi[0], hi[1]};
      }
      FENCE();
#pragma unroll
      for (int n = 0; n < 15; ++n)
        __builtin_amdgcn_raw_buffer_store_b128(raw[n], srd, lane * 16 + (n & 3) * 1024, OFF_W2F + ((4 * (w2 & 1) + w) * 240 + 15 * (w2 >> 1) + (n & ~3)) * 1024, SC1);
      FENCE();
      stash();
    } else {
      const rsrc_t rg = mkrsrc(wg, D_ * F_ * 2), ru = mkrsrc(wu, D_ * F_ * 2);
      const unsigned rw = 5 * w + lane / 12, ck = lane % 12, ui = w == 0 ? 160 + w1 : w == 1 && (w1 & 3) == 3 ? 224 + (w1 >> 2) : 240;
      const unsigned vo = lane < 60 ? rw * (F_ * 2) + ck * 16 : OOB, so = (160 * (w1 & 3) * F_ + 96 * (w1 >> 2)) * 2;
      u32x4 raw[16];
#pragma unroll
      for (int n = 0; n < 8; ++n) {
        raw[2 * n] = __builtin_amdgcn_raw_buffer_load_b128(rg, vo, so + n * 20 * F_ * 2, NT);
        raw[2 * n + 1] = __builtin_amdgcn_raw_buffer_load_b128(ru, vo, so + n * 20 * F_ * 2, NT);
      }
      FENCE();
      tag = load_tag(ws);
      char* o = smem + (lane < 60 ? rw * RP1 + ck * 32 : 384);
      u32x4 fr[15];
#pragma unroll
      for (int h = 0; h < 2; ++h) {
#pragma unroll
        for (int n = h ? 5 : 0; n < (h ? 8 : 5); ++n) {
          *(u32x4*)(o + n * 20 * RP1) = u32x4{raw[2 * n][0], raw[2 * n + 1][0], raw[2 * n][1], raw[2 * n + 1][1]};
          *(u32x4*)(o + n * 20 * RP1 + (lane < 60 ? 16 : 0)) = u32x4{raw[2 * n][2], raw[2 * n + 1][2], raw[2 * n][3], raw[2 * n + 1][3]};
        }
        BAR();
#pragma unroll
        for (int n = h ? 9 : 0; n < (h ? 15 : 9); ++n) {
          const int ks = n / 3, i = n % 3;
          const char* a = smem + (32 * ks + 4 * g + q) * RP1 + (12 * w + (p4 < 3 ? 4 * p4 + i : 4 * i + 3)) * 8;
          const u32x2 lo = tr_read(a), hi = tr_read(a + 16 * RP1);
          fr[n] = u32x4{lo[0], lo[1], hi[0], hi[1]};
        }
        FENCE();
#pragma unroll
        for (int n = h ? 9 : 0; n < (h ? 15 : 9); ++n)
          __builtin_amdgcn_raw_buffer_store_b128(fr[n], srd, lane * 16 + (n & 3) * 1024, OFF_W1F + (((w1 & ~3u) + w) * 60 + 15 * (w1 & 3) + (n & ~3)) * 1024, SC1);
        FENCE();
      }
      if (ui < 240) { GEMV_LOAD(ui, lu); asm volatile("s_waitcnt vmcnt(21)" ::: "memory"); } else asm volatile("s_waitcnt vmcnt(0)" ::: "memory");
      asm volatile("s_barrier" ::: "memory");
      if (w == 3) put_flag(srd, OFF_FW1 + w1 * 128, lane, tag);
      if (ui < 240) {
        GEMV_UNIT(ui, lu, 0);
        asm volatile("s_waitcnt vmcnt(0)" ::: "memory");
        put_flag(srd, U_FLAG(ui), lane, tag);
      }
      poll_flags<1, 4>(srd, OFF_FW1 + (j * 4 + (lane & 3)) * 128, tag);
      W1_LOAD();
      wdone = 0 ? 255 : 127;
      stash();
      if (w > 1 || (w == 1 && (w1 & 15) == 0)) {
        const unsigned c0 = (6 - w) * 640 + 160 * (w == 1 ? w1 >> 4 : w1 & 3);
        poll_flags<1, 4>(srd, OFF_FA2 + ((lane & 3) * 60 + ((c0 + min(64 * ((lane >> 2) & 3), 159u)) >> 6)) * 128, tag);
        f32x4 m[4];
#pragma unroll
        for (int k = 0; k < 4; ++k) m[k] = __builtin_bit_cast(f32x4, __builtin_amdgcn_raw_buffer_load_b128(srd, lane < 40 ? lane * 16 : OOB, OFF_MODP + k * 15360 + c0 * 4, SC1));
        f32x4 v = (m[0] + m[1]) + (m[2] + m[3]);
#pragma unroll
        for (int e = 0; e < 4; ++e) v[e] = rbf(v[e]);
        if (w == 1) __builtin_amdgcn_raw_buffer_store_b128(__builtin_bit_cast(u32x4, v), srd, lane < 40 ? lane * 16 : OOB, OFF_GB + 40 * w1, SC1);
        else {
          const float of = w == 2 ? 1.f : 0.f;
          *(u32x2*)(smem + L_WS + (w - 2) * 1024 + lane * 8) = u32x2{pack_bf16(of + v[0], of + v[1]), pack_bf16(of + v[2], of + v[3])};
        }
      }
      BAR();
      f32x4 aU[3];
      u32x2 s0[5], s1[5];
#pragma unroll
      for (int ks = 0; ks < 5; ++ks) {
        const char* t = smem + L_WS + (lane & 1) * 1024 + (32 * ks + 4 * g) * 2;
        s0[ks] = *(const u32x2*)t; s1[ks] = *(const u32x2*)(t + 32);
      }
#pragma unroll
      for (int i = 0; i < 3; ++i) aU[i] = f32x4{0, 0, 0, 0};
      FENCE();
#pragma unroll
      for (int n = 0; n < 15; ++n) mfma_v(aU[n % 3], u32x4{s0[n / 3][0], s0[n / 3][1], s1[n / 3][0], s1[n / 3][1]}, fr[n]);
      asm volatile("s_nop 7\n\ts_nop 1" : "+v"(aU[0]), "+v"(aU[1]), "+v"(aU[2]));
      const float u = g == 0 ? aU[0][0] : g == 1 ? aU[1][0] : aU[2][0], bb = g == 0 ? aU[0][1] : g == 1 ? aU[1][1] : aU[2][1];
      const float uh = rbf(u), bh = rbf(bb);
      __builtin_amdgcn_raw_buffer_store_b64(u32x2{pack_bf16(uh, u - uh), pack_bf16(bh, bb - bh)}, srd,
                                            g < 3 ? OFF_UB + ((((w1 & ~3u) + w) * 3 + g) * 64 + (w1 & 3) * 16 + (lane & 15)) * 8 : OOB, 0, SC1);
    }
  }
  if (role == 0) {
    const unsigned d = 8 * (cb >> 1) + 2 * w + (cb & 1);
    asm volatile(
  "s_nop 4\n\t"
  "buffer_load_dwordx2 v[160:161], %[vf1], %[srd], 0 offen sc1\n\t"
  "s_lshl_b32 s45, %[w], 12\n\t"
  "s_mov_b64 s[80:81], %[ws]\n\t"
  "v_mov_b32 v252, %[tlo]\n\t"
  "v_mov_b32 v253, %[thi]\n\t"
  "v_mov_b32 v254, %[tlo]\n\t"
  "v_mov_b32 v255, %[thi]\n\t"
  "v_mov_b32 v162, 140288\n\t"
  "v_mov_b32 v163, 1\n\t"
  "s_mov_b64 exec, 1\n\t"
  "s_waitcnt vmcnt(1)\n\t"
  "ds_add_rtn_u32 v164, v162, v163\n\t"
  "s_waitcnt lgkmcnt(0)\n\t"
  "s_mov_b64 exec, -1\n\t"
  "v_readfirstlane_b32 s44, v164\n\t"
  "s_nop 3\n\t"
  "s_cmp_lg_u32 s44, 3\n\t"
  "s_cbranch_scc1 Lnox%=\n\t"
  "buffer_store_dwordx4 v[252:255], %[vf3], %[srd], 0 offen sc1\n\t"
  "s_waitcnt vmcnt(1)\n\t"
  "s_branch Lx%=\n\t"
  "Lnox%=:\n\t"
  "s_waitcnt vmcnt(0)\n\t"
  "Lx%=:\n\t"
  "v_cmp_ne_u64 vcc, v[252:253], v[160:161]\n\t"
  "s_nop 1\n\t"
  "s_cbranch_vccnz La1_slow%=\n\t"
  "La1_back%=:\n\t"
  "s_add_u32 m0, s45, 68608\n\t"
  "s_add_u32 s44, %[sKV], 0\n\t"
  "buffer_load_dwordx4 %[vkv], %[srd], s44 offen sc1 lds\n\t"
  "s_add_u32 m0, s45, 69632\n\t"
  "s_add_u32 s44, %[sKV], 163840\n\t"
  "buffer_load_dwordx4 %[vkv], %[srd], s44 offen sc1 lds\n\t"
  "s_add_u32 m0, s45, 70656\n\t"
  "s_add_u32 s44, %[sKV], 327680\n\t"
  "buffer_load_dwordx4 %[vkv], %[srd], s44 offen sc1 lds\n\t"
  "s_add_u32 m0, s45, 71680\n\t"
  "s_add_u32 s44, %[sKV], 491520\n\t"
  "buffer_load_dwordx4 %[vkv], %[srd], s44 offen sc1 lds\n\t"
  "buffer_load_dwordx2 v[250:251], %[vfw], %[srd], 0 offen sc1\n\t"
  "ds_read_b64 v[64:65], %[va] offset:0\n\t"
  "ds_read_b64 v[66:67], %[va] offset:8\n\t"
  "ds_read_b64 v[68:69], %[va] offset:512\n\t"
  "ds_read_b64 v[70:71], %[va] offset:520\n\t"
  "ds_read_b64 v[72:73], %[va] offset:1024\n\t"
  "ds_read_b64 v[74:75], %[va] offset:1032\n\t"
  "ds_read_b64 v[76:77], %[va] offset:1536\n\t"
  "ds_read_b64 v[78:79], %[va] offset:1544\n\t"
  "ds_read_b64 v[80:81], %[va] offset:2048\n\t"
  "ds_read_b64 v[82:83], %[va] offset:2056\n\t"
  "ds_read_b64 v[84:85], %[va] offset:2560\n\t"
  "ds_read_b64 v[86:87], %[va] offset:2568\n\t"
  "ds_read_b64 v[88:89], %[va] offset:3072\n\t"
  "ds_read_b64 v[90:91], %[va] offset:3080\n\t"
  "ds_read_b64 v[92:93], %[va] offset:3584\n\t"
  "ds_read_b64 v[94:95], %[va] offset:3592\n\t"
  "s_waitcnt vmcnt(0) lgkmcnt(0)\n\t"
  "s_barrier\n\t"
  "ds_read_b128 v[124:127], %[vb0] offset:15360\n\t"
  "ds_read_b128 v[120:123], %[vb0] offset:13312\n\t"
  "ds_read_b128 v[116:119], %[vb0] offset:11264\n\t"
  "ds_read_b128 v[112:115], %[vb0] offset:9216\n\t"
  "ds_read_b128 v[108:111], %[vb0] offset:7168\n\t"
  "ds_read_b128 v[104:107], %[vb0] offset:5120\n\t"
  "ds_read_b128 v[100:103], %[vb0] offset:3072\n\t"
  "ds_read_b128 v[96:99], %[vb0] offset:1024\n\t"
  "v_cmp_eq_u64 s[54:55], v[252:253], v[250:251]\n\t"
  "s_nop 3\n\t"
  "s_cmp_eq_u64 s[54:55], -1\n\t"
  "s_cselect_b32 s53, 1, 0\n\t"
  "s_cselect_b32 s56, %[sW1], 1879048192\n\t"
  "s_mov_b32 %[wdone], s53\n\t"
  "s_waitcnt lgkmcnt(7)\n\t"
  "v_mfma_f32_16x16x32_bf16 v[60:63], v[64:67], v[124:127], 0\n\t"
  "ds_read_b128 v[156:159], %[vb1] offset:15360\n\t"
  "buffer_load_dwordx4 a[0:3], %[vl16], %[srd], s56 offen offset:0 sc1\n\t"
  "s_waitcnt lgkmcnt(7)\n\t"
  "v_mfma_f32_16x16x32_bf16 v[56:59], v[64:67], v[120:123], 0\n\t"
  "s_waitcnt lgkmcnt(6)\n\t"
  "v_mfma_f32_16x16x32_bf16 v[52:55], v[64:67], v[116:119], 0\n\t"
  "buffer_load_dwordx2 v[250:251], %[vfw], %[srd], 0 offen sc1\n\t"
  "s_waitcnt lgkmcnt(5)\n\t"
  "v_mfma_f32_16x16x32_bf16 v[48:51], v[64:67], v[112:115], 0\n\t"
  "s_waitcnt lgkmcnt(4)\n\t"
  "v_mfma_f32_16x16x32_bf16 v[44:47], v[64:67], v[108:111], 0\n\t"
  "ds_read_b128 v[152:155], %[vb1] offset:13312\n\t"
  "s_waitcnt lgkmcnt(4)\n\t"
  "v_mfma_f32_16x16x32_bf16 v[40:43], v[64:67], v[104:107], 0\n\t"
  "buffer_load_dwordx4 a[4:7], %[vl16], %[srd], s56 offen offset:1024 sc1\n\t"
  "s_waitcnt lgkmcnt(3)\n\t"
  "v_mfma_f32_16x16x32_bf16 v[36:39], v[64:67], v[100:103], 0\n\t"
  "s_waitcnt lgkmcnt(2)\n\t"
  "v_mfma_f32_16x16x32_bf16 v[32:35], v[64:67], v[96:99], 0\n\t"
  "v_mfma_f32_16x16x32_bf16 v[60:63], %[vd], v[124:127], v[60:63]\n\t"
  "ds_read_b128 v[148:151], %[vb1] offset:11264\n\t"
  "v_mfma_f32_16x16x32_bf16 v[56:59], %[vd], v[120:123], v[56:59]\n\t"
  "v_mfma_f32_16x16x32_bf16 v[52:55], %[vd], v[116:119], v[52:55]\n\t"
  "buffer_load_dwordx4 a[8:11], %[vl16], %[srd], s56 offen offset:2048 sc1\n\t"
  "v_mfma_f32_16x16x32_bf16 v[48:51], %[vd], v[112:115], v[48:51]\n\t"
  "v_mfma_f32_16x16x32_bf16 v[44:47], %[vd], v[108:111], v[44:47]\n\t"
  "ds_read_b128 v[144:147], %[vb1] offset:9216\n\t"
  "v_mfma_f32_16x16x32_bf16 v[40:43], %[vd], v[104:107], v[40:43]\n\t"
  "v_mfma_f32_16x16x32_bf16 v[36:39], %[vd], v[100:103], v[36:39]\n\t"
  "v_mfma_f32_16x16x32_bf16 v[32:35], %[vd], v[96:99], v[32:35]\n\t"
  "ds_read_b64 v[64:65], %[va] offset:64\n\t"
  "ds_read_b64 v[66:67], %[va] offset:72\n\t"
  "v_mfma_f32_16x16x32_bf16 v[60:63], v[68:71], v[120:123], v[60:63]\n\t"
  "ds_read_b128 v[140:143], %[vb1] offset:7168\n\t"
  "buffer_load_dwordx4 a[12:15], %[vl16], %[srd], s56 offen offset:3072 sc1\n\t"
  "v_mfma_f32_16x16x32_bf16 v[56:59], v[68:71], v[116:119], v[56:59]\n\t"
  "v_mfma_f32_16x16x32_bf16 v[52:55], v[68:71], v[112:115], v[52:55]\n\t"
  "v_mfma_f32_16x16x32_bf16 v[48:51], v[68:71], v[108:111], v[48:51]\n\t"
  "v_mfma_f32_16x16x32_bf16 v[44:47], v[68:71], v[104:107], v[44:47]\n\t"
  "ds_read_b128 v[136:139], %[vb1] offset:5120\n\t"
  "v_mfma_f32_16x16x32_bf16 v[40:43], v[68:71], v[100:103], v[40:43]\n\t"
  "s_add_u32 s56, s56, 4096\n\t"
  "v_mfma_f32_16x16x32_bf16 v[36:39], v[68:71], v[96:99], v[36:39]\n\t"
  "ds_read_b64 v[68:69], %[va] offset:576\n\t"
  "ds_read_b64 v[70:71], %[va] offset:584\n\t"
  "v_mfma_f32_16x16x32_bf16 v[60:63], v[72:75], v[116:119], v[60:63]\n\t"
  "v_mfma_f32_16x16x32_bf16 v[56:59], v[72:75], v[112:115], v[56:59]\n\t"
  "ds_read_b128 v[132:135], %[vb1] offset:3072\n\t"
  "v_mfma_f32_16x16x32_bf16 v[52:55], v[72:75], v[108:111], v[52:55]\n\t"
  "v_mfma_f32_16x16x32_bf16 v[48:51], v[72:75], v[104:107], v[48:51]\n\t"
  "buffer_load_dwordx4 a[16:19], %[vl16], %[srd], s56 offen offset:0 sc1\n\t"
  "v_mfma_f32_16x16x32_bf16 v[44:47], v[72:75], v[100:103], v[44:47]\n\t"
  "v_mfma_f32_16x16x32_bf16 v[40:43], v[72:75], v[96:99], v[40:43]\n\t"
  "ds_read_b128 v[128:131], %[vb1] offset:1024\n\t"
  "ds_read_b64 v[72:73], %[va] offset:1088\n\t"
  "v_mfma_f32_16x16x32_bf16 v[60:63], v[76:79], v[112:115], v[60:63]\n\t"
  "ds_read_b64 v[74:75], %[va] offset:1096\n\t"
  "v_mfma_f32_16x16x32_bf16 v[56:59], v[76:79], v[108:111], v[56:59]\n\t"
  "v_mfma_f32_16x16x32_bf16 v[52:55], v[76:79], v[104:107], v[52:55]\n\t"
  "v_mfma_f32_16x16x32_bf16 v[48:51], v[76:79], v[100:103], v[48:51]\n\t"
  "buffer_load_dwordx4 a[20:23], %[vl16], %[srd], s56 offen offset:1024 sc1\n\t"
  "v_mfma_f32_16x16x32_bf16 v[44:47], v[76:79], v[96:99], v[44:47]\n\t"
  "ds_read_b64 v[76:77], %[va] offset:1600\n\t"
  "ds_read_b64 v[78:79], %[va] offset:1608\n\t"
  "v_mfma_f32_16x16x32_bf16 v[60:63], v[80:83], v[108:111], v[60:63]\n\t"
  "v_mfma_f32_16x16x32_bf16 v[56:59], v[80:83], v[104:107], v[56:59]\n\t"
  "v_mfma_f32_16x16x32_bf16 v[52:55], v[80:83], v[100:103], v[52:55]\n\t"
  "v_mfma_f32_16x16x32_bf16 v[48:51], v[80:83], v[96:99], v[48:51]\n\t"
  "ds_read_b64 v[80:81], %[va] offset:2112\n\t"
  "ds_read_b64 v[82:83], %[va] offset:2120\n\t"
  "v_mfma_f32_16x16x32_bf16 v[60:63], v[84:87], v[104:107], v[60:63]\n\t"
  "buffer_load_dwordx4 a[24:27], %[vl16], %[srd], s56 offen offset:2048 sc1\n\t"
  "v_mfma_f32_16x16x32_bf16 v[56:59], v[84:87], v[100:103], v[56:59]\n\t"
  "v_mfma_f32_16x16x32_bf16 v[52:55], v[84:87], v[96:99], v[52:55]\n\t"
  "ds_read_b64 v[84:85], %[va] offset:2624\n\t"
  "ds_read_b64 v[86:87], %[va] offset:2632\n\t"
  "v_mfma_f32_16x16x32_bf16 v[60:63], v[88:91], v[100:103], v[60:63]\n\t"
  "v_mfma_f32_16x16x32_bf16 v[56:59], v[88:91], v[96:99], v[56:59]\n\t"
  "ds_read_b64 v[88:89], %[va] offset:3136\n\t"
  "ds_read_b64 v[90:91], %[va] offset:3144\n\t"
  "v_mfma_f32_16x16x32_bf16 v[60:63], v[92:95], v[96:99], v[60:63]\n\t"
  "s_waitcnt lgkmcnt(10)\n\t"
  "ds_read_b64 v[92:93], %[va] offset:3648\n\t"
  "v_mfma_f32_16x16x32_bf16 v[60:63], v[64:67], v[156:159], v[60:63]\n\t"
  "ds_read_b128 v[124:127], %[vb2] offset:15360\n\t"
  "ds_read_b64 v[94:95], %[va] offset:3656\n\t"
  "v_mfma_f32_16x16x32_bf16 v[56:59], v[64:67], v[152:155], v[56:59]\n\t"
  "s_waitcnt vmcnt(6)\n\t"
  "v_cmp_eq_u64 s[54:55], v[252:253], v[250:251]\n\t"
  "v_mfma_f32_16x16x32_bf16 v[52:55], v[64:67], v[148:151], v[52:55]\n\t"
  "buffer_load_dwordx4 a[28:31], %[vl16], %[srd], s56 offen offset:3072 sc1\n\t"
  "v_mfma_f32_16x16x32_bf16 v[48:51], v[64:67], v[144:147], v[48:51]\n\t"
  "ds_read_b128 v[120:123], %[vb2] offset:13312\n\t"
  "v_mfma_f32_16x16x32_bf16 v[44:47], v[64:67], v[140:143], v[44:47]\n\t"
  "s_cmp_eq_u64 s[54:55], -1\n\t"
  "s_cselect_b32 s53, 2, 0\n\t"
  "s_cselect_b32 s57, %[sW1], 1879048192\n\t"
  "v_mfma_f32_16x16x32_bf16 v[40:43], v[64:67], v[136:139], v[40:43]\n\t"
  "s_or_b32 %[wdone], %[wdone], s53\n\t"
  "buffer_load_dwordx2 v[250:251], %[vfw], %[srd], 0 offen sc1\n\t"
  "v_mfma_f32_16x16x32_bf16 v[36:39], v[64:67], v[132:135], v[36:39]\n\t"
  "ds_read_b128 v[116:119], %[vb2] offset:11264\n\t"
  "s_add_u32 s57, s57, 8192\n\t"
  "v_mfma_f32_16x16x32_bf16 v[32:35], v[64:67], v[128:131], v[32:35]\n\t"
  "ds_read_b64 v[64:65], %[va] offset:128\n\t"
  "ds_read_b64 v[66:67], %[va] offset:136\n\t"
  "v_mfma_f32_16x16x32_bf16 v[60:63], v[68:71], v[152:155], v[60:63]\n\t"
  "buffer_load_dwordx4 a[32:35], %[vl16], %[srd], s57 offen offset:0 sc1\n\t"
  "v_mfma_f32_16x16x32_bf16 v[56:59], v[68:71], v[148:151], v[56:59]\n\t"
  "ds_read_b128 v[112:115], %[vb2] offset:9216\n\t"
  "v_mfma_f32_16x16x32_bf16 v[52:55], v[68:71], v[144:147], v[52:55]\n\t"
  "v_mfma_f32_16x16x32_bf16 v[48:51], v[68:71], v[140:143], v[48:51]\n\t"
  "v_mfma_f32_16x16x32_bf16 v[44:47], v[68:71], v[136:139], v[44:47]\n\t"
  "ds_read_b128 v[108:111], %[vb2] offset:7168\n\t"
  "buffer_load_dwordx4 a[36:39], %[vl16], %[srd], s57 offen offset:1024 sc1\n\t"
  "v_mfma_f32_16x16x32_bf16 v[40:43], v[68:71], v[132:135], v[40:43]\n\t"
  "v_mfma_f32_16x16x32_bf16 v[36:39], v[68:71], v[128:131], v[36:39]\n\t"
  "s_waitcnt lgkmcnt(11)\n\t"
  "ds_read_b64 v[68:69], %[va] offset:640\n\t"
  "v_mfma_f32_16x16x32_bf16 v[60:63], v[72:75], v[148:151], v[60:63]\n\t"
  "ds_read_b128 v[104:107], %[vb2] offset:5120\n\t"
  "ds_read_b64 v[70:71], %[va] offset:648\n\t"
  "v_mfma_f32_16x16x32_bf16 v[56:59], v[72:75], v[144:147], v[56:59]\n\t"
  "v_mfma_f32_16x16x32_bf16 v[52:55], v[72:75], v[140:143], v[52:55]\n\t"
  "buffer_load_dwordx4 a[40:43], %[vl16], %[srd], s57 offen offset:2048 sc1\n\t"
  "v_mfma_f32_16x16x32_bf16 v[48:51], v[72:75], v[136:139], v[48:51]\n\t"
  "ds_read_b128 v[100:103], %[vb2] offset:3072\n\t"
  "v_mfma_f32_16x16x32_bf16 v[44:47], v[72:75], v[132:135], v[44:47]\n\t"
  "v_mfma_f32_16x16x32_bf16 v[40:43], v[72:75], v[128:131], v[40:43]\n\t"
  "ds_read_b64 v[72:73], %[va] offset:1152\n\t"
  "ds_read_b64 v[74:75], %[va] offset:1160\n\t"
  "v_mfma_f32_16x16x32_bf16 v[60:63], v[76:79], v[144:147], v[60:63]\n\t"
  "ds_read_b128 v[96:99], %[vb2] offset:1024\n\t"
  "buffer_load_dwordx4 a[44:47], %[vl16], %[srd], s57 offen offset:3072 sc1\n\t"
  "v_mfma_f32_16x16x32_bf16 v[56:59], v[76:79], v[140:143], v[56:59]\n\t"
  "v_mfma_f32_16x16x32_bf16 v[52:55], v[76:79], v[136:139], v[52:55]\n\t"
  "v_mfma_f32_16x16x32_bf16 v[48:51], v[76:79], v[132:135], v[48:51]\n\t"
  "v_mfma_f32_16x16x32_bf16 v[44:47], v[76:79], v[128:131], v[44:47]\n\t"
  "ds_read_b64 v[76:77], %[va] offset:1664\n\t"
  "ds_read_b64 v[78:79], %[va] offset:1672\n\t"
  "v_mfma_f32_16x16x32_bf16 v[60:63], v[80:83], v[140:143], v[60:63]\n\t"
  "s_add_u32 s57, s57, 4096\n\t"
  "v_mfma_f32_16x16x32_bf16 v[56:59], v[80:83], v[136:139], v[56:59]\n\t"
  "v_mfma_f32_16x16x32_bf16 v[52:55], v[80:83], v[132:135], v[52:55]\n\t"
  "v_mfma_f32_16x16x32_bf16 v[48:51], v[80:83], v[128:131], v[48:51]\n\t"
  "ds_read_b64 v[80:81], %[va] offset:2176\n\t"
  "ds_read_b64 v[82:83], %[va] offset:2184\n\t"
  "v_mfma_f32_16x16x32_bf16 v[60:63], v[84:87], v[136:139], v[60:63]\n\t"
  "buffer_load_dwordx4 a[48:51], %[vl16], %[srd], s57 offen offset:0 sc1\n\t"
  "v_mfma_f32_16x16x32_bf16 v[56:59], v[84:87], v[132:135], v[56:59]\n\t"
  "v_mfma_f32_16x16x32_bf16 v[52:55], v[84:87], v[128:131], v[52:55]\n\t"
  "s_waitcnt lgkmcnt(15)\n\t"
  "ds_read_b64 v[84:85], %[va] offset:2688\n\t"
  "v_mfma_f32_16x16x32_bf16 v[60:63], v[88:91], v[132:135], v[60:63]\n\t"
  "ds_read_b64 v[86:87], %[va] offset:2696\n\t"
  "v_mfma_f32_16x16x32_bf16 v[56:59], v[88:91], v[128:131], v[56:59]\n\t"
  "ds_read_b64 v[88:89], %[va] offset:3200\n\t"
  "ds_read_b64 v[90:91], %[va] offset:3208\n\t"
  "v_mfma_f32_16x16x32_bf16 v[60:63], v[92:95], v[128:131], v[60:63]\n\t"
  "s_waitcnt lgkmcnt(8)\n\t"
  "ds_read_b64 v[92:93], %[va] offset:3712\n\t"
  "v_mfma_f32_16x16x32_bf16 v[60:63], v[64:67], v[124:127], v[60:63]\n\t"
  "ds_read_b128 v[156:159], %[vb3] offset:15360\n\t"
  "ds_read_b64 v[94:95], %[va] offset:3720\n\t"
  "v_mfma_f32_16x16x32_bf16 v[56:59], v[64:67], v[120:123], v[56:59]\n\t"
  "buffer_load_dwordx4 a[52:55], %[vl16], %[srd], s57 offen offset:1024 sc1\n\t"
  "v_mfma_f32_16x16x32_bf16 v[52:55], v[64:67], v[116:119], v[52:55]\n\t"
  "v_mfma_f32_16x16x32_bf16 v[48:51], v[64:67], v[112:115], v[48:51]\n\t"
  "ds_read_b128 v[152:155], %[vb3] offset:13312\n\t"
  "buffer_load_dwordx4 a[56:59], %[vl16], %[srd], s57 offen offset:2048 sc1\n\t"
  "v_mfma_f32_16x16x32_bf16 v[44:47], v[64:67], v[108:111], v[44:47]\n\t"
  "v_mfma_f32_16x16x32_bf16 v[40:43], v[64:67], v[104:107], v[40:43]\n\t"
  "v_mfma_f32_16x16x32_bf16 v[36:39], v[64:67], v[100:103], v[36:39]\n\t"
  "ds_read_b128 v[148:151], %[vb3] offset:11264\n\t"
  "v_mfma_f32_16x16x32_bf16 v[32:35], v[64:67], v[96:99], v[32:35]\n\t"
  "ds_read_b64 v[64:65], %[va] offset:192\n\t"
  "ds_read_b64 v[66:67], %[va] offset:200\n\t"
  "v_mfma_f32_16x16x32_bf16 v[60:63], v[68:71], v[120:123], v[60:63]\n\t"
  "buffer_load_dwordx4 a[60:63], %[vl16], %[srd], s57 offen offset:3072 sc1\n\t"
  "v_mfma_f32_16x16x32_bf16 v[56:59], v[68:71], v[116:119], v[56:59]\n\t"
  "ds_read_b128 v[144:147], %[vb3] offset:9216\n\t"
  "s_waitcnt vmcnt(8)\n\t"
  "v_cmp_eq_u64 s[54:55], v[252:253], v[250:251]\n\t"
  "v_mfma_f32_16x16x32_bf16 v[52:55], v[68:71], v[112:115], v[52:55]\n\t"
  "v_mfma_f32_16x16x32_bf16 v[48:51], v[68:71], v[108:111], v[48:51]\n\t"
  "v_mfma_f32_16x16x32_bf16 v[44:47], v[68:71], v[104:107], v[44:47]\n\t"
  "ds_read_b128 v[140:143], %[vb3] offset:7168\n\t"
  "s_cmp_eq_u64 s[54:55], -1\n\t"
  "s_cselect_b32 s53, 4, 0\n\t"
  "s_cselect_b32 s58, %[sW1], 1879048192\n\t"
  "v_mfma_f32_16x16x32_bf16 v[40:43], v[68:71], v[100:103], v[40:43]\n\t"
  "s_or_b32 %[wdone], %[wdone], s53\n\t"
  "s_add_u32 s58, s58, 16384\n\t"
  "v_mfma_f32_16x16x32_bf16 v[36:39], v[68:71], v[96:99], v[36:39]\n\t"
  "s_waitcnt lgkmcnt(11)\n\t"
  "ds_read_b64 v[68:69], %[va] offset:704\n\t"
  "v_mfma_f32_16x16x32_bf16 v[60:63], v[72:75], v[116:119], v[60:63]\n\t"
  "ds_read_b128 v[136:139], %[vb3] offset:5120\n\t"
  "ds_read_b64 v[70:71], %[va] offset:712\n\t"
  "v_mfma_f32_16x16x32_bf16 v[56:59], v[72:75], v[112:115], v[56:59]\n\t"
  "buffer_load_dwordx4 a[64:67], %[vl16], %[srd], s58 offen offset:0 sc1\n\t"
  "v_mfma_f32_16x16x32_bf16 v[52:55], v[72:75], v[108:111], v[52:55]\n\t"
  "v_mfma_f32_16x16x32_bf16 v[48:51], v[72:75], v[104:107], v[48:51]\n\t"
  "ds_read_b128 v[132:135], %[vb3] offset:3072\n\t"
  "buffer_load_dwordx4 a[68:71], %[vl16], %[srd], s58 offen offset:1024 sc1\n\t"
  "v_mfma_f32_16x16x32_bf16 v[44:47], v[72:75], v[100:103], v[44:47]\n\t"
  "v_mfma_f32_16x16x32_bf16 v[40:43], v[72:75], v[96:99], v[40:43]\n\t"
  "ds_read_b64 v[72:73], %[va] offset:1216\n\t"
  "ds_read_b64 v[74:75], %[va] offset:1224\n\t"
  "v_mfma_f32_16x16x32_bf16 v[60:63], v[76:79], v[112:115], v[60:63]\n\t"
  "ds_read_b128 v[128:131], %[vb3] offset:1024\n\t"
  "v_mfma_f32_16x16x32_bf16 v[56:59], v[76:79], v[108:111], v[56:59]\n\t"
  "buffer_load_dwordx4 a[72:75], %[vl16], %[srd], s58 offen offset:2048 sc1\n\t"
  "v_mfma_f32_16x16x32_bf16 v[52:55], v[76:79], v[104:107], v[52:55]\n\t"
  "v_mfma_f32_16x16x32_bf16 v[48:51], v[76:79], v[100:103], v[48:51]\n\t"
  "v_mfma_f32_16x16x32_bf16 v[44:47], v[76:79], v[96:99], v[44:47]\n\t"
  "ds_read_b64 v[76:77], %[va] offset:1728\n\t"
  "ds_read_b64 v[78:79], %[va] offset:1736\n\t"
  "v_mfma_f32_16x16x32_bf16 v[60:63], v[80:83], v[108:111], v[60:63]\n\t"
  "buffer_load_dwordx4 a[76:79], %[vl16], %[srd], s58 offen offset:3072 sc1\n\t"
  "v_mfma_f32_16x16x32_bf16 v[56:59], v[80:83], v[104:107], v[56:59]\n\t"
  "v_mfma_f32_16x16x32_bf16 v[52:55], v[80:83], v[100:103], v[52:55]\n\t"
  "s_add_u32 s58, s58, 4096\n\t"
  "v_mfma_f32_16x16x32_bf16 v[48:51], v[80:83], v[96:99], v[48:51]\n\t"
  "ds_read_b64 v[80:81], %[va] offset:2240\n\t"
  "ds_read_b64 v[82:83], %[va] offset:2248\n\t"
  "v_mfma_f32_16x16x32_bf16 v[60:63], v[84:87], v[104:107], v[60:63]\n\t"
  "v_mfma_f32_16x16x32_bf16 v[56:59], v[84:87], v[100:103], v[56:59]\n\t"
  "v_mfma_f32_16x16x32_bf16 v[52:55], v[84:87], v[96:99], v[52:55]\n\t"
  "s_waitcnt lgkmcnt(15)\n\t"
  "ds_read_b64 v[84:85], %[va] offset:2752\n\t"
  "v_mfma_f32_16x16x32_bf16 v[60:63], v[88:91], v[100:103], v[60:63]\n\t"
  "ds_read_b64 v[86:87], %[va] offset:2760\n\t"
  "buffer_load_dwordx4 a[80:83], %[vl16], %[srd], s58 offen offset:0 sc1\n\t"
  "v_mfma_f32_16x16x32_bf16 v[56:59], v[88:91], v[96:99], v[56:59]\n\t"
  "ds_read_b64 v[88:89], %[va] offset:3264\n\t"
  "ds_read_b64 v[90:91], %[va] offset:3272\n\t"
  "v_mfma_f32_16x16x32_bf16 v[60:63], v[92:95], v[96:99], v[60:63]\n\t"
  "s_waitcnt lgkmcnt(8)\n\t"
  "ds_read_b64 v[92:93], %[va] offset:3776\n\t"
  "v_mfma_f32_16x16x32_bf16 v[60:63], v[64:67], v[156:159], v[60:63]\n\t"
  "ds_read_b128 v[124:127], %[vb0] offset:14336\n\t"
  "ds_read_b64 v[94:95], %[va] offset:3784\n\t"
  "v_mfma_f32_16x16x32_bf16 v[56:59], v[64:67], v[152:155], v[56:59]\n\t"
  "buffer_load_dwordx4 a[84:87], %[vl16], %[srd], s58 offen offset:1024 sc1\n\t"
  "v_mfma_f32_16x16x32_bf16 v[52:55], v[64:67], v[148:151], v[52:55]\n\t"
  "v_mfma_f32_16x16x32_bf16 v[48:51], v[64:67], v[144:147], v[48:51]\n\t"
  "ds_read_b128 v[120:123], %[vb0] offset:12288\n\t"
  "buffer_load_dwordx4 a[88:91], %[vl16], %[srd], s58 offen offset:2048 sc1\n\t"
  "v_mfma_f32_16x16x32_bf16 v[44:47], v[64:67], v[140:143], v[44:47]\n\t"
  "v_mfma_f32_16x16x32_bf16 v[40:43], v[64:67], v[136:139], v[40:43]\n\t"
  "v_mfma_f32_16x16x32_bf16 v[36:39], v[64:67], v[132:135], v[36:39]\n\t"
  "ds_read_b128 v[116:119], %[vb0] offset:10240\n\t"
  "buffer_load_dwordx4 a[92:95], %[vl16], %[srd], s58 offen offset:3072 sc1\n\t"
  "v_mfma_f32_16x16x32_bf16 v[32:35], v[64:67], v[128:131], v[32:35]\n\t"
  "ds_read_b64 v[64:65], %[va] offset:256\n\t"
  "ds_read_b64 v[66:67], %[va] offset:264\n\t"
  "v_mfma_f32_16x16x32_bf16 v[60:63], v[68:71], v[152:155], v[60:63]\n\t"
  "v_mfma_f32_16x16x32_bf16 v[56:59], v[68:71], v[148:151], v[56:59]\n\t"
  "ds_read_b128 v[112:115], %[vb0] offset:8192\n\t"
  "s_add_u32 s58, s58, 4096\n\t"
  "v_mfma_f32_16x16x32_bf16 v[52:55], v[68:71], v[144:147], v[52:55]\n\t"
  "v_mfma_f32_16x16x32_bf16 v[48:51], v[68:71], v[140:143], v[48:51]\n\t"
  "v_mfma_f32_16x16x32_bf16 v[44:47], v[68:71], v[136:139], v[44:47]\n\t"
  "ds_read_b128 v[108:111], %[vb0] offset:6144\n\t"
  "v_mfma_f32_16x16x32_bf16 v[40:43], v[68:71], v[132:135], v[40:43]\n\t"
  "buffer_load_dwordx4 a[96:99], %[vl16], %[srd], s58 offen offset:0 sc1\n\t"
  "v_mfma_f32_16x16x32_bf16 v[36:39], v[68:71], v[128:131], v[36:39]\n\t"
  "s_waitcnt lgkmcnt(11)\n\t"
  "ds_read_b64 v[68:69], %[va] offset:768\n\t"
  "v_mfma_f32_16x16x32_bf16 v[60:63], v[72:75], v[148:151], v[60:63]\n\t"
  "ds_read_b128 v[104:107], %[vb0] offset:4096\n\t"
  "ds_read_b64 v[70:71], %[va] offset:776\n\t"
  "v_mfma_f32_16x16x32_bf16 v[56:59], v[72:75], v[144:147], v[56:59]\n\t"
  "buffer_load_dwordx4 a[100:103], %[vl16], %[srd], s58 offen offset:1024 sc1\n\t"
  "v_mfma_f32_16x16x32_bf16 v[52:55], v[72:75], v[140:143], v[52:55]\n\t"
  "v_mfma_f32_16x16x32_bf16 v[48:51], v[72:75], v[136:139], v[48:51]\n\t"
  "ds_read_b128 v[100:103], %[vb0] offset:2048\n\t"
  "v_mfma_f32_16x16x32_bf16 v[44:47], v[72:75], v[132:135], v[44:47]\n\t"
  "v_mfma_f32_16x16x32_bf16 v[40:43], v[72:75], v[128:131], v[40:43]\n\t"
  "ds_read_b64 v[72:73], %[va] offset:1280\n\t"
  "ds_read_b64 v[74:75], %[va] offset:1288\n\t"
  "v_mfma_f32_16x16x32_bf16 v[60:63], v[76:79], v[144:147], v[60:63]\n\t"
  "ds_read_b128 v[96:99], %[vb0] offset:0\n\t"
  "v_mfma_f32_16x16x32_bf16 v[56:59], v[76:79], v[140:143], v[56:59]\n\t"
  "v_mfma_f32_16x16x32_bf16 v[52:55], v[76:79], v[136:139], v[52:55]\n\t"
  "v_mfma_f32_16x16x32_bf16 v[48:51], v[76:79], v[132:135], v[48:51]\n\t"
  "buffer_load_dwordx2 v[162:163], %[vf2], %[srd], 0 offen sc1\n\t"
  "v_mfma_f32_16x16x32_bf16 v[44:47], v[76:79], v[128:131], v[44:47]\n\t"
  "ds_read_b64 v[76:77], %[va] offset:1792\n\t"
  "ds_read_b64 v[78:79], %[va] offset:1800\n\t"
  "v_mfma_f32_16x16x32_bf16 v[60:63], v[80:83], v[140:143], v[60:63]\n\t"
  "v_mfma_f32_16x16x32_bf16 v[56:59], v[80:83], v[136:139], v[56:59]\n\t"
  "v_mfma_f32_16x16x32_bf16 v[52:55], v[80:83], v[132:135], v[52:55]\n\t"
  "v_mfma_f32_16x16x32_bf16 v[48:51], v[80:83], v[128:131], v[48:51]\n\t"
  "ds_read_b64 v[80:81], %[va] offset:2304\n\t"
  "ds_read_b64 v[82:83], %[va] offset:2312\n\t"
  "v_mfma_f32_16x16x32_bf16 v[60:63], v[84:87], v[136:139], v[60:63]\n\t"
  "v_mfma_f32_16x16x32_bf16 v[56:59], v[84:87], v[132:135], v[56:59]\n\t"
  "v_mfma_f32_16x16x32_bf16 v[52:55], v[84:87], v[128:131], v[52:55]\n\t"
  "s_waitcnt lgkmcnt(15)\n\t"
  "ds_read_b64 v[84:85], %[va] offset:2816\n\t"
  "v_mfma_f32_16x16x32_bf16 v[60:63], v[88:91], v[132:135], v[60:63]\n\t"
  "ds_read_b64 v[86:87], %[va] offset:2824\n\t"
  "v_mfma_f32_16x16x32_bf16 v[56:59], v[88:91], v[128:131], v[56:59]\n\t"
  "ds_read_b64 v[88:89], %[va] offset:3328\n\t"
  "ds_read_b64 v[90:91], %[va] offset:3336\n\t"
  "v_mfma_f32_16x16x32_bf16 v[60:63], v[92:95], v[128:131], v[60:63]\n\t"
  "s_waitcnt lgkmcnt(8)\n\t"
  "ds_read_b64 v[92:93], %[va] offset:3840\n\t"
  "v_mfma_f32_16x16x32_bf16 v[60:63], v[64:67], v[124:127], v[60:63]\n\t"
  "ds_read_b128 v[156:159], %[vb1] offset:14336\n\t"
  "ds_read_b64 v[94:95], %[va] offset:3848\n\t"
  "v_mfma_f32_16x16x32_bf16 v[56:59], v[64:67], v[120:123], v[56:59]\n\t"
  "v_mfma_f32_16x16x32_bf16 v[52:55], v[64:67], v[116:119], v[52:55]\n\t"
  "v_mfma_f32_16x16x32_bf16 v[48:51], v[64:67], v[112:115], v[48:51]\n\t"
  "ds_read_b128 v[152:155], %[vb1] offset:12288\n\t"
  "v_mfma_f32_16x16x32_bf16 v[44:47], v[64:67], v[108:111], v[44:47]\n\t"
  "v_mfma_f32_16x16x32_bf16 v[40:43], v[64:67], v[104:107], v[40:43]\n\t"
  "v_mfma_f32_16x16x32_bf16 v[36:39], v[64:67], v[100:103], v[36:39]\n\t"
  "ds_read_b128 v[148:151], %[vb1] offset:10240\n\t"
  "v_mfma_f32_16x16x32_bf16 v[32:35], v[64:67], v[96:99], v[32:35]\n\t"
  "ds_read_b64 v[64:65], %[va] offset:320\n\t"
  "ds_read_b64 v[66:67], %[va] offset:328\n\t"
  "v_mfma_f32_16x16x32_bf16 v[60:63], v[68:71], v[120:123], v[60:63]\n\t"
  "v_mfma_f32_16x16x32_bf16 v[56:59], v[68:71], v[116:119], v[56:59]\n\t"
  "ds_read_b128 v[144:147], %[vb1] offset:8192\n\t"
  "v_mfma_f32_16x16x32_bf16 v[52:55], v[68:71], v[112:115], v[52:55]\n\t"
  "v_mfma_f32_16x16x32_bf16 v[48:51], v[68:71], v[108:111], v[48:51]\n\t"
  "v_mfma_f32_16x16x32_bf16 v[44:47], v[68:71], v[104:107], v[44:47]\n\t"
  "ds_read_b128 v[140:143], %[vb1] offset:6144\n\t"
  "buffer_load_dwordx2 v[250:251], %[vfw], %[srd], 0 offen sc1\n\t"
  "v_mfma_f32_16x16x32_bf16 v[40:43], v[68:71], v[100:103], v[40:43]\n\t"
  "v_mfma_f32_16x16x32_bf16 v[36:39], v[68:71], v[96:99], v[36:39]\n\t"
  "s_waitcnt lgkmcnt(11)\n\t"
  "ds_read_b64 v[68:69], %[va] offset:832\n\t"
  "v_mfma_f32_16x16x32_bf16 v[60:63], v[72:75], v[116:119], v[60:63]\n\t"
  "ds_read_b128 v[136:139], %[vb1] offset:4096\n\t"
  "ds_read_b64 v[70:71], %[va] offset:840\n\t"
  "v_mfma_f32_16x16x32_bf16 v[56:59], v[72:75], v[112:115], v[56:59]\n\t"
  "v_mfma_f32_16x16x32_bf16 v[52:55], v[72:75], v[108:111], v[52:55]\n\t"
  "v_mfma_f32_16x16x32_bf16 v[48:51], v[72:75], v[104:107], v[48:51]\n\t"
  "ds_read_b128 v[132:135], %[vb1] offset:2048\n\t"
  "v_mfma_f32_16x16x32_bf16 v[44:47], v[72:75], v[100:103], v[44:47]\n\t"
  "v_mfma_f32_16x16x32_bf16 v[40:43], v[72:75], v[96:99], v[40:43]\n\t"
  "ds_read_b64 v[72:73], %[va] offset:1344\n\t"
  "ds_read_b64 v[74:75], %[va] offset:1352\n\t"
  "v_mfma_f32_16x16x32_bf16 v[60:63], v[76:79], v[112:115], v[60:63]\n\t"
  "ds_read_b128 v[128:131], %[vb1] offset:0\n\t"
  "v_mfma_f32_16x16x32_bf16 v[56:59], v[76:79], v[108:111], v[56:59]\n\t"
  "v_mfma_f32_16x16x32_bf16 v[52:55], v[76:79], v[104:107], v[52:55]\n\t"
  "v_mfma_f32_16x16x32_bf16 v[48:51], v[76:79], v[100:103], v[48:51]\n\t"
  "v_mfma_f32_16x16x32_bf16 v[44:47], v[76:79], v[96:99], v[44:47]\n\t"
  "ds_read_b64 v[76:77], %[va] offset:1856\n\t"
  "ds_read_b64 v[78:79], %[va] offset:1864\n\t"
  "v_mfma_f32_16x16x32_bf16 v[60:63], v[80:83], v[108:111], v[60:63]\n\t"
  "v_mfma_f32_16x16x32_bf16 v[56:59], v[80:83], v[104:107], v[56:59]\n\t"
  "v_mfma_f32_16x16x32_bf16 v[52:55], v[80:83], v[100:103], v[52:55]\n\t"
  "s_waitcnt vmcnt(1)\n\t"
  "v_cmp_ne_u64 vcc, v[252:253], v[162:163]\n\t"
  "v_mfma_f32_16x16x32_bf16 v[48:51], v[80:83], v[96:99], v[48:51]\n\t"
  "ds_read_b64 v[80:81], %[va] offset:2368\n\t"
  "ds_read_b64 v[82:83], %[va] offset:2376\n\t"
  "v_mfma_f32_16x16x32_bf16 v[60:63], v[84:87], v[104:107], v[60:63]\n\t"
  "s_cbranch_vccnz La2_slow%=\n\t"
  "La2_back%=:\n\t"
  "v_mfma_f32_16x16x32_bf16 v[56:59], v[84:87], v[100:103], v[56:59]\n\t"
  "buffer_load_dword v244, %[vmod], %[srd], 0 offen sc1\n\t"
  "s_add_u32 m0, s45, 84992\n\t"
  "v_mfma_f32_16x16x32_bf16 v[52:55], v[84:87], v[96:99], v[52:55]\n\t"
  "s_waitcnt lgkmcnt(15)\n\t"
  "ds_read_b64 v[84:85], %[va] offset:2880\n\t"
  "v_mfma_f32_16x16x32_bf16 v[60:63], v[88:91], v[100:103], v[60:63]\n\t"
  "ds_read_b64 v[86:87], %[va] offset:2888\n\t"
  "s_add_u32 s44, %[sKV], 2621440\n\t"
  "v_mfma_f32_16x16x32_bf16 v[56:59], v[88:91], v[96:99], v[56:59]\n\t"
  "ds_read_b64 v[88:89], %[va] offset:3392\n\t"
  "ds_read_b64 v[90:91], %[va] offset:3400\n\t"
  "v_mfma_f32_16x16x32_bf16 v[60:63], v[92:95], v[96:99], v[60:63]\n\t"
  "s_waitcnt lgkmcnt(8)\n\t"
  "ds_read_b64 v[92:93], %[va] offset:3904\n\t"
  "v_mfma_f32_16x16x32_bf16 v[60:63], v[64:67], v[156:159], v[60:63]\n\t"
  "ds_read_b128 v[124:127], %[vb2] offset:14336\n\t"
  "ds_read_b64 v[94:95], %[va] offset:3912\n\t"
  "v_mfma_f32_16x16x32_bf16 v[56:59], v[64:67], v[152:155], v[56:59]\n\t"
  "buffer_load_dwordx4 %[vkv], %[srd], s44 offen sc1 lds\n\t"
  "s_add_u32 m0, s45, 86016\n\t"
  "v_mfma_f32_16x16x32_bf16 v[52:55], v[64:67], v[148:151], v[52:55]\n\t"
  "s_add_u32 s44, %[sKV], 2785280\n\t"
  "buffer_load_dwordx4 %[vkv], %[srd], s44 offen sc1 lds\n\t"
  "v_mfma_f32_16x16x32_bf16 v[48:51], v[64:67], v[144:147], v[48:51]\n\t"
  "ds_read_b128 v[120:123], %[vb2] offset:12288\n\t"
  "s_add_u32 m0, s45, 87040\n\t"
  "v_mfma_f32_16x16x32_bf16 v[44:47], v[64:67], v[140:143], v[44:47]\n\t"
  "s_add_u32 s44, %[sKV], 2949120\n\t"
  "buffer_load_dwordx4 %[vkv], %[srd], s44 offen sc1 lds\n\t"
  "v_mfma_f32_16x16x32_bf16 v[40:43], v[64:67], v[136:139], v[40:43]\n\t"
  "s_add_u32 m0, s45, 88064\n\t"
  "s_add_u32 s44, %[sKV], 3112960\n\t"
  "v_mfma_f32_16x16x32_bf16 v[36:39], v[64:67], v[132:135], v[36:39]\n\t"
  "ds_read_b128 v[116:119], %[vb2] offset:10240\n\t"
  "buffer_load_dwordx4 %[vkv], %[srd], s44 offen sc1 lds\n\t"
  "v_mfma_f32_16x16x32_bf16 v[32:35], v[64:67], v[128:131], v[32:35]\n\t"
  "ds_read_b64 v[64:65], %[va] offset:384\n\t"
  "ds_read_b64 v[66:67], %[va] offset:392\n\t"
  "v_mfma_f32_16x16x32_bf16 v[60:63], v[68:71], v[152:155], v[60:63]\n\t"
  "s_add_u32 m0, s45, 101376\n\t"
  "s_add_u32 s44, s45, 1159168\n\t"
  "v_mfma_f32_16x16x32_bf16 v[56:59], v[68:71], v[148:151], v[56:59]\n\t"
  "ds_read_b128 v[112:115], %[vb2] offset:8192\n\t"
  "buffer_load_dwordx4 %[vl16], %[srd], s44 offen offset:0 sc1 lds\n\t"
  "v_mfma_f32_16x16x32_bf16 v[52:55], v[68:71], v[144:147], v[52:55]\n\t"
  "buffer_load_dwordx4 %[vl16], %[srd], s44 offen offset:1024 sc1 lds\n\t"
  "buffer_load_dwordx4 %[vl16], %[srd], s44 offen offset:2048 sc1 lds\n\t"
  "v_mfma_f32_16x16x32_bf16 v[48:51], v[68:71], v[140:143], v[48:51]\n\t"
  "buffer_load_dwordx4 %[vl16], %[srd], s44 offen offset:3072 sc1 lds\n\t"
  "v_mfma_f32_16x16x32_bf16 v[44:47], v[68:71], v[136:139], v[44:47]\n\t"
  "ds_read_b128 v[108:111], %[vb2] offset:6144\n\t"
  "v_mfma_f32_16x16x32_bf16 v[40:43], v[68:71], v[132:135], v[40:43]\n\t"
  "v_mfma_f32_16x16x32_bf16 v[36:39], v[68:71], v[128:131], v[36:39]\n\t"
  "s_waitcnt lgkmcnt(11)\n\t"
  "ds_read_b64 v[68:69], %[va] offset:896\n\t"
  "v_mfma_f32_16x16x32_bf16 v[60:63], v[72:75], v[148:151], v[60:63]\n\t"
  "ds_read_b128 v[104:107], %[vb2] offset:4096\n\t"
  "ds_read_b64 v[70:71], %[va] offset:904\n\t"
  "v_mfma_f32_16x16x32_bf16 v[56:59], v[72:75], v[144:147], v[56:59]\n\t"
  "s_waitcnt vmcnt(9)\n\t"
  "v_cmp_eq_u64 s[54:55], v[252:253], v[250:251]\n\t"
  "v_mfma_f32_16x16x32_bf16 v[52:55], v[72:75], v[140:143], v[52:55]\n\t"
  "v_mfma_f32_16x16x32_bf16 v[48:51], v[72:75], v[136:139], v[48:51]\n\t"
  "ds_read_b128 v[100:103], %[vb2] offset:2048\n\t"
  "v_mfma_f32_16x16x32_bf16 v[44:47], v[72:75], v[132:135], v[44:47]\n\t"
  "s_cmp_eq_u64 s[54:55], -1\n\t"
  "s_cselect_b32 s53, 8, 0\n\t"
  "s_cselect_b32 s59, %[sW1], 1879048192\n\t"
  "v_mfma_f32_16x16x32_bf16 v[40:43], v[72:75], v[128:131], v[40:43]\n\t"
  "ds_read_b64 v[72:73], %[va] offset:1408\n\t"
  "ds_read_b64 v[74:75], %[va] offset:1416\n\t"
  "v_mfma_f32_16x16x32_bf16 v[60:63], v[76:79], v[144:147], v[60:63]\n\t"
  "ds_read_b128 v[96:99], %[vb2] offset:0\n\t"
  "s_or_b32 %[wdone], %[wdone], s53\n\t"
  "v_mfma_f32_16x16x32_bf16 v[56:59], v[76:79], v[140:143], v[56:59]\n\t"
  "s_add_u32 s59, s59, 26624\n\t"
  "v_mfma_f32_16x16x32_bf16 v[52:55], v[76:79], v[136:139], v[52:55]\n\t"
  "v_mfma_f32_16x16x32_bf16 v[48:51], v[76:79], v[132:135], v[48:51]\n\t"
  "v_mfma_f32_16x16x32_bf16 v[44:47], v[76:79], v[128:131], v[44:47]\n\t"
  "ds_read_b64 v[76:77], %[va] offset:1920\n\t"
  "ds_read_b64 v[78:79], %[va] offset:1928\n\t"
  "v_mfma_f32_16x16x32_bf16 v[60:63], v[80:83], v[140:143], v[60:63]\n\t"
  "buffer_load_dwordx4 a[104:107], %[vl16], %[srd], s59 offen offset:0 sc1\n\t"
  "v_mfma_f32_16x16x32_bf16 v[56:59], v[80:83], v[136:139], v[56:59]\n\t"
  "v_mfma_f32_16x16x32_bf16 v[52:55], v[80:83], v[132:135], v[52:55]\n\t"
  "v_mfma_f32_16x16x32_bf16 v[48:51], v[80:83], v[128:131], v[48:51]\n\t"
  "ds_read_b64 v[80:81], %[va] offset:2432\n\t"
  "ds_read_b64 v[82:83], %[va] offset:2440\n\t"
  "v_mfma_f32_16x16x32_bf16 v[60:63], v[84:87], v[136:139], v[60:63]\n\t"
  "buffer_load_dwordx4 a[108:111], %[vl16], %[srd], s59 offen offset:1024 sc1\n\t"
  "v_mfma_f32_16x16x32_bf16 v[56:59], v[84:87], v[132:135], v[56:59]\n\t"
  "v_mfma_f32_16x16x32_bf16 v[52:55], v[84:87], v[128:131], v[52:55]\n\t"
  "s_waitcnt lgkmcnt(15)\n\t"
  "ds_read_b64 v[84:85], %[va] offset:2944\n\t"
  "v_mfma_f32_16x16x32_bf16 v[60:63], v[88:91], v[132:135], v[60:63]\n\t"
  "ds_read_b64 v[86:87], %[va] offset:2952\n\t"
  "v_mfma_f32_16x16x32_bf16 v[56:59], v[88:91], v[128:131], v[56:59]\n\t"
  "ds_read_b64 v[88:89], %[va] offset:3456\n\t"
  "ds_read_b64 v[90:91], %[va] offset:3464\n\t"
  "v_mfma_f32_16x16x32_bf16 v[60:63], v[92:95], v[128:131], v[60:63]\n\t"
  "s_waitcnt lgkmcnt(8)\n\t"
  "ds_read_b64 v[92:93], %[va] offset:3968\n\t"
  "v_mfma_f32_16x16x32_bf16 v[60:63], v[64:67], v[124:127], v[60:63]\n\t"
  "ds_read_b128 v[156:159], %[vb3] offset:14336\n\t"
  "ds_read_b64 v[94:95], %[va] offset:3976\n\t"
  "v_mfma_f32_16x16x32_bf16 v[56:59], v[64:67], v[120:123], v[56:59]\n\t"
  "buffer_load_dwordx4 a[112:115], %[vl16], %[srd], s59 offen offset:2048 sc1\n\t"
  "v_mfma_f32_16x16x32_bf16 v[52:55], v[64:67], v[116:119], v[52:55]\n\t"
  "buffer_load_dwordx4 a[116:119], %[vl16], %[srd], s59 offen offset:3072 sc1\n\t"
  "v_mfma_f32_16x16x32_bf16 v[48:51], v[64:67], v[112:115], v[48:51]\n\t"
  "ds_read_b128 v[152:155], %[vb3] offset:12288\n\t"
  "v_mfma_f32_16x16x32_bf16 v[44:47], v[64:67], v[108:111], v[44:47]\n\t"
  "v_mfma_f32_16x16x32_bf16 v[40:43], v[64:67], v[104:107], v[40:43]\n\t"
  "v_mfma_f32_16x16x32_bf16 v[36:39], v[64:67], v[100:103], v[36:39]\n\t"
  "ds_read_b128 v[148:151], %[vb3] offset:10240\n\t"
  "s_add_u32 s59, s59, 4096\n\t"
  "v_mfma_f32_16x16x32_bf16 v[32:35], v[64:67], v[96:99], v[32:35]\n\t"
  "ds_read_b64 v[64:65], %[va] offset:448\n\t"
  "ds_read_b64 v[66:67], %[va] offset:456\n\t"
  "v_mfma_f32_16x16x32_bf16 v[60:63], v[68:71], v[120:123], v[60:63]\n\t"
  "v_mfma_f32_16x16x32_bf16 v[56:59], v[68:71], v[116:119], v[56:59]\n\t"
  "ds_read_b128 v[144:147], %[vb3] offset:8192\n\t"
  "v_mfma_f32_16x16x32_bf16 v[52:55], v[68:71], v[112:115], v[52:55]\n\t"
  "buffer_load_dwordx4 a[120:123], %[vl16], %[srd], s59 offen offset:0 sc1\n\t"
  "v_mfma_f32_16x16x32_bf16 v[48:51], v[68:71], v[108:111], v[48:51]\n\t"
  "v_mfma_f32_16x16x32_bf16 v[44:47], v[68:71], v[104:107], v[44:47]\n\t"
  "ds_read_b128 v[140:143], %[vb3] offset:6144\n\t"
  "v_mfma_f32_16x16x32_bf16 v[40:43], v[68:71], v[100:103], v[40:43]\n\t"
  "v_mfma_f32_16x16x32_bf16 v[36:39], v[68:71], v[96:99], v[36:39]\n\t"
  "s_waitcnt lgkmcnt(11)\n\t"
  "ds_read_b64 v[68:69], %[va] offset:960\n\t"
  "v_mfma_f32_16x16x32_bf16 v[60:63], v[72:75], v[116:119], v[60:63]\n\t"
  "ds_read_b128 v[136:139], %[vb3] offset:4096\n\t"
  "ds_read_b64 v[70:71], %[va] offset:968\n\t"
  "v_mfma_f32_16x16x32_bf16 v[56:59], v[72:75], v[112:115], v[56:59]\n\t"
  "buffer_load_dwordx4 a[124:127], %[vl16], %[srd], s59 offen offset:1024 sc1\n\t"
  "v_mfma_f32_16x16x32_bf16 v[52:55], v[72:75], v[108:111], v[52:55]\n\t"
  "v_mfma_f32_16x16x32_bf16 v[48:51], v[72:75], v[104:107], v[48:51]\n\t"
  "ds_read_b128 v[132:135], %[vb3] offset:2048\n\t"
  "buffer_load_dwordx4 a[128:131], %[vl16], %[srd], s59 offen offset:2048 sc1\n\t"
  "v_mfma_f32_16x16x32_bf16 v[44:47], v[72:75], v[100:103], v[44:47]\n\t"
  "v_mfma_f32_16x16x32_bf16 v[40:43], v[72:75], v[96:99], v[40:43]\n\t"
  "ds_read_b64 v[72:73], %[va] offset:1472\n\t"
  "ds_read_b64 v[74:75], %[va] offset:1480\n\t"
  "v_mfma_f32_16x16x32_bf16 v[60:63], v[76:79], v[112:115], v[60:63]\n\t"
  "ds_read_b128 v[128:131], %[vb3] offset:0\n\t"
  "s_waitcnt vmcnt(15)\n\t"
  "v_add_f32_dpp v244, v244, v244 quad_perm:[1,0,3,2] row_mask:0xf bank_mask:0xf\n\t"
  "v_mfma_f32_16x16x32_bf16 v[56:59], v[76:79], v[108:111], v[56:59]\n\t"
  "buffer_load_dwordx4 a[132:135], %[vl16], %[srd], s59 offen offset:3072 sc1\n\t"
  "v_add_f32_dpp v244, v244, v244 quad_perm:[2,3,0,1] row_mask:0xf bank_mask:0xf\n\t"
  "v_mfma_f32_16x16x32_bf16 v[52:55], v[76:79], v[104:107], v[52:55]\n\t"
  "v_cvt_pk_bf16_f32 v244, 0, v244\n\t"
  "v_add_f32 v245, 1.0, v244\n\t"
  "v_mfma_f32_16x16x32_bf16 v[48:51], v[76:79], v[100:103], v[48:51]\n\t"
  "v_cvt_pk_bf16_f32 v245, 0, v245\n\t"
  "v_readlane_b32 s46, v244, 0\n\t"
  "v_mfma_f32_16x16x32_bf16 v[44:47], v[76:79], v[96:99], v[44:47]\n\t"
  "ds_read_b64 v[76:77], %[va] offset:1984\n\t"
  "ds_read_b64 v[78:79], %[va] offset:1992\n\t"
  "v_mfma_f32_16x16x32_bf16 v[60:63], v[80:83], v[108:111], v[60:63]\n\t"
  "s_add_u32 s59, s59, 4096\n\t"
  "v_readlane_b32 s48, v244, 8\n\t"
  "v_mfma_f32_16x16x32_bf16 v[56:59], v[80:83], v[104:107], v[56:59]\n\t"
  "v_readlane_b32 s47, v245, 4\n\t"
  "v_readlane_b32 s51, v245, 12\n\t"
  "v_mfma_f32_16x16x32_bf16 v[52:55], v[80:83], v[100:103], v[52:55]\n\t"
  "v_mfma_f32_16x16x32_bf16 v[48:51], v[80:83], v[96:99], v[48:51]\n\t"
  "ds_read_b64 v[80:81], %[va] offset:2496\n\t"
  "ds_read_b64 v[82:83], %[va] offset:2504\n\t"
  "v_mfma_f32_16x16x32_bf16 v[60:63], v[84:87], v[104:107], v[60:63]\n\t"
  "buffer_load_dwordx4 a[136:139], %[vl16], %[srd], s59 offen offset:0 sc1\n\t"
  "v_mfma_f32_16x16x32_bf16 v[56:59], v[84:87], v[100:103], v[56:59]\n\t"
  "s_and_b32 s52, s51, 0x7fffffff\n\t"
  "s_cselect_b32 s51, s51, 0x2b800000\n\t"
  "v_mfma_f32_16x16x32_bf16 v[52:55], v[84:87], v[96:99], v[52:55]\n\t"
  "s_waitcnt lgkmcnt(15)\n\t"
  "ds_read_b64 v[84:85], %[va] offset:3008\n\t"
  "v_mfma_f32_16x16x32_bf16 v[60:63], v[88:91], v[100:103], v[60:63]\n\t"
  "ds_read_b64 v[86:87], %[va] offset:3016\n\t"
  "v_mov_b32 v245, s48\n\t"
  "v_mfma_f32_16x16x32_bf16 v[56:59], v[88:91], v[96:99], v[56:59]\n\t"
  "ds_read_b64 v[88:89], %[va] offset:3520\n\t"
  "ds_read_b64 v[90:91], %[va] offset:3528\n\t"
  "v_mfma_f32_16x16x32_bf16 v[60:63], v[92:95], v[96:99], v[60:63]\n\t"
  "s_waitcnt lgkmcnt(8)\n\t"
  "ds_read_b64 v[92:93], %[va] offset:4032\n\t"
  "v_mfma_f32_16x16x32_bf16 v[60:63], v[64:67], v[156:159], v[60:63]\n\t"
  "ds_read_b64 v[94:95], %[va] offset:4040\n\t"
  "buffer_load_dwordx4 a[140:143], %[vl16], %[srd], s59 offen offset:1024 sc1\n\t"
  "v_mfma_f32_16x16x32_bf16 v[56:59], v[64:67], v[152:155], v[56:59]\n\t"
  "v_mul_f32 v245, s51, v245\n\t"
  "v_mul_f32 v244, s47, v245\n\t"
  "v_mfma_f32_16x16x32_bf16 v[52:55], v[64:67], v[148:151], v[52:55]\n\t"
  "buffer_load_dwordx4 a[144:147], %[vl16], %[srd], s59 offen offset:2048 sc1\n\t"
  "v_mul_f32 v245, s46, v245\n\t"
  "v_mfma_f32_16x16x32_bf16 v[48:51], v[64:67], v[144:147], v[48:51]\n\t"
  "s_waitcnt vmcnt(11)\n\t"
  "s_barrier\n\t"
  "v_mfma_f32_16x16x32_bf16 v[44:47], v[64:67], v[140:143], v[44:47]\n\t"
  "v_mov_b32 v246, s51\n\t"
  "ds_read_b64 v[224:225], %[vx] offset:0\n\t"
  "v_mfma_f32_16x16x32_bf16 v[40:43], v[64:67], v[136:139], v[40:43]\n\t"
  "ds_read_b128 v[160:163], %[vs] offset:0\n\t"
  "ds_read_b128 v[164:167], %[vs2] offset:0\n\t"
  "v_mfma_f32_16x16x32_bf16 v[36:39], v[64:67], v[132:135], v[36:39]\n\t"
  "buffer_load_dwordx4 a[148:151], %[vl16], %[srd], s59 offen offset:3072 sc1\n\t"
  "ds_read_b64 v[226:227], %[vx] offset:2048\n\t"
  "v_mfma_f32_16x16x32_bf16 v[32:35], v[64:67], v[128:131], v[32:35]\n\t"
  "ds_read_b128 v[168:171], %[vs] offset:2048\n\t"
  "ds_read_b128 v[172:175], %[vs2] offset:2048\n\t"
  "v_mfma_f32_16x16x32_bf16 v[60:63], v[68:71], v[152:155], v[60:63]\n\t"
  "ds_read_b64 v[228:229], %[vx] offset:4096\n\t"
  "ds_read_b128 v[176:179], %[vs] offset:4096\n\t"
  "v_mfma_f32_16x16x32_bf16 v[56:59], v[68:71], v[148:151], v[56:59]\n\t"
  "ds_read_b128 v[180:183], %[vs2] offset:4096\n\t"
  "ds_read_b64 v[230:231], %[vx] offset:6144\n\t"
  "v_mfma_f32_16x16x32_bf16 v[52:55], v[68:71], v[144:147], v[52:55]\n\t"
  "s_add_u32 s59, s59, 4096\n\t"
  "ds_read_b128 v[184:187], %[vs] offset:6144\n\t"
  "v_mfma_f32_16x16x32_bf16 v[48:51], v[68:71], v[140:143], v[48:51]\n\t"
  "ds_read_b128 v[188:191], %[vs2] offset:6144\n\t"
  "ds_read_b64 v[232:233], %[vx] offset:8192\n\t"
  "v_mfma_f32_16x16x32_bf16 v[44:47], v[68:71], v[136:139], v[44:47]\n\t"
  "ds_read_b128 v[192:195], %[vs] offset:8192\n\t"
  "ds_read_b128 v[196:199], %[vs2] offset:8192\n\t"
  "v_mfma_f32_16x16x32_bf16 v[40:43], v[68:71], v[132:135], v[40:43]\n\t"
  "ds_read_b64 v[234:235], %[vx] offset:10240\n\t"
  "ds_read_b128 v[200:203], %[vs] offset:10240\n\t"
  "v_mfma_f32_16x16x32_bf16 v[36:39], v[68:71], v[128:131], v[36:39]\n\t"
  "s_waitcnt lgkmcnt(15)\n\t"
  "buffer_load_dwordx4 a[152:155], %[vl16], %[srd], s59 offen offset:0 sc1\n\t"
  "v_mfma_f32_16x16x32_bf16 v[60:63], v[72:75], v[148:151], v[60:63]\n\t"
  "ds_read_b128 v[204:207], %[vs2] offset:10240\n\t"
  "ds_read_b64 v[236:237], %[vx] offset:12288\n\t"
  "v_mfma_f32_16x16x32_bf16 v[56:59], v[72:75], v[144:147], v[56:59]\n\t"
  "ds_read_b128 v[208:211], %[vs] offset:12288\n\t"
  "ds_read_b128 v[212:215], %[vs2] offset:12288\n\t"
  "v_mfma_f32_16x16x32_bf16 v[52:55], v[72:75], v[140:143], v[52:55]\n\t"
  "ds_read_b64 v[238:239], %[vx] offset:14336\n\t"
  "ds_read_b128 v[216:219], %[vs] offset:14336\n\t"
  "v_mfma_f32_16x16x32_bf16 v[48:51], v[72:75], v[136:139], v[48:51]\n\t"
  "buffer_load_dwordx4 a[156:159], %[vl16], %[srd], s59 offen offset:1024 sc1\n\t"
  "ds_read_b128 v[220:223], %[vs2] offset:14336\n\t"
  "v_mfma_f32_16x16x32_bf16 v[44:47], v[72:75], v[132:135], v[44:47]\n\t"
  "v_mfma_f32_16x16x32_bf16 v[40:43], v[72:75], v[128:131], v[40:43]\n\t"
  "v_mfma_f32_16x16x32_bf16 v[60:63], v[76:79], v[144:147], v[60:63]\n\t"
  "v_mfma_f32_16x16x32_bf16 v[56:59], v[76:79], v[140:143], v[56:59]\n\t"
  "buffer_load_dwordx4 a[160:163], %[vl16], %[srd], s59 offen offset:2048 sc1\n\t"
  "v_mfma_f32_16x16x32_bf16 v[52:55], v[76:79], v[136:139], v[52:55]\n\t"
  "v_mfma_f32_16x16x32_bf16 v[48:51], v[76:79], v[132:135], v[48:51]\n\t"
  "v_mfma_f32_16x16x32_bf16 v[44:47], v[76:79], v[128:131], v[44:47]\n\t"
  "v_mfma_f32_16x16x32_bf16 v[60:63], v[80:83], v[140:143], v[60:63]\n\t"
  "buffer_load_dwordx4 a[164:167], %[vl16], %[srd], s59 offen offset:3072 sc1\n\t"
  "v_mfma_f32_16x16x32_bf16 v[56:59], v[80:83], v[136:139], v[56:59]\n\t"
  "v_mfma_f32_16x16x32_bf16 v[52:55], v[80:83], v[132:135], v[52:55]\n\t"
  "v_mfma_f32_16x16x32_bf16 v[48:51], v[80:83], v[128:131], v[48:51]\n\t"
  "v_mfma_f32_16x16x32_bf16 v[60:63], v[84:87], v[136:139], v[60:63]\n\t"
  "v_mfma_f32_16x16x32_bf16 v[56:59], v[84:87], v[132:135], v[56:59]\n\t"
  "v_mfma_f32_16x16x32_bf16 v[52:55], v[84:87], v[128:131], v[52:55]\n\t"
  "v_mfma_f32_16x16x32_bf16 v[60:63], v[88:91], v[132:135], v[60:63]\n\t"
  "v_mfma_f32_16x16x32_bf16 v[56:59], v[88:91], v[128:131], v[56:59]\n\t"
  "v_mfma_f32_16x16x32_bf16 v[60:63], v[92:95], v[128:131], v[60:63]\n\t"
  "v_lshlrev_b32 v96, 16, v224\n\t"
  "v_and_b32 v97, 0xffff0000, v224\n\t"
  "v_lshlrev_b32 v98, 16, v225\n\t"
  "v_and_b32 v99, 0xffff0000, v225\n\t"
  "s_waitcnt lgkmcnt(15)\n\t"
  "v_lshlrev_b32 v100, 16, v226\n\t"
  "v_and_b32 v101, 0xffff0000, v226\n\t"
  "v_lshlrev_b32 v102, 16, v227\n\t"
  "v_and_b32 v103, 0xffff0000, v227\n\t"
  "v_pk_fma_f32 v[160:161], v[96:97], v[160:161], v[162:163]\n\t"
  "v_pk_fma_f32 v[164:165], v[98:99], v[164:165], v[166:167]\n\t"
  "v_pk_fma_f32 v[168:169], v[100:101], v[168:169], v[170:171]\n\t"
  "v_pk_fma_f32 v[172:173], v[102:103], v[172:173], v[174:175]\n\t"
  "v_pk_mul_f32 v[96:97], v[96:97], v[246:247] op_sel_hi:[1,0]\n\t"
  "v_pk_mul_f32 v[98:99], v[98:99], v[246:247] op_sel_hi:[1,0]\n\t"
  "v_pk_mul_f32 v[100:101], v[100:101], v[246:247] op_sel_hi:[1,0]\n\t"
  "v_pk_mul_f32 v[102:103], v[102:103], v[246:247] op_sel_hi:[1,0]\n\t"
  "v_pk_fma_f32 v[160:161], v[160:161], v[244:245], v[244:245] op_sel:[0,0,1] op_sel_hi:[1,0,1]\n\t"
  "v_pk_fma_f32 v[164:165], v[164:165], v[244:245], v[244:245] op_sel:[0,0,1] op_sel_hi:[1,0,1]\n\t"
  "v_pk_fma_f32 v[168:169], v[168:169], v[244:245], v[244:245] op_sel:[0,0,1] op_sel_hi:[1,0,1]\n\t"
  "v_pk_fma_f32 v[172:173], v[172:173], v[244:245], v[244:245] op_sel:[0,0,1] op_sel_hi:[1,0,1]\n\t"
  "v_pk_fma_f32 v[96:97], v[160:161], v[32:33], v[96:97]\n\t"
  "v_pk_fma_f32 v[98:99], v[164:165], v[34:35], v[98:99]\n\t"
  "v_pk_fma_f32 v[100:101], v[168:169], v[36:37], v[100:101]\n\t"
  "v_pk_fma_f32 v[102:103], v[172:173], v[38:39], v[102:103]\n\t"
  "v_cvt_pk_bf16_f32 v64, v96, v97\n\t"
  "v_cvt_pk_bf16_f32 v66, v100, v101\n\t"
  "v_cvt_pk_bf16_f32 v65, v98, v99\n\t"
  "v_cvt_pk_bf16_f32 v67, v102, v103\n\t"
  "s_add_u32 s44, %[sO], 0\n\t"
  "v_permlane16_swap_b32 v64, v66\n\t"
  "v_permlane16_swap_b32 v65, v67\n\t"
  "buffer_store_dwordx4 v[64:67], %[vo], %[srd], s44 offen sc1\n\t"
  "v_lshlrev_b32 v104, 16, v228\n\t"
  "v_and_b32 v105, 0xffff0000, v228\n\t"
  "v_lshlrev_b32 v106, 16, v229\n\t"
  "v_and_b32 v107, 0xffff0000, v229\n\t"
  "s_waitcnt lgkmcnt(14)\n\t"
  "v_lshlrev_b32 v108, 16, v230\n\t"
  "v_and_b32 v109, 0xffff0000, v230\n\t"
  "v_lshlrev_b32 v110, 16, v231\n\t"
  "v_and_b32 v111, 0xffff0000, v231\n\t"
  "v_pk_fma_f32 v[176:177], v[104:105], v[176:177], v[178:179]\n\t"
  "v_pk_fma_f32 v[180:181], v[106:107], v[180:181], v[182:183]\n\t"
  "s_waitcnt lgkmcnt(13)\n\t"
  "v_pk_fma_f32 v[184:185], v[108:109], v[184:185], v[186:187]\n\t"
  "s_waitcnt lgkmcnt(12)\n\t"
  "v_pk_fma_f32 v[188:189], v[110:111], v[188:189], v[190:191]\n\t"
  "v_pk_mul_f32 v[104:105], v[104:105], v[246:247] op_sel_hi:[1,0]\n\t"
  "v_pk_mul_f32 v[106:107], v[106:107], v[246:247] op_sel_hi:[1,0]\n\t"
  "v_pk_mul_f32 v[108:109], v[108:109], v[246:247] op_sel_hi:[1,0]\n\t"
  "v_pk_mul_f32 v[110:111], v[110:111], v[246:247] op_sel_hi:[1,0]\n\t"
  "v_pk_fma_f32 v[176:177], v[176:177], v[244:245], v[244:245] op_sel:[0,0,1] op_sel_hi:[1,0,1]\n\t"
  "v_pk_fma_f32 v[180:181], v[180:181], v[244:245], v[244:245] op_sel:[0,0,1] op_sel_hi:[1,0,1]\n\t"
  "v_pk_fma_f32 v[184:185], v[184:185], v[244:245], v[244:245] op_sel:[0,0,1] op_sel_hi:[1,0,1]\n\t"
  "v_pk_fma_f32 v[188:189], v[188:189], v[244:245], v[244:245] op_sel:[0,0,1] op_sel_hi:[1,0,1]\n\t"
  "v_pk_fma_f32 v[104:105], v[176:177], v[40:41], v[104:105]\n\t"
  "v_pk_fma_f32 v[106:107], v[180:181], v[42:43], v[106:107]\n\t"
  "v_pk_fma_f32 v[108:109], v[184:185], v[44:45], v[108:109]\n\t"
  "v_pk_fma_f32 v[110:111], v[188:189], v[46:47], v[110:111]\n\t"
  "v_cvt_pk_bf16_f32 v68, v104, v105\n\t"
  "v_cvt_pk_bf16_f32 v70, v108, v109\n\t"
  "v_cvt_pk_bf16_f32 v69, v106, v107\n\t"
  "v_cvt_pk_bf16_f32 v71, v110, v111\n\t"
  "s_add_u32 s44, %[sO], 655360\n\t"
  "v_permlane16_swap_b32 v68, v70\n\t"
  "v_permlane16_swap_b32 v69, v71\n\t"
  "buffer_store_dwordx4 v[68:71], %[vo], %[srd], s44 offen sc1\n\t"
  "s_waitcnt lgkmcnt(11)\n\t"
  "v_lshlrev_b32 v112, 16, v232\n\t"
  "v_and_b32 v113, 0xffff0000, v232\n\t"
  "v_lshlrev_b32 v114, 16, v233\n\t"
  "v_and_b32 v115, 0xffff0000, v233\n\t"
  "s_waitcnt lgkmcnt(8)\n\t"
  "v_lshlrev_b32 v116, 16, v234\n\t"
  "v_and_b32 v117, 0xffff0000, v234\n\t"
  "v_lshlrev_b32 v118, 16, v235\n\t"
  "v_and_b32 v119, 0xffff0000, v235\n\t"
  "v_pk_fma_f32 v[192:193], v[112:113], v[192:193], v[194:195]\n\t"
  "v_pk_fma_f32 v[196:197], v[114:115], v[196:197], v[198:199]\n\t"
  "s_waitcnt lgkmcnt(7)\n\t"
  "v_pk_fma_f32 v[200:201], v[116:117], v[200:201], v[202:203]\n\t"
  "s_waitcnt lgkmcnt(6)\n\t"
  "v_pk_fma_f32 v[204:205], v[118:119], v[204:205], v[206:207]\n\t"
  "v_pk_mul_f32 v[112:113], v[112:113], v[246:247] op_sel_hi:[1,0]\n\t"
  "v_pk_mul_f32 v[114:115], v[114:115], v[246:247] op_sel_hi:[1,0]\n\t"
  "v_pk_mul_f32 v[116:117], v[116:117], v[246:247] op_sel_hi:[1,0]\n\t"
  "v_pk_mul_f32 v[118:119], v[118:119], v[246:247] op_sel_hi:[1,0]\n\t"
  "v_pk_fma_f32 v[192:193], v[192:193], v[244:245], v[244:245] op_sel:[0,0,1] op_sel_hi:[1,0,1]\n\t"
  "v_pk_fma_f32 v[196:197], v[196:197], v[244:245], v[244:245] op_sel:[0,0,1] op_sel_hi:[1,0,1]\n\t"
  "v_pk_fma_f32 v[200:201], v[200:201], v[244:245], v[244:245] op_sel:[0,0,1] op_sel_hi:[1,0,1]\n\t"
  "v_pk_fma_f32 v[204:205], v[204:205], v[244:245], v[244:245] op_sel:[0,0,1] op_sel_hi:[1,0,1]\n\t"
  "v_pk_fma_f32 v[112:113], v[192:193], v[48:49], v[112:113]\n\t"
  "v_pk_fma_f32 v[114:115], v[196:197], v[50:51], v[114:115]\n\t"
  "v_pk_fma_f32 v[116:117], v[200:201], v[52:53], v[116:117]\n\t"
  "v_pk_fma_f32 v[118:119], v[204:205], v[54:55], v[118:119]\n\t"
  "v_cvt_pk_bf16_f32 v72, v112, v113\n\t"
  "v_cvt_pk_bf16_f32 v74, v116, v117\n\t"
  "v_cvt_pk_bf16_f32 v73, v114, v115\n\t"
  "v_cvt_pk_bf16_f32 v75, v118, v119\n\t"
  "s_add_u32 s44, %[sO], 1310720\n\t"
  "v_permlane16_swap_b32 v72, v74\n\t"
  "v_permlane16_swap_b32 v73, v75\n\t"
  "buffer_store_dwordx4 v[72:75], %[vo], %[srd], s44 offen sc1\n\t"
  "s_waitcnt lgkmcnt(5)\n\t"
  "v_lshlrev_b32 v120, 16, v236\n\t"
  "v_and_b32 v121, 0xffff0000, v236\n\t"
  "v_lshlrev_b32 v122, 16, v237\n\t"
  "v_and_b32 v123, 0xffff0000, v237\n\t"
  "s_waitcnt lgkmcnt(2)\n\t"
  "v_lshlrev_b32 v124, 16, v238\n\t"
  "v_and_b32 v125, 0xffff0000, v238\n\t"
  "v_lshlrev_b32 v126, 16, v239\n\t"
  "v_and_b32 v127, 0xffff0000, v239\n\t"
  "v_pk_fma_f32 v[208:209], v[120:121], v[208:209], v[210:211]\n\t"
  "v_pk_fma_f32 v[212:213], v[122:123], v[212:213], v[214:215]\n\t"
  "s_waitcnt lgkmcnt(1)\n\t"
  "v_pk_fma_f32 v[216:217], v[124:125], v[216:217], v[218:219]\n\t"
  "s_waitcnt lgkmcnt(0)\n\t"
  "v_pk_fma_f32 v[220:221], v[126:127], v[220:221], v[222:223]\n\t"
  "v_pk_mul_f32 v[120:121], v[120:121], v[246:247] op_sel_hi:[1,0]\n\t"
  "v_pk_mul_f32 v[122:123], v[122:123], v[246:247] op_sel_hi:[1,0]\n\t"
  "v_pk_mul_f32 v[124:125], v[124:125], v[246:247] op_sel_hi:[1,0]\n\t"
  "v_pk_mul_f32 v[126:127], v[126:127], v[246:247] op_sel_hi:[1,0]\n\t"
  "v_pk_fma_f32 v[208:209], v[208:209], v[244:245], v[244:245] op_sel:[0,0,1] op_sel_hi:[1,0,1]\n\t"
  "v_pk_fma_f32 v[212:213], v[212:213], v[244:245], v[244:245] op_sel:[0,0,1] op_sel_hi:[1,0,1]\n\t"
  "v_pk_fma_f32 v[216:217], v[216:217], v[244:245], v[244:245] op_sel:[0,0,1] op_sel_hi:[1,0,1]\n\t"
  "v_pk_fma_f32 v[220:221], v[220:221], v[244:245], v[244:245] op_sel:[0,0,1] op_sel_hi:[1,0,1]\n\t"
  "v_pk_fma_f32 v[120:121], v[208:209], v[56:57], v[120:121]\n\t"
  "v_pk_fma_f32 v[122:123], v[212:213], v[58:59], v[122:123]\n\t"
  "v_pk_fma_f32 v[124:125], v[216:217], v[60:61], v[124:125]\n\t"
  "v_pk_fma_f32 v[126:127], v[220:221], v[62:63], v[126:127]\n\t"
  "v_cvt_pk_bf16_f32 v76, v120, v121\n\t"
  "v_cvt_pk_bf16_f32 v78, v124, v125\n\t"
  "v_cvt_pk_bf16_f32 v77, v122, v123\n\t"
  "v_cvt_pk_bf16_f32 v79, v126, v127\n\t"
  "s_add_u32 s44, %[sO], 1966080\n\t"
  "v_permlane16_swap_b32 v76, v78\n\t"
  "v_permlane16_swap_b32 v77, v79\n\t"
  "buffer_store_dwordx4 v[76:79], %[vo], %[srd], s44 offen sc1\n\t"
  "s_nop 1\n\t"
  "s_branch Lcend%=\n\t"
  "La1_slow%=:\n\t"
  "s_nop 7\n\t"
  "s_nop 3\n\t"
  "s_mov_b32 s49, 0x40000\n\t"
  "La1_loop%=:\n\t"
  "s_sleep 3\n\t"
  "buffer_load_dwordx2 v[160:161], %[vf1], %[srd], 0 offen sc1\n\t"
  "s_waitcnt vmcnt(0)\n\t"
  "v_cmp_ne_u64 vcc, v[252:253], v[160:161]\n\t"
  "s_nop 3\n\t"
  "s_cmp_eq_u64 vcc, 0\n\t"
  "s_cbranch_scc1 La1_back%=\n\t"
  "s_sub_u32 s49, s49, 1\n\t"
  "s_cmp_lg_u32 s49, 0\n\t"
  "s_cbranch_scc1 La1_loop%=\n\t"
  "s_branch La1_back%=\n\t"
  "La2_slow%=:\n\t"
  "s_nop 7\n\t"
  "s_nop 3\n\t"
  "s_mov_b32 s49, 0x40000\n\t"
  "La2_loop%=:\n\t"
  "s_sleep 3\n\t"
  "buffer_load_dwordx2 v[162:163], %[vf2], %[srd], 0 offen sc1\n\t"
  "s_waitcnt vmcnt(0)\n\t"
  "v_cmp_ne_u64 vcc, v[252:253], v[162:163]\n\t"
  "s_nop 3\n\t"
  "s_cmp_eq_u64 vcc, 0\n\t"
  "s_cbranch_scc1 La2_back%=\n\t"
  "s_sub_u32 s49, s49, 1\n\t"
  "s_cmp_lg_u32 s49, 0\n\t"
  "s_cbranch_scc1 La2_loop%=\n\t"
  "s_branch La2_back%=\n\t"
  "Lcend%=:\n\t"
      : [wdone] "=&s"(wdone), [pm] "=&s"(pmask)
      : [srd] "s"(srd), [ws] "s"(ws), [sO] "s"(OFF_X2P + d * 128u), [w] "s"(w), [sKV] "s"(OFF_KVT + cb * 128u + w * 655360u),
        [tlo] "s"((u32)tag), [thi] "s"((u32)(tag >> 32)), [sW1] "s"(OFF_W1F + (j * 4 + w) * 61440u),
        [vf2] "v"(c_vf2),
        [vf1] "v"(c_vf1), [vp0] "v"(c_vp0), [vsa] "v"(lb + L_STASH + tid * 16 + 12), [vf3] "v"(c_vf3), [vfw] "v"(c_vfw),
        [va] "v"(c_va), [vb0] "v"(c_vb0), [vb1] "v"(c_vb1), [vb2] "v"(c_vb2), [vb3] "v"(c_vb3), [vx] "v"(c_vx), [vs] "v"(c_vs), [vs2] "v"(c_vs2), [vo] "v"(c_vo), [vd] "v"(c_vd),
        [vl16] "v"(c_vl16), [vkv] "v"(c_vkv),
        [vmod] "v"(c_vmod)
      : "memory", "vcc", "scc",
"a0", "a1", "a2", "a3", "a4", "a5", "a6", "a7", "a8", "a9", "a10", "a11", "a12", "a13", "a14", "a15", "a16", "a17", "a18", "a19", "a20", "a21", "a22", "a23", "a24", "a25", "a26", "a27", "a28", "a29", "a30", "a31", "a32", "a33", "a34", "a35", "a36", "a37", "a38", "a39", "a40", "a41", "a42", "a43", "a44", "a45", "a46", "a47", "a48", "a49", "a50", "a51", "a52", "a53", "a54", "a55", "a56", "a57", "a58", "a59", "a60", "a61", "a62", "a63", "a64", "a65", "a66", "a67", "a68", "a69", "a70", "a71", "a72", "a73", "a74", "a75", "a76", "a77", "a78", "a79", "a80", "a81", "a82", "a83", "a84", "a85", "a86", "a87", "a88", "a89", "a90", "a91", "a92", "a93", "a94", "a95", "a96", "a97", "a98", "a99", "a100", "a101", "a102", "a103", "a104", "a105", "a106", "a107", "a108", "a109", "a110", "a111", "a112", "a113", "a114", "a115", "a116", "a117", "a118", "a119", "a120", "a121", "a122", "a123", "a124", "a125", "a126", "a127", "a128", "a129", "a130", "a131", "a132", "a133", "a134", "a135", "a136", "a137", "a138", "a139", "a140", "a141", "a142", "a143", "a144", "a145", "a146", "a147", "a148", "a149", "a150", "a151", "a152", "a153", "a154", "a155", "a156", "a157", "a158", "a159", "a160", "a161", "a162", "a163", "a164", "a165", "a166", "a167", "s44", "s45", "s46", "s47", "s48", "s49", "s51", "s52", "s53", "s54", "s55", "s56", "s57", "s58", "s59", "s80", "s81", "v32", "v33", "v34", "v35", "v36", "v37", "v38", "v39", "v40", "v41", "v42", "v43", "v44", "v45", "v46", "v47", "v48", "v49", "v50", "v51", "v52", "v53", "v54", "v55", "v56", "v57", "v58", "v59", "v60", "v61", "v62", "v63", "v64", "v65", "v66", "v67", "v68", "v69", "v70", "v71", "v72", "v73", "v74", "v75", "v76", "v77", "v78", "v79", "v80", "v81", "v82", "v83", "v84", "v85", "v86", "v87", "v88", "v89", "v90", "v91", "v92", "v93", "v94", "v95", "v96", "v97", "v98", "v99", "v100", "v101", "v102", "v103", "v104", "v105", "v106", "v107", "v108", "v109", "v110", "v111", "v112", "v113", "v114", "v115", "v116", "v117", "v118", "v119", "v120", "v121", "v122", "v123", "v124", "v125", "v126", "v127", "v128", "v129", "v130", "v131", "v132", "v133", "v134", "v135", "v136", "v137", "v138", "v139", "v140", "v141", "v142", "v143", "v144", "v145", "v146", "v147", "v148", "v149", "v150", "v151", "v152", "v153", "v154", "v155", "v156", "v157", "v158", "v159", "v160", "v161", "v162", "v163", "v164", "v165", "v166", "v167", "v168", "v169", "v170", "v171", "v172", "v173", "v174", "v175", "v176", "v177", "v178", "v179", "v180", "v181", "v182", "v183", "v184", "v185", "v186", "v187", "v188", "v189", "v190", "v191", "v192", "v193", "v194", "v195", "v196", "v197", "v198", "v199", "v200", "v201", "v202", "v203", "v204", "v205", "v206", "v207", "v208", "v209", "v210", "v211", "v212", "v213", "v214", "v215", "v216", "v217", "v218", "v219", "v220", "v221", "v222", "v223", "v224", "v225", "v226", "v227", "v228", "v229", "v230", "v231", "v232", "v233", "v234", "v235", "v236", "v237", "v238", "v239", "v244", "v245", "v246", "v247", "v250", "v251", "v252", "v253", "v254", "v255"
    );
  }
  asm volatile(
  "s_mov_b64 s[68:69], %[ws]\n\t"
  "v_mbcnt_lo_u32_b32 v20, -1, 0\n\t"
  "v_mbcnt_hi_u32_b32 v20, -1, v20\n\t"
  "s_lshl_b32 s52, %[w], 10\n\t"
  "s_add_u32 s52, s52, 143360\n\t"
  "v_lshl_add_u32 v20, v20, 4, s52\n\t"
  "ds_read_b128 v[0:3], v20 offset:0\n\t"
  "ds_read_b128 v[4:7], v20 offset:4096\n\t"
  "ds_read_b128 v[8:11], v20 offset:8192\n\t"
  "ds_read_b128 v[12:15], v20 offset:12288\n\t"
  "ds_read_b128 v[16:19], v20 offset:16384\n\t"
  "s_waitcnt lgkmcnt(0)\n\t"
  "v_mov_b32 v132, %[tlo]\n\t"
  "v_mov_b32 v133, %[thi]\n\t"
  "v_mov_b32 v134, %[tlo]\n\t"
  "v_mov_b32 v135, %[thi]\n\t"
  "s_lshl_b32 s52, %[w], 12\n\t"
  "s_mov_b64 s[60:61], 0\n\t"
  "s_mov_b32 s62, 0\n\t"
  "v_bfrev_b32 v128, 1\n\t"
  "v_add_u32 v124, 40960, v6\n\t"
  "v_mov_b32 v112, 0\n\t"
  "v_mov_b32 v113, 0\n\t"
  "v_mov_b32 v114, 0\n\t"
  "v_mov_b32 v115, 0\n\t"
  "v_mov_b32 v116, 0\n\t"
  "v_mov_b32 v117, 0\n\t"
  "v_mov_b32 v118, 0\n\t"
  "v_mov_b32 v119, 0\n\t"
  "v_mov_b32 v120, 0\n\t"
  "v_mov_b32 v121, 0\n\t"
  "v_and_b32 v136, 0xf0, v1\n\t"
  "v_lshlrev_b32 v137, 3, v136\n\t"
  "v_lshlrev_b32 v136, 2, v136\n\t"
  "s_add_u32 s54, %[sST], 65536\n\t"
  "s_mul_i32 s48, %[w], 20480\n\t"
  "v_add_u32 v141, s48, v1\n\t"
  "v_xor_b32 v109, 32, v5\n\t"
  "v_mov_b32 v108, v5\n\t"
  "v_lshrrev_b32 v139, 2, v1\n\t"
  "s_mov_b32 s47, 419840\n\t"
  "v_not_b32 v126, v132\n\t"
  "v_mov_b32 v127, 0\n\t"
  "s_waitcnt vmcnt(0) lgkmcnt(0)\n\t"
  "s_barrier\n\t"
  "buffer_store_dwordx4 v[132:135], v0, %[srd], 0 offen sc1\n\t"
  "s_movk_i32 s59, 0\n\t"
  "buffer_store_dwordx4 v[112:115], v9, %[srd], s59 offen sc1\n\t"
  "s_movk_i32 s59, 8192\n\t"
  "buffer_store_dwordx4 v[112:115], v9, %[srd], s59 offen sc1\n\t"
  "s_movk_i32 s59, 16384\n\t"
  "buffer_store_dwordx4 v[112:115], v9, %[srd], s59 offen sc1\n\t"
  "s_movk_i32 s59, 24576\n\t"
  "buffer_store_dwordx4 v[112:115], v9, %[srd], s59 offen sc1\n\t"
  "s_bitcmp1_b32 %[wdone], 3\n\t"
  "s_cbranch_scc1 Lw3%=\n\t"
  "s_bitcmp1_b32 %[wdone], 6\n\t"
  "s_cbranch_scc1 Lpw_done%=\n\t"
  "s_mov_b32 s49, 0x40000\n\t"
  "Lpw_loop%=:\n\t"
  "buffer_load_dwordx2 v[130:131], v2, %[srd], 0 offen sc1\n\t"
  "s_waitcnt vmcnt(0)\n\t"
  "v_cmp_eq_u64 vcc, v[132:133], v[130:131]\n\t"
  "s_nop 3\n\t"
  "s_orn2_b64 vcc, vcc, 15\n\t"
  "s_cmp_eq_u64 vcc, -1\n\t"
  "s_cbranch_scc1 Lpw_done%=\n\t"
  "s_sleep 3\n\t"
  "s_sub_u32 s49, s49, 1\n\t"
  "s_cmp_lg_u32 s49, 0\n\t"
  "s_cbranch_scc1 Lpw_loop%=\n\t"
  "Lpw_done%=:\n\t"
  "s_add_u32 s44, %[sW1], 24576\n\t"
  "buffer_load_dwordx4 a[104:107], v1, %[srd], s44 offen offset:2048 sc1\n\t"
  "s_add_u32 s44, %[sW1], 24576\n\t"
  "buffer_load_dwordx4 a[108:111], v1, %[srd], s44 offen offset:3072 sc1\n\t"
  "s_add_u32 s44, %[sW1], 28672\n\t"
  "buffer_load_dwordx4 a[112:115], v1, %[srd], s44 offen offset:0 sc1\n\t"
  "s_add_u32 s44, %[sW1], 28672\n\t"
  "buffer_load_dwordx4 a[116:119], v1, %[srd], s44 offen offset:1024 sc1\n\t"
  "s_add_u32 s44, %[sW1], 28672\n\t"
  "buffer_load_dwordx4 a[120:123], v1, %[srd], s44 offen offset:2048 sc1\n\t"
  "s_add_u32 s44, %[sW1], 28672\n\t"
  "buffer_load_dwordx4 a[124:127], v1, %[srd], s44 offen offset:3072 sc1\n\t"
  "s_add_u32 s44, %[sW1], 32768\n\t"
  "buffer_load_dwordx4 a[128:131], v1, %[srd], s44 offen offset:0 sc1\n\t"
  "s_add_u32 s44, %[sW1], 32768\n\t"
  "buffer_load_dwordx4 a[132:135], v1, %[srd], s44 offen offset:1024 sc1\n\t"
  "s_add_u32 s44, %[sW1], 32768\n\t"
  "buffer_load_dwordx4 a[136:139], v1, %[srd], s44 offen offset:2048 sc1\n\t"
  "s_add_u32 s44, %[sW1], 32768\n\t"
  "buffer_load_dwordx4 a[140:143], v1, %[srd], s44 offen offset:3072 sc1\n\t"
  "s_add_u32 s44, %[sW1], 36864\n\t"
  "buffer_load_dwordx4 a[144:147], v1, %[srd], s44 offen offset:0 sc1\n\t"
  "s_add_u32 s44, %[sW1], 36864\n\t"
  "buffer_load_dwordx4 a[148:151], v1, %[srd], s44 offen offset:1024 sc1\n\t"
  "s_add_u32 s44, %[sW1], 36864\n\t"
  "buffer_load_dwordx4 a[152:155], v1, %[srd], s44 offen offset:2048 sc1\n\t"
  "s_add_u32 s44, %[sW1], 36864\n\t"
  "buffer_load_dwordx4 a[156:159], v1, %[srd], s44 offen offset:3072 sc1\n\t"
  "s_add_u32 s44, %[sW1], 40960\n\t"
  "buffer_load_dwordx4 a[160:163], v1, %[srd], s44 offen offset:0 sc1\n\t"
  "s_add_u32 s44, %[sW1], 40960\n\t"
  "buffer_load_dwordx4 a[164:167], v1, %[srd], s44 offen offset:1024 sc1\n\t"
  "Lw3%=:\n\t"
  "s_bitcmp1_b32 %[wdone], 2\n\t"
  "s_cbranch_scc1 Lw2%=\n\t"
  "s_add_u32 s44, %[sW1], 16384\n\t"
  "buffer_load_dwordx4 a[64:67], v1, %[srd], s44 offen offset:0 sc1\n\t"
  "s_add_u32 s44, %[sW1], 16384\n\t"
  "buffer_load_dwordx4 a[68:71], v1, %[srd], s44 offen offset:1024 sc1\n\t"
  "s_add_u32 s44, %[sW1], 16384\n\t"
  "buffer_load_dwordx4 a[72:75], v1, %[srd], s44 offen offset:2048 sc1\n\t"
  "s_add_u32 s44, %[sW1], 16384\n\t"
  "buffer_load_dwordx4 a[76:79], v1, %[srd], s44 offen offset:3072 sc1\n\t"
  "s_add_u32 s44, %[sW1], 20480\n\t"
  "buffer_load_dwordx4 a[80:83], v1, %[srd], s44 offen offset:0 sc1\n\t"
  "s_add_u32 s44, %[sW1], 20480\n\t"
  "buffer_load_dwordx4 a[84:87], v1, %[srd], s44 offen offset:1024 sc1\n\t"
  "s_add_u32 s44, %[sW1], 20480\n\t"
  "buffer_load_dwordx4 a[88:91], v1, %[srd], s44 offen offset:2048 sc1\n\t"
  "s_add_u32 s44, %[sW1], 20480\n\t"
  "buffer_load_dwordx4 a[92:95], v1, %[srd], s44 offen offset:3072 sc1\n\t"
  "s_add_u32 s44, %[sW1], 24576\n\t"
  "buffer_load_dwordx4 a[96:99], v1, %[srd], s44 offen offset:0 sc1\n\t"
  "s_add_u32 s44, %[sW1], 24576\n\t"
  "buffer_load_dwordx4 a[100:103], v1, %[srd], s44 offen offset:1024 sc1\n\t"
  "Lw2%=:\n\t"
  "s_bitcmp1_b32 %[wdone], 1\n\t"
  "s_cbranch_scc1 Lw1%=\n\t"
  "s_add_u32 s44, %[sW1], 8192\n\t"
  "buffer_load_dwordx4 a[32:35], v1, %[srd], s44 offen offset:0 sc1\n\t"
  "s_add_u32 s44, %[sW1], 8192\n\t"
  "buffer_load_dwordx4 a[36:39], v1, %[srd], s44 offen offset:1024 sc1\n\t"
  "s_add_u32 s44, %[sW1], 8192\n\t"
  "buffer_load_dwordx4 a[40:43], v1, %[srd], s44 offen offset:2048 sc1\n\t"
  "s_add_u32 s44, %[sW1], 8192\n\t"
  "buffer_load_dwordx4 a[44:47], v1, %[srd], s44 offen offset:3072 sc1\n\t"
  "s_add_u32 s44, %[sW1], 12288\n\t"
  "buffer_load_dwordx4 a[48:51], v1, %[srd], s44 offen offset:0 sc1\n\t"
  "s_add_u32 s44, %[sW1], 12288\n\t"
  "buffer_load_dwordx4 a[52:55], v1, %[srd], s44 offen offset:1024 sc1\n\t"
  "s_add_u32 s44, %[sW1], 12288\n\t"
  "buffer_load_dwordx4 a[56:59], v1, %[srd], s44 offen offset:2048 sc1\n\t"
  "s_add_u32 s44, %[sW1], 12288\n\t"
  "buffer_load_dwordx4 a[60:63], v1, %[srd], s44 offen offset:3072 sc1\n\t"
  "Lw1%=:\n\t"
  "s_bitcmp1_b32 %[wdone], 0\n\t"
  "s_cbranch_scc1 Lw0%=\n\t"
  "s_add_u32 s44, %[sW1], 0\n\t"
  "buffer_load_dwordx4 a[0:3], v1, %[srd], s44 offen offset:0 sc1\n\t"
  "s_add_u32 s44, %[sW1], 0\n\t"
  "buffer_load_dwordx4 a[4:7], v1, %[srd], s44 offen offset:1024 sc1\n\t"
  "s_add_u32 s44, %[sW1], 0\n\t"
  "buffer_load_dwordx4 a[8:11], v1, %[srd], s44 offen offset:2048 sc1\n\t"
  "s_add_u32 s44, %[sW1], 0\n\t"
  "buffer_load_dwordx4 a[12:15], v1, %[srd], s44 offen offset:3072 sc1\n\t"
  "s_add_u32 s44, %[sW1], 4096\n\t"
  "buffer_load_dwordx4 a[16:19], v1, %[srd], s44 offen offset:0 sc1\n\t"
  "s_add_u32 s44, %[sW1], 4096\n\t"
  "buffer_load_dwordx4 a[20:23], v1, %[srd], s44 offen offset:1024 sc1\n\t"
  "s_add_u32 s44, %[sW1], 4096\n\t"
  "buffer_load_dwordx4 a[24:27], v1, %[srd], s44 offen offset:2048 sc1\n\t"
  "s_add_u32 s44, %[sW1], 4096\n\t"
  "buffer_load_dwordx4 a[28:31], v1, %[srd], s44 offen offset:3072 sc1\n\t"
  "Lw0%=:\n\t"
  "s_bitcmp1_b32 %[wdone], 7\n\t"
  "s_cselect_b32 s58, 5, 0\n\t"
  "s_mov_b32 s49, 0x40000\n\t"
  "Lpx_loop%=:\n\t"
  "buffer_load_dwordx2 v[126:127], v3, %[srd], 0 offen sc1\n\t"
  "s_cmp_lg_u32 s58, 0\n\t"
  "s_cbranch_scc1 Lpk0%=\n\t"
  "s_add_u32 s44, %[sW1], 40960\n\t"
  "buffer_load_dwordx4 a[168:171], v1, %[srd], s44 offen offset:2048 sc1\n\t"
  "s_add_u32 s44, %[sW1], 40960\n\t"
  "buffer_load_dwordx4 a[172:175], v1, %[srd], s44 offen offset:3072 sc1\n\t"
  "s_add_u32 s44, %[sW1], 45056\n\t"
  "buffer_load_dwordx4 a[176:179], v1, %[srd], s44 offen offset:0 sc1\n\t"
  "s_add_u32 s44, %[sW1], 45056\n\t"
  "buffer_load_dwordx4 a[180:183], v1, %[srd], s44 offen offset:1024 sc1\n\t"
  "s_mov_b32 s58, 1\n\t"
  "s_waitcnt vmcnt(4)\n\t"
  "s_branch Lpx_chk%=\n\t"
  "Lpk0%=:\n\t"
  "s_cmp_lg_u32 s58, 1\n\t"
  "s_cbranch_scc1 Lpk1%=\n\t"
  "s_add_u32 s44, %[sW1], 45056\n\t"
  "buffer_load_dwordx4 a[184:187], v1, %[srd], s44 offen offset:2048 sc1\n\t"
  "s_add_u32 s44, %[sW1], 45056\n\t"
  "buffer_load_dwordx4 a[188:191], v1, %[srd], s44 offen offset:3072 sc1\n\t"
  "s_add_u32 s44, %[sW1], 49152\n\t"
  "buffer_load_dwordx4 a[192:195], v1, %[srd], s44 offen offset:0 sc1\n\t"
  "s_add_u32 s44, %[sW1], 49152\n\t"
  "buffer_load_dwordx4 a[196:199], v1, %[srd], s44 offen offset:1024 sc1\n\t"
  "s_mov_b32 s58, 2\n\t"
  "s_waitcnt vmcnt(4)\n\t"
  "s_branch Lpx_chk%=\n\t"
  "Lpk1%=:\n\t"
  "s_cmp_lg_u32 s58, 2\n\t"
  "s_cbranch_scc1 Lpk2%=\n\t"
  "s_add_u32 s44, %[sW1], 49152\n\t"
  "buffer_load_dwordx4 a[200:203], v1, %[srd], s44 offen offset:2048 sc1\n\t"
  "s_add_u32 s44, %[sW1], 49152\n\t"
  "buffer_load_dwordx4 a[204:207], v1, %[srd], s44 offen offset:3072 sc1\n\t"
  "s_add_u32 s44, %[sW1], 53248\n\t"
  "buffer_load_dwordx4 a[208:211], v1, %[srd], s44 offen offset:0 sc1\n\t"
  "s_add_u32 s44, %[sW1], 53248\n\t"
  "buffer_load_dwordx4 a[212:215], v1, %[srd], s44 offen offset:1024 sc1\n\t"
  "s_mov_b32 s58, 3\n\t"
  "s_waitcnt vmcnt(4)\n\t"
  "s_branch Lpx_chk%=\n\t"
  "Lpk2%=:\n\t"
  "s_cmp_lg_u32 s58, 3\n\t"
  "s_cbranch_scc1 Lpk3%=\n\t"
  "s_add_u32 s44, %[sW1], 53248\n\t"
  "buffer_load_dwordx4 a[216:219], v1, %[srd], s44 offen offset:2048 sc1\n\t"
  "s_add_u32 s44, %[sW1], 53248\n\t"
  "buffer_load_dwordx4 a[220:223], v1, %[srd], s44 offen offset:3072 sc1\n\t"
  "s_add_u32 s44, %[sW1], 57344\n\t"
  "buffer_load_dwordx4 a[224:227], v1, %[srd], s44 offen offset:0 sc1\n\t"
  "s_add_u32 s44, %[sW1], 57344\n\t"
  "buffer_load_dwordx4 a[228:231], v1, %[srd], s44 offen offset:1024 sc1\n\t"
  "s_mov_b32 s58, 4\n\t"
  "s_waitcnt vmcnt(4)\n\t"
  "s_branch Lpx_chk%=\n\t"
  "Lpk3%=:\n\t"
  "s_cmp_lg_u32 s58, 4\n\t"
  "s_cbranch_scc1 Lpk4%=\n\t"
  "s_add_u32 s44, %[sW1], 57344\n\t"
  "buffer_load_dwordx4 a[232:235], v1, %[srd], s44 offen offset:2048 sc1\n\t"
  "s_add_u32 s44, %[sW1], 57344\n\t"
  "buffer_load_dwordx4 a[236:239], v1, %[srd], s44 offen offset:3072 sc1\n\t"
  "s_mov_b32 s58, 5\n\t"
  "s_waitcnt vmcnt(2)\n\t"
  "s_branch Lpx_chk%=\n\t"
  "Lpk4%=:\n\t"
  "s_sleep 2\n\t"
  "s_waitcnt vmcnt(0)\n\t"
  "Lpx_chk%=:\n\t"
  "v_cmp_eq_u64 vcc, v[132:133], v[126:127]\n\t"
  "s_nop 3\n\t"
  "s_or_b64 s[60:61], s[60:61], vcc\n\t"
  "s_cmp_eq_u64 s[60:61], -1\n\t"
  "s_cbranch_scc1 Lpx_done%=\n\t"
  "v_cndmask_b32 v3, v3, v128, vcc\n\t"
  "s_bitcmp1_b32 s62, 0\n\t"
  "s_cbranch_scc1 Le1%=\n\t"
  "s_bfe_u32 s63, s60, 0x80000\n\t"
  "s_cmpk_lg_u32 s63, 0xff\n\t"
  "s_cbranch_scc1 Le1%=\n\t"
  "s_bitset1_b32 s62, 0\n\t"
  "s_add_u32 m0, s52, 0\n\t"
  "s_mov_b32 s46, 0\n\t"
  "buffer_load_dwordx4 v4, %[srd], s46 offen offset:0 sc1 lds\n\t"
  "buffer_load_dwordx4 v4, %[srd], s46 offen offset:1024 sc1 lds\n\t"
  "buffer_load_dwordx4 v4, %[srd], s46 offen offset:2048 sc1 lds\n\t"
  "buffer_load_dwordx4 v4, %[srd], s46 offen offset:3072 sc1 lds\n\t"
  "Le1%=:\n\t"
  "s_bitcmp1_b32 s62, 1\n\t"
  "s_cbranch_scc1 Le2%=\n\t"
  "s_bfe_u32 s63, s60, 0x80008\n\t"
  "s_cmpk_lg_u32 s63, 0xff\n\t"
  "s_cbranch_scc1 Le2%=\n\t"
  "s_bitset1_b32 s62, 1\n\t"
  "s_add_u32 m0, s52, 16384\n\t"
  "s_mov_b32 s46, 16384\n\t"
  "buffer_load_dwordx4 v4, %[srd], s46 offen offset:0 sc1 lds\n\t"
  "buffer_load_dwordx4 v4, %[srd], s46 offen offset:1024 sc1 lds\n\t"
  "buffer_load_dwordx4 v4, %[srd], s46 offen offset:2048 sc1 lds\n\t"
  "buffer_load_dwordx4 v4, %[srd], s46 offen offset:3072 sc1 lds\n\t"
  "Le2%=:\n\t"
  "s_bitcmp1_b32 s62, 2\n\t"
  "s_cbranch_scc1 Le3%=\n\t"
  "s_bfe_u32 s63, s60, 0x80010\n\t"
  "s_cmpk_lg_u32 s63, 0xff\n\t"
  "s_cbranch_scc1 Le3%=\n\t"
  "s_bitset1_b32 s62, 2\n\t"
  "s_add_u32 m0, s52, 32768\n\t"
  "s_mov_b32 s46, 32768\n\t"
  "buffer_load_dwordx4 v4, %[srd], s46 offen offset:0 sc1 lds\n\t"
  "buffer_load_dwordx4 v4, %[srd], s46 offen offset:1024 sc1 lds\n\t"
  "buffer_load_dwordx4 v4, %[srd], s46 offen offset:2048 sc1 lds\n\t"
  "buffer_load_dwordx4 v4, %[srd], s46 offen offset:3072 sc1 lds\n\t"
  "Le3%=:\n\t"
  "s_bitcmp1_b32 s62, 3\n\t"
  "s_cbranch_scc1 Le4%=\n\t"
  "s_bfe_u32 s63, s60, 0x80018\n\t"
  "s_cmpk_lg_u32 s63, 0xff\n\t"
  "s_cbranch_scc1 Le4%=\n\t"
  "s_bitset1_b32 s62, 3\n\t"
  "s_add_u32 m0, s52, 49152\n\t"
  "s_mov_b32 s46, 49152\n\t"
  "buffer_load_dwordx4 v4, %[srd], s46 offen offset:0 sc1 lds\n\t"
  "buffer_load_dwordx4 v4, %[srd], s46 offen offset:1024 sc1 lds\n\t"
  "buffer_load_dwordx4 v4, %[srd], s46 offen offset:2048 sc1 lds\n\t"
  "buffer_load_dwordx4 v4, %[srd], s46 offen offset:3072 sc1 lds\n\t"
  "Le4%=:\n\t"
  "s_bitcmp1_b32 s62, 4\n\t"
  "s_cbranch_scc1 Le5%=\n\t"
  "s_bfe_u32 s63, s61, 0x80000\n\t"
  "s_cmpk_lg_u32 s63, 0xff\n\t"
  "s_cbranch_scc1 Le5%=\n\t"
  "s_bitset1_b32 s62, 4\n\t"
  "s_add_u32 m0, s52, 65536\n\t"
  "s_mov_b32 s46, 65536\n\t"
  "buffer_load_dwordx4 v4, %[srd], s46 offen offset:0 sc1 lds\n\t"
  "buffer_load_dwordx4 v4, %[srd], s46 offen offset:1024 sc1 lds\n\t"
  "buffer_load_dwordx4 v4, %[srd], s46 offen offset:2048 sc1 lds\n\t"
  "buffer_load_dwordx4 v4, %[srd], s46 offen offset:3072 sc1 lds\n\t"
  "Le5%=:\n\t"
  "s_bitcmp1_b32 s62, 5\n\t"
  "s_cbranch_scc1 Le6%=\n\t"
  "s_ashr_i32 s63, s61, 8\n\t"
  "s_cmp_lg_u32 s63, -1\n\t"
  "s_cbranch_scc1 Le6%=\n\t"
  "s_bitset1_b32 s62, 5\n\t"
  "buffer_load_dword v142, v139, %[srd], s47 offen offset:0 sc1\n\t"
  "buffer_load_dword v143, v139, %[srd], s47 offen offset:256 sc1\n\t"
  "buffer_load_dword v248, v139, %[srd], s47 offen offset:512 sc1\n\t"
  "buffer_load_dword v249, v139, %[srd], s47 offen offset:768 sc1\n\t"
  "buffer_load_dword v250, v139, %[srd], s47 offen offset:1024 sc1\n\t"
  "buffer_load_dword v251, v139, %[srd], s47 offen offset:1280 sc1\n\t"
  "buffer_load_dword v252, v139, %[srd], s47 offen offset:1536 sc1\n\t"
  "buffer_load_dword v253, v139, %[srd], s47 offen offset:1792 sc1\n\t"
  "buffer_load_dword v254, v139, %[srd], s47 offen offset:2048 sc1\n\t"
  "buffer_load_dword v255, v139, %[srd], s47 offen offset:2304 sc1\n\t"
  "Le6%=:\n\t"
  "s_sub_u32 s49, s49, 1\n\t"
  "s_cmp_lg_u32 s49, 0\n\t"
  "s_cbranch_scc1 Lpx_loop%=\n\t"
  "Lpx_done%=:\n\t"
  "s_bitcmp1_b32 s62, 0\n\t"
  "s_cbranch_scc1 Lf0%=\n\t"
  "s_add_u32 m0, s52, 0\n\t"
  "s_mov_b32 s46, 0\n\t"
  "buffer_load_dwordx4 v4, %[srd], s46 offen offset:0 sc1 lds\n\t"
  "buffer_load_dwordx4 v4, %[srd], s46 offen offset:1024 sc1 lds\n\t"
  "buffer_load_dwordx4 v4, %[srd], s46 offen offset:2048 sc1 lds\n\t"
  "buffer_load_dwordx4 v4, %[srd], s46 offen offset:3072 sc1 lds\n\t"
  "Lf0%=:\n\t"
  "s_bitcmp1_b32 s62, 1\n\t"
  "s_cbranch_scc1 Lf1%=\n\t"
  "s_add_u32 m0, s52, 16384\n\t"
  "s_mov_b32 s46, 16384\n\t"
  "buffer_load_dwordx4 v4, %[srd], s46 offen offset:0 sc1 lds\n\t"
  "buffer_load_dwordx4 v4, %[srd], s46 offen offset:1024 sc1 lds\n\t"
  "buffer_load_dwordx4 v4, %[srd], s46 offen offset:2048 sc1 lds\n\t"
  "buffer_load_dwordx4 v4, %[srd], s46 offen offset:3072 sc1 lds\n\t"
  "Lf1%=:\n\t"
  "s_bitcmp1_b32 s62, 2\n\t"
  "s_cbranch_scc1 Lf2%=\n\t"
  "s_add_u32 m0, s52, 32768\n\t"
  "s_mov_b32 s46, 32768\n\t"
  "buffer_load_dwordx4 v4, %[srd], s46 offen offset:0 sc1 lds\n\t"
  "buffer_load_dwordx4 v4, %[srd], s46 offen offset:1024 sc1 lds\n\t"
  "buffer_load_dwordx4 v4, %[srd], s46 offen offset:2048 sc1 lds\n\t"
  "buffer_load_dwordx4 v4, %[srd], s46 offen offset:3072 sc1 lds\n\t"
  "Lf2%=:\n\t"
  "s_bitcmp1_b32 s62, 3\n\t"
  "s_cbranch_scc1 Lf3%=\n\t"
  "s_add_u32 m0, s52, 49152\n\t"
  "s_mov_b32 s46, 49152\n\t"
  "buffer_load_dwordx4 v4, %[srd], s46 offen offset:0 sc1 lds\n\t"
  "buffer_load_dwordx4 v4, %[srd], s46 offen offset:1024 sc1 lds\n\t"
  "buffer_load_dwordx4 v4, %[srd], s46 offen offset:2048 sc1 lds\n\t"
  "buffer_load_dwordx4 v4, %[srd], s46 offen offset:3072 sc1 lds\n\t"
  "Lf3%=:\n\t"
  "s_bitcmp1_b32 s62, 4\n\t"
  "s_cbranch_scc1 Lf4%=\n\t"
  "s_add_u32 m0, s52, 65536\n\t"
  "s_mov_b32 s46, 65536\n\t"
  "buffer_load_dwordx4 v4, %[srd], s46 offen offset:0 sc1 lds\n\t"
  "buffer_load_dwordx4 v4, %[srd], s46 offen offset:1024 sc1 lds\n\t"
  "buffer_load_dwordx4 v4, %[srd], s46 offen offset:2048 sc1 lds\n\t"
  "buffer_load_dwordx4 v4, %[srd], s46 offen offset:3072 sc1 lds\n\t"
  "Lf4%=:\n\t"
  "s_bitcmp1_b32 s62, 5\n\t"
  "s_cbranch_scc1 Lf5%=\n\t"
  "s_waitcnt vmcnt(29)\n\t"
  "buffer_load_dword v142, v139, %[srd], s47 offen offset:0 sc1\n\t"
  "s_waitcnt vmcnt(29)\n\t"
  "buffer_load_dword v143, v139, %[srd], s47 offen offset:256 sc1\n\t"
  "s_waitcnt vmcnt(29)\n\t"
  "buffer_load_dword v248, v139, %[srd], s47 offen offset:512 sc1\n\t"
  "s_waitcnt vmcnt(29)\n\t"
  "buffer_load_dword v249, v139, %[srd], s47 offen offset:768 sc1\n\t"
  "s_waitcnt vmcnt(29)\n\t"
  "buffer_load_dword v250, v139, %[srd], s47 offen offset:1024 sc1\n\t"
  "s_waitcnt vmcnt(29)\n\t"
  "buffer_load_dword v251, v139, %[srd], s47 offen offset:1280 sc1\n\t"
  "s_waitcnt vmcnt(29)\n\t"
  "buffer_load_dword v252, v139, %[srd], s47 offen offset:1536 sc1\n\t"
  "s_waitcnt vmcnt(29)\n\t"
  "buffer_load_dword v253, v139, %[srd], s47 offen offset:1792 sc1\n\t"
  "s_waitcnt vmcnt(29)\n\t"
  "buffer_load_dword v254, v139, %[srd], s47 offen offset:2048 sc1\n\t"
  "s_waitcnt vmcnt(29)\n\t"
  "buffer_load_dword v255, v139, %[srd], s47 offen offset:2304 sc1\n\t"
  "Lf5%=:\n\t"
  "s_waitcnt vmcnt(0)\n\t"
  "s_barrier\n\t"
  "ds_read_b64_tr_b16 v[68:69], v108 offset:0\n\t"
  "ds_read_b64_tr_b16 v[70:71], v108 offset:2048\n\t"
  "ds_read_b64_tr_b16 v[72:73], v109 offset:0\n\t"
  "ds_read_b64_tr_b16 v[74:75], v109 offset:2048\n\t"
  "ds_read_b64_tr_b16 v[76:77], v108 offset:4096\n\t"
  "ds_read_b64_tr_b16 v[78:79], v108 offset:6144\n\t"
  "ds_read_b64_tr_b16 v[80:81], v109 offset:4096\n\t"
  "ds_read_b64_tr_b16 v[82:83], v109 offset:6144\n\t"
  "ds_read_u16_d16_hi v112, v6 offset:0\n\t"
  "ds_read_u16_d16_hi v113, v6 offset:8192\n\t"
  "ds_read_u16_d16_hi v114, v6 offset:16384\n\t"
  "ds_read_u16_d16_hi v115, v6 offset:24576\n\t"
  "ds_read_u16_d16_hi v116, v6 offset:32768\n\t"
  "ds_read_u16_d16_hi v117, v124 offset:0\n\t"
  "ds_read_u16_d16_hi v118, v124 offset:8192\n\t"
  "ds_read_u16_d16_hi v119, v124 offset:16384\n\t"
  "ds_read_u16_d16_hi v120, v124 offset:24576\n\t"
  "ds_read_u16_d16_hi v121, v124 offset:32768\n\t"
  "s_cmp_gt_u32 s58, 0\n\t"
  "s_cbranch_scc1 Lc0%=\n\t"
  "s_add_u32 s44, %[sW1], 40960\n\t"
  "buffer_load_dwordx4 a[168:171], v1, %[srd], s44 offen offset:2048 sc1\n\t"
  "s_add_u32 s44, %[sW1], 40960\n\t"
  "buffer_load_dwordx4 a[172:175], v1, %[srd], s44 offen offset:3072 sc1\n\t"
  "s_add_u32 s44, %[sW1], 45056\n\t"
  "buffer_load_dwordx4 a[176:179], v1, %[srd], s44 offen offset:0 sc1\n\t"
  "s_add_u32 s44, %[sW1], 45056\n\t"
  "buffer_load_dwordx4 a[180:183], v1, %[srd], s44 offen offset:1024 sc1\n\t"
  "Lc0%=:\n\t"
  "s_cmp_gt_u32 s58, 1\n\t"
  "s_cbranch_scc1 Lc1%=\n\t"
  "s_add_u32 s44, %[sW1], 45056\n\t"
  "buffer_load_dwordx4 a[184:187], v1, %[srd], s44 offen offset:2048 sc1\n\t"
  "s_add_u32 s44, %[sW1], 45056\n\t"
  "buffer_load_dwordx4 a[188:191], v1, %[srd], s44 offen offset:3072 sc1\n\t"
  "s_add_u32 s44, %[sW1], 49152\n\t"
  "buffer_load_dwordx4 a[192:195], v1, %[srd], s44 offen offset:0 sc1\n\t"
  "s_add_u32 s44, %[sW1], 49152\n\t"
  "buffer_load_dwordx4 a[196:199], v1, %[srd], s44 offen offset:1024 sc1\n\t"
  "Lc1%=:\n\t"
  "s_cmp_gt_u32 s58, 2\n\t"
  "s_cbranch_scc1 Lc2%=\n\t"
  "s_add_u32 s44, %[sW1], 49152\n\t"
  "buffer_load_dwordx4 a[200:203], v1, %[srd], s44 offen offset:2048 sc1\n\t"
  "s_add_u32 s44, %[sW1], 49152\n\t"
  "buffer_load_dwordx4 a[204:207], v1, %[srd], s44 offen offset:3072 sc1\n\t"
  "s_add_u32 s44, %[sW1], 53248\n\t"
  "buffer_load_dwordx4 a[208:211], v1, %[srd], s44 offen offset:0 sc1\n\t"
  "s_add_u32 s44, %[sW1], 53248\n\t"
  "buffer_load_dwordx4 a[212:215], v1, %[srd], s44 offen offset:1024 sc1\n\t"
  "Lc2%=:\n\t"
  "s_cmp_gt_u32 s58, 3\n\t"
  "s_cbranch_scc1 Lc3%=\n\t"
  "s_add_u32 s44, %[sW1], 53248\n\t"
  "buffer_load_dwordx4 a[216:219], v1, %[srd], s44 offen offset:2048 sc1\n\t"
  "s_add_u32 s44, %[sW1], 53248\n\t"
  "buffer_load_dwordx4 a[220:223], v1, %[srd], s44 offen offset:3072 sc1\n\t"
  "s_add_u32 s44, %[sW1], 57344\n\t"
  "buffer_load_dwordx4 a[224:227], v1, %[srd], s44 offen offset:0 sc1\n\t"
  "s_add_u32 s44, %[sW1], 57344\n\t"
  "buffer_load_dwordx4 a[228:231], v1, %[srd], s44 offen offset:1024 sc1\n\t"
  "Lc3%=:\n\t"
  "s_cmp_gt_u32 s58, 4\n\t"
  "s_cbranch_scc1 Lc4%=\n\t"
  "s_add_u32 s44, %[sW1], 57344\n\t"
  "buffer_load_dwordx4 a[232:235], v1, %[srd], s44 offen offset:2048 sc1\n\t"
  "s_add_u32 s44, %[sW1], 57344\n\t"
  "buffer_load_dwordx4 a[236:239], v1, %[srd], s44 offen offset:3072 sc1\n\t"
  "Lc4%=:\n\t"
  "s_waitcnt lgkmcnt(15)\n\t"
  "v_mfma_f32_16x16x32_bf16 v[20:23], a[0:3], v[68:71], 0\n\t"
  "ds_read_b64_tr_b16 v[84:85], v108 offset:8192\n\t"
  "v_add_u32 v110, 40960, v5\n\t"
  "v_mfma_f32_16x16x32_bf16 v[24:27], a[4:7], v[68:71], 0\n\t"
  "ds_read_b64_tr_b16 v[86:87], v108 offset:10240\n\t"
  "v_xor_b32 v111, 32, v5\n\t"
  "v_mfma_f32_16x16x32_bf16 v[28:31], a[8:11], v[68:71], 0\n\t"
  "ds_read_b64_tr_b16 v[88:89], v109 offset:8192\n\t"
  "v_add_u32 v111, 40960, v111\n\t"
  "s_waitcnt lgkmcnt(15)\n\t"
  "v_mfma_f32_16x16x32_bf16 v[32:35], a[0:3], v[72:75], 0\n\t"
  "ds_read_b64_tr_b16 v[90:91], v109 offset:10240\n\t"
  "s_add_u32 m0, s52, 81920\n\t"
  "v_mfma_f32_16x16x32_bf16 v[36:39], a[4:7], v[72:75], 0\n\t"
  "s_mov_b32 s46, 1310720\n\t"
  "buffer_load_dwordx4 v4, %[srd], s46 offen offset:0 sc1 lds\n\t"
  "v_mfma_f32_16x16x32_bf16 v[40:43], a[8:11], v[72:75], 0\n\t"
  "s_waitcnt lgkmcnt(14)\n\t"
  "v_mfma_f32_16x16x32_bf16 v[20:23], a[12:15], v[76:79], v[20:23]\n\t"
  "ds_read_b64_tr_b16 v[68:69], v108 offset:12288\n\t"
  "v_mfma_f32_16x16x32_bf16 v[24:27], a[16:19], v[76:79], v[24:27]\n\t"
  "ds_read_b64_tr_b16 v[70:71], v108 offset:14336\n\t"
  "buffer_load_dwordx4 v4, %[srd], s46 offen offset:1024 sc1 lds\n\t"
  "v_mfma_f32_16x16x32_bf16 v[28:31], a[20:23], v[76:79], v[28:31]\n\t"
  "ds_read_b64_tr_b16 v[72:73], v109 offset:12288\n\t"
  "v_mfma_f32_16x16x32_bf16 v[32:35], a[12:15], v[80:83], v[32:35]\n\t"
  "ds_read_b64_tr_b16 v[74:75], v109 offset:14336\n\t"
  "s_waitcnt lgkmcnt(15)\n\t"
  "v_mul_f32 v112, v112, v142\n\t"
  "v_mul_f32 v113, v113, v143\n\t"
  "v_mfma_f32_16x16x32_bf16 v[36:39], a[16:19], v[80:83], v[36:39]\n\t"
  "v_mul_f32 v114, v114, v248\n\t"
  "s_waitcnt lgkmcnt(14)\n\t"
  "v_mul_f32 v115, v115, v249\n\t"
  "s_waitcnt lgkmcnt(13)\n\t"
  "v_mul_f32 v116, v116, v250\n\t"
  "v_mfma_f32_16x16x32_bf16 v[40:43], a[20:23], v[80:83], v[40:43]\n\t"
  "s_waitcnt lgkmcnt(4)\n\t"
  "v_mul_f32 v117, v117, v251\n\t"
  "v_mul_f32 v118, v118, v252\n\t"
  "v_mul_f32 v119, v119, v253\n\t"
  "v_mfma_f32_16x16x32_bf16 v[20:23], a[24:27], v[84:87], v[20:23]\n\t"
  "ds_read_b64_tr_b16 v[76:77], v108 offset:16384\n\t"
  "v_mul_f32 v120, v120, v254\n\t"
  "v_mul_f32 v121, v121, v255\n\t"
  "v_add_f32 v122, v112, v113\n\t"
  "v_mfma_f32_16x16x32_bf16 v[24:27], a[28:31], v[84:87], v[24:27]\n\t"
  "ds_read_b64_tr_b16 v[78:79], v108 offset:18432\n\t"
  "v_mul_f32 v123, v112, v112\n\t"
  "v_fmac_f32 v123, v113, v113\n\t"
  "v_add_f32 v122, v122, v114\n\t"
  "v_mfma_f32_16x16x32_bf16 v[28:31], a[32:35], v[84:87], v[28:31]\n\t"
  "ds_read_b64_tr_b16 v[80:81], v109 offset:16384\n\t"
  "v_fmac_f32 v123, v114, v114\n\t"
  "v_add_f32 v122, v122, v115\n\t"
  "v_fmac_f32 v123, v115, v115\n\t"
  "v_mfma_f32_16x16x32_bf16 v[32:35], a[24:27], v[88:91], v[32:35]\n\t"
  "ds_read_b64_tr_b16 v[82:83], v109 offset:18432\n\t"
  "v_add_f32 v122, v122, v116\n\t"
  "v_fmac_f32 v123, v116, v116\n\t"
  "v_add_f32 v122, v122, v117\n\t"
  "v_mfma_f32_16x16x32_bf16 v[36:39], a[28:31], v[88:91], v[36:39]\n\t"
  "v_fmac_f32 v123, v117, v117\n\t"
  "v_add_f32 v122, v122, v118\n\t"
  "v_fmac_f32 v123, v118, v118\n\t"
  "v_add_f32 v122, v122, v119\n\t"
  "v_mfma_f32_16x16x32_bf16 v[40:43], a[32:35], v[88:91], v[40:43]\n\t"
  "s_waitcnt lgkmcnt(4)\n\t"
  "v_fmac_f32 v123, v119, v119\n\t"
  "v_add_f32 v122, v122, v120\n\t"
  "v_fmac_f32 v123, v120, v120\n\t"
  "v_mfma_f32_16x16x32_bf16 v[20:23], a[36:39], v[68:71], v[20:23]\n\t"
  "ds_read_b64_tr_b16 v[84:85], v108 offset:20480\n\t"
  "v_add_f32 v122, v122, v121\n\t"
  "v_fmac_f32 v123, v121, v121\n\t"
  "buffer_load_dwordx4 v4, %[srd], s46 offen offset:2048 sc1 lds\n\t"
  "v_mfma_f32_16x16x32_bf16 v[24:27], a[40:43], v[68:71], v[24:27]\n\t"
  "ds_read_b64_tr_b16 v[86:87], v108 offset:22528\n\t"
  "v_add_f32_dpp v122, v122, v122 quad_perm:[1,0,3,2] row_mask:0xf bank_mask:0xf\n\t"
  "v_add_f32_dpp v123, v123, v123 quad_perm:[1,0,3,2] row_mask:0xf bank_mask:0xf\n\t"
  "buffer_load_dwordx4 v4, %[srd], s46 offen offset:3072 sc1 lds\n\t"
  "v_mfma_f32_16x16x32_bf16 v[28:31], a[44:47], v[68:71], v[28:31]\n\t"
  "ds_read_b64_tr_b16 v[88:89], v109 offset:20480\n\t"
  "v_add_f32_dpp v122, v122, v122 quad_perm:[2,3,0,1] row_mask:0xf bank_mask:0xf\n\t"
  "v_add_f32_dpp v123, v123, v123 quad_perm:[2,3,0,1] row_mask:0xf bank_mask:0xf\n\t"
  "s_add_u32 m0, s52, 98304\n\t"
  "v_mfma_f32_16x16x32_bf16 v[32:35], a[36:39], v[72:75], v[32:35]\n\t"
  "ds_read_b64_tr_b16 v[90:91], v109 offset:22528\n\t"
  "v_add_f32_dpp v122, v122, v122 row_half_mirror row_mask:0xf bank_mask:0xf\n\t"
  "v_add_f32_dpp v123, v123, v123 row_half_mirror row_mask:0xf bank_mask:0xf\n\t"
  "s_mov_b32 s46, 1327104\n\t"
  "v_mfma_f32_16x16x32_bf16 v[36:39], a[40:43], v[72:75], v[36:39]\n\t"
  "v_add_f32_dpp v122, v122, v122 row_mirror row_mask:0xf bank_mask:0xf\n\t"
  "v_add_f32_dpp v123, v123, v123 row_mirror row_mask:0xf bank_mask:0xf\n\t"
  "buffer_load_dwordx4 v4, %[srd], s46 offen offset:0 sc1 lds\n\t"
  "v_add_f32_dpp v122, v122, v122 row_bcast:15 row_mask:0xa bank_mask:0xf\n\t"
  "v_mfma_f32_16x16x32_bf16 v[40:43], a[44:47], v[72:75], v[40:43]\n\t"
  "s_waitcnt lgkmcnt(4)\n\t"
  "v_add_f32_dpp v123, v123, v123 row_bcast:15 row_mask:0xa bank_mask:0xf\n\t"
  "v_add_f32_dpp v122, v122, v122 row_bcast:31 row_mask:0xc bank_mask:0xf\n\t"
  "buffer_load_dwordx4 v4, %[srd], s46 offen offset:1024 sc1 lds\n\t"
  "v_mfma_f32_16x16x32_bf16 v[20:23], a[48:51], v[76:79], v[20:23]\n\t"
  "ds_read_b64_tr_b16 v[68:69], v108 offset:24576\n\t"
  "v_add_f32_dpp v123, v123, v123 row_bcast:31 row_mask:0xc bank_mask:0xf\n\t"
  "v_mul_f32 v122, 0xbacccccd, v122\n\t"
  "v_mul_f32 v123, 0x3acccccd, v123\n\t"
  "v_mfma_f32_16x16x32_bf16 v[24:27], a[52:55], v[76:79], v[24:27]\n\t"
  "ds_read_b64_tr_b16 v[70:71], v108 offset:26624\n\t"
  "v_fma_f32 v123, -v122, v122, v123\n\t"
  "v_max_f32 v123, 0, v123\n\t"
  "v_cvt_pk_bf16_f32 v123, 0, v123\n\t"
  "v_mfma_f32_16x16x32_bf16 v[28:31], a[56:59], v[76:79], v[28:31]\n\t"
  "ds_read_b64_tr_b16 v[72:73], v109 offset:24576\n\t"
  "v_cvt_pk_bf16_f32 v126, v122, v122\n\t"
  "v_add_f32 v123, 0x3727c5ac, v123\n\t"
  "v_cvt_pk_bf16_f32 v123, 0, v123\n\t"
  "v_mfma_f32_16x16x32_bf16 v[32:35], a[48:51], v[80:83], v[32:35]\n\t"
  "ds_read_b64_tr_b16 v[74:75], v109 offset:26624\n\t"
  "v_sqrt_f32 v123, v123\n\t"
  "buffer_load_dwordx4 v4, %[srd], s46 offen offset:2048 sc1 lds\n\t"
  "v_cvt_pk_bf16_f32 v127, v123, v123\n\t"
  "v_mfma_f32_16x16x32_bf16 v[36:39], a[52:55], v[80:83], v[36:39]\n\t"
  "buffer_store_dwordx2 v[126:127], v7, %[srd], %[sST] offen sc1\n\t"
  "buffer_load_dwordx4 v4, %[srd], s46 offen offset:3072 sc1 lds\n\t"
  "s_add_u32 m0, s52, 114688\n\t"
  "v_add_u32 v124, 122880, v6\n\t"
  "v_mfma_f32_16x16x32_bf16 v[40:43], a[56:59], v[80:83], v[40:43]\n\t"
  "s_waitcnt lgkmcnt(4)\n\t"
  "v_add_u32 v125, 81920, v6\n\t"
  "v_mfma_f32_16x16x32_bf16 v[20:23], a[60:63], v[84:87], v[20:23]\n\t"
  "ds_read_b64_tr_b16 v[76:77], v108 offset:28672\n\t"
  "v_mov_b32 v112, 0\n\t"
  "v_mfma_f32_16x16x32_bf16 v[24:27], a[64:67], v[84:87], v[24:27]\n\t"
  "ds_read_b64_tr_b16 v[78:79], v108 offset:30720\n\t"
  "s_mov_b32 s46, 1343488\n\t"
  "v_mfma_f32_16x16x32_bf16 v[28:31], a[68:71], v[84:87], v[28:31]\n\t"
  "ds_read_b64_tr_b16 v[80:81], v109 offset:28672\n\t"
  "v_mov_b32 v113, 0\n\t"
  "v_mfma_f32_16x16x32_bf16 v[32:35], a[60:63], v[88:91], v[32:35]\n\t"
  "ds_read_b64_tr_b16 v[82:83], v109 offset:30720\n\t"
  "buffer_load_dwordx4 v4, %[srd], s46 offen offset:0 sc1 lds\n\t"
  "v_mfma_f32_16x16x32_bf16 v[36:39], a[64:67], v[88:91], v[36:39]\n\t"
  "v_mov_b32 v114, 0\n\t"
  "v_mov_b32 v115, 0\n\t"
  "v_mfma_f32_16x16x32_bf16 v[40:43], a[68:71], v[88:91], v[40:43]\n\t"
  "s_waitcnt lgkmcnt(4)\n\t"
  "v_mov_b32 v116, 0\n\t"
  "v_mfma_f32_16x16x32_bf16 v[20:23], a[72:75], v[68:71], v[20:23]\n\t"
  "ds_read_b64_tr_b16 v[84:85], v108 offset:32768\n\t"
  "buffer_load_dwordx4 v4, %[srd], s46 offen offset:1024 sc1 lds\n\t"
  "v_mfma_f32_16x16x32_bf16 v[24:27], a[76:79], v[68:71], v[24:27]\n\t"
  "ds_read_b64_tr_b16 v[86:87], v108 offset:34816\n\t"
  "v_mov_b32 v117, 0\n\t"
  "v_mfma_f32_16x16x32_bf16 v[28:31], a[80:83], v[68:71], v[28:31]\n\t"
  "ds_read_b64_tr_b16 v[88:89], v109 offset:32768\n\t"
  "buffer_load_dwordx4 v4, %[srd], s46 offen offset:2048 sc1 lds\n\t"
  "v_mfma_f32_16x16x32_bf16 v[32:35], a[72:75], v[72:75], v[32:35]\n\t"
  "ds_read_b64_tr_b16 v[90:91], v109 offset:34816\n\t"
  "v_mov_b32 v118, 0\n\t"
  "v_mfma_f32_16x16x32_bf16 v[36:39], a[76:79], v[72:75], v[36:39]\n\t"
  "buffer_load_dwordx4 v4, %[srd], s46 offen offset:3072 sc1 lds\n\t"
  "v_mov_b32 v119, 0\n\t"
  "v_mfma_f32_16x16x32_bf16 v[40:43], a[80:83], v[72:75], v[40:43]\n\t"
  "s_waitcnt lgkmcnt(4)\n\t"
  "v_mov_b32 v120, 0\n\t"
  "v_mfma_f32_16x16x32_bf16 v[20:23], a[84:87], v[76:79], v[20:23]\n\t"
  "ds_read_b64_tr_b16 v[68:69], v108 offset:36864\n\t"
  "v_mov_b32 v121, 0\n\t"
  "v_mfma_f32_16x16x32_bf16 v[24:27], a[88:91], v[76:79], v[24:27]\n\t"
  "ds_read_b64_tr_b16 v[70:71], v108 offset:38912\n\t"
  "s_add_u32 m0, s52, 131072\n\t"
  "v_mfma_f32_16x16x32_bf16 v[28:31], a[92:95], v[76:79], v[28:31]\n\t"
  "ds_read_b64_tr_b16 v[72:73], v109 offset:36864\n\t"
  "v_mfma_f32_16x16x32_bf16 v[32:35], a[84:87], v[80:83], v[32:35]\n\t"
  "ds_read_b64_tr_b16 v[74:75], v109 offset:38912\n\t"
  "s_mov_b32 s46, 1359872\n\t"
  "v_mfma_f32_16x16x32_bf16 v[36:39], a[88:91], v[80:83], v[36:39]\n\t"
  "buffer_load_dwordx2 v[138:139], v2, %[srd], 0 offen sc1\n\t"
  "v_mfma_f32_16x16x32_bf16 v[40:43], a[92:95], v[80:83], v[40:43]\n\t"
  "s_waitcnt lgkmcnt(4)\n\t"
  "v_mfma_f32_16x16x32_bf16 v[20:23], a[96:99], v[84:87], v[20:23]\n\t"
  "ds_read_b64_tr_b16 v[76:77], v110 offset:0\n\t"
  "buffer_load_dwordx4 v4, %[srd], s46 offen offset:0 sc1 lds\n\t"
  "v_mfma_f32_16x16x32_bf16 v[24:27], a[100:103], v[84:87], v[24:27]\n\t"
  "ds_read_b64_tr_b16 v[78:79], v110 offset:2048\n\t"
  "v_mfma_f32_16x16x32_bf16 v[28:31], a[104:107], v[84:87], v[28:31]\n\t"
  "ds_read_b64_tr_b16 v[80:81], v111 offset:0\n\t"
  "buffer_load_dwordx4 v4, %[srd], s46 offen offset:1024 sc1 lds\n\t"
  "v_mfma_f32_16x16x32_bf16 v[32:35], a[96:99], v[88:91], v[32:35]\n\t"
  "ds_read_b64_tr_b16 v[82:83], v111 offset:2048\n\t"
  "v_mfma_f32_16x16x32_bf16 v[36:39], a[100:103], v[88:91], v[36:39]\n\t"
  "buffer_load_dwordx4 v4, %[srd], s46 offen offset:2048 sc1 lds\n\t"
  "v_mfma_f32_16x16x32_bf16 v[40:43], a[104:107], v[88:91], v[40:43]\n\t"
  "s_waitcnt lgkmcnt(4)\n\t"
  "v_mfma_f32_16x16x32_bf16 v[20:23], a[108:111], v[68:71], v[20:23]\n\t"
  "ds_read_b64_tr_b16 v[84:85], v110 offset:4096\n\t"
  "v_mfma_f32_16x16x32_bf16 v[24:27], a[112:115], v[68:71], v[24:27]\n\t"
  "ds_read_b64_tr_b16 v[86:87], v110 offset:6144\n\t"
  "buffer_load_dwordx4 v4, %[srd], s46 offen offset:3072 sc1 lds\n\t"
  "v_mfma_f32_16x16x32_bf16 v[28:31], a[116:119], v[68:71], v[28:31]\n\t"
  "ds_read_b64_tr_b16 v[88:89], v111 offset:4096\n\t"
  "v_mfma_f32_16x16x32_bf16 v[32:35], a[108:111], v[72:75], v[32:35]\n\t"
  "ds_read_b64_tr_b16 v[90:91], v111 offset:6144\n\t"
  "s_add_u32 m0, s52, 147456\n\t"
  "v_mfma_f32_16x16x32_bf16 v[36:39], a[112:115], v[72:75], v[36:39]\n\t"
  "v_mfma_f32_16x16x32_bf16 v[40:43], a[116:119], v[72:75], v[40:43]\n\t"
  "s_waitcnt lgkmcnt(4)\n\t"
  "v_mfma_f32_16x16x32_bf16 v[20:23], a[120:123], v[76:79], v[20:23]\n\t"
  "ds_read_b64_tr_b16 v[68:69], v110 offset:8192\n\t"
  "v_xor_b32 v108, 64, v5\n\t"
  "v_mfma_f32_16x16x32_bf16 v[24:27], a[124:127], v[76:79], v[24:27]\n\t"
  "ds_read_b64_tr_b16 v[70:71], v110 offset:10240\n\t"
  "v_add_u32 v108, 0, v108\n\t"
  "v_mfma_f32_16x16x32_bf16 v[28:31], a[128:131], v[76:79], v[28:31]\n\t"
  "ds_read_b64_tr_b16 v[72:73], v111 offset:8192\n\t"
  "v_xor_b32 v109, 96, v5\n\t"
  "v_mfma_f32_16x16x32_bf16 v[32:35], a[120:123], v[80:83], v[32:35]\n\t"
  "ds_read_b64_tr_b16 v[74:75], v111 offset:10240\n\t"
  "v_add_u32 v109, 0, v109\n\t"
  "v_mfma_f32_16x16x32_bf16 v[36:39], a[124:127], v[80:83], v[36:39]\n\t"
  "s_mov_b32 s46, 1376256\n\t"
  "buffer_load_dwordx4 v4, %[srd], s46 offen offset:0 sc1 lds\n\t"
  "v_mfma_f32_16x16x32_bf16 v[40:43], a[128:131], v[80:83], v[40:43]\n\t"
  "s_waitcnt lgkmcnt(4)\n\t"
  "buffer_load_dwordx4 v4, %[srd], s46 offen offset:1024 sc1 lds\n\t"
  "v_mfma_f32_16x16x32_bf16 v[20:23], a[132:135], v[84:87], v[20:23]\n\t"
  "ds_read_b64_tr_b16 v[76:77], v110 offset:12288\n\t"
  "v_mfma_f32_16x16x32_bf16 v[24:27], a[136:139], v[84:87], v[24:27]\n\t"
  "ds_read_b64_tr_b16 v[78:79], v110 offset:14336\n\t"
  "buffer_load_dwordx4 v4, %[srd], s46 offen offset:2048 sc1 lds\n\t"
  "v_mfma_f32_16x16x32_bf16 v[28:31], a[140:143], v[84:87], v[28:31]\n\t"
  "ds_read_b64_tr_b16 v[80:81], v111 offset:12288\n\t"
  "v_mfma_f32_16x16x32_bf16 v[32:35], a[132:135], v[88:91], v[32:35]\n\t"
  "ds_read_b64_tr_b16 v[82:83], v111 offset:14336\n\t"
  "buffer_load_dwordx4 v4, %[srd], s46 offen offset:3072 sc1 lds\n\t"
  "v_mfma_f32_16x16x32_bf16 v[36:39], a[136:139], v[88:91], v[36:39]\n\t"
  "v_mfma_f32_16x16x32_bf16 v[40:43], a[140:143], v[88:91], v[40:43]\n\t"
  "s_waitcnt lgkmcnt(4)\n\t"
  "v_mfma_f32_16x16x32_bf16 v[20:23], a[144:147], v[68:71], v[20:23]\n\t"
  "ds_read_b64_tr_b16 v[84:85], v110 offset:16384\n\t"
  "v_mfma_f32_16x16x32_bf16 v[24:27], a[148:151], v[68:71], v[24:27]\n\t"
  "ds_read_b64_tr_b16 v[86:87], v110 offset:18432\n\t"
  "v_mfma_f32_16x16x32_bf16 v[28:31], a[152:155], v[68:71], v[28:31]\n\t"
  "ds_read_b64_tr_b16 v[88:89], v111 offset:16384\n\t"
  "v_mfma_f32_16x16x32_bf16 v[32:35], a[144:147], v[72:75], v[32:35]\n\t"
  "ds_read_b64_tr_b16 v[90:91], v111 offset:18432\n\t"
  "v_mfma_f32_16x16x32_bf16 v[36:39], a[148:151], v[72:75], v[36:39]\n\t"
  "v_mfma_f32_16x16x32_bf16 v[40:43], a[152:155], v[72:75], v[40:43]\n\t"
  "s_waitcnt lgkmcnt(4)\n\t"
  "v_mfma_f32_16x16x32_bf16 v[20:23], a[156:159], v[76:79], v[20:23]\n\t"
  "ds_read_b64_tr_b16 v[68:69], v110 offset:20480\n\t"
  "v_mfma_f32_16x16x32_bf16 v[24:27], a[160:163], v[76:79], v[24:27]\n\t"
  "ds_read_b64_tr_b16 v[70:71], v110 offset:22528\n\t"
  "v_mfma_f32_16x16x32_bf16 v[28:31], a[164:167], v[76:79], v[28:31]\n\t"
  "ds_read_b64_tr_b16 v[72:73], v111 offset:20480\n\t"
  "v_mfma_f32_16x16x32_bf16 v[32:35], a[156:159], v[80:83], v[32:35]\n\t"
  "ds_read_b64_tr_b16 v[74:75], v111 offset:22528\n\t"
  "v_mfma_f32_16x16x32_bf16 v[36:39], a[160:163], v[80:83], v[36:39]\n\t"
  "v_mfma_f32_16x16x32_bf16 v[40:43], a[164:167], v[80:83], v[40:43]\n\t"
  "s_waitcnt lgkmcnt(4)\n\t"
  "s_waitcnt vmcnt(39)\n\t"
  "v_mfma_f32_16x16x32_bf16 v[20:23], a[168:171], v[84:87], v[20:23]\n\t"
  "ds_read_b64_tr_b16 v[76:77], v110 offset:24576\n\t"
  "s_waitcnt vmcnt(38)\n\t"
  "v_mfma_f32_16x16x32_bf16 v[24:27], a[172:175], v[84:87], v[24:27]\n\t"
  "ds_read_b64_tr_b16 v[78:79], v110 offset:26624\n\t"
  "s_waitcnt vmcnt(37)\n\t"
  "v_mfma_f32_16x16x32_bf16 v[28:31], a[176:179], v[84:87], v[28:31]\n\t"
  "ds_read_b64_tr_b16 v[80:81], v111 offset:24576\n\t"
  "v_mfma_f32_16x16x32_bf16 v[32:35], a[168:171], v[88:91], v[32:35]\n\t"
  "ds_read_b64_tr_b16 v[82:83], v111 offset:26624\n\t"
  "v_mfma_f32_16x16x32_bf16 v[36:39], a[172:175], v[88:91], v[36:39]\n\t"
  "v_mfma_f32_16x16x32_bf16 v[40:43], a[176:179], v[88:91], v[40:43]\n\t"
  "s_waitcnt lgkmcnt(4)\n\t"
  "s_waitcnt vmcnt(36)\n\t"
  "v_mfma_f32_16x16x32_bf16 v[20:23], a[180:183], v[68:71], v[20:23]\n\t"
  "ds_read_b64_tr_b16 v[84:85], v110 offset:28672\n\t"
  "s_waitcnt vmcnt(35)\n\t"
  "v_mfma_f32_16x16x32_bf16 v[24:27], a[184:187], v[68:71], v[24:27]\n\t"
  "ds_read_b64_tr_b16 v[86:87], v110 offset:30720\n\t"
  "s_waitcnt vmcnt(34)\n\t"
  "v_mfma_f32_16x16x32_bf16 v[28:31], a[188:191], v[68:71], v[28:31]\n\t"
  "ds_read_b64_tr_b16 v[88:89], v111 offset:28672\n\t"
  "v_mfma_f32_16x16x32_bf16 v[32:35], a[180:183], v[72:75], v[32:35]\n\t"
  "ds_read_b64_tr_b16 v[90:91], v111 offset:30720\n\t"
  "v_mfma_f32_16x16x32_bf16 v[36:39], a[184:187], v[72:75], v[36:39]\n\t"
  "v_mfma_f32_16x16x32_bf16 v[40:43], a[188:191], v[72:75], v[40:43]\n\t"
  "s_waitcnt lgkmcnt(4)\n\t"
  "s_waitcnt vmcnt(33)\n\t"
  "v_mfma_f32_16x16x32_bf16 v[20:23], a[192:195], v[76:79], v[20:23]\n\t"
  "ds_read_b64_tr_b16 v[68:69], v110 offset:32768\n\t"
  "s_waitcnt vmcnt(8)\n\t"
  "v_cmp_ne_u64 vcc, v[132:133], v[138:139]\n\t"
  "v_mfma_f32_16x16x32_bf16 v[24:27], a[196:199], v[76:79], v[24:27]\n\t"
  "ds_read_b64_tr_b16 v[70:71], v110 offset:34816\n\t"
  "v_mfma_f32_16x16x32_bf16 v[28:31], a[200:203], v[76:79], v[28:31]\n\t"
  "ds_read_b64_tr_b16 v[72:73], v111 offset:32768\n\t"
  "s_cbranch_vccnz Lw2_slow%=\n\t"
  "Lw2_back%=:\n\t"
  "v_mfma_f32_16x16x32_bf16 v[32:35], a[192:195], v[80:83], v[32:35]\n\t"
  "ds_read_b64_tr_b16 v[74:75], v111 offset:34816\n\t"
  "v_mfma_f32_16x16x32_bf16 v[36:39], a[196:199], v[80:83], v[36:39]\n\t"
  "v_mfma_f32_16x16x32_bf16 v[40:43], a[200:203], v[80:83], v[40:43]\n\t"
  "s_waitcnt lgkmcnt(4)\n\t"
  "v_mfma_f32_16x16x32_bf16 v[20:23], a[204:207], v[84:87], v[20:23]\n\t"
  "ds_read_b64_tr_b16 v[76:77], v110 offset:36864\n\t"
  "v_lshrrev_b32 v138, 1, v1\n\t"
  "v_mfma_f32_16x16x32_bf16 v[24:27], a[208:211], v[84:87], v[24:27]\n\t"
  "ds_read_b64_tr_b16 v[78:79], v110 offset:38912\n\t"
  "buffer_load_dwordx2 a[240:241], v138, %[srd], %[sUB] offen offset:0 sc1\n\t"
  "v_mfma_f32_16x16x32_bf16 v[28:31], a[212:215], v[84:87], v[28:31]\n\t"
  "ds_read_b64_tr_b16 v[80:81], v111 offset:36864\n\t"
  "buffer_load_dwordx2 a[242:243], v138, %[srd], %[sUB] offen offset:512 sc1\n\t"
  "v_mfma_f32_16x16x32_bf16 v[32:35], a[204:207], v[88:91], v[32:35]\n\t"
  "ds_read_b64_tr_b16 v[82:83], v111 offset:38912\n\t"
  "buffer_load_dwordx2 a[244:245], v138, %[srd], %[sUB] offen offset:1024 sc1\n\t"
  "v_mfma_f32_16x16x32_bf16 v[36:39], a[208:211], v[88:91], v[36:39]\n\t"
  "s_add_u32 s45, %[sW2], 0\n\t"
  "buffer_load_dwordx4 v[144:147], v1, %[srd], s45 offen offset:0 sc1\n\t"
  "v_mfma_f32_16x16x32_bf16 v[40:43], a[212:215], v[88:91], v[40:43]\n\t"
  "s_waitcnt lgkmcnt(4)\n\t"
  "v_mfma_f32_16x16x32_bf16 v[20:23], a[216:219], v[68:71], v[20:23]\n\t"
  "ds_read_b64_tr_b16 v[84:85], v108 offset:0\n\t"
  "v_mfma_f32_16x16x32_bf16 v[24:27], a[220:223], v[68:71], v[24:27]\n\t"
  "ds_read_b64_tr_b16 v[86:87], v108 offset:2048\n\t"
  "v_mfma_f32_16x16x32_bf16 v[28:31], a[224:227], v[68:71], v[28:31]\n\t"
  "ds_read_b64_tr_b16 v[88:89], v109 offset:0\n\t"
  "v_mfma_f32_16x16x32_bf16 v[32:35], a[216:219], v[72:75], v[32:35]\n\t"
  "ds_read_b64_tr_b16 v[90:91], v109 offset:2048\n\t"
  "buffer_load_dwordx4 v[148:151], v1, %[srd], s45 offen offset:1024 sc1\n\t"
  "v_mfma_f32_16x16x32_bf16 v[36:39], a[220:223], v[72:75], v[36:39]\n\t"
  "v_mfma_f32_16x16x32_bf16 v[40:43], a[224:227], v[72:75], v[40:43]\n\t"
  "s_waitcnt lgkmcnt(4)\n\t"
  "v_mfma_f32_16x16x32_bf16 v[20:23], a[228:231], v[76:79], v[20:23]\n\t"
  "ds_read_b64_tr_b16 v[68:69], v108 offset:4096\n\t"
  "v_mfma_f32_16x16x32_bf16 v[24:27], a[232:235], v[76:79], v[24:27]\n\t"
  "ds_read_b64_tr_b16 v[70:71], v108 offset:6144\n\t"
  "buffer_load_dwordx4 v[152:155], v1, %[srd], s45 offen offset:2048 sc1\n\t"
  "v_mfma_f32_16x16x32_bf16 v[28:31], a[236:239], v[76:79], v[28:31]\n\t"
  "ds_read_b64_tr_b16 v[72:73], v109 offset:4096\n\t"
  "v_mfma_f32_16x16x32_bf16 v[32:35], a[228:231], v[80:83], v[32:35]\n\t"
  "ds_read_b64_tr_b16 v[74:75], v109 offset:6144\n\t"
  "v_mfma_f32_16x16x32_bf16 v[36:39], a[232:235], v[80:83], v[36:39]\n\t"
  "v_mfma_f32_16x16x32_bf16 v[40:43], a[236:239], v[80:83], v[40:43]\n\t"
  "s_waitcnt lgkmcnt(4)\n\t"
  "v_mfma_f32_16x16x32_bf16 v[44:47], a[0:3], v[84:87], 0\n\t"
  "ds_read_b64_tr_b16 v[76:77], v108 offset:8192\n\t"
  "v_xor_b32 v110, 64, v5\n\t"
  "v_mfma_f32_16x16x32_bf16 v[48:51], a[4:7], v[84:87], 0\n\t"
  "ds_read_b64_tr_b16 v[78:79], v108 offset:10240\n\t"
  "v_add_u32 v110, 40960, v110\n\t"
  "v_mfma_f32_16x16x32_bf16 v[52:55], a[8:11], v[84:87], 0\n\t"
  "ds_read_b64_tr_b16 v[80:81], v109 offset:8192\n\t"
  "v_xor_b32 v111, 96, v5\n\t"
  "v_mfma_f32_16x16x32_bf16 v[56:59], a[0:3], v[88:91], 0\n\t"
  "ds_read_b64_tr_b16 v[82:83], v109 offset:10240\n\t"
  "v_add_u32 v111, 40960, v111\n\t"
  "v_mfma_f32_16x16x32_bf16 v[60:63], a[4:7], v[88:91], 0\n\t"
  "buffer_load_dwordx4 v[156:159], v1, %[srd], s45 offen offset:3072 sc1\n\t"
  "s_add_u32 s45, %[sW2], 4096\n\t"
  "v_mfma_f32_16x16x32_bf16 v[64:67], a[8:11], v[88:91], 0\n\t"
  "s_waitcnt lgkmcnt(4)\n\t"
  "v_mfma_f32_16x16x32_bf16 v[44:47], a[12:15], v[68:71], v[44:47]\n\t"
  "ds_read_b64_tr_b16 v[84:85], v108 offset:12288\n\t"
  "buffer_load_dwordx2 v[92:93], v136, %[srd], %[sST] offen offset:0 sc1\n\t"
  "v_mfma_f32_16x16x32_bf16 v[48:51], a[16:19], v[68:71], v[48:51]\n\t"
  "ds_read_b64_tr_b16 v[86:87], v108 offset:14336\n\t"
  "buffer_load_dwordx2 v[94:95], v136, %[srd], %[sST] offen offset:1024 sc1\n\t"
  "v_mfma_f32_16x16x32_bf16 v[52:55], a[20:23], v[68:71], v[52:55]\n\t"
  "ds_read_b64_tr_b16 v[88:89], v109 offset:12288\n\t"
  "buffer_load_dwordx2 v[96:97], v136, %[srd], %[sST] offen offset:2048 sc1\n\t"
  "v_mfma_f32_16x16x32_bf16 v[56:59], a[12:15], v[72:75], v[56:59]\n\t"
  "ds_read_b64_tr_b16 v[90:91], v109 offset:14336\n\t"
  "buffer_load_dwordx2 v[98:99], v136, %[srd], %[sST] offen offset:3072 sc1\n\t"
  "v_mfma_f32_16x16x32_bf16 v[60:63], a[16:19], v[72:75], v[60:63]\n\t"
  "buffer_load_dwordx4 v[160:163], v1, %[srd], s45 offen offset:0 sc1\n\t"
  "v_mfma_f32_16x16x32_bf16 v[64:67], a[20:23], v[72:75], v[64:67]\n\t"
  "s_waitcnt lgkmcnt(4)\n\t"
  "v_mfma_f32_16x16x32_bf16 v[44:47], a[24:27], v[76:79], v[44:47]\n\t"
  "ds_read_b64_tr_b16 v[68:69], v108 offset:16384\n\t"
  "v_mfma_f32_16x16x32_bf16 v[48:51], a[28:31], v[76:79], v[48:51]\n\t"
  "ds_read_b64_tr_b16 v[70:71], v108 offset:18432\n\t"
  "buffer_load_dwordx4 v[164:167], v1, %[srd], s45 offen offset:1024 sc1\n\t"
  "v_mfma_f32_16x16x32_bf16 v[52:55], a[32:35], v[76:79], v[52:55]\n\t"
  "ds_read_b64_tr_b16 v[72:73], v109 offset:16384\n\t"
  "s_waitcnt vmcnt(13)\n\t"
  "s_barrier\n\t"
  "v_mfma_f32_16x16x32_bf16 v[56:59], a[24:27], v[80:83], v[56:59]\n\t"
  "ds_read_b64_tr_b16 v[74:75], v109 offset:18432\n\t"
  "ds_read_u16_d16_hi v112, v125 offset:0\n\t"
  "v_mfma_f32_16x16x32_bf16 v[60:63], a[28:31], v[80:83], v[60:63]\n\t"
  "ds_read_u16_d16_hi v113, v125 offset:8192\n\t"
  "ds_read_b64_tr_b16 a[246:247], v10 offset:0\n\t"
  "v_mfma_f32_16x16x32_bf16 v[64:67], a[32:35], v[80:83], v[64:67]\n\t"
  "s_waitcnt lgkmcnt(7)\n\t"
  "ds_read_u16_d16_hi v114, v125 offset:16384\n\t"
  "v_mfma_f32_16x16x32_bf16 v[44:47], a[36:39], v[84:87], v[44:47]\n\t"
  "ds_read_b64_tr_b16 v[76:77], v108 offset:20480\n\t"
  "buffer_load_dwordx4 v[168:171], v1, %[srd], s45 offen offset:2048 sc1\n\t"
  "v_mfma_f32_16x16x32_bf16 v[48:51], a[40:43], v[84:87], v[48:51]\n\t"
  "ds_read_b64_tr_b16 v[78:79], v108 offset:22528\n\t"
  "ds_read_u16_d16_hi v115, v125 offset:24576\n\t"
  "v_mfma_f32_16x16x32_bf16 v[52:55], a[44:47], v[84:87], v[52:55]\n\t"
  "ds_read_b64_tr_b16 v[80:81], v109 offset:20480\n\t"
  "ds_read_u16_d16_hi v116, v125 offset:32768\n\t"
  "v_mfma_f32_16x16x32_bf16 v[56:59], a[36:39], v[88:91], v[56:59]\n\t"
  "ds_read_b64_tr_b16 v[82:83], v109 offset:22528\n\t"
  "ds_read_u16_d16_hi v117, v124 offset:0\n\t"
  "v_mfma_f32_16x16x32_bf16 v[60:63], a[40:43], v[88:91], v[60:63]\n\t"
  "buffer_load_dwordx4 v[172:175], v1, %[srd], s45 offen offset:3072 sc1\n\t"
  "ds_read_u16_d16_hi v118, v124 offset:8192\n\t"
  "v_mfma_f32_16x16x32_bf16 v[64:67], a[44:47], v[88:91], v[64:67]\n\t"
  "s_waitcnt lgkmcnt(12)\n\t"
  "ds_read_u16_d16_hi v119, v124 offset:16384\n\t"
  "v_mfma_f32_16x16x32_bf16 v[44:47], a[48:51], v[68:71], v[44:47]\n\t"
  "ds_read_b64_tr_b16 v[84:85], v108 offset:24576\n\t"
  "ds_read_u16_d16_hi v120, v124 offset:24576\n\t"
  "v_mfma_f32_16x16x32_bf16 v[48:51], a[52:55], v[68:71], v[48:51]\n\t"
  "ds_read_b64_tr_b16 v[86:87], v108 offset:26624\n\t"
  "ds_read_u16_d16_hi v121, v124 offset:32768\n\t"
  "v_mfma_f32_16x16x32_bf16 v[52:55], a[56:59], v[68:71], v[52:55]\n\t"
  "ds_read_b64_tr_b16 v[88:89], v109 offset:24576\n\t"
  "ds_read_b64_tr_b16 a[248:249], v11 offset:0\n\t"
  "v_mfma_f32_16x16x32_bf16 v[56:59], a[48:51], v[72:75], v[56:59]\n\t"
  "ds_read_b64_tr_b16 v[90:91], v109 offset:26624\n\t"
  "s_add_u32 s45, %[sW2], 8192\n\t"
  "v_mfma_f32_16x16x32_bf16 v[60:63], a[52:55], v[72:75], v[60:63]\n\t"
  "ds_read_b64_tr_b16 a[250:251], v10 offset:4096\n\t"
  "ds_read_b64_tr_b16 a[252:253], v11 offset:4096\n\t"
  "v_mfma_f32_16x16x32_bf16 v[64:67], a[56:59], v[72:75], v[64:67]\n\t"
  "s_waitcnt lgkmcnt(12)\n\t"
  "ds_read_b64_tr_b16 a[254:255], v12 offset:0\n\t"
  "v_mfma_f32_16x16x32_bf16 v[44:47], a[60:63], v[76:79], v[44:47]\n\t"
  "ds_read_b64_tr_b16 v[68:69], v108 offset:28672\n\t"
  "v_mfma_f32_16x16x32_bf16 v[48:51], a[64:67], v[76:79], v[48:51]\n\t"
  "ds_read_b64_tr_b16 v[70:71], v108 offset:30720\n\t"
  "v_mfma_f32_16x16x32_bf16 v[52:55], a[68:71], v[76:79], v[52:55]\n\t"
  "ds_read_b64_tr_b16 v[72:73], v109 offset:28672\n\t"
  "buffer_load_dwordx4 v[176:179], v1, %[srd], s45 offen offset:0 sc1\n\t"
  "v_mfma_f32_16x16x32_bf16 v[56:59], a[60:63], v[80:83], v[56:59]\n\t"
  "ds_read_b64_tr_b16 v[74:75], v109 offset:30720\n\t"
  "v_mfma_f32_16x16x32_bf16 v[60:63], a[64:67], v[80:83], v[60:63]\n\t"
  "v_mfma_f32_16x16x32_bf16 v[64:67], a[68:71], v[80:83], v[64:67]\n\t"
  "s_waitcnt lgkmcnt(7)\n\t"
  "v_mul_f32 v112, v112, v142\n\t"
  "v_mfma_f32_16x16x32_bf16 v[44:47], a[72:75], v[84:87], v[44:47]\n\t"
  "ds_read_b64_tr_b16 v[76:77], v108 offset:32768\n\t"
  "buffer_load_dwordx4 v[180:183], v1, %[srd], s45 offen offset:1024 sc1\n\t"
  "v_mfma_f32_16x16x32_bf16 v[48:51], a[76:79], v[84:87], v[48:51]\n\t"
  "ds_read_b64_tr_b16 v[78:79], v108 offset:34816\n\t"
  "v_mul_f32 v113, v113, v143\n\t"
  "v_mfma_f32_16x16x32_bf16 v[52:55], a[80:83], v[84:87], v[52:55]\n\t"
  "ds_read_b64_tr_b16 v[80:81], v109 offset:32768\n\t"
  "v_mul_f32 v114, v114, v248\n\t"
  "v_mfma_f32_16x16x32_bf16 v[56:59], a[72:75], v[88:91], v[56:59]\n\t"
  "ds_read_b64_tr_b16 v[82:83], v109 offset:34816\n\t"
  "v_mul_f32 v115, v115, v249\n\t"
  "v_mfma_f32_16x16x32_bf16 v[60:63], a[76:79], v[88:91], v[60:63]\n\t"
  "v_mul_f32 v116, v116, v250\n\t"
  "v_mul_f32 v117, v117, v251\n\t"
  "v_mfma_f32_16x16x32_bf16 v[64:67], a[80:83], v[88:91], v[64:67]\n\t"
  "s_waitcnt lgkmcnt(4)\n\t"
  "buffer_load_dwordx4 v[184:187], v1, %[srd], s45 offen offset:2048 sc1\n\t"
  "v_mfma_f32_16x16x32_bf16 v[44:47], a[84:87], v[68:71], v[44:47]\n\t"
  "ds_read_b64_tr_b16 v[84:85], v108 offset:36864\n\t"
  "v_mul_f32 v118, v118, v252\n\t"
  "v_mfma_f32_16x16x32_bf16 v[48:51], a[88:91], v[68:71], v[48:51]\n\t"
  "ds_read_b64_tr_b16 v[86:87], v108 offset:38912\n\t"
  "v_mul_f32 v119, v119, v253\n\t"
  "v_mfma_f32_16x16x32_bf16 v[52:55], a[92:95], v[68:71], v[52:55]\n\t"
  "ds_read_b64_tr_b16 v[88:89], v109 offset:36864\n\t"
  "v_mul_f32 v120, v120, v254\n\t"
  "v_mfma_f32_16x16x32_bf16 v[56:59], a[84:87], v[72:75], v[56:59]\n\t"
  "ds_read_b64_tr_b16 v[90:91], v109 offset:38912\n\t"
  "buffer_load_dwordx4 v[188:191], v1, %[srd], s45 offen offset:3072 sc1\n\t"
  "v_mfma_f32_16x16x32_bf16 v[60:63], a[88:91], v[72:75], v[60:63]\n\t"
  "v_mul_f32 v121, v121, v255\n\t"
  "v_add_f32 v122, v112, v113\n\t"
  "v_mfma_f32_16x16x32_bf16 v[64:67], a[92:95], v[72:75], v[64:67]\n\t"
  "s_waitcnt lgkmcnt(4)\n\t"
  "v_mul_f32 v123, v112, v112\n\t"
  "v_mfma_f32_16x16x32_bf16 v[44:47], a[96:99], v[76:79], v[44:47]\n\t"
  "ds_read_b64_tr_b16 v[68:69], v110 offset:0\n\t"
  "v_fmac_f32 v123, v113, v113\n\t"
  "v_mfma_f32_16x16x32_bf16 v[48:51], a[100:103], v[76:79], v[48:51]\n\t"
  "ds_read_b64_tr_b16 v[70:71], v110 offset:2048\n\t"
  "v_add_f32 v122, v122, v114\n\t"
  "v_mfma_f32_16x16x32_bf16 v[52:55], a[104:107], v[76:79], v[52:55]\n\t"
  "ds_read_b64_tr_b16 v[72:73], v111 offset:0\n\t"
  "s_add_u32 s45, %[sW2], 12288\n\t"
  "v_mfma_f32_16x16x32_bf16 v[56:59], a[96:99], v[80:83], v[56:59]\n\t"
  "ds_read_b64_tr_b16 v[74:75], v111 offset:2048\n\t"
  "v_fmac_f32 v123, v114, v114\n\t"
  "v_mfma_f32_16x16x32_bf16 v[60:63], a[100:103], v[80:83], v[60:63]\n\t"
  "v_add_f32 v122, v122, v115\n\t"
  "v_fmac_f32 v123, v115, v115\n\t"
  "v_mfma_f32_16x16x32_bf16 v[64:67], a[104:107], v[80:83], v[64:67]\n\t"
  "s_waitcnt lgkmcnt(4)\n\t"
  "v_add_f32 v122, v122, v116\n\t"
  "v_mfma_f32_16x16x32_bf16 v[44:47], a[108:111], v[84:87], v[44:47]\n\t"
  "ds_read_b64_tr_b16 v[76:77], v110 offset:4096\n\t"
  "buffer_load_dwordx4 v[192:195], v1, %[srd], s45 offen offset:0 sc1\n\t"
  "v_mfma_f32_16x16x32_bf16 v[48:51], a[112:115], v[84:87], v[48:51]\n\t"
  "ds_read_b64_tr_b16 v[78:79], v110 offset:6144\n\t"
  "v_fmac_f32 v123, v116, v116\n\t"
  "v_mfma_f32_16x16x32_bf16 v[52:55], a[116:119], v[84:87], v[52:55]\n\t"
  "ds_read_b64_tr_b16 v[80:81], v111 offset:4096\n\t"
  "v_add_f32 v122, v122, v117\n\t"
  "v_mfma_f32_16x16x32_bf16 v[56:59], a[108:111], v[88:91], v[56:59]\n\t"
  "ds_read_b64_tr_b16 v[82:83], v111 offset:6144\n\t"
  "v_fmac_f32 v123, v117, v117\n\t"
  "v_mfma_f32_16x16x32_bf16 v[60:63], a[112:115], v[88:91], v[60:63]\n\t"
  "v_add_f32 v122, v122, v118\n\t"
  "v_fmac_f32 v123, v118, v118\n\t"
  "v_mfma_f32_16x16x32_bf16 v[64:67], a[116:119], v[88:91], v[64:67]\n\t"
  "s_waitcnt lgkmcnt(4)\n\t"
  "s_waitcnt vmcnt(11)\n\t"
  "v_min_u32 v131, v93, v95\n\t"
  "v_mfma_f32_16x16x32_bf16 v[44:47], a[120:123], v[68:71], v[44:47]\n\t"
  "ds_read_b64_tr_b16 v[84:85], v110 offset:8192\n\t"
  "s_waitcnt vmcnt(9)\n\t"
  "v_min3_u32 v131, v131, v97, v99\n\t"
  "v_mfma_f32_16x16x32_bf16 v[48:51], a[124:127], v[68:71], v[48:51]\n\t"
  "ds_read_b64_tr_b16 v[86:87], v110 offset:10240\n\t"
  "v_cmp_eq_u32 vcc, 0, v131\n\t"
  "v_mfma_f32_16x16x32_bf16 v[52:55], a[128:131], v[68:71], v[52:55]\n\t"
  "ds_read_b64_tr_b16 v[88:89], v111 offset:8192\n\t"
  "v_add_u32 v108, 81920, v5\n\t"
  "v_mfma_f32_16x16x32_bf16 v[56:59], a[120:123], v[72:75], v[56:59]\n\t"
  "ds_read_b64_tr_b16 v[90:91], v111 offset:10240\n\t"
  "s_cbranch_vccnz Lst0_slow%=\n\t"
  "Lst0_back%=:\n\t"
  "v_mfma_f32_16x16x32_bf16 v[60:63], a[124:127], v[72:75], v[60:63]\n\t"
  "v_xor_b32 v109, 32, v5\n\t"
  "v_add_u32 v109, 81920, v109\n\t"
  "v_mfma_f32_16x16x32_bf16 v[64:67], a[128:131], v[72:75], v[64:67]\n\t"
  "s_waitcnt lgkmcnt(4)\n\t"
  "buffer_load_dwordx4 v[196:199], v1, %[srd], s45 offen offset:1024 sc1\n\t"
  "v_mfma_f32_16x16x32_bf16 v[44:47], a[132:135], v[76:79], v[44:47]\n\t"
  "ds_read_b64_tr_b16 v[68:69], v110 offset:12288\n\t"
  "buffer_load_dwordx4 v[200:203], v1, %[srd], s45 offen offset:2048 sc1\n\t"
  "v_mfma_f32_16x16x32_bf16 v[48:51], a[136:139], v[76:79], v[48:51]\n\t"
  "ds_read_b64_tr_b16 v[70:71], v110 offset:14336\n\t"
  "v_add_f32 v122, v122, v119\n\t"
  "v_mfma_f32_16x16x32_bf16 v[52:55], a[140:143], v[76:79], v[52:55]\n\t"
  "ds_read_b64_tr_b16 v[72:73], v111 offset:12288\n\t"
  "buffer_load_dwordx4 v[204:207], v1, %[srd], s45 offen offset:3072 sc1\n\t"
  "v_mfma_f32_16x16x32_bf16 v[56:59], a[132:135], v[80:83], v[56:59]\n\t"
  "ds_read_b64_tr_b16 v[74:75], v111 offset:14336\n\t"
  "v_fmac_f32 v123, v119, v119\n\t"
  "v_mfma_f32_16x16x32_bf16 v[60:63], a[136:139], v[80:83], v[60:63]\n\t"
  "v_add_f32 v122, v122, v120\n\t"
  "v_fmac_f32 v123, v120, v120\n\t"
  "v_mfma_f32_16x16x32_bf16 v[64:67], a[140:143], v[80:83], v[64:67]\n\t"
  "v_add_f32 v122, v122, v121\n\t"
  "v_fmac_f32 v123, v121, v121\n\t"
  "v_mfma_f32_16x16x16_bf16 v[20:23], a[240:241], v[92:93], v[20:23]\n\t"
  "s_add_u32 s45, %[sW2], 16384\n\t"
  "v_add_f32_dpp v122, v122, v122 quad_perm:[1,0,3,2] row_mask:0xf bank_mask:0xf\n\t"
  "v_mfma_f32_16x16x16_bf16 v[24:27], a[242:243], v[92:93], v[24:27]\n\t"
  "v_add_f32_dpp v123, v123, v123 quad_perm:[1,0,3,2] row_mask:0xf bank_mask:0xf\n\t"
  "v_add_f32_dpp v122, v122, v122 quad_perm:[2,3,0,1] row_mask:0xf bank_mask:0xf\n\t"
  "v_mfma_f32_16x16x16_bf16 v[28:31], a[244:245], v[92:93], v[28:31]\n\t"
  "v_add_f32_dpp v123, v123, v123 quad_perm:[2,3,0,1] row_mask:0xf bank_mask:0xf\n\t"
  "v_add_f32_dpp v122, v122, v122 row_half_mirror row_mask:0xf bank_mask:0xf\n\t"
  "v_mfma_f32_16x16x16_bf16 v[32:35], a[240:241], v[94:95], v[32:35]\n\t"
  "v_add_f32_dpp v123, v123, v123 row_half_mirror row_mask:0xf bank_mask:0xf\n\t"
  "v_add_f32_dpp v122, v122, v122 row_mirror row_mask:0xf bank_mask:0xf\n\t"
  "v_mfma_f32_16x16x16_bf16 v[36:39], a[242:243], v[94:95], v[36:39]\n\t"
  "v_add_f32_dpp v123, v123, v123 row_mirror row_mask:0xf bank_mask:0xf\n\t"
  "v_add_f32_dpp v122, v122, v122 row_bcast:15 row_mask:0xa bank_mask:0xf\n\t"
  "v_mfma_f32_16x16x16_bf16 v[40:43], a[244:245], v[94:95], v[40:43]\n\t"
  "s_waitcnt lgkmcnt(4)\n\t"
  "buffer_load_dwordx4 v[208:211], v1, %[srd], s45 offen offset:0 sc1\n\t"
  "v_mfma_f32_16x16x32_bf16 v[44:47], a[144:147], v[84:87], v[44:47]\n\t"
  "ds_read_b64_tr_b16 v[76:77], v110 offset:16384\n\t"
  "v_add_f32_dpp v123, v123, v123 row_bcast:15 row_mask:0xa bank_mask:0xf\n\t"
  "v_mfma_f32_16x16x32_bf16 v[48:51], a[148:151], v[84:87], v[48:51]\n\t"
  "ds_read_b64_tr_b16 v[78:79], v110 offset:18432\n\t"
  "v_add_f32_dpp v122, v122, v122 row_bcast:31 row_mask:0xc bank_mask:0xf\n\t"
  "v_mfma_f32_16x16x32_bf16 v[52:55], a[152:155], v[84:87], v[52:55]\n\t"
  "ds_read_b64_tr_b16 v[80:81], v111 offset:16384\n\t"
  "v_add_f32_dpp v123, v123, v123 row_bcast:31 row_mask:0xc bank_mask:0xf\n\t"
  "v_mfma_f32_16x16x32_bf16 v[56:59], a[144:147], v[88:91], v[56:59]\n\t"
  "ds_read_b64_tr_b16 v[82:83], v111 offset:18432\n\t"
  "v_mul_f32 v122, 0xbacccccd, v122\n\t"
  "v_mfma_f32_16x16x32_bf16 v[60:63], a[148:151], v[88:91], v[60:63]\n\t"
  "buffer_load_dwordx4 v[212:215], v1, %[srd], s45 offen offset:1024 sc1\n\t"
  "v_mul_f32 v123, 0x3acccccd, v123\n\t"
  "v_mfma_f32_16x16x32_bf16 v[64:67], a[152:155], v[88:91], v[64:67]\n\t"
  "s_waitcnt lgkmcnt(4)\n\t"
  "v_fma_f32 v123, -v122, v122, v123\n\t"
  "v_max_f32 v123, 0, v123\n\t"
  "v_cvt_pk_bf16_f32 v123, 0, v123\n\t"
  "v_cvt_pk_bf16_f32 v126, v122, v122\n\t"
  "v_add_f32 v123, 0x3727c5ac, v123\n\t"
  "v_cvt_pk_bf16_f32 v123, 0, v123\n\t"
  "v_sqrt_f32 v123, v123\n\t"
  "s_nop 0\n\t"
  "v_cvt_pk_bf16_f32 v127, v123, v123\n\t"
  "buffer_store_dwordx2 v[126:127], v7, %[srd], s54 offen sc1\n\t"
  "v_mfma_f32_16x16x32_bf16 v[44:47], a[156:159], v[68:71], v[44:47]\n\t"
  "ds_read_b64_tr_b16 v[84:85], v110 offset:20480\n\t"
  "v_and_b32 v128, 0xffff0000, v93\n\t"
  "v_mfma_f32_16x16x32_bf16 v[48:51], a[160:163], v[68:71], v[48:51]\n\t"
  "ds_read_b64_tr_b16 v[86:87], v110 offset:22528\n\t"
  "v_and_b32 v129, 0xffff0000, v95\n\t"
  "v_mfma_f32_16x16x32_bf16 v[52:55], a[164:167], v[68:71], v[52:55]\n\t"
  "ds_read_b64_tr_b16 v[88:89], v111 offset:20480\n\t"
  "buffer_load_dwordx4 v[216:219], v1, %[srd], s45 offen offset:2048 sc1\n\t"
  "v_mfma_f32_16x16x32_bf16 v[56:59], a[156:159], v[72:75], v[56:59]\n\t"
  "ds_read_b64_tr_b16 v[90:91], v111 offset:22528\n\t"
  "v_rcp_f32 v128, v128\n\t"
  "v_mfma_f32_16x16x32_bf16 v[60:63], a[160:163], v[72:75], v[60:63]\n\t"
  "v_rcp_f32 v129, v129\n\t"
  "v_mul_f32 v128, v128, v128\n\t"
  "v_mfma_f32_16x16x32_bf16 v[64:67], a[164:167], v[72:75], v[64:67]\n\t"
  "s_waitcnt lgkmcnt(4)\n\t"
  "v_mul_f32 v129, v129, v129\n\t"
  "v_mfma_f32_16x16x32_bf16 v[44:47], a[168:171], v[76:79], v[44:47]\n\t"
  "ds_read_b64_tr_b16 v[68:69], v110 offset:24576\n\t"
  "v_max_f32 v20, 0, v20\n\t"
  "v_mfma_f32_16x16x32_bf16 v[48:51], a[172:175], v[76:79], v[48:51]\n\t"
  "ds_read_b64_tr_b16 v[70:71], v110 offset:26624\n\t"
  "buffer_load_dwordx4 v[220:223], v1, %[srd], s45 offen offset:3072 sc1\n\t"
  "v_mfma_f32_16x16x32_bf16 v[52:55], a[176:179], v[76:79], v[52:55]\n\t"
  "ds_read_b64_tr_b16 v[72:73], v111 offset:24576\n\t"
  "v_max_f32 v21, 0, v21\n\t"
  "v_mfma_f32_16x16x32_bf16 v[56:59], a[168:171], v[80:83], v[56:59]\n\t"
  "ds_read_b64_tr_b16 v[74:75], v111 offset:26624\n\t"
  "v_mul_f32 v22, v22, v128\n\t"
  "v_mfma_f32_16x16x32_bf16 v[60:63], a[172:175], v[80:83], v[60:63]\n\t"
  "v_mul_f32 v23, v23, v128\n\t"
  "v_mul_f32 v20, v20, v22\n\t"
  "v_mfma_f32_16x16x32_bf16 v[64:67], a[176:179], v[80:83], v[64:67]\n\t"
  "s_waitcnt lgkmcnt(4)\n\t"
  "s_add_u32 s45, %[sW2], 20480\n\t"
  "v_mfma_f32_16x16x32_bf16 v[44:47], a[180:183], v[84:87], v[44:47]\n\t"
  "ds_read_b64_tr_b16 v[76:77], v110 offset:28672\n\t"
  "v_mul_f32 v21, v21, v23\n\t"
  "v_mfma_f32_16x16x32_bf16 v[48:51], a[184:187], v[84:87], v[48:51]\n\t"
  "ds_read_b64_tr_b16 v[78:79], v110 offset:30720\n\t"
  "v_cvt_pk_bf16_f32 v116, v20, v21\n\t"
  "v_mfma_f32_16x16x32_bf16 v[52:55], a[188:191], v[84:87], v[52:55]\n\t"
  "ds_read_b64_tr_b16 v[80:81], v111 offset:28672\n\t"
  "v_cvt_pk_bf16_f32 v112, v20, v21\n\t"
  "v_mfma_f32_16x16x32_bf16 v[56:59], a[180:183], v[88:91], v[56:59]\n\t"
  "ds_read_b64_tr_b16 v[82:83], v111 offset:30720\n\t"
  "v_max_f32 v24, 0, v24\n\t"
  "v_mfma_f32_16x16x32_bf16 v[60:63], a[184:187], v[88:91], v[60:63]\n\t"
  "buffer_load_dwordx4 v[224:227], v1, %[srd], s45 offen offset:0 sc1\n\t"
  "v_max_f32 v25, 0, v25\n\t"
  "v_mfma_f32_16x16x32_bf16 v[64:67], a[188:191], v[88:91], v[64:67]\n\t"
  "s_waitcnt lgkmcnt(4)\n\t"
  "v_mul_f32 v26, v26, v128\n\t"
  "v_mfma_f32_16x16x32_bf16 v[44:47], a[192:195], v[68:71], v[44:47]\n\t"
  "ds_read_b64_tr_b16 v[84:85], v110 offset:32768\n\t"
  "v_mul_f32 v27, v27, v128\n\t"
  "v_mfma_f32_16x16x32_bf16 v[48:51], a[196:199], v[68:71], v[48:51]\n\t"
  "ds_read_b64_tr_b16 v[86:87], v110 offset:34816\n\t"
  "v_mul_f32 v24, v24, v26\n\t"
  "v_mfma_f32_16x16x32_bf16 v[52:55], a[200:203], v[68:71], v[52:55]\n\t"
  "ds_read_b64_tr_b16 v[88:89], v111 offset:32768\n\t"
  "buffer_load_dwordx4 v[228:231], v1, %[srd], s45 offen offset:1024 sc1\n\t"
  "v_mfma_f32_16x16x32_bf16 v[56:59], a[192:195], v[72:75], v[56:59]\n\t"
  "ds_read_b64_tr_b16 v[90:91], v111 offset:34816\n\t"
  "v_mul_f32 v25, v25, v27\n\t"
  "v_mfma_f32_16x16x32_bf16 v[60:63], a[196:199], v[72:75], v[60:63]\n\t"
  "v_cvt_pk_bf16_f32 v117, v24, v25\n\t"
  "v_cvt_pk_bf16_f32 v113, v24, v25\n\t"
  "v_mfma_f32_16x16x32_bf16 v[64:67], a[200:203], v[72:75], v[64:67]\n\t"
  "s_waitcnt lgkmcnt(4)\n\t"
  "v_max_f32 v28, 0, v28\n\t"
  "v_mfma_f32_16x16x32_bf16 v[44:47], a[204:207], v[76:79], v[44:47]\n\t"
  "ds_read_b64_tr_b16 v[68:69], v110 offset:36864\n\t"
  "v_max_f32 v29, 0, v29\n\t"
  "v_mfma_f32_16x16x32_bf16 v[48:51], a[208:211], v[76:79], v[48:51]\n\t"
  "ds_read_b64_tr_b16 v[70:71], v110 offset:38912\n\t"
  "buffer_load_dwordx4 v[232:235], v1, %[srd], s45 offen offset:2048 sc1\n\t"
  "v_mfma_f32_16x16x32_bf16 v[52:55], a[212:215], v[76:79], v[52:55]\n\t"
  "ds_read_b64_tr_b16 v[72:73], v111 offset:36864\n\t"
  "v_mul_f32 v30, v30, v128\n\t"
  "v_mfma_f32_16x16x32_bf16 v[56:59], a[204:207], v[80:83], v[56:59]\n\t"
  "ds_read_b64_tr_b16 v[74:75], v111 offset:38912\n\t"
  "v_mul_f32 v31, v31, v128\n\t"
  "v_mfma_f32_16x16x32_bf16 v[60:63], a[208:211], v[80:83], v[60:63]\n\t"
  "v_mul_f32 v28, v28, v30\n\t"
  "v_permlane16_swap_b32 v116, v117\n\t"
  "v_mfma_f32_16x16x32_bf16 v[64:67], a[212:215], v[80:83], v[64:67]\n\t"
  "s_waitcnt lgkmcnt(4)\n\t"
  "buffer_load_dwordx4 v[236:239], v1, %[srd], s45 offen offset:3072 sc1\n\t"
  "v_mfma_f32_16x16x32_bf16 v[44:47], a[216:219], v[84:87], v[44:47]\n\t"
  "ds_read_b64_tr_b16 v[76:77], v108 offset:0\n\t"
  "v_mul_f32 v29, v29, v31\n\t"
  "v_mfma_f32_16x16x32_bf16 v[48:51], a[220:223], v[84:87], v[48:51]\n\t"
  "ds_read_b64_tr_b16 v[78:79], v108 offset:2048\n\t"
  "v_cvt_pk_bf16_f32 v118, v28, v29\n\t"
  "v_mfma_f32_16x16x32_bf16 v[52:55], a[224:227], v[84:87], v[52:55]\n\t"
  "ds_read_b64_tr_b16 v[80:81], v109 offset:0\n\t"
  "v_cvt_pk_bf16_f32 v114, v28, v29\n\t"
  "v_mfma_f32_16x16x32_bf16 v[56:59], a[216:219], v[88:91], v[56:59]\n\t"
  "ds_read_b64_tr_b16 v[82:83], v109 offset:2048\n\t"
  "s_mov_b32 s55, 0\n\t"
  "v_mfma_f32_16x16x32_bf16 v[60:63], a[220:223], v[88:91], v[60:63]\n\t"
  "s_add_u32 s45, %[sW2], 24576\n\t"
  "v_permlane16_swap_b32 v118, v115\n\t"
  "v_mfma_f32_16x16x32_bf16 v[64:67], a[224:227], v[88:91], v[64:67]\n\t"
  "s_waitcnt lgkmcnt(4)\n\t"
  "v_permlane32_swap_b32 v117, v115\n\t"
  "v_mfma_f32_16x16x32_bf16 v[44:47], a[228:231], v[68:71], v[44:47]\n\t"
  "ds_read_b64_tr_b16 v[84:85], v108 offset:4096\n\t"
  "buffer_store_dwordx4 v[112:115], v8, %[srd], s55 offen sc1\n\t"
  "v_mfma_f32_16x16x32_bf16 v[48:51], a[232:235], v[68:71], v[48:51]\n\t"
  "ds_read_b64_tr_b16 v[86:87], v108 offset:6144\n\t"
  "v_max_f32 v32, 0, v32\n\t"
  "v_mfma_f32_16x16x32_bf16 v[52:55], a[236:239], v[68:71], v[52:55]\n\t"
  "ds_read_b64_tr_b16 v[88:89], v109 offset:4096\n\t"
  "buffer_load_dwordx4 v[240:243], v1, %[srd], s45 offen offset:0 sc1\n\t"
  "v_mfma_f32_16x16x32_bf16 v[56:59], a[228:231], v[72:75], v[56:59]\n\t"
  "ds_read_b64_tr_b16 v[90:91], v109 offset:6144\n\t"
  "v_max_f32 v33, 0, v33\n\t"
  "v_mfma_f32_16x16x32_bf16 v[60:63], a[232:235], v[72:75], v[60:63]\n\t"
  "v_mul_f32 v34, v34, v129\n\t"
  "v_mul_f32 v35, v35, v129\n\t"
  "v_mfma_f32_16x16x32_bf16 v[64:67], a[236:239], v[72:75], v[64:67]\n\t"
  "v_mul_f32 v32, v32, v34\n\t"
  "v_mul_f32 v33, v33, v35\n\t"
  "v_mfma_f32_16x16x16_bf16 v[44:47], a[240:241], v[96:97], v[44:47]\n\t"
  "v_cvt_pk_bf16_f32 v124, v32, v33\n\t"
  "v_cvt_pk_bf16_f32 v120, v32, v33\n\t"
  "v_mfma_f32_16x16x16_bf16 v[48:51], a[242:243], v[96:97], v[48:51]\n\t"
  "buffer_load_dwordx4 v[244:247], v1, %[srd], s45 offen offset:1024 sc1\n\t"
  "v_max_f32 v36, 0, v36\n\t"
  "v_mfma_f32_16x16x16_bf16 v[52:55], a[244:245], v[96:97], v[52:55]\n\t"
  "v_max_f32 v37, 0, v37\n\t"
  "v_mul_f32 v38, v38, v129\n\t"
  "v_mfma_f32_16x16x16_bf16 v[56:59], a[240:241], v[98:99], v[56:59]\n\t"
  "v_mul_f32 v39, v39, v129\n\t"
  "v_mul_f32 v36, v36, v38\n\t"
  "v_mfma_f32_16x16x16_bf16 v[60:63], a[242:243], v[98:99], v[60:63]\n\t"
  "v_mul_f32 v37, v37, v39\n\t"
  "v_cvt_pk_bf16_f32 v125, v36, v37\n\t"
  "v_cvt_pk_bf16_f32 v121, v36, v37\n\t"
  "v_max_f32 v40, 0, v40\n\t"
  "v_max_f32 v41, 0, v41\n\t"
  "v_mul_f32 v42, v42, v129\n\t"
  "v_mul_f32 v43, v43, v129\n\t"
  "v_mul_f32 v40, v40, v42\n\t"
  "v_permlane16_swap_b32 v124, v125\n\t"
  "v_mul_f32 v41, v41, v43\n\t"
  "v_cvt_pk_bf16_f32 v126, v40, v41\n\t"
  "v_cvt_pk_bf16_f32 v122, v40, v41\n\t"
  "s_mov_b32 s55, 49152\n\t"
  "v_permlane16_swap_b32 v126, v123\n\t"
  "s_nop 1\n\t"
  "v_permlane32_swap_b32 v125, v123\n\t"
  "buffer_store_dwordx4 v[120:123], v8, %[srd], s55 offen sc1\n\t"
  "v_mfma_f32_16x16x16_bf16 v[64:67], a[244:245], v[98:99], v[64:67]\n\t"
  "s_waitcnt lgkmcnt(4)\n\t"
  "v_mfma_f32_16x16x32_bf16 v[20:23], a[0:3], v[76:79], 0\n\t"
  "ds_read_b64_tr_b16 v[68:69], v108 offset:8192\n\t"
  "v_add_u32 v110, 122880, v5\n\t"
  "v_mfma_f32_16x16x32_bf16 v[24:27], a[4:7], v[76:79], 0\n\t"
  "ds_read_b64_tr_b16 v[70:71], v108 offset:10240\n\t"
  "v_xor_b32 v111, 32, v5\n\t"
  "v_mfma_f32_16x16x32_bf16 v[28:31], a[8:11], v[76:79], 0\n\t"
  "ds_read_b64_tr_b16 v[72:73], v109 offset:8192\n\t"
  "v_add_u32 v111, 122880, v111\n\t"
  "v_mfma_f32_16x16x32_bf16 v[32:35], a[0:3], v[80:83], 0\n\t"
  "ds_read_b64_tr_b16 v[74:75], v109 offset:10240\n\t"
  "v_and_b32 v128, 0xffff0000, v97\n\t"
  "v_mfma_f32_16x16x32_bf16 v[36:39], a[4:7], v[80:83], 0\n\t"
  "v_and_b32 v129, 0xffff0000, v99\n\t"
  "v_rcp_f32 v128, v128\n\t"
  "v_mfma_f32_16x16x32_bf16 v[40:43], a[8:11], v[80:83], 0\n\t"
  "s_waitcnt lgkmcnt(4)\n\t"
  "v_rcp_f32 v129, v129\n\t"
  "v_mfma_f32_16x16x32_bf16 v[20:23], a[12:15], v[84:87], v[20:23]\n\t"
  "ds_read_b64_tr_b16 v[76:77], v108 offset:12288\n\t"
  "s_barrier\n\t"
  "v_mfma_f32_16x16x32_bf16 v[24:27], a[16:19], v[84:87], v[24:27]\n\t"
  "ds_read_b64_tr_b16 v[78:79], v108 offset:14336\n\t"
  "v_mul_f32 v128, v128, v128\n\t"
  "v_mfma_f32_16x16x32_bf16 v[28:31], a[20:23], v[84:87], v[28:31]\n\t"
  "ds_read_b64_tr_b16 v[80:81], v109 offset:12288\n\t"
  "v_mul_f32 v129, v129, v129\n\t"
  "v_mfma_f32_16x16x32_bf16 v[32:35], a[12:15], v[88:91], v[32:35]\n\t"
  "ds_read_b64_tr_b16 v[82:83], v109 offset:14336\n\t"
  "s_mul_i32 s52, %[w], 20480\n\t"
  "v_mfma_f32_16x16x32_bf16 v[36:39], a[16:19], v[88:91], v[36:39]\n\t"
  "v_max_f32 v44, 0, v44\n\t"
  "v_max_f32 v45, 0, v45\n\t"
  "v_mfma_f32_16x16x32_bf16 v[40:43], a[20:23], v[88:91], v[40:43]\n\t"
  "s_waitcnt lgkmcnt(4)\n\t"
  "v_mul_f32 v46, v46, v128\n\t"
  "v_mfma_f32_16x16x32_bf16 v[20:23], a[24:27], v[68:71], v[20:23]\n\t"
  "ds_read_b64_tr_b16 v[84:85], v108 offset:16384\n\t"
  "s_add_u32 m0, s52, 0\n\t"
  "v_mfma_f32_16x16x32_bf16 v[24:27], a[28:31], v[68:71], v[24:27]\n\t"
  "ds_read_b64_tr_b16 v[86:87], v108 offset:18432\n\t"
  "v_mul_f32 v47, v47, v128\n\t"
  "v_mfma_f32_16x16x32_bf16 v[28:31], a[32:35], v[68:71], v[28:31]\n\t"
  "ds_read_b64_tr_b16 v[88:89], v109 offset:16384\n\t"
  "v_mul_f32 v44, v44, v46\n\t"
  "v_mfma_f32_16x16x32_bf16 v[32:35], a[24:27], v[72:75], v[32:35]\n\t"
  "ds_read_b64_tr_b16 v[90:91], v109 offset:18432\n\t"
  "v_mul_f32 v45, v45, v47\n\t"
  "v_mfma_f32_16x16x32_bf16 v[36:39], a[28:31], v[72:75], v[36:39]\n\t"
  "s_add_u32 s46, %[sW2], 26624\n\t"
  "v_cvt_pk_bf16_f32 v116, v44, v45\n\t"
  "v_mfma_f32_16x16x32_bf16 v[40:43], a[32:35], v[72:75], v[40:43]\n\t"
  "s_waitcnt lgkmcnt(4)\n\t"
  "v_cvt_pk_bf16_f32 v112, v44, v45\n\t"
  "v_mfma_f32_16x16x32_bf16 v[20:23], a[36:39], v[76:79], v[20:23]\n\t"
  "ds_read_b64_tr_b16 v[68:69], v108 offset:20480\n\t"
  "v_max_f32 v48, 0, v48\n\t"
  "v_mfma_f32_16x16x32_bf16 v[24:27], a[40:43], v[76:79], v[24:27]\n\t"
  "ds_read_b64_tr_b16 v[70:71], v108 offset:22528\n\t"
  "buffer_load_dwordx4 v1, %[srd], s46 offen offset:0 sc1 lds\n\t"
  "v_mfma_f32_16x16x32_bf16 v[28:31], a[44:47], v[76:79], v[28:31]\n\t"
  "ds_read_b64_tr_b16 v[72:73], v109 offset:20480\n\t"
  "v_max_f32 v49, 0, v49\n\t"
  "v_mfma_f32_16x16x32_bf16 v[32:35], a[36:39], v[80:83], v[32:35]\n\t"
  "ds_read_b64_tr_b16 v[74:75], v109 offset:22528\n\t"
  "v_mul_f32 v50, v50, v128\n\t"
  "v_mfma_f32_16x16x32_bf16 v[36:39], a[40:43], v[80:83], v[36:39]\n\t"
  "buffer_load_dwordx4 v1, %[srd], s46 offen offset:1024 sc1 lds\n\t"
  "v_mul_f32 v51, v51, v128\n\t"
  "v_mfma_f32_16x16x32_bf16 v[40:43], a[44:47], v[80:83], v[40:43]\n\t"
  "s_waitcnt lgkmcnt(4)\n\t"
  "v_mul_f32 v48, v48, v50\n\t"
  "v_mfma_f32_16x16x32_bf16 v[20:23], a[48:51], v[84:87], v[20:23]\n\t"
  "ds_read_b64_tr_b16 v[76:77], v108 offset:24576\n\t"
  "v_mul_f32 v49, v49, v51\n\t"
  "v_mfma_f32_16x16x32_bf16 v[24:27], a[52:55], v[84:87], v[24:27]\n\t"
  "ds_read_b64_tr_b16 v[78:79], v108 offset:26624\n\t"
  "v_cvt_pk_bf16_f32 v117, v48, v49\n\t"
  "v_mfma_f32_16x16x32_bf16 v[28:31], a[56:59], v[84:87], v[28:31]\n\t"
  "ds_read_b64_tr_b16 v[80:81], v109 offset:24576\n\t"
  "buffer_load_dwordx4 v1, %[srd], s46 offen offset:2048 sc1 lds\n\t"
  "v_mfma_f32_16x16x32_bf16 v[32:35], a[48:51], v[88:91], v[32:35]\n\t"
  "ds_read_b64_tr_b16 v[82:83], v109 offset:26624\n\t"
  "v_cvt_pk_bf16_f32 v113, v48, v49\n\t"
  "v_mfma_f32_16x16x32_bf16 v[36:39], a[52:55], v[88:91], v[36:39]\n\t"
  "v_max_f32 v52, 0, v52\n\t"
  "v_max_f32 v53, 0, v53\n\t"
  "v_mfma_f32_16x16x32_bf16 v[40:43], a[56:59], v[88:91], v[40:43]\n\t"
  "s_waitcnt lgkmcnt(4)\n\t"
  "buffer_load_dwordx4 v1, %[srd], s46 offen offset:3072 sc1 lds\n\t"
  "v_mfma_f32_16x16x32_bf16 v[20:23], a[60:63], v[68:71], v[20:23]\n\t"
  "ds_read_b64_tr_b16 v[84:85], v108 offset:28672\n\t"
  "v_mul_f32 v54, v54, v128\n\t"
  "v_mfma_f32_16x16x32_bf16 v[24:27], a[64:67], v[68:71], v[24:27]\n\t"
  "ds_read_b64_tr_b16 v[86:87], v108 offset:30720\n\t"
  "v_mul_f32 v55, v55, v128\n\t"
  "v_mfma_f32_16x16x32_bf16 v[28:31], a[68:71], v[68:71], v[28:31]\n\t"
  "ds_read_b64_tr_b16 v[88:89], v109 offset:28672\n\t"
  "v_mul_f32 v52, v52, v54\n\t"
  "v_mfma_f32_16x16x32_bf16 v[32:35], a[60:63], v[72:75], v[32:35]\n\t"
  "ds_read_b64_tr_b16 v[90:91], v109 offset:30720\n\t"
  "s_add_u32 m0, s52, 4096\n\t"
  "v_mfma_f32_16x16x32_bf16 v[36:39], a[64:67], v[72:75], v[36:39]\n\t"
  "v_permlane16_swap_b32 v116, v117\n\t"
  "v_mul_f32 v53, v53, v55\n\t"
  "v_mfma_f32_16x16x32_bf16 v[40:43], a[68:71], v[72:75], v[40:43]\n\t"
  "s_waitcnt lgkmcnt(4)\n\t"
  "v_cvt_pk_bf16_f32 v118, v52, v53\n\t"
  "v_mfma_f32_16x16x32_bf16 v[20:23], a[72:75], v[76:79], v[20:23]\n\t"
  "ds_read_b64_tr_b16 v[68:69], v108 offset:32768\n\t"
  "s_add_u32 s46, %[sW2], 30720\n\t"
  "v_mfma_f32_16x16x32_bf16 v[24:27], a[76:79], v[76:79], v[24:27]\n\t"
  "ds_read_b64_tr_b16 v[70:71], v108 offset:34816\n\t"
  "v_cvt_pk_bf16_f32 v114, v52, v53\n\t"
  "v_mfma_f32_16x16x32_bf16 v[28:31], a[80:83], v[76:79], v[28:31]\n\t"
  "ds_read_b64_tr_b16 v[72:73], v109 offset:32768\n\t"
  "s_mov_b32 s55, 98304\n\t"
  "v_mfma_f32_16x16x32_bf16 v[32:35], a[72:75], v[80:83], v[32:35]\n\t"
  "ds_read_b64_tr_b16 v[74:75], v109 offset:34816\n\t"
  "buffer_load_dwordx4 v1, %[srd], s46 offen offset:0 sc1 lds\n\t"
  "v_mfma_f32_16x16x32_bf16 v[36:39], a[76:79], v[80:83], v[36:39]\n\t"
  "v_permlane16_swap_b32 v118, v115\n\t"
  "v_mfma_f32_16x16x32_bf16 v[40:43], a[80:83], v[80:83], v[40:43]\n\t"
  "s_waitcnt lgkmcnt(4)\n\t"
  "v_permlane32_swap_b32 v117, v115\n\t"
  "v_mfma_f32_16x16x32_bf16 v[20:23], a[84:87], v[84:87], v[20:23]\n\t"
  "ds_read_b64_tr_b16 v[76:77], v108 offset:36864\n\t"
  "buffer_store_dwordx4 v[112:115], v8, %[srd], s55 offen sc1\n\t"
  "v_mfma_f32_16x16x32_bf16 v[24:27], a[88:91], v[84:87], v[24:27]\n\t"
  "ds_read_b64_tr_b16 v[78:79], v108 offset:38912\n\t"
  "buffer_load_dwordx4 v1, %[srd], s46 offen offset:1024 sc1 lds\n\t"
  "v_mfma_f32_16x16x32_bf16 v[28:31], a[92:95], v[84:87], v[28:31]\n\t"
  "ds_read_b64_tr_b16 v[80:81], v109 offset:36864\n\t"
  "v_max_f32 v56, 0, v56\n\t"
  "v_mfma_f32_16x16x32_bf16 v[32:35], a[84:87], v[88:91], v[32:35]\n\t"
  "ds_read_b64_tr_b16 v[82:83], v109 offset:38912\n\t"
  "v_max_f32 v57, 0, v57\n\t"
  "v_mfma_f32_16x16x32_bf16 v[36:39], a[88:91], v[88:91], v[36:39]\n\t"
  "buffer_load_dwordx4 v1, %[srd], s46 offen offset:2048 sc1 lds\n\t"
  "v_mul_f32 v58, v58, v129\n\t"
  "v_mfma_f32_16x16x32_bf16 v[40:43], a[92:95], v[88:91], v[40:43]\n\t"
  "s_waitcnt lgkmcnt(4)\n\t"
  "v_mul_f32 v59, v59, v129\n\t"
  "v_mfma_f32_16x16x32_bf16 v[20:23], a[96:99], v[68:71], v[20:23]\n\t"
  "ds_read_b64_tr_b16 v[84:85], v110 offset:0\n\t"
  "v_mul_f32 v56, v56, v58\n\t"
  "v_mfma_f32_16x16x32_bf16 v[24:27], a[100:103], v[68:71], v[24:27]\n\t"
  "ds_read_b64_tr_b16 v[86:87], v110 offset:2048\n\t"
  "buffer_load_dwordx4 v1, %[srd], s46 offen offset:3072 sc1 lds\n\t"
  "v_mfma_f32_16x16x32_bf16 v[28:31], a[104:107], v[68:71], v[28:31]\n\t"
  "ds_read_b64_tr_b16 v[88:89], v111 offset:0\n\t"
  "v_mul_f32 v57, v57, v59\n\t"
  "v_mfma_f32_16x16x32_bf16 v[32:35], a[96:99], v[72:75], v[32:35]\n\t"
  "ds_read_b64_tr_b16 v[90:91], v111 offset:2048\n\t"
  "v_cvt_pk_bf16_f32 v124, v56, v57\n\t"
  "v_mfma_f32_16x16x32_bf16 v[36:39], a[100:103], v[72:75], v[36:39]\n\t"
  "buffer_load_dwordx2 v[100:101], v136, %[srd], s54 offen offset:0 sc1\n\t"
  "buffer_load_dwordx2 v[102:103], v136, %[srd], s54 offen offset:1024 sc1\n\t"
  "v_mfma_f32_16x16x32_bf16 v[40:43], a[104:107], v[72:75], v[40:43]\n\t"
  "s_waitcnt lgkmcnt(4)\n\t"
  "buffer_load_dwordx2 v[104:105], v136, %[srd], s54 offen offset:2048 sc1\n\t"
  "v_mfma_f32_16x16x32_bf16 v[20:23], a[108:111], v[76:79], v[20:23]\n\t"
  "ds_read_b64_tr_b16 v[68:69], v110 offset:4096\n\t"
  "s_movk_i32 s56, 0\n\t"
  "v_mfma_f32_16x16x32_bf16 v[24:27], a[112:115], v[76:79], v[24:27]\n\t"
  "ds_read_b64_tr_b16 v[70:71], v110 offset:6144\n\t"
  "s_waitcnt vmcnt(12)\n\t"
  "v_mfma_f32_16x16x32_bf16 v[28:31], a[116:119], v[76:79], v[28:31]\n\t"
  "ds_read_b64_tr_b16 v[72:73], v111 offset:4096\n\t"
  "buffer_load_dwordx2 v[106:107], v136, %[srd], s54 offen offset:3072 sc1\n\t"
  "v_mfma_f32_16x16x32_bf16 v[32:35], a[108:111], v[80:83], v[32:35]\n\t"
  "ds_read_b64_tr_b16 v[74:75], v111 offset:6144\n\t"
  "buffer_store_dwordx4 v[132:135], v9, %[srd], s56 offen sc1\n\t"
  "v_mfma_f32_16x16x32_bf16 v[36:39], a[112:115], v[80:83], v[36:39]\n\t"
  "s_add_u32 m0, s52, 8192\n\t"
  "s_add_u32 s46, %[sW2], 34816\n\t"
  "v_mfma_f32_16x16x32_bf16 v[40:43], a[116:119], v[80:83], v[40:43]\n\t"
  "s_waitcnt lgkmcnt(4)\n\t"
  "v_cvt_pk_bf16_f32 v120, v56, v57\n\t"
  "v_mfma_f32_16x16x32_bf16 v[20:23], a[120:123], v[84:87], v[20:23]\n\t"
  "ds_read_b64_tr_b16 v[76:77], v110 offset:8192\n\t"
  "v_xor_b32 v108, 64, v5\n\t"
  "v_mfma_f32_16x16x32_bf16 v[24:27], a[124:127], v[84:87], v[24:27]\n\t"
  "ds_read_b64_tr_b16 v[78:79], v110 offset:10240\n\t"
  "v_add_u32 v108, 81920, v108\n\t"
  "v_mfma_f32_16x16x32_bf16 v[28:31], a[128:131], v[84:87], v[28:31]\n\t"
  "ds_read_b64_tr_b16 v[80:81], v111 offset:8192\n\t"
  "v_xor_b32 v109, 96, v5\n\t"
  "v_mfma_f32_16x16x32_bf16 v[32:35], a[120:123], v[88:91], v[32:35]\n\t"
  "ds_read_b64_tr_b16 v[82:83], v111 offset:10240\n\t"
  "v_add_u32 v109, 81920, v109\n\t"
  "v_mfma_f32_16x16x32_bf16 v[36:39], a[124:127], v[88:91], v[36:39]\n\t"
  "buffer_load_dwordx4 v1, %[srd], s46 offen offset:0 sc1 lds\n\t"
  "buffer_load_dwordx4 v1, %[srd], s46 offen offset:1024 sc1 lds\n\t"
  "v_mfma_f32_16x16x32_bf16 v[40:43], a[128:131], v[88:91], v[40:43]\n\t"
  "s_waitcnt lgkmcnt(4)\n\t"
  "v_max_f32 v60, 0, v60\n\t"
  "v_mfma_f32_16x16x32_bf16 v[20:23], a[132:135], v[68:71], v[20:23]\n\t"
  "ds_read_b64_tr_b16 v[84:85], v110 offset:12288\n\t"
  "buffer_load_dwordx4 v1, %[srd], s46 offen offset:2048 sc1 lds\n\t"
  "v_mfma_f32_16x16x32_bf16 v[24:27], a[136:139], v[68:71], v[24:27]\n\t"
  "ds_read_b64_tr_b16 v[86:87], v110 offset:14336\n\t"
  "v_max_f32 v61, 0, v61\n\t"
  "v_mfma_f32_16x16x32_bf16 v[28:31], a[140:143], v[68:71], v[28:31]\n\t"
  "ds_read_b64_tr_b16 v[88:89], v111 offset:12288\n\t"
  "v_mul_f32 v62, v62, v129\n\t"
  "v_mfma_f32_16x16x32_bf16 v[32:35], a[132:135], v[72:75], v[32:35]\n\t"
  "ds_read_b64_tr_b16 v[90:91], v111 offset:14336\n\t"
  "v_mul_f32 v63, v63, v129\n\t"
  "v_mfma_f32_16x16x32_bf16 v[36:39], a[136:139], v[72:75], v[36:39]\n\t"
  "buffer_load_dwordx4 v1, %[srd], s46 offen offset:3072 sc1 lds\n\t"
  "v_mul_f32 v60, v60, v62\n\t"
  "v_mfma_f32_16x16x32_bf16 v[40:43], a[140:143], v[72:75], v[40:43]\n\t"
  "s_waitcnt lgkmcnt(4)\n\t"
  "v_mul_f32 v61, v61, v63\n\t"
  "v_mfma_f32_16x16x32_bf16 v[20:23], a[144:147], v[76:79], v[20:23]\n\t"
  "ds_read_b64_tr_b16 v[68:69], v110 offset:16384\n\t"
  "v_cvt_pk_bf16_f32 v125, v60, v61\n\t"
  "v_mfma_f32_16x16x32_bf16 v[24:27], a[148:151], v[76:79], v[24:27]\n\t"
  "ds_read_b64_tr_b16 v[70:71], v110 offset:18432\n\t"
  "s_add_u32 m0, s52, 12288\n\t"
  "v_mfma_f32_16x16x32_bf16 v[28:31], a[152:155], v[76:79], v[28:31]\n\t"
  "ds_read_b64_tr_b16 v[72:73], v111 offset:16384\n\t"
  "v_cvt_pk_bf16_f32 v121, v60, v61\n\t"
  "v_mfma_f32_16x16x32_bf16 v[32:35], a[144:147], v[80:83], v[32:35]\n\t"
  "ds_read_b64_tr_b16 v[74:75], v111 offset:18432\n\t"
  "v_max_f32 v64, 0, v64\n\t"
  "v_mfma_f32_16x16x32_bf16 v[36:39], a[148:151], v[80:83], v[36:39]\n\t"
  "s_add_u32 s46, %[sW2], 38912\n\t"
  "v_max_f32 v65, 0, v65\n\t"
  "v_mfma_f32_16x16x32_bf16 v[40:43], a[152:155], v[80:83], v[40:43]\n\t"
  "s_waitcnt lgkmcnt(4)\n\t"
  "v_mul_f32 v66, v66, v129\n\t"
  "v_mfma_f32_16x16x32_bf16 v[20:23], a[156:159], v[84:87], v[20:23]\n\t"
  "ds_read_b64_tr_b16 v[76:77], v110 offset:20480\n\t"
  "v_mul_f32 v67, v67, v129\n\t"
  "v_mfma_f32_16x16x32_bf16 v[24:27], a[160:163], v[84:87], v[24:27]\n\t"
  "ds_read_b64_tr_b16 v[78:79], v110 offset:22528\n\t"
  "v_mul_f32 v64, v64, v66\n\t"
  "v_mfma_f32_16x16x32_bf16 v[28:31], a[164:167], v[84:87], v[28:31]\n\t"
  "ds_read_b64_tr_b16 v[80:81], v111 offset:20480\n\t"
  "buffer_load_dwordx4 v1, %[srd], s46 offen offset:0 sc1 lds\n\t"
  "v_mfma_f32_16x16x32_bf16 v[32:35], a[156:159], v[88:91], v[32:35]\n\t"
  "ds_read_b64_tr_b16 v[82:83], v111 offset:22528\n\t"
  "v_permlane16_swap_b32 v124, v125\n\t"
  "v_mfma_f32_16x16x32_bf16 v[36:39], a[160:163], v[88:91], v[36:39]\n\t"
  "v_mul_f32 v65, v65, v67\n\t"
  "v_cvt_pk_bf16_f32 v126, v64, v65\n\t"
  "v_mfma_f32_16x16x32_bf16 v[40:43], a[164:167], v[88:91], v[40:43]\n\t"
  "s_waitcnt lgkmcnt(4)\n\t"
  "buffer_load_dwordx4 v1, %[srd], s46 offen offset:1024 sc1 lds\n\t"
  "v_mfma_f32_16x16x32_bf16 v[20:23], a[168:171], v[68:71], v[20:23]\n\t"
  "ds_read_b64_tr_b16 v[84:85], v110 offset:24576\n\t"
  "v_cvt_pk_bf16_f32 v122, v64, v65\n\t"
  "v_mfma_f32_16x16x32_bf16 v[24:27], a[172:175], v[68:71], v[24:27]\n\t"
  "ds_read_b64_tr_b16 v[86:87], v110 offset:26624\n\t"
  "s_mov_b32 s55, 147456\n\t"
  "v_mfma_f32_16x16x32_bf16 v[28:31], a[176:179], v[68:71], v[28:31]\n\t"
  "ds_read_b64_tr_b16 v[88:89], v111 offset:24576\n\t"
  "v_permlane16_swap_b32 v126, v123\n\t"
  "v_mfma_f32_16x16x32_bf16 v[32:35], a[168:171], v[72:75], v[32:35]\n\t"
  "ds_read_b64_tr_b16 v[90:91], v111 offset:26624\n\t"
  "buffer_load_dwordx4 v1, %[srd], s46 offen offset:2048 sc1 lds\n\t"
  "v_mfma_f32_16x16x32_bf16 v[36:39], a[172:175], v[72:75], v[36:39]\n\t"
  "v_permlane32_swap_b32 v125, v123\n\t"
  "buffer_store_dwordx4 v[120:123], v8, %[srd], s55 offen sc1\n\t"
  "v_mfma_f32_16x16x32_bf16 v[40:43], a[176:179], v[72:75], v[40:43]\n\t"
  "s_waitcnt lgkmcnt(4)\n\t"
  "v_mfma_f32_16x16x32_bf16 v[20:23], a[180:183], v[76:79], v[20:23]\n\t"
  "ds_read_b64_tr_b16 v[68:69], v110 offset:28672\n\t"
  "buffer_load_dwordx4 v1, %[srd], s46 offen offset:3072 sc1 lds\n\t"
  "v_mfma_f32_16x16x32_bf16 v[24:27], a[184:187], v[76:79], v[24:27]\n\t"
  "ds_read_b64_tr_b16 v[70:71], v110 offset:30720\n\t"
  "v_mfma_f32_16x16x32_bf16 v[28:31], a[188:191], v[76:79], v[28:31]\n\t"
  "ds_read_b64_tr_b16 v[72:73], v111 offset:28672\n\t"
  "v_mfma_f32_16x16x32_bf16 v[32:35], a[180:183], v[80:83], v[32:35]\n\t"
  "ds_read_b64_tr_b16 v[74:75], v111 offset:30720\n\t"
  "s_add_u32 m0, s52, 16384\n\t"
  "v_mfma_f32_16x16x32_bf16 v[36:39], a[184:187], v[80:83], v[36:39]\n\t"
  "v_mfma_f32_16x16x32_bf16 v[40:43], a[188:191], v[80:83], v[40:43]\n\t"
  "s_waitcnt lgkmcnt(4)\n\t"
  "v_mfma_f32_16x16x32_bf16 v[20:23], a[192:195], v[84:87], v[20:23]\n\t"
  "ds_read_b64_tr_b16 v[76:77], v110 offset:32768\n\t"
  "v_mfma_f32_16x16x32_bf16 v[24:27], a[196:199], v[84:87], v[24:27]\n\t"
  "ds_read_b64_tr_b16 v[78:79], v110 offset:34816\n\t"
  "s_add_u32 s46, %[sW2], 43008\n\t"
  "v_mfma_f32_16x16x32_bf16 v[28:31], a[200:203], v[84:87], v[28:31]\n\t"
  "ds_read_b64_tr_b16 v[80:81], v111 offset:32768\n\t"
  "v_mfma_f32_16x16x32_bf16 v[32:35], a[192:195], v[88:91], v[32:35]\n\t"
  "ds_read_b64_tr_b16 v[82:83], v111 offset:34816\n\t"
  "v_mfma_f32_16x16x32_bf16 v[36:39], a[196:199], v[88:91], v[36:39]\n\t"
  "buffer_load_dwordx4 v1, %[srd], s46 offen offset:0 sc1 lds\n\t"
  "v_mfma_f32_16x16x32_bf16 v[40:43], a[200:203], v[88:91], v[40:43]\n\t"
  "s_waitcnt lgkmcnt(4)\n\t"
  "v_mfma_f32_16x16x32_bf16 v[20:23], a[204:207], v[68:71], v[20:23]\n\t"
  "ds_read_b64_tr_b16 v[84:85], v110 offset:36864\n\t"
  "v_mfma_f32_16x16x32_bf16 v[24:27], a[208:211], v[68:71], v[24:27]\n\t"
  "ds_read_b64_tr_b16 v[86:87], v110 offset:38912\n\t"
  "buffer_load_dwordx4 v1, %[srd], s46 offen offset:1024 sc1 lds\n\t"
  "v_mfma_f32_16x16x32_bf16 v[28:31], a[212:215], v[68:71], v[28:31]\n\t"
  "ds_read_b64_tr_b16 v[88:89], v111 offset:36864\n\t"
  "v_mfma_f32_16x16x32_bf16 v[32:35], a[204:207], v[72:75], v[32:35]\n\t"
  "ds_read_b64_tr_b16 v[90:91], v111 offset:38912\n\t"
  "v_mfma_f32_16x16x32_bf16 v[36:39], a[208:211], v[72:75], v[36:39]\n\t"
  "s_waitcnt vmcnt(14)\n\t"
  "v_min_u32 v131, v101, v103\n\t"
  "v_mfma_f32_16x16x32_bf16 v[40:43], a[212:215], v[72:75], v[40:43]\n\t"
  "s_waitcnt lgkmcnt(4)\n\t"
  "s_waitcnt vmcnt(12)\n\t"
  "v_min3_u32 v131, v131, v105, v107\n\t"
  "v_mfma_f32_16x16x32_bf16 v[20:23], a[216:219], v[76:79], v[20:23]\n\t"
  "ds_read_b64_tr_b16 v[68:69], v108 offset:0\n\t"
  "v_cmp_eq_u32 vcc, 0, v131\n\t"
  "v_mfma_f32_16x16x32_bf16 v[24:27], a[220:223], v[76:79], v[24:27]\n\t"
  "ds_read_b64_tr_b16 v[70:71], v108 offset:2048\n\t"
  "buffer_load_dwordx4 v1, %[srd], s46 offen offset:2048 sc1 lds\n\t"
  "v_mfma_f32_16x16x32_bf16 v[28:31], a[224:227], v[76:79], v[28:31]\n\t"
  "ds_read_b64_tr_b16 v[72:73], v109 offset:0\n\t"
  "s_cbranch_vccnz Lst1_slow%=\n\t"
  "Lst1_back%=:\n\t"
  "v_mfma_f32_16x16x32_bf16 v[32:35], a[216:219], v[80:83], v[32:35]\n\t"
  "ds_read_b64_tr_b16 v[74:75], v109 offset:2048\n\t"
  "buffer_load_dwordx4 v1, %[srd], s46 offen offset:3072 sc1 lds\n\t"
  "v_mfma_f32_16x16x32_bf16 v[36:39], a[220:223], v[80:83], v[36:39]\n\t"
  "v_mfma_f32_16x16x32_bf16 v[40:43], a[224:227], v[80:83], v[40:43]\n\t"
  "s_waitcnt lgkmcnt(4)\n\t"
  "v_mfma_f32_16x16x32_bf16 v[20:23], a[228:231], v[84:87], v[20:23]\n\t"
  "ds_read_b64_tr_b16 v[76:77], v108 offset:4096\n\t"
  "v_mfma_f32_16x16x32_bf16 v[24:27], a[232:235], v[84:87], v[24:27]\n\t"
  "ds_read_b64_tr_b16 v[78:79], v108 offset:6144\n\t"
  "v_mfma_f32_16x16x32_bf16 v[28:31], a[236:239], v[84:87], v[28:31]\n\t"
  "ds_read_b64_tr_b16 v[80:81], v109 offset:4096\n\t"
  "v_mfma_f32_16x16x32_bf16 v[32:35], a[228:231], v[88:91], v[32:35]\n\t"
  "ds_read_b64_tr_b16 v[82:83], v109 offset:6144\n\t"
  "v_mfma_f32_16x16x32_bf16 v[36:39], a[232:235], v[88:91], v[36:39]\n\t"
  "v_mfma_f32_16x16x32_bf16 v[40:43], a[236:239], v[88:91], v[40:43]\n\t"
  "v_mfma_f32_16x16x16_bf16 v[20:23], a[240:241], v[100:101], v[20:23]\n\t"
  "v_mfma_f32_16x16x16_bf16 v[24:27], a[242:243], v[100:101], v[24:27]\n\t"
  "v_mfma_f32_16x16x16_bf16 v[28:31], a[244:245], v[100:101], v[28:31]\n\t"
  "v_mfma_f32_16x16x16_bf16 v[32:35], a[240:241], v[102:103], v[32:35]\n\t"
  "v_mfma_f32_16x16x16_bf16 v[36:39], a[242:243], v[102:103], v[36:39]\n\t"
  "v_mfma_f32_16x16x16_bf16 v[40:43], a[244:245], v[102:103], v[40:43]\n\t"
  "s_waitcnt lgkmcnt(4)\n\t"
  "v_mfma_f32_16x16x32_bf16 v[44:47], a[0:3], v[68:71], 0\n\t"
  "ds_read_b64_tr_b16 v[84:85], v108 offset:8192\n\t"
  "v_xor_b32 v110, 64, v5\n\t"
  "v_mfma_f32_16x16x32_bf16 v[48:51], a[4:7], v[68:71], 0\n\t"
  "ds_read_b64_tr_b16 v[86:87], v108 offset:10240\n\t"
  "v_add_u32 v110, 122880, v110\n\t"
  "v_mfma_f32_16x16x32_bf16 v[52:55], a[8:11], v[68:71], 0\n\t"
  "ds_read_b64_tr_b16 v[88:89], v109 offset:8192\n\t"
  "v_xor_b32 v111, 96, v5\n\t"
  "v_mfma_f32_16x16x32_bf16 v[56:59], a[0:3], v[72:75], 0\n\t"
  "ds_read_b64_tr_b16 v[90:91], v109 offset:10240\n\t"
  "v_add_u32 v111, 122880, v111\n\t"
  "v_mfma_f32_16x16x32_bf16 v[60:63], a[4:7], v[72:75], 0\n\t"
  "v_and_b32 v128, 0xffff0000, v101\n\t"
  "v_and_b32 v129, 0xffff0000, v103\n\t"
  "v_mfma_f32_16x16x32_bf16 v[64:67], a[8:11], v[72:75], 0\n\t"
  "s_waitcnt lgkmcnt(4)\n\t"
  "v_rcp_f32 v128, v128\n\t"
  "v_mfma_f32_16x16x32_bf16 v[44:47], a[12:15], v[76:79], v[44:47]\n\t"
  "ds_read_b64_tr_b16 v[68:69], v108 offset:12288\n\t"
  "s_add_u32 s45, %[sW2], 47104\n\t"
  "v_mfma_f32_16x16x32_bf16 v[48:51], a[16:19], v[76:79], v[48:51]\n\t"
  "ds_read_b64_tr_b16 v[70:71], v108 offset:14336\n\t"
  "v_rcp_f32 v129, v129\n\t"
  "v_mfma_f32_16x16x32_bf16 v[52:55], a[20:23], v[76:79], v[52:55]\n\t"
  "ds_read_b64_tr_b16 v[72:73], v109 offset:12288\n\t"
  "v_mul_f32 v128, v128, v128\n\t"
  "v_mfma_f32_16x16x32_bf16 v[56:59], a[12:15], v[80:83], v[56:59]\n\t"
  "ds_read_b64_tr_b16 v[74:75], v109 offset:14336\n\t"
  "v_mul_f32 v129, v129, v129\n\t"
  "v_mfma_f32_16x16x32_bf16 v[60:63], a[16:19], v[80:83], v[60:63]\n\t"
  "buffer_load_dwordx4 a[0:3], v1, %[srd], s45 offen offset:0 sc1\n\t"
  "v_max_f32 v20, 0, v20\n\t"
  "v_mfma_f32_16x16x32_bf16 v[64:67], a[20:23], v[80:83], v[64:67]\n\t"
  "s_waitcnt lgkmcnt(4)\n\t"
  "v_max_f32 v21, 0, v21\n\t"
  "v_mfma_f32_16x16x32_bf16 v[44:47], a[24:27], v[84:87], v[44:47]\n\t"
  "ds_read_b64_tr_b16 v[76:77], v108 offset:16384\n\t"
  "v_mul_f32 v22, v22, v128\n\t"
  "v_mfma_f32_16x16x32_bf16 v[48:51], a[28:31], v[84:87], v[48:51]\n\t"
  "ds_read_b64_tr_b16 v[78:79], v108 offset:18432\n\t"
  "v_mul_f32 v23, v23, v128\n\t"
  "v_mfma_f32_16x16x32_bf16 v[52:55], a[32:35], v[84:87], v[52:55]\n\t"
  "ds_read_b64_tr_b16 v[80:81], v109 offset:16384\n\t"
  "v_mul_f32 v20, v20, v22\n\t"
  "v_mfma_f32_16x16x32_bf16 v[56:59], a[24:27], v[88:91], v[56:59]\n\t"
  "ds_read_b64_tr_b16 v[82:83], v109 offset:18432\n\t"
  "buffer_load_dwordx4 a[4:7], v1, %[srd], s45 offen offset:1024 sc1\n\t"
  "v_mfma_f32_16x16x32_bf16 v[60:63], a[28:31], v[88:91], v[60:63]\n\t"
  "v_mul_f32 v21, v21, v23\n\t"
  "v_cvt_pk_bf16_f32 v116, v20, v21\n\t"
  "v_mfma_f32_16x16x32_bf16 v[64:67], a[32:35], v[88:91], v[64:67]\n\t"
  "s_waitcnt lgkmcnt(4)\n\t"
  "v_cvt_pk_bf16_f32 v112, v20, v21\n\t"
  "v_mfma_f32_16x16x32_bf16 v[44:47], a[36:39], v[68:71], v[44:47]\n\t"
  "ds_read_b64_tr_b16 v[84:85], v108 offset:20480\n\t"
  "s_movk_i32 s56, 8192\n\t"
  "v_mfma_f32_16x16x32_bf16 v[48:51], a[40:43], v[68:71], v[48:51]\n\t"
  "ds_read_b64_tr_b16 v[86:87], v108 offset:22528\n\t"
  "s_waitcnt vmcnt(7)\n\t"
  "v_mfma_f32_16x16x32_bf16 v[52:55], a[44:47], v[68:71], v[52:55]\n\t"
  "ds_read_b64_tr_b16 v[88:89], v109 offset:20480\n\t"
  "buffer_store_dwordx4 v[132:135], v9, %[srd], s56 offen sc1\n\t"
  "v_mfma_f32_16x16x32_bf16 v[56:59], a[36:39], v[72:75], v[56:59]\n\t"
  "ds_read_b64_tr_b16 v[90:91], v109 offset:22528\n\t"
  "buffer_load_dwordx4 a[8:11], v1, %[srd], s45 offen offset:2048 sc1\n\t"
  "v_mfma_f32_16x16x32_bf16 v[60:63], a[40:43], v[72:75], v[60:63]\n\t"
  "v_max_f32 v24, 0, v24\n\t"
  "v_max_f32 v25, 0, v25\n\t"
  "v_mfma_f32_16x16x32_bf16 v[64:67], a[44:47], v[72:75], v[64:67]\n\t"
  "s_waitcnt lgkmcnt(4)\n\t"
  "v_mul_f32 v26, v26, v128\n\t"
  "v_mfma_f32_16x16x32_bf16 v[44:47], a[48:51], v[76:79], v[44:47]\n\t"
  "ds_read_b64_tr_b16 v[68:69], v108 offset:24576\n\t"
  "v_mul_f32 v27, v27, v128\n\t"
  "v_mfma_f32_16x16x32_bf16 v[48:51], a[52:55], v[76:79], v[48:51]\n\t"
  "ds_read_b64_tr_b16 v[70:71], v108 offset:26624\n\t"
  "buffer_load_dwordx4 a[12:15], v1, %[srd], s45 offen offset:3072 sc1\n\t"
  "v_mfma_f32_16x16x32_bf16 v[52:55], a[56:59], v[76:79], v[52:55]\n\t"
  "ds_read_b64_tr_b16 v[72:73], v109 offset:24576\n\t"
  "v_mul_f32 v24, v24, v26\n\t"
  "v_mfma_f32_16x16x32_bf16 v[56:59], a[48:51], v[80:83], v[56:59]\n\t"
  "ds_read_b64_tr_b16 v[74:75], v109 offset:26624\n\t"
  "v_mul_f32 v25, v25, v27\n\t"
  "v_mfma_f32_16x16x32_bf16 v[60:63], a[52:55], v[80:83], v[60:63]\n\t"
  "v_cvt_pk_bf16_f32 v117, v24, v25\n\t"
  "v_cvt_pk_bf16_f32 v113, v24, v25\n\t"
  "v_mfma_f32_16x16x32_bf16 v[64:67], a[56:59], v[80:83], v[64:67]\n\t"
  "s_waitcnt lgkmcnt(4)\n\t"
  "s_add_u32 s45, %[sW2], 51200\n\t"
  "v_mfma_f32_16x16x32_bf16 v[44:47], a[60:63], v[84:87], v[44:47]\n\t"
  "ds_read_b64_tr_b16 v[76:77], v108 offset:28672\n\t"
  "v_max_f32 v28, 0, v28\n\t"
  "v_mfma_f32_16x16x32_bf16 v[48:51], a[64:67], v[84:87], v[48:51]\n\t"
  "ds_read_b64_tr_b16 v[78:79], v108 offset:30720\n\t"
  "v_max_f32 v29, 0, v29\n\t"
  "v_mfma_f32_16x16x32_bf16 v[52:55], a[68:71], v[84:87], v[52:55]\n\t"
  "ds_read_b64_tr_b16 v[80:81], v109 offset:28672\n\t"
  "v_mul_f32 v30, v30, v128\n\t"
  "v_mfma_f32_16x16x32_bf16 v[56:59], a[60:63], v[88:91], v[56:59]\n\t"
  "ds_read_b64_tr_b16 v[82:83], v109 offset:30720\n\t"
  "v_mul_f32 v31, v31, v128\n\t"
  "v_mfma_f32_16x16x32_bf16 v[60:63], a[64:67], v[88:91], v[60:63]\n\t"
  "buffer_load_dwordx4 a[16:19], v1, %[srd], s45 offen offset:0 sc1\n\t"
  "v_mul_f32 v28, v28, v30\n\t"
  "v_mfma_f32_16x16x32_bf16 v[64:67], a[68:71], v[88:91], v[64:67]\n\t"
  "s_waitcnt lgkmcnt(4)\n\t"
  "v_permlane16_swap_b32 v116, v117\n\t"
  "v_mfma_f32_16x16x32_bf16 v[44:47], a[72:75], v[68:71], v[44:47]\n\t"
  "ds_read_b64_tr_b16 v[84:85], v108 offset:32768\n\t"
  "v_mul_f32 v29, v29, v31\n\t"
  "v_mfma_f32_16x16x32_bf16 v[48:51], a[76:79], v[68:71], v[48:51]\n\t"
  "ds_read_b64_tr_b16 v[86:87], v108 offset:34816\n\t"
  "v_cvt_pk_bf16_f32 v118, v28, v29\n\t"
  "v_mfma_f32_16x16x32_bf16 v[52:55], a[80:83], v[68:71], v[52:55]\n\t"
  "ds_read_b64_tr_b16 v[88:89], v109 offset:32768\n\t"
  "v_cvt_pk_bf16_f32 v114, v28, v29\n\t"
  "v_mfma_f32_16x16x32_bf16 v[56:59], a[72:75], v[72:75], v[56:59]\n\t"
  "ds_read_b64_tr_b16 v[90:91], v109 offset:34816\n\t"
  "buffer_load_dwordx4 a[20:23], v1, %[srd], s45 offen offset:1024 sc1\n\t"
  "v_mfma_f32_16x16x32_bf16 v[60:63], a[76:79], v[72:75], v[60:63]\n\t"
  "s_mov_b32 s55, 3145728\n\t"
  "v_permlane16_swap_b32 v118, v115\n\t"
  "v_mfma_f32_16x16x32_bf16 v[64:67], a[80:83], v[72:75], v[64:67]\n\t"
  "s_waitcnt lgkmcnt(4)\n\t"
  "v_permlane32_swap_b32 v117, v115\n\t"
  "v_mfma_f32_16x16x32_bf16 v[44:47], a[84:87], v[76:79], v[44:47]\n\t"
  "ds_read_b64_tr_b16 v[68:69], v108 offset:36864\n\t"
  "s_add_u32 s57, %[sFL], 0\n\t"
  "v_mfma_f32_16x16x32_bf16 v[48:51], a[88:91], v[76:79], v[48:51]\n\t"
  "ds_read_b64_tr_b16 v[70:71], v108 offset:38912\n\t"
  "buffer_load_dwordx2 v[138:139], v137, %[srd], s57 offen sc1\n\t"
  "v_mfma_f32_16x16x32_bf16 v[52:55], a[92:95], v[76:79], v[52:55]\n\t"
  "ds_read_b64_tr_b16 v[72:73], v109 offset:36864\n\t"
  "buffer_load_dwordx4 a[24:27], v1, %[srd], s45 offen offset:2048 sc1\n\t"
  "v_mfma_f32_16x16x32_bf16 v[56:59], a[84:87], v[80:83], v[56:59]\n\t"
  "ds_read_b64_tr_b16 v[74:75], v109 offset:38912\n\t"
  "buffer_store_dwordx4 v[112:115], v8, %[srd], s55 offen sc1\n\t"
  "v_mfma_f32_16x16x32_bf16 v[60:63], a[88:91], v[80:83], v[60:63]\n\t"
  "v_max_f32 v32, 0, v32\n\t"
  "v_max_f32 v33, 0, v33\n\t"
  "v_mfma_f32_16x16x32_bf16 v[64:67], a[92:95], v[80:83], v[64:67]\n\t"
  "s_waitcnt lgkmcnt(4)\n\t"
  "v_mul_f32 v34, v34, v129\n\t"
  "v_mfma_f32_16x16x32_bf16 v[44:47], a[96:99], v[84:87], v[44:47]\n\t"
  "ds_read_b64_tr_b16 v[76:77], v110 offset:0\n\t"
  "v_mul_f32 v35, v35, v129\n\t"
  "v_mfma_f32_16x16x32_bf16 v[48:51], a[100:103], v[84:87], v[48:51]\n\t"
  "ds_read_b64_tr_b16 v[78:79], v110 offset:2048\n\t"
  "buffer_load_dwordx4 a[28:31], v1, %[srd], s45 offen offset:3072 sc1\n\t"
  "v_mfma_f32_16x16x32_bf16 v[52:55], a[104:107], v[84:87], v[52:55]\n\t"
  "ds_read_b64_tr_b16 v[80:81], v111 offset:0\n\t"
  "v_mul_f32 v32, v32, v34\n\t"
  "v_mfma_f32_16x16x32_bf16 v[56:59], a[96:99], v[88:91], v[56:59]\n\t"
  "ds_read_b64_tr_b16 v[82:83], v111 offset:2048\n\t"
  "v_mul_f32 v33, v33, v35\n\t"
  "v_mfma_f32_16x16x32_bf16 v[60:63], a[100:103], v[88:91], v[60:63]\n\t"
  "v_cvt_pk_bf16_f32 v124, v32, v33\n\t"
  "v_cvt_pk_bf16_f32 v120, v32, v33\n\t"
  "v_mfma_f32_16x16x32_bf16 v[64:67], a[104:107], v[88:91], v[64:67]\n\t"
  "s_waitcnt lgkmcnt(4)\n\t"
  "s_add_u32 s45, %[sW2], 55296\n\t"
  "v_mfma_f32_16x16x32_bf16 v[44:47], a[108:111], v[68:71], v[44:47]\n\t"
  "ds_read_b64_tr_b16 v[84:85], v110 offset:4096\n\t"
  "v_max_f32 v36, 0, v36\n\t"
  "v_mfma_f32_16x16x32_bf16 v[48:51], a[112:115], v[68:71], v[48:51]\n\t"
  "ds_read_b64_tr_b16 v[86:87], v110 offset:6144\n\t"
  "v_max_f32 v37, 0, v37\n\t"
  "v_mfma_f32_16x16x32_bf16 v[52:55], a[116:119], v[68:71], v[52:55]\n\t"
  "ds_read_b64_tr_b16 v[88:89], v111 offset:4096\n\t"
  "v_mul_f32 v38, v38, v129\n\t"
  "v_mfma_f32_16x16x32_bf16 v[56:59], a[108:111], v[72:75], v[56:59]\n\t"
  "ds_read_b64_tr_b16 v[90:91], v111 offset:6144\n\t"
  "v_mul_f32 v39, v39, v129\n\t"
  "v_mfma_f32_16x16x32_bf16 v[60:63], a[112:115], v[72:75], v[60:63]\n\t"
  "buffer_load_dwordx4 a[32:35], v1, %[srd], s45 offen offset:0 sc1\n\t"
  "v_mul_f32 v36, v36, v38\n\t"
  "v_mfma_f32_16x16x32_bf16 v[64:67], a[116:119], v[72:75], v[64:67]\n\t"
  "s_waitcnt lgkmcnt(4)\n\t"
  "v_mul_f32 v37, v37, v39\n\t"
  "v_mfma_f32_16x16x32_bf16 v[44:47], a[120:123], v[76:79], v[44:47]\n\t"
  "ds_read_b64_tr_b16 v[68:69], v110 offset:8192\n\t"
  "v_cvt_pk_bf16_f32 v125, v36, v37\n\t"
  "v_mfma_f32_16x16x32_bf16 v[48:51], a[124:127], v[76:79], v[48:51]\n\t"
  "ds_read_b64_tr_b16 v[70:71], v110 offset:10240\n\t"
  "v_cvt_pk_bf16_f32 v121, v36, v37\n\t"
  "v_mfma_f32_16x16x32_bf16 v[52:55], a[128:131], v[76:79], v[52:55]\n\t"
  "ds_read_b64_tr_b16 v[72:73], v111 offset:8192\n\t"
  "v_max_f32 v40, 0, v40\n\t"
  "v_mfma_f32_16x16x32_bf16 v[56:59], a[120:123], v[80:83], v[56:59]\n\t"
  "ds_read_b64_tr_b16 v[74:75], v111 offset:10240\n\t"
  "buffer_load_dwordx4 a[36:39], v1, %[srd], s45 offen offset:1024 sc1\n\t"
  "v_mfma_f32_16x16x32_bf16 v[60:63], a[124:127], v[80:83], v[60:63]\n\t"
  "v_max_f32 v41, 0, v41\n\t"
  "v_mul_f32 v42, v42, v129\n\t"
  "v_mfma_f32_16x16x32_bf16 v[64:67], a[128:131], v[80:83], v[64:67]\n\t"
  "s_waitcnt lgkmcnt(4)\n\t"
  "v_mul_f32 v43, v43, v129\n\t"
  "v_mfma_f32_16x16x32_bf16 v[44:47], a[132:135], v[84:87], v[44:47]\n\t"
  "ds_read_b64_tr_b16 v[76:77], v110 offset:12288\n\t"
  "v_mul_f32 v40, v40, v42\n\t"
  "v_mfma_f32_16x16x32_bf16 v[48:51], a[136:139], v[84:87], v[48:51]\n\t"
  "ds_read_b64_tr_b16 v[78:79], v110 offset:14336\n\t"
  "v_permlane16_swap_b32 v124, v125\n\t"
  "v_mfma_f32_16x16x32_bf16 v[52:55], a[140:143], v[84:87], v[52:55]\n\t"
  "ds_read_b64_tr_b16 v[80:81], v111 offset:12288\n\t"
  "buffer_load_dwordx4 a[40:43], v1, %[srd], s45 offen offset:2048 sc1\n\t"
  "v_mfma_f32_16x16x32_bf16 v[56:59], a[132:135], v[88:91], v[56:59]\n\t"
  "ds_read_b64_tr_b16 v[82:83], v111 offset:14336\n\t"
  "v_mul_f32 v41, v41, v43\n\t"
  "v_mfma_f32_16x16x32_bf16 v[60:63], a[136:139], v[88:91], v[60:63]\n\t"
  "v_cvt_pk_bf16_f32 v126, v40, v41\n\t"
  "v_cvt_pk_bf16_f32 v122, v40, v41\n\t"
  "v_mfma_f32_16x16x32_bf16 v[64:67], a[140:143], v[88:91], v[64:67]\n\t"
  "s_waitcnt lgkmcnt(4)\n\t"
  "s_mov_b32 s55, 3194880\n\t"
  "v_mfma_f32_16x16x32_bf16 v[44:47], a[144:147], v[68:71], v[44:47]\n\t"
  "ds_read_b64_tr_b16 v[84:85], v110 offset:16384\n\t"
  "buffer_load_dwordx4 a[44:47], v1, %[srd], s45 offen offset:3072 sc1\n\t"
  "v_mfma_f32_16x16x32_bf16 v[48:51], a[148:151], v[68:71], v[48:51]\n\t"
  "ds_read_b64_tr_b16 v[86:87], v110 offset:18432\n\t"
  "v_permlane16_swap_b32 v126, v123\n\t"
  "v_mfma_f32_16x16x32_bf16 v[52:55], a[152:155], v[68:71], v[52:55]\n\t"
  "ds_read_b64_tr_b16 v[88:89], v111 offset:16384\n\t"
  "v_permlane32_swap_b32 v125, v123\n\t"
  "v_mfma_f32_16x16x32_bf16 v[56:59], a[144:147], v[72:75], v[56:59]\n\t"
  "ds_read_b64_tr_b16 v[90:91], v111 offset:18432\n\t"
  "buffer_store_dwordx4 v[120:123], v8, %[srd], s55 offen sc1\n\t"
  "v_mfma_f32_16x16x32_bf16 v[60:63], a[148:151], v[72:75], v[60:63]\n\t"
  "v_and_b32 v128, 0xffff0000, v105\n\t"
  "v_and_b32 v129, 0xffff0000, v107\n\t"
  "v_mfma_f32_16x16x32_bf16 v[64:67], a[152:155], v[72:75], v[64:67]\n\t"
  "s_waitcnt lgkmcnt(4)\n\t"
  "s_add_u32 s45, %[sW2], 59392\n\t"
  "v_mfma_f32_16x16x32_bf16 v[44:47], a[156:159], v[76:79], v[44:47]\n\t"
  "ds_read_b64_tr_b16 v[68:69], v110 offset:20480\n\t"
  "v_rcp_f32 v128, v128\n\t"
  "v_mfma_f32_16x16x32_bf16 v[48:51], a[160:163], v[76:79], v[48:51]\n\t"
  "ds_read_b64_tr_b16 v[70:71], v110 offset:22528\n\t"
  "v_rcp_f32 v129, v129\n\t"
  "v_mfma_f32_16x16x32_bf16 v[52:55], a[164:167], v[76:79], v[52:55]\n\t"
  "ds_read_b64_tr_b16 v[72:73], v111 offset:20480\n\t"
  "v_mul_f32 v128, v128, v128\n\t"
  "v_mfma_f32_16x16x32_bf16 v[56:59], a[156:159], v[80:83], v[56:59]\n\t"
  "ds_read_b64_tr_b16 v[74:75], v111 offset:22528\n\t"
  "v_mul_f32 v129, v129, v129\n\t"
  "v_mfma_f32_16x16x32_bf16 v[60:63], a[160:163], v[80:83], v[60:63]\n\t"
  "buffer_load_dwordx4 a[48:51], v1, %[srd], s45 offen offset:0 sc1\n\t"
  "v_mfma_f32_16x16x32_bf16 v[64:67], a[164:167], v[80:83], v[64:67]\n\t"
  "s_waitcnt lgkmcnt(4)\n\t"
  "s_waitcnt vmcnt(17)\n\t"
  "v_mfma_f32_16x16x32_bf16 v[44:47], a[168:171], v[84:87], v[44:47]\n\t"
  "ds_read_b64_tr_b16 v[76:77], v110 offset:24576\n\t"
  "ds_read_b128 a[160:163], v141 offset:0\n\t"
  "v_mfma_f32_16x16x32_bf16 v[48:51], a[172:175], v[84:87], v[48:51]\n\t"
  "ds_read_b64_tr_b16 v[78:79], v110 offset:26624\n\t"
  "ds_read_b128 a[164:167], v141 offset:1024\n\t"
  "v_mfma_f32_16x16x32_bf16 v[52:55], a[176:179], v[84:87], v[52:55]\n\t"
  "ds_read_b64_tr_b16 v[80:81], v111 offset:24576\n\t"
  "v_mfma_f32_16x16x32_bf16 v[56:59], a[168:171], v[88:91], v[56:59]\n\t"
  "ds_read_b64_tr_b16 v[82:83], v111 offset:26624\n\t"
  "buffer_load_dwordx4 a[52:55], v1, %[srd], s45 offen offset:1024 sc1\n\t"
  "v_mfma_f32_16x16x32_bf16 v[60:63], a[172:175], v[88:91], v[60:63]\n\t"
  "v_mfma_f32_16x16x32_bf16 v[64:67], a[176:179], v[88:91], v[64:67]\n\t"
  "s_waitcnt lgkmcnt(6)\n\t"
  "ds_read_b128 a[168:171], v141 offset:2048\n\t"
  "v_mfma_f32_16x16x32_bf16 v[44:47], a[180:183], v[68:71], v[44:47]\n\t"
  "ds_read_b64_tr_b16 v[84:85], v110 offset:28672\n\t"
  "ds_read_b128 a[172:175], v141 offset:3072\n\t"
  "v_mfma_f32_16x16x32_bf16 v[48:51], a[184:187], v[68:71], v[48:51]\n\t"
  "ds_read_b64_tr_b16 v[86:87], v110 offset:30720\n\t"
  "ds_read_b128 a[176:179], v141 offset:4096\n\t"
  "v_mfma_f32_16x16x32_bf16 v[52:55], a[188:191], v[68:71], v[52:55]\n\t"
  "ds_read_b64_tr_b16 v[88:89], v111 offset:28672\n\t"
  "v_mfma_f32_16x16x32_bf16 v[56:59], a[180:183], v[72:75], v[56:59]\n\t"
  "ds_read_b64_tr_b16 v[90:91], v111 offset:30720\n\t"
  "s_waitcnt vmcnt(10)\n\t"
  "v_cmp_ne_u64 vcc, v[132:133], v[138:139]\n\t"
  "v_mfma_f32_16x16x32_bf16 v[60:63], a[184:187], v[72:75], v[60:63]\n\t"
  "v_mfma_f32_16x16x32_bf16 v[64:67], a[188:191], v[72:75], v[64:67]\n\t"
  "s_waitcnt lgkmcnt(7)\n\t"
  "s_cbranch_vccnz Lfl0_slow%=\n\t"
  "Lfl0_back%=:\n\t"
  "v_mfma_f32_16x16x32_bf16 v[44:47], a[192:195], v[76:79], v[44:47]\n\t"
  "ds_read_b64_tr_b16 v[68:69], v110 offset:32768\n\t"
  "s_add_u32 s44, %[sH2ld], 0\n\t"
  "v_mfma_f32_16x16x32_bf16 v[48:51], a[196:199], v[76:79], v[48:51]\n\t"
  "ds_read_b64_tr_b16 v[70:71], v110 offset:34816\n\t"
  "ds_read_b128 a[180:183], v141 offset:5120\n\t"
  "v_mfma_f32_16x16x32_bf16 v[52:55], a[200:203], v[76:79], v[52:55]\n\t"
  "ds_read_b64_tr_b16 v[72:73], v111 offset:32768\n\t"
  "buffer_load_dwordx4 a[56:59], v1, %[srd], s44 offen offset:0 sc1\n\t"
  "v_mfma_f32_16x16x32_bf16 v[56:59], a[192:195], v[80:83], v[56:59]\n\t"
  "ds_read_b64_tr_b16 v[74:75], v111 offset:34816\n\t"
  "ds_read_b128 a[184:187], v141 offset:6144\n\t"
  "v_mfma_f32_16x16x32_bf16 v[60:63], a[196:199], v[80:83], v[60:63]\n\t"
  "buffer_load_dwordx4 a[60:63], v1, %[srd], s44 offen offset:1024 sc1\n\t"
  "ds_read_b128 a[188:191], v141 offset:7168\n\t"
  "v_mfma_f32_16x16x32_bf16 v[64:67], a[200:203], v[80:83], v[64:67]\n\t"
  "s_waitcnt lgkmcnt(7)\n\t"
  "ds_read_b128 a[192:195], v141 offset:8192\n\t"
  "v_mfma_f32_16x16x32_bf16 v[44:47], a[204:207], v[84:87], v[44:47]\n\t"
  "ds_read_b64_tr_b16 v[76:77], v110 offset:36864\n\t"
  "buffer_load_dwordx4 a[64:67], v1, %[srd], s44 offen offset:2048 sc1\n\t"
  "v_mfma_f32_16x16x32_bf16 v[48:51], a[208:211], v[84:87], v[48:51]\n\t"
  "ds_read_b64_tr_b16 v[78:79], v110 offset:38912\n\t"
  "ds_read_b128 a[196:199], v141 offset:9216\n\t"
  "v_mfma_f32_16x16x32_bf16 v[52:55], a[212:215], v[84:87], v[52:55]\n\t"
  "ds_read_b64_tr_b16 v[80:81], v111 offset:36864\n\t"
  "buffer_load_dwordx4 a[68:71], v1, %[srd], s44 offen offset:3072 sc1\n\t"
  "v_mfma_f32_16x16x32_bf16 v[56:59], a[204:207], v[88:91], v[56:59]\n\t"
  "ds_read_b64_tr_b16 v[82:83], v111 offset:38912\n\t"
  "ds_read_b128 a[200:203], v141 offset:10240\n\t"
  "v_mfma_f32_16x16x32_bf16 v[60:63], a[208:211], v[88:91], v[60:63]\n\t"
  "s_add_u32 s44, %[sH2ld], 4096\n\t"
  "v_mfma_f32_16x16x32_bf16 v[64:67], a[212:215], v[88:91], v[64:67]\n\t"
  "s_waitcnt lgkmcnt(9)\n\t"
  "ds_read_b128 a[204:207], v141 offset:11264\n\t"
  "v_mfma_f32_16x16x32_bf16 v[44:47], a[216:219], v[68:71], v[44:47]\n\t"
  "buffer_load_dwordx4 a[72:75], v1, %[srd], s44 offen offset:0 sc1\n\t"
  "ds_read_b128 a[208:211], v141 offset:12288\n\t"
  "v_mfma_f32_16x16x32_bf16 v[48:51], a[220:223], v[68:71], v[48:51]\n\t"
  "ds_read_b128 a[212:215], v141 offset:13312\n\t"
  "v_mfma_f32_16x16x32_bf16 v[52:55], a[224:227], v[68:71], v[52:55]\n\t"
  "buffer_load_dwordx4 a[76:79], v1, %[srd], s44 offen offset:1024 sc1\n\t"
  "v_mfma_f32_16x16x32_bf16 v[56:59], a[216:219], v[72:75], v[56:59]\n\t"
  "v_mfma_f32_16x16x32_bf16 v[60:63], a[220:223], v[72:75], v[60:63]\n\t"
  "buffer_load_dwordx4 a[80:83], v1, %[srd], s44 offen offset:2048 sc1\n\t"
  "v_mfma_f32_16x16x32_bf16 v[64:67], a[224:227], v[72:75], v[64:67]\n\t"
  "s_waitcnt lgkmcnt(4)\n\t"
  "ds_read_b128 a[216:219], v141 offset:14336\n\t"
  "v_mfma_f32_16x16x32_bf16 v[44:47], a[228:231], v[76:79], v[44:47]\n\t"
  "buffer_load_dwordx4 a[84:87], v1, %[srd], s44 offen offset:3072 sc1\n\t"
  "ds_read_b128 a[220:223], v141 offset:15360\n\t"
  "v_mfma_f32_16x16x32_bf16 v[48:51], a[232:235], v[76:79], v[48:51]\n\t"
  "ds_read_b128 a[224:227], v141 offset:16384\n\t"
  "v_mfma_f32_16x16x32_bf16 v[52:55], a[236:239], v[76:79], v[52:55]\n\t"
  "s_add_u32 s44, %[sH2ld], 8192\n\t"
  "v_mfma_f32_16x16x32_bf16 v[56:59], a[228:231], v[80:83], v[56:59]\n\t"
  "v_mfma_f32_16x16x32_bf16 v[60:63], a[232:235], v[80:83], v[60:63]\n\t"
  "buffer_load_dwordx4 a[88:91], v1, %[srd], s44 offen offset:0 sc1\n\t"
  "v_mfma_f32_16x16x32_bf16 v[64:67], a[236:239], v[80:83], v[64:67]\n\t"
  "ds_read_b128 a[228:231], v141 offset:17408\n\t"
  "ds_read_b128 a[232:235], v141 offset:18432\n\t"
  "v_mfma_f32_16x16x16_bf16 v[44:47], a[240:241], v[104:105], v[44:47]\n\t"
  "s_add_u32 s57, %[sFL], 8192\n\t"
  "buffer_load_dwordx2 v[138:139], v137, %[srd], s57 offen sc1\n\t"
  "v_mfma_f32_16x16x16_bf16 v[48:51], a[242:243], v[104:105], v[48:51]\n\t"
  "buffer_load_dwordx4 a[92:95], v1, %[srd], s44 offen offset:1024 sc1\n\t"
  "ds_read_b128 a[236:239], v141 offset:19456\n\t"
  "v_mfma_f32_16x16x16_bf16 v[52:55], a[244:245], v[104:105], v[52:55]\n\t"
  "buffer_load_dwordx4 a[96:99], v1, %[srd], s44 offen offset:2048 sc1\n\t"
  "v_mfma_f32_16x16x16_bf16 v[56:59], a[240:241], v[106:107], v[56:59]\n\t"
  "v_mfma_f32_16x16x16_bf16 v[60:63], a[242:243], v[106:107], v[60:63]\n\t"
  "buffer_load_dwordx4 a[100:103], v1, %[srd], s44 offen offset:3072 sc1\n\t"
  "v_mfma_f32_16x16x16_bf16 v[64:67], a[244:245], v[106:107], v[64:67]\n\t"
  "s_movk_i32 s56, 16384\n\t"
  "s_waitcnt vmcnt(15)\n\t"
  "buffer_store_dwordx4 v[132:135], v9, %[srd], s56 offen sc1\n\t"
  "v_max_f32 v44, 0, v44\n\t"
  "v_max_f32 v45, 0, v45\n\t"
  "v_pk_mul_f32 v[46:47], v[46:47], v[128:129] op_sel:[0,0] op_sel_hi:[1,0]\n\t"
  "v_pk_mul_f32 v[44:45], v[44:45], v[46:47]\n\t"
  "v_cvt_pk_bf16_f32 v116, v44, v45\n\t"
  "v_cvt_pk_bf16_f32 v112, v44, v45\n\t"
  "v_max_f32 v48, 0, v48\n\t"
  "v_max_f32 v49, 0, v49\n\t"
  "v_pk_mul_f32 v[50:51], v[50:51], v[128:129] op_sel:[0,0] op_sel_hi:[1,0]\n\t"
  "v_pk_mul_f32 v[48:49], v[48:49], v[50:51]\n\t"
  "v_cvt_pk_bf16_f32 v117, v48, v49\n\t"
  "v_cvt_pk_bf16_f32 v113, v48, v49\n\t"
  "v_max_f32 v52, 0, v52\n\t"
  "v_max_f32 v53, 0, v53\n\t"
  "v_pk_mul_f32 v[54:55], v[54:55], v[128:129] op_sel:[0,0] op_sel_hi:[1,0]\n\t"
  "v_permlane16_swap_b32 v116, v117\n\t"
  "v_pk_mul_f32 v[52:53], v[52:53], v[54:55]\n\t"
  "v_cvt_pk_bf16_f32 v118, v52, v53\n\t"
  "v_cvt_pk_bf16_f32 v114, v52, v53\n\t"
  "s_mov_b32 s55, 3244032\n\t"
  "v_permlane16_swap_b32 v118, v115\n\t"
  "s_nop 1\n\t"
  "v_permlane32_swap_b32 v117, v115\n\t"
  "buffer_store_dwordx4 v[112:115], v8, %[srd], s55 offen sc1\n\t"
  "v_max_f32 v56, 0, v56\n\t"
  "v_max_f32 v57, 0, v57\n\t"
  "v_pk_mul_f32 v[58:59], v[58:59], v[128:129] op_sel:[0,1] op_sel_hi:[1,1]\n\t"
  "v_pk_mul_f32 v[56:57], v[56:57], v[58:59]\n\t"
  "v_cvt_pk_bf16_f32 v124, v56, v57\n\t"
  "v_cvt_pk_bf16_f32 v120, v56, v57\n\t"
  "v_max_f32 v60, 0, v60\n\t"
  "v_max_f32 v61, 0, v61\n\t"
  "v_pk_mul_f32 v[62:63], v[62:63], v[128:129] op_sel:[0,1] op_sel_hi:[1,1]\n\t"
  "v_pk_mul_f32 v[60:61], v[60:61], v[62:63]\n\t"
  "v_cvt_pk_bf16_f32 v125, v60, v61\n\t"
  "v_cvt_pk_bf16_f32 v121, v60, v61\n\t"
  "v_max_f32 v64, 0, v64\n\t"
  "v_max_f32 v65, 0, v65\n\t"
  "v_pk_mul_f32 v[66:67], v[66:67], v[128:129] op_sel:[0,1] op_sel_hi:[1,1]\n\t"
  "v_permlane16_swap_b32 v124, v125\n\t"
  "v_pk_mul_f32 v[64:65], v[64:65], v[66:67]\n\t"
  "v_cvt_pk_bf16_f32 v126, v64, v65\n\t"
  "v_cvt_pk_bf16_f32 v122, v64, v65\n\t"
  "s_mov_b32 s55, 3293184\n\t"
  "v_permlane16_swap_b32 v126, v123\n\t"
  "s_nop 1\n\t"
  "v_permlane32_swap_b32 v125, v123\n\t"
  "buffer_store_dwordx4 v[120:123], v8, %[srd], s55 offen sc1\n\t"
  "s_waitcnt lgkmcnt(0)\n\t"
  "s_barrier\n\t"
  "s_waitcnt vmcnt(15)\n\t"
  "v_mfma_f32_16x16x32_bf16 v[20:23], v[144:147], a[56:59], 0\n\t"
  "v_lshrrev_b32 v63, 3, v1\n\t"
  "v_and_b32 v63, 0x60, v63\n\t"
  "v_mfma_f32_16x16x32_bf16 v[24:27], v[148:151], a[56:59], 0\n\t"
  "v_lshrrev_b32 v62, 1, v63\n\t"
  "buffer_load_dwordx4 v[68:71], v63, %[srd], %[sRT] offen offset:0 sc1\n\t"
  "v_mfma_f32_16x16x32_bf16 v[28:31], v[152:155], a[56:59], 0\n\t"
  "buffer_load_dwordx4 v[72:75], v63, %[srd], %[sRT] offen offset:16 sc1\n\t"
  "buffer_load_dwordx4 v[76:79], v63, %[srd], %[sRT] offen offset:128 sc1\n\t"
  "v_mfma_f32_16x16x32_bf16 v[32:35], v[156:159], a[56:59], 0\n\t"
  "buffer_load_dwordx4 v[80:83], v63, %[srd], %[sRT] offen offset:144 sc1\n\t"
  "buffer_load_dwordx4 v[84:87], v62, %[srd], %[sRT] offen offset:256 sc1\n\t"
  "v_mfma_f32_16x16x32_bf16 v[36:39], v[160:163], a[56:59], 0\n\t"
  "buffer_load_dwordx4 v[64:67], v17, %[srd], %[sRT] offen offset:0 sc1\n\t"
  "buffer_load_dwordx4 v[108:111], v17, %[srd], %[sRT] offen offset:16 sc1\n\t"
  "s_waitcnt vmcnt(21)\n\t"
  "v_mfma_f32_16x16x32_bf16 v[20:23], v[164:167], a[60:63], v[20:23]\n\t"
  "s_mul_i32 s48, %[w], -15360\n\t"
  "s_add_u32 s48, s48, 81920\n\t"
  "v_mfma_f32_16x16x32_bf16 v[24:27], v[168:171], a[60:63], v[24:27]\n\t"
  "v_add_u32 v62, s48, v13\n\t"
  "v_add_u32 v60, 61440, v14\n\t"
  "v_mfma_f32_16x16x32_bf16 v[28:31], v[172:175], a[60:63], v[28:31]\n\t"
  "v_add_u32 v61, 61440, v15\n\t"
  "v_accvgpr_read_b32 v89, a246\n\t"
  "v_mfma_f32_16x16x32_bf16 v[32:35], v[176:179], a[60:63], v[32:35]\n\t"
  "v_lshlrev_b32 v88, 16, v89\n\t"
  "v_and_b32 v89, 0xffff0000, v89\n\t"
  "v_mfma_f32_16x16x32_bf16 v[36:39], v[180:183], a[60:63], v[36:39]\n\t"
  "v_accvgpr_read_b32 v91, a247\n\t"
  "v_lshlrev_b32 v90, 16, v91\n\t"
  "s_waitcnt vmcnt(20)\n\t"
  "v_mfma_f32_16x16x32_bf16 v[20:23], v[184:187], a[64:67], v[20:23]\n\t"
  "v_and_b32 v91, 0xffff0000, v91\n\t"
  "v_accvgpr_read_b32 v93, a248\n\t"
  "v_mfma_f32_16x16x32_bf16 v[24:27], v[188:191], a[64:67], v[24:27]\n\t"
  "v_lshlrev_b32 v92, 16, v93\n\t"
  "v_and_b32 v93, 0xffff0000, v93\n\t"
  "v_mfma_f32_16x16x32_bf16 v[28:31], v[192:195], a[64:67], v[28:31]\n\t"
  "v_accvgpr_read_b32 v95, a249\n\t"
  "v_lshlrev_b32 v94, 16, v95\n\t"
  "v_mfma_f32_16x16x32_bf16 v[32:35], v[196:199], a[64:67], v[32:35]\n\t"
  "v_and_b32 v95, 0xffff0000, v95\n\t"
  "v_accvgpr_read_b32 v97, a250\n\t"
  "v_mfma_f32_16x16x32_bf16 v[36:39], v[200:203], a[64:67], v[36:39]\n\t"
  "s_waitcnt vmcnt(13)\n\t"
  "v_cmp_ne_u64 vcc, v[132:133], v[138:139]\n\t"
  "v_mfma_f32_16x16x32_bf16 v[20:23], v[204:207], a[68:71], v[20:23]\n\t"
  "v_lshlrev_b32 v96, 16, v97\n\t"
  "v_and_b32 v97, 0xffff0000, v97\n\t"
  "v_mfma_f32_16x16x32_bf16 v[24:27], v[208:211], a[68:71], v[24:27]\n\t"
  "s_cbranch_vccnz Lfl1_slow%=\n\t"
  "Lfl1_back%=:\n\t"
  "v_mfma_f32_16x16x32_bf16 v[28:31], v[212:215], a[68:71], v[28:31]\n\t"
  "v_accvgpr_read_b32 v99, a251\n\t"
  "v_lshlrev_b32 v98, 16, v99\n\t"
  "v_mfma_f32_16x16x32_bf16 v[32:35], v[216:219], a[68:71], v[32:35]\n\t"
  "s_add_u32 s45, %[sH2ld], 98304\n\t"
  "v_and_b32 v99, 0xffff0000, v99\n\t"
  "v_mfma_f32_16x16x32_bf16 v[36:39], v[220:223], a[68:71], v[36:39]\n\t"
  "v_accvgpr_read_b32 v101, a252\n\t"
  "v_lshlrev_b32 v100, 16, v101\n\t"
  "v_mfma_f32_16x16x32_bf16 v[20:23], v[224:227], a[72:75], v[20:23]\n\t"
  "v_and_b32 v101, 0xffff0000, v101\n\t"
  "v_accvgpr_read_b32 v103, a253\n\t"
  "v_mfma_f32_16x16x32_bf16 v[24:27], v[228:231], a[72:75], v[24:27]\n\t"
  "buffer_load_dwordx4 a[104:107], v1, %[srd], s45 offen offset:0 sc1\n\t"
  "v_lshlrev_b32 v102, 16, v103\n\t"
  "v_mfma_f32_16x16x32_bf16 v[28:31], v[232:235], a[72:75], v[28:31]\n\t"
  "v_and_b32 v103, 0xffff0000, v103\n\t"
  "v_accvgpr_read_b32 v105, a254\n\t"
  "v_mfma_f32_16x16x32_bf16 v[32:35], v[236:239], a[72:75], v[32:35]\n\t"
  "v_lshlrev_b32 v104, 16, v105\n\t"
  "v_and_b32 v105, 0xffff0000, v105\n\t"
  "v_mfma_f32_16x16x32_bf16 v[36:39], v[240:243], a[72:75], v[36:39]\n\t"
  "buffer_load_dwordx4 a[108:111], v1, %[srd], s45 offen offset:1024 sc1\n\t"
  "v_accvgpr_read_b32 v107, a255\n\t"
  "v_mfma_f32_16x16x32_bf16 v[20:23], v[244:247], a[76:79], v[20:23]\n\t"
  "v_lshlrev_b32 v106, 16, v107\n\t"
  "v_and_b32 v107, 0xffff0000, v107\n\t"
  "v_mfma_f32_16x16x32_bf16 v[24:27], a[160:163], a[76:79], v[24:27]\n\t"
  "s_movk_i32 s56, 24576\n\t"
  "s_waitcnt vmcnt(9)\n\t"
  "v_mfma_f32_16x16x32_bf16 v[28:31], a[164:167], a[76:79], v[28:31]\n\t"
  "buffer_store_dwordx4 v[132:135], v9, %[srd], s56 offen sc1\n\t"
  "buffer_load_dwordx4 a[112:115], v1, %[srd], s45 offen offset:2048 sc1\n\t"
  "v_mfma_f32_16x16x32_bf16 v[32:35], a[168:171], a[76:79], v[32:35]\n\t"
  "v_mfma_f32_16x16x32_bf16 v[36:39], a[172:175], a[76:79], v[36:39]\n\t"
  "v_mfma_f32_16x16x32_bf16 v[20:23], a[176:179], a[80:83], v[20:23]\n\t"
  "buffer_load_dwordx4 a[116:119], v1, %[srd], s45 offen offset:3072 sc1\n\t"
  "s_add_u32 s57, %[sFL], 16384\n\t"
  "v_mfma_f32_16x16x32_bf16 v[24:27], a[180:183], a[80:83], v[24:27]\n\t"
  "buffer_load_dwordx2 v[138:139], v137, %[srd], s57 offen sc1\n\t"
  "v_mfma_f32_16x16x32_bf16 v[28:31], a[184:187], a[80:83], v[28:31]\n\t"
  "v_mfma_f32_16x16x32_bf16 v[32:35], a[188:191], a[80:83], v[32:35]\n\t"
  "v_mfma_f32_16x16x32_bf16 v[36:39], a[192:195], a[80:83], v[36:39]\n\t"
  "s_add_u32 s45, %[sH2ld], 102400\n\t"
  "v_mfma_f32_16x16x32_bf16 v[20:23], a[196:199], a[84:87], v[20:23]\n\t"
  "v_mfma_f32_16x16x32_bf16 v[24:27], a[200:203], a[84:87], v[24:27]\n\t"
  "v_mfma_f32_16x16x32_bf16 v[28:31], a[204:207], a[84:87], v[28:31]\n\t"
  "buffer_load_dwordx4 a[120:123], v1, %[srd], s45 offen offset:0 sc1\n\t"
  "v_mfma_f32_16x16x32_bf16 v[32:35], a[208:211], a[84:87], v[32:35]\n\t"
  "v_mfma_f32_16x16x32_bf16 v[36:39], a[212:215], a[84:87], v[36:39]\n\t"
  "v_mfma_f32_16x16x32_bf16 v[20:23], a[216:219], a[88:91], v[20:23]\n\t"
  "buffer_load_dwordx4 a[124:127], v1, %[srd], s45 offen offset:1024 sc1\n\t"
  "v_mfma_f32_16x16x32_bf16 v[24:27], a[220:223], a[88:91], v[24:27]\n\t"
  "s_waitcnt vmcnt(14)\n\t"
  "v_mul_f32 v88, v88, v68\n\t"
  "v_mfma_f32_16x16x32_bf16 v[28:31], a[224:227], a[88:91], v[28:31]\n\t"
  "v_mul_f32 v89, v89, v69\n\t"
  "v_mul_f32 v90, v90, v70\n\t"
  "v_mfma_f32_16x16x32_bf16 v[32:35], a[228:231], a[88:91], v[32:35]\n\t"
  "buffer_load_dwordx4 a[128:131], v1, %[srd], s45 offen offset:2048 sc1\n\t"
  "v_mul_f32 v91, v91, v71\n\t"
  "v_mfma_f32_16x16x32_bf16 v[36:39], a[232:235], a[88:91], v[36:39]\n\t"
  "ds_write_b128 v62, v[88:91] offset:0\n\t"
  "s_waitcnt vmcnt(14)\n\t"
  "v_mul_f32 v92, v92, v72\n\t"
  "v_mfma_f32_16x16x32_bf16 v[20:23], a[236:239], a[92:95], v[20:23]\n\t"
  "v_mul_f32 v93, v93, v73\n\t"
  "v_mul_f32 v94, v94, v74\n\t"
  "v_mfma_f32_16x16x32_bf16 v[24:27], a[0:3], a[92:95], v[24:27]\n\t"
  "buffer_load_dwordx4 a[132:135], v1, %[srd], s45 offen offset:3072 sc1\n\t"
  "v_mul_f32 v95, v95, v75\n\t"
  "v_mfma_f32_16x16x32_bf16 v[28:31], a[4:7], a[92:95], v[28:31]\n\t"
  "ds_write_b128 v62, v[92:95] offset:1024\n\t"
  "s_waitcnt vmcnt(14)\n\t"
  "v_mul_f32 v96, v96, v76\n\t"
  "v_mfma_f32_16x16x32_bf16 v[32:35], a[8:11], a[92:95], v[32:35]\n\t"
  "v_mul_f32 v97, v97, v77\n\t"
  "v_mul_f32 v98, v98, v78\n\t"
  "v_mfma_f32_16x16x32_bf16 v[36:39], a[12:15], a[92:95], v[36:39]\n\t"
  "v_mul_f32 v99, v99, v79\n\t"
  "ds_write_b128 v62, v[96:99] offset:2048\n\t"
  "v_mfma_f32_16x16x32_bf16 v[20:23], a[16:19], a[96:99], v[20:23]\n\t"
  "s_add_u32 s45, %[sH2ld], 106496\n\t"
  "s_waitcnt vmcnt(13)\n\t"
  "v_mul_f32 v100, v100, v80\n\t"
  "v_mfma_f32_16x16x32_bf16 v[24:27], a[20:23], a[96:99], v[24:27]\n\t"
  "v_mul_f32 v101, v101, v81\n\t"
  "v_mul_f32 v102, v102, v82\n\t"
  "v_mfma_f32_16x16x32_bf16 v[28:31], a[24:27], a[96:99], v[28:31]\n\t"
  "v_mul_f32 v103, v103, v83\n\t"
  "ds_write_b128 v62, v[100:103] offset:3072\n\t"
  "v_mfma_f32_16x16x32_bf16 v[32:35], a[28:31], a[96:99], v[32:35]\n\t"
  "buffer_load_dwordx4 a[136:139], v1, %[srd], s45 offen offset:0 sc1\n\t"
  "s_waitcnt vmcnt(13)\n\t"
  "v_mul_f32 v104, v104, v84\n\t"
  "v_mfma_f32_16x16x32_bf16 v[36:39], a[32:35], a[96:99], v[36:39]\n\t"
  "v_mul_f32 v105, v105, v85\n\t"
  "v_mul_f32 v106, v106, v86\n\t"
  "v_mfma_f32_16x16x32_bf16 v[20:23], a[36:39], a[100:103], v[20:23]\n\t"
  "v_mul_f32 v107, v107, v87\n\t"
  "ds_write_b128 v62, v[104:107] offset:4096\n\t"
  "v_mfma_f32_16x16x32_bf16 v[24:27], a[40:43], a[100:103], v[24:27]\n\t"
  "buffer_load_dwordx4 a[140:143], v1, %[srd], s45 offen offset:1024 sc1\n\t"
  "v_mfma_f32_16x16x32_bf16 v[28:31], a[44:47], a[100:103], v[28:31]\n\t"
  "v_mfma_f32_16x16x32_bf16 v[32:35], a[48:51], a[100:103], v[32:35]\n\t"
  "v_mfma_f32_16x16x32_bf16 v[36:39], a[52:55], a[100:103], v[36:39]\n\t"
  "s_waitcnt vmcnt(10)\n\t"
  "buffer_load_dwordx4 a[144:147], v1, %[srd], s45 offen offset:2048 sc1\n\t"
  "v_mfma_f32_16x16x32_bf16 v[40:43], v[144:147], a[104:107], 0\n\t"
  "v_mfma_f32_16x16x32_bf16 v[44:47], v[148:151], a[104:107], 0\n\t"
  "v_mfma_f32_16x16x32_bf16 v[48:51], v[152:155], a[104:107], 0\n\t"
  "buffer_load_dwordx4 a[148:151], v1, %[srd], s45 offen offset:3072 sc1\n\t"
  "v_mfma_f32_16x16x32_bf16 v[52:55], v[156:159], a[104:107], 0\n\t"
  "ds_write_b128 v13, v[20:23] offset:0\n\t"
  "v_mfma_f32_16x16x32_bf16 v[56:59], v[160:163], a[104:107], 0\n\t"
  "v_mfma_f32_16x16x32_bf16 v[40:43], v[164:167], a[108:111], v[40:43]\n\t"
  "v_mfma_f32_16x16x32_bf16 v[44:47], v[168:171], a[108:111], v[44:47]\n\t"
  "ds_write_b128 v13, v[24:27] offset:1024\n\t"
  "v_mfma_f32_16x16x32_bf16 v[48:51], v[172:175], a[108:111], v[48:51]\n\t"
  "v_mfma_f32_16x16x32_bf16 v[52:55], v[176:179], a[108:111], v[52:55]\n\t"
  "v_mfma_f32_16x16x32_bf16 v[56:59], v[180:183], a[108:111], v[56:59]\n\t"
  "s_waitcnt vmcnt(9)\n\t"
  "ds_write_b128 v13, v[28:31] offset:2048\n\t"
  "v_mfma_f32_16x16x32_bf16 v[40:43], v[184:187], a[112:115], v[40:43]\n\t"
  "v_mfma_f32_16x16x32_bf16 v[44:47], v[188:191], a[112:115], v[44:47]\n\t"
  "v_mfma_f32_16x16x32_bf16 v[48:51], v[192:195], a[112:115], v[48:51]\n\t"
  "ds_write_b128 v13, v[32:35] offset:3072\n\t"
  "v_mfma_f32_16x16x32_bf16 v[52:55], v[196:199], a[112:115], v[52:55]\n\t"
  "v_mfma_f32_16x16x32_bf16 v[56:59], v[200:203], a[112:115], v[56:59]\n\t"
  "v_mfma_f32_16x16x32_bf16 v[40:43], v[204:207], a[116:119], v[40:43]\n\t"
  "ds_write_b128 v13, v[36:39] offset:4096\n\t"
  "v_mfma_f32_16x16x32_bf16 v[44:47], v[208:211], a[116:119], v[44:47]\n\t"
  "v_mfma_f32_16x16x32_bf16 v[48:51], v[212:215], a[116:119], v[48:51]\n\t"
  "v_mfma_f32_16x16x32_bf16 v[52:55], v[216:219], a[116:119], v[52:55]\n\t"
  "v_mfma_f32_16x16x32_bf16 v[56:59], v[220:223], a[116:119], v[56:59]\n\t"
  "s_waitcnt vmcnt(6)\n\t"
  "v_mfma_f32_16x16x32_bf16 v[40:43], v[224:227], a[120:123], v[40:43]\n\t"
  "v_mfma_f32_16x16x32_bf16 v[44:47], v[228:231], a[120:123], v[44:47]\n\t"
  "v_cmp_ne_u64 vcc, v[132:133], v[138:139]\n\t"
  "v_mfma_f32_16x16x32_bf16 v[48:51], v[232:235], a[120:123], v[48:51]\n\t"
  "v_mfma_f32_16x16x32_bf16 v[52:55], v[236:239], a[120:123], v[52:55]\n\t"
  "s_cbranch_vccnz Lfl2_slow%=\n\t"
  "Lfl2_back%=:\n\t"
  "v_mfma_f32_16x16x32_bf16 v[56:59], v[240:243], a[120:123], v[56:59]\n\t"
  "s_add_u32 s44, %[sH2ld], 3145728\n\t"
  "s_waitcnt lgkmcnt(0)\n\t"
  "s_barrier\n\t"
  "v_mfma_f32_16x16x32_bf16 v[40:43], v[244:247], a[124:127], v[40:43]\n\t"
  "ds_read_b128 v[68:71], v14 offset:0\n\t"
  "ds_read_b128 v[72:75], v15 offset:0\n\t"
  "v_mfma_f32_16x16x32_bf16 v[44:47], a[160:163], a[124:127], v[44:47]\n\t"
  "ds_read_b128 v[76:79], v14 offset:20480\n\t"
  "ds_read_b128 v[80:83], v15 offset:20480\n\t"
  "v_mfma_f32_16x16x32_bf16 v[48:51], a[164:167], a[124:127], v[48:51]\n\t"
  "buffer_load_dwordx4 a[152:155], v1, %[srd], s44 offen offset:0 sc1\n\t"
  "ds_read_b128 v[84:87], v14 offset:40960\n\t"
  "v_mfma_f32_16x16x32_bf16 v[52:55], a[168:171], a[124:127], v[52:55]\n\t"
  "ds_read_b128 v[88:91], v15 offset:40960\n\t"
  "ds_read_b128 v[92:95], v60 offset:0\n\t"
  "v_mfma_f32_16x16x32_bf16 v[56:59], a[172:175], a[124:127], v[56:59]\n\t"
  "s_waitcnt vmcnt(5)\n\t"
  "ds_read_b128 v[96:99], v61 offset:0\n\t"
  "v_mfma_f32_16x16x32_bf16 v[40:43], a[176:179], a[128:131], v[40:43]\n\t"
  "buffer_load_dwordx4 a[156:159], v1, %[srd], s44 offen offset:1024 sc1\n\t"
  "s_add_u32 s57, %[sFL], 24576\n\t"
  "v_mfma_f32_16x16x32_bf16 v[44:47], a[180:183], a[128:131], v[44:47]\n\t"
  "buffer_load_dwordx2 v[138:139], v137, %[srd], s57 offen sc1\n\t"
  "ds_read_b128 v[100:103], v60 offset:20480\n\t"
  "v_mfma_f32_16x16x32_bf16 v[48:51], a[184:187], a[128:131], v[48:51]\n\t"
  "ds_read_b128 v[104:107], v61 offset:20480\n\t"
  "v_mfma_f32_16x16x32_bf16 v[52:55], a[188:191], a[128:131], v[52:55]\n\t"
  "buffer_load_dwordx4 v[112:115], v1, %[srd], s44 offen offset:2048 sc1\n\t"
  "v_mfma_f32_16x16x32_bf16 v[56:59], a[192:195], a[128:131], v[56:59]\n\t"
  "v_mfma_f32_16x16x32_bf16 v[40:43], a[196:199], a[132:135], v[40:43]\n\t"
  "v_mfma_f32_16x16x32_bf16 v[44:47], a[200:203], a[132:135], v[44:47]\n\t"
  "buffer_load_dwordx4 v[116:119], v1, %[srd], s44 offen offset:3072 sc1\n\t"
  "v_mfma_f32_16x16x32_bf16 v[48:51], a[204:207], a[132:135], v[48:51]\n\t"
  "v_mfma_f32_16x16x32_bf16 v[52:55], a[208:211], a[132:135], v[52:55]\n\t"
  "v_mfma_f32_16x16x32_bf16 v[56:59], a[212:215], a[132:135], v[56:59]\n\t"
  "s_waitcnt vmcnt(7)\n\t"
  "v_mfma_f32_16x16x32_bf16 v[40:43], a[216:219], a[136:139], v[40:43]\n\t"
  "s_add_u32 s44, %[sH2ld], 3149824\n\t"
  "s_waitcnt lgkmcnt(0)\n\t"
  "v_mfma_f32_16x16x32_bf16 v[44:47], a[220:223], a[136:139], v[44:47]\n\t"
  "v_add_f32 v68, v68, v76\n\t"
  "v_add_f32 v69, v69, v77\n\t"
  "v_mfma_f32_16x16x32_bf16 v[48:51], a[224:227], a[136:139], v[48:51]\n\t"
  "v_add_f32 v70, v70, v78\n\t"
  "v_add_f32 v71, v71, v79\n\t"
  "v_mfma_f32_16x16x32_bf16 v[52:55], a[228:231], a[136:139], v[52:55]\n\t"
  "buffer_load_dwordx4 v[120:123], v1, %[srd], s44 offen offset:0 sc1\n\t"
  "v_add_f32 v72, v72, v80\n\t"
  "v_mfma_f32_16x16x32_bf16 v[56:59], a[232:235], a[136:139], v[56:59]\n\t"
  "v_add_f32 v73, v73, v81\n\t"
  "v_add_f32 v74, v74, v82\n\t"
  "v_mfma_f32_16x16x32_bf16 v[40:43], a[236:239], a[140:143], v[40:43]\n\t"
  "v_add_f32 v75, v75, v83\n\t"
  "v_add_f32 v84, v84, v92\n\t"
  "v_mfma_f32_16x16x32_bf16 v[44:47], a[0:3], a[140:143], v[44:47]\n\t"
  "buffer_load_dwordx4 v[124:127], v1, %[srd], s44 offen offset:1024 sc1\n\t"
  "v_add_f32 v85, v85, v93\n\t"
  "v_mfma_f32_16x16x32_bf16 v[48:51], a[4:7], a[140:143], v[48:51]\n\t"
  "v_add_f32 v86, v86, v94\n\t"
  "v_add_f32 v87, v87, v95\n\t"
  "v_mfma_f32_16x16x32_bf16 v[52:55], a[8:11], a[140:143], v[52:55]\n\t"
  "v_add_f32 v88, v88, v96\n\t"
  "v_add_f32 v89, v89, v97\n\t"
  "v_mfma_f32_16x16x32_bf16 v[56:59], a[12:15], a[140:143], v[56:59]\n\t"
  "s_waitcnt vmcnt(7)\n\t"
  "buffer_load_dwordx4 v[128:131], v1, %[srd], s44 offen offset:2048 sc1\n\t"
  "v_mfma_f32_16x16x32_bf16 v[40:43], a[16:19], a[144:147], v[40:43]\n\t"
  "v_add_f32 v90, v90, v98\n\t"
  "v_add_f32 v91, v91, v99\n\t"
  "v_mfma_f32_16x16x32_bf16 v[44:47], a[20:23], a[144:147], v[44:47]\n\t"
  "v_add_f32 v68, v68, v84\n\t"
  "v_add_f32 v69, v69, v85\n\t"
  "v_mfma_f32_16x16x32_bf16 v[48:51], a[24:27], a[144:147], v[48:51]\n\t"
  "buffer_load_dwordx4 a[240:243], v1, %[srd], s44 offen offset:3072 sc1\n\t"
  "v_add_f32 v70, v70, v86\n\t"
  "v_mfma_f32_16x16x32_bf16 v[52:55], a[28:31], a[144:147], v[52:55]\n\t"
  "v_add_f32 v71, v71, v87\n\t"
  "v_add_f32 v72, v72, v88\n\t"
  "v_mfma_f32_16x16x32_bf16 v[56:59], a[32:35], a[144:147], v[56:59]\n\t"
  "v_add_f32 v73, v73, v89\n\t"
  "v_add_f32 v74, v74, v90\n\t"
  "v_mfma_f32_16x16x32_bf16 v[40:43], a[36:39], a[148:151], v[40:43]\n\t"
  "v_add_f32 v75, v75, v91\n\t"
  "v_fma_f32 v68, v68, v64, v100\n\t"
  "v_mfma_f32_16x16x32_bf16 v[44:47], a[40:43], a[148:151], v[44:47]\n\t"
  "s_add_u32 s44, %[sH2ld], 3153920\n\t"
  "v_fma_f32 v69, v69, v65, v101\n\t"
  "v_mfma_f32_16x16x32_bf16 v[48:51], a[44:47], a[148:151], v[48:51]\n\t"
  "v_fma_f32 v70, v70, v66, v102\n\t"
  "v_fma_f32 v71, v71, v67, v103\n\t"
  "v_mfma_f32_16x16x32_bf16 v[52:55], a[48:51], a[148:151], v[52:55]\n\t"
  "v_fma_f32 v72, v72, v108, v104\n\t"
  "v_fma_f32 v73, v73, v109, v105\n\t"
  "v_mfma_f32_16x16x32_bf16 v[56:59], a[52:55], a[148:151], v[56:59]\n\t"
  "s_waitcnt vmcnt(7)\n\t"
  "buffer_load_dwordx4 v[248:251], v1, %[srd], s44 offen offset:0 sc1\n\t"
  "v_mfma_f32_16x16x32_bf16 v[20:23], v[144:147], a[152:155], 0\n\t"
  "v_fma_f32 v74, v74, v110, v106\n\t"
  "v_fma_f32 v75, v75, v111, v107\n\t"
  "v_mfma_f32_16x16x32_bf16 v[24:27], v[148:151], a[152:155], 0\n\t"
  "v_cvt_pk_bf16_f32 v76, v68, v69\n\t"
  "v_cvt_pk_bf16_f32 v77, v70, v71\n\t"
  "v_mfma_f32_16x16x32_bf16 v[28:31], v[152:155], a[152:155], 0\n\t"
  "buffer_load_dwordx4 v[252:255], v1, %[srd], s44 offen offset:1024 sc1\n\t"
  "v_cvt_pk_bf16_f32 v78, v72, v73\n\t"
  "v_mfma_f32_16x16x32_bf16 v[32:35], v[156:159], a[152:155], 0\n\t"
  "v_cvt_pk_bf16_f32 v79, v74, v75\n\t"
  "s_mov_b32 s55, 0\n\t"
  "v_mfma_f32_16x16x32_bf16 v[36:39], v[160:163], a[152:155], 0\n\t"
  "buffer_store_dwordx4 v[76:79], v16, %[srdo], s55 offen\n\t"
  "ds_write_b128 v13, v[40:43] offset:5120\n\t"
  "v_mfma_f32_16x16x32_bf16 v[20:23], v[164:167], a[156:159], v[20:23]\n\t"
  "buffer_load_dwordx4 a[56:59], v1, %[srd], s44 offen offset:2048 sc1\n\t"
  "v_mfma_f32_16x16x32_bf16 v[24:27], v[168:171], a[156:159], v[24:27]\n\t"
  "v_mfma_f32_16x16x32_bf16 v[28:31], v[172:175], a[156:159], v[28:31]\n\t"
  "ds_write_b128 v13, v[44:47] offset:6144\n\t"
  "v_mfma_f32_16x16x32_bf16 v[32:35], v[176:179], a[156:159], v[32:35]\n\t"
  "buffer_load_dwordx4 a[60:63], v1, %[srd], s44 offen offset:3072 sc1\n\t"
  "v_mfma_f32_16x16x32_bf16 v[36:39], v[180:183], a[156:159], v[36:39]\n\t"
  "s_waitcnt vmcnt(9)\n\t"
  "v_mfma_f32_16x16x32_bf16 v[20:23], v[184:187], v[112:115], v[20:23]\n\t"
  "ds_write_b128 v13, v[48:51] offset:7168\n\t"
  "v_mfma_f32_16x16x32_bf16 v[24:27], v[188:191], v[112:115], v[24:27]\n\t"
  "v_mfma_f32_16x16x32_bf16 v[28:31], v[192:195], v[112:115], v[28:31]\n\t"
  "v_mfma_f32_16x16x32_bf16 v[32:35], v[196:199], v[112:115], v[32:35]\n\t"
  "ds_write_b128 v13, v[52:55] offset:8192\n\t"
  "v_mfma_f32_16x16x32_bf16 v[36:39], v[200:203], v[112:115], v[36:39]\n\t"
  "v_mfma_f32_16x16x32_bf16 v[20:23], v[204:207], v[116:119], v[20:23]\n\t"
  "v_mfma_f32_16x16x32_bf16 v[24:27], v[208:211], v[116:119], v[24:27]\n\t"
  "ds_write_b128 v13, v[56:59] offset:9216\n\t"
  "v_mfma_f32_16x16x32_bf16 v[28:31], v[212:215], v[116:119], v[28:31]\n\t"
  "v_mfma_f32_16x16x32_bf16 v[32:35], v[216:219], v[116:119], v[32:35]\n\t"
  "v_mfma_f32_16x16x32_bf16 v[36:39], v[220:223], v[116:119], v[36:39]\n\t"
  "s_waitcnt vmcnt(7)\n\t"
  "v_mfma_f32_16x16x32_bf16 v[20:23], v[224:227], v[120:123], v[20:23]\n\t"
  "v_mfma_f32_16x16x32_bf16 v[24:27], v[228:231], v[120:123], v[24:27]\n\t"
  "v_cmp_ne_u64 vcc, v[132:133], v[138:139]\n\t"
  "v_mfma_f32_16x16x32_bf16 v[28:31], v[232:235], v[120:123], v[28:31]\n\t"
  "v_mfma_f32_16x16x32_bf16 v[32:35], v[236:239], v[120:123], v[32:35]\n\t"
  "s_cbranch_vccnz Lfl3_slow%=\n\t"
  "Lfl3_back%=:\n\t"
  "v_mfma_f32_16x16x32_bf16 v[36:39], v[240:243], v[120:123], v[36:39]\n\t"
  "s_add_u32 s45, %[sH2ld], 3244032\n\t"
  "s_waitcnt lgkmcnt(0)\n\t"
  "s_barrier\n\t"
  "v_mfma_f32_16x16x32_bf16 v[20:23], v[244:247], v[124:127], v[20:23]\n\t"
  "ds_read_b128 v[68:71], v14 offset:5120\n\t"
  "ds_read_b128 v[72:75], v15 offset:5120\n\t"
  "v_mfma_f32_16x16x32_bf16 v[24:27], a[160:163], v[124:127], v[24:27]\n\t"
  "buffer_load_dwordx4 a[104:107], v1, %[srd], s45 offen offset:0 sc1\n\t"
  "ds_read_b128 v[76:79], v14 offset:25600\n\t"
  "v_mfma_f32_16x16x32_bf16 v[28:31], a[164:167], v[124:127], v[28:31]\n\t"
  "ds_read_b128 v[80:83], v15 offset:25600\n\t"
  "ds_read_b128 v[84:87], v14 offset:46080\n\t"
  "v_mfma_f32_16x16x32_bf16 v[32:35], a[168:171], v[124:127], v[32:35]\n\t"
  "buffer_load_dwordx4 a[108:111], v1, %[srd], s45 offen offset:1024 sc1\n\t"
  "ds_read_b128 v[88:91], v15 offset:46080\n\t"
  "v_mfma_f32_16x16x32_bf16 v[36:39], a[172:175], v[124:127], v[36:39]\n\t"
  "s_waitcnt vmcnt(7)\n\t"
  "ds_read_b128 v[92:95], v60 offset:5120\n\t"
  "v_mfma_f32_16x16x32_bf16 v[20:23], a[176:179], v[128:131], v[20:23]\n\t"
  "ds_read_b128 v[96:99], v61 offset:5120\n\t"
  "ds_read_b128 v[100:103], v60 offset:25600\n\t"
  "v_mfma_f32_16x16x32_bf16 v[24:27], a[180:183], v[128:131], v[24:27]\n\t"
  "buffer_load_dwordx4 a[64:67], v1, %[srd], s45 offen offset:2048 sc1\n\t"
  "ds_read_b128 v[104:107], v61 offset:25600\n\t"
  "v_mfma_f32_16x16x32_bf16 v[28:31], a[184:187], v[128:131], v[28:31]\n\t"
  "v_mfma_f32_16x16x32_bf16 v[32:35], a[188:191], v[128:131], v[32:35]\n\t"
  "buffer_load_dwordx4 a[68:71], v1, %[srd], s45 offen offset:3072 sc1\n\t"
  "v_mfma_f32_16x16x32_bf16 v[36:39], a[192:195], v[128:131], v[36:39]\n\t"
  "v_mfma_f32_16x16x32_bf16 v[20:23], a[196:199], a[240:243], v[20:23]\n\t"
  "v_mfma_f32_16x16x32_bf16 v[24:27], a[200:203], a[240:243], v[24:27]\n\t"
  "s_add_u32 s45, %[sH2ld], 3248128\n\t"
  "v_mfma_f32_16x16x32_bf16 v[28:31], a[204:207], a[240:243], v[28:31]\n\t"
  "v_mfma_f32_16x16x32_bf16 v[32:35], a[208:211], a[240:243], v[32:35]\n\t"
  "buffer_load_dwordx4 a[72:75], v1, %[srd], s45 offen offset:0 sc1\n\t"
  "v_mfma_f32_16x16x32_bf16 v[36:39], a[212:215], a[240:243], v[36:39]\n\t"
  "s_waitcnt vmcnt(8)\n\t"
  "s_waitcnt lgkmcnt(0)\n\t"
  "v_mfma_f32_16x16x32_bf16 v[20:23], a[216:219], v[248:251], v[20:23]\n\t"
  "buffer_load_dwordx4 a[76:79], v1, %[srd], s45 offen offset:1024 sc1\n\t"
  "v_add_f32 v68, v68, v76\n\t"
  "v_mfma_f32_16x16x32_bf16 v[24:27], a[220:223], v[248:251], v[24:27]\n\t"
  "v_add_f32 v69, v69, v77\n\t"
  "v_add_f32 v70, v70, v78\n\t"
  "v_mfma_f32_16x16x32_bf16 v[28:31], a[224:227], v[248:251], v[28:31]\n\t"
  "v_add_f32 v71, v71, v79\n\t"
  "v_add_f32 v72, v72, v80\n\t"
  "v_mfma_f32_16x16x32_bf16 v[32:35], a[228:231], v[248:251], v[32:35]\n\t"
  "buffer_load_dwordx4 a[80:83], v1, %[srd], s45 offen offset:2048 sc1\n\t"
  "v_add_f32 v73, v73, v81\n\t"
  "v_mfma_f32_16x16x32_bf16 v[36:39], a[232:235], v[248:251], v[36:39]\n\t"
  "v_add_f32 v74, v74, v82\n\t"
  "v_add_f32 v75, v75, v83\n\t"
  "v_mfma_f32_16x16x32_bf16 v[20:23], a[236:239], v[252:255], v[20:23]\n\t"
  "buffer_load_dwordx4 a[84:87], v1, %[srd], s45 offen offset:3072 sc1\n\t"
  "v_add_f32 v84, v84, v92\n\t"
  "v_mfma_f32_16x16x32_bf16 v[24:27], a[0:3], v[252:255], v[24:27]\n\t"
  "v_add_f32 v85, v85, v93\n\t"
  "v_add_f32 v86, v86, v94\n\t"
  "v_mfma_f32_16x16x32_bf16 v[28:31], a[4:7], v[252:255], v[28:31]\n\t"
  "v_add_f32 v87, v87, v95\n\t"
  "v_add_f32 v88, v88, v96\n\t"
  "v_mfma_f32_16x16x32_bf16 v[32:35], a[8:11], v[252:255], v[32:35]\n\t"
  "s_add_u32 s45, %[sH2ld], 3252224\n\t"
  "v_add_f32 v89, v89, v97\n\t"
  "v_mfma_f32_16x16x32_bf16 v[36:39], a[12:15], v[252:255], v[36:39]\n\t"
  "s_waitcnt vmcnt(8)\n\t"
  "v_add_f32 v90, v90, v98\n\t"
  "v_mfma_f32_16x16x32_bf16 v[20:23], a[16:19], a[56:59], v[20:23]\n\t"
  "buffer_load_dwordx4 a[88:91], v1, %[srd], s45 offen offset:0 sc1\n\t"
  "v_add_f32 v91, v91, v99\n\t"
  "v_mfma_f32_16x16x32_bf16 v[24:27], a[20:23], a[56:59], v[24:27]\n\t"
  "v_add_f32 v68, v68, v84\n\t"
  "v_add_f32 v69, v69, v85\n\t"
  "v_mfma_f32_16x16x32_bf16 v[28:31], a[24:27], a[56:59], v[28:31]\n\t"
  "buffer_load_dwordx4 a[92:95], v1, %[srd], s45 offen offset:1024 sc1\n\t"
  "v_add_f32 v70, v70, v86\n\t"
  "v_mfma_f32_16x16x32_bf16 v[32:35], a[28:31], a[56:59], v[32:35]\n\t"
  "v_add_f32 v71, v71, v87\n\t"
  "v_add_f32 v72, v72, v88\n\t"
  "v_mfma_f32_16x16x32_bf16 v[36:39], a[32:35], a[56:59], v[36:39]\n\t"
  "v_add_f32 v73, v73, v89\n\t"
  "v_add_f32 v74, v74, v90\n\t"
  "v_mfma_f32_16x16x32_bf16 v[20:23], a[36:39], a[60:63], v[20:23]\n\t"
  "buffer_load_dwordx4 a[96:99], v1, %[srd], s45 offen offset:2048 sc1\n\t"
  "v_add_f32 v75, v75, v91\n\t"
  "v_mfma_f32_16x16x32_bf16 v[24:27], a[40:43], a[60:63], v[24:27]\n\t"
  "v_fma_f32 v68, v68, v64, v100\n\t"
  "v_fma_f32 v69, v69, v65, v101\n\t"
  "v_mfma_f32_16x16x32_bf16 v[28:31], a[44:47], a[60:63], v[28:31]\n\t"
  "buffer_load_dwordx4 a[100:103], v1, %[srd], s45 offen offset:3072 sc1\n\t"
  "v_fma_f32 v70, v70, v66, v102\n\t"
  "v_mfma_f32_16x16x32_bf16 v[32:35], a[48:51], a[60:63], v[32:35]\n\t"
  "v_fma_f32 v71, v71, v67, v103\n\t"
  "v_fma_f32 v72, v72, v108, v104\n\t"
  "v_mfma_f32_16x16x32_bf16 v[36:39], a[52:55], a[60:63], v[36:39]\n\t"
  "s_waitcnt vmcnt(10)\n\t"
  "v_fma_f32 v73, v73, v109, v105\n\t"
  "v_mfma_f32_16x16x32_bf16 v[40:43], v[144:147], a[104:107], 0\n\t"
  "v_fma_f32 v74, v74, v110, v106\n\t"
  "v_fma_f32 v75, v75, v111, v107\n\t"
  "v_mfma_f32_16x16x32_bf16 v[44:47], v[148:151], a[104:107], 0\n\t"
  "v_cvt_pk_bf16_f32 v76, v68, v69\n\t"
  "v_cvt_pk_bf16_f32 v77, v70, v71\n\t"
  "v_mfma_f32_16x16x32_bf16 v[48:51], v[152:155], a[104:107], 0\n\t"
  "v_cvt_pk_bf16_f32 v78, v72, v73\n\t"
  "v_cvt_pk_bf16_f32 v79, v74, v75\n\t"
  "v_mfma_f32_16x16x32_bf16 v[52:55], v[156:159], a[104:107], 0\n\t"
  "s_mov_b32 s55, 40960\n\t"
  "buffer_store_dwordx4 v[76:79], v16, %[srdo], s55 offen\n\t"
  "v_mfma_f32_16x16x32_bf16 v[56:59], v[160:163], a[104:107], 0\n\t"
  "ds_write_b128 v13, v[20:23] offset:10240\n\t"
  "v_mfma_f32_16x16x32_bf16 v[40:43], v[164:167], a[108:111], v[40:43]\n\t"
  "v_mfma_f32_16x16x32_bf16 v[44:47], v[168:171], a[108:111], v[44:47]\n\t"
  "v_mfma_f32_16x16x32_bf16 v[48:51], v[172:175], a[108:111], v[48:51]\n\t"
  "ds_write_b128 v13, v[24:27] offset:11264\n\t"
  "v_mfma_f32_16x16x32_bf16 v[52:55], v[176:179], a[108:111], v[52:55]\n\t"
  "v_mfma_f32_16x16x32_bf16 v[56:59], v[180:183], a[108:111], v[56:59]\n\t"
  "s_waitcnt vmcnt(9)\n\t"
  "v_mfma_f32_16x16x32_bf16 v[40:43], v[184:187], a[64:67], v[40:43]\n\t"
  "ds_write_b128 v13, v[28:31] offset:12288\n\t"
  "v_mfma_f32_16x16x32_bf16 v[44:47], v[188:191], a[64:67], v[44:47]\n\t"
  "v_mfma_f32_16x16x32_bf16 v[48:51], v[192:195], a[64:67], v[48:51]\n\t"
  "v_mfma_f32_16x16x32_bf16 v[52:55], v[196:199], a[64:67], v[52:55]\n\t"
  "ds_write_b128 v13, v[32:35] offset:13312\n\t"
  "v_mfma_f32_16x16x32_bf16 v[56:59], v[200:203], a[64:67], v[56:59]\n\t"
  "v_mfma_f32_16x16x32_bf16 v[40:43], v[204:207], a[68:71], v[40:43]\n\t"
  "v_mfma_f32_16x16x32_bf16 v[44:47], v[208:211], a[68:71], v[44:47]\n\t"
  "ds_write_b128 v13, v[36:39] offset:14336\n\t"
  "v_mfma_f32_16x16x32_bf16 v[48:51], v[212:215], a[68:71], v[48:51]\n\t"
  "v_mfma_f32_16x16x32_bf16 v[52:55], v[216:219], a[68:71], v[52:55]\n\t"
  "v_mfma_f32_16x16x32_bf16 v[56:59], v[220:223], a[68:71], v[56:59]\n\t"
  "s_waitcnt vmcnt(7)\n\t"
  "v_mfma_f32_16x16x32_bf16 v[40:43], v[224:227], a[72:75], v[40:43]\n\t"
  "v_mfma_f32_16x16x32_bf16 v[44:47], v[228:231], a[72:75], v[44:47]\n\t"
  "v_mfma_f32_16x16x32_bf16 v[48:51], v[232:235], a[72:75], v[48:51]\n\t"
  "v_mfma_f32_16x16x32_bf16 v[52:55], v[236:239], a[72:75], v[52:55]\n\t"
  "v_mfma_f32_16x16x32_bf16 v[56:59], v[240:243], a[72:75], v[56:59]\n\t"
  "s_waitcnt lgkmcnt(0)\n\t"
  "s_barrier\n\t"
  "v_mfma_f32_16x16x32_bf16 v[40:43], v[244:247], a[76:79], v[40:43]\n\t"
  "ds_read_b128 v[68:71], v14 offset:10240\n\t"
  "ds_read_b128 v[72:75], v15 offset:10240\n\t"
  "v_mfma_f32_16x16x32_bf16 v[44:47], a[160:163], a[76:79], v[44:47]\n\t"
  "ds_read_b128 v[76:79], v14 offset:30720\n\t"
  "ds_read_b128 v[80:83], v15 offset:30720\n\t"
  "v_mfma_f32_16x16x32_bf16 v[48:51], a[164:167], a[76:79], v[48:51]\n\t"
  "ds_read_b128 v[84:87], v14 offset:51200\n\t"
  "ds_read_b128 v[88:91], v15 offset:51200\n\t"
  "v_mfma_f32_16x16x32_bf16 v[52:55], a[168:171], a[76:79], v[52:55]\n\t"
  "ds_read_b128 v[92:95], v60 offset:10240\n\t"
  "ds_read_b128 v[96:99], v61 offset:10240\n\t"
  "v_mfma_f32_16x16x32_bf16 v[56:59], a[172:175], a[76:79], v[56:59]\n\t"
  "s_waitcnt vmcnt(5)\n\t"
  "ds_read_b128 v[100:103], v60 offset:30720\n\t"
  "v_mfma_f32_16x16x32_bf16 v[40:43], a[176:179], a[80:83], v[40:43]\n\t"
  "ds_read_b128 v[104:107], v61 offset:30720\n\t"
  "v_mfma_f32_16x16x32_bf16 v[44:47], a[180:183], a[80:83], v[44:47]\n\t"
  "v_mfma_f32_16x16x32_bf16 v[48:51], a[184:187], a[80:83], v[48:51]\n\t"
  "v_mfma_f32_16x16x32_bf16 v[52:55], a[188:191], a[80:83], v[52:55]\n\t"
  "v_mfma_f32_16x16x32_bf16 v[56:59], a[192:195], a[80:83], v[56:59]\n\t"
  "v_mfma_f32_16x16x32_bf16 v[40:43], a[196:199], a[84:87], v[40:43]\n\t"
  "v_mfma_f32_16x16x32_bf16 v[44:47], a[200:203], a[84:87], v[44:47]\n\t"
  "v_mfma_f32_16x16x32_bf16 v[48:51], a[204:207], a[84:87], v[48:51]\n\t"
  "v_mfma_f32_16x16x32_bf16 v[52:55], a[208:211], a[84:87], v[52:55]\n\t"
  "s_waitcnt lgkmcnt(0)\n\t"
  "v_add_f32 v68, v68, v76\n\t"
  "v_mfma_f32_16x16x32_bf16 v[56:59], a[212:215], a[84:87], v[56:59]\n\t"
  "s_waitcnt vmcnt(3)\n\t"
  "v_add_f32 v69, v69, v77\n\t"
  "v_mfma_f32_16x16x32_bf16 v[40:43], a[216:219], a[88:91], v[40:43]\n\t"
  "v_add_f32 v70, v70, v78\n\t"
  "v_add_f32 v71, v71, v79\n\t"
  "v_mfma_f32_16x16x32_bf16 v[40:43], a[236:239], a[92:95], v[40:43]\n\t"
  "v_add_f32 v72, v72, v80\n\t"
  "v_add_f32 v73, v73, v81\n\t"
  "s_waitcnt vmcnt(2)\n\t"
  "v_mfma_f32_16x16x32_bf16 v[40:43], a[16:19], a[96:99], v[40:43]\n\t"
  "v_add_f32 v74, v74, v82\n\t"
  "v_add_f32 v75, v75, v83\n\t"
  "s_waitcnt vmcnt(1)\n\t"
  "v_mfma_f32_16x16x32_bf16 v[40:43], a[36:39], a[100:103], v[40:43]\n\t"
  "v_add_f32 v84, v84, v92\n\t"
  "v_add_f32 v85, v85, v93\n\t"
  "v_mfma_f32_16x16x32_bf16 v[44:47], a[220:223], a[88:91], v[44:47]\n\t"
  "v_add_f32 v86, v86, v94\n\t"
  "v_add_f32 v87, v87, v95\n\t"
  "v_mfma_f32_16x16x32_bf16 v[44:47], a[0:3], a[92:95], v[44:47]\n\t"
  "v_add_f32 v88, v88, v96\n\t"
  "v_add_f32 v89, v89, v97\n\t"
  "v_mfma_f32_16x16x32_bf16 v[44:47], a[20:23], a[96:99], v[44:47]\n\t"
  "v_add_f32 v90, v90, v98\n\t"
  "v_add_f32 v91, v91, v99\n\t"
  "v_mfma_f32_16x16x32_bf16 v[44:47], a[40:43], a[100:103], v[44:47]\n\t"
  "v_add_f32 v68, v68, v84\n\t"
  "v_add_f32 v69, v69, v85\n\t"
  "v_mfma_f32_16x16x32_bf16 v[48:51], a[224:227], a[88:91], v[48:51]\n\t"
  "v_add_f32 v70, v70, v86\n\t"
  "v_add_f32 v71, v71, v87\n\t"
  "v_mfma_f32_16x16x32_bf16 v[48:51], a[4:7], a[92:95], v[48:51]\n\t"
  "ds_write_b128 v13, v[40:43] offset:15360\n\t"
  "v_add_f32 v72, v72, v88\n\t"
  "v_mfma_f32_16x16x32_bf16 v[48:51], a[24:27], a[96:99], v[48:51]\n\t"
  "v_add_f32 v73, v73, v89\n\t"
  "v_add_f32 v74, v74, v90\n\t"
  "v_mfma_f32_16x16x32_bf16 v[48:51], a[44:47], a[100:103], v[48:51]\n\t"
  "v_add_f32 v75, v75, v91\n\t"
  "v_fma_f32 v68, v68, v64, v100\n\t"
  "v_mfma_f32_16x16x32_bf16 v[52:55], a[228:231], a[88:91], v[52:55]\n\t"
  "v_fma_f32 v69, v69, v65, v101\n\t"
  "v_fma_f32 v70, v70, v66, v102\n\t"
  "v_mfma_f32_16x16x32_bf16 v[52:55], a[8:11], a[92:95], v[52:55]\n\t"
  "ds_write_b128 v13, v[44:47] offset:16384\n\t"
  "v_fma_f32 v71, v71, v67, v103\n\t"
  "v_mfma_f32_16x16x32_bf16 v[52:55], a[28:31], a[96:99], v[52:55]\n\t"
  "v_fma_f32 v72, v72, v108, v104\n\t"
  "v_fma_f32 v73, v73, v109, v105\n\t"
  "v_mfma_f32_16x16x32_bf16 v[52:55], a[48:51], a[100:103], v[52:55]\n\t"
  "v_fma_f32 v74, v74, v110, v106\n\t"
  "v_fma_f32 v75, v75, v111, v107\n\t"
  "v_mfma_f32_16x16x32_bf16 v[56:59], a[232:235], a[88:91], v[56:59]\n\t"
  "v_cvt_pk_bf16_f32 v76, v68, v69\n\t"
  "v_cvt_pk_bf16_f32 v77, v70, v71\n\t"
  "v_mfma_f32_16x16x32_bf16 v[56:59], a[12:15], a[92:95], v[56:59]\n\t"
  "ds_write_b128 v13, v[48:51] offset:17408\n\t"
  "v_cvt_pk_bf16_f32 v78, v72, v73\n\t"
  "v_mfma_f32_16x16x32_bf16 v[56:59], a[32:35], a[96:99], v[56:59]\n\t"
  "v_cvt_pk_bf16_f32 v79, v74, v75\n\t"
  "s_mov_b32 s55, 1310720\n\t"
  "v_mfma_f32_16x16x32_bf16 v[56:59], a[52:55], a[100:103], v[56:59]\n\t"
  "buffer_store_dwordx4 v[76:79], v16, %[srdo], s55 offen\n\t"
  "ds_write_b128 v13, v[52:55] offset:18432\n\t"
  "s_nop 5\n\t"
  "ds_write_b128 v13, v[56:59] offset:19456\n\t"
  "buffer_store_dwordx2 v[132:133], v18, %[srd], 0 offen sc1\n\t"
  "s_waitcnt lgkmcnt(0)\n\t"
  "s_barrier\n\t"
  "ds_read_b128 v[68:71], v14 offset:15360\n\t"
  "ds_read_b128 v[72:75], v15 offset:15360\n\t"
  "ds_read_b128 v[76:79], v14 offset:35840\n\t"
  "ds_read_b128 v[80:83], v15 offset:35840\n\t"
  "ds_read_b128 v[84:87], v14 offset:56320\n\t"
  "ds_read_b128 v[88:91], v15 offset:56320\n\t"
  "ds_read_b128 v[92:95], v60 offset:15360\n\t"
  "ds_read_b128 v[96:99], v61 offset:15360\n\t"
  "ds_read_b128 v[100:103], v60 offset:35840\n\t"
  "ds_read_b128 v[104:107], v61 offset:35840\n\t"
  "s_waitcnt lgkmcnt(7)\n\t"
  "v_pk_add_f32 v[68:69], v[68:69], v[76:77]\n\t"
  "v_pk_add_f32 v[70:71], v[70:71], v[78:79]\n\t"
  "s_waitcnt lgkmcnt(6)\n\t"
  "v_pk_add_f32 v[72:73], v[72:73], v[80:81]\n\t"
  "v_pk_add_f32 v[74:75], v[74:75], v[82:83]\n\t"
  "s_waitcnt lgkmcnt(3)\n\t"
  "v_pk_add_f32 v[84:85], v[84:85], v[92:93]\n\t"
  "v_pk_add_f32 v[86:87], v[86:87], v[94:95]\n\t"
  "s_waitcnt lgkmcnt(2)\n\t"
  "v_pk_add_f32 v[88:89], v[88:89], v[96:97]\n\t"
  "v_pk_add_f32 v[90:91], v[90:91], v[98:99]\n\t"
  "v_pk_add_f32 v[68:69], v[68:69], v[84:85]\n\t"
  "v_pk_add_f32 v[70:71], v[70:71], v[86:87]\n\t"
  "v_pk_add_f32 v[72:73], v[72:73], v[88:89]\n\t"
  "v_pk_add_f32 v[74:75], v[74:75], v[90:91]\n\t"
  "s_waitcnt lgkmcnt(1)\n\t"
  "v_pk_fma_f32 v[68:69], v[68:69], v[64:65], v[100:101]\n\t"
  "v_pk_fma_f32 v[70:71], v[70:71], v[66:67], v[102:103]\n\t"
  "s_waitcnt lgkmcnt(0)\n\t"
  "v_pk_fma_f32 v[72:73], v[72:73], v[108:109], v[104:105]\n\t"
  "v_pk_fma_f32 v[74:75], v[74:75], v[110:111], v[106:107]\n\t"
  "v_cvt_pk_bf16_f32 v76, v68, v69\n\t"
  "v_cvt_pk_bf16_f32 v77, v70, v71\n\t"
  "v_cvt_pk_bf16_f32 v78, v72, v73\n\t"
  "v_cvt_pk_bf16_f32 v79, v74, v75\n\t"
  "s_mov_b32 s55, 1351680\n\t"
  "buffer_store_dwordx4 v[76:79], v16, %[srdo], s55 offen\n\t"
  "s_branch Lfend%=\n\t"
  "Lst0_slow%=:\n\t"
  "s_nop 7\n\t"
  "s_nop 3\n\t"
  "s_mov_b32 s49, 0x40000\n\t"
  "Lst0_loop%=:\n\t"
  "s_sleep 2\n\t"
  "buffer_load_dwordx2 v[92:93], v136, %[srd], %[sST] offen offset:0 sc1\n\t"
  "buffer_load_dwordx2 v[94:95], v136, %[srd], %[sST] offen offset:1024 sc1\n\t"
  "buffer_load_dwordx2 v[96:97], v136, %[srd], %[sST] offen offset:2048 sc1\n\t"
  "buffer_load_dwordx2 v[98:99], v136, %[srd], %[sST] offen offset:3072 sc1\n\t"
  "s_waitcnt vmcnt(0)\n\t"
  "v_min_u32 v131, v93, v95\n\t"
  "v_min3_u32 v131, v131, v97, v99\n\t"
  "v_cmp_eq_u32 vcc, 0, v131\n\t"
  "s_nop 3\n\t"
  "s_cmp_eq_u64 vcc, 0\n\t"
  "s_cbranch_scc1 Lst0_back%=\n\t"
  "s_sub_u32 s49, s49, 1\n\t"
  "s_cmp_lg_u32 s49, 0\n\t"
  "s_cbranch_scc1 Lst0_loop%=\n\t"
  "s_branch Lst0_back%=\n\t"
  "Lw2_slow%=:\n\t"
  "s_nop 7\n\t"
  "s_nop 3\n\t"
  "s_mov_b32 s49, 0x40000\n\t"
  "Lw2_loop%=:\n\t"
  "s_sleep 2\n\t"
  "buffer_load_dwordx2 v[138:139], v2, %[srd], 0 offen sc1\n\t"
  "s_waitcnt vmcnt(0)\n\t"
  "v_cmp_ne_u64 vcc, v[132:133], v[138:139]\n\t"
  "s_nop 3\n\t"
  "s_cmp_eq_u64 vcc, 0\n\t"
  "s_cbranch_scc1 Lw2_back%=\n\t"
  "s_sub_u32 s49, s49, 1\n\t"
  "s_cmp_lg_u32 s49, 0\n\t"
  "s_cbranch_scc1 Lw2_loop%=\n\t"
  "s_branch Lw2_back%=\n\t"
  "Lst1_slow%=:\n\t"
  "s_nop 7\n\t"
  "s_nop 3\n\t"
  "s_waitcnt vmcnt(0)\n\t"
  "s_movk_i32 s59, 8192\n\t"
  "buffer_store_dwordx4 v[132:135], v9, %[srd], s59 offen sc1\n\t"
  "s_mov_b32 s49, 0x40000\n\t"
  "Lst1_loop%=:\n\t"
  "s_sleep 2\n\t"
  "buffer_load_dwordx2 v[100:101], v136, %[srd], s54 offen offset:0 sc1\n\t"
  "buffer_load_dwordx2 v[102:103], v136, %[srd], s54 offen offset:1024 sc1\n\t"
  "buffer_load_dwordx2 v[104:105], v136, %[srd], s54 offen offset:2048 sc1\n\t"
  "buffer_load_dwordx2 v[106:107], v136, %[srd], s54 offen offset:3072 sc1\n\t"
  "s_waitcnt vmcnt(0)\n\t"
  "v_min_u32 v131, v101, v103\n\t"
  "v_min3_u32 v131, v131, v105, v107\n\t"
  "v_cmp_eq_u32 vcc, 0, v131\n\t"
  "s_nop 3\n\t"
  "s_cmp_eq_u64 vcc, 0\n\t"
  "s_cbranch_scc1 Lst1_back%=\n\t"
  "s_sub_u32 s49, s49, 1\n\t"
  "s_cmp_lg_u32 s49, 0\n\t"
  "s_cbranch_scc1 Lst1_loop%=\n\t"
  "s_branch Lst1_back%=\n\t"
  "Lfl0_slow%=:\n\t"
  "s_nop 7\n\t"
  "s_nop 3\n\t"
  "s_waitcnt vmcnt(0)\n\t"
  "s_movk_i32 s59, 16384\n\t"
  "buffer_store_dwordx4 v[132:135], v9, %[srd], s59 offen sc1\n\t"
  "s_mov_b32 s49, 0x40000\n\t"
  "Lfl0_loop%=:\n\t"
  "s_sleep 2\n\t"
  "s_add_u32 s57, %[sFL], 0\n\t"
  "buffer_load_dwordx2 v[138:139], v137, %[srd], s57 offen sc1\n\t"
  "s_waitcnt vmcnt(0)\n\t"
  "v_cmp_ne_u64 vcc, v[132:133], v[138:139]\n\t"
  "s_nop 3\n\t"
  "s_cmp_eq_u64 vcc, 0\n\t"
  "s_cbranch_scc1 Lfl0_back%=\n\t"
  "s_sub_u32 s49, s49, 1\n\t"
  "s_cmp_lg_u32 s49, 0\n\t"
  "s_cbranch_scc1 Lfl0_loop%=\n\t"
  "s_branch Lfl0_back%=\n\t"
  "Lfl1_slow%=:\n\t"
  "s_nop 7\n\t"
  "s_nop 3\n\t"
  "s_waitcnt vmcnt(0)\n\t"
  "s_movk_i32 s59, 24576\n\t"
  "buffer_store_dwordx4 v[132:135], v9, %[srd], s59 offen sc1\n\t"
  "s_mov_b32 s49, 0x40000\n\t"
  "Lfl1_loop%=:\n\t"
  "s_sleep 2\n\t"
  "s_add_u32 s57, %[sFL], 8192\n\t"
  "buffer_load_dwordx2 v[138:139], v137, %[srd], s57 offen sc1\n\t"
  "s_waitcnt vmcnt(0)\n\t"
  "v_cmp_ne_u64 vcc, v[132:133], v[138:139]\n\t"
  "s_nop 3\n\t"
  "s_cmp_eq_u64 vcc, 0\n\t"
  "s_cbranch_scc1 Lfl1_back%=\n\t"
  "s_sub_u32 s49, s49, 1\n\t"
  "s_cmp_lg_u32 s49, 0\n\t"
  "s_cbranch_scc1 Lfl1_loop%=\n\t"
  "s_branch Lfl1_back%=\n\t"
  "Lfl2_slow%=:\n\t"
  "s_nop 7\n\t"
  "s_nop 3\n\t"
  "s_mov_b32 s49, 0x40000\n\t"
  "Lfl2_loop%=:\n\t"
  "s_sleep 2\n\t"
  "s_add_u32 s57, %[sFL], 16384\n\t"
  "buffer_load_dwordx2 v[138:139], v137, %[srd], s57 offen sc1\n\t"
  "s_waitcnt vmcnt(0)\n\t"
  "v_cmp_ne_u64 vcc, v[132:133], v[138:139]\n\t"
  "s_nop 3\n\t"
  "s_cmp_eq_u64 vcc, 0\n\t"
  "s_cbranch_scc1 Lfl2_back%=\n\t"
  "s_sub_u32 s49, s49, 1\n\t"
  "s_cmp_lg_u32 s49, 0\n\t"
  "s_cbranch_scc1 Lfl2_loop%=\n\t"
  "s_branch Lfl2_back%=\n\t"
  "Lfl3_slow%=:\n\t"
  "s_nop 7\n\t"
  "s_nop 3\n\t"
  "s_mov_b32 s49, 0x40000\n\t"
  "Lfl3_loop%=:\n\t"
  "s_sleep 2\n\t"
  "s_add_u32 s57, %[sFL], 24576\n\t"
  "buffer_load_dwordx2 v[138:139], v137, %[srd], s57 offen sc1\n\t"
  "s_waitcnt vmcnt(0)\n\t"
  "v_cmp_ne_u64 vcc, v[132:133], v[138:139]\n\t"
  "s_nop 3\n\t"
  "s_cmp_eq_u64 vcc, 0\n\t"
  "s_cbranch_scc1 Lfl3_back%=\n\t"
  "s_sub_u32 s49, s49, 1\n\t"
  "s_cmp_lg_u32 s49, 0\n\t"
  "s_cbranch_scc1 Lfl3_loop%=\n\t"
  "s_branch Lfl3_back%=\n\t"
  "Lfend%=:\n\t"
  ".Lk_end:\n\t"
    :
    : [srd] "s"(srd), [srdo] "s"(srdo), [w] "s"(w), [ws] "s"(ws),
      [tlo] "s"((u32)tag), [thi] "s"((u32)(tag >> 32)), [sW1] "s"(OFF_W1F + (j * 4 + w) * 61440u), [sW2] "s"(OFF_W2F + (ng * 4 + w) * 61440u),
      [sUB] "s"(OFF_UB + (j * 4 + w) * 1536u),
      [sH2ld] "s"(OFF_H2F + ((4 * p + sub) * 48 + 12 * w) * 1024u), [sST] "s"(OFF_ST2 + p * 4096u), [sFL] "s"(OFF_FL + p * 32768u + w * 2048u),
      [sRT] "s"(OFF_RT + ng * 320u), [wdone] "s"(wdone), [pm] "s"(pmask)
    : "memory", "vcc", "scc",
"a0", "a1", "a2", "a3", "a4", "a5", "a6", "a7", "a8", "a9", "a10", "a11", "a12", "a13", "a14", "a15", "a16", "a17", "a18", "a19", "a20", "a21", "a22", "a23", "a24", "a25", "a26", "a27", "a28", "a29", "a30", "a31", "a32", "a33", "a34", "a35", "a36", "a37", "a38", "a39", "a40", "a41", "a42", "a43", "a44", "a45", "a46", "a47", "a48", "a49", "a50", "a51", "a52", "a53", "a54", "a55", "a56", "a57", "a58", "a59", "a60", "a61", "a62", "a63", "a64", "a65", "a66", "a67", "a68", "a69", "a70", "a71", "a72", "a73", "a74", "a75", "a76", "a77", "a78", "a79", "a80", "a81", "a82", "a83", "a84", "a85", "a86", "a87", "a88", "a89", "a90", "a91", "a92", "a93", "a94", "a95", "a96", "a97", "a98", "a99", "a100", "a101", "a102", "a103", "a104", "a105", "a106", "a107", "a108", "a109", "a110", "a111", "a112", "a113", "a114", "a115", "a116", "a117", "a118", "a119", "a120", "a121", "a122", "a123", "a124", "a125", "a126", "a127", "a128", "a129", "a130", "a131", "a132", "a133", "a134", "a135", "a136", "a137", "a138", "a139", "a140", "a141", "a142", "a143", "a144", "a145", "a146", "a147", "a148", "a149", "a150", "a151", "a152", "a153", "a154", "a155", "a156", "a157", "a158", "a159", "a160", "a161", "a162", "a163", "a164", "a165", "a166", "a167", "a168", "a169", "a170", "a171", "a172", "a173", "a174", "a175", "a176", "a177", "a178", "a179", "a180", "a181", "a182", "a183", "a184", "a185", "a186", "a187", "a188", "a189", "a190", "a191", "a192", "a193", "a194", "a195", "a196", "a197", "a198", "a199", "a200", "a201", "a202", "a203", "a204", "a205", "a206", "a207", "a208", "a209", "a210", "a211", "a212", "a213", "a214", "a215", "a216", "a217", "a218", "a219", "a220", "a221", "a222", "a223", "a224", "a225", "a226", "a227", "a228", "a229", "a230", "a231", "a232", "a233", "a234", "a235", "a236", "a237", "a238", "a239", "a240", "a241", "a242", "a243", "a244", "a245", "a246", "a247", "a248", "a249", "a250", "a251", "a252", "a253", "a254", "a255", "s44", "s45", "s46", "s47", "s48", "s49", "s52", "s54", "s55", "s56", "s57", "s58", "s59", "s60", "s61", "s62", "s63", "s68", "s69", "v0", "v1", "v2", "v3", "v4", "v5", "v6", "v7", "v8", "v9", "v10", "v11", "v12", "v13", "v14", "v15", "v16", "v17", "v18", "v19", "v20", "v21", "v22", "v23", "v24", "v25", "v26", "v27", "v28", "v29", "v30", "v31", "v32", "v33", "v34", "v35", "v36", "v37", "v38", "v39", "v40", "v41", "v42", "v43", "v44", "v45", "v46", "v47", "v48", "v49", "v50", "v51", "v52", "v53", "v54", "v55", "v56", "v57", "v58", "v59", "v60", "v61", "v62", "v63", "v64", "v65", "v66", "v67", "v68", "v69", "v70", "v71", "v72", "v73", "v74", "v75", "v76", "v77", "v78", "v79", "v80", "v81", "v82", "v83", "v84", "v85", "v86", "v87", "v88", "v89", "v90", "v91", "v92", "v93", "v94", "v95", "v96", "v97", "v98", "v99", "v100", "v101", "v102", "v103", "v104", "v105", "v106", "v107", "v108", "v109", "v110", "v111", "v112", "v113", "v114", "v115", "v116", "v117", "v118", "v119", "v120", "v121", "v122", "v123", "v124", "v125", "v126", "v127", "v128", "v129", "v130", "v131", "v132", "v133", "v134", "v135", "v136", "v137", "v138", "v139", "v141", "v142", "v143", "v144", "v145", "v146", "v147", "v148", "v149", "v150", "v151", "v152", "v153", "v154", "v155", "v156", "v157", "v158", "v159", "v160", "v161", "v162", "v163", "v164", "v165", "v166", "v167", "v168", "v169", "v170", "v171", "v172", "v173", "v174", "v175", "v176", "v177", "v178", "v179", "v180", "v181", "v182", "v183", "v184", "v185", "v186", "v187", "v188", "v189", "v190", "v191", "v192", "v193", "v194", "v195", "v196", "v197", "v198", "v199", "v200", "v201", "v202", "v203", "v204", "v205", "v206", "v207", "v208", "v209", "v210", "v211", "v212", "v213", "v214", "v215", "v216", "v217", "v218", "v219", "v220", "v221", "v222", "v223", "v224", "v225", "v226", "v227", "v228", "v229", "v230", "v231", "v232", "v233", "v234", "v235", "v236", "v237", "v238", "v239", "v240", "v241", "v242", "v243", "v244", "v245", "v246", "v247", "v248", "v249", "v250", "v251", "v252", "v253", "v254", "v255"
  );
}

extern "C" void kernel_launch(void* const* d_in, const int* in_sizes, int n_in, void* d_out, int out_size,
                              void* d_ws, size_t ws_size, hipStream_t stream) {
  k_main<<<256, 256, 0, stream>>>((const u16*)d_in[0], (const u16*)d_in[1], (const u16*)d_in[2], (const u16*)d_in[3], (const u16*)d_in[4],
                                  (const u16*)d_in[5], (const u16*)d_in[7], (const u16*)d_in[8], (const u16*)d_in[9], (const u16*)d_in[10],
                                  (const u16*)d_in[11], (char*)d_ws, (u16*)d_out);
}
```
